```python
import torch

def _softmax(x, axis=-1):
    return torch.softmax(x, dim=axis)
def _gelu(x):
    return torch.nn.functional.gelu(x, approximate="tanh")
def _silu(x):
    return torch.nn.functional.silu(x)
def _sigmoid(x):
    return torch.sigmoid(x)
def _layernorm(x, axis=-1, eps=1e-5):
    m = x.mean(dim=axis, keepdim=True)
    v = x.var(dim=axis, keepdim=True, unbiased=False)
    return (x - m) / torch.sqrt(v + eps)
def _scatter_add(tgt, idx, src, axis=0):
    return tgt.clone().index_add_(axis, idx, src)
def _segment_sum(src, seg, n):
    out = torch.zeros((n,) + src.shape[1:], dtype=src.dtype)
    return out.index_add_(0, seg, src)
def _pad(x, pw):
    flat = tuple(v for pair in reversed(pw) for v in pair)
    return torch.nn.functional.pad(x, flat)

kernel_name = 'standalone__winograd__vision'

def setup_inputs():

    _w = (lambda a: (lambda t: t.to(torch.bfloat16) if t.is_floating_point() else t)(torch.as_tensor(a)))
    H = W = 512 // 2 * 2
    C = 192
    return {
        "x": _w((lambda *s: torch.randn(*s, dtype=torch.bfloat16) * 0.05)(1, H, W, C)),
        "w": _w((lambda *s: torch.randn(*s, dtype=torch.bfloat16) * 0.05)(3, 3, C, C)),
    }

def reference(x, w):
    N, H, W, C = x.shape
    K = w.shape[-1]
    G  = torch.as_tensor([[1.0,0.0,0.0],[0.5,0.5,0.5],[0.5,-0.5,0.5],[0.0,0.0,1.0]], dtype=x.dtype)
    Bt = torch.as_tensor([[1.0,0.0,-1.0,0.0],[0.0,1.0,1.0,0.0],[0.0,-1.0,1.0,0.0],[0.0,1.0,0.0,-1.0]], dtype=x.dtype)
    At = torch.as_tensor([[1.0,1.0,1.0,0.0],[0.0,1.0,-1.0,-1.0]], dtype=x.dtype)
    U = torch.einsum("ar,rsck,bs->abck", G, w, G)
    xp = _pad(x, ((0,0),(1,1),(1,1),(0,0)))
    nH, nW = H // 2, W // 2
    patches = []
    for i in range(4):
        for j in range(4):
            patches.append(xp[:, i:i + 2 * nH:2, j:j + 2 * nW:2, :])
    d = torch.stack(patches).reshape(4, 4, N * nH * nW, C)
    V = torch.einsum("ai,ijpc,bj->abpc", Bt, d, Bt)
    M = torch.einsum("abck,abpc->abpk", U, V)
    Y = torch.einsum("ma,abpk,nb->pmnk", At, M, At)
    Y = Y.reshape(N, nH, nW, 2, 2, K)
    Y = (lambda x, *d: x.permute(*d))(Y, 0, 1, 3, 2, 4, 5).reshape(N, H, W, K)
    return Y
```

```cpp
#include <hip/hip_runtime.h>
#include <hip/hip_bf16.h>
#include <stdint.h>

#define IMG_W 512
#define CH 192
#define PXB 384
#define ROWB (IMG_W * PXB)
#define U_BYTES 1179648

__global__ __launch_bounds__(256, 1) void wino_main_kernel(const char* __restrict__ x, char* __restrict__ Uq, char* __restrict__ out, const char* __restrict__ wt) {
  __shared__ __attribute__((aligned(1024))) char smem[163840];
  unsigned tid = threadIdx.x;
  asm volatile(
"s_load_dwordx8 s[76:83], %1, 0x0\n\t"
"s_mov_b32 s66, %2\n\t"
"s_getpc_b64 s[6:7]\n\t"
"Lwino_pc:\n\t"
"s_bfe_u32 s42, %2, 0x50003\n\t"
"s_lshl_b32 s42, s42, 12\n\t"
"v_lshl_add_u32 v203, v205, 4, s42\n\t"
"v_min_u32 v203, (Lwino_end-Lwino_pc-16)&-16, v203\n\t"
"global_load_dwordx4 v[196:199], v203, s[6:7]\n\t"
"v_and_b32 v204, 63, v205\n\t"
"v_readfirstlane_b32 s73, v205\n\t"
"v_lshrrev_b32 v120, 3, v204\n\t"
"v_and_b32 v121, 7, v204\n\t"
"s_lshr_b32 s73, s73, 6\n\t"
"s_lshr_b32 s56, s66, 5\n\t"
"s_mul_i32 s56, s56, 12288\n\t"
"s_bfe_u32 s61, s66, 0x20001\n\t"
"s_mul_i32 s61, s61, 1536\n\t"
"s_add_u32 s56, s56, s61\n\t"
"s_bitcmp1_b32 s66, 0\n\t"
"s_cselect_b32 s61, 48, 0\n\t"
"s_add_u32 s56, s56, s61\n\t"
"s_bfe_u32 s62, s66, 0x20003\n\t"
"s_mul_i32 s61, s62, 96\n\t"
"s_add_u32 s56, s56, s61\n\t"
"s_cmp_lt_u32 s66, 192\n\t"
"s_cselect_b32 s56, s56, 2.0\n\t"
"v_bfe_u32 v42, v204, 2, 2\n\t"
"v_and_or_b32 v42, v204, 16, v42\n\t"
"v_mul_u32_u24 v42, 0x180, v42\n\t"
"v_lshrrev_b32 v44, 5, v204\n\t"
"v_mul_u32_u24 v43, 0x12000, v44\n\t"
"v_and_b32 v45, 3, v204\n\t"
"v_min_u32 v41, 2, v45\n\t"
"v_lshl_add_u32 v41, v41, 4, v42\n\t"
"v_add_u32 v41, v41, v43\n\t"
"v_lshlrev_b32 v43, 2, v204\n\t"
"v_and_or_b32 v45, v43, 16, v45\n\t"
"v_mul_u32_u24 v45, 0x180, v45\n\t"
"v_lshl_add_u32 v44, v44, 3, v120\n\t"
"v_lshl_add_u32 v45, v44, 1, v45\n\t"
"v_mov_b32 v40, 0\n\t"
"s_mul_i32 s61, s73, 147456\n\t"
"s_add_u32 s61, s61, s56\n\t"
"s_lshl3_add_u32 s62, s73, s56\n\t"
"s_add_u32 s62, s62, 589824\n\t"
"s_lshl_b32 s64, s73, 10\n\t"
"s_and_b32 s60, s66, 7\n\t"
"s_lshr_b32 s65, s66, 3\n\t"
"s_mul_i32 s24, s60, 12582912\n\t"
"s_mul_i32 s63, s65, 6144\n\t"
"s_add_u32 s24, s24, s63\n\t"
"s_add_u32 s23, s24, 196224\n\t"
"s_mul_i32 s63, s73, 786432\n\t"
"s_mul_i32 s26, s73, 4608\n\t"
"s_mov_b32 s48, 189696\n\t"
"s_cmp_eq_u32 s65, 0\n\t"
"s_cselect_b64 s[40:41], 0xff, 0\n\t"
"s_cselect_b64 s[46:47], -1, 0\n\t"
"v_mul_u32_u24 v122, 0x300, v120\n\t"
"v_xor_b32 v123, 4, v121\n\t"
"v_add_u32 v122, s63, v122\n\t"
"v_and_b32 v124, 2, v120\n\t"
"v_xor_b32 v124, v124, v123\n\t"
"v_lshl_add_u32 v128, v124, 4, v122\n\t"
"v_cndmask_b32 v236, v128, 2.0, s[40:41]\n\t"
"v_add_u32 v122, 195840, v122\n\t"
"v_add_u32 v124, -1, v120\n\t"
"v_ashrrev_i32 v125, 31, v124\n\t"
"v_sub_u32 v124, v124, v125\n\t"
"v_and_b32 v124, 2, v124\n\t"
"v_xor_b32 v124, v124, v123\n\t"
"v_mad_i32_i24 v125, v125, s48, v122\n\t"
"v_lshl_add_u32 v129, v124, 4, v125\n\t"
"s_lshl_b64 s[40:41], s[40:41], 8\n\t"
"v_cndmask_b32 v237, v129, 2.0, s[40:41]\n\t"
"v_add_u32 v122, 195840, v122\n\t"
"v_add_u32 v124, -2, v120\n\t"
"v_ashrrev_i32 v125, 31, v124\n\t"
"v_sub_u32 v124, v124, v125\n\t"
"v_and_b32 v124, 2, v124\n\t"
"v_and_or_b32 v124, v125, 4, v124\n\t"
"v_xor_b32 v124, v124, v121\n\t"
"v_mad_i32_i24 v125, v125, s48, v122\n\t"
"v_lshl_add_u32 v130, v124, 4, v125\n\t"
"s_lshl_b64 s[40:41], s[40:41], 8\n\t"
"v_cndmask_b32 v238, v130, 2.0, s[40:41]\n\t"
"v_add_u32 v122, 195840, v122\n\t"
"v_add_u32 v124, -3, v120\n\t"
"v_ashrrev_i32 v125, 31, v124\n\t"
"v_sub_u32 v124, v124, v125\n\t"
"v_and_b32 v124, 2, v124\n\t"
"v_xor_b32 v124, v124, v121\n\t"
"v_mad_i32_i24 v125, v125, s48, v122\n\t"
"v_lshl_add_u32 v131, v124, 4, v125\n\t"
"s_lshl_b64 s[40:41], s[40:41], 8\n\t"
"v_cndmask_b32 v239, v131, 2.0, s[40:41]\n\t"
"v_add_u32 v124, 5, v120\n\t"
"v_and_b32 v124, 2, v124\n\t"
"v_xor_b32 v124, v124, v121\n\t"
"v_add_u32 v122, 0x1800, v122\n\t"
"v_lshl_add_u32 v240, v124, 4, v122\n\t"
"s_mov_b32 s36, -1\n\t"
"s_mov_b32 s37, 0\n\t"
"s_mov_b32 s54, 663552\n\t"
"s_mov_b32 s55, 0x20000\n\t"
"s_mov_b32 s14, 100663296\n\t"
"s_mov_b32 s15, 0x20000\n\t"
"s_add_u32 m0, s64, 0\n\t"
"s_waitcnt lgkmcnt(0)\n\t"
"s_mov_b32 s52, s82\n\t"
"s_and_b32 s53, s83, 0xffff\n\t"
"buffer_load_dwordx4 v41, s[52:55], s61 offen lds\n\t"
"buffer_load_short_d16_hi v40, v45, s[52:55], s62 offen\n\t"
"s_mov_b32 s12, s76\n\t"
"s_and_b32 s13, s77, 0xffff\n\t"
"s_add_u32 m0, s26, 83200\n\t"
"s_nop 0\n\t"
"buffer_load_dwordx4 v236, s[12:15], s23 offen offset:0 nt lds\n\t"
"s_add_u32 m0, s26, 84224\n\t"
"s_nop 0\n\t"
"buffer_load_dwordx4 v237, s[12:15], s23 offen offset:0 nt lds\n\t"
"s_add_u32 m0, s26, 85248\n\t"
"s_nop 0\n\t"
"buffer_load_dwordx4 v238, s[12:15], s23 offen offset:0 nt lds\n\t"
"s_add_u32 m0, s26, 86272\n\t"
"s_nop 0\n\t"
"buffer_load_dwordx4 v239, s[12:15], s23 offen offset:0 nt lds\n\t"
"s_add_u32 m0, s26, 87296\n\t"
"s_mov_b64 exec, s[36:37]\n\t"
"buffer_load_dwordx4 v240, s[12:15], s23 offen offset:0 nt lds\n\t"
"s_mov_b64 exec, -1\n\t"
"s_mul_i32 s49, s73, 36\n\t"
"s_mul_i32 s43, s73, 576\n\t"
"s_sub_u32 s42, s23, 203520\n\t"
"s_mov_b32 s44, -1\n\t"
"s_mov_b32 s45, 15\n\t"
"v_add_u32 v124, s49, v204\n\t"
"v_lshrrev_b32 v119, 3, v124\n\t"
"v_and_b32 v124, 7, v124\n\t"
"v_mul_u32_u24 v127, 0x300, v119\n\t"
"v_cmp_gt_u32 vcc, 9, v119\n\t"
"v_add_u32 v119, -9, v119\n\t"
"v_ashrrev_i32 v125, 31, v119\n\t"
"v_add_u32 v127, s42, v127\n\t"
"v_mad_i32_i24 v127, v125, s48, v127\n\t"
"v_and_b32 v125, 9, v125\n\t"
"v_add_u32 v119, v119, v125\n\t"
"v_and_b32 v125, 2, v119\n\t"
"v_xor_b32 v124, v124, v125\n\t"
"v_lshl_add_u32 v133, v124, 4, v127\n\t"
"s_cmp_eq_u32 s60, 0\n\t"
"s_cselect_b64 s[50:51], vcc, 0\n\t"
"v_cmp_eq_u32 vcc, 0, v119\n\t"
"v_cndmask_b32 v246, v133, 2.0, s[50:51]\n\t"
"s_and_b64 vcc, vcc, s[46:47]\n\t"
"v_cndmask_b32 v246, v246, 2.0, vcc\n\t"
"s_add_u32 m0, s43, 80896\n\t"
"s_mov_b64 exec, s[44:45]\n\t"
"buffer_load_dwordx4 v246, s[12:15], 0 offen offset:0 nt lds\n\t"
"s_mov_b64 exec, -1\n\t"
"s_mul_i32 s60, s73, 48\n\t"
"s_add_u32 s60, s60, 118784\n\t"
"s_mul_i32 s70, s66, 16\n\t"
"s_add_u32 s70, s70, 1900544\n\t"
"s_lshl_b32 s61, s73, 8\n\t"
"s_or_b32 s61, s61, s66\n\t"
"s_cmp_lt_u32 s61, 192\n\t"
"s_cselect_b32 s70, s70, 2.0\n\t"
"s_mov_b32 s10, 0x200000\n\t"
"s_mov_b32 s11, 0x20000\n\t"
"s_mov_b32 s8, s78\n\t"
"s_and_b32 s9, s79, 0xffff\n\t"
"v_min_u32 v189, 47, v204\n\t"
"v_add_u32 v189, s60, v189\n\t"
"v_mul_u32_u24 v189, 16, v189\n\t"
"s_lshr_b32 s61, s66, 3\n\t"
"s_lshl2_add_u32 s61, s61, s73\n\t"
"s_lshl_b32 s60, s61, 8\n\t"
"s_cmp_lt_u32 s61, 96\n\t"
"s_cselect_b32 s60, s60, 2.0\n\t"
"v_lshrrev_b32 v118, 1, v204\n\t"
"v_mul_u32_u24 v118, 220928, v118\n\t"
"v_lshl_add_u32 v118, v204, 7, v118\n\t"
"s_mov_b64 exec, 63\n\t"
"buffer_load_dword v118, v118, s[52:55], s60 offen\n\t"
"s_mov_b64 exec, -1\n\t"
"buffer_load_dword v187, off, s[8:11], s70 sc1\n\t"
"buffer_load_dword v188, v189, s[8:11], 0 offen sc1\n\t"
"v_add_u32 v107, 0, v204\n\t"
"v_mul_u32_u24 v104, 0xab, v107\n\t"
"v_lshrrev_b32 v104, 10, v104\n\t"
"v_mul_u32_u24 v104, 0x120, v104\n\t"
"v_lshl_add_u32 v104, v107, 4, v104\n\t"
"v_add_u32 v107, 64, v204\n\t"
"v_mul_u32_u24 v105, 0xab, v107\n\t"
"v_lshrrev_b32 v105, 10, v105\n\t"
"v_mul_u32_u24 v105, 0x120, v105\n\t"
"v_lshl_add_u32 v105, v107, 4, v105\n\t"
"v_add_u32 v107, 128, v204\n\t"
"v_mul_u32_u24 v106, 0xab, v107\n\t"
"v_lshrrev_b32 v106, 10, v106\n\t"
"v_mul_u32_u24 v106, 0x120, v106\n\t"
"v_lshl_add_u32 v106, v107, 4, v106\n\t"
"s_mul_i32 s74, s73, 6144\n\t"
"s_mul_i32 s72, s73, 96\n\t"
"s_cmp_lt_u32 s73, 2\n\t"
"s_cselect_b32 s75, 0, 65280\n\t"
"s_add_u32 s75, s75, s74\n\t"
"s_add_u32 s75, s75, 65536\n\t"
"s_lshr_b32 s60, s66, 5\n\t"
"s_bfe_u32 s62, s66, 0x20003\n\t"
"s_and_b32 s61, s60, 1\n\t"
"s_lshl1_add_u32 s61, s73, s61\n\t"
"s_cmp_ge_u32 s61, 3\n\t"
"s_cselect_b32 s63, 9, 0\n\t"
"s_add_u32 s61, s61, s63\n\t"
"s_mul_i32 s62, s62, 3\n\t"
"s_add_u32 s61, s61, s62\n\t"
"s_lshr_b32 s60, s60, 1\n\t"
"s_mul_i32 s62, s60, 384\n\t"
"s_add_u32 s61, s61, s62\n\t"
"s_lshl_b32 s57, s61, 10\n\t"
"s_and_b32 s61, s66, 7\n\t"
"s_lshl_b32 s61, s61, 7\n\t"
"s_add_u32 s57, s57, s61\n\t"
"s_cmp_lt_u32 s60, 1\n\t"
"s_cselect_b32 s58, 0x3e000000, 0.5\n\t"
"s_cmp_lt_u32 s60, 2\n\t"
"s_cselect_b32 s59, 0x3e800000, 1.0\n\t"
"s_cmp_lt_u32 s73, 3\n\t"
"s_cselect_b32 s67, s66, 0xff\n\t"
"s_cmp_lt_u32 s67, 192\n\t"
"s_cselect_b32 s57, s57, 2.0\n\t"
"s_lshl_b32 s60, s73, 3\n\t"
"s_add_u32 s60, s60, 0\n\t"
"s_lshl_b32 s61, s73, 11\n\t"
"s_add_u32 s61, s61, 4096\n\t"
"v_lshl_add_u32 v46, v121, 6, s60\n\t"
"v_lshl_add_u32 v46, v44, 1, v46\n\t"
"v_lshl_add_u32 v42, v204, 1, s61\n\t"
"v_lshl_add_u32 v43, v204, 4, s61\n\t"
"v_mul_u32_u24 v44, 0x6000, v120\n\t"
"v_lshl_add_u32 v44, v121, 4, v44\n\t"
"v_mov_b32 v32, 0\n\t"
"v_mov_b32 v33, 0\n\t"
"v_mov_b32 v34, 0\n\t"
"v_mov_b32 v35, 0\n\t"
"v_mov_b32 v36, 0\n\t"
"v_mov_b32 v37, 0\n\t"
"v_mov_b32 v38, 0\n\t"
"v_mov_b32 v39, 0\n\t"
"s_waitcnt vmcnt(9)\n\t"
"s_barrier\n\t"
"ds_read_u16_d16_hi v32, v46 offset:0\n\t"
"ds_read_u16_d16_hi v33, v46 offset:512\n\t"
"ds_read_u16_d16_hi v34, v46 offset:1024\n\t"
"ds_read_u16_d16_hi v35, v46 offset:1536\n\t"
"ds_read_u16_d16_hi v36, v46 offset:2048\n\t"
"ds_read_u16_d16_hi v37, v46 offset:2560\n\t"
"ds_read_u16_d16_hi v38, v46 offset:3072\n\t"
"ds_read_u16_d16_hi v39, v46 offset:3584\n\t"
"s_waitcnt lgkmcnt(1)\n\t"
"v_add_f32 v48, v32, v38\n\t"
"s_waitcnt lgkmcnt(0)\n\t"
"v_add_f32 v49, v33, v39\n\t"
"v_add_f32 v50, v34, v40\n\t"
"v_add_f32 v51, v48, v35\n\t"
"v_sub_f32 v54, v48, v35\n\t"
"v_add_f32 v52, v49, v36\n\t"
"v_sub_f32 v55, v49, v36\n\t"
"v_add_f32 v53, v50, v37\n\t"
"v_sub_f32 v56, v50, v37\n\t"
"v_add_f32 v57, v32, v34\n\t"
"v_add_f32 v58, v51, v53\n\t"
"v_add_f32 v59, v54, v56\n\t"
"v_add_f32 v60, v38, v40\n\t"
"v_add_f32 v62, v57, v33\n\t"
"v_sub_f32 v63, v57, v33\n\t"
"v_add_f32 v64, v58, v52\n\t"
"v_sub_f32 v65, v58, v52\n\t"
"v_add_f32 v66, v59, v55\n\t"
"v_sub_f32 v67, v59, v55\n\t"
"v_add_f32 v68, v60, v39\n\t"
"v_sub_f32 v69, v60, v39\n\t"
"v_mul_f32 v70, s59, v32\n\t"
"v_mul_f32 v71, 0x3e800000, v62\n\t"
"v_mul_f32 v72, 0x3e800000, v63\n\t"
"v_mul_f32 v73, -s59, v34\n\t"
"v_mul_f32 v74, s58, v51\n\t"
"v_mul_f32 v75, 0x3e800000, v64\n\t"
"v_mul_f32 v76, 0x3e800000, v65\n\t"
"v_mul_f32 v77, -s58, v53\n\t"
"v_mul_f32 v78, s58, v54\n\t"
"v_mul_f32 v79, 0x3e800000, v66\n\t"
"v_mul_f32 v80, 0x3e800000, v67\n\t"
"v_mul_f32 v81, -s58, v56\n\t"
"v_mul_f32 v82, -s59, v38\n\t"
"v_mul_f32 v83, 0xbe800000, v68\n\t"
"v_mul_f32 v84, 0xbe800000, v69\n\t"
"v_mul_f32 v85, s59, v40\n\t"
"v_cvt_pk_bf16_f32 v86, v70, v71\n\t"
"v_cvt_pk_bf16_f32 v87, v72, v73\n\t"
"v_cvt_pk_bf16_f32 v88, v74, v75\n\t"
"v_cvt_pk_bf16_f32 v89, v76, v77\n\t"
"v_cvt_pk_bf16_f32 v90, v78, v79\n\t"
"v_cvt_pk_bf16_f32 v91, v80, v81\n\t"
"v_cvt_pk_bf16_f32 v92, v82, v83\n\t"
"v_cvt_pk_bf16_f32 v93, v84, v85\n\t"
"ds_write_b16 v42, v86 offset:0\n\t"
"ds_write_b16_d16_hi v42, v86 offset:128\n\t"
"ds_write_b16 v42, v87 offset:256\n\t"
"ds_write_b16_d16_hi v42, v87 offset:384\n\t"
"ds_write_b16 v42, v88 offset:512\n\t"
"ds_write_b16_d16_hi v42, v88 offset:640\n\t"
"ds_write_b16 v42, v89 offset:768\n\t"
"ds_write_b16_d16_hi v42, v89 offset:896\n\t"
"ds_write_b16 v42, v90 offset:1024\n\t"
"ds_write_b16_d16_hi v42, v90 offset:1152\n\t"
"ds_write_b16 v42, v91 offset:1280\n\t"
"ds_write_b16_d16_hi v42, v91 offset:1408\n\t"
"ds_write_b16 v42, v92 offset:1536\n\t"
"ds_write_b16_d16_hi v42, v92 offset:1664\n\t"
"ds_write_b16 v42, v93 offset:1792\n\t"
"ds_write_b16_d16_hi v42, v93 offset:1920\n\t"
"ds_read_b128 v[96:99], v43 offset:0\n\t"
"ds_read_b128 v[100:103], v43 offset:1024\n\t"
"s_add_u32 s60, s57, 0\n\t"
"s_waitcnt lgkmcnt(1)\n\t"
"buffer_store_dwordx4 v[96:99], v44, s[8:11], s60 offen sc1\n\t"
"s_add_u32 s60, s57, 196608\n\t"
"s_waitcnt lgkmcnt(0)\n\t"
"buffer_store_dwordx4 v[100:103], v44, s[8:11], s60 offen sc1\n\t"
"v_and_b32 v122, 15, v204\n\t"
"v_lshrrev_b32 v123, 4, v204\n\t"
"v_bfe_u32 v124, v204, 2, 2\n\t"
"v_and_b32 v125, 3, v204\n\t"
"v_lshlrev_b32 v125, 3, v125\n\t"
"s_mov_b32 s60, 0x1be4\n\t"
"s_lshl_b32 s61, s73, 10\n\t"
"s_mov_b32 s62, 512\n\t"
"s_add_u32 s63, s26, 1152\n\t"
"v_and_b32 v126, 14, v204\n\t"
"v_bfe_u32 v126, s60, v126, 2\n\t"
"v_xor_b32 v126, v126, v123\n\t"
"v_lshlrev_b32 v126, 4, v126\n\t"
"v_lshl_add_u32 v216, v122, 6, v126\n\t"
"v_add_u32 v221, s61, v216\n\t"
"s_add_u32 m0, s74, 16384\n\t"
"s_add_u32 s27, s72, 0\n\t"
"buffer_load_dwordx4 v104, s[52:55], s27 offen lds\n\t"
"v_lshrrev_b32 v126, 1, v121\n\t"
"v_xor_b32 v126, v126, v123\n\t"
"v_lshl_add_u32 v126, v126, 4, s61\n\t"
"v_lshl_add_u32 v126, v121, 6, v126\n\t"
"v_and_b32 v127, 8, v204\n\t"
"v_lshl_add_u32 v217, v127, 9, v126\n\t"
"v_xad_u32 v219, v217, 48, s62\n\t"
"v_sub_u32 v126, 4, v124\n\t"
"v_cmp_gt_u32 vcc, 32, v204\n\t"
"v_mov_b32 v135, 0x3f80bf80\n\t"
"s_nop 0\n\t"
"v_cndmask_b32 v126, v126, v124, vcc\n\t"
"v_and_b32 v127, 2, v126\n\t"
"v_lshl_add_u32 v127, v127, 4, v125\n\t"
"v_lshl_add_u32 v127, v126, 7, v127\n\t"
"s_add_u32 m0, s74, 17408\n\t"
"s_add_u32 s27, s72, 0\n\t"
"buffer_load_dwordx4 v105, s[52:55], s27 offen lds\n\t"
"v_and_b32 v126, 1, v123\n\t"
"v_mul_u32_u24 v126, 1216, v126\n\t"
"v_add3_u32 v226, v126, v127, s63\n\t"
"v_min_u32 v126, 2, v124\n\t"
"v_cmp_gt_u32 vcc, 48, v204\n\t"
"s_nop 1\n\t"
"v_cndmask_b32 v126, v126, v123, vcc\n\t"
"v_and_b32 v134, 1, v126\n\t"
"v_cndmask_b32 v127, 4, v124, vcc\n\t"
"v_lshl_add_u32 v134, v134, 6, v125\n\t"
"v_lshl_add_u32 v134, v127, 7, v134\n\t"
"v_and_b32 v127, 2, v127\n\t"
"v_lshl_add_u32 v134, v127, 4, v134\n\t"
"v_mul_u32_u24 v126, 2304, v126\n\t"
"v_add3_u32 v227, v126, v134, s26\n\t"
"s_add_u32 m0, s74, 18432\n\t"
"s_add_u32 s27, s72, 0\n\t"
"buffer_load_dwordx4 v106, s[52:55], s27 offen lds\n\t"
"v_and_b32 v126, 24, v204\n\t"
"v_cmp_eq_u32 vcc, 8, v126\n\t"
"v_mov_b32 v134, 0xbf803f80\n\t"
"s_nop 0\n\t"
"v_cndmask_b32 v134, v134, v135, vcc\n\t"
"v_lshrrev_b32 v135, 16, v134\n\t"
"v_cmp_eq_u32 vcc, 7, v121\n\t"
"s_nop 1\n\t"
"v_cndmask_b32 v135, 0, v135, vcc\n\t"
"v_cmp_gt_u32 vcc, 32, v204\n\t"
"s_mov_b32 s48, 384\n\t"
"s_cmp_eq_u32 s65, 31\n\t"
"v_cndmask_b32 v134, v135, v134, vcc\n\t"
"v_cndmask_b32 v136, 4, v121, vcc\n\t"
"v_mov_b32 v135, 0\n\t"
"v_and_b32 v137, 1, v136\n\t"
"v_lshlrev_b32 v137, 4, v137\n\t"
"v_lshrrev_b32 v136, 1, v136\n\t"
"v_lshlrev_b64 v[134:135], v137, v[134:135]\n\t"
"v_cmp_eq_u32 vcc, 0, v136\n\t"
"s_cselect_b64 s[40:41], 0xff, 0\n\t"
"s_cselect_b64 s[46:47], -1, 0\n\t"
"s_add_u32 m0, s74, 19456\n\t"
"s_add_u32 s27, s72, 12288\n\t"
"buffer_load_dwordx4 v104, s[52:55], s27 offen lds\n\t"
"v_cndmask_b32 v140, 0, v134, vcc\n\t"
"v_cndmask_b32 v141, 0, v135, vcc\n\t"
"v_cmp_eq_u32 vcc, 1, v136\n\t"
"v_accvgpr_write_b32 a220, v140\n\t"
"v_xad_u32 v241, v128, 64, s48\n\t"
"v_cndmask_b32 v141, v141, v134, vcc\n\t"
"v_cndmask_b32 v142, 0, v135, vcc\n\t"
"v_cmp_eq_u32 vcc, 2, v136\n\t"
"v_accvgpr_write_b32 a221, v141\n\t"
"s_add_u32 m0, s74, 20480\n\t"
"s_add_u32 s27, s72, 12288\n\t"
"buffer_load_dwordx4 v105, s[52:55], s27 offen lds\n\t"
"v_xad_u32 v242, v129, 64, s48\n\t"
"v_cndmask_b32 v142, v142, v134, vcc\n\t"
"v_cndmask_b32 v143, 0, v135, vcc\n\t"
"v_cmp_eq_u32 vcc, 3, v136\n\t"
"v_accvgpr_write_b32 a222, v142\n\t"
"v_cndmask_b32 v242, v242, 2.0, s[40:41]\n\t"
"v_cndmask_b32 v143, v143, v134, vcc\n\t"
"v_accvgpr_write_b32 a223, v143\n\t"
"s_lshl_b64 s[40:41], s[40:41], 8\n\t"
"v_xad_u32 v243, v130, 64, s48\n\t"
"v_cndmask_b32 v243, v243, 2.0, s[40:41]\n\t"
"s_lshl_b64 s[40:41], s[40:41], 8\n\t"
"v_xad_u32 v244, v131, 64, s48\n\t"
"v_cndmask_b32 v244, v244, 2.0, s[40:41]\n\t"
"s_add_u32 m0, s74, 21504\n\t"
"s_add_u32 s27, s72, 12288\n\t"
"buffer_load_dwordx4 v106, s[52:55], s27 offen lds\n\t"
"s_lshl_b64 s[40:41], s[40:41], 8\n\t"
"v_xad_u32 v245, v240, 64, s48\n\t"
"v_cndmask_b32 v245, v245, 2.0, s[40:41]\n\t"
"v_cmp_eq_u32 vcc, 8, v119\n\t"
"v_xad_u32 v247, v133, 64, s48\n\t"
"s_and_b64 vcc, vcc, s[46:47]\n\t"
"v_cndmask_b32 v247, v247, 2.0, s[50:51]\n\t"
"v_cndmask_b32 v247, v247, 2.0, vcc\n\t"
"v_bfe_u32 v126, v204, 3, 1\n\t"
"v_lshrrev_b32 v127, 4, v204\n\t"
"v_sub_u32 v127, v127, v126\n\t"
"v_mov_b32 v134, 0xbf803f80\n\t"
"v_mov_b32 v135, 0x3f80bf80\n\t"
"v_cmp_eq_u32 vcc, 0, v127\n\t"
"s_add_u32 m0, s74, 40960\n\t"
"s_add_u32 s27, s72, 442368\n\t"
"buffer_load_dwordx4 v104, s[52:55], s27 offen lds\n\t"
"v_add_u32 v126, 4, v126\n\t"
"v_cndmask_b32 v134, 0, v134, vcc\n\t"
"v_cmp_eq_u32 vcc, 1, v127\n\t"
"s_nop 1\n\t"
"v_cndmask_b32 v134, v134, v135, vcc\n\t"
"v_cmp_eq_u32 vcc, 7, v121\n\t"
"s_nop 1\n\t"
"v_cndmask_b32 v135, 0, v135, vcc\n\t"
"v_cmp_gt_u32 vcc, 48, v204\n\t"
"s_nop 1\n\t"
"v_cndmask_b32 v134, v135, v134, vcc\n\t"
"v_mov_b32 v135, 0\n\t"
"v_cndmask_b32 v136, v126, v121, vcc\n\t"
"v_and_b32 v137, 1, v136\n\t"
"v_lshlrev_b32 v137, 4, v137\n\t"
"v_lshrrev_b32 v136, 1, v136\n\t"
"v_lshlrev_b64 v[134:135], v137, v[134:135]\n\t"
"s_add_u32 m0, s74, 41984\n\t"
"s_add_u32 s27, s72, 442368\n\t"
"buffer_load_dwordx4 v105, s[52:55], s27 offen lds\n\t"
"v_cmp_eq_u32 vcc, 0, v136\n\t"
"s_mov_b32 s16, s80\n\t"
"s_and_b32 s17, s81, 0xffff\n\t"
"v_cndmask_b32 v140, 0, v134, vcc\n\t"
"v_cndmask_b32 v141, 0, v135, vcc\n\t"
"v_cmp_eq_u32 vcc, 1, v136\n\t"
"v_accvgpr_write_b32 a224, v140\n\t"
"v_and_b32 v144, 0xfefffeff, v140\n\t"
"v_cndmask_b32 v141, v141, v134, vcc\n\t"
"v_cndmask_b32 v142, 0, v135, vcc\n\t"
"v_cmp_eq_u32 vcc, 2, v136\n\t"
"v_accvgpr_write_b32 a225, v141\n\t"
"v_xor_b32 v148, 0x80008000, v144\n\t"
"v_cndmask_b32 v142, v142, v134, vcc\n\t"
"v_cndmask_b32 v143, 0, v135, vcc\n\t"
"s_add_u32 m0, s74, 43008\n\t"
"s_add_u32 s27, s72, 442368\n\t"
"buffer_load_dwordx4 v106, s[52:55], s27 offen lds\n\t"
"v_cmp_eq_u32 vcc, 3, v136\n\t"
"v_accvgpr_write_b32 a226, v142\n\t"
"v_and_b32 v145, 0xfefffeff, v141\n\t"
"v_cndmask_b32 v143, v143, v134, vcc\n\t"
"v_accvgpr_write_b32 a227, v143\n\t"
"v_xor_b32 v149, 0x80008000, v145\n\t"
"v_and_b32 v146, 0xfefffeff, v142\n\t"
"v_xor_b32 v150, 0x80008000, v146\n\t"
"v_and_b32 v147, 0xfefffeff, v143\n\t"
"v_xor_b32 v151, 0x80008000, v147\n\t"
"s_mov_b32 s18, 100663296\n\t"
"s_mov_b32 s19, 0x20000\n\t"
"s_add_u32 s22, s23, 3145728\n\t"
"s_mov_b32 s25, 2.0\n\t"
"s_add_u32 m0, s74, 44032\n\t"
"s_add_u32 s27, s72, 454656\n\t"
"buffer_load_dwordx4 v104, s[52:55], s27 offen lds\n\t"
"s_mov_b32 s21, 4\n\t"
"s_mov_b32 s38, -2.0\n\t"
"s_mov_b32 s39, -2.0\n\t"
"s_mov_b32 s68, 3000\n\t"
"s_mul_i32 s61, s73, 3072\n\t"
"v_lshl_add_u32 v248, v204, 4, s61\n\t"
"v_mul_u32_u24 v107, 24, v204\n\t"
"v_add_u32 v47, s75, v107\n\t"
"v_add_u32 v107, s74, v107\n\t"
"v_lshrrev_b32 v112, 4, v204\n\t"
"v_bfe_u32 v113, v204, 2, 2\n\t"
"v_cmp_eq_u32 vcc, v112, v113\n\t"
"v_lshlrev_b32 v112, 4, v204\n\t"
"v_mov_b32 v113, 0x3e00\n\t"
"s_add_u32 m0, s74, 45056\n\t"
"s_add_u32 s27, s72, 454656\n\t"
"buffer_load_dwordx4 v105, s[52:55], s27 offen lds\n\t"
"v_lshlrev_b32 v113, v112, v113\n\t"
"v_cndmask_b32 v113, 0, v113, vcc\n\t"
"v_and_b32 v112, 2, v204\n\t"
"v_cmp_eq_u32 vcc, 0, v112\n\t"
"v_mov_b32 v108, 0\n\t"
"v_mov_b32 v109, 0\n\t"
"v_cndmask_b32 v110, 0, v113, vcc\n\t"
"v_cndmask_b32 v111, v113, 0, vcc\n\t"
"v_xor_b32 v114, 0x80008000, v110\n\t"
"v_xor_b32 v115, 0x80008000, v111\n\t"
"v_mov_b32 v112, 0\n\t"
"v_mov_b32 v113, 0\n\t"
"v_mov_b32 v116, 0\n\t"
"v_mov_b32 v117, 0\n\t"
"s_add_u32 m0, s74, 46080\n\t"
"s_add_u32 s27, s72, 454656\n\t"
"buffer_load_dwordx4 v106, s[52:55], s27 offen lds\n\t"
"s_waitcnt vmcnt(17)\n\t"
"s_waitcnt vmcnt(12)\n\t"
"v_readfirstlane_b32 s71, v187\n\t"
"s_barrier\n\t"
"s_add_u32 s71, s71, 1\n\t"
"v_mov_b32 v215, s71\n\t"
"s_mov_b64 exec, 1\n\t"
"buffer_store_dword v215, off, s[8:11], s70 sc1\n\t"
"s_mov_b64 exec, -1\n\t"
"v_add_u32 v249, 80896, v226\n\t"
"v_xor_b32 v250, 32, v249\n\t"
"v_xor_b32 v251, 64, v249\n\t"
"v_xor_b32 v252, 96, v249\n\t"
"ds_read_b64_tr_b16 a[176:177], v249 offset:0\n\t"
"ds_read_b64_tr_b16 a[178:179], v249 offset:512\n\t"
"ds_read_b64_tr_b16 a[180:181], v250 offset:0\n\t"
"ds_read_b64_tr_b16 a[182:183], v250 offset:512\n\t"
"s_add_u32 m0, s75, 0\n\t"
"s_add_u32 s27, s72, 221184\n\t"
"buffer_load_dwordx4 v104, s[52:55], s27 offen lds\n\t"
"ds_read_b64_tr_b16 a[184:185], v251 offset:2304\n\t"
"ds_read_b64_tr_b16 a[186:187], v251 offset:2816\n\t"
"ds_read_b64_tr_b16 a[188:189], v252 offset:2304\n\t"
"ds_read_b64_tr_b16 a[190:191], v252 offset:2816\n\t"
"s_add_u32 m0, s75, 1024\n\t"
"s_add_u32 s27, s72, 221184\n\t"
"buffer_load_dwordx4 v105, s[52:55], s27 offen lds\n\t"
"ds_read_b64_tr_b16 a[192:193], v251 offset:0\n\t"
"ds_read_b64_tr_b16 a[194:195], v251 offset:512\n\t"
"ds_read_b64_tr_b16 a[196:197], v252 offset:0\n\t"
"ds_read_b64_tr_b16 a[198:199], v252 offset:512\n\t"
"s_add_u32 m0, s75, 2048\n\t"
"s_add_u32 s27, s72, 221184\n\t"
"buffer_load_dwordx4 v106, s[52:55], s27 offen lds\n\t"
"ds_read_b64_tr_b16 a[200:201], v249 offset:2304\n\t"
"ds_read_b64_tr_b16 a[202:203], v249 offset:2816\n\t"
"ds_read_b64_tr_b16 a[204:205], v250 offset:2304\n\t"
"ds_read_b64_tr_b16 a[206:207], v250 offset:2816\n\t"
"s_add_u32 m0, s75, 3072\n\t"
"s_add_u32 s27, s72, 233472\n\t"
"buffer_load_dwordx4 v104, s[52:55], s27 offen lds\n\t"
"s_waitcnt lgkmcnt(14)\n\t"
"v_mfma_f32_16x16x32_bf16 v[0:3], a[176:179], a[220:223], 0\n\t"
"s_waitcnt lgkmcnt(12)\n\t"
"v_mfma_f32_16x16x32_bf16 v[4:7], a[180:183], a[220:223], 0\n\t"
"s_waitcnt lgkmcnt(10)\n\t"
"v_mfma_f32_16x16x32_bf16 v[8:11], a[184:187], a[220:223], 0\n\t"
"s_waitcnt lgkmcnt(8)\n\t"
"v_mfma_f32_16x16x32_bf16 v[12:15], a[188:191], a[220:223], 0\n\t"
"s_add_u32 m0, s75, 4096\n\t"
"s_add_u32 s27, s72, 233472\n\t"
"buffer_load_dwordx4 v105, s[52:55], s27 offen lds\n\t"
"s_add_u32 m0, s75, 5120\n\t"
"s_add_u32 s27, s72, 233472\n\t"
"buffer_load_dwordx4 v106, s[52:55], s27 offen lds\n\t"
"s_waitcnt lgkmcnt(6)\n\t"
"v_mfma_f32_16x16x32_bf16 v[16:19], a[192:195], a[220:223], 0\n\t"
"s_waitcnt lgkmcnt(4)\n\t"
"v_mfma_f32_16x16x32_bf16 v[20:23], a[196:199], a[220:223], 0\n\t"
"s_waitcnt lgkmcnt(2)\n\t"
"v_mfma_f32_16x16x32_bf16 v[24:27], a[200:203], a[220:223], 0\n\t"
"s_waitcnt lgkmcnt(0)\n\t"
"v_mfma_f32_16x16x32_bf16 v[28:31], a[204:207], a[220:223], 0\n\t"
"s_add_u32 m0, s26, 124544\n\t"
"s_nop 0\n\t"
"buffer_load_dwordx4 v236, s[12:15], s23 offen offset:128 nt lds\n\t"
"s_add_u32 m0, s26, 125568\n\t"
"s_nop 0\n\t"
"buffer_load_dwordx4 v237, s[12:15], s23 offen offset:128 nt lds\n\t"
"v_cvt_pk_bf16_f32 v0, v0, v1\n\t"
"v_cvt_pk_bf16_f32 v1, v2, v3\n\t"
"v_cvt_pk_bf16_f32 v2, v4, v5\n\t"
"v_cvt_pk_bf16_f32 v3, v6, v7\n\t"
"ds_write_b128 v217, v[0:3] offset:0\n\t"
"v_cvt_pk_bf16_f32 v8, v8, v9\n\t"
"v_cvt_pk_bf16_f32 v9, v10, v11\n\t"
"v_cvt_pk_bf16_f32 v10, v12, v13\n\t"
"v_cvt_pk_bf16_f32 v11, v14, v15\n\t"
"ds_write_b128 v219, v[8:11] offset:0\n\t"
"v_cvt_pk_bf16_f32 v16, v16, v17\n\t"
"v_cvt_pk_bf16_f32 v17, v18, v19\n\t"
"v_cvt_pk_bf16_f32 v18, v20, v21\n\t"
"v_cvt_pk_bf16_f32 v19, v22, v23\n\t"
"ds_write_b128 v217, v[16:19] offset:8192\n\t"
"v_cvt_pk_bf16_f32 v24, v24, v25\n\t"
"v_cvt_pk_bf16_f32 v25, v26, v27\n\t"
"v_cvt_pk_bf16_f32 v26, v28, v29\n\t"
"v_cvt_pk_bf16_f32 v27, v30, v31\n\t"
"ds_write_b128 v219, v[24:27] offset:8192\n\t"
"v_add_u32 v249, 80896, v227\n\t"
"v_xor_b32 v250, 32, v249\n\t"
"v_xor_b32 v251, 64, v249\n\t"
"v_xor_b32 v252, 96, v249\n\t"
"ds_read_b64_tr_b16 a[176:177], v249 offset:0\n\t"
"ds_read_b64_tr_b16 a[178:179], v249 offset:512\n\t"
"ds_read_b64_tr_b16 a[180:181], v250 offset:0\n\t"
"ds_read_b64_tr_b16 a[182:183], v250 offset:512\n\t"
"s_add_u32 m0, s26, 126592\n\t"
"s_nop 0\n\t"
"buffer_load_dwordx4 v238, s[12:15], s23 offen offset:128 nt lds\n\t"
"ds_read_b64_tr_b16 a[184:185], v251 offset:0\n\t"
"ds_read_b64_tr_b16 a[186:187], v251 offset:512\n\t"
"ds_read_b64_tr_b16 a[188:189], v252 offset:0\n\t"
"ds_read_b64_tr_b16 a[190:191], v252 offset:512\n\t"
"s_add_u32 m0, s26, 127616\n\t"
"s_nop 0\n\t"
"buffer_load_dwordx4 v239, s[12:15], s23 offen offset:128 nt lds\n\t"
"ds_read_b64_tr_b16 a[192:193], v249 offset:1152\n\t"
"ds_read_b64_tr_b16 a[194:195], v249 offset:1664\n\t"
"ds_read_b64_tr_b16 a[196:197], v250 offset:1152\n\t"
"ds_read_b64_tr_b16 a[198:199], v250 offset:1664\n\t"
"s_add_u32 m0, s26, 128640\n\t"
"s_mov_b64 exec, s[36:37]\n\t"
"buffer_load_dwordx4 v240, s[12:15], s23 offen offset:128 nt lds\n\t"
"s_mov_b64 exec, -1\n\t"
"ds_read_b64_tr_b16 a[200:201], v251 offset:1152\n\t"
"ds_read_b64_tr_b16 a[202:203], v251 offset:1664\n\t"
"ds_read_b64_tr_b16 a[204:205], v252 offset:1152\n\t"
"ds_read_b64_tr_b16 a[206:207], v252 offset:1664\n\t"
"s_add_u32 m0, s43, 122240\n\t"
"s_mov_b64 exec, s[44:45]\n\t"
"buffer_load_dwordx4 v246, s[12:15], 0 offen offset:128 nt lds\n\t"
"s_mov_b64 exec, -1\n\t"
"s_waitcnt vmcnt(19)\n\t"
"ds_read_b64_tr_b16 a[48:49], v107 offset:16384\n\t"
"ds_read_b64_tr_b16 a[50:51], v107 offset:17920\n\t"
"ds_read_b64_tr_b16 a[52:53], v107 offset:19456\n\t"
"ds_read_b64_tr_b16 a[54:55], v107 offset:20992\n\t"
"ds_read_b64_tr_b16 a[56:57], v107 offset:16392\n\t"
"ds_read_b64_tr_b16 a[58:59], v107 offset:17928\n\t"
"ds_read_b64_tr_b16 a[60:61], v107 offset:19464\n\t"
"ds_read_b64_tr_b16 a[62:63], v107 offset:21000\n\t"
"ds_read_b64_tr_b16 a[64:65], v107 offset:16400\n\t"
"ds_read_b64_tr_b16 a[66:67], v107 offset:17936\n\t"
"ds_read_b64_tr_b16 a[68:69], v107 offset:19472\n\t"
"ds_read_b64_tr_b16 a[70:71], v107 offset:21008\n\t"
"s_waitcnt vmcnt(13)\n\t"
"ds_read_b64_tr_b16 a[72:73], v107 offset:40960\n\t"
"ds_read_b64_tr_b16 a[74:75], v107 offset:42496\n\t"
"ds_read_b64_tr_b16 a[76:77], v107 offset:44032\n\t"
"ds_read_b64_tr_b16 a[78:79], v107 offset:45568\n\t"
"ds_read_b64_tr_b16 a[80:81], v107 offset:40968\n\t"
"ds_read_b64_tr_b16 a[82:83], v107 offset:42504\n\t"
"ds_read_b64_tr_b16 a[84:85], v107 offset:44040\n\t"
"ds_read_b64_tr_b16 a[86:87], v107 offset:45576\n\t"
"ds_read_b64_tr_b16 a[88:89], v107 offset:40976\n\t"
"ds_read_b64_tr_b16 a[90:91], v107 offset:42512\n\t"
"ds_read_b64_tr_b16 a[92:93], v107 offset:44048\n\t"
"ds_read_b64_tr_b16 a[94:95], v107 offset:45584\n\t"
"s_waitcnt lgkmcnt(15)\n\t"
"v_mfma_f32_16x16x32_bf16 v[0:3], v[110:113], a[48:51], 0\n\t"
"s_waitcnt lgkmcnt(10)\n\t"
"v_mfma_f32_16x16x32_bf16 v[0:3], v[110:113], a[72:75], v[0:3]\n\t"
"v_mfma_f32_16x16x32_bf16 v[8:11], v[108:111], a[48:51], 0\n\t"
"v_mfma_f32_16x16x32_bf16 v[8:11], v[108:111], a[72:75], v[8:11]\n\t"
"s_add_u32 m0, s26, 103936\n\t"
"s_nop 0\n\t"
"buffer_load_dwordx4 v241, s[12:15], s23 offen offset:0 nt lds\n\t"
"v_mfma_f32_16x16x32_bf16 v[16:19], v[110:113], a[52:55], 0\n\t"
"s_waitcnt lgkmcnt(8)\n\t"
"v_mfma_f32_16x16x32_bf16 v[16:19], v[110:113], a[76:79], v[16:19]\n\t"
"v_mfma_f32_16x16x32_bf16 v[24:27], v[108:111], a[52:55], 0\n\t"
"v_mfma_f32_16x16x32_bf16 v[24:27], v[108:111], a[76:79], v[24:27]\n\t"
"s_add_u32 m0, s26, 104960\n\t"
"s_nop 0\n\t"
"buffer_load_dwordx4 v242, s[12:15], s23 offen offset:0 nt lds\n\t"
"s_waitcnt vmcnt(8)\n\t"
"ds_read_b64_tr_b16 a[0:1], v47 offset:0\n\t"
"ds_read_b64_tr_b16 a[2:3], v47 offset:1536\n\t"
"ds_read_b64_tr_b16 a[4:5], v47 offset:3072\n\t"
"ds_read_b64_tr_b16 a[6:7], v47 offset:4608\n\t"
"ds_read_b64_tr_b16 a[8:9], v47 offset:8\n\t"
"ds_read_b64_tr_b16 a[10:11], v47 offset:1544\n\t"
"ds_read_b64_tr_b16 a[12:13], v47 offset:3080\n\t"
"ds_read_b64_tr_b16 a[14:15], v47 offset:4616\n\t"
"ds_read_b64_tr_b16 a[16:17], v47 offset:16\n\t"
"ds_read_b64_tr_b16 a[18:19], v47 offset:1552\n\t"
"ds_read_b64_tr_b16 a[20:21], v47 offset:3088\n\t"
"ds_read_b64_tr_b16 a[22:23], v47 offset:4624\n\t"
"v_mfma_f32_16x16x32_bf16 v[32:35], v[110:113], a[56:59], 0\n\t"
"s_waitcnt lgkmcnt(15)\n\t"
"v_mfma_f32_16x16x32_bf16 v[32:35], v[110:113], a[80:83], v[32:35]\n\t"
"v_mfma_f32_16x16x32_bf16 v[96:99], v[108:111], a[56:59], 0\n\t"
"v_mfma_f32_16x16x32_bf16 v[96:99], v[108:111], a[80:83], v[96:99]\n\t"
"s_add_u32 m0, s26, 105984\n\t"
"s_nop 0\n\t"
"buffer_load_dwordx4 v243, s[12:15], s23 offen offset:0 nt lds\n\t"
"v_mfma_f32_16x16x32_bf16 v[120:123], v[110:113], a[60:63], 0\n\t"
"v_mfma_f32_16x16x32_bf16 v[120:123], v[110:113], a[84:87], v[120:123]\n\t"
"v_mfma_f32_16x16x32_bf16 v[128:131], v[108:111], a[60:63], 0\n\t"
"v_mfma_f32_16x16x32_bf16 v[128:131], v[108:111], a[84:87], v[128:131]\n\t"
"s_add_u32 m0, s26, 107008\n\t"
"s_nop 0\n\t"
"buffer_load_dwordx4 v244, s[12:15], s23 offen offset:0 nt lds\n\t"
"v_mfma_f32_16x16x32_bf16 v[136:139], v[110:113], a[64:67], 0\n\t"
"s_waitcnt lgkmcnt(14)\n\t"
"v_mfma_f32_16x16x32_bf16 v[136:139], v[110:113], a[88:91], v[136:139]\n\t"
"v_mfma_f32_16x16x32_bf16 v[152:155], v[108:111], a[64:67], 0\n\t"
"v_mfma_f32_16x16x32_bf16 v[152:155], v[108:111], a[88:91], v[152:155]\n\t"
"s_add_u32 m0, s26, 108032\n\t"
"s_mov_b64 exec, s[36:37]\n\t"
"buffer_load_dwordx4 v245, s[12:15], s23 offen offset:0 nt lds\n\t"
"s_mov_b64 exec, -1\n\t"
"v_mfma_f32_16x16x32_bf16 v[160:163], v[110:113], a[68:71], 0\n\t"
"s_waitcnt lgkmcnt(12)\n\t"
"v_mfma_f32_16x16x32_bf16 v[160:163], v[110:113], a[92:95], v[160:163]\n\t"
"v_mfma_f32_16x16x32_bf16 v[168:171], v[108:111], a[68:71], 0\n\t"
"v_mfma_f32_16x16x32_bf16 v[168:171], v[108:111], a[92:95], v[168:171]\n\t"
"s_add_u32 m0, s43, 101632\n\t"
"s_mov_b64 exec, s[44:45]\n\t"
"buffer_load_dwordx4 v247, s[12:15], 0 offen offset:0 nt lds\n\t"
"s_mov_b64 exec, -1\n\t"
"s_waitcnt lgkmcnt(10)\n\t"
"v_mfma_f32_16x16x32_bf16 v[4:7], v[110:113], a[0:3], v[0:3]\n\t"
"v_mfma_f32_16x16x32_bf16 v[0:3], v[114:117], a[0:3], v[0:3]\n\t"
"v_mfma_f32_16x16x32_bf16 v[12:15], v[108:111], a[0:3], v[8:11]\n\t"
"v_mfma_f32_16x16x32_bf16 v[8:11], v[112:115], a[0:3], v[8:11]\n\t"
"s_waitcnt lgkmcnt(8)\n\t"
"v_mfma_f32_16x16x32_bf16 v[20:23], v[110:113], a[4:7], v[16:19]\n\t"
"v_mfma_f32_16x16x32_bf16 v[16:19], v[114:117], a[4:7], v[16:19]\n\t"
"v_mfma_f32_16x16x32_bf16 v[28:31], v[108:111], a[4:7], v[24:27]\n\t"
"s_nop 0\n\t"
"v_cvt_pk_bf16_f32 v48, v4, v5\n\t"
"v_cvt_pk_bf16_f32 v49, v6, v7\n\t"
"v_mfma_f32_16x16x32_bf16 v[24:27], v[112:115], a[4:7], v[24:27]\n\t"
"v_cvt_pk_bf16_f32 v72, v0, v1\n\t"
"v_cvt_pk_bf16_f32 v73, v2, v3\n\t"
"s_waitcnt lgkmcnt(6)\n\t"
"v_mfma_f32_16x16x32_bf16 v[36:39], v[110:113], a[8:11], v[32:35]\n\t"
"v_cvt_pk_bf16_f32 v50, v12, v13\n\t"
"v_cvt_pk_bf16_f32 v51, v14, v15\n\t"
"v_mfma_f32_16x16x32_bf16 v[32:35], v[114:117], a[8:11], v[32:35]\n\t"
"v_cvt_pk_bf16_f32 v74, v8, v9\n\t"
"v_cvt_pk_bf16_f32 v75, v10, v11\n\t"
"v_mfma_f32_16x16x32_bf16 v[100:103], v[108:111], a[8:11], v[96:99]\n\t"
"v_cvt_pk_bf16_f32 v52, v20, v21\n\t"
"v_cvt_pk_bf16_f32 v53, v22, v23\n\t"
"v_mfma_f32_16x16x32_bf16 v[96:99], v[112:115], a[8:11], v[96:99]\n\t"
"v_cvt_pk_bf16_f32 v76, v16, v17\n\t"
"v_cvt_pk_bf16_f32 v77, v18, v19\n\t"
"s_waitcnt lgkmcnt(4)\n\t"
"v_mfma_f32_16x16x32_bf16 v[124:127], v[110:113], a[12:15], v[120:123]\n\t"
"v_cvt_pk_bf16_f32 v54, v28, v29\n\t"
"v_cvt_pk_bf16_f32 v55, v30, v31\n\t"
"v_mfma_f32_16x16x32_bf16 v[120:123], v[114:117], a[12:15], v[120:123]\n\t"
"v_cvt_pk_bf16_f32 v78, v24, v25\n\t"
"v_cvt_pk_bf16_f32 v79, v26, v27\n\t"
"v_mfma_f32_16x16x32_bf16 v[132:135], v[108:111], a[12:15], v[128:131]\n\t"
"v_cvt_pk_bf16_f32 v56, v36, v37\n\t"
"v_cvt_pk_bf16_f32 v57, v38, v39\n\t"
"v_mfma_f32_16x16x32_bf16 v[128:131], v[112:115], a[12:15], v[128:131]\n\t"
"v_cvt_pk_bf16_f32 v80, v32, v33\n\t"
"v_cvt_pk_bf16_f32 v81, v34, v35\n\t"
"s_waitcnt lgkmcnt(2)\n\t"
"v_mfma_f32_16x16x32_bf16 v[140:143], v[110:113], a[16:19], v[136:139]\n\t"
"v_cvt_pk_bf16_f32 v58, v100, v101\n\t"
"v_cvt_pk_bf16_f32 v59, v102, v103\n\t"
"v_mfma_f32_16x16x32_bf16 v[136:139], v[114:117], a[16:19], v[136:139]\n\t"
"v_cvt_pk_bf16_f32 v82, v96, v97\n\t"
"v_cvt_pk_bf16_f32 v83, v98, v99\n\t"
"v_mfma_f32_16x16x32_bf16 v[156:159], v[108:111], a[16:19], v[152:155]\n\t"
"v_cvt_pk_bf16_f32 v60, v124, v125\n\t"
"v_cvt_pk_bf16_f32 v61, v126, v127\n\t"
"v_mfma_f32_16x16x32_bf16 v[152:155], v[112:115], a[16:19], v[152:155]\n\t"
"v_cvt_pk_bf16_f32 v84, v120, v121\n\t"
"v_cvt_pk_bf16_f32 v85, v122, v123\n\t"
"s_waitcnt lgkmcnt(0)\n\t"
"v_mfma_f32_16x16x32_bf16 v[164:167], v[110:113], a[20:23], v[160:163]\n\t"
"v_cvt_pk_bf16_f32 v62, v132, v133\n\t"
"v_cvt_pk_bf16_f32 v63, v134, v135\n\t"
"v_mfma_f32_16x16x32_bf16 v[160:163], v[114:117], a[20:23], v[160:163]\n\t"
"v_cvt_pk_bf16_f32 v86, v128, v129\n\t"
"v_cvt_pk_bf16_f32 v87, v130, v131\n\t"
"v_mfma_f32_16x16x32_bf16 v[172:175], v[108:111], a[20:23], v[168:171]\n\t"
"v_cvt_pk_bf16_f32 v64, v140, v141\n\t"
"v_cvt_pk_bf16_f32 v65, v142, v143\n\t"
"v_mfma_f32_16x16x32_bf16 v[168:171], v[112:115], a[20:23], v[168:171]\n\t"
"v_cvt_pk_bf16_f32 v88, v136, v137\n\t"
"v_cvt_pk_bf16_f32 v89, v138, v139\n\t"
"v_cvt_pk_bf16_f32 v66, v156, v157\n\t"
"v_cvt_pk_bf16_f32 v67, v158, v159\n\t"
"v_cvt_pk_bf16_f32 v90, v152, v153\n\t"
"v_cvt_pk_bf16_f32 v91, v154, v155\n\t"
"v_cvt_pk_bf16_f32 v68, v164, v165\n\t"
"v_cvt_pk_bf16_f32 v69, v166, v167\n\t"
"v_cvt_pk_bf16_f32 v92, v160, v161\n\t"
"v_cvt_pk_bf16_f32 v93, v162, v163\n\t"
"v_cvt_pk_bf16_f32 v70, v172, v173\n\t"
"v_cvt_pk_bf16_f32 v71, v174, v175\n\t"
"v_cvt_pk_bf16_f32 v94, v168, v169\n\t"
"v_cvt_pk_bf16_f32 v95, v170, v171\n\t"
"s_barrier\n\t"
"v_mfma_f32_16x16x32_bf16 v[192:195], a[176:179], v[144:147], 0\n\t"
"ds_read_b128 a[144:147], v216 offset:0\n\t"
"v_add_u32 v249, 122368, v227\n\t"
"v_xor_b32 v250, 32, v249\n\t"
"v_xor_b32 v251, 64, v249\n\t"
"v_xor_b32 v252, 96, v249\n\t"
"v_mfma_f32_16x16x32_bf16 v[196:199], a[180:183], v[144:147], 0\n\t"
"ds_read_b128 a[160:163], v216 offset:8192\n\t"
"ds_read_b128 a[148:151], v216 offset:1024\n\t"
"v_mfma_f32_16x16x32_bf16 v[200:203], a[184:187], v[144:147], 0\n\t"
"ds_read_b128 a[164:167], v216 offset:9216\n\t"
"ds_read_b128 a[152:155], v216 offset:2048\n\t"
"v_mfma_f32_16x16x32_bf16 v[204:207], a[188:191], v[144:147], 0\n\t"
"ds_read_b128 a[168:171], v216 offset:10240\n\t"
"ds_read_b128 a[156:159], v216 offset:3072\n\t"
"v_mfma_f32_16x16x32_bf16 v[208:211], a[192:195], v[148:151], 0\n\t"
"ds_read_b128 a[172:175], v216 offset:11264\n\t"
"v_cvt_pk_bf16_f32 v192, v192, v193\n\t"
"v_mfma_f32_16x16x32_bf16 v[212:215], a[196:199], v[148:151], 0\n\t"
"v_cvt_pk_bf16_f32 v193, v194, v195\n\t"
"v_cvt_pk_bf16_f32 v194, v196, v197\n\t"
"s_waitcnt lgkmcnt(6)\n\t"
"v_mfma_f32_16x16x32_bf16 v[96:99], v[48:51], a[144:147], 0\n\t"
"v_cvt_pk_bf16_f32 v195, v198, v199\n\t"
"v_cvt_pk_bf16_f32 v200, v200, v201\n\t"
"v_mfma_f32_16x16x32_bf16 v[96:99], v[52:55], a[160:163], v[96:99]\n\t"
"v_cvt_pk_bf16_f32 v201, v202, v203\n\t"
"v_cvt_pk_bf16_f32 v202, v204, v205\n\t"
"v_cvt_pk_bf16_f32 v203, v206, v207\n\t"
"v_mbcnt_lo_u32_b32 v176, -1, 0\n\t"
"v_mfma_f32_16x16x32_bf16 v[112:115], v[56:59], a[144:147], 0\n\t"
"v_cvt_pk_bf16_f32 v208, v208, v209\n\t"
"v_cvt_pk_bf16_f32 v209, v210, v211\n\t"
"v_mfma_f32_16x16x32_bf16 v[112:115], v[60:63], a[160:163], v[112:115]\n\t"
"ds_write_b128 v221, v[192:195] offset:16384\n\t"
"v_cvt_pk_bf16_f32 v210, v212, v213\n\t"
"v_cvt_pk_bf16_f32 v211, v214, v215\n\t"
"v_mfma_f32_16x16x32_bf16 v[128:131], v[64:67], a[144:147], 0\n\t"
"ds_read_b128 a[144:147], v216 offset:4096\n\t"
"v_mbcnt_hi_u32_b32 v176, -1, v176\n\t"
"v_mfma_f32_16x16x32_bf16 v[128:131], v[68:71], a[160:163], v[128:131]\n\t"
"buffer_load_dword v190, v189, s[8:11], 0 offen sc1\n\t"
"ds_read_b128 a[160:163], v216 offset:12288\n\t"
"v_and_b32 v153, 7, v176\n\t"
"s_waitcnt lgkmcnt(7)\n\t"
"v_mfma_f32_16x16x32_bf16 v[132:135], v[64:67], a[148:151], 0\n\t"
"v_and_b32 v158, 24, v176\n\t"
"v_cmp_eq_u32 vcc, 8, v158\n\t"
"v_mfma_f32_16x16x32_bf16 v[132:135], v[68:71], a[164:167], v[132:135]\n\t"
"ds_write_b128 v221, v[200:203] offset:24576\n\t"
"ds_write_b128 v221, v[208:211] offset:20480\n\t"
"v_mfma_f32_16x16x32_bf16 v[116:119], v[56:59], a[148:151], 0\n\t"
"v_mov_b32 v166, 0x3f80\n\t"
"v_mov_b32 v167, 0xbf80\n\t"
"v_mfma_f32_16x16x32_bf16 v[116:119], v[60:63], a[164:167], v[116:119]\n\t"
"v_cndmask_b32 v166, v166, v167, vcc\n\t"
"v_mov_b32 v168, v153\n\t"
"v_mov_b32 v167, 0\n\t"
"v_and_b32 v169, 1, v168\n\t"
"s_waitcnt vmcnt(7)\n\t"
"s_barrier\n\t"
"v_mfma_f32_16x16x32_bf16 v[192:195], a[200:203], v[148:151], 0\n\t"
"ds_read_b64_tr_b16 a[176:177], v249 offset:0\n\t"
"ds_read_b64_tr_b16 a[178:179], v249 offset:512\n\t"
"v_mfma_f32_16x16x32_bf16 v[100:103], v[48:51], a[148:151], 0\n\t"
"ds_read_b128 a[148:151], v216 offset:5120\n\t"
"v_lshlrev_b32 v169, 4, v169\n\t"
"v_mfma_f32_16x16x32_bf16 v[100:103], v[52:55], a[164:167], v[100:103]\n\t"
"ds_read_b128 a[164:167], v216 offset:13312\n\t"
"ds_read_b64_tr_b16 a[180:181], v250 offset:0\n\t"
"ds_read_b64_tr_b16 a[182:183], v250 offset:512\n\t"
"v_lshrrev_b32 v168, 1, v168\n\t"
"s_waitcnt lgkmcnt(13)\n\t"
"v_mfma_f32_16x16x32_bf16 v[104:107], v[48:51], a[152:155], 0\n\t"
"ds_read_b64_tr_b16 a[184:185], v251 offset:0\n\t"
"v_lshlrev_b64 v[166:167], v169, v[166:167]\n\t"
"v_mfma_f32_16x16x32_bf16 v[104:107], v[52:55], a[168:171], v[104:107]\n\t"
"v_cvt_pk_bf16_f32 v192, v192, v193\n\t"
"v_cvt_pk_bf16_f32 v193, v194, v195\n\t"
"ds_read_b64_tr_b16 a[186:187], v251 offset:512\n\t"
"ds_read_b64_tr_b16 a[188:189], v252 offset:0\n\t"
"v_mfma_f32_16x16x32_bf16 v[196:199], a[204:207], v[148:151], 0\n\t"
"ds_read_b64_tr_b16 a[190:191], v252 offset:512\n\t"
"ds_read_b64_tr_b16 a[192:193], v249 offset:1152\n\t"
"ds_read_b64_tr_b16 a[194:195], v249 offset:1664\n\t"
"v_cmp_eq_u32 vcc, 0, v168\n\t"
"v_mfma_f32_16x16x32_bf16 v[120:123], v[56:59], a[152:155], 0\n\t"
"ds_read_b64_tr_b16 a[196:197], v250 offset:1152\n\t"
"v_cndmask_b32 v172, 0, v166, vcc\n\t"
"v_mfma_f32_16x16x32_bf16 v[120:123], v[60:63], a[168:171], v[120:123]\n\t"
"ds_read_b64_tr_b16 a[198:199], v250 offset:1664\n\t"
"ds_read_b64_tr_b16 a[200:201], v251 offset:1152\n\t"
"ds_read_b64_tr_b16 a[202:203], v251 offset:1664\n\t"
"v_accvgpr_write_b32 a212, v172\n\t"
"v_mfma_f32_16x16x32_bf16 v[136:139], v[64:67], a[152:155], 0\n\t"
"ds_read_b64_tr_b16 a[204:205], v252 offset:1152\n\t"
"v_cndmask_b32 v173, 0, v167, vcc\n\t"
"v_mfma_f32_16x16x32_bf16 v[136:139], v[68:71], a[168:171], v[136:139]\n\t"
"v_cvt_pk_bf16_f32 v194, v196, v197\n\t"
"v_cvt_pk_bf16_f32 v195, v198, v199\n\t"
"ds_read_b64_tr_b16 a[206:207], v252 offset:1664\n\t"
"ds_read_b128 a[152:155], v216 offset:6144\n\t"
"s_waitcnt lgkmcnt(15)\n\t"
"v_mfma_f32_16x16x32_bf16 v[140:143], v[64:67], a[156:159], 0\n\t"
"ds_read_b128 a[168:171], v216 offset:14336\n\t"
"v_cmp_eq_u32 vcc, 1, v168\n\t"
"v_mfma_f32_16x16x32_bf16 v[140:143], v[68:71], a[172:175], v[140:143]\n\t"
"ds_write_b128 v221, v[192:195] offset:28672\n\t"
"v_cndmask_b32 v173, v173, v166, vcc\n\t"
"v_accvgpr_write_b32 a213, v173\n\t"
"v_mfma_f32_16x16x32_bf16 v[124:127], v[56:59], a[156:159], 0\n\t"
"v_cndmask_b32 v174, 0, v167, vcc\n\t"
"v_cmp_eq_u32 vcc, 2, v168\n\t"
"v_mfma_f32_16x16x32_bf16 v[124:127], v[60:63], a[172:175], v[124:127]\n\t"
"s_nop 0\n\t"
"v_cndmask_b32 v174, v174, v166, vcc\n\t"
"v_accvgpr_write_b32 a214, v174\n\t"
"v_cndmask_b32 v175, 0, v167, vcc\n\t"
"v_cmp_eq_u32 vcc, 3, v168\n\t"
"v_mfma_f32_16x16x32_bf16 v[108:111], v[48:51], a[156:159], 0\n\t"
"ds_read_b128 a[156:159], v216 offset:7168\n\t"
"v_cndmask_b32 v175, v175, v166, vcc\n\t"
"v_mfma_f32_16x16x32_bf16 v[108:111], v[52:55], a[172:175], v[108:111]\n\t"
"buffer_load_dword v191, v189, s[8:11], 0 offen sc1\n\t"
"ds_read_b128 a[172:175], v216 offset:15360\n\t"
"v_accvgpr_write_b32 a215, v175\n\t"
"v_mfma_f32_16x16x32_bf16 v[0:3], v[72:75], a[144:147], v[96:99]\n\t"
"v_cmp_gt_u32 vcc, 32, v176\n\t"
"v_lshlrev_b32 v166, 8, v172\n\t"
"v_mfma_f32_16x16x32_bf16 v[0:3], v[76:79], a[160:163], v[0:3]\n\t"
"v_and_b32 v166, 0x80008000, v166\n\t"
"v_xor_b32 v166, v166, v172\n\t"
"v_cndmask_b32 v167, v166, v172, vcc\n\t"
"v_cndmask_b32 v166, v172, v166, vcc\n\t"
"v_mfma_f32_16x16x32_bf16 v[16:19], v[80:83], a[144:147], v[112:115]\n\t"
"v_accvgpr_write_b32 a208, v167\n\t"
"v_accvgpr_write_b32 a216, v166\n\t"
"v_mfma_f32_16x16x32_bf16 v[16:19], v[84:87], a[160:163], v[16:19]\n\t"
"v_lshlrev_b32 v166, 8, v173\n\t"
"v_and_b32 v166, 0x80008000, v166\n\t"
"v_xor_b32 v166, v166, v173\n\t"
"v_cndmask_b32 v167, v166, v173, vcc\n\t"
"v_mfma_f32_16x16x32_bf16 v[32:35], v[88:91], a[144:147], v[128:131]\n\t"
"v_cndmask_b32 v166, v173, v166, vcc\n\t"
"v_accvgpr_write_b32 a209, v167\n\t"
"v_mfma_f32_16x16x32_bf16 v[32:35], v[92:95], a[160:163], v[32:35]\n\t"
"v_accvgpr_write_b32 a217, v166\n\t"
"v_lshlrev_b32 v166, 8, v174\n\t"
"v_and_b32 v166, 0x80008000, v166\n\t"
"v_xor_b32 v166, v166, v174\n\t"
"v_mfma_f32_16x16x32_bf16 v[36:39], v[88:91], a[148:151], v[132:135]\n\t"
"v_cndmask_b32 v167, v166, v174, vcc\n\t"
"v_cndmask_b32 v166, v174, v166, vcc\n\t"
"v_mfma_f32_16x16x32_bf16 v[36:39], v[92:95], a[164:167], v[36:39]\n\t"
"v_accvgpr_write_b32 a210, v167\n\t"
"v_accvgpr_write_b32 a218, v166\n\t"
"v_lshlrev_b32 v166, 8, v175\n\t"
"v_and_b32 v166, 0x80008000, v166\n\t"
"v_mfma_f32_16x16x32_bf16 v[20:23], v[80:83], a[148:151], v[116:119]\n\t"
"v_xor_b32 v166, v166, v175\n\t"
"v_cndmask_b32 v167, v166, v175, vcc\n\t"
"v_mfma_f32_16x16x32_bf16 v[20:23], v[84:87], a[164:167], v[20:23]\n\t"
"v_cndmask_b32 v166, v175, v166, vcc\n\t"
"v_accvgpr_write_b32 a211, v167\n\t"
"v_accvgpr_write_b32 a219, v166\n\t"
"v_mfma_f32_16x16x32_bf16 v[4:7], v[72:75], a[148:151], v[100:103]\n\t"
"v_mfma_f32_16x16x32_bf16 v[4:7], v[76:79], a[164:167], v[4:7]\n\t"
"s_waitcnt lgkmcnt(0)\n\t"
"s_waitcnt vmcnt(1)\n\t"
"v_cmp_eq_u32 vcc, v190, v188\n\t"
"s_nop 4\n\t"
"s_cmp_eq_u64 vcc, 0\n\t"
"s_cbranch_scc1 Lgo_%=\n\t"
"s_sub_u32 s68, s68, 1\n\t"
"s_waitcnt vmcnt(0)\n\t"
"Lchk_%=:\n\t"
"v_cmp_eq_u32 vcc, v191, v188\n\t"
"s_nop 4\n\t"
"s_cmp_eq_u64 vcc, 0\n\t"
"s_cbranch_scc1 Lgo_%=\n\t"
"s_sub_u32 s68, s68, 1\n\t"
"s_cmp_eq_u32 s68, 0\n\t"
"s_cbranch_scc1 Lfb_%=\n\t"
"buffer_load_dword v191, v189, s[8:11], 0 offen sc1\n\t"
"s_waitcnt vmcnt(0)\n\t"
"s_branch Lchk_%=\n\t"
"Lgo_%=:\n\t"
"s_barrier\n\t"
"v_mfma_f32_16x16x32_bf16 v[8:11], v[72:75], a[152:155], v[104:107]\n\t"
"ds_read_b128 a[144:147], v216 offset:16384\n\t"
"v_add_u32 v249, 80896, v226\n\t"
"v_mfma_f32_16x16x32_bf16 v[8:11], v[76:79], a[168:171], v[8:11]\n\t"
"ds_read_b128 a[160:163], v216 offset:24576\n\t"
"ds_read_b128 a[148:151], v216 offset:17408\n\t"
"v_mfma_f32_16x16x32_bf16 v[24:27], v[80:83], a[152:155], v[120:123]\n\t"
"ds_read_b128 a[164:167], v216 offset:25600\n\t"
"v_xor_b32 v250, 32, v249\n\t"
"v_mfma_f32_16x16x32_bf16 v[24:27], v[84:87], a[168:171], v[24:27]\n\t"
"s_mov_b32 s20, 393216\n\t"
"buffer_load_dwordx4 a[96:99], v248, s[8:11], s20 offen offset:0 sc1\n\t"
"v_mfma_f32_16x16x32_bf16 v[40:43], v[88:91], a[152:155], v[136:139]\n\t"
"v_xor_b32 v251, 64, v249\n\t"
"v_xor_b32 v252, 96, v249\n\t"
"v_mfma_f32_16x16x32_bf16 v[40:43], v[92:95], a[168:171], v[40:43]\n\t"
"buffer_load_dwordx4 a[100:103], v248, s[8:11], s20 offen offset:1024 sc1\n\t"
"buffer_load_dwordx4 a[104:107], v248, s[8:11], s20 offen offset:2048 sc1\n\t"
"v_mfma_f32_16x16x32_bf16 v[200:203], a[176:179], a[224:227], 0\n\t"
"s_mov_b32 s20, 405504\n\t"
"buffer_load_dwordx4 a[108:111], v248, s[8:11], s20 offen offset:0 sc1\n\t"
"buffer_load_dwordx4 a[112:115], v248, s[8:11], s20 offen offset:1024 sc1\n\t"
"v_mfma_f32_16x16x32_bf16 v[44:47], v[88:91], a[156:159], v[140:143]\n\t"
"ds_read_b128 a[152:155], v216 offset:18432\n\t"
"v_mbcnt_lo_u32_b32 v176, -1, 0\n\t"
"v_mfma_f32_16x16x32_bf16 v[44:47], v[92:95], a[172:175], v[44:47]\n\t"
"buffer_load_dwordx4 a[116:119], v248, s[8:11], s20 offen offset:2048 sc1\n\t"
"s_mov_b32 s20, 688128\n\t"
"buffer_load_dwordx4 a[120:123], v248, s[8:11], s20 offen offset:0 sc1\n\t"
"v_mfma_f32_16x16x32_bf16 v[204:207], a[180:183], a[224:227], 0\n\t"
"buffer_load_dwordx4 a[124:127], v248, s[8:11], s20 offen offset:1024 sc1\n\t"
"v_mfma_f32_16x16x32_bf16 v[28:31], v[80:83], a[156:159], v[124:127]\n\t"
"ds_read_b128 a[168:171], v216 offset:26624\n\t"
"v_cvt_pk_bf16_f32 v200, v200, v201\n\t"
"v_mfma_f32_16x16x32_bf16 v[28:31], v[84:87], a[172:175], v[28:31]\n\t"
"buffer_load_dwordx4 a[128:131], v248, s[8:11], s20 offen offset:2048 sc1\n\t"
"s_mov_b32 s20, 700416\n\t"
"buffer_load_dwordx4 a[132:135], v248, s[8:11], s20 offen offset:0 sc1\n\t"
"v_mfma_f32_16x16x32_bf16 v[12:15], v[72:75], a[156:159], v[108:111]\n\t"
"v_cvt_pk_bf16_f32 v201, v202, v203\n\t"
"ds_read_b128 a[156:159], v216 offset:19456\n\t"
"v_mfma_f32_16x16x32_bf16 v[12:15], v[76:79], a[172:175], v[12:15]\n\t"
"buffer_load_dwordx4 a[136:139], v248, s[8:11], s20 offen offset:1024 sc1\n\t"
"buffer_load_dwordx4 a[140:143], v248, s[8:11], s20 offen offset:2048 sc1\n\t"
"v_mfma_f32_16x16x32_bf16 v[208:211], a[184:187], a[224:227], 0\n\t"
"v_pk_fma_f32 v[96:97], v[96:97], s[38:39], v[0:1] neg_lo:[1,0,1] neg_hi:[1,0,1]\n\t"
"v_pk_fma_f32 v[98:99], v[98:99], s[38:39], v[2:3] neg_lo:[1,0,1] neg_hi:[1,0,1]\n\t"
"v_pk_fma_f32 v[112:113], v[112:113], s[38:39], v[16:17] neg_lo:[1,0,1] neg_hi:[1,0,1]\n\t"
"v_pk_fma_f32 v[114:115], v[114:115], s[38:39], v[18:19] neg_lo:[1,0,1] neg_hi:[1,0,1]\n\t"
"v_pk_fma_f32 v[128:129], v[128:129], s[38:39], v[32:33] neg_lo:[1,0,1] neg_hi:[1,0,1]\n\t"
"v_pk_fma_f32 v[130:131], v[130:131], s[38:39], v[34:35] neg_lo:[1,0,1] neg_hi:[1,0,1]\n\t"
"v_pk_fma_f32 v[132:133], v[132:133], s[38:39], v[36:37] neg_lo:[1,0,1] neg_hi:[1,0,1]\n\t"
"v_pk_fma_f32 v[134:135], v[134:135], s[38:39], v[38:39] neg_lo:[1,0,1] neg_hi:[1,0,1]\n\t"
"v_pk_fma_f32 v[116:117], v[116:117], s[38:39], v[20:21] neg_lo:[1,0,1] neg_hi:[1,0,1]\n\t"
"v_pk_fma_f32 v[118:119], v[118:119], s[38:39], v[22:23] neg_lo:[1,0,1] neg_hi:[1,0,1]\n\t"
"v_pk_fma_f32 v[100:101], v[100:101], s[38:39], v[4:5] neg_lo:[1,0,1] neg_hi:[1,0,1]\n\t"
"v_pk_fma_f32 v[102:103], v[102:103], s[38:39], v[6:7] neg_lo:[1,0,1] neg_hi:[1,0,1]\n\t"
"v_pk_fma_f32 v[104:105], v[104:105], s[38:39], v[8:9] neg_lo:[1,0,1] neg_hi:[1,0,1]\n\t"
"v_pk_fma_f32 v[106:107], v[106:107], s[38:39], v[10:11] neg_lo:[1,0,1] neg_hi:[1,0,1]\n\t"
"v_pk_fma_f32 v[120:121], v[120:121], s[38:39], v[24:25] neg_lo:[1,0,1] neg_hi:[1,0,1]\n\t"
"v_pk_fma_f32 v[122:123], v[122:123], s[38:39], v[26:27] neg_lo:[1,0,1] neg_hi:[1,0,1]\n\t"
"v_pk_fma_f32 v[136:137], v[136:137], s[38:39], v[40:41] neg_lo:[1,0,1] neg_hi:[1,0,1]\n\t"
"v_pk_fma_f32 v[138:139], v[138:139], s[38:39], v[42:43] neg_lo:[1,0,1] neg_hi:[1,0,1]\n\t"
"v_pk_fma_f32 v[140:141], v[140:141], s[38:39], v[44:45] neg_lo:[1,0,1] neg_hi:[1,0,1]\n\t"
"v_pk_fma_f32 v[142:143], v[142:143], s[38:39], v[46:47] neg_lo:[1,0,1] neg_hi:[1,0,1]\n\t"
"v_pk_fma_f32 v[124:125], v[124:125], s[38:39], v[28:29] neg_lo:[1,0,1] neg_hi:[1,0,1]\n\t"
"v_pk_fma_f32 v[126:127], v[126:127], s[38:39], v[30:31] neg_lo:[1,0,1] neg_hi:[1,0,1]\n\t"
"v_pk_fma_f32 v[108:109], v[108:109], s[38:39], v[12:13] neg_lo:[1,0,1] neg_hi:[1,0,1]\n\t"
"v_pk_fma_f32 v[110:111], v[110:111], s[38:39], v[14:15] neg_lo:[1,0,1] neg_hi:[1,0,1]\n\t"
"s_mov_b32 s20, 172032\n\t"
"buffer_load_dwordx4 a[0:3], v248, s[8:11], s20 offen offset:0 sc1\n\t"
"v_cvt_pk_bf16_f32 v202, v204, v205\n\t"
"v_cvt_pk_bf16_f32 v203, v206, v207\n\t"
"s_waitcnt lgkmcnt(5)\n\t"
"v_mfma_f32_16x16x32_bf16 v[0:3], a[48:51], a[144:147], v[0:3]\n\t"
"ds_read_b128 a[172:175], v216 offset:27648\n\t"
"v_mbcnt_hi_u32_b32 v176, -1, v176\n\t"
"v_mfma_f32_16x16x32_bf16 v[0:3], a[52:55], a[160:163], v[0:3]\n\t"
"buffer_load_dwordx4 a[4:7], v248, s[8:11], s20 offen offset:1024 sc1\n\t"
"ds_write_b128 v221, v[200:203] offset:0\n\t"
"v_mfma_f32_16x16x32_bf16 v[212:215], a[188:191], a[224:227], 0\n\t"
"v_and_b32 v154, 15, v176\n\t"
"v_lshrrev_b32 v155, 4, v176\n\t"
"v_mfma_f32_16x16x32_bf16 v[16:19], a[56:59], a[144:147], v[16:19]\n\t"
"v_cvt_pk_bf16_f32 v208, v208, v209\n\t"
"v_cvt_pk_bf16_f32 v209, v210, v211\n\t"
"v_mfma_f32_16x16x32_bf16 v[16:19], a[60:63], a[160:163], v[16:19]\n\t"
"buffer_load_dwordx4 a[8:11], v248, s[8:11], s20 offen offset:2048 sc1\n\t"
"v_bfe_u32 v156, v176, 2, 2\n\t"
"v_and_b32 v157, 3, v176\n\t"
"v_mfma_f32_16x16x32_bf16 v[32:35], a[64:67], a[144:147], v[32:35]\n\t"
"ds_read_b128 a[144:147], v216 offset:20480\n\t"
"v_lshlrev_b32 v157, 3, v157\n\t"
"v_mfma_f32_16x16x32_bf16 v[32:35], a[68:71], a[160:163], v[32:35]\n\t"
"v_cvt_pk_bf16_f32 v210, v212, v213\n\t"
"v_cvt_pk_bf16_f32 v211, v214, v215\n\t"
"ds_read_b128 a[160:163], v216 offset:28672\n\t"
"s_add_u32 s60, s43, 39424\n\t"
"v_mfma_f32_16x16x32_bf16 v[192:195], a[192:195], a[224:227], 0\n\t"
"ds_write_b128 v221, v[208:211] offset:8192\n\t"
"s_waitcnt lgkmcnt(8)\n\t"
"v_mfma_f32_16x16x32_bf16 v[36:39], a[64:67], a[148:151], v[36:39]\n\t"
"s_add_u32 s61, s26, 20800\n\t"
"s_mul_i32 s62, s73, 96\n\t"
"v_mfma_f32_16x16x32_bf16 v[36:39], a[68:71], a[164:167], v[36:39]\n\t"
"s_mov_b32 s20, 184320\n\t"
"buffer_load_dwordx4 a[12:15], v248, s[8:11], s20 offen offset:0 sc1\n\t"
"s_add_u32 s62, s62, 32768\n\t"
"v_min_u32 v158, 35, v176\n\t"
"v_mfma_f32_16x16x32_bf16 v[20:23], a[56:59], a[148:151], v[20:23]\n\t"
"v_lshl_add_u32 v222, v158, 4, s60\n\t"
"v_add_u32 v223, 62208, v222\n\t"
"v_mfma_f32_16x16x32_bf16 v[20:23], a[60:63], a[164:167], v[20:23]\n\t"
"buffer_load_dwordx4 a[16:19], v248, s[8:11], s20 offen offset:1024 sc1\n\t"
"v_cvt_pk_bf16_f32 v192, v192, v193\n\t"
"v_cvt_pk_bf16_f32 v193, v194, v195\n\t"
"v_mfma_f32_16x16x32_bf16 v[196:199], a[196:199], a[224:227], 0\n\t"
"v_and_b32 v158, 1, v155\n\t"
"v_add_u32 v159, v156, v158\n\t"
"v_lshl_add_u32 v166, v159, 7, v157\n\t"
"v_and_b32 v159, 2, v159\n\t"
"s_barrier\n\t"
"v_mfma_f32_16x16x32_bf16 v[4:7], a[48:51], a[148:151], v[4:7]\n\t"
"ds_read_b128 a[148:151], v216 offset:21504\n\t"
"v_lshl_add_u32 v166, v159, 4, v166\n\t"
"v_mfma_f32_16x16x32_bf16 v[4:7], a[52:55], a[164:167], v[4:7]\n\t"
"ds_read_b128 a[164:167], v216 offset:29696\n\t"
"ds_read_b64_tr_b16 a[176:177], v251 offset:20736\n\t"
"ds_read_b64_tr_b16 a[178:179], v251 offset:21248\n\t"
"v_mul_u32_u24 v159, 20736, v158\n\t"
"v_mfma_f32_16x16x32_bf16 v[200:203], a[200:203], a[224:227], 0\n\t"
"buffer_load_dwordx4 a[20:23], v248, s[8:11], s20 offen offset:2048 sc1\n\t"
"s_waitcnt lgkmcnt(10)\n\t"
"v_mfma_f32_16x16x32_bf16 v[8:11], a[48:51], a[152:155], v[8:11]\n\t"
"v_cvt_pk_bf16_f32 v194, v196, v197\n\t"
"v_cvt_pk_bf16_f32 v195, v198, v199\n\t"
"v_mfma_f32_16x16x32_bf16 v[8:11], a[52:55], a[168:171], v[8:11]\n\t"
"ds_write_b128 v221, v[192:195] offset:4096\n\t"
"ds_read_b64_tr_b16 a[180:181], v252 offset:20736\n\t"
"ds_read_b64_tr_b16 a[182:183], v252 offset:21248\n\t"
"v_mfma_f32_16x16x32_bf16 v[24:27], a[56:59], a[152:155], v[24:27]\n\t"
"ds_read_b64_tr_b16 a[184:185], v249 offset:23040\n\t"
"v_sub_u32 v166, v166, v159\n\t"
"v_mfma_f32_16x16x32_bf16 v[24:27], a[60:63], a[168:171], v[24:27]\n\t"
"s_add_u32 m0, s43, 142976\n\t"
"s_mov_b64 exec, s[44:45]\n\t"
"buffer_load_dwordx4 v247, s[12:15], 0 offen offset:128 nt lds\n\t"
"s_mov_b64 exec, -1\n\t"
"v_cvt_pk_bf16_f32 v200, v200, v201\n\t"
"v_cvt_pk_bf16_f32 v201, v202, v203\n\t"
"v_mfma_f32_16x16x32_bf16 v[204:207], a[204:207], a[224:227], 0\n\t"
"ds_read_b64_tr_b16 a[186:187], v249 offset:23552\n\t"
"ds_read_b64_tr_b16 a[188:189], v250 offset:23040\n\t"
"v_mfma_f32_16x16x32_bf16 v[40:43], a[64:67], a[152:155], v[40:43]\n\t"
"ds_read_b64_tr_b16 a[190:191], v250 offset:23552\n\t"
"v_lshrrev_b32 v159, 5, v176\n\t"
"v_mfma_f32_16x16x32_bf16 v[40:43], a[68:71], a[168:171], v[40:43]\n\t"
"ds_read_b64_tr_b16 a[192:193], v249 offset:20736\n\t"
"ds_read_b64_tr_b16 a[194:195], v249 offset:21248\n\t"
"ds_read_b64_tr_b16 a[196:197], v250 offset:20736\n\t"
"v_xor_b32 v158, v158, v159\n\t"
"s_waitcnt lgkmcnt(15)\n\t"
"v_mfma_f32_16x16x32_bf16 v[44:47], a[64:67], a[156:159], v[44:47]\n\t"
"ds_read_b64_tr_b16 a[198:199], v250 offset:21248\n\t"
"v_lshlrev_b32 v158, 6, v158\n\t"
"v_mfma_f32_16x16x32_bf16 v[44:47], a[68:71], a[172:175], v[44:47]\n\t"
"ds_read_b64_tr_b16 a[200:201], v251 offset:23040\n\t"
"ds_read_b64_tr_b16 a[202:203], v251 offset:23552\n\t"
"ds_read_b64_tr_b16 a[204:205], v252 offset:23040\n\t"
"v_cvt_pk_bf16_f32 v202, v204, v205\n\t"
"v_mfma_f32_16x16x32_bf16 v[28:31], a[56:59], a[156:159], v[28:31]\n\t"
"ds_read_b64_tr_b16 a[206:207], v252 offset:23552\n\t"
"v_cvt_pk_bf16_f32 v203, v206, v207\n\t"
"v_mfma_f32_16x16x32_bf16 v[28:31], a[60:63], a[172:175], v[28:31]\n\t"
"ds_write_b128 v221, v[200:203] offset:12288\n\t"
"ds_read_b128 a[152:155], v216 offset:22528\n\t"
"ds_read_b128 a[168:171], v216 offset:30720\n\t"
"v_mfma_f32_16x16x32_bf16 v[12:15], a[48:51], a[156:159], v[12:15]\n\t"
"ds_read_b128 a[156:159], v216 offset:23552\n\t"
"v_sub_u32 v166, v166, v158\n\t"
"v_mfma_f32_16x16x32_bf16 v[12:15], a[52:55], a[172:175], v[12:15]\n\t"
"s_add_u32 m0, s26, 145280\n\t"
"s_nop 0\n\t"
"buffer_load_dwordx4 v241, s[12:15], s23 offen offset:128 nt lds\n\t"
"ds_read_b128 a[172:175], v216 offset:31744\n\t"
"v_mul_u32_u24 v159, 1152, v159\n\t"
"v_mfma_f32_16x16x32_bf16 v[96:99], a[72:75], a[144:147], v[96:99]\n\t"
"v_add3_u32 v166, v166, v159, s61\n\t"
"v_add_u32 v224, 1152, v166\n\t"
"v_mfma_f32_16x16x32_bf16 v[96:99], a[76:79], a[160:163], v[96:99]\n\t"
"v_add_u32 v225, v159, v166\n\t"
"v_mul_u32_u24 v158, 400, v154\n\t"
"v_mul_u32_u24 v159, 24, v155\n\t"
"v_add3_u32 v228, v158, v159, s62\n\t"
"v_mfma_f32_16x16x32_bf16 v[112:115], a[80:83], a[144:147], v[112:115]\n\t"
"s_mul_i32 s60, s73, 3200\n\t"
"s_add_u32 s60, s60, 32768\n\t"
"v_mfma_f32_16x16x32_bf16 v[112:115], a[84:87], a[160:163], v[112:115]\n\t"
"s_mul_i32 s61, s73, 393216\n\t"
"v_add_u32 v158, 0, v176\n\t"
"v_mul_u32_u24 v159, 0xab, v158\n\t"
"v_lshrrev_b32 v159, 12, v159\n\t"
"v_mfma_f32_16x16x32_bf16 v[128:131], a[88:91], a[144:147], v[128:131]\n\t"
"v_add_lshl_u32 v166, v158, v159, 4\n\t"
"v_add_u32 v229, s60, v166\n\t"
"v_mfma_f32_16x16x32_bf16 v[128:131], a[92:95], a[160:163], v[128:131]\n\t"
"v_mad_u32_u24 v158, v159, 24, v158\n\t"
"v_lshl_add_u32 v232, v158, 4, s61\n\t"
"v_add_u32 v158, 64, v176\n\t"
"v_mul_u32_u24 v159, 0xab, v158\n\t"
"s_waitcnt lgkmcnt(15)\n\t"
"v_mfma_f32_16x16x32_bf16 v[132:135], a[88:91], a[148:151], v[132:135]\n\t"
"v_lshrrev_b32 v159, 12, v159\n\t"
"v_add_lshl_u32 v166, v158, v159, 4\n\t"
"v_mfma_f32_16x16x32_bf16 v[132:135], a[92:95], a[164:167], v[132:135]\n\t"
"v_add_u32 v230, s60, v166\n\t"
"v_mad_u32_u24 v158, v159, 24, v158\n\t"
"v_lshl_add_u32 v233, v158, 4, s61\n\t"
"v_add_u32 v158, 128, v176\n\t"
"v_mfma_f32_16x16x32_bf16 v[116:119], a[80:83], a[148:151], v[116:119]\n\t"
"v_mul_u32_u24 v159, 0xab, v158\n\t"
"v_lshrrev_b32 v159, 12, v159\n\t"
"v_mfma_f32_16x16x32_bf16 v[116:119], a[84:87], a[164:167], v[116:119]\n\t"
"v_add_lshl_u32 v166, v158, v159, 4\n\t"
"v_add_u32 v231, s60, v166\n\t"
"v_mad_u32_u24 v158, v159, 24, v158\n\t"
"v_lshl_add_u32 v234, v158, 4, s61\n\t"
"v_mfma_f32_16x16x32_bf16 v[100:103], a[72:75], a[148:151], v[100:103]\n\t"
"v_mfma_f32_16x16x32_bf16 v[100:103], a[76:79], a[164:167], v[100:103]\n\t"
"s_waitcnt lgkmcnt(0)\n\t"
"s_add_u32 s71, s71, 1\n\t"
"v_mov_b32 v215, s71\n\t"
"s_mov_b64 exec, 1\n\t"
"buffer_store_dword v215, off, s[8:11], s70 sc1\n\t"
"s_mov_b64 exec, -1\n\t"
"s_barrier\n\t"
"v_mfma_f32_16x16x32_bf16 v[104:107], a[72:75], a[152:155], v[104:107]\n\t"
"ds_read_b128 a[144:147], v216 offset:0\n\t"
"v_add_u32 v249, 80896, v227\n\t"
"v_mfma_f32_16x16x32_bf16 v[104:107], a[76:79], a[168:171], v[104:107]\n\t"
"ds_read_b128 a[160:163], v216 offset:8192\n\t"
"ds_read_b128 a[148:151], v216 offset:1024\n\t"
"v_mfma_f32_16x16x32_bf16 v[120:123], a[80:83], a[152:155], v[120:123]\n\t"
"ds_read_b128 a[164:167], v216 offset:9216\n\t"
"v_xor_b32 v250, 32, v249\n\t"
"v_mfma_f32_16x16x32_bf16 v[120:123], a[84:87], a[168:171], v[120:123]\n\t"
"v_xor_b32 v251, 64, v249\n\t"
"v_mfma_f32_16x16x32_bf16 v[136:139], a[88:91], a[152:155], v[136:139]\n\t"
"v_xor_b32 v252, 96, v249\n\t"
"v_mfma_f32_16x16x32_bf16 v[136:139], a[92:95], a[168:171], v[136:139]\n\t"
"s_add_u32 m0, s26, 146304\n\t"
"s_nop 0\n\t"
"buffer_load_dwordx4 v242, s[12:15], s23 offen offset:128 nt lds\n\t"
"v_mfma_f32_16x16x32_bf16 v[208:211], a[176:179], a[220:223], 0\n\t"
"ds_read_b128 a[152:155], v216 offset:2048\n\t"
"v_mfma_f32_16x16x32_bf16 v[140:143], a[88:91], a[156:159], v[140:143]\n\t"
"ds_read_b128 a[168:171], v216 offset:10240\n\t"
"v_mfma_f32_16x16x32_bf16 v[140:143], a[92:95], a[172:175], v[140:143]\n\t"
"ds_read_b64_tr_b16 a[176:177], v251 offset:20736\n\t"
"ds_read_b64_tr_b16 a[178:179], v251 offset:21248\n\t"
"v_mfma_f32_16x16x32_bf16 v[212:215], a[180:183], a[220:223], 0\n\t"
"ds_read_b64_tr_b16 a[180:181], v252 offset:20736\n\t"
"v_mfma_f32_16x16x32_bf16 v[124:127], a[80:83], a[156:159], v[124:127]\n\t"
"v_cvt_pk_bf16_f32 v208, v208, v209\n\t"
"v_mfma_f32_16x16x32_bf16 v[124:127], a[84:87], a[172:175], v[124:127]\n\t"
"v_cvt_pk_bf16_f32 v209, v210, v211\n\t"
"ds_read_b64_tr_b16 a[182:183], v252 offset:21248\n\t"
"v_mfma_f32_16x16x32_bf16 v[108:111], a[72:75], a[156:159], v[108:111]\n\t"
"ds_read_b128 a[156:159], v216 offset:3072\n\t"
"v_mfma_f32_16x16x32_bf16 v[108:111], a[76:79], a[172:175], v[108:111]\n\t"
"v_cvt_pk_bf16_f32 v210, v212, v213\n\t"
"v_cvt_pk_bf16_f32 v211, v214, v215\n\t"
"v_mfma_f32_16x16x32_bf16 v[192:195], a[184:187], a[220:223], 0\n\t"
"ds_read_b128 a[172:175], v216 offset:11264\n\t"
"s_waitcnt vmcnt(20) lgkmcnt(10)\n\t"
"v_mfma_f32_16x16x32_bf16 v[0:3], a[96:99], a[144:147], v[0:3]\n\t"
"ds_read_b64_tr_b16 a[184:185], v249 offset:20736\n\t"
"v_mfma_f32_16x16x32_bf16 v[0:3], a[100:103], a[160:163], v[0:3]\n\t"
"ds_write_b128 v217, v[208:211] offset:16384\n\t"
"v_mfma_f32_16x16x32_bf16 v[196:199], a[188:191], a[220:223], 0\n\t"
"ds_read_b64_tr_b16 a[186:187], v249 offset:21248\n\t"
"s_waitcnt vmcnt(18)\n\t"
"v_mfma_f32_16x16x32_bf16 v[16:19], a[104:107], a[144:147], v[16:19]\n\t"
"v_cvt_pk_bf16_f32 v192, v192, v193\n\t"
"v_mfma_f32_16x16x32_bf16 v[16:19], a[108:111], a[160:163], v[16:19]\n\t"
"v_cvt_pk_bf16_f32 v193, v194, v195\n\t"
"ds_read_b64_tr_b16 a[188:189], v250 offset:20736\n\t"
"s_waitcnt vmcnt(16)\n\t"
"v_mfma_f32_16x16x32_bf16 v[32:35], a[112:115], a[144:147], v[32:35]\n\t"
"ds_read_b128 a[144:147], v216 offset:4096\n\t"
"v_mfma_f32_16x16x32_bf16 v[32:35], a[116:119], a[160:163], v[32:35]\n\t"
"v_cvt_pk_bf16_f32 v194, v196, v197\n\t"
"v_mfma_f32_16x16x32_bf16 v[200:203], a[192:195], a[220:223], 0\n\t"
"s_add_u32 m0, s26, 147328\n\t"
"s_nop 0\n\t"
"buffer_load_dwordx4 v243, s[12:15], s23 offen offset:128 nt lds\n\t"
"s_waitcnt lgkmcnt(13)\n\t"
"v_mfma_f32_16x16x32_bf16 v[36:39], a[112:115], a[148:151], v[36:39]\n\t"
"v_cvt_pk_bf16_f32 v195, v198, v199\n\t"
"v_mfma_f32_16x16x32_bf16 v[36:39], a[116:119], a[164:167], v[36:39]\n\t"
"ds_read_b128 a[160:163], v216 offset:12288\n\t"
"v_mfma_f32_16x16x32_bf16 v[20:23], a[104:107], a[148:151], v[20:23]\n\t"
"ds_read_b64_tr_b16 a[190:191], v250 offset:21248\n\t"
"v_mfma_f32_16x16x32_bf16 v[20:23], a[108:111], a[164:167], v[20:23]\n\t"
"ds_write_b128 v219, v[192:195] offset:16384\n\t"
"v_mfma_f32_16x16x32_bf16 v[204:207], a[196:199], a[220:223], 0\n\t"
"v_cvt_pk_bf16_f32 v200, v200, v201\n\t"
"v_cvt_pk_bf16_f32 v201, v202, v203\n\t"
"v_mfma_f32_16x16x32_bf16 v[4:7], a[96:99], a[148:151], v[4:7]\n\t"
"ds_read_b128 a[148:151], v216 offset:5120\n\t"
"v_mfma_f32_16x16x32_bf16 v[4:7], a[100:103], a[164:167], v[4:7]\n\t"
"ds_read_b128 a[164:167], v216 offset:13312\n\t"
"v_mfma_f32_16x16x32_bf16 v[208:211], a[200:203], a[220:223], 0\n\t"
"ds_read_b64_tr_b16 a[192:193], v251 offset:21888\n\t"
"ds_read_b64_tr_b16 a[194:195], v251 offset:22400\n\t"
"s_waitcnt lgkmcnt(15)\n\t"
"v_mfma_f32_16x16x32_bf16 v[8:11], a[96:99], a[152:155], v[8:11]\n\t"
"v_cvt_pk_bf16_f32 v202, v204, v205\n\t"
"v_mfma_f32_16x16x32_bf16 v[8:11], a[100:103], a[168:171], v[8:11]\n\t"
"v_cvt_pk_bf16_f32 v203, v206, v207\n\t"
"v_mfma_f32_16x16x32_bf16 v[24:27], a[104:107], a[152:155], v[24:27]\n\t"
"ds_read_b64_tr_b16 a[196:197], v252 offset:21888\n\t"
"v_mfma_f32_16x16x32_bf16 v[24:27], a[108:111], a[168:171], v[24:27]\n\t"
"v_cvt_pk_bf16_f32 v208, v208, v209\n\t"
"v_cvt_pk_bf16_f32 v209, v210, v211\n\t"
"v_mfma_f32_16x16x32_bf16 v[212:215], a[204:207], a[220:223], 0\n\t"
"ds_read_b64_tr_b16 a[198:199], v252 offset:22400\n\t"
"v_mfma_f32_16x16x32_bf16 v[40:43], a[112:115], a[152:155], v[40:43]\n\t"
"ds_read_b64_tr_b16 a[200:201], v249 offset:21888\n\t"
"v_mfma_f32_16x16x32_bf16 v[40:43], a[116:119], a[168:171], v[40:43]\n\t"
"s_add_u32 m0, s26, 148352\n\t"
"s_nop 0\n\t"
"buffer_load_dwordx4 v244, s[12:15], s23 offen offset:128 nt lds\n\t"
"s_waitcnt lgkmcnt(15)\n\t"
"v_mfma_f32_16x16x32_bf16 v[44:47], a[112:115], a[156:159], v[44:47]\n\t"
"ds_read_b64_tr_b16 a[202:203], v249 offset:22400\n\t"
"v_mfma_f32_16x16x32_bf16 v[44:47], a[116:119], a[172:175], v[44:47]\n\t"
"ds_write_b128 v217, v[200:203] offset:24576\n\t"
"v_mfma_f32_16x16x32_bf16 v[28:31], a[104:107], a[156:159], v[28:31]\n\t"
"ds_read_b64_tr_b16 a[204:205], v250 offset:21888\n\t"
"v_mfma_f32_16x16x32_bf16 v[28:31], a[108:111], a[172:175], v[28:31]\n\t"
"s_add_u32 m0, s26, 149376\n\t"
"s_mov_b64 exec, s[36:37]\n\t"
"buffer_load_dwordx4 v245, s[12:15], s23 offen offset:128 nt lds\n\t"
"s_mov_b64 exec, -1\n\t"
"v_mfma_f32_16x16x32_bf16 v[12:15], a[96:99], a[156:159], v[12:15]\n\t"
"ds_read_b64_tr_b16 a[206:207], v250 offset:22400\n\t"
"v_mfma_f32_16x16x32_bf16 v[12:15], a[100:103], a[172:175], v[12:15]\n\t"
"v_cvt_pk_bf16_f32 v210, v212, v213\n\t"
"s_waitcnt vmcnt(17) lgkmcnt(13)\n\t"
"v_mfma_f32_16x16x32_bf16 v[96:99], a[120:123], a[144:147], v[96:99]\n\t"
"v_cvt_pk_bf16_f32 v211, v214, v215\n\t"
"ds_read_b128 a[152:155], v216 offset:6144\n\t"
"v_mfma_f32_16x16x32_bf16 v[96:99], a[124:127], a[160:163], v[96:99]\n\t"
"ds_write_b128 v219, v[208:211] offset:24576\n\t"
"s_waitcnt vmcnt(15)\n\t"
"v_mfma_f32_16x16x32_bf16 v[112:115], a[128:131], a[144:147], v[112:115]\n\t"
"v_mfma_f32_16x16x32_bf16 v[112:115], a[132:135], a[160:163], v[112:115]\n\t"
"ds_read_b128 a[168:171], v216 offset:14336\n\t"
"ds_read_b128 a[156:159], v216 offset:7168\n\t"
"s_waitcnt vmcnt(13)\n\t"
"v_mfma_f32_16x16x32_bf16 v[128:131], a[136:139], a[144:147], v[128:131]\n\t"
"ds_read_b128 a[172:175], v216 offset:15360\n\t"
"v_mfma_f32_16x16x32_bf16 v[128:131], a[140:143], a[160:163], v[128:131]\n\t"
"s_waitcnt lgkmcnt(14)\n\t"
"v_mfma_f32_16x16x32_bf16 v[132:135], a[136:139], a[148:151], v[132:135]\n\t"
"v_mfma_f32_16x16x32_bf16 v[132:135], a[140:143], a[164:167], v[132:135]\n\t"
"s_mov_b32 s20, 270336\n\t"
"buffer_load_dwordx4 a[24:27], v248, s[8:11], s20 offen offset:0 sc1\n\t"
"v_mfma_f32_16x16x32_bf16 v[116:119], a[128:131], a[148:151], v[116:119]\n\t"
"v_mfma_f32_16x16x32_bf16 v[116:119], a[132:135], a[164:167], v[116:119]\n\t"
"buffer_load_dwordx4 a[28:31], v248, s[8:11], s20 offen offset:1024 sc1\n\t"
"v_mfma_f32_16x16x32_bf16 v[100:103], a[120:123], a[148:151], v[100:103]\n\t"
"v_mfma_f32_16x16x32_bf16 v[100:103], a[124:127], a[164:167], v[100:103]\n\t"
"s_waitcnt lgkmcnt(0)\n\t"
"s_barrier\n\t"
"v_mfma_f32_16x16x32_bf16 v[104:107], a[120:123], a[152:155], v[104:107]\n\t"
"ds_read_b128 a[144:147], v216 offset:16384\n\t"
"v_add_u32 v249, 122368, v227\n\t"
"v_mfma_f32_16x16x32_bf16 v[104:107], a[124:127], a[168:171], v[104:107]\n\t"
"ds_read_b128 a[160:163], v216 offset:24576\n\t"
"ds_read_b128 a[148:151], v216 offset:17408\n\t"
"v_mfma_f32_16x16x32_bf16 v[120:123], a[128:131], a[152:155], v[120:123]\n\t"
"ds_read_b128 a[164:167], v216 offset:25600\n\t"
"v_xor_b32 v250, 32, v249\n\t"
"v_mfma_f32_16x16x32_bf16 v[120:123], a[132:135], a[168:171], v[120:123]\n\t"
"buffer_load_dwordx4 a[32:35], v248, s[8:11], s20 offen offset:2048 sc1\n\t"
"v_mfma_f32_16x16x32_bf16 v[136:139], a[136:139], a[152:155], v[136:139]\n\t"
"v_xor_b32 v251, 64, v249\n\t"
"v_xor_b32 v252, 96, v249\n\t"
"v_mfma_f32_16x16x32_bf16 v[136:139], a[140:143], a[168:171], v[136:139]\n\t"
"s_mov_b32 s20, 282624\n\t"
"buffer_load_dwordx4 a[36:39], v248, s[8:11], s20 offen offset:0 sc1\n\t"
"v_mfma_f32_16x16x32_bf16 v[192:195], a[176:179], a[224:227], 0\n\t"
"buffer_load_dwordx4 a[40:43], v248, s[8:11], s20 offen offset:1024 sc1\n\t"
"v_mfma_f32_16x16x32_bf16 v[140:143], a[136:139], a[156:159], v[140:143]\n\t"
"ds_read_b128 a[152:155], v216 offset:18432\n\t"
"v_mfma_f32_16x16x32_bf16 v[140:143], a[140:143], a[172:175], v[140:143]\n\t"
"buffer_load_dwordx4 a[44:47], v248, s[8:11], s20 offen offset:2048 sc1\n\t"
"v_mfma_f32_16x16x32_bf16 v[196:199], a[180:183], a[224:227], 0\n\t"
"s_mov_b32 s20, 73728\n\t"
"buffer_load_dwordx4 a[48:51], v248, s[8:11], s20 offen offset:0 sc1\n\t"
"v_mfma_f32_16x16x32_bf16 v[124:127], a[128:131], a[156:159], v[124:127]\n\t"
"ds_read_b128 a[168:171], v216 offset:26624\n\t"
"v_cvt_pk_bf16_f32 v192, v192, v193\n\t"
"v_mfma_f32_16x16x32_bf16 v[124:127], a[132:135], a[172:175], v[124:127]\n\t"
"buffer_load_dwordx4 a[52:55], v248, s[8:11], s20 offen offset:1024 sc1\n\t"
"v_mfma_f32_16x16x32_bf16 v[108:111], a[120:123], a[156:159], v[108:111]\n\t"
"v_cvt_pk_bf16_f32 v193, v194, v195\n\t"
"ds_read_b128 a[156:159], v216 offset:19456\n\t"
"v_mfma_f32_16x16x32_bf16 v[108:111], a[124:127], a[172:175], v[108:111]\n\t"
"v_cvt_pk_bf16_f32 v194, v196, v197\n\t"
"v_cvt_pk_bf16_f32 v195, v198, v199\n\t"
"v_mfma_f32_16x16x32_bf16 v[200:203], a[184:187], a[224:227], 0\n\t"
"ds_read_b128 a[172:175], v216 offset:27648\n\t"
"s_waitcnt vmcnt(19) lgkmcnt(6)\n\t"
"v_mfma_f32_16x16x32_bf16 v[144:147], a[0:3], a[144:147], 0\n\t"
"v_mfma_f32_16x16x32_bf16 v[144:147], a[4:7], a[160:163], v[144:147]\n\t"
"buffer_load_dwordx4 a[56:59], v248, s[8:11], s20 offen offset:2048 sc1\n\t"
"v_mfma_f32_16x16x32_bf16 v[204:207], a[188:191], a[224:227], 0\n\t"
"ds_write_b128 v221, v[192:195] offset:0\n\t"
"s_waitcnt vmcnt(18)\n\t"
"v_mfma_f32_16x16x32_bf16 v[160:163], a[8:11], a[144:147], 0\n\t"
"v_cvt_pk_bf16_f32 v200, v200, v201\n\t"
"v_cvt_pk_bf16_f32 v201, v202, v203\n\t"
"v_mfma_f32_16x16x32_bf16 v[160:163], a[12:15], a[160:163], v[160:163]\n\t"
"s_mov_b32 s20, 86016\n\t"
"buffer_load_dwordx4 a[60:63], v248, s[8:11], s20 offen offset:0 sc1\n\t"
"s_waitcnt vmcnt(17)\n\t"
"v_mfma_f32_16x16x32_bf16 v[176:179], a[16:19], a[144:147], 0\n\t"
"ds_read_b128 a[144:147], v216 offset:20480\n\t"
"v_mfma_f32_16x16x32_bf16 v[176:179], a[20:23], a[160:163], v[176:179]\n\t"
"v_cvt_pk_bf16_f32 v202, v204, v205\n\t"
"v_cvt_pk_bf16_f32 v203, v206, v207\n\t"
"v_mfma_f32_16x16x32_bf16 v[208:211], a[192:195], a[224:227], 0\n\t"
"buffer_load_dwordx4 a[64:67], v248, s[8:11], s20 offen offset:1024 sc1\n\t"
"s_waitcnt lgkmcnt(6)\n\t"
"v_mfma_f32_16x16x32_bf16 v[180:183], a[16:19], a[148:151], 0\n\t"
"ds_read_b128 a[160:163], v216 offset:28672\n\t"
"v_mfma_f32_16x16x32_bf16 v[180:183], a[20:23], a[164:167], v[180:183]\n\t"
"ds_write_b128 v221, v[200:203] offset:8192\n\t"
"v_mfma_f32_16x16x32_bf16 v[164:167], a[8:11], a[148:151], 0\n\t"
"v_mfma_f32_16x16x32_bf16 v[164:167], a[12:15], a[164:167], v[164:167]\n\t"
"buffer_load_dwordx4 a[68:71], v248, s[8:11], s20 offen offset:2048 sc1\n\t"
"v_cvt_pk_bf16_f32 v208, v208, v209\n\t"
"v_cvt_pk_bf16_f32 v209, v210, v211\n\t"
"v_mfma_f32_16x16x32_bf16 v[212:215], a[196:199], a[224:227], 0\n\t"
"s_waitcnt vmcnt(12)\n\t"
"s_barrier\n\t"
"v_mfma_f32_16x16x32_bf16 v[148:151], a[0:3], a[148:151], 0\n\t"
"ds_read_b128 a[148:151], v216 offset:21504\n\t"
"v_mfma_f32_16x16x32_bf16 v[148:151], a[4:7], a[164:167], v[148:151]\n\t"
"s_mov_b32 s20, 368640\n\t"
"buffer_load_dwordx4 a[72:75], v248, s[8:11], s20 offen offset:0 sc1\n\t"
"buffer_load_dwordx4 a[76:79], v248, s[8:11], s20 offen offset:1024 sc1\n\t"
"v_mfma_f32_16x16x32_bf16 v[192:195], a[200:203], a[224:227], 0\n\t"
"buffer_load_dwordx4 a[80:83], v248, s[8:11], s20 offen offset:2048 sc1\n\t"
"ds_read_b128 a[164:167], v216 offset:29696\n\t"
"ds_read_b64_tr_b16 a[176:177], v251 offset:20736\n\t"
"s_waitcnt lgkmcnt(9)\n\t"
"v_mfma_f32_16x16x32_bf16 v[152:155], a[0:3], a[152:155], 0\n\t"
"v_cvt_pk_bf16_f32 v210, v212, v213\n\t"
"v_cvt_pk_bf16_f32 v211, v214, v215\n\t"
"v_mfma_f32_16x16x32_bf16 v[152:155], a[4:7], a[168:171], v[152:155]\n\t"
"s_mov_b32 s20, 380928\n\t"
"buffer_load_dwordx4 a[84:87], v248, s[8:11], s20 offen offset:0 sc1\n\t"
"v_mfma_f32_16x16x32_bf16 v[168:171], a[8:11], a[152:155], 0\n\t"
"ds_read_b64_tr_b16 a[178:179], v251 offset:21248\n\t"
"v_mfma_f32_16x16x32_bf16 v[168:171], a[12:15], a[168:171], v[168:171]\n\t"
"buffer_load_dwordx4 a[88:91], v248, s[8:11], s20 offen offset:1024 sc1\n\t"
"v_mfma_f32_16x16x32_bf16 v[196:199], a[204:207], a[224:227], 0\n\t"
"v_cvt_pk_bf16_f32 v192, v192, v193\n\t"
"v_cvt_pk_bf16_f32 v193, v194, v195\n\t"
"v_mfma_f32_16x16x32_bf16 v[184:187], a[16:19], a[152:155], 0\n\t"
"ds_read_b64_tr_b16 a[180:181], v252 offset:20736\n\t"
"v_mfma_f32_16x16x32_bf16 v[184:187], a[20:23], a[168:171], v[184:187]\n\t"
"buffer_load_dwordx4 a[92:95], v248, s[8:11], s20 offen offset:2048 sc1\n\t"
"ds_write_b128 v221, v[208:211] offset:4096\n\t"
"s_waitcnt lgkmcnt(10)\n\t"
"v_mfma_f32_16x16x32_bf16 v[188:191], a[16:19], a[156:159], 0\n\t"
"ds_read_b64_tr_b16 a[182:183], v252 offset:21248\n\t"
"v_mfma_f32_16x16x32_bf16 v[188:191], a[20:23], a[172:175], v[188:191]\n\t"
"ds_read_b64_tr_b16 a[184:185], v249 offset:20736\n\t"
"ds_read_b64_tr_b16 a[186:187], v249 offset:21248\n\t"
"v_mfma_f32_16x16x32_bf16 v[172:175], a[8:11], a[156:159], 0\n\t"
"ds_read_b64_tr_b16 a[188:189], v250 offset:20736\n\t"
"v_cvt_pk_bf16_f32 v194, v196, v197\n\t"
"v_mfma_f32_16x16x32_bf16 v[172:175], a[12:15], a[172:175], v[172:175]\n\t"
"s_mov_b32 s20, 466944\n\t"
"buffer_load_dwordx4 a[96:99], v248, s[8:11], s20 offen offset:0 sc1\n\t"
"v_mfma_f32_16x16x32_bf16 v[156:159], a[0:3], a[156:159], 0\n\t"
"ds_read_b64_tr_b16 a[190:191], v250 offset:21248\n\t"
"v_cvt_pk_bf16_f32 v195, v198, v199\n\t"
"v_mfma_f32_16x16x32_bf16 v[156:159], a[4:7], a[172:175], v[156:159]\n\t"
"ds_write_b128 v221, v[192:195] offset:12288\n\t"
"s_waitcnt vmcnt(17) lgkmcnt(13)\n\t"
"v_mfma_f32_16x16x32_bf16 v[48:51], a[24:27], a[144:147], v[144:147]\n\t"
"ds_read_b64_tr_b16 a[192:193], v251 offset:21888\n\t"
"v_mfma_f32_16x16x32_bf16 v[48:51], a[28:31], a[160:163], v[48:51]\n\t"
"ds_read_b64_tr_b16 a[194:195], v251 offset:22400\n\t"
"ds_read_b64_tr_b16 a[196:197], v252 offset:21888\n\t"
"ds_read_b64_tr_b16 a[198:199], v252 offset:22400\n\t"
"ds_read_b64_tr_b16 a[200:201], v249 offset:21888\n\t"
"ds_read_b64_tr_b16 a[202:203], v249 offset:22400\n\t"
"ds_read_b64_tr_b16 a[204:205], v250 offset:21888\n\t"
"ds_read_b64_tr_b16 a[206:207], v250 offset:22400\n\t"
"s_waitcnt vmcnt(15)\n\t"
"v_mfma_f32_16x16x32_bf16 v[64:67], a[32:35], a[144:147], v[160:163]\n\t"
"ds_read_b128 a[152:155], v216 offset:22528\n\t"
"v_mfma_f32_16x16x32_bf16 v[64:67], a[36:39], a[160:163], v[64:67]\n\t"
"buffer_load_dwordx4 a[100:103], v248, s[8:11], s20 offen offset:1024 sc1\n\t"
"s_waitcnt vmcnt(14)\n\t"
"v_mfma_f32_16x16x32_bf16 v[80:83], a[40:43], a[144:147], v[176:179]\n\t"
"ds_read_b128 a[168:171], v216 offset:30720\n\t"
"v_mfma_f32_16x16x32_bf16 v[80:83], a[44:47], a[160:163], v[80:83]\n\t"
"ds_read_b128 a[156:159], v216 offset:23552\n\t"
"ds_read_b128 a[172:175], v216 offset:31744\n\t"
"s_waitcnt lgkmcnt(15)\n\t"
"v_mfma_f32_16x16x32_bf16 v[84:87], a[40:43], a[148:151], v[180:183]\n\t"
"v_mfma_f32_16x16x32_bf16 v[84:87], a[44:47], a[164:167], v[84:87]\n\t"
"buffer_load_dwordx4 a[104:107], v248, s[8:11], s20 offen offset:2048 sc1\n\t"
"v_mfma_f32_16x16x32_bf16 v[68:71], a[32:35], a[148:151], v[164:167]\n\t"
"v_mfma_f32_16x16x32_bf16 v[68:71], a[36:39], a[164:167], v[68:71]\n\t"
"s_mov_b32 s20, 479232\n\t"
"buffer_load_dwordx4 a[108:111], v248, s[8:11], s20 offen offset:0 sc1\n\t"
"v_mfma_f32_16x16x32_bf16 v[52:55], a[24:27], a[148:151], v[148:151]\n\t"
"v_mfma_f32_16x16x32_bf16 v[52:55], a[28:31], a[164:167], v[52:55]\n\t"
"buffer_load_dwordx4 a[112:115], v248, s[8:11], s20 offen offset:1024 sc1\n\t"
"s_waitcnt lgkmcnt(0)\n\t"
"s_add_u32 s71, s71, 1\n\t"
"v_mov_b32 v215, s71\n\t"
"s_mov_b64 exec, 1\n\t"
"buffer_store_dword v215, off, s[8:11], s70 sc1\n\t"
"s_mov_b64 exec, -1\n\t"
"s_barrier\n\t"
"v_mfma_f32_16x16x32_bf16 v[56:59], a[24:27], a[152:155], v[152:155]\n\t"
"ds_read_b128 a[144:147], v216 offset:0\n\t"
"v_add_u32 v249, 80896, v225\n\t"
"v_mfma_f32_16x16x32_bf16 v[56:59], a[28:31], a[168:171], v[56:59]\n\t"
"ds_read_b128 a[160:163], v216 offset:8192\n\t"
"ds_read_b128 a[148:151], v216 offset:1024\n\t"
"v_mfma_f32_16x16x32_bf16 v[72:75], a[32:35], a[152:155], v[168:171]\n\t"
"ds_read_b128 a[164:167], v216 offset:9216\n\t"
"v_xor_b32 v250, 32, v249\n\t"
"v_mfma_f32_16x16x32_bf16 v[72:75], a[36:39], a[168:171], v[72:75]\n\t"
"buffer_load_dwordx4 a[116:119], v248, s[8:11], s20 offen offset:2048 sc1\n\t"
"v_mfma_f32_16x16x32_bf16 v[88:91], a[40:43], a[152:155], v[184:187]\n\t"
"v_xor_b32 v251, 64, v249\n\t"
"v_xor_b32 v252, 96, v249\n\t"
"v_mfma_f32_16x16x32_bf16 v[88:91], a[44:47], a[168:171], v[88:91]\n\t"
"s_mov_b32 s20, 761856\n\t"
"buffer_load_dwordx4 a[120:123], v248, s[8:11], s20 offen offset:0 sc1\n\t"
"v_mfma_f32_16x16x32_bf16 v[200:203], a[176:179], a[224:227], 0\n\t"
"ds_read_b128 a[152:155], v216 offset:2048\n\t"
"ds_read_b128 a[168:171], v216 offset:10240\n\t"
"v_mfma_f32_16x16x32_bf16 v[92:95], a[40:43], a[156:159], v[188:191]\n\t"
"ds_read_b64_tr_b16 a[176:177], v249 offset:0\n\t"
"v_mfma_f32_16x16x32_bf16 v[92:95], a[44:47], a[172:175], v[92:95]\n\t"
"ds_read_b64_tr_b16 a[178:179], v249 offset:512\n\t"
"v_mfma_f32_16x16x32_bf16 v[204:207], a[180:183], a[224:227], 0\n\t"
"buffer_load_dwordx4 a[124:127], v248, s[8:11], s20 offen offset:1024 sc1\n\t"
"v_mfma_f32_16x16x32_bf16 v[76:79], a[32:35], a[156:159], v[172:175]\n\t"
"v_cvt_pk_bf16_f32 v200, v200, v201\n\t"
"v_cvt_pk_bf16_f32 v201, v202, v203\n\t"
"v_mfma_f32_16x16x32_bf16 v[76:79], a[36:39], a[172:175], v[76:79]\n\t"
"ds_read_b64_tr_b16 a[180:181], v250 offset:0\n\t"
"ds_read_b64_tr_b16 a[182:183], v250 offset:512\n\t"
"v_mfma_f32_16x16x32_bf16 v[60:63], a[24:27], a[156:159], v[156:159]\n\t"
"ds_read_b128 a[156:159], v216 offset:3072\n\t"
"v_mfma_f32_16x16x32_bf16 v[60:63], a[28:31], a[172:175], v[60:63]\n\t"
"buffer_load_dwordx4 a[128:131], v248, s[8:11], s20 offen offset:2048 sc1\n\t"
"v_cvt_pk_bf16_f32 v202, v204, v205\n\t"
"v_cvt_pk_bf16_f32 v203, v206, v207\n\t"
"v_mfma_f32_16x16x32_bf16 v[208:211], a[184:187], a[224:227], 0\n\t"
"v_pk_fma_f32 v[144:145], v[144:145], s[38:39], v[48:49] neg_lo:[1,0,1] neg_hi:[1,0,1]\n\t"
"v_pk_fma_f32 v[146:147], v[146:147], s[38:39], v[50:51] neg_lo:[1,0,1] neg_hi:[1,0,1]\n\t"
"v_pk_fma_f32 v[160:161], v[160:161], s[38:39], v[64:65] neg_lo:[1,0,1] neg_hi:[1,0,1]\n\t"
"v_pk_fma_f32 v[162:163], v[162:163], s[38:39], v[66:67] neg_lo:[1,0,1] neg_hi:[1,0,1]\n\t"
"v_pk_fma_f32 v[176:177], v[176:177], s[38:39], v[80:81] neg_lo:[1,0,1] neg_hi:[1,0,1]\n\t"
"v_pk_fma_f32 v[178:179], v[178:179], s[38:39], v[82:83] neg_lo:[1,0,1] neg_hi:[1,0,1]\n\t"
"v_pk_fma_f32 v[180:181], v[180:181], s[38:39], v[84:85] neg_lo:[1,0,1] neg_hi:[1,0,1]\n\t"
"v_pk_fma_f32 v[182:183], v[182:183], s[38:39], v[86:87] neg_lo:[1,0,1] neg_hi:[1,0,1]\n\t"
"v_pk_fma_f32 v[164:165], v[164:165], s[38:39], v[68:69] neg_lo:[1,0,1] neg_hi:[1,0,1]\n\t"
"v_pk_fma_f32 v[166:167], v[166:167], s[38:39], v[70:71] neg_lo:[1,0,1] neg_hi:[1,0,1]\n\t"
"v_pk_fma_f32 v[148:149], v[148:149], s[38:39], v[52:53] neg_lo:[1,0,1] neg_hi:[1,0,1]\n\t"
"v_pk_fma_f32 v[150:151], v[150:151], s[38:39], v[54:55] neg_lo:[1,0,1] neg_hi:[1,0,1]\n\t"
"v_pk_fma_f32 v[152:153], v[152:153], s[38:39], v[56:57] neg_lo:[1,0,1] neg_hi:[1,0,1]\n\t"
"v_pk_fma_f32 v[154:155], v[154:155], s[38:39], v[58:59] neg_lo:[1,0,1] neg_hi:[1,0,1]\n\t"
"v_pk_fma_f32 v[168:169], v[168:169], s[38:39], v[72:73] neg_lo:[1,0,1] neg_hi:[1,0,1]\n\t"
"v_pk_fma_f32 v[170:171], v[170:171], s[38:39], v[74:75] neg_lo:[1,0,1] neg_hi:[1,0,1]\n\t"
"v_pk_fma_f32 v[184:185], v[184:185], s[38:39], v[88:89] neg_lo:[1,0,1] neg_hi:[1,0,1]\n\t"
"v_pk_fma_f32 v[186:187], v[186:187], s[38:39], v[90:91] neg_lo:[1,0,1] neg_hi:[1,0,1]\n\t"
"v_pk_fma_f32 v[188:189], v[188:189], s[38:39], v[92:93] neg_lo:[1,0,1] neg_hi:[1,0,1]\n\t"
"v_pk_fma_f32 v[190:191], v[190:191], s[38:39], v[94:95] neg_lo:[1,0,1] neg_hi:[1,0,1]\n\t"
"v_pk_fma_f32 v[172:173], v[172:173], s[38:39], v[76:77] neg_lo:[1,0,1] neg_hi:[1,0,1]\n\t"
"v_pk_fma_f32 v[174:175], v[174:175], s[38:39], v[78:79] neg_lo:[1,0,1] neg_hi:[1,0,1]\n\t"
"v_pk_fma_f32 v[156:157], v[156:157], s[38:39], v[60:61] neg_lo:[1,0,1] neg_hi:[1,0,1]\n\t"
"v_pk_fma_f32 v[158:159], v[158:159], s[38:39], v[62:63] neg_lo:[1,0,1] neg_hi:[1,0,1]\n\t"
"ds_read_b128 a[172:175], v216 offset:11264\n\t"
"ds_read_b64_tr_b16 a[184:185], v251 offset:2304\n\t"
"s_waitcnt vmcnt(20) lgkmcnt(11)\n\t"
"v_mfma_f32_16x16x32_bf16 v[48:51], a[48:51], a[144:147], v[48:51]\n\t"
"ds_read_b64_tr_b16 a[186:187], v251 offset:2816\n\t"
"v_mfma_f32_16x16x32_bf16 v[48:51], a[52:55], a[160:163], v[48:51]\n\t"
"s_mov_b32 s20, 774144\n\t"
"buffer_load_dwordx4 a[132:135], v248, s[8:11], s20 offen offset:0 sc1\n\t"
"ds_write_b128 v221, v[200:203] offset:16384\n\t"
"v_mfma_f32_16x16x32_bf16 v[212:215], a[188:191], a[224:227], 0\n\t"
"ds_read_b64_tr_b16 a[188:189], v252 offset:2304\n\t"
"ds_read_b64_tr_b16 a[190:191], v252 offset:2816\n\t"
"s_waitcnt vmcnt(19)\n\t"
"v_mfma_f32_16x16x32_bf16 v[64:67], a[56:59], a[144:147], v[64:67]\n\t"
"v_cvt_pk_bf16_f32 v208, v208, v209\n\t"
"v_cvt_pk_bf16_f32 v209, v210, v211\n\t"
"v_mfma_f32_16x16x32_bf16 v[64:67], a[60:63], a[160:163], v[64:67]\n\t"
"buffer_load_dwordx4 a[136:139], v248, s[8:11], s20 offen offset:1024 sc1\n\t"
"s_waitcnt vmcnt(18)\n\t"
"v_mfma_f32_16x16x32_bf16 v[80:83], a[64:67], a[144:147], v[80:83]\n\t"
"ds_read_b128 a[144:147], v216 offset:4096\n\t"
"v_mfma_f32_16x16x32_bf16 v[80:83], a[68:71], a[160:163], v[80:83]\n\t"
"v_cvt_pk_bf16_f32 v210, v212, v213\n\t"
"v_cvt_pk_bf16_f32 v211, v214, v215\n\t"
"ds_read_b128 a[160:163], v216 offset:12288\n\t"
"v_mfma_f32_16x16x32_bf16 v[192:195], a[192:195], a[224:227], 0\n\t"
"ds_write_b128 v221, v[208:211] offset:24576\n\t"
"s_waitcnt lgkmcnt(15)\n\t"
"v_mfma_f32_16x16x32_bf16 v[84:87], a[64:67], a[148:151], v[84:87]\n\t"
"ds_read_b64_tr_b16 a[192:193], v251 offset:0\n\t"
"v_mfma_f32_16x16x32_bf16 v[84:87], a[68:71], a[164:167], v[84:87]\n\t"
"buffer_load_dwordx4 a[140:143], v248, s[8:11], s20 offen offset:2048 sc1\n\t"
"ds_read_b64_tr_b16 a[194:195], v251 offset:512\n\t"
"v_mfma_f32_16x16x32_bf16 v[68:71], a[56:59], a[148:151], v[68:71]\n\t"
"v_mfma_f32_16x16x32_bf16 v[68:71], a[60:63], a[164:167], v[68:71]\n\t"
"s_mov_b32 s20, 24576\n\t"
"buffer_load_dwordx4 a[0:3], v248, s[8:11], s20 offen offset:0 sc1\n\t"
"v_cvt_pk_bf16_f32 v192, v192, v193\n\t"
"v_cvt_pk_bf16_f32 v193, v194, v195\n\t"
"v_mfma_f32_16x16x32_bf16 v[196:199], a[196:199], a[224:227], 0\n\t"
"ds_read_b64_tr_b16 a[196:197], v252 offset:0\n\t"
"ds_read_b64_tr_b16 a[198:199], v252 offset:512\n\t"
"v_mfma_f32_16x16x32_bf16 v[52:55], a[48:51], a[148:151], v[52:55]\n\t"
"ds_read_b128 a[148:151], v216 offset:5120\n\t"
"v_mfma_f32_16x16x32_bf16 v[52:55], a[52:55], a[164:167], v[52:55]\n\t"
"ds_read_b128 a[164:167], v216 offset:13312\n\t"
"v_mfma_f32_16x16x32_bf16 v[200:203], a[200:203], a[224:227], 0\n\t"
"buffer_load_dwordx4 a[4:7], v248, s[8:11], s20 offen offset:1024 sc1\n\t"
"ds_read_b64_tr_b16 a[200:201], v249 offset:2304\n\t"
"ds_read_b64_tr_b16 a[202:203], v249 offset:2816\n\t"
"s_waitcnt lgkmcnt(15)\n\t"
"v_mfma_f32_16x16x32_bf16 v[56:59], a[48:51], a[152:155], v[56:59]\n\t"
"v_cvt_pk_bf16_f32 v194, v196, v197\n\t"
"v_cvt_pk_bf16_f32 v195, v198, v199\n\t"
"v_mfma_f32_16x16x32_bf16 v[56:59], a[52:55], a[168:171], v[56:59]\n\t"
"ds_write_b128 v221, v[192:195] offset:20480\n\t"
"v_mfma_f32_16x16x32_bf16 v[72:75], a[56:59], a[152:155], v[72:75]\n\t"
"v_mfma_f32_16x16x32_bf16 v[72:75], a[60:63], a[168:171], v[72:75]\n\t"
"buffer_load_dwordx4 a[8:11], v248, s[8:11], s20 offen offset:2048 sc1\n\t"
"v_cvt_pk_bf16_f32 v200, v200, v201\n\t"
"v_cvt_pk_bf16_f32 v201, v202, v203\n\t"
"v_mfma_f32_16x16x32_bf16 v[204:207], a[204:207], a[224:227], 0\n\t"
"ds_read_b64_tr_b16 a[204:205], v250 offset:2304\n\t"
"ds_read_b64_tr_b16 a[206:207], v250 offset:2816\n\t"
"v_mfma_f32_16x16x32_bf16 v[88:91], a[64:67], a[152:155], v[88:91]\n\t"
"ds_read_b128 a[152:155], v216 offset:6144\n\t"
"v_mfma_f32_16x16x32_bf16 v[88:91], a[68:71], a[168:171], v[88:91]\n\t"
"s_mov_b32 s20, 36864\n\t"
"buffer_load_dwordx4 a[12:15], v248, s[8:11], s20 offen offset:0 sc1\n\t"
"ds_read_b128 a[168:171], v216 offset:14336\n\t"
"v_mfma_f32_16x16x32_bf16 v[92:95], a[64:67], a[156:159], v[92:95]\n\t"
"v_mfma_f32_16x16x32_bf16 v[92:95], a[68:71], a[172:175], v[92:95]\n\t"
"buffer_load_dwordx4 a[16:19], v248, s[8:11], s20 offen offset:1024 sc1\n\t"
"buffer_load_dwordx4 a[20:23], v248, s[8:11], s20 offen offset:2048 sc1\n\t"
"v_mfma_f32_16x16x32_bf16 v[76:79], a[56:59], a[156:159], v[76:79]\n\t"
"v_cvt_pk_bf16_f32 v202, v204, v205\n\t"
"v_cvt_pk_bf16_f32 v203, v206, v207\n\t"
"v_mfma_f32_16x16x32_bf16 v[76:79], a[60:63], a[172:175], v[76:79]\n\t"
"s_mov_b32 s20, 49152\n\t"
"buffer_load_dwordx4 a[24:27], v248, s[8:11], s20 offen offset:0 sc1\n\t"
"buffer_load_dwordx4 a[28:31], v248, s[8:11], s20 offen offset:1024 sc1\n\t"
"v_mfma_f32_16x16x32_bf16 v[60:63], a[48:51], a[156:159], v[60:63]\n\t"
"ds_read_b128 a[156:159], v216 offset:7168\n\t"
"v_mfma_f32_16x16x32_bf16 v[60:63], a[52:55], a[172:175], v[60:63]\n\t"
"ds_write_b128 v221, v[200:203] offset:28672\n\t"
"buffer_load_dwordx4 a[32:35], v248, s[8:11], s20 offen offset:2048 sc1\n\t"
"s_waitcnt vmcnt(26) lgkmcnt(15)\n\t"
"v_mfma_f32_16x16x32_bf16 v[144:147], a[72:75], a[144:147], v[144:147]\n\t"
"ds_read_b128 a[172:175], v216 offset:15360\n\t"
"v_mfma_f32_16x16x32_bf16 v[144:147], a[76:79], a[160:163], v[144:147]\n\t"
"s_mov_b32 s20, 61440\n\t"
"buffer_load_dwordx4 a[36:39], v248, s[8:11], s20 offen offset:0 sc1\n\t"
"buffer_load_dwordx4 a[40:43], v248, s[8:11], s20 offen offset:1024 sc1\n\t"
"s_waitcnt vmcnt(26)\n\t"
"v_mfma_f32_16x16x32_bf16 v[160:163], a[80:83], a[144:147], v[160:163]\n\t"
"v_mfma_f32_16x16x32_bf16 v[160:163], a[84:87], a[160:163], v[160:163]\n\t"
"buffer_load_dwordx4 a[44:47], v248, s[8:11], s20 offen offset:2048 sc1\n\t"
"s_mov_b32 s20, 319488\n\t"
"buffer_load_dwordx4 a[48:51], v248, s[8:11], s20 offen offset:0 sc1\n\t"
"s_waitcnt vmcnt(26)\n\t"
"v_mfma_f32_16x16x32_bf16 v[176:179], a[88:91], a[144:147], v[176:179]\n\t"
"v_mfma_f32_16x16x32_bf16 v[176:179], a[92:95], a[160:163], v[176:179]\n\t"
"buffer_load_dwordx4 a[52:55], v248, s[8:11], s20 offen offset:1024 sc1\n\t"
"buffer_load_dwordx4 a[56:59], v248, s[8:11], s20 offen offset:2048 sc1\n\t"
"s_waitcnt lgkmcnt(10)\n\t"
"v_mfma_f32_16x16x32_bf16 v[180:183], a[88:91], a[148:151], v[180:183]\n\t"
"v_mfma_f32_16x16x32_bf16 v[180:183], a[92:95], a[164:167], v[180:183]\n\t"
"s_mov_b32 s20, 331776\n\t"
"buffer_load_dwordx4 a[60:63], v248, s[8:11], s20 offen offset:0 sc1\n\t"
"buffer_load_dwordx4 a[64:67], v248, s[8:11], s20 offen offset:1024 sc1\n\t"
"v_mfma_f32_16x16x32_bf16 v[164:167], a[80:83], a[148:151], v[164:167]\n\t"
"v_mfma_f32_16x16x32_bf16 v[164:167], a[84:87], a[164:167], v[164:167]\n\t"
"buffer_load_dwordx4 a[68:71], v248, s[8:11], s20 offen offset:2048 sc1\n\t"
"s_add_u32 m0, s43, 39168\n\t"
"s_mov_b64 exec, s[44:45]\n\t"
"buffer_load_dwordx4 v246, s[12:15], 0 offen offset:256 nt lds\n\t"
"s_mov_b64 exec, -1\n\t"
"v_mfma_f32_16x16x32_bf16 v[148:151], a[72:75], a[148:151], v[148:151]\n\t"
"v_mfma_f32_16x16x32_bf16 v[148:151], a[76:79], a[164:167], v[148:151]\n\t"
"s_add_u32 m0, s43, 59904\n\t"
"s_mov_b64 exec, s[44:45]\n\t"
"buffer_load_dwordx4 v247, s[12:15], 0 offen offset:256 nt lds\n\t"
"s_mov_b64 exec, -1\n\t"
"s_waitcnt lgkmcnt(0)\n\t"
"s_barrier\n\t"
"Lunit_%=:\n\t"
"v_mfma_f32_16x16x32_bf16 v[152:155], a[72:75], a[152:155], v[152:155]\n\t"
"ds_read_b128 a[144:147], v216 offset:16384\n\t"
"v_mfma_f32_16x16x32_bf16 v[152:155], a[76:79], a[168:171], v[152:155]\n\t"
"ds_read_b128 a[160:163], v216 offset:24576\n\t"
"ds_read_b128 a[148:151], v216 offset:17408\n\t"
"s_add_u32 m0, s26, 41472\n\t"
"s_nop 0\n\t"
"buffer_load_dwordx4 v236, s[12:15], s23 offen offset:256 nt lds\n\t"
"v_mfma_f32_16x16x32_bf16 v[168:171], a[80:83], a[152:155], v[168:171]\n\t"
"ds_read_b128 a[164:167], v216 offset:25600\n\t"
"v_mfma_f32_16x16x32_bf16 v[168:171], a[84:87], a[168:171], v[168:171]\n\t"
"v_mfma_f32_16x16x32_bf16 v[184:187], a[88:91], a[152:155], v[184:187]\n\t"
"ds_read_b128 a[152:155], v216 offset:18432\n\t"
"v_mfma_f32_16x16x32_bf16 v[184:187], a[92:95], a[168:171], v[184:187]\n\t"
"ds_read_b128 a[168:171], v216 offset:26624\n\t"
"v_mfma_f32_16x16x32_bf16 v[192:195], a[176:179], a[208:211], 0\n\t"
"s_add_u32 m0, s26, 42496\n\t"
"s_nop 0\n\t"
"buffer_load_dwordx4 v237, s[12:15], s23 offen offset:256 nt lds\n\t"
"v_mfma_f32_16x16x32_bf16 v[188:191], a[88:91], a[156:159], v[188:191]\n\t"
"ds_read_b64_tr_b16 a[176:177], v249 offset:1152\n\t"
"v_mfma_f32_16x16x32_bf16 v[188:191], a[92:95], a[172:175], v[188:191]\n\t"
"ds_read_b64_tr_b16 a[178:179], v249 offset:1664\n\t"
"v_mfma_f32_16x16x32_bf16 v[196:199], a[180:183], a[208:211], 0\n\t"
"ds_read_b64_tr_b16 a[180:181], v250 offset:1152\n\t"
"ds_read_b64_tr_b16 a[182:183], v250 offset:1664\n\t"
"v_mfma_f32_16x16x32_bf16 v[172:175], a[80:83], a[156:159], v[172:175]\n\t"
"v_cvt_pk_bf16_f32 v192, v192, v193\n\t"
"v_cvt_pk_bf16_f32 v193, v194, v195\n\t"
"v_mfma_f32_16x16x32_bf16 v[172:175], a[84:87], a[172:175], v[172:175]\n\t"
"s_add_u32 m0, s26, 43520\n\t"
"s_nop 0\n\t"
"buffer_load_dwordx4 v238, s[12:15], s23 offen offset:256 nt lds\n\t"
"v_mfma_f32_16x16x32_bf16 v[156:159], a[72:75], a[156:159], v[156:159]\n\t"
"ds_read_b128 a[156:159], v216 offset:19456\n\t"
"v_mfma_f32_16x16x32_bf16 v[156:159], a[76:79], a[172:175], v[156:159]\n\t"
"v_cvt_pk_bf16_f32 v194, v196, v197\n\t"
"v_cvt_pk_bf16_f32 v195, v198, v199\n\t"
"v_mfma_f32_16x16x32_bf16 v[200:203], a[184:187], a[208:211], 0\n\t"
"ds_read_b128 a[172:175], v216 offset:27648\n\t"
"ds_read_b64_tr_b16 a[184:185], v251 offset:3456\n\t"
"s_waitcnt vmcnt(31) lgkmcnt(11)\n\t"
"v_mfma_f32_16x16x32_bf16 v[48:51], a[96:99], a[144:147], v[48:51]\n\t"
"ds_read_b64_tr_b16 a[186:187], v251 offset:3968\n\t"
"v_mfma_f32_16x16x32_bf16 v[48:51], a[100:103], a[160:163], v[48:51]\n\t"
"ds_write_b128 v217, v[192:195] offset:0\n\t"
"v_mfma_f32_16x16x32_bf16 v[204:207], a[188:191], a[208:211], 0\n\t"
"s_add_u32 m0, s26, 44544\n\t"
"s_nop 0\n\t"
"buffer_load_dwordx4 v239, s[12:15], s23 offen offset:256 nt lds\n\t"
"v_mfma_f32_16x16x32_bf16 v[64:67], a[104:107], a[144:147], v[64:67]\n\t"
"v_cvt_pk_bf16_f32 v200, v200, v201\n\t"
"v_mfma_f32_16x16x32_bf16 v[64:67], a[108:111], a[160:163], v[64:67]\n\t"
"v_cvt_pk_bf16_f32 v201, v202, v203\n\t"
"s_waitcnt vmcnt(30)\n\t"
"v_mfma_f32_16x16x32_bf16 v[80:83], a[112:115], a[144:147], v[80:83]\n\t"
"ds_read_b128 a[144:147], v216 offset:20480\n\t"
"v_mfma_f32_16x16x32_bf16 v[80:83], a[116:119], a[160:163], v[80:83]\n\t"
"v_cvt_pk_bf16_f32 v202, v204, v205\n\t"
"v_cvt_pk_bf16_f32 v203, v206, v207\n\t"
"v_mfma_f32_16x16x32_bf16 v[208:211], a[192:195], a[208:211], 0\n\t"
"s_add_u32 m0, s26, 45568\n\t"
"s_mov_b64 exec, s[36:37]\n\t"
"buffer_load_dwordx4 v240, s[12:15], s23 offen offset:256 nt lds\n\t"
"s_mov_b64 exec, -1\n\t"
"s_waitcnt lgkmcnt(12)\n\t"
"v_mfma_f32_16x16x32_bf16 v[84:87], a[112:115], a[148:151], v[84:87]\n\t"
"ds_read_b128 a[160:163], v216 offset:28672\n\t"
"v_mfma_f32_16x16x32_bf16 v[84:87], a[116:119], a[164:167], v[84:87]\n\t"
"ds_read_b64_tr_b16 a[188:189], v252 offset:3456\n\t"
"v_mfma_f32_16x16x32_bf16 v[68:71], a[104:107], a[148:151], v[68:71]\n\t"
"ds_read_b64_tr_b16 a[190:191], v252 offset:3968\n\t"
"v_mfma_f32_16x16x32_bf16 v[68:71], a[108:111], a[164:167], v[68:71]\n\t"
"ds_write_b128 v219, v[200:203] offset:0\n\t"
"v_mfma_f32_16x16x32_bf16 v[212:215], a[196:199], a[208:211], 0\n\t"
"v_cvt_pk_bf16_f32 v208, v208, v209\n\t"
"v_cvt_pk_bf16_f32 v209, v210, v211\n\t"
"v_mfma_f32_16x16x32_bf16 v[52:55], a[96:99], a[148:151], v[52:55]\n\t"
"ds_read_b128 a[148:151], v216 offset:21504\n\t"
"v_mfma_f32_16x16x32_bf16 v[52:55], a[100:103], a[164:167], v[52:55]\n\t"
"s_add_u32 m0, s26, 62208\n\t"
"s_nop 0\n\t"
"buffer_load_dwordx4 v241, s[12:15], s23 offen offset:256 nt lds\n\t"
"v_mfma_f32_16x16x32_bf16 v[192:195], a[200:203], a[208:211], 0\n\t"
"ds_read_b128 a[164:167], v216 offset:29696\n\t"
"s_waitcnt lgkmcnt(15)\n\t"
"v_mfma_f32_16x16x32_bf16 v[56:59], a[96:99], a[152:155], v[56:59]\n\t"
"v_cvt_pk_bf16_f32 v210, v212, v213\n\t"
"v_mfma_f32_16x16x32_bf16 v[56:59], a[100:103], a[168:171], v[56:59]\n\t"
"v_cvt_pk_bf16_f32 v211, v214, v215\n\t"
"ds_read_b64_tr_b16 a[192:193], v251 offset:1152\n\t"
"v_mfma_f32_16x16x32_bf16 v[72:75], a[104:107], a[152:155], v[72:75]\n\t"
"ds_read_b64_tr_b16 a[194:195], v251 offset:1664\n\t"
"v_mfma_f32_16x16x32_bf16 v[72:75], a[108:111], a[168:171], v[72:75]\n\t"
"s_add_u32 m0, s26, 63232\n\t"
"s_nop 0\n\t"
"buffer_load_dwordx4 v242, s[12:15], s23 offen offset:256 nt lds\n\t"
"v_mfma_f32_16x16x32_bf16 v[196:199], a[204:207], a[208:211], 0\n\t"
"v_cvt_pk_bf16_f32 v192, v192, v193\n\t"
"v_mfma_f32_16x16x32_bf16 v[88:91], a[112:115], a[152:155], v[88:91]\n\t"
"v_cvt_pk_bf16_f32 v193, v194, v195\n\t"
"ds_read_b64_tr_b16 a[196:197], v252 offset:1152\n\t"
"v_mfma_f32_16x16x32_bf16 v[88:91], a[116:119], a[168:171], v[88:91]\n\t"
"ds_read_b64_tr_b16 a[198:199], v252 offset:1664\n\t"
"s_waitcnt lgkmcnt(14)\n\t"
"v_mfma_f32_16x16x32_bf16 v[92:95], a[112:115], a[156:159], v[92:95]\n\t"
"ds_read_b64_tr_b16 a[200:201], v249 offset:3456\n\t"
"v_mfma_f32_16x16x32_bf16 v[92:95], a[116:119], a[172:175], v[92:95]\n\t"
"s_add_u32 m0, s26, 64256\n\t"
"s_nop 0\n\t"
"buffer_load_dwordx4 v243, s[12:15], s23 offen offset:256 nt lds\n\t"
"v_mfma_f32_16x16x32_bf16 v[76:79], a[104:107], a[156:159], v[76:79]\n\t"
"ds_read_b64_tr_b16 a[202:203], v249 offset:3968\n\t"
"v_mfma_f32_16x16x32_bf16 v[76:79], a[108:111], a[172:175], v[76:79]\n\t"
"ds_write_b128 v217, v[208:211] offset:8192\n\t"
"v_mfma_f32_16x16x32_bf16 v[60:63], a[96:99], a[156:159], v[60:63]\n\t"
"ds_read_b64_tr_b16 a[204:205], v250 offset:3456\n\t"
"v_mfma_f32_16x16x32_bf16 v[60:63], a[100:103], a[172:175], v[60:63]\n\t"
"ds_read_b64_tr_b16 a[206:207], v250 offset:3968\n\t"
"v_cvt_pk_bf16_f32 v194, v196, v197\n\t"
"s_waitcnt vmcnt(29) lgkmcnt(14)\n\t"
"v_mfma_f32_16x16x32_bf16 v[144:147], a[120:123], a[144:147], v[144:147]\n\t"
"v_cvt_pk_bf16_f32 v195, v198, v199\n\t"
"v_mfma_f32_16x16x32_bf16 v[144:147], a[124:127], a[160:163], v[144:147]\n\t"
"ds_write_b128 v219, v[192:195] offset:8192\n\t"
"s_add_u32 m0, s26, 65280\n\t"
"s_nop 0\n\t"
"buffer_load_dwordx4 v244, s[12:15], s23 offen offset:256 nt lds\n\t"
"v_mfma_f32_16x16x32_bf16 v[160:163], a[128:131], a[144:147], v[160:163]\n\t"
"v_mfma_f32_16x16x32_bf16 v[160:163], a[132:135], a[160:163], v[160:163]\n\t"
"v_pk_add_f32 v[0:1], v[0:1], v[48:49]\n\t"
"v_pk_add_f32 v[2:3], v[2:3], v[50:51]\n\t"
"v_pk_fma_f32 v[48:49], v[48:49], s[38:39], v[0:1]\n\t"
"v_pk_fma_f32 v[50:51], v[50:51], s[38:39], v[2:3]\n\t"
"v_pk_add_f32 v[16:17], v[16:17], v[64:65]\n\t"
"v_pk_add_f32 v[18:19], v[18:19], v[66:67]\n\t"
"v_pk_fma_f32 v[64:65], v[64:65], s[38:39], v[16:17]\n\t"
"v_pk_fma_f32 v[66:67], v[66:67], s[38:39], v[18:19]\n\t"
"v_pk_add_f32 v[32:33], v[32:33], v[80:81]\n\t"
"v_pk_add_f32 v[34:35], v[34:35], v[82:83]\n\t"
"v_pk_fma_f32 v[80:81], v[80:81], s[38:39], v[32:33]\n\t"
"v_pk_fma_f32 v[82:83], v[82:83], s[38:39], v[34:35]\n\t"
"v_pk_add_f32 v[36:37], v[36:37], v[84:85]\n\t"
"v_pk_add_f32 v[38:39], v[38:39], v[86:87]\n\t"
"v_pk_fma_f32 v[84:85], v[84:85], s[38:39], v[36:37]\n\t"
"v_pk_fma_f32 v[86:87], v[86:87], s[38:39], v[38:39]\n\t"
"v_pk_add_f32 v[20:21], v[20:21], v[68:69]\n\t"
"v_pk_add_f32 v[22:23], v[22:23], v[70:71]\n\t"
"v_pk_fma_f32 v[68:69], v[68:69], s[38:39], v[20:21]\n\t"
"v_pk_fma_f32 v[70:71], v[70:71], s[38:39], v[22:23]\n\t"
"v_pk_add_f32 v[4:5], v[4:5], v[52:53]\n\t"
"v_pk_add_f32 v[6:7], v[6:7], v[54:55]\n\t"
"v_pk_fma_f32 v[52:53], v[52:53], s[38:39], v[4:5]\n\t"
"v_pk_fma_f32 v[54:55], v[54:55], s[38:39], v[6:7]\n\t"
"v_pk_add_f32 v[8:9], v[8:9], v[56:57]\n\t"
"v_pk_add_f32 v[10:11], v[10:11], v[58:59]\n\t"
"v_pk_fma_f32 v[56:57], v[56:57], s[38:39], v[8:9]\n\t"
"v_pk_fma_f32 v[58:59], v[58:59], s[38:39], v[10:11]\n\t"
"v_pk_add_f32 v[24:25], v[24:25], v[72:73]\n\t"
"v_pk_add_f32 v[26:27], v[26:27], v[74:75]\n\t"
"v_pk_fma_f32 v[72:73], v[72:73], s[38:39], v[24:25]\n\t"
"v_pk_fma_f32 v[74:75], v[74:75], s[38:39], v[26:27]\n\t"
"v_pk_add_f32 v[40:41], v[40:41], v[88:89]\n\t"
"v_pk_add_f32 v[42:43], v[42:43], v[90:91]\n\t"
"v_pk_fma_f32 v[88:89], v[88:89], s[38:39], v[40:41]\n\t"
"v_pk_fma_f32 v[90:91], v[90:91], s[38:39], v[42:43]\n\t"
"v_pk_add_f32 v[44:45], v[44:45], v[92:93]\n\t"
"v_pk_add_f32 v[46:47], v[46:47], v[94:95]\n\t"
"v_pk_fma_f32 v[92:93], v[92:93], s[38:39], v[44:45]\n\t"
"v_pk_fma_f32 v[94:95], v[94:95], s[38:39], v[46:47]\n\t"
"v_pk_add_f32 v[28:29], v[28:29], v[76:77]\n\t"
"v_pk_add_f32 v[30:31], v[30:31], v[78:79]\n\t"
"v_pk_fma_f32 v[76:77], v[76:77], s[38:39], v[28:29]\n\t"
"v_pk_fma_f32 v[78:79], v[78:79], s[38:39], v[30:31]\n\t"
"v_pk_add_f32 v[12:13], v[12:13], v[60:61]\n\t"
"v_pk_add_f32 v[14:15], v[14:15], v[62:63]\n\t"
"v_pk_fma_f32 v[60:61], v[60:61], s[38:39], v[12:13]\n\t"
"v_pk_fma_f32 v[62:63], v[62:63], s[38:39], v[14:15]\n\t"
"s_waitcnt vmcnt(29)\n\t"
"v_mfma_f32_16x16x32_bf16 v[176:179], a[136:139], a[144:147], v[176:179]\n\t"
"ds_read_b128 a[152:155], v216 offset:22528\n\t"
"v_mfma_f32_16x16x32_bf16 v[176:179], a[140:143], a[160:163], v[176:179]\n\t"
"ds_read_b128 a[168:171], v216 offset:30720\n\t"
"ds_read_b128 a[156:159], v216 offset:23552\n\t"
"ds_read_b128 a[172:175], v216 offset:31744\n\t"
"s_add_u32 m0, s26, 66304\n\t"
"s_mov_b64 exec, s[36:37]\n\t"
"buffer_load_dwordx4 v245, s[12:15], s23 offen offset:256 nt lds\n\t"
"s_mov_b64 exec, -1\n\t"
"s_waitcnt lgkmcnt(14)\n\t"
"v_mfma_f32_16x16x32_bf16 v[180:183], a[136:139], a[148:151], v[180:183]\n\t"
"v_mfma_f32_16x16x32_bf16 v[180:183], a[140:143], a[164:167], v[180:183]\n\t"
"v_mfma_f32_16x16x32_bf16 v[164:167], a[128:131], a[148:151], v[164:167]\n\t"
"v_mfma_f32_16x16x32_bf16 v[164:167], a[132:135], a[164:167], v[164:167]\n\t"
"v_mfma_f32_16x16x32_bf16 v[148:151], a[120:123], a[148:151], v[148:151]\n\t"
"v_mfma_f32_16x16x32_bf16 v[148:151], a[124:127], a[164:167], v[148:151]\n\t"
"s_waitcnt lgkmcnt(0)\n\t"
"s_barrier\n\t"
"v_mfma_f32_16x16x32_bf16 v[152:155], a[120:123], a[152:155], v[152:155]\n\t"
"ds_read_b128 a[144:147], v216 offset:0\n\t"
"v_add_u32 v249, 122368, v225\n\t"
"v_mfma_f32_16x16x32_bf16 v[152:155], a[124:127], a[168:171], v[152:155]\n\t"
"ds_read_b128 a[160:163], v216 offset:8192\n\t"
"ds_read_b128 a[148:151], v216 offset:1024\n\t"
"v_mfma_f32_16x16x32_bf16 v[168:171], a[128:131], a[152:155], v[168:171]\n\t"
"ds_read_b128 a[164:167], v216 offset:9216\n\t"
"v_xor_b32 v250, 32, v249\n\t"
"v_mfma_f32_16x16x32_bf16 v[168:171], a[132:135], a[168:171], v[168:171]\n\t"
"v_xor_b32 v251, 64, v249\n\t"
"v_mfma_f32_16x16x32_bf16 v[184:187], a[136:139], a[152:155], v[184:187]\n\t"
"v_xor_b32 v252, 96, v249\n\t"
"ds_read_b128 a[152:155], v216 offset:2048\n\t"
"v_mfma_f32_16x16x32_bf16 v[184:187], a[140:143], a[168:171], v[184:187]\n\t"
"ds_read_b128 a[168:171], v216 offset:10240\n\t"
"v_mfma_f32_16x16x32_bf16 v[200:203], a[176:179], a[208:211], 0\n\t"
"ds_read_b64_tr_b16 a[176:177], v249 offset:0\n\t"
"ds_read_b64_tr_b16 a[178:179], v249 offset:512\n\t"
"v_mfma_f32_16x16x32_bf16 v[188:191], a[136:139], a[156:159], v[188:191]\n\t"
"v_mfma_f32_16x16x32_bf16 v[188:191], a[140:143], a[172:175], v[188:191]\n\t"
"v_mfma_f32_16x16x32_bf16 v[204:207], a[180:183], a[208:211], 0\n\t"
"ds_read_b64_tr_b16 a[180:181], v250 offset:0\n\t"
"ds_read_b64_tr_b16 a[182:183], v250 offset:512\n\t"
"v_mfma_f32_16x16x32_bf16 v[172:175], a[128:131], a[156:159], v[172:175]\n\t"
"v_cvt_pk_bf16_f32 v200, v200, v201\n\t"
"v_cvt_pk_bf16_f32 v201, v202, v203\n\t"
"v_mfma_f32_16x16x32_bf16 v[172:175], a[132:135], a[172:175], v[172:175]\n\t"
"v_mfma_f32_16x16x32_bf16 v[156:159], a[120:123], a[156:159], v[156:159]\n\t"
"ds_read_b128 a[156:159], v216 offset:3072\n\t"
"v_mfma_f32_16x16x32_bf16 v[156:159], a[124:127], a[172:175], v[156:159]\n\t"
"s_mov_b32 s20, 356352\n\t"
"buffer_load_dwordx4 a[88:91], v248, s[8:11], s20 offen offset:1024 sc1\n\t"
"v_mfma_f32_16x16x32_bf16 v[208:211], a[184:187], a[208:211], 0\n\t"
"v_cvt_pk_bf16_f32 v202, v204, v205\n\t"
"v_cvt_pk_bf16_f32 v203, v206, v207\n\t"
"s_waitcnt vmcnt(24) lgkmcnt(9)\n\t"
"v_mfma_f32_16x16x32_bf16 v[0:3], a[0:3], a[144:147], v[0:3]\n\t"
"ds_read_b128 a[172:175], v216 offset:11264\n\t"
"v_mfma_f32_16x16x32_bf16 v[0:3], a[4:7], a[160:163], v[0:3]\n\t"
"ds_write_b128 v217, v[200:203] offset:16384\n\t"
"v_mfma_f32_16x16x32_bf16 v[212:215], a[188:191], a[208:211], 0\n\t"
"v_pk_add_f32 v[96:97], v[96:97], v[144:145]\n\t"
"v_pk_add_f32 v[98:99], v[98:99], v[146:147]\n\t"
"v_pk_fma_f32 v[144:145], v[144:145], s[38:39], v[96:97]\n\t"
"v_pk_fma_f32 v[146:147], v[146:147], s[38:39], v[98:99]\n\t"
"v_pk_add_f32 v[112:113], v[112:113], v[160:161]\n\t"
"v_pk_add_f32 v[114:115], v[114:115], v[162:163]\n\t"
"v_pk_fma_f32 v[160:161], v[160:161], s[38:39], v[112:113]\n\t"
"v_pk_fma_f32 v[162:163], v[162:163], s[38:39], v[114:115]\n\t"
"v_pk_add_f32 v[128:129], v[128:129], v[176:177]\n\t"
"v_pk_add_f32 v[130:131], v[130:131], v[178:179]\n\t"
"v_pk_fma_f32 v[176:177], v[176:177], s[38:39], v[128:129]\n\t"
"v_pk_fma_f32 v[178:179], v[178:179], s[38:39], v[130:131]\n\t"
"v_pk_add_f32 v[132:133], v[132:133], v[180:181]\n\t"
"v_pk_add_f32 v[134:135], v[134:135], v[182:183]\n\t"
"v_pk_fma_f32 v[180:181], v[180:181], s[38:39], v[132:133]\n\t"
"v_pk_fma_f32 v[182:183], v[182:183], s[38:39], v[134:135]\n\t"
"v_pk_add_f32 v[116:117], v[116:117], v[164:165]\n\t"
"v_pk_add_f32 v[118:119], v[118:119], v[166:167]\n\t"
"v_pk_fma_f32 v[164:165], v[164:165], s[38:39], v[116:117]\n\t"
"v_pk_fma_f32 v[166:167], v[166:167], s[38:39], v[118:119]\n\t"
"v_pk_add_f32 v[100:101], v[100:101], v[148:149]\n\t"
"v_pk_add_f32 v[102:103], v[102:103], v[150:151]\n\t"
"v_pk_fma_f32 v[148:149], v[148:149], s[38:39], v[100:101]\n\t"
"v_pk_fma_f32 v[150:151], v[150:151], s[38:39], v[102:103]\n\t"
"v_pk_add_f32 v[104:105], v[104:105], v[152:153]\n\t"
"v_pk_add_f32 v[106:107], v[106:107], v[154:155]\n\t"
"v_pk_fma_f32 v[152:153], v[152:153], s[38:39], v[104:105]\n\t"
"v_pk_fma_f32 v[154:155], v[154:155], s[38:39], v[106:107]\n\t"
"v_pk_add_f32 v[120:121], v[120:121], v[168:169]\n\t"
"v_pk_add_f32 v[122:123], v[122:123], v[170:171]\n\t"
"v_pk_fma_f32 v[168:169], v[168:169], s[38:39], v[120:121]\n\t"
"v_pk_fma_f32 v[170:171], v[170:171], s[38:39], v[122:123]\n\t"
"v_pk_add_f32 v[136:137], v[136:137], v[184:185]\n\t"
"v_pk_add_f32 v[138:139], v[138:139], v[186:187]\n\t"
"v_pk_fma_f32 v[184:185], v[184:185], s[38:39], v[136:137]\n\t"
"v_pk_fma_f32 v[186:187], v[186:187], s[38:39], v[138:139]\n\t"
"v_pk_add_f32 v[140:141], v[140:141], v[188:189]\n\t"
"v_pk_add_f32 v[142:143], v[142:143], v[190:191]\n\t"
"v_pk_fma_f32 v[188:189], v[188:189], s[38:39], v[140:141]\n\t"
"v_pk_fma_f32 v[190:191], v[190:191], s[38:39], v[142:143]\n\t"
"v_pk_add_f32 v[124:125], v[124:125], v[172:173]\n\t"
"v_pk_add_f32 v[126:127], v[126:127], v[174:175]\n\t"
"v_pk_fma_f32 v[172:173], v[172:173], s[38:39], v[124:125]\n\t"
"v_pk_fma_f32 v[174:175], v[174:175], s[38:39], v[126:127]\n\t"
"v_pk_add_f32 v[108:109], v[108:109], v[156:157]\n\t"
"v_pk_add_f32 v[110:111], v[110:111], v[158:159]\n\t"
"v_pk_fma_f32 v[156:157], v[156:157], s[38:39], v[108:109]\n\t"
"v_pk_fma_f32 v[158:159], v[158:159], s[38:39], v[110:111]\n\t"
"ds_read_b64_tr_b16 a[184:185], v251 offset:2304\n\t"
"ds_read_b64_tr_b16 a[186:187], v251 offset:2816\n\t"
"v_mfma_f32_16x16x32_bf16 v[16:19], a[8:11], a[144:147], v[16:19]\n\t"
"v_cvt_pk_bf16_f32 v208, v208, v209\n\t"
"v_cvt_pk_bf16_f32 v209, v210, v211\n\t"
"v_mfma_f32_16x16x32_bf16 v[16:19], a[12:15], a[160:163], v[16:19]\n\t"
"ds_read_b64_tr_b16 a[188:189], v252 offset:2304\n\t"
"ds_read_b64_tr_b16 a[190:191], v252 offset:2816\n\t"
"v_mfma_f32_16x16x32_bf16 v[32:35], a[16:19], a[144:147], v[32:35]\n\t"
"ds_read_b128 a[144:147], v216 offset:4096\n\t"
"v_mfma_f32_16x16x32_bf16 v[32:35], a[20:23], a[160:163], v[32:35]\n\t"
"v_cvt_pk_bf16_f32 v210, v212, v213\n\t"
"v_cvt_pk_bf16_f32 v211, v214, v215\n\t"
"v_mfma_f32_16x16x32_bf16 v[192:195], a[192:195], a[208:211], 0\n\t"
"ds_read_b128 a[160:163], v216 offset:12288\n\t"
"s_waitcnt lgkmcnt(15)\n\t"
"v_mfma_f32_16x16x32_bf16 v[36:39], a[16:19], a[148:151], v[36:39]\n\t"
"ds_read_b64_tr_b16 a[192:193], v251 offset:0\n\t"
"v_mfma_f32_16x16x32_bf16 v[36:39], a[20:23], a[164:167], v[36:39]\n\t"
"ds_write_b128 v219, v[208:211] offset:16384\n\t"
"v_mfma_f32_16x16x32_bf16 v[20:23], a[8:11], a[148:151], v[20:23]\n\t"
"ds_read_b64_tr_b16 a[194:195], v251 offset:512\n\t"
"v_mfma_f32_16x16x32_bf16 v[20:23], a[12:15], a[164:167], v[20:23]\n\t"
"v_cvt_pk_bf16_f32 v192, v192, v193\n\t"
"v_cvt_pk_bf16_f32 v193, v194, v195\n\t"
"v_mfma_f32_16x16x32_bf16 v[196:199], a[196:199], a[208:211], 0\n\t"
"ds_read_b64_tr_b16 a[196:197], v252 offset:0\n\t"
"ds_read_b64_tr_b16 a[198:199], v252 offset:512\n\t"
"v_mfma_f32_16x16x32_bf16 v[4:7], a[0:3], a[148:151], v[4:7]\n\t"
"ds_read_b128 a[148:151], v216 offset:5120\n\t"
"v_mfma_f32_16x16x32_bf16 v[4:7], a[4:7], a[164:167], v[4:7]\n\t"
"ds_read_b128 a[164:167], v216 offset:13312\n\t"
"v_mfma_f32_16x16x32_bf16 v[200:203], a[200:203], a[208:211], 0\n\t"
"ds_read_b64_tr_b16 a[200:201], v249 offset:2304\n\t"
"ds_read_b64_tr_b16 a[202:203], v249 offset:2816\n\t"
"s_waitcnt lgkmcnt(15)\n\t"
"v_mfma_f32_16x16x32_bf16 v[8:11], a[0:3], a[152:155], v[8:11]\n\t"
"v_cvt_pk_bf16_f32 v194, v196, v197\n\t"
"v_mfma_f32_16x16x32_bf16 v[8:11], a[4:7], a[168:171], v[8:11]\n\t"
"buffer_load_dwordx4 a[92:95], v248, s[8:11], s20 offen offset:2048 sc1\n\t"
"v_mfma_f32_16x16x32_bf16 v[24:27], a[8:11], a[152:155], v[24:27]\n\t"
"v_cvt_pk_bf16_f32 v195, v198, v199\n\t"
"v_mfma_f32_16x16x32_bf16 v[24:27], a[12:15], a[168:171], v[24:27]\n\t"
"v_cvt_pk_bf16_f32 v200, v200, v201\n\t"
"v_cvt_pk_bf16_f32 v201, v202, v203\n\t"
"v_mfma_f32_16x16x32_bf16 v[204:207], a[204:207], a[208:211], 0\n\t"
"ds_read_b64_tr_b16 a[204:205], v250 offset:2304\n\t"
"v_mfma_f32_16x16x32_bf16 v[40:43], a[16:19], a[152:155], v[40:43]\n\t"
"ds_read_b64_tr_b16 a[206:207], v250 offset:2816\n\t"
"v_mfma_f32_16x16x32_bf16 v[40:43], a[20:23], a[168:171], v[40:43]\n\t"
"buffer_load_dwordx4 a[84:87], v248, s[8:11], s20 offen offset:0 sc1\n\t"
"v_mfma_f32_16x16x32_bf16 v[44:47], a[16:19], a[156:159], v[44:47]\n\t"
"ds_read_b128 a[152:155], v216 offset:6144\n\t"
"v_mfma_f32_16x16x32_bf16 v[44:47], a[20:23], a[172:175], v[44:47]\n\t"
"s_mov_b32 s20, 344064\n\t"
"buffer_load_dwordx4 a[80:83], v248, s[8:11], s20 offen offset:2048 sc1\n\t"
"v_mfma_f32_16x16x32_bf16 v[28:31], a[8:11], a[156:159], v[28:31]\n\t"
"v_cvt_pk_bf16_f32 v202, v204, v205\n\t"
"v_mfma_f32_16x16x32_bf16 v[28:31], a[12:15], a[172:175], v[28:31]\n\t"
"ds_write_b128 v217, v[192:195] offset:24576\n\t"
"v_mfma_f32_16x16x32_bf16 v[12:15], a[0:3], a[156:159], v[12:15]\n\t"
"v_cvt_pk_bf16_f32 v203, v206, v207\n\t"
"v_mfma_f32_16x16x32_bf16 v[12:15], a[4:7], a[172:175], v[12:15]\n\t"
"ds_write_b128 v219, v[200:203] offset:24576\n\t"
"s_waitcnt vmcnt(20) lgkmcnt(14)\n\t"
"v_mfma_f32_16x16x32_bf16 v[48:51], a[24:27], a[144:147], v[48:51]\n\t"
"ds_read_b128 a[168:171], v216 offset:14336\n\t"
"v_mfma_f32_16x16x32_bf16 v[48:51], a[28:31], a[160:163], v[48:51]\n\t"
"buffer_load_dwordx4 a[72:75], v248, s[8:11], s20 offen offset:0 sc1\n\t"
"v_mfma_f32_16x16x32_bf16 v[64:67], a[32:35], a[144:147], v[64:67]\n\t"
"ds_read_b128 a[156:159], v216 offset:7168\n\t"
"v_mfma_f32_16x16x32_bf16 v[64:67], a[36:39], a[160:163], v[64:67]\n\t"
"ds_read_b128 a[172:175], v216 offset:15360\n\t"
"v_mfma_f32_16x16x32_bf16 v[80:83], a[40:43], a[144:147], v[80:83]\n\t"
"v_mfma_f32_16x16x32_bf16 v[80:83], a[44:47], a[160:163], v[80:83]\n\t"
"buffer_load_dwordx4 a[76:79], v248, s[8:11], s20 offen offset:1024 sc1\n\t"
"s_waitcnt lgkmcnt(10)\n\t"
"v_mfma_f32_16x16x32_bf16 v[84:87], a[40:43], a[148:151], v[84:87]\n\t"
"v_mfma_f32_16x16x32_bf16 v[84:87], a[44:47], a[164:167], v[84:87]\n\t"
"s_mov_b32 s20, 430080\n\t"
"buffer_load_dwordx4 a[112:115], v248, s[8:11], s20 offen offset:1024 sc1\n\t"
"v_mfma_f32_16x16x32_bf16 v[68:71], a[32:35], a[148:151], v[68:71]\n\t"
"v_mfma_f32_16x16x32_bf16 v[68:71], a[36:39], a[164:167], v[68:71]\n\t"
"buffer_load_dwordx4 a[116:119], v248, s[8:11], s20 offen offset:2048 sc1\n\t"
"v_mfma_f32_16x16x32_bf16 v[52:55], a[24:27], a[148:151], v[52:55]\n\t"
"v_mfma_f32_16x16x32_bf16 v[52:55], a[28:31], a[164:167], v[52:55]\n\t"
"s_waitcnt lgkmcnt(0)\n\t"
"s_barrier\n\t"
"v_mfma_f32_16x16x32_bf16 v[56:59], a[24:27], a[152:155], v[56:59]\n\t"
"ds_read_b128 a[144:147], v216 offset:16384\n\t"
"v_mfma_f32_16x16x32_bf16 v[56:59], a[28:31], a[168:171], v[56:59]\n\t"
"ds_read_b128 a[160:163], v216 offset:24576\n\t"
"ds_read_b128 a[148:151], v216 offset:17408\n\t"
"v_mfma_f32_16x16x32_bf16 v[72:75], a[32:35], a[152:155], v[72:75]\n\t"
"ds_read_b128 a[164:167], v216 offset:25600\n\t"
"v_mfma_f32_16x16x32_bf16 v[72:75], a[36:39], a[168:171], v[72:75]\n\t"
"buffer_load_dwordx4 a[108:111], v248, s[8:11], s20 offen offset:0 sc1\n\t"
"v_mfma_f32_16x16x32_bf16 v[88:91], a[40:43], a[152:155], v[88:91]\n\t"
"ds_read_b128 a[152:155], v216 offset:18432\n\t"
"v_mfma_f32_16x16x32_bf16 v[88:91], a[44:47], a[168:171], v[88:91]\n\t"
"s_mov_b32 s20, 417792\n\t"
"buffer_load_dwordx4 a[104:107], v248, s[8:11], s20 offen offset:2048 sc1\n\t"
"v_mfma_f32_16x16x32_bf16 v[208:211], a[176:179], a[208:211], 0\n\t"
"buffer_load_dwordx4 a[96:99], v248, s[8:11], s20 offen offset:0 sc1\n\t"
"v_mfma_f32_16x16x32_bf16 v[92:95], a[40:43], a[156:159], v[92:95]\n\t"
"ds_read_b128 a[168:171], v216 offset:26624\n\t"
"v_mfma_f32_16x16x32_bf16 v[92:95], a[44:47], a[172:175], v[92:95]\n\t"
"buffer_load_dwordx4 a[100:103], v248, s[8:11], s20 offen offset:1024 sc1\n\t"
"v_mfma_f32_16x16x32_bf16 v[212:215], a[180:183], a[208:211], 0\n\t"
"ds_read_b64_tr_b16 a[176:177], v249 offset:1152\n\t"
"ds_read_b64_tr_b16 a[178:179], v249 offset:1664\n\t"
"v_mfma_f32_16x16x32_bf16 v[76:79], a[32:35], a[156:159], v[76:79]\n\t"
"v_cvt_pk_bf16_f32 v208, v208, v209\n\t"
"v_cvt_pk_bf16_f32 v209, v210, v211\n\t"
"v_mfma_f32_16x16x32_bf16 v[76:79], a[36:39], a[172:175], v[76:79]\n\t"
"ds_read_b64_tr_b16 a[180:181], v250 offset:1152\n\t"
"ds_read_b64_tr_b16 a[182:183], v250 offset:1664\n\t"
"v_mfma_f32_16x16x32_bf16 v[60:63], a[24:27], a[156:159], v[60:63]\n\t"
"ds_read_b128 a[156:159], v216 offset:19456\n\t"
"v_mfma_f32_16x16x32_bf16 v[60:63], a[28:31], a[172:175], v[60:63]\n\t"
"s_mov_b32 s20, 454656\n\t"
"buffer_load_dwordx4 a[136:139], v248, s[8:11], s20 offen offset:1024 sc1\n\t"
"v_mfma_f32_16x16x32_bf16 v[192:195], a[184:187], a[208:211], 0\n\t"
"v_cvt_pk_bf16_f32 v210, v212, v213\n\t"
"v_cvt_pk_bf16_f32 v211, v214, v215\n\t"
"s_waitcnt vmcnt(23) lgkmcnt(9)\n\t"
"v_mfma_f32_16x16x32_bf16 v[96:99], a[48:51], a[144:147], v[96:99]\n\t"
"ds_read_b128 a[172:175], v216 offset:27648\n\t"
"v_mfma_f32_16x16x32_bf16 v[96:99], a[52:55], a[160:163], v[96:99]\n\t"
"buffer_load_dwordx4 a[140:143], v248, s[8:11], s20 offen offset:2048 sc1\n\t"
"v_mfma_f32_16x16x32_bf16 v[196:199], a[188:191], a[208:211], 0\n\t"
"ds_write_b128 v217, v[208:211] offset:0\n\t"
"v_mfma_f32_16x16x32_bf16 v[112:115], a[56:59], a[144:147], v[112:115]\n\t"
"v_cvt_pk_bf16_f32 v192, v192, v193\n\t"
"v_cvt_pk_bf16_f32 v193, v194, v195\n\t"
"v_mfma_f32_16x16x32_bf16 v[112:115], a[60:63], a[160:163], v[112:115]\n\t"
"buffer_load_dwordx4 a[132:135], v248, s[8:11], s20 offen offset:0 sc1\n\t"
"v_mfma_f32_16x16x32_bf16 v[128:131], a[64:67], a[144:147], v[128:131]\n\t"
"ds_read_b128 a[144:147], v216 offset:20480\n\t"
"v_mfma_f32_16x16x32_bf16 v[128:131], a[68:71], a[160:163], v[128:131]\n\t"
"v_cvt_pk_bf16_f32 v194, v196, v197\n\t"
"v_cvt_pk_bf16_f32 v195, v198, v199\n\t"
"v_mfma_f32_16x16x32_bf16 v[200:203], a[192:195], a[208:211], 0\n\t"
"s_mov_b32 s20, 442368\n\t"
"buffer_load_dwordx4 a[128:131], v248, s[8:11], s20 offen offset:2048 sc1\n\t"
"s_waitcnt lgkmcnt(10)\n\t"
"v_mfma_f32_16x16x32_bf16 v[132:135], a[64:67], a[148:151], v[132:135]\n\t"
"ds_read_b128 a[160:163], v216 offset:28672\n\t"
"v_mfma_f32_16x16x32_bf16 v[132:135], a[68:71], a[164:167], v[132:135]\n\t"
"ds_write_b128 v219, v[192:195] offset:0\n\t"
"v_mfma_f32_16x16x32_bf16 v[116:119], a[56:59], a[148:151], v[116:119]\n\t"
"ds_read_b64_tr_b16 a[184:185], v251 offset:3456\n\t"
"v_mfma_f32_16x16x32_bf16 v[116:119], a[60:63], a[164:167], v[116:119]\n\t"
"buffer_load_dwordx4 a[120:123], v248, s[8:11], s20 offen offset:0 sc1\n\t"
"v_mfma_f32_16x16x32_bf16 v[204:207], a[196:199], a[208:211], 0\n\t"
"v_cvt_pk_bf16_f32 v200, v200, v201\n\t"
"v_cvt_pk_bf16_f32 v201, v202, v203\n\t"
"v_mfma_f32_16x16x32_bf16 v[100:103], a[48:51], a[148:151], v[100:103]\n\t"
"ds_read_b128 a[148:151], v216 offset:21504\n\t"
"v_mfma_f32_16x16x32_bf16 v[100:103], a[52:55], a[164:167], v[100:103]\n\t"
"buffer_load_dwordx4 a[124:127], v248, s[8:11], s20 offen offset:1024 sc1\n\t"
"v_mfma_f32_16x16x32_bf16 v[208:211], a[200:203], a[208:211], 0\n\t"
"ds_read_b128 a[164:167], v216 offset:29696\n\t"
"ds_read_b64_tr_b16 a[186:187], v251 offset:3968\n\t"
"s_waitcnt lgkmcnt(14)\n\t"
"v_mfma_f32_16x16x32_bf16 v[104:107], a[48:51], a[152:155], v[104:107]\n\t"
"v_cvt_pk_bf16_f32 v202, v204, v205\n\t"
"v_cvt_pk_bf16_f32 v203, v206, v207\n\t"
"v_mfma_f32_16x16x32_bf16 v[104:107], a[52:55], a[168:171], v[104:107]\n\t"
"ds_write_b128 v217, v[200:203] offset:8192\n\t"
"v_mfma_f32_16x16x32_bf16 v[120:123], a[56:59], a[152:155], v[120:123]\n\t"
"ds_read_b64_tr_b16 a[188:189], v252 offset:3456\n\t"
"v_mfma_f32_16x16x32_bf16 v[120:123], a[60:63], a[168:171], v[120:123]\n\t"
"s_mov_b32 s20, 724992\n\t"
"buffer_load_dwordx4 a[16:19], v248, s[8:11], s20 offen offset:1024 sc1\n\t"
"v_mfma_f32_16x16x32_bf16 v[212:215], a[204:207], a[208:211], 0\n\t"
"v_cvt_pk_bf16_f32 v208, v208, v209\n\t"
"v_cvt_pk_bf16_f32 v209, v210, v211\n\t"
"v_mfma_f32_16x16x32_bf16 v[136:139], a[64:67], a[152:155], v[136:139]\n\t"
"ds_read_b64_tr_b16 a[190:191], v252 offset:3968\n\t"
"v_mfma_f32_16x16x32_bf16 v[136:139], a[68:71], a[168:171], v[136:139]\n\t"
"buffer_load_dwordx4 a[20:23], v248, s[8:11], s20 offen offset:2048 sc1\n\t"
"s_waitcnt lgkmcnt(11)\n\t"
"v_mfma_f32_16x16x32_bf16 v[140:143], a[64:67], a[156:159], v[140:143]\n\t"
"ds_read_b64_tr_b16 a[192:193], v251 offset:1152\n\t"
"v_mfma_f32_16x16x32_bf16 v[140:143], a[68:71], a[172:175], v[140:143]\n\t"
"ds_read_b64_tr_b16 a[194:195], v251 offset:1664\n\t"
"ds_read_b64_tr_b16 a[196:197], v252 offset:1152\n\t"
"v_mfma_f32_16x16x32_bf16 v[124:127], a[56:59], a[156:159], v[124:127]\n\t"
"ds_read_b64_tr_b16 a[198:199], v252 offset:1664\n\t"
"v_cvt_pk_bf16_f32 v210, v212, v213\n\t"
"v_mfma_f32_16x16x32_bf16 v[124:127], a[60:63], a[172:175], v[124:127]\n\t"
"buffer_load_dwordx4 a[12:15], v248, s[8:11], s20 offen offset:0 sc1\n\t"
"v_mfma_f32_16x16x32_bf16 v[108:111], a[48:51], a[156:159], v[108:111]\n\t"
"ds_read_b64_tr_b16 a[200:201], v249 offset:3456\n\t"
"v_cvt_pk_bf16_f32 v211, v214, v215\n\t"
"v_mfma_f32_16x16x32_bf16 v[108:111], a[52:55], a[172:175], v[108:111]\n\t"
"ds_write_b128 v219, v[208:211] offset:8192\n\t"
"s_waitcnt vmcnt(15) lgkmcnt(14)\n\t"
"v_mfma_f32_16x16x32_bf16 v[144:147], a[72:75], a[144:147], v[144:147]\n\t"
"ds_read_b64_tr_b16 a[202:203], v249 offset:3968\n\t"
"v_mfma_f32_16x16x32_bf16 v[144:147], a[76:79], a[160:163], v[144:147]\n\t"
"ds_read_b64_tr_b16 a[204:205], v250 offset:3456\n\t"
"ds_read_b64_tr_b16 a[206:207], v250 offset:3968\n\t"
"v_mfma_f32_16x16x32_bf16 v[160:163], a[80:83], a[144:147], v[160:163]\n\t"
"ds_read_b128 a[152:155], v216 offset:22528\n\t"
"v_mfma_f32_16x16x32_bf16 v[160:163], a[84:87], a[160:163], v[160:163]\n\t"
"s_mov_b32 s20, 712704\n\t"
"buffer_load_dwordx4 a[8:11], v248, s[8:11], s20 offen offset:2048 sc1\n\t"
"v_mfma_f32_16x16x32_bf16 v[176:179], a[88:91], a[144:147], v[176:179]\n\t"
"ds_read_b128 a[168:171], v216 offset:30720\n\t"
"v_mfma_f32_16x16x32_bf16 v[176:179], a[92:95], a[160:163], v[176:179]\n\t"
"ds_read_b128 a[156:159], v216 offset:23552\n\t"
"ds_read_b128 a[172:175], v216 offset:31744\n\t"
"s_waitcnt lgkmcnt(15)\n\t"
"v_mfma_f32_16x16x32_bf16 v[180:183], a[88:91], a[148:151], v[180:183]\n\t"
"v_mfma_f32_16x16x32_bf16 v[180:183], a[92:95], a[164:167], v[180:183]\n\t"
"buffer_load_dwordx4 a[0:3], v248, s[8:11], s20 offen offset:0 sc1\n\t"
"v_mfma_f32_16x16x32_bf16 v[164:167], a[80:83], a[148:151], v[164:167]\n\t"
"v_mfma_f32_16x16x32_bf16 v[164:167], a[84:87], a[164:167], v[164:167]\n\t"
"buffer_load_dwordx4 a[4:7], v248, s[8:11], s20 offen offset:1024 sc1\n\t"
"v_mfma_f32_16x16x32_bf16 v[148:151], a[72:75], a[148:151], v[148:151]\n\t"
"v_mfma_f32_16x16x32_bf16 v[148:151], a[76:79], a[164:167], v[148:151]\n\t"
"s_mov_b32 s20, 749568\n\t"
"buffer_load_dwordx4 a[40:43], v248, s[8:11], s20 offen offset:1024 sc1\n\t"
"s_waitcnt lgkmcnt(0)\n\t"
"s_barrier\n\t"
"v_mfma_f32_16x16x32_bf16 v[152:155], a[72:75], a[152:155], v[152:155]\n\t"
"ds_read_b128 a[144:147], v216 offset:0\n\t"
"v_add_u32 v249, 39424, v225\n\t"
"v_mfma_f32_16x16x32_bf16 v[152:155], a[76:79], a[168:171], v[152:155]\n\t"
"ds_read_b128 a[160:163], v216 offset:8192\n\t"
"ds_read_b128 a[148:151], v216 offset:1024\n\t"
"v_mfma_f32_16x16x32_bf16 v[168:171], a[80:83], a[152:155], v[168:171]\n\t"
"ds_read_b128 a[164:167], v216 offset:9216\n\t"
"v_xor_b32 v250, 32, v249\n\t"
"v_mfma_f32_16x16x32_bf16 v[168:171], a[84:87], a[168:171], v[168:171]\n\t"
"buffer_load_dwordx4 a[44:47], v248, s[8:11], s20 offen offset:2048 sc1\n\t"
"v_mfma_f32_16x16x32_bf16 v[184:187], a[88:91], a[152:155], v[184:187]\n\t"
"v_xor_b32 v251, 64, v249\n\t"
"v_xor_b32 v252, 96, v249\n\t"
"v_mfma_f32_16x16x32_bf16 v[184:187], a[92:95], a[168:171], v[184:187]\n\t"
"buffer_load_dwordx4 a[36:39], v248, s[8:11], s20 offen offset:0 sc1\n\t"
"v_mfma_f32_16x16x32_bf16 v[192:195], a[176:179], a[208:211], 0\n\t"
"s_mov_b32 s20, 737280\n\t"
"buffer_load_dwordx4 a[32:35], v248, s[8:11], s20 offen offset:2048 sc1\n\t"
"v_mfma_f32_16x16x32_bf16 v[188:191], a[88:91], a[156:159], v[188:191]\n\t"
"ds_read_b128 a[152:155], v216 offset:2048\n\t"
"v_mfma_f32_16x16x32_bf16 v[188:191], a[92:95], a[172:175], v[188:191]\n\t"
"ds_read_b128 a[168:171], v216 offset:10240\n\t"
"ds_read_b64_tr_b16 a[176:177], v249 offset:0\n\t"
"v_mfma_f32_16x16x32_bf16 v[196:199], a[180:183], a[208:211], 0\n\t"
"ds_read_b64_tr_b16 a[178:179], v249 offset:512\n\t"
"ds_read_b64_tr_b16 a[180:181], v250 offset:0\n\t"
"v_mfma_f32_16x16x32_bf16 v[172:175], a[80:83], a[156:159], v[172:175]\n\t"
"v_cvt_pk_bf16_f32 v192, v192, v193\n\t"
"v_cvt_pk_bf16_f32 v193, v194, v195\n\t"
"v_mfma_f32_16x16x32_bf16 v[172:175], a[84:87], a[172:175], v[172:175]\n\t"
"ds_read_b64_tr_b16 a[182:183], v250 offset:512\n\t"
"v_mfma_f32_16x16x32_bf16 v[156:159], a[72:75], a[156:159], v[156:159]\n\t"
"ds_read_b128 a[156:159], v216 offset:3072\n\t"
"v_mfma_f32_16x16x32_bf16 v[156:159], a[76:79], a[172:175], v[156:159]\n\t"
"buffer_load_dwordx4 a[24:27], v248, s[8:11], s20 offen offset:0 sc1\n\t"
"v_mfma_f32_16x16x32_bf16 v[200:203], a[184:187], a[208:211], 0\n\t"
"buffer_load_dwordx4 a[28:31], v248, s[8:11], s20 offen offset:1024 sc1\n\t"
"s_waitcnt vmcnt(18) lgkmcnt(9)\n\t"
"v_mfma_f32_16x16x32_bf16 v[0:3], a[96:99], a[144:147], v[0:3]\n\t"
"v_cvt_pk_bf16_f32 v194, v196, v197\n\t"
"v_cvt_pk_bf16_f32 v195, v198, v199\n\t"
"v_mfma_f32_16x16x32_bf16 v[0:3], a[100:103], a[160:163], v[0:3]\n\t"
"ds_read_b128 a[172:175], v216 offset:11264\n\t"
"ds_read_b64_tr_b16 a[184:185], v251 offset:2304\n\t"
"v_mfma_f32_16x16x32_bf16 v[204:207], a[188:191], a[208:211], 0\n\t"
"s_mov_b32 s20, 823296\n\t"
"buffer_load_dwordx4 a[64:67], v248, s[8:11], s20 offen offset:1024 sc1\n\t"
"v_mfma_f32_16x16x32_bf16 v[16:19], a[104:107], a[144:147], v[16:19]\n\t"
"v_cvt_pk_bf16_f32 v200, v200, v201\n\t"
"v_mfma_f32_16x16x32_bf16 v[16:19], a[108:111], a[160:163], v[16:19]\n\t"
"ds_write_b128 v217, v[192:195] offset:16384\n\t"
"v_mfma_f32_16x16x32_bf16 v[32:35], a[112:115], a[144:147], v[32:35]\n\t"
"v_cvt_pk_bf16_f32 v201, v202, v203\n\t"
"ds_read_b128 a[144:147], v216 offset:4096\n\t"
"v_mfma_f32_16x16x32_bf16 v[32:35], a[116:119], a[160:163], v[32:35]\n\t"
"buffer_load_dwordx4 a[68:71], v248, s[8:11], s20 offen offset:2048 sc1\n\t"
"v_mfma_f32_16x16x32_bf16 v[208:211], a[192:195], a[208:211], 0\n\t"
"v_cvt_pk_bf16_f32 v202, v204, v205\n\t"
"s_waitcnt lgkmcnt(11)\n\t"
"v_mfma_f32_16x16x32_bf16 v[36:39], a[112:115], a[148:151], v[36:39]\n\t"
"v_cvt_pk_bf16_f32 v203, v206, v207\n\t"
"ds_read_b128 a[160:163], v216 offset:12288\n\t"
"v_mfma_f32_16x16x32_bf16 v[36:39], a[116:119], a[164:167], v[36:39]\n\t"
"buffer_load_dwordx4 a[60:63], v248, s[8:11], s20 offen offset:0 sc1\n\t"
"v_mfma_f32_16x16x32_bf16 v[20:23], a[104:107], a[148:151], v[20:23]\n\t"
"ds_read_b64_tr_b16 a[186:187], v251 offset:2816\n\t"
"v_mfma_f32_16x16x32_bf16 v[20:23], a[108:111], a[164:167], v[20:23]\n\t"
"ds_write_b128 v219, v[200:203] offset:16384\n\t"
"v_mfma_f32_16x16x32_bf16 v[212:215], a[196:199], a[208:211], 0\n\t"
"v_cvt_pk_bf16_f32 v208, v208, v209\n\t"
"v_cvt_pk_bf16_f32 v209, v210, v211\n\t"
"v_mfma_f32_16x16x32_bf16 v[4:7], a[96:99], a[148:151], v[4:7]\n\t"
"ds_read_b128 a[148:151], v216 offset:5120\n\t"
"v_mfma_f32_16x16x32_bf16 v[4:7], a[100:103], a[164:167], v[4:7]\n\t"
"s_mov_b32 s20, 811008\n\t"
"buffer_load_dwordx4 a[56:59], v248, s[8:11], s20 offen offset:2048 sc1\n\t"
"v_mfma_f32_16x16x32_bf16 v[192:195], a[200:203], a[208:211], 0\n\t"
"ds_read_b128 a[164:167], v216 offset:13312\n\t"
"ds_read_b64_tr_b16 a[188:189], v252 offset:2304\n\t"
"s_waitcnt lgkmcnt(15)\n\t"
"v_mfma_f32_16x16x32_bf16 v[8:11], a[96:99], a[152:155], v[8:11]\n\t"
"v_cvt_pk_bf16_f32 v210, v212, v213\n\t"
"v_cvt_pk_bf16_f32 v211, v214, v215\n\t"
"v_mfma_f32_16x16x32_bf16 v[8:11], a[100:103], a[168:171], v[8:11]\n\t"
"buffer_load_dwordx4 a[48:51], v248, s[8:11], s20 offen offset:0 sc1\n\t"
"v_mfma_f32_16x16x32_bf16 v[24:27], a[104:107], a[152:155], v[24:27]\n\t"
"ds_read_b64_tr_b16 a[190:191], v252 offset:2816\n\t"
"v_mfma_f32_16x16x32_bf16 v[24:27], a[108:111], a[168:171], v[24:27]\n\t"
"v_cvt_pk_bf16_f32 v192, v192, v193\n\t"
"v_cvt_pk_bf16_f32 v193, v194, v195\n\t"
"v_mfma_f32_16x16x32_bf16 v[196:199], a[204:207], a[208:211], 0\n\t"
"buffer_load_dwordx4 a[52:55], v248, s[8:11], s20 offen offset:1024 sc1\n\t"
"v_mfma_f32_16x16x32_bf16 v[40:43], a[112:115], a[152:155], v[40:43]\n\t"
"ds_read_b64_tr_b16 a[192:193], v251 offset:0\n\t"
"v_mfma_f32_16x16x32_bf16 v[40:43], a[116:119], a[168:171], v[40:43]\n\t"
"ds_write_b128 v217, v[208:211] offset:24576\n\t"
"s_waitcnt lgkmcnt(12)\n\t"
"v_mfma_f32_16x16x32_bf16 v[44:47], a[112:115], a[156:159], v[44:47]\n\t"
"ds_read_b64_tr_b16 a[194:195], v251 offset:512\n\t"
"v_mfma_f32_16x16x32_bf16 v[44:47], a[116:119], a[172:175], v[44:47]\n\t"
"s_mov_b32 s20, 847872\n\t"
"buffer_load_dwordx4 a[88:91], v248, s[8:11], s20 offen offset:1024 sc1\n\t"
"v_mfma_f32_16x16x32_bf16 v[28:31], a[104:107], a[156:159], v[28:31]\n\t"
"ds_read_b64_tr_b16 a[196:197], v252 offset:0\n\t"
"v_cvt_pk_bf16_f32 v194, v196, v197\n\t"
"v_mfma_f32_16x16x32_bf16 v[28:31], a[108:111], a[172:175], v[28:31]\n\t"
"ds_read_b64_tr_b16 a[198:199], v252 offset:512\n\t"
"ds_read_b64_tr_b16 a[200:201], v249 offset:2304\n\t"
"v_mfma_f32_16x16x32_bf16 v[12:15], a[96:99], a[156:159], v[12:15]\n\t"
"ds_read_b64_tr_b16 a[202:203], v249 offset:2816\n\t"
"v_cvt_pk_bf16_f32 v195, v198, v199\n\t"
"v_mfma_f32_16x16x32_bf16 v[12:15], a[100:103], a[172:175], v[12:15]\n\t"
"ds_write_b128 v219, v[192:195] offset:24576\n\t"
"s_waitcnt vmcnt(19) lgkmcnt(14)\n\t"
"v_mfma_f32_16x16x32_bf16 v[48:51], a[120:123], a[144:147], v[48:51]\n\t"
"ds_read_b64_tr_b16 a[204:205], v250 offset:2304\n\t"
"v_mfma_f32_16x16x32_bf16 v[48:51], a[124:127], a[160:163], v[48:51]\n\t"
"ds_read_b64_tr_b16 a[206:207], v250 offset:2816\n\t"
"buffer_load_dwordx4 a[92:95], v248, s[8:11], s20 offen offset:2048 sc1\n\t"
"v_mfma_f32_16x16x32_bf16 v[64:67], a[128:131], a[144:147], v[64:67]\n\t"
"ds_read_b128 a[152:155], v216 offset:6144\n\t"
"v_mfma_f32_16x16x32_bf16 v[64:67], a[132:135], a[160:163], v[64:67]\n\t"
"ds_read_b128 a[168:171], v216 offset:14336\n\t"
"ds_read_b128 a[156:159], v216 offset:7168\n\t"
"v_mfma_f32_16x16x32_bf16 v[80:83], a[136:139], a[144:147], v[80:83]\n\t"
"ds_read_b128 a[172:175], v216 offset:15360\n\t"
"v_mfma_f32_16x16x32_bf16 v[80:83], a[140:143], a[160:163], v[80:83]\n\t"
"buffer_load_dwordx4 a[84:87], v248, s[8:11], s20 offen offset:0 sc1\n\t"
"s_waitcnt lgkmcnt(15)\n\t"
"v_mfma_f32_16x16x32_bf16 v[84:87], a[136:139], a[148:151], v[84:87]\n\t"
"v_mfma_f32_16x16x32_bf16 v[84:87], a[140:143], a[164:167], v[84:87]\n\t"
"s_mov_b32 s20, 835584\n\t"
"buffer_load_dwordx4 a[80:83], v248, s[8:11], s20 offen offset:2048 sc1\n\t"
"v_mfma_f32_16x16x32_bf16 v[68:71], a[128:131], a[148:151], v[68:71]\n\t"
"v_mfma_f32_16x16x32_bf16 v[68:71], a[132:135], a[164:167], v[68:71]\n\t"
"buffer_load_dwordx4 a[72:75], v248, s[8:11], s20 offen offset:0 sc1\n\t"
"v_mfma_f32_16x16x32_bf16 v[52:55], a[120:123], a[148:151], v[52:55]\n\t"
"v_mfma_f32_16x16x32_bf16 v[52:55], a[124:127], a[164:167], v[52:55]\n\t"
"s_waitcnt lgkmcnt(0)\n\t"
"s_barrier\n\t"
"v_mfma_f32_16x16x32_bf16 v[56:59], a[120:123], a[152:155], v[56:59]\n\t"
"ds_read_b128 a[144:147], v216 offset:16384\n\t"
"v_mfma_f32_16x16x32_bf16 v[56:59], a[124:127], a[168:171], v[56:59]\n\t"
"ds_read_b128 a[160:163], v216 offset:24576\n\t"
"ds_read_b128 a[148:151], v216 offset:17408\n\t"
"v_mfma_f32_16x16x32_bf16 v[72:75], a[128:131], a[152:155], v[72:75]\n\t"
"ds_read_b128 a[164:167], v216 offset:25600\n\t"
"v_mfma_f32_16x16x32_bf16 v[72:75], a[132:135], a[168:171], v[72:75]\n\t"
"buffer_load_dwordx4 a[76:79], v248, s[8:11], s20 offen offset:1024 sc1\n\t"
"v_mfma_f32_16x16x32_bf16 v[88:91], a[136:139], a[152:155], v[88:91]\n\t"
"ds_read_b128 a[152:155], v216 offset:18432\n\t"
"v_mfma_f32_16x16x32_bf16 v[88:91], a[140:143], a[168:171], v[88:91]\n\t"
"s_mov_b32 s20, 1118208\n\t"
"buffer_load_dwordx4 a[112:115], v248, s[8:11], s20 offen offset:1024 sc1\n\t"
"v_mfma_f32_16x16x32_bf16 v[200:203], a[176:179], a[208:211], 0\n\t"
"buffer_load_dwordx4 a[116:119], v248, s[8:11], s20 offen offset:2048 sc1\n\t"
"v_mfma_f32_16x16x32_bf16 v[92:95], a[136:139], a[156:159], v[92:95]\n\t"
"ds_read_b128 a[168:171], v216 offset:26624\n\t"
"v_mfma_f32_16x16x32_bf16 v[92:95], a[140:143], a[172:175], v[92:95]\n\t"
"buffer_load_dwordx4 a[108:111], v248, s[8:11], s20 offen offset:0 sc1\n\t"
"v_mfma_f32_16x16x32_bf16 v[204:207], a[180:183], a[208:211], 0\n\t"
"ds_read_b64_tr_b16 a[176:177], v249 offset:1152\n\t"
"ds_read_b64_tr_b16 a[178:179], v249 offset:1664\n\t"
"v_mfma_f32_16x16x32_bf16 v[76:79], a[128:131], a[156:159], v[76:79]\n\t"
"v_cvt_pk_bf16_f32 v200, v200, v201\n\t"
"v_cvt_pk_bf16_f32 v201, v202, v203\n\t"
"v_mfma_f32_16x16x32_bf16 v[76:79], a[132:135], a[172:175], v[76:79]\n\t"
"ds_read_b64_tr_b16 a[180:181], v250 offset:1152\n\t"
"ds_read_b64_tr_b16 a[182:183], v250 offset:1664\n\t"
"v_mfma_f32_16x16x32_bf16 v[60:63], a[120:123], a[156:159], v[60:63]\n\t"
"ds_read_b128 a[156:159], v216 offset:19456\n\t"
"v_mfma_f32_16x16x32_bf16 v[60:63], a[124:127], a[172:175], v[60:63]\n\t"
"s_mov_b32 s20, 1105920\n\t"
"buffer_load_dwordx4 a[104:107], v248, s[8:11], s20 offen offset:2048 sc1\n\t"
"v_mfma_f32_16x16x32_bf16 v[208:211], a[184:187], a[208:211], 0\n\t"
"v_cvt_pk_bf16_f32 v202, v204, v205\n\t"
"v_cvt_pk_bf16_f32 v203, v206, v207\n\t"
"s_waitcnt vmcnt(22) lgkmcnt(9)\n\t"
"v_mfma_f32_16x16x32_bf16 v[96:99], a[0:3], a[144:147], v[96:99]\n\t"
"ds_read_b128 a[172:175], v216 offset:27648\n\t"
"v_mfma_f32_16x16x32_bf16 v[96:99], a[4:7], a[160:163], v[96:99]\n\t"
"buffer_load_dwordx4 a[96:99], v248, s[8:11], s20 offen offset:0 sc1\n\t"
"v_mfma_f32_16x16x32_bf16 v[212:215], a[188:191], a[208:211], 0\n\t"
"ds_write_b128 v217, v[200:203] offset:0\n\t"
"v_mfma_f32_16x16x32_bf16 v[112:115], a[8:11], a[144:147], v[112:115]\n\t"
"v_cvt_pk_bf16_f32 v208, v208, v209\n\t"
"v_cvt_pk_bf16_f32 v209, v210, v211\n\t"
"v_mfma_f32_16x16x32_bf16 v[112:115], a[12:15], a[160:163], v[112:115]\n\t"
"buffer_load_dwordx4 a[100:103], v248, s[8:11], s20 offen offset:1024 sc1\n\t"
"v_mfma_f32_16x16x32_bf16 v[128:131], a[16:19], a[144:147], v[128:131]\n\t"
"ds_read_b128 a[144:147], v216 offset:20480\n\t"
"v_mfma_f32_16x16x32_bf16 v[128:131], a[20:23], a[160:163], v[128:131]\n\t"
"v_cvt_pk_bf16_f32 v210, v212, v213\n\t"
"v_cvt_pk_bf16_f32 v211, v214, v215\n\t"
"v_mfma_f32_16x16x32_bf16 v[192:195], a[192:195], a[208:211], 0\n\t"
"ds_read_b128 a[160:163], v216 offset:28672\n\t"
"ds_read_b64_tr_b16 a[184:185], v251 offset:3456\n\t"
"s_waitcnt lgkmcnt(12)\n\t"
"v_mfma_f32_16x16x32_bf16 v[132:135], a[16:19], a[148:151], v[132:135]\n\t"
"ds_read_b64_tr_b16 a[186:187], v251 offset:3968\n\t"
"v_mfma_f32_16x16x32_bf16 v[132:135], a[20:23], a[164:167], v[132:135]\n\t"
"s_mov_b32 s20, 1142784\n\t"
"buffer_load_dwordx4 a[136:139], v248, s[8:11], s20 offen offset:1024 sc1\n\t"
"v_mfma_f32_16x16x32_bf16 v[116:119], a[8:11], a[148:151], v[116:119]\n\t"
"ds_read_b64_tr_b16 a[188:189], v252 offset:3456\n\t"
"v_mfma_f32_16x16x32_bf16 v[116:119], a[12:15], a[164:167], v[116:119]\n\t"
"buffer_load_dwordx4 a[140:143], v248, s[8:11], s20 offen offset:2048 sc1\n\t"
"v_mfma_f32_16x16x32_bf16 v[196:199], a[196:199], a[208:211], 0\n\t"
"ds_write_b128 v219, v[208:211] offset:0\n\t"
"v_mfma_f32_16x16x32_bf16 v[100:103], a[0:3], a[148:151], v[100:103]\n\t"
"v_cvt_pk_bf16_f32 v192, v192, v193\n\t"
"v_cvt_pk_bf16_f32 v193, v194, v195\n\t"
"v_mfma_f32_16x16x32_bf16 v[100:103], a[4:7], a[164:167], v[100:103]\n\t"
"ds_read_b128 a[148:151], v216 offset:21504\n\t"
"ds_read_b128 a[164:167], v216 offset:29696\n\t"
"v_mfma_f32_16x16x32_bf16 v[200:203], a[200:203], a[208:211], 0\n\t"
"buffer_load_dwordx4 a[132:135], v248, s[8:11], s20 offen offset:0 sc1\n\t"
"s_waitcnt lgkmcnt(15)\n\t"
"v_mfma_f32_16x16x32_bf16 v[104:107], a[0:3], a[152:155], v[104:107]\n\t"
"v_cvt_pk_bf16_f32 v194, v196, v197\n\t"
"v_cvt_pk_bf16_f32 v195, v198, v199\n\t"
"v_mfma_f32_16x16x32_bf16 v[104:107], a[4:7], a[168:171], v[104:107]\n\t"
"ds_write_b128 v217, v[192:195] offset:8192\n\t"
"v_mfma_f32_16x16x32_bf16 v[120:123], a[8:11], a[152:155], v[120:123]\n\t"
"ds_read_b64_tr_b16 a[190:191], v252 offset:3968\n\t"
"v_mfma_f32_16x16x32_bf16 v[120:123], a[12:15], a[168:171], v[120:123]\n\t"
"s_mov_b32 s20, 1130496\n\t"
"buffer_load_dwordx4 a[128:131], v248, s[8:11], s20 offen offset:2048 sc1\n\t"
"v_mfma_f32_16x16x32_bf16 v[204:207], a[204:207], a[208:211], 0\n\t"
"v_cvt_pk_bf16_f32 v200, v200, v201\n\t"
"v_cvt_pk_bf16_f32 v201, v202, v203\n\t"
"v_mfma_f32_16x16x32_bf16 v[136:139], a[16:19], a[152:155], v[136:139]\n\t"
"ds_read_b64_tr_b16 a[192:193], v251 offset:1152\n\t"
"v_mfma_f32_16x16x32_bf16 v[136:139], a[20:23], a[168:171], v[136:139]\n\t"
"buffer_load_dwordx4 a[120:123], v248, s[8:11], s20 offen offset:0 sc1\n\t"
"s_waitcnt lgkmcnt(12)\n\t"
"v_mfma_f32_16x16x32_bf16 v[140:143], a[16:19], a[156:159], v[140:143]\n\t"
"ds_read_b64_tr_b16 a[194:195], v251 offset:1664\n\t"
"v_mfma_f32_16x16x32_bf16 v[140:143], a[20:23], a[172:175], v[140:143]\n\t"
"ds_read_b64_tr_b16 a[196:197], v252 offset:1152\n\t"
"ds_read_b64_tr_b16 a[198:199], v252 offset:1664\n\t"
"v_mfma_f32_16x16x32_bf16 v[124:127], a[8:11], a[156:159], v[124:127]\n\t"
"ds_read_b64_tr_b16 a[200:201], v249 offset:3456\n\t"
"v_cvt_pk_bf16_f32 v202, v204, v205\n\t"
"v_mfma_f32_16x16x32_bf16 v[124:127], a[12:15], a[172:175], v[124:127]\n\t"
"buffer_load_dwordx4 a[124:127], v248, s[8:11], s20 offen offset:1024 sc1\n\t"
"v_mfma_f32_16x16x32_bf16 v[108:111], a[0:3], a[156:159], v[108:111]\n\t"
"ds_read_b64_tr_b16 a[202:203], v249 offset:3968\n\t"
"v_cvt_pk_bf16_f32 v203, v206, v207\n\t"
"v_mfma_f32_16x16x32_bf16 v[108:111], a[4:7], a[172:175], v[108:111]\n\t"
"ds_write_b128 v219, v[200:203] offset:8192\n\t"
"s_waitcnt vmcnt(24) lgkmcnt(15)\n\t"
"v_mfma_f32_16x16x32_bf16 v[144:147], a[24:27], a[144:147], v[144:147]\n\t"
"ds_read_b64_tr_b16 a[204:205], v250 offset:3456\n\t"
"v_mfma_f32_16x16x32_bf16 v[144:147], a[28:31], a[160:163], v[144:147]\n\t"
"ds_read_b64_tr_b16 a[206:207], v250 offset:3968\n\t"
"ds_read_b128 a[152:155], v216 offset:22528\n\t"
"v_mfma_f32_16x16x32_bf16 v[160:163], a[32:35], a[144:147], v[160:163]\n\t"
"ds_read_b128 a[168:171], v216 offset:30720\n\t"
"v_mfma_f32_16x16x32_bf16 v[160:163], a[36:39], a[160:163], v[160:163]\n\t"
"s_mov_b32 s20, 135168\n\t"
"buffer_load_dwordx4 a[16:19], v248, s[8:11], s20 offen offset:1024 sc1\n\t"
"v_mfma_f32_16x16x32_bf16 v[176:179], a[40:43], a[144:147], v[176:179]\n\t"
"ds_read_b128 a[156:159], v216 offset:23552\n\t"
"v_mfma_f32_16x16x32_bf16 v[176:179], a[44:47], a[160:163], v[176:179]\n\t"
"ds_read_b128 a[172:175], v216 offset:31744\n\t"
"s_waitcnt lgkmcnt(15)\n\t"
"v_mfma_f32_16x16x32_bf16 v[180:183], a[40:43], a[148:151], v[180:183]\n\t"
"v_mfma_f32_16x16x32_bf16 v[180:183], a[44:47], a[164:167], v[180:183]\n\t"
"buffer_load_dwordx4 a[20:23], v248, s[8:11], s20 offen offset:2048 sc1\n\t"
"v_mfma_f32_16x16x32_bf16 v[164:167], a[32:35], a[148:151], v[164:167]\n\t"
"v_mfma_f32_16x16x32_bf16 v[164:167], a[36:39], a[164:167], v[164:167]\n\t"
"buffer_load_dwordx4 a[12:15], v248, s[8:11], s20 offen offset:0 sc1\n\t"
"v_mfma_f32_16x16x32_bf16 v[148:151], a[24:27], a[148:151], v[148:151]\n\t"
"v_mfma_f32_16x16x32_bf16 v[148:151], a[28:31], a[164:167], v[148:151]\n\t"
"s_mov_b32 s20, 122880\n\t"
"buffer_load_dwordx4 a[8:11], v248, s[8:11], s20 offen offset:2048 sc1\n\t"
"s_waitcnt lgkmcnt(0)\n\t"
"s_barrier\n\t"
"v_mfma_f32_16x16x32_bf16 v[152:155], a[24:27], a[152:155], v[152:155]\n\t"
"ds_read_b128 a[144:147], v216 offset:0\n\t"
"v_add_u32 v249, 80896, v224\n\t"
"v_mfma_f32_16x16x32_bf16 v[152:155], a[28:31], a[168:171], v[152:155]\n\t"
"ds_read_b128 a[160:163], v216 offset:8192\n\t"
"ds_read_b128 a[148:151], v216 offset:1024\n\t"
"v_mfma_f32_16x16x32_bf16 v[168:171], a[32:35], a[152:155], v[168:171]\n\t"
"ds_read_b128 a[164:167], v216 offset:9216\n\t"
"v_xor_b32 v250, 32, v249\n\t"
"v_mfma_f32_16x16x32_bf16 v[168:171], a[36:39], a[168:171], v[168:171]\n\t"
"buffer_load_dwordx4 a[0:3], v248, s[8:11], s20 offen offset:0 sc1\n\t"
"v_mfma_f32_16x16x32_bf16 v[184:187], a[40:43], a[152:155], v[184:187]\n\t"
"v_xor_b32 v251, 64, v249\n\t"
"v_xor_b32 v252, 96, v249\n\t"
"v_mfma_f32_16x16x32_bf16 v[184:187], a[44:47], a[168:171], v[184:187]\n\t"
"ds_read_b128 a[152:155], v216 offset:2048\n\t"
"v_mfma_f32_16x16x32_bf16 v[208:211], a[176:179], a[208:211], 0\n\t"
"buffer_load_dwordx4 a[4:7], v248, s[8:11], s20 offen offset:1024 sc1\n\t"
"v_mfma_f32_16x16x32_bf16 v[188:191], a[40:43], a[156:159], v[188:191]\n\t"
"ds_read_b128 a[168:171], v216 offset:10240\n\t"
"v_mfma_f32_16x16x32_bf16 v[188:191], a[44:47], a[172:175], v[188:191]\n\t"
"s_mov_b32 s20, 159744\n\t"
"buffer_load_dwordx4 a[40:43], v248, s[8:11], s20 offen offset:1024 sc1\n\t"
"v_mfma_f32_16x16x32_bf16 v[212:215], a[180:183], a[208:211], 0\n\t"
"ds_read_b64_tr_b16 a[176:177], v249 offset:0\n\t"
"ds_read_b64_tr_b16 a[178:179], v249 offset:512\n\t"
"v_mfma_f32_16x16x32_bf16 v[172:175], a[32:35], a[156:159], v[172:175]\n\t"
"v_cvt_pk_bf16_f32 v208, v208, v209\n\t"
"v_cvt_pk_bf16_f32 v209, v210, v211\n\t"
"v_mfma_f32_16x16x32_bf16 v[172:175], a[36:39], a[172:175], v[172:175]\n\t"
"ds_read_b64_tr_b16 a[180:181], v250 offset:0\n\t"
"v_mfma_f32_16x16x32_bf16 v[156:159], a[24:27], a[156:159], v[156:159]\n\t"
"ds_read_b128 a[156:159], v216 offset:3072\n\t"
"v_mfma_f32_16x16x32_bf16 v[156:159], a[28:31], a[172:175], v[156:159]\n\t"
"buffer_load_dwordx4 a[44:47], v248, s[8:11], s20 offen offset:2048 sc1\n\t"
"v_mfma_f32_16x16x32_bf16 v[192:195], a[184:187], a[208:211], 0\n\t"
"buffer_load_dwordx4 a[36:39], v248, s[8:11], s20 offen offset:0 sc1\n\t"
"s_waitcnt vmcnt(27) lgkmcnt(8)\n\t"
"v_mfma_f32_16x16x32_bf16 v[0:3], a[48:51], a[144:147], v[0:3]\n\t"
"v_cvt_pk_bf16_f32 v210, v212, v213\n\t"
"v_cvt_pk_bf16_f32 v211, v214, v215\n\t"
"v_mfma_f32_16x16x32_bf16 v[0:3], a[52:55], a[160:163], v[0:3]\n\t"
"ds_read_b128 a[172:175], v216 offset:11264\n\t"
"ds_read_b64_tr_b16 a[182:183], v250 offset:512\n\t"
"v_mfma_f32_16x16x32_bf16 v[196:199], a[188:191], a[208:211], 0\n\t"
"ds_read_b64_tr_b16 a[184:185], v251 offset:2304\n\t"
"v_mfma_f32_16x16x32_bf16 v[16:19], a[56:59], a[144:147], v[16:19]\n\t"
"v_cvt_pk_bf16_f32 v192, v192, v193\n\t"
"v_cvt_pk_bf16_f32 v193, v194, v195\n\t"
"v_mfma_f32_16x16x32_bf16 v[16:19], a[60:63], a[160:163], v[16:19]\n\t"
"s_mov_b32 s20, 147456\n\t"
"buffer_load_dwordx4 a[32:35], v248, s[8:11], s20 offen offset:2048 sc1\n\t"
"v_mfma_f32_16x16x32_bf16 v[32:35], a[64:67], a[144:147], v[32:35]\n\t"
"ds_read_b128 a[144:147], v216 offset:4096\n\t"
"v_mfma_f32_16x16x32_bf16 v[32:35], a[68:71], a[160:163], v[32:35]\n\t"
"ds_write_b128 v217, v[208:211] offset:16384\n\t"
"v_mfma_f32_16x16x32_bf16 v[200:203], a[192:195], a[208:211], 0\n\t"
"buffer_load_dwordx4 a[24:27], v248, s[8:11], s20 offen offset:0 sc1\n\t"
"s_waitcnt lgkmcnt(11)\n\t"
"v_mfma_f32_16x16x32_bf16 v[36:39], a[64:67], a[148:151], v[36:39]\n\t"
"v_cvt_pk_bf16_f32 v194, v196, v197\n\t"
"v_mfma_f32_16x16x32_bf16 v[36:39], a[68:71], a[164:167], v[36:39]\n\t"
"v_cvt_pk_bf16_f32 v195, v198, v199\n\t"
"ds_read_b128 a[160:163], v216 offset:12288\n\t"
"v_mfma_f32_16x16x32_bf16 v[20:23], a[56:59], a[148:151], v[20:23]\n\t"
"ds_read_b64_tr_b16 a[186:187], v251 offset:2816\n\t"
"v_mfma_f32_16x16x32_bf16 v[20:23], a[60:63], a[164:167], v[20:23]\n\t"
"buffer_load_dwordx4 a[28:31], v248, s[8:11], s20 offen offset:1024 sc1\n\t"
"v_mfma_f32_16x16x32_bf16 v[204:207], a[196:199], a[208:211], 0\n\t"
"ds_write_b128 v219, v[192:195] offset:16384\n\t"
"v_mfma_f32_16x16x32_bf16 v[4:7], a[48:51], a[148:151], v[4:7]\n\t"
"v_cvt_pk_bf16_f32 v200, v200, v201\n\t"
"v_cvt_pk_bf16_f32 v201, v202, v203\n\t"
"v_mfma_f32_16x16x32_bf16 v[4:7], a[52:55], a[164:167], v[4:7]\n\t"
"ds_read_b128 a[148:151], v216 offset:5120\n\t"
"v_mfma_f32_16x16x32_bf16 v[208:211], a[200:203], a[208:211], 0\n\t"
"ds_read_b128 a[164:167], v216 offset:13312\n\t"
"ds_read_b64_tr_b16 a[188:189], v252 offset:2304\n\t"
"s_waitcnt lgkmcnt(15)\n\t"
"v_mfma_f32_16x16x32_bf16 v[8:11], a[48:51], a[152:155], v[8:11]\n\t"
"v_cvt_pk_bf16_f32 v202, v204, v205\n\t"
"v_cvt_pk_bf16_f32 v203, v206, v207\n\t"
"v_mfma_f32_16x16x32_bf16 v[8:11], a[52:55], a[168:171], v[8:11]\n\t"
"ds_read_b64_tr_b16 a[190:191], v252 offset:2816\n\t"
"v_mfma_f32_16x16x32_bf16 v[24:27], a[56:59], a[152:155], v[24:27]\n\t"
"ds_read_b64_tr_b16 a[192:193], v251 offset:0\n\t"
"v_mfma_f32_16x16x32_bf16 v[24:27], a[60:63], a[168:171], v[24:27]\n\t"
"v_cvt_pk_bf16_f32 v208, v208, v209\n\t"
"v_cvt_pk_bf16_f32 v209, v210, v211\n\t"
"v_mfma_f32_16x16x32_bf16 v[212:215], a[204:207], a[208:211], 0\n\t"
"ds_write_b128 v217, v[200:203] offset:24576\n\t"
"v_mfma_f32_16x16x32_bf16 v[40:43], a[64:67], a[152:155], v[40:43]\n\t"
"ds_read_b64_tr_b16 a[194:195], v251 offset:512\n\t"
"v_mfma_f32_16x16x32_bf16 v[40:43], a[68:71], a[168:171], v[40:43]\n\t"
"ds_read_b64_tr_b16 a[196:197], v252 offset:0\n\t"
"ds_read_b64_tr_b16 a[198:199], v252 offset:512\n\t"
"s_waitcnt lgkmcnt(15)\n\t"
"v_mfma_f32_16x16x32_bf16 v[44:47], a[64:67], a[156:159], v[44:47]\n\t"
"ds_read_b64_tr_b16 a[200:201], v249 offset:2304\n\t"
"v_mfma_f32_16x16x32_bf16 v[44:47], a[68:71], a[172:175], v[44:47]\n\t"
"s_mov_b32 s20, 233472\n\t"
"buffer_load_dwordx4 a[64:67], v248, s[8:11], s20 offen offset:1024 sc1\n\t"
"v_mfma_f32_16x16x32_bf16 v[28:31], a[56:59], a[156:159], v[28:31]\n\t"
"ds_read_b64_tr_b16 a[202:203], v249 offset:2816\n\t"
"v_cvt_pk_bf16_f32 v210, v212, v213\n\t"
"v_mfma_f32_16x16x32_bf16 v[28:31], a[60:63], a[172:175], v[28:31]\n\t"
"buffer_load_dwordx4 a[68:71], v248, s[8:11], s20 offen offset:2048 sc1\n\t"
"v_mfma_f32_16x16x32_bf16 v[12:15], a[48:51], a[156:159], v[12:15]\n\t"
"ds_read_b64_tr_b16 a[204:205], v250 offset:2304\n\t"
"v_cvt_pk_bf16_f32 v211, v214, v215\n\t"
"v_mfma_f32_16x16x32_bf16 v[12:15], a[52:55], a[172:175], v[12:15]\n\t"
"buffer_load_dwordx4 a[60:63], v248, s[8:11], s20 offen offset:0 sc1\n\t"
"s_waitcnt vmcnt(27) lgkmcnt(14)\n\t"
"v_mfma_f32_16x16x32_bf16 v[48:51], a[72:75], a[144:147], v[48:51]\n\t"
"ds_read_b64_tr_b16 a[206:207], v250 offset:2816\n\t"
"v_mfma_f32_16x16x32_bf16 v[48:51], a[76:79], a[160:163], v[48:51]\n\t"
"ds_write_b128 v219, v[208:211] offset:24576\n\t"
"s_mov_b32 s20, 221184\n\t"
"buffer_load_dwordx4 a[56:59], v248, s[8:11], s20 offen offset:2048 sc1\n\t"
"v_mfma_f32_16x16x32_bf16 v[64:67], a[80:83], a[144:147], v[64:67]\n\t"
"v_mfma_f32_16x16x32_bf16 v[64:67], a[84:87], a[160:163], v[64:67]\n\t"
"buffer_load_dwordx4 a[48:51], v248, s[8:11], s20 offen offset:0 sc1\n\t"
"v_mfma_f32_16x16x32_bf16 v[80:83], a[88:91], a[144:147], v[80:83]\n\t"
"ds_read_b128 a[152:155], v216 offset:6144\n\t"
"v_mfma_f32_16x16x32_bf16 v[80:83], a[92:95], a[160:163], v[80:83]\n\t"
"ds_read_b128 a[168:171], v216 offset:14336\n\t"
"ds_read_b128 a[156:159], v216 offset:7168\n\t"
"ds_read_b128 a[172:175], v216 offset:15360\n\t"
"buffer_load_dwordx4 a[52:55], v248, s[8:11], s20 offen offset:1024 sc1\n\t"
"s_waitcnt lgkmcnt(15)\n\t"
"v_mfma_f32_16x16x32_bf16 v[84:87], a[88:91], a[148:151], v[84:87]\n\t"
"v_mfma_f32_16x16x32_bf16 v[84:87], a[92:95], a[164:167], v[84:87]\n\t"
"v_mfma_f32_16x16x32_bf16 v[68:71], a[80:83], a[148:151], v[68:71]\n\t"
"v_mfma_f32_16x16x32_bf16 v[68:71], a[84:87], a[164:167], v[68:71]\n\t"
"v_mfma_f32_16x16x32_bf16 v[52:55], a[72:75], a[148:151], v[52:55]\n\t"
"v_mfma_f32_16x16x32_bf16 v[52:55], a[76:79], a[164:167], v[52:55]\n\t"
"s_waitcnt lgkmcnt(0)\n\t"
"s_barrier\n\t"
"v_mfma_f32_16x16x32_bf16 v[56:59], a[72:75], a[152:155], v[56:59]\n\t"
"ds_read_b128 a[144:147], v216 offset:16384\n\t"
"v_mfma_f32_16x16x32_bf16 v[56:59], a[76:79], a[168:171], v[56:59]\n\t"
"ds_read_b128 a[160:163], v216 offset:24576\n\t"
"ds_read_b128 a[148:151], v216 offset:17408\n\t"
"v_mfma_f32_16x16x32_bf16 v[72:75], a[80:83], a[152:155], v[72:75]\n\t"
"ds_read_b128 a[164:167], v216 offset:25600\n\t"
"v_mfma_f32_16x16x32_bf16 v[72:75], a[84:87], a[168:171], v[72:75]\n\t"
"ds_read_b128 a[240:243], v222 offset:59904\n\t"
"v_mfma_f32_16x16x32_bf16 v[88:91], a[88:91], a[152:155], v[88:91]\n\t"
"ds_read_b128 a[244:247], v223 offset:18432\n\t"
"v_mfma_f32_16x16x32_bf16 v[88:91], a[92:95], a[168:171], v[88:91]\n\t"
"ds_read_b128 a[152:155], v216 offset:18432\n\t"
"ds_read_b128 a[168:171], v216 offset:26624\n\t"
"v_mfma_f32_16x16x32_bf16 v[192:195], a[176:179], a[212:215], 0\n\t"
"v_mfma_f32_16x16x32_bf16 v[92:95], a[88:91], a[156:159], v[92:95]\n\t"
"v_mfma_f32_16x16x32_bf16 v[92:95], a[92:95], a[172:175], v[92:95]\n\t"
"s_mov_b32 s20, 258048\n\t"
"buffer_load_dwordx4 a[88:91], v248, s[8:11], s20 offen offset:1024 sc1\n\t"
"v_mfma_f32_16x16x32_bf16 v[196:199], a[180:183], a[212:215], 0\n\t"
"buffer_load_dwordx4 a[92:95], v248, s[8:11], s20 offen offset:2048 sc1\n\t"
"v_mfma_f32_16x16x32_bf16 v[76:79], a[80:83], a[156:159], v[76:79]\n\t"
"s_nop 0\n\t"
"v_cvt_pk_bf16_f32 v192, v192, v193\n\t"
"v_cvt_pk_bf16_f32 v193, v194, v195\n\t"
"v_mfma_f32_16x16x32_bf16 v[76:79], a[84:87], a[172:175], v[76:79]\n\t"
"buffer_load_dwordx4 a[84:87], v248, s[8:11], s20 offen offset:0 sc1\n\t"
"v_mfma_f32_16x16x32_bf16 v[60:63], a[72:75], a[156:159], v[60:63]\n\t"
"ds_read_b128 a[156:159], v216 offset:19456\n\t"
"v_mfma_f32_16x16x32_bf16 v[60:63], a[76:79], a[172:175], v[60:63]\n\t"
"s_mov_b32 s20, 245760\n\t"
"buffer_load_dwordx4 a[80:83], v248, s[8:11], s20 offen offset:2048 sc1\n\t"
"v_mfma_f32_16x16x32_bf16 v[200:203], a[184:187], a[212:215], 0\n\t"
"buffer_load_dwordx4 a[72:75], v248, s[8:11], s20 offen offset:0 sc1\n\t"
"s_waitcnt vmcnt(29) lgkmcnt(7)\n\t"
"v_mfma_f32_16x16x32_bf16 v[96:99], a[96:99], a[144:147], v[96:99]\n\t"
"v_cvt_pk_bf16_f32 v194, v196, v197\n\t"
"v_cvt_pk_bf16_f32 v195, v198, v199\n\t"
"v_mfma_f32_16x16x32_bf16 v[96:99], a[100:103], a[160:163], v[96:99]\n\t"
"ds_read_b128 a[172:175], v216 offset:27648\n\t"
"v_mfma_f32_16x16x32_bf16 v[204:207], a[188:191], a[212:215], 0\n\t"
"buffer_load_dwordx4 a[76:79], v248, s[8:11], s20 offen offset:1024 sc1\n\t"
"v_mfma_f32_16x16x32_bf16 v[112:115], a[104:107], a[144:147], v[112:115]\n\t"
"v_cvt_pk_bf16_f32 v200, v200, v201\n\t"
"v_mfma_f32_16x16x32_bf16 v[112:115], a[108:111], a[160:163], v[112:115]\n\t"
"v_cvt_pk_bf16_f32 v201, v202, v203\n\t"
"v_mfma_f32_16x16x32_bf16 v[128:131], a[112:115], a[144:147], v[128:131]\n\t"
"ds_read_b128 a[144:147], v216 offset:20480\n\t"
"v_mfma_f32_16x16x32_bf16 v[128:131], a[116:119], a[160:163], v[128:131]\n\t"
"ds_write_b128 v217, v[192:195] offset:0\n\t"
"v_mfma_f32_16x16x32_bf16 v[208:211], a[192:195], a[212:215], 0\n\t"
"v_cvt_pk_bf16_f32 v202, v204, v205\n\t"
"s_waitcnt lgkmcnt(8)\n\t"
"v_mfma_f32_16x16x32_bf16 v[132:135], a[112:115], a[148:151], v[132:135]\n\t"
"v_cvt_pk_bf16_f32 v203, v206, v207\n\t"
"v_mfma_f32_16x16x32_bf16 v[132:135], a[116:119], a[164:167], v[132:135]\n\t"
"ds_read_b128 a[160:163], v216 offset:28672\n\t"
"v_mfma_f32_16x16x32_bf16 v[116:119], a[104:107], a[148:151], v[116:119]\n\t"
"v_mfma_f32_16x16x32_bf16 v[116:119], a[108:111], a[164:167], v[116:119]\n\t"
"ds_write_b128 v219, v[200:203] offset:0\n\t"
"v_mfma_f32_16x16x32_bf16 v[212:215], a[196:199], a[212:215], 0\n\t"
"v_cvt_pk_bf16_f32 v208, v208, v209\n\t"
"v_cvt_pk_bf16_f32 v209, v210, v211\n\t"
"v_mfma_f32_16x16x32_bf16 v[100:103], a[96:99], a[148:151], v[100:103]\n\t"
"ds_read_b128 a[148:151], v216 offset:21504\n\t"
"v_mfma_f32_16x16x32_bf16 v[100:103], a[100:103], a[164:167], v[100:103]\n\t"
"ds_read_b128 a[164:167], v216 offset:29696\n\t"
"v_mfma_f32_16x16x32_bf16 v[192:195], a[200:203], a[212:215], 0\n\t"
"s_waitcnt lgkmcnt(11)\n\t"
"ds_write_b128 v222, a[240:243] offset:41472\n\t"
"s_waitcnt lgkmcnt(9)\n\t"
"v_mfma_f32_16x16x32_bf16 v[104:107], a[96:99], a[152:155], v[104:107]\n\t"
"v_cvt_pk_bf16_f32 v210, v212, v213\n\t"
"v_cvt_pk_bf16_f32 v211, v214, v215\n\t"
"v_mfma_f32_16x16x32_bf16 v[104:107], a[100:103], a[168:171], v[104:107]\n\t"
"v_mfma_f32_16x16x32_bf16 v[120:123], a[104:107], a[152:155], v[120:123]\n\t"
"v_mfma_f32_16x16x32_bf16 v[120:123], a[108:111], a[168:171], v[120:123]\n\t"
"v_cvt_pk_bf16_f32 v192, v192, v193\n\t"
"v_cvt_pk_bf16_f32 v193, v194, v195\n\t"
"v_mfma_f32_16x16x32_bf16 v[196:199], a[204:207], a[212:215], 0\n\t"
"ds_write_b128 v217, v[208:211] offset:8192\n\t"
"v_mfma_f32_16x16x32_bf16 v[136:139], a[112:115], a[152:155], v[136:139]\n\t"
"ds_read_b128 a[152:155], v216 offset:22528\n\t"
"v_mfma_f32_16x16x32_bf16 v[136:139], a[116:119], a[168:171], v[136:139]\n\t"
"ds_read_b128 a[168:171], v216 offset:30720\n\t"
"s_waitcnt lgkmcnt(10)\n\t"
"v_mfma_f32_16x16x32_bf16 v[140:143], a[112:115], a[156:159], v[140:143]\n\t"
"v_mfma_f32_16x16x32_bf16 v[140:143], a[116:119], a[172:175], v[140:143]\n\t"
"s_mov_b32 s20, 528384\n\t"
"buffer_load_dwordx4 a[112:115], v248, s[8:11], s20 offen offset:1024 sc1\n\t"
"v_mfma_f32_16x16x32_bf16 v[124:127], a[104:107], a[156:159], v[124:127]\n\t"
"v_cvt_pk_bf16_f32 v194, v196, v197\n\t"
"v_cvt_pk_bf16_f32 v195, v198, v199\n\t"
"v_mfma_f32_16x16x32_bf16 v[124:127], a[108:111], a[172:175], v[124:127]\n\t"
"buffer_load_dwordx4 a[116:119], v248, s[8:11], s20 offen offset:2048 sc1\n\t"
"v_mfma_f32_16x16x32_bf16 v[108:111], a[96:99], a[156:159], v[108:111]\n\t"
"ds_read_b128 a[156:159], v216 offset:23552\n\t"
"v_mfma_f32_16x16x32_bf16 v[108:111], a[100:103], a[172:175], v[108:111]\n\t"
"buffer_load_dwordx4 a[108:111], v248, s[8:11], s20 offen offset:0 sc1\n\t"
"s_waitcnt vmcnt(27) lgkmcnt(8)\n\t"
"v_mfma_f32_16x16x32_bf16 v[144:147], a[120:123], a[144:147], v[144:147]\n\t"
"ds_read_b128 a[172:175], v216 offset:31744\n\t"
"v_mfma_f32_16x16x32_bf16 v[144:147], a[124:127], a[160:163], v[144:147]\n\t"
"ds_write_b128 v219, v[192:195] offset:8192\n\t"
"s_mov_b32 s20, 516096\n\t"
"buffer_load_dwordx4 a[104:107], v248, s[8:11], s20 offen offset:2048 sc1\n\t"
"v_mfma_f32_16x16x32_bf16 v[160:163], a[128:131], a[144:147], v[160:163]\n\t"
"v_mfma_f32_16x16x32_bf16 v[160:163], a[132:135], a[160:163], v[160:163]\n\t"
"buffer_load_dwordx4 a[96:99], v248, s[8:11], s20 offen offset:0 sc1\n\t"
"buffer_load_dwordx4 a[100:103], v248, s[8:11], s20 offen offset:1024 sc1\n\t"
"v_pk_add_f32 v[0:1], v[0:1], v[96:97]\n\t"
"v_pk_add_f32 v[2:3], v[2:3], v[98:99]\n\t"
"v_pk_fma_f32 v[96:97], v[96:97], s[38:39], v[0:1]\n\t"
"v_pk_fma_f32 v[98:99], v[98:99], s[38:39], v[2:3]\n\t"
"v_pk_add_f32 v[16:17], v[16:17], v[112:113]\n\t"
"v_pk_add_f32 v[18:19], v[18:19], v[114:115]\n\t"
"v_pk_fma_f32 v[112:113], v[112:113], s[38:39], v[16:17]\n\t"
"v_pk_fma_f32 v[114:115], v[114:115], s[38:39], v[18:19]\n\t"
"v_pk_add_f32 v[32:33], v[32:33], v[128:129]\n\t"
"v_pk_add_f32 v[34:35], v[34:35], v[130:131]\n\t"
"v_pk_fma_f32 v[128:129], v[128:129], s[38:39], v[32:33]\n\t"
"v_pk_fma_f32 v[130:131], v[130:131], s[38:39], v[34:35]\n\t"
"v_pk_add_f32 v[36:37], v[36:37], v[132:133]\n\t"
"v_pk_add_f32 v[38:39], v[38:39], v[134:135]\n\t"
"v_pk_fma_f32 v[132:133], v[132:133], s[38:39], v[36:37]\n\t"
"v_pk_fma_f32 v[134:135], v[134:135], s[38:39], v[38:39]\n\t"
"v_pk_add_f32 v[20:21], v[20:21], v[116:117]\n\t"
"v_pk_add_f32 v[22:23], v[22:23], v[118:119]\n\t"
"v_pk_fma_f32 v[116:117], v[116:117], s[38:39], v[20:21]\n\t"
"v_pk_fma_f32 v[118:119], v[118:119], s[38:39], v[22:23]\n\t"
"v_pk_add_f32 v[4:5], v[4:5], v[100:101]\n\t"
"v_pk_add_f32 v[6:7], v[6:7], v[102:103]\n\t"
"v_pk_fma_f32 v[100:101], v[100:101], s[38:39], v[4:5]\n\t"
"v_pk_fma_f32 v[102:103], v[102:103], s[38:39], v[6:7]\n\t"
"v_pk_add_f32 v[8:9], v[8:9], v[104:105]\n\t"
"v_pk_add_f32 v[10:11], v[10:11], v[106:107]\n\t"
"v_pk_fma_f32 v[104:105], v[104:105], s[38:39], v[8:9]\n\t"
"v_pk_fma_f32 v[106:107], v[106:107], s[38:39], v[10:11]\n\t"
"v_pk_add_f32 v[24:25], v[24:25], v[120:121]\n\t"
"v_pk_add_f32 v[26:27], v[26:27], v[122:123]\n\t"
"v_pk_fma_f32 v[120:121], v[120:121], s[38:39], v[24:25]\n\t"
"v_pk_fma_f32 v[122:123], v[122:123], s[38:39], v[26:27]\n\t"
"v_pk_add_f32 v[40:41], v[40:41], v[136:137]\n\t"
"v_pk_add_f32 v[42:43], v[42:43], v[138:139]\n\t"
"v_pk_fma_f32 v[136:137], v[136:137], s[38:39], v[40:41]\n\t"
"v_pk_fma_f32 v[138:139], v[138:139], s[38:39], v[42:43]\n\t"
"v_pk_add_f32 v[44:45], v[44:45], v[140:141]\n\t"
"v_pk_add_f32 v[46:47], v[46:47], v[142:143]\n\t"
"v_pk_fma_f32 v[140:141], v[140:141], s[38:39], v[44:45]\n\t"
"v_pk_fma_f32 v[142:143], v[142:143], s[38:39], v[46:47]\n\t"
"v_pk_add_f32 v[28:29], v[28:29], v[124:125]\n\t"
"v_pk_add_f32 v[30:31], v[30:31], v[126:127]\n\t"
"v_pk_fma_f32 v[124:125], v[124:125], s[38:39], v[28:29]\n\t"
"v_pk_fma_f32 v[126:127], v[126:127], s[38:39], v[30:31]\n\t"
"v_pk_add_f32 v[12:13], v[12:13], v[108:109]\n\t"
"v_pk_add_f32 v[14:15], v[14:15], v[110:111]\n\t"
"v_pk_fma_f32 v[108:109], v[108:109], s[38:39], v[12:13]\n\t"
"v_pk_fma_f32 v[110:111], v[110:111], s[38:39], v[14:15]\n\t"
"v_mfma_f32_16x16x32_bf16 v[176:179], a[136:139], a[144:147], v[176:179]\n\t"
"v_mfma_f32_16x16x32_bf16 v[176:179], a[140:143], a[160:163], v[176:179]\n\t"
"ds_write_b128 v223, a[244:247] offset:0\n\t"
"s_waitcnt lgkmcnt(8)\n\t"
"v_mfma_f32_16x16x32_bf16 v[180:183], a[136:139], a[148:151], v[180:183]\n\t"
"v_mfma_f32_16x16x32_bf16 v[180:183], a[140:143], a[164:167], v[180:183]\n\t"
"v_mfma_f32_16x16x32_bf16 v[164:167], a[128:131], a[148:151], v[164:167]\n\t"
"v_mfma_f32_16x16x32_bf16 v[164:167], a[132:135], a[164:167], v[164:167]\n\t"
"v_mfma_f32_16x16x32_bf16 v[148:151], a[120:123], a[148:151], v[148:151]\n\t"
"v_mfma_f32_16x16x32_bf16 v[148:151], a[124:127], a[164:167], v[148:151]\n\t"
"s_waitcnt lgkmcnt(0)\n\t"
"s_barrier\n\t"
"v_mfma_f32_16x16x32_bf16 v[152:155], a[120:123], a[152:155], v[152:155]\n\t"
"ds_read_b128 a[144:147], v216 offset:0\n\t"
"v_add_u32 v249, 122368, v224\n\t"
"v_mfma_f32_16x16x32_bf16 v[152:155], a[124:127], a[168:171], v[152:155]\n\t"
"ds_read_b128 a[160:163], v216 offset:8192\n\t"
"ds_read_b128 a[148:151], v216 offset:1024\n\t"
"s_add_u32 m0, s26, 83200\n\t"
"s_nop 0\n\t"
"buffer_load_dwordx4 v236, s[12:15], s22 offen offset:0 nt lds\n\t"
"v_mfma_f32_16x16x32_bf16 v[168:171], a[128:131], a[152:155], v[168:171]\n\t"
"ds_read_b128 a[164:167], v216 offset:9216\n\t"
"v_mfma_f32_16x16x32_bf16 v[168:171], a[132:135], a[168:171], v[168:171]\n\t"
"v_xor_b32 v250, 32, v249\n\t"
"v_mfma_f32_16x16x32_bf16 v[184:187], a[136:139], a[152:155], v[184:187]\n\t"
"v_xor_b32 v251, 64, v249\n\t"
"v_mfma_f32_16x16x32_bf16 v[184:187], a[140:143], a[168:171], v[184:187]\n\t"
"v_xor_b32 v252, 96, v249\n\t"
"ds_read_b128 a[152:155], v216 offset:2048\n\t"
"v_mfma_f32_16x16x32_bf16 v[200:203], a[176:179], a[216:219], 0\n\t"
"s_add_u32 m0, s26, 84224\n\t"
"s_nop 0\n\t"
"buffer_load_dwordx4 v237, s[12:15], s22 offen offset:0 nt lds\n\t"
"v_mfma_f32_16x16x32_bf16 v[188:191], a[136:139], a[156:159], v[188:191]\n\t"
"ds_read_b128 a[168:171], v216 offset:10240\n\t"
"v_mfma_f32_16x16x32_bf16 v[188:191], a[140:143], a[172:175], v[188:191]\n\t"
"ds_read_b64_tr_b16 a[176:177], v249 offset:0\n\t"
"v_mfma_f32_16x16x32_bf16 v[204:207], a[180:183], a[216:219], 0\n\t"
"ds_read_b64_tr_b16 a[178:179], v249 offset:512\n\t"
"ds_read_b64_tr_b16 a[180:181], v250 offset:0\n\t"
"v_mfma_f32_16x16x32_bf16 v[172:175], a[128:131], a[156:159], v[172:175]\n\t"
"v_cvt_pk_bf16_f32 v200, v200, v201\n\t"
"v_mfma_f32_16x16x32_bf16 v[172:175], a[132:135], a[172:175], v[172:175]\n\t"
"s_add_u32 m0, s26, 85248\n\t"
"s_nop 0\n\t"
"buffer_load_dwordx4 v238, s[12:15], s22 offen offset:0 nt lds\n\t"
"v_mfma_f32_16x16x32_bf16 v[156:159], a[120:123], a[156:159], v[156:159]\n\t"
"v_cvt_pk_bf16_f32 v201, v202, v203\n\t"
"v_mfma_f32_16x16x32_bf16 v[156:159], a[124:127], a[172:175], v[156:159]\n\t"
"v_cvt_pk_bf16_f32 v202, v204, v205\n\t"
"v_cvt_pk_bf16_f32 v203, v206, v207\n\t"
"v_mfma_f32_16x16x32_bf16 v[208:211], a[184:187], a[216:219], 0\n\t"
"ds_read_b128 a[156:159], v216 offset:3072\n\t"
"s_waitcnt vmcnt(27) lgkmcnt(8)\n\t"
"v_mfma_f32_16x16x32_bf16 v[0:3], a[0:3], a[144:147], v[0:3]\n\t"
"ds_read_b128 a[172:175], v216 offset:11264\n\t"
"v_mfma_f32_16x16x32_bf16 v[0:3], a[4:7], a[160:163], v[0:3]\n\t"
"ds_write_b128 v217, v[200:203] offset:16384\n\t"
"v_mfma_f32_16x16x32_bf16 v[212:215], a[188:191], a[216:219], 0\n\t"
"s_add_u32 m0, s26, 86272\n\t"
"s_nop 0\n\t"
"buffer_load_dwordx4 v239, s[12:15], s22 offen offset:0 nt lds\n\t"
"v_pk_add_f32 v[48:49], v[48:49], v[144:145]\n\t"
"v_pk_add_f32 v[50:51], v[50:51], v[146:147]\n\t"
"v_pk_fma_f32 v[144:145], v[144:145], s[38:39], v[48:49]\n\t"
"v_pk_fma_f32 v[146:147], v[146:147], s[38:39], v[50:51]\n\t"
"v_pk_add_f32 v[64:65], v[64:65], v[160:161]\n\t"
"v_pk_add_f32 v[66:67], v[66:67], v[162:163]\n\t"
"v_pk_fma_f32 v[160:161], v[160:161], s[38:39], v[64:65]\n\t"
"v_pk_fma_f32 v[162:163], v[162:163], s[38:39], v[66:67]\n\t"
"v_pk_add_f32 v[80:81], v[80:81], v[176:177]\n\t"
"v_pk_add_f32 v[82:83], v[82:83], v[178:179]\n\t"
"v_pk_fma_f32 v[176:177], v[176:177], s[38:39], v[80:81]\n\t"
"v_pk_fma_f32 v[178:179], v[178:179], s[38:39], v[82:83]\n\t"
"v_pk_add_f32 v[84:85], v[84:85], v[180:181]\n\t"
"v_pk_add_f32 v[86:87], v[86:87], v[182:183]\n\t"
"v_pk_fma_f32 v[180:181], v[180:181], s[38:39], v[84:85]\n\t"
"v_pk_fma_f32 v[182:183], v[182:183], s[38:39], v[86:87]\n\t"
"v_pk_add_f32 v[68:69], v[68:69], v[164:165]\n\t"
"v_pk_add_f32 v[70:71], v[70:71], v[166:167]\n\t"
"v_pk_fma_f32 v[164:165], v[164:165], s[38:39], v[68:69]\n\t"
"v_pk_fma_f32 v[166:167], v[166:167], s[38:39], v[70:71]\n\t"
"v_pk_add_f32 v[52:53], v[52:53], v[148:149]\n\t"
"v_pk_add_f32 v[54:55], v[54:55], v[150:151]\n\t"
"v_pk_fma_f32 v[148:149], v[148:149], s[38:39], v[52:53]\n\t"
"v_pk_fma_f32 v[150:151], v[150:151], s[38:39], v[54:55]\n\t"
"v_pk_add_f32 v[56:57], v[56:57], v[152:153]\n\t"
"v_pk_add_f32 v[58:59], v[58:59], v[154:155]\n\t"
"v_pk_fma_f32 v[152:153], v[152:153], s[38:39], v[56:57]\n\t"
"v_pk_fma_f32 v[154:155], v[154:155], s[38:39], v[58:59]\n\t"
"v_pk_add_f32 v[72:73], v[72:73], v[168:169]\n\t"
"v_pk_add_f32 v[74:75], v[74:75], v[170:171]\n\t"
"v_pk_fma_f32 v[168:169], v[168:169], s[38:39], v[72:73]\n\t"
"v_pk_fma_f32 v[170:171], v[170:171], s[38:39], v[74:75]\n\t"
"v_pk_add_f32 v[88:89], v[88:89], v[184:185]\n\t"
"v_pk_add_f32 v[90:91], v[90:91], v[186:187]\n\t"
"v_pk_fma_f32 v[184:185], v[184:185], s[38:39], v[88:89]\n\t"
"v_pk_fma_f32 v[186:187], v[186:187], s[38:39], v[90:91]\n\t"
"v_pk_add_f32 v[92:93], v[92:93], v[188:189]\n\t"
"v_pk_add_f32 v[94:95], v[94:95], v[190:191]\n\t"
"v_pk_fma_f32 v[188:189], v[188:189], s[38:39], v[92:93]\n\t"
"v_pk_fma_f32 v[190:191], v[190:191], s[38:39], v[94:95]\n\t"
"v_pk_add_f32 v[76:77], v[76:77], v[172:173]\n\t"
"v_pk_add_f32 v[78:79], v[78:79], v[174:175]\n\t"
"v_pk_fma_f32 v[172:173], v[172:173], s[38:39], v[76:77]\n\t"
"v_pk_fma_f32 v[174:175], v[174:175], s[38:39], v[78:79]\n\t"
"v_pk_add_f32 v[60:61], v[60:61], v[156:157]\n\t"
"v_pk_add_f32 v[62:63], v[62:63], v[158:159]\n\t"
"v_pk_fma_f32 v[156:157], v[156:157], s[38:39], v[60:61]\n\t"
"v_pk_fma_f32 v[158:159], v[158:159], s[38:39], v[62:63]\n\t"
"v_mfma_f32_16x16x32_bf16 v[16:19], a[8:11], a[144:147], v[16:19]\n\t"
"v_cvt_pk_bf16_f32 v208, v208, v209\n\t"
"v_mfma_f32_16x16x32_bf16 v[16:19], a[12:15], a[160:163], v[16:19]\n\t"
"v_cvt_pk_bf16_f32 v209, v210, v211\n\t"
"ds_read_b64_tr_b16 a[182:183], v250 offset:512\n\t"
"v_mfma_f32_16x16x32_bf16 v[32:35], a[16:19], a[144:147], v[32:35]\n\t"
"ds_read_b128 a[144:147], v216 offset:4096\n\t"
"v_mfma_f32_16x16x32_bf16 v[32:35], a[20:23], a[160:163], v[32:35]\n\t"
"v_cvt_pk_bf16_f32 v210, v212, v213\n\t"
"v_cvt_pk_bf16_f32 v211, v214, v215\n\t"
"v_mfma_f32_16x16x32_bf16 v[192:195], a[192:195], a[216:219], 0\n\t"
"s_add_u32 m0, s26, 87296\n\t"
"s_mov_b64 exec, s[36:37]\n\t"
"buffer_load_dwordx4 v240, s[12:15], s22 offen offset:0 nt lds\n\t"
"s_mov_b64 exec, -1\n\t"
"s_waitcnt lgkmcnt(10)\n\t"
"v_mfma_f32_16x16x32_bf16 v[36:39], a[16:19], a[148:151], v[36:39]\n\t"
"ds_read_b128 a[160:163], v216 offset:12288\n\t"
"v_mfma_f32_16x16x32_bf16 v[36:39], a[20:23], a[164:167], v[36:39]\n\t"
"ds_read_b64_tr_b16 a[184:185], v251 offset:2304\n\t"
"v_mfma_f32_16x16x32_bf16 v[20:23], a[8:11], a[148:151], v[20:23]\n\t"
"ds_read_b64_tr_b16 a[186:187], v251 offset:2816\n\t"
"v_mfma_f32_16x16x32_bf16 v[20:23], a[12:15], a[164:167], v[20:23]\n\t"
"ds_write_b128 v219, v[208:211] offset:16384\n\t"
"v_mfma_f32_16x16x32_bf16 v[196:199], a[196:199], a[216:219], 0\n\t"
"v_cvt_pk_bf16_f32 v192, v192, v193\n\t"
"v_cvt_pk_bf16_f32 v193, v194, v195\n\t"
"v_mfma_f32_16x16x32_bf16 v[4:7], a[0:3], a[148:151], v[4:7]\n\t"
"ds_read_b128 a[148:151], v216 offset:5120\n\t"
"v_mfma_f32_16x16x32_bf16 v[4:7], a[4:7], a[164:167], v[4:7]\n\t"
"s_add_u32 m0, s26, 103936\n\t"
"s_nop 0\n\t"
"buffer_load_dwordx4 v241, s[12:15], s22 offen offset:0 nt lds\n\t"
"v_mfma_f32_16x16x32_bf16 v[200:203], a[200:203], a[216:219], 0\n\t"
"ds_read_b128 a[164:167], v216 offset:13312\n\t"
"s_waitcnt lgkmcnt(14)\n\t"
"v_mfma_f32_16x16x32_bf16 v[8:11], a[0:3], a[152:155], v[8:11]\n\t"
"v_cvt_pk_bf16_f32 v194, v196, v197\n\t"
"v_cvt_pk_bf16_f32 v195, v198, v199\n\t"
"v_mfma_f32_16x16x32_bf16 v[8:11], a[4:7], a[168:171], v[8:11]\n\t"
"ds_read_b64_tr_b16 a[188:189], v252 offset:2304\n\t"
"v_mfma_f32_16x16x32_bf16 v[24:27], a[8:11], a[152:155], v[24:27]\n\t"
"ds_read_b64_tr_b16 a[190:191], v252 offset:2816\n\t"
"v_mfma_f32_16x16x32_bf16 v[24:27], a[12:15], a[168:171], v[24:27]\n\t"
"s_add_u32 m0, s26, 104960\n\t"
"s_nop 0\n\t"
"buffer_load_dwordx4 v242, s[12:15], s22 offen offset:0 nt lds\n\t"
"v_mfma_f32_16x16x32_bf16 v[204:207], a[204:207], a[216:219], 0\n\t"
"v_cvt_pk_bf16_f32 v200, v200, v201\n\t"
"v_cvt_pk_bf16_f32 v201, v202, v203\n\t"
"v_mfma_f32_16x16x32_bf16 v[40:43], a[16:19], a[152:155], v[40:43]\n\t"
"ds_read_b64_tr_b16 a[192:193], v251 offset:0\n\t"
"v_mfma_f32_16x16x32_bf16 v[40:43], a[20:23], a[168:171], v[40:43]\n\t"
"ds_write_b128 v217, v[192:195] offset:24576\n\t"
"s_waitcnt lgkmcnt(13)\n\t"
"v_mfma_f32_16x16x32_bf16 v[44:47], a[16:19], a[156:159], v[44:47]\n\t"
"ds_read_b64_tr_b16 a[194:195], v251 offset:512\n\t"
"v_mfma_f32_16x16x32_bf16 v[44:47], a[20:23], a[172:175], v[44:47]\n\t"
"s_add_u32 m0, s26, 105984\n\t"
"s_nop 0\n\t"
"buffer_load_dwordx4 v243, s[12:15], s22 offen offset:0 nt lds\n\t"
"v_mfma_f32_16x16x32_bf16 v[28:31], a[8:11], a[156:159], v[28:31]\n\t"
"ds_read_b64_tr_b16 a[196:197], v252 offset:0\n\t"
"v_mfma_f32_16x16x32_bf16 v[28:31], a[12:15], a[172:175], v[28:31]\n\t"
"ds_read_b64_tr_b16 a[198:199], v252 offset:512\n\t"
"ds_read_b64_tr_b16 a[200:201], v249 offset:2304\n\t"
"v_mfma_f32_16x16x32_bf16 v[12:15], a[0:3], a[156:159], v[12:15]\n\t"
"ds_read_b64_tr_b16 a[202:203], v249 offset:2816\n\t"
"v_mfma_f32_16x16x32_bf16 v[12:15], a[4:7], a[172:175], v[12:15]\n\t"
"ds_read_b64_tr_b16 a[204:205], v250 offset:2304\n\t"
"ds_read_b64_tr_b16 a[206:207], v250 offset:2816\n\t"
"s_waitcnt vmcnt(26) lgkmcnt(15)\n\t"
"v_mfma_f32_16x16x32_bf16 v[48:51], a[24:27], a[144:147], v[48:51]\n\t"
"v_cvt_pk_bf16_f32 v202, v204, v205\n\t"
"v_mfma_f32_16x16x32_bf16 v[48:51], a[28:31], a[160:163], v[48:51]\n\t"
"s_add_u32 m0, s26, 107008\n\t"
"s_nop 0\n\t"
"buffer_load_dwordx4 v244, s[12:15], s22 offen offset:0 nt lds\n\t"
"v_cvt_pk_bf16_f32 v203, v206, v207\n\t"
"v_mfma_f32_16x16x32_bf16 v[64:67], a[32:35], a[144:147], v[64:67]\n\t"
"v_mfma_f32_16x16x32_bf16 v[64:67], a[36:39], a[160:163], v[64:67]\n\t"
"ds_write_b128 v219, v[200:203] offset:24576\n\t"
"v_mfma_f32_16x16x32_bf16 v[80:83], a[40:43], a[144:147], v[80:83]\n\t"
"ds_read_b128 a[152:155], v216 offset:6144\n\t"
"v_mfma_f32_16x16x32_bf16 v[80:83], a[44:47], a[160:163], v[80:83]\n\t"
"ds_read_b128 a[168:171], v216 offset:14336\n\t"
"ds_read_b128 a[156:159], v216 offset:7168\n\t"
"ds_read_b128 a[172:175], v216 offset:15360\n\t"
"s_add_u32 m0, s26, 108032\n\t"
"s_mov_b64 exec, s[36:37]\n\t"
"buffer_load_dwordx4 v245, s[12:15], s22 offen offset:0 nt lds\n\t"
"s_mov_b64 exec, -1\n\t"
"s_waitcnt lgkmcnt(15)\n\t"
"v_mfma_f32_16x16x32_bf16 v[84:87], a[40:43], a[148:151], v[84:87]\n\t"
"v_mfma_f32_16x16x32_bf16 v[84:87], a[44:47], a[164:167], v[84:87]\n\t"
"v_mfma_f32_16x16x32_bf16 v[68:71], a[32:35], a[148:151], v[68:71]\n\t"
"v_mfma_f32_16x16x32_bf16 v[68:71], a[36:39], a[164:167], v[68:71]\n\t"
"v_mfma_f32_16x16x32_bf16 v[52:55], a[24:27], a[148:151], v[52:55]\n\t"
"v_mfma_f32_16x16x32_bf16 v[52:55], a[28:31], a[164:167], v[52:55]\n\t"
"s_waitcnt lgkmcnt(0)\n\t"
"s_barrier\n\t"
"v_mfma_f32_16x16x32_bf16 v[56:59], a[24:27], a[152:155], v[56:59]\n\t"
"ds_read_b128 a[144:147], v216 offset:16384\n\t"
"v_mfma_f32_16x16x32_bf16 v[56:59], a[28:31], a[168:171], v[56:59]\n\t"
"ds_read_b128 a[160:163], v216 offset:24576\n\t"
"ds_read_b128 a[148:151], v216 offset:17408\n\t"
"v_mfma_f32_16x16x32_bf16 v[72:75], a[32:35], a[152:155], v[72:75]\n\t"
"ds_read_b128 a[164:167], v216 offset:25600\n\t"
"v_mfma_f32_16x16x32_bf16 v[72:75], a[36:39], a[168:171], v[72:75]\n\t"
"v_mfma_f32_16x16x32_bf16 v[88:91], a[40:43], a[152:155], v[88:91]\n\t"
"ds_read_b128 a[152:155], v216 offset:18432\n\t"
"v_mfma_f32_16x16x32_bf16 v[88:91], a[44:47], a[168:171], v[88:91]\n\t"
"ds_read_b128 a[168:171], v216 offset:26624\n\t"
"v_mfma_f32_16x16x32_bf16 v[208:211], a[176:179], a[212:215], 0\n\t"
"v_mfma_f32_16x16x32_bf16 v[92:95], a[40:43], a[156:159], v[92:95]\n\t"
"v_mfma_f32_16x16x32_bf16 v[92:95], a[44:47], a[172:175], v[92:95]\n\t"
"v_mfma_f32_16x16x32_bf16 v[212:215], a[180:183], a[212:215], 0\n\t"
"v_mfma_f32_16x16x32_bf16 v[76:79], a[32:35], a[156:159], v[76:79]\n\t"
"s_nop 3\n\t"
"v_cvt_pk_bf16_f32 v208, v208, v209\n\t"
"v_cvt_pk_bf16_f32 v209, v210, v211\n\t"
"v_mfma_f32_16x16x32_bf16 v[76:79], a[36:39], a[172:175], v[76:79]\n\t"
"v_mfma_f32_16x16x32_bf16 v[60:63], a[24:27], a[156:159], v[60:63]\n\t"
"ds_read_b128 a[156:159], v216 offset:19456\n\t"
"v_mfma_f32_16x16x32_bf16 v[60:63], a[28:31], a[172:175], v[60:63]\n\t"
"s_mov_b32 s20, 552960\n\t"
"buffer_load_dwordx4 a[136:139], v248, s[8:11], s20 offen offset:1024 sc1\n\t"
"v_mfma_f32_16x16x32_bf16 v[192:195], a[184:187], a[212:215], 0\n\t"
"v_cvt_pk_bf16_f32 v210, v212, v213\n\t"
"v_cvt_pk_bf16_f32 v211, v214, v215\n\t"
"s_waitcnt vmcnt(23) lgkmcnt(5)\n\t"
"v_mfma_f32_16x16x32_bf16 v[96:99], a[48:51], a[144:147], v[96:99]\n\t"
"ds_read_b128 a[172:175], v216 offset:27648\n\t"
"v_mfma_f32_16x16x32_bf16 v[96:99], a[52:55], a[160:163], v[96:99]\n\t"
"ds_write_b128 v217, v[208:211] offset:0\n\t"
"v_mfma_f32_16x16x32_bf16 v[196:199], a[188:191], a[212:215], 0\n\t"
"v_mfma_f32_16x16x32_bf16 v[112:115], a[56:59], a[144:147], v[112:115]\n\t"
"v_cvt_pk_bf16_f32 v192, v192, v193\n\t"
"v_cvt_pk_bf16_f32 v193, v194, v195\n\t"
"v_mfma_f32_16x16x32_bf16 v[112:115], a[60:63], a[160:163], v[112:115]\n\t"
"v_mfma_f32_16x16x32_bf16 v[128:131], a[64:67], a[144:147], v[128:131]\n\t"
"ds_read_b128 a[144:147], v216 offset:20480\n\t"
"v_mfma_f32_16x16x32_bf16 v[128:131], a[68:71], a[160:163], v[128:131]\n\t"
"s_nop 0\n\t"
"v_cvt_pk_bf16_f32 v194, v196, v197\n\t"
"v_cvt_pk_bf16_f32 v195, v198, v199\n\t"
"v_mfma_f32_16x16x32_bf16 v[200:203], a[192:195], a[212:215], 0\n\t"
"ds_read_b128 a[160:163], v216 offset:28672\n\t"
"s_waitcnt lgkmcnt(7)\n\t"
"v_mfma_f32_16x16x32_bf16 v[132:135], a[64:67], a[148:151], v[132:135]\n\t"
"v_mfma_f32_16x16x32_bf16 v[132:135], a[68:71], a[164:167], v[132:135]\n\t"
"ds_write_b128 v219, v[192:195] offset:0\n\t"
"v_mfma_f32_16x16x32_bf16 v[116:119], a[56:59], a[148:151], v[116:119]\n\t"
"v_mfma_f32_16x16x32_bf16 v[116:119], a[60:63], a[164:167], v[116:119]\n\t"
"s_nop 0\n\t"
"v_cvt_pk_bf16_f32 v200, v200, v201\n\t"
"v_cvt_pk_bf16_f32 v201, v202, v203\n\t"
"v_mfma_f32_16x16x32_bf16 v[204:207], a[196:199], a[212:215], 0\n\t"
"v_mfma_f32_16x16x32_bf16 v[100:103], a[48:51], a[148:151], v[100:103]\n\t"
"ds_read_b128 a[148:151], v216 offset:21504\n\t"
"v_mfma_f32_16x16x32_bf16 v[100:103], a[52:55], a[164:167], v[100:103]\n\t"
"ds_read_b128 a[164:167], v216 offset:29696\n\t"
"v_mfma_f32_16x16x32_bf16 v[208:211], a[200:203], a[212:215], 0\n\t"
"s_waitcnt lgkmcnt(8)\n\t"
"v_mfma_f32_16x16x32_bf16 v[104:107], a[48:51], a[152:155], v[104:107]\n\t"
"s_nop 0\n\t"
"v_cvt_pk_bf16_f32 v202, v204, v205\n\t"
"v_cvt_pk_bf16_f32 v203, v206, v207\n\t"
"v_mfma_f32_16x16x32_bf16 v[104:107], a[52:55], a[168:171], v[104:107]\n\t"
"ds_write_b128 v217, v[200:203] offset:8192\n\t"
"v_mfma_f32_16x16x32_bf16 v[120:123], a[56:59], a[152:155], v[120:123]\n\t"
"v_mfma_f32_16x16x32_bf16 v[120:123], a[60:63], a[168:171], v[120:123]\n\t"
"v_cvt_pk_bf16_f32 v208, v208, v209\n\t"
"v_cvt_pk_bf16_f32 v209, v210, v211\n\t"
"v_mfma_f32_16x16x32_bf16 v[212:215], a[204:207], a[212:215], 0\n\t"
"v_mfma_f32_16x16x32_bf16 v[136:139], a[64:67], a[152:155], v[136:139]\n\t"
"ds_read_b128 a[152:155], v216 offset:22528\n\t"
"v_mfma_f32_16x16x32_bf16 v[136:139], a[68:71], a[168:171], v[136:139]\n\t"
"ds_read_b128 a[168:171], v216 offset:30720\n\t"
"s_waitcnt lgkmcnt(9)\n\t"
"v_mfma_f32_16x16x32_bf16 v[140:143], a[64:67], a[156:159], v[140:143]\n\t"
"v_mfma_f32_16x16x32_bf16 v[140:143], a[68:71], a[172:175], v[140:143]\n\t"
"s_nop 0\n\t"
"v_cvt_pk_bf16_f32 v210, v212, v213\n\t"
"v_cvt_pk_bf16_f32 v211, v214, v215\n\t"
"v_mfma_f32_16x16x32_bf16 v[124:127], a[56:59], a[156:159], v[124:127]\n\t"
"v_mfma_f32_16x16x32_bf16 v[124:127], a[60:63], a[172:175], v[124:127]\n\t"
"ds_write_b128 v219, v[208:211] offset:8192\n\t"
"v_mfma_f32_16x16x32_bf16 v[108:111], a[48:51], a[156:159], v[108:111]\n\t"
"ds_read_b128 a[156:159], v216 offset:23552\n\t"
"v_mfma_f32_16x16x32_bf16 v[108:111], a[52:55], a[172:175], v[108:111]\n\t"
"ds_read_b128 a[172:175], v216 offset:31744\n\t"
"s_waitcnt vmcnt(17) lgkmcnt(9)\n\t"
"v_mfma_f32_16x16x32_bf16 v[144:147], a[72:75], a[144:147], v[144:147]\n\t"
"v_mfma_f32_16x16x32_bf16 v[144:147], a[76:79], a[160:163], v[144:147]\n\t"
"buffer_load_dwordx4 a[140:143], v248, s[8:11], s20 offen offset:2048 sc1\n\t"
"v_mfma_f32_16x16x32_bf16 v[160:163], a[80:83], a[144:147], v[160:163]\n\t"
"v_mfma_f32_16x16x32_bf16 v[160:163], a[84:87], a[160:163], v[160:163]\n\t"
"v_mfma_f32_16x16x32_bf16 v[176:179], a[88:91], a[144:147], v[176:179]\n\t"
"v_mfma_f32_16x16x32_bf16 v[176:179], a[92:95], a[160:163], v[176:179]\n\t"
"buffer_load_dwordx4 a[132:135], v248, s[8:11], s20 offen offset:0 sc1\n\t"
"s_waitcnt lgkmcnt(6)\n\t"
"v_mfma_f32_16x16x32_bf16 v[180:183], a[88:91], a[148:151], v[180:183]\n\t"
"v_mfma_f32_16x16x32_bf16 v[180:183], a[92:95], a[164:167], v[180:183]\n\t"
"v_mfma_f32_16x16x32_bf16 v[164:167], a[80:83], a[148:151], v[164:167]\n\t"
"v_mfma_f32_16x16x32_bf16 v[164:167], a[84:87], a[164:167], v[164:167]\n\t"
"s_mov_b32 s20, 540672\n\t"
"buffer_load_dwordx4 a[128:131], v248, s[8:11], s20 offen offset:2048 sc1\n\t"
"v_mfma_f32_16x16x32_bf16 v[148:151], a[72:75], a[148:151], v[148:151]\n\t"
"v_mfma_f32_16x16x32_bf16 v[148:151], a[76:79], a[164:167], v[148:151]\n\t"
"buffer_load_dwordx4 a[120:123], v248, s[8:11], s20 offen offset:0 sc1\n\t"
"s_waitcnt lgkmcnt(0)\n\t"
"s_barrier\n\t"
"v_mfma_f32_16x16x32_bf16 v[152:155], a[72:75], a[152:155], v[152:155]\n\t"
"ds_read_b128 a[144:147], v216 offset:0\n\t"
"v_add_u32 v249, 39424, v224\n\t"
"v_mfma_f32_16x16x32_bf16 v[152:155], a[76:79], a[168:171], v[152:155]\n\t"
"ds_read_b128 a[160:163], v216 offset:8192\n\t"
"ds_read_b128 a[148:151], v216 offset:1024\n\t"
"v_mfma_f32_16x16x32_bf16 v[168:171], a[80:83], a[152:155], v[168:171]\n\t"
"ds_read_b128 a[164:167], v216 offset:9216\n\t"
"v_xor_b32 v250, 32, v249\n\t"
"v_mfma_f32_16x16x32_bf16 v[168:171], a[84:87], a[168:171], v[168:171]\n\t"
"buffer_load_dwordx4 a[124:127], v248, s[8:11], s20 offen offset:1024 sc1\n\t"
"v_mfma_f32_16x16x32_bf16 v[184:187], a[88:91], a[152:155], v[184:187]\n\t"
"v_xor_b32 v251, 64, v249\n\t"
"v_xor_b32 v252, 96, v249\n\t"
"v_mfma_f32_16x16x32_bf16 v[184:187], a[92:95], a[168:171], v[184:187]\n\t"
"s_mov_b32 s20, 626688\n\t"
"buffer_load_dwordx4 a[16:19], v248, s[8:11], s20 offen offset:1024 sc1\n\t"
"v_mfma_f32_16x16x32_bf16 v[192:195], a[176:179], a[216:219], 0\n\t"
"ds_read_b128 a[152:155], v216 offset:2048\n\t"
"ds_read_b128 a[168:171], v216 offset:10240\n\t"
"v_mfma_f32_16x16x32_bf16 v[188:191], a[88:91], a[156:159], v[188:191]\n\t"
"ds_read_b64_tr_b16 a[176:177], v249 offset:0\n\t"
"v_mfma_f32_16x16x32_bf16 v[188:191], a[92:95], a[172:175], v[188:191]\n\t"
"buffer_load_dwordx4 a[20:23], v248, s[8:11], s20 offen offset:2048 sc1\n\t"
"v_mfma_f32_16x16x32_bf16 v[196:199], a[180:183], a[216:219], 0\n\t"
"ds_read_b64_tr_b16 a[178:179], v249 offset:512\n\t"
"ds_read_b64_tr_b16 a[180:181], v250 offset:0\n\t"
"v_mfma_f32_16x16x32_bf16 v[172:175], a[80:83], a[156:159], v[172:175]\n\t"
"v_cvt_pk_bf16_f32 v192, v192, v193\n\t"
"v_cvt_pk_bf16_f32 v193, v194, v195\n\t"
"v_mfma_f32_16x16x32_bf16 v[172:175], a[84:87], a[172:175], v[172:175]\n\t"
"buffer_load_dwordx4 a[12:15], v248, s[8:11], s20 offen offset:0 sc1\n\t"
"v_mfma_f32_16x16x32_bf16 v[156:159], a[72:75], a[156:159], v[156:159]\n\t"
"ds_read_b128 a[156:159], v216 offset:3072\n\t"
"v_mfma_f32_16x16x32_bf16 v[156:159], a[76:79], a[172:175], v[156:159]\n\t"
"v_cvt_pk_bf16_f32 v194, v196, v197\n\t"
"v_cvt_pk_bf16_f32 v195, v198, v199\n\t"
"v_mfma_f32_16x16x32_bf16 v[200:203], a[184:187], a[216:219], 0\n\t"
"s_mov_b32 s20, 614400\n\t"
"buffer_load_dwordx4 a[8:11], v248, s[8:11], s20 offen offset:2048 sc1\n\t"
"s_waitcnt vmcnt(20) lgkmcnt(8)\n\t"
"v_mfma_f32_16x16x32_bf16 v[0:3], a[96:99], a[144:147], v[0:3]\n\t"
"ds_read_b128 a[172:175], v216 offset:11264\n\t"
"v_mfma_f32_16x16x32_bf16 v[0:3], a[100:103], a[160:163], v[0:3]\n\t"
"ds_write_b128 v217, v[192:195] offset:16384\n\t"
"v_mfma_f32_16x16x32_bf16 v[204:207], a[188:191], a[216:219], 0\n\t"
"ds_read_b64_tr_b16 a[182:183], v250 offset:512\n\t"
"ds_read_b64_tr_b16 a[184:185], v251 offset:2304\n\t"
"v_mfma_f32_16x16x32_bf16 v[16:19], a[104:107], a[144:147], v[16:19]\n\t"
"v_cvt_pk_bf16_f32 v200, v200, v201\n\t"
"v_cvt_pk_bf16_f32 v201, v202, v203\n\t"
"v_mfma_f32_16x16x32_bf16 v[16:19], a[108:111], a[160:163], v[16:19]\n\t"
"buffer_load_dwordx4 a[0:3], v248, s[8:11], s20 offen offset:0 sc1\n\t"
"v_mfma_f32_16x16x32_bf16 v[32:35], a[112:115], a[144:147], v[32:35]\n\t"
"ds_read_b128 a[144:147], v216 offset:4096\n\t"
"v_mfma_f32_16x16x32_bf16 v[32:35], a[116:119], a[160:163], v[32:35]\n\t"
"buffer_load_dwordx4 a[4:7], v248, s[8:11], s20 offen offset:1024 sc1\n\t"
"v_mfma_f32_16x16x32_bf16 v[208:211], a[192:195], a[216:219], 0\n\t"
"v_cvt_pk_bf16_f32 v202, v204, v205\n\t"
"v_cvt_pk_bf16_f32 v203, v206, v207\n\t"
"s_waitcnt lgkmcnt(11)\n\t"
"v_mfma_f32_16x16x32_bf16 v[36:39], a[112:115], a[148:151], v[36:39]\n\t"
"ds_read_b128 a[160:163], v216 offset:12288\n\t"
"v_mfma_f32_16x16x32_bf16 v[36:39], a[116:119], a[164:167], v[36:39]\n\t"
"ds_write_b128 v219, v[200:203] offset:16384\n\t"
"v_mfma_f32_16x16x32_bf16 v[20:23], a[104:107], a[148:151], v[20:23]\n\t"
"ds_read_b64_tr_b16 a[186:187], v251 offset:2816\n\t"
"v_mfma_f32_16x16x32_bf16 v[20:23], a[108:111], a[164:167], v[20:23]\n\t"
"s_mov_b32 s20, 651264\n\t"
"buffer_load_dwordx4 a[40:43], v248, s[8:11], s20 offen offset:1024 sc1\n\t"
"v_mfma_f32_16x16x32_bf16 v[212:215], a[196:199], a[216:219], 0\n\t"
"v_cvt_pk_bf16_f32 v208, v208, v209\n\t"
"v_cvt_pk_bf16_f32 v209, v210, v211\n\t"
"v_mfma_f32_16x16x32_bf16 v[4:7], a[96:99], a[148:151], v[4:7]\n\t"
"ds_read_b128 a[148:151], v216 offset:5120\n\t"
"v_mfma_f32_16x16x32_bf16 v[4:7], a[100:103], a[164:167], v[4:7]\n\t"
"buffer_load_dwordx4 a[44:47], v248, s[8:11], s20 offen offset:2048 sc1\n\t"
"v_mfma_f32_16x16x32_bf16 v[192:195], a[200:203], a[216:219], 0\n\t"
"ds_read_b128 a[164:167], v216 offset:13312\n\t"
"ds_read_b64_tr_b16 a[188:189], v252 offset:2304\n\t"
"s_waitcnt lgkmcnt(15)\n\t"
"v_mfma_f32_16x16x32_bf16 v[8:11], a[96:99], a[152:155], v[8:11]\n\t"
"v_cvt_pk_bf16_f32 v210, v212, v213\n\t"
"v_cvt_pk_bf16_f32 v211, v214, v215\n\t"
"v_mfma_f32_16x16x32_bf16 v[8:11], a[100:103], a[168:171], v[8:11]\n\t"
"buffer_load_dwordx4 a[36:39], v248, s[8:11], s20 offen offset:0 sc1\n\t"
"v_mfma_f32_16x16x32_bf16 v[24:27], a[104:107], a[152:155], v[24:27]\n\t"
"ds_read_b64_tr_b16 a[190:191], v252 offset:2816\n\t"
"v_mfma_f32_16x16x32_bf16 v[24:27], a[108:111], a[168:171], v[24:27]\n\t"
"v_cvt_pk_bf16_f32 v192, v192, v193\n\t"
"v_cvt_pk_bf16_f32 v193, v194, v195\n\t"
"v_mfma_f32_16x16x32_bf16 v[196:199], a[204:207], a[216:219], 0\n\t"
"ds_write_b128 v217, v[208:211] offset:24576\n\t"
"v_mfma_f32_16x16x32_bf16 v[40:43], a[112:115], a[152:155], v[40:43]\n\t"
"ds_read_b64_tr_b16 a[192:193], v251 offset:0\n\t"
"v_mfma_f32_16x16x32_bf16 v[40:43], a[116:119], a[168:171], v[40:43]\n\t"
"s_mov_b32 s20, 638976\n\t"
"buffer_load_dwordx4 a[32:35], v248, s[8:11], s20 offen offset:2048 sc1\n\t"
"s_waitcnt lgkmcnt(13)\n\t"
"v_mfma_f32_16x16x32_bf16 v[44:47], a[112:115], a[156:159], v[44:47]\n\t"
"ds_read_b64_tr_b16 a[194:195], v251 offset:512\n\t"
"v_mfma_f32_16x16x32_bf16 v[44:47], a[116:119], a[172:175], v[44:47]\n\t"
"buffer_load_dwordx4 a[24:27], v248, s[8:11], s20 offen offset:0 sc1\n\t"
"v_mfma_f32_16x16x32_bf16 v[28:31], a[104:107], a[156:159], v[28:31]\n\t"
"ds_read_b64_tr_b16 a[196:197], v252 offset:0\n\t"
"v_cvt_pk_bf16_f32 v194, v196, v197\n\t"
"v_mfma_f32_16x16x32_bf16 v[28:31], a[108:111], a[172:175], v[28:31]\n\t"
"ds_read_b64_tr_b16 a[198:199], v252 offset:512\n\t"
"ds_read_b64_tr_b16 a[200:201], v249 offset:2304\n\t"
"v_mfma_f32_16x16x32_bf16 v[12:15], a[96:99], a[156:159], v[12:15]\n\t"
"ds_read_b64_tr_b16 a[202:203], v249 offset:2816\n\t"
"v_cvt_pk_bf16_f32 v195, v198, v199\n\t"
"v_mfma_f32_16x16x32_bf16 v[12:15], a[100:103], a[172:175], v[12:15]\n\t"
"ds_write_b128 v219, v[192:195] offset:24576\n\t"
"s_waitcnt vmcnt(11) lgkmcnt(14)\n\t"
"v_mfma_f32_16x16x32_bf16 v[48:51], a[120:123], a[144:147], v[48:51]\n\t"
"ds_read_b64_tr_b16 a[204:205], v250 offset:2304\n\t"
"v_mfma_f32_16x16x32_bf16 v[48:51], a[124:127], a[160:163], v[48:51]\n\t"
"ds_read_b64_tr_b16 a[206:207], v250 offset:2816\n\t"
"ds_read_b128 a[152:155], v216 offset:6144\n\t"
"v_mfma_f32_16x16x32_bf16 v[64:67], a[128:131], a[144:147], v[64:67]\n\t"
"ds_read_b128 a[168:171], v216 offset:14336\n\t"
"v_mfma_f32_16x16x32_bf16 v[64:67], a[132:135], a[160:163], v[64:67]\n\t"
"buffer_load_dwordx4 a[28:31], v248, s[8:11], s20 offen offset:1024 sc1\n\t"
"v_mfma_f32_16x16x32_bf16 v[80:83], a[136:139], a[144:147], v[80:83]\n\t"
"ds_read_b128 a[156:159], v216 offset:7168\n\t"
"v_mfma_f32_16x16x32_bf16 v[80:83], a[140:143], a[160:163], v[80:83]\n\t"
"s_mov_b32 s20, 921600\n\t"
"buffer_load_dwordx4 a[64:67], v248, s[8:11], s20 offen offset:1024 sc1\n\t"
"ds_read_b128 a[172:175], v216 offset:15360\n\t"
"s_waitcnt lgkmcnt(15)\n\t"
"v_mfma_f32_16x16x32_bf16 v[84:87], a[136:139], a[148:151], v[84:87]\n\t"
"v_mfma_f32_16x16x32_bf16 v[84:87], a[140:143], a[164:167], v[84:87]\n\t"
"buffer_load_dwordx4 a[68:71], v248, s[8:11], s20 offen offset:2048 sc1\n\t"
"v_mfma_f32_16x16x32_bf16 v[68:71], a[128:131], a[148:151], v[68:71]\n\t"
"v_mfma_f32_16x16x32_bf16 v[68:71], a[132:135], a[164:167], v[68:71]\n\t"
"buffer_load_dwordx4 a[60:63], v248, s[8:11], s20 offen offset:0 sc1\n\t"
"v_mfma_f32_16x16x32_bf16 v[52:55], a[120:123], a[148:151], v[52:55]\n\t"
"v_mfma_f32_16x16x32_bf16 v[52:55], a[124:127], a[164:167], v[52:55]\n\t"
"s_mov_b32 s20, 909312\n\t"
"buffer_load_dwordx4 a[56:59], v248, s[8:11], s20 offen offset:2048 sc1\n\t"
"s_waitcnt lgkmcnt(0)\n\t"
"s_barrier\n\t"
"v_mfma_f32_16x16x32_bf16 v[56:59], a[120:123], a[152:155], v[56:59]\n\t"
"ds_read_b128 a[144:147], v216 offset:16384\n\t"
"v_mfma_f32_16x16x32_bf16 v[56:59], a[124:127], a[168:171], v[56:59]\n\t"
"ds_read_b128 a[160:163], v216 offset:24576\n\t"
"ds_read_b128 a[148:151], v216 offset:17408\n\t"
"v_mfma_f32_16x16x32_bf16 v[72:75], a[128:131], a[152:155], v[72:75]\n\t"
"ds_read_b128 a[164:167], v216 offset:25600\n\t"
"v_mfma_f32_16x16x32_bf16 v[72:75], a[132:135], a[168:171], v[72:75]\n\t"
"buffer_load_dwordx4 a[48:51], v248, s[8:11], s20 offen offset:0 sc1\n\t"
"v_mfma_f32_16x16x32_bf16 v[88:91], a[136:139], a[152:155], v[88:91]\n\t"
"ds_read_b128 a[152:155], v216 offset:18432\n\t"
"v_mfma_f32_16x16x32_bf16 v[88:91], a[140:143], a[168:171], v[88:91]\n\t"
"buffer_load_dwordx4 a[52:55], v248, s[8:11], s20 offen offset:1024 sc1\n\t"
"v_mfma_f32_16x16x32_bf16 v[200:203], a[176:179], a[212:215], 0\n\t"
"s_mov_b32 s20, 946176\n\t"
"buffer_load_dwordx4 a[88:91], v248, s[8:11], s20 offen offset:1024 sc1\n\t"
"v_mfma_f32_16x16x32_bf16 v[92:95], a[136:139], a[156:159], v[92:95]\n\t"
"ds_read_b128 a[168:171], v216 offset:26624\n\t"
"v_mfma_f32_16x16x32_bf16 v[92:95], a[140:143], a[172:175], v[92:95]\n\t"
"v_mfma_f32_16x16x32_bf16 v[204:207], a[180:183], a[212:215], 0\n\t"
"v_mfma_f32_16x16x32_bf16 v[76:79], a[128:131], a[156:159], v[76:79]\n\t"
"s_nop 0\n\t"
"v_cvt_pk_bf16_f32 v200, v200, v201\n\t"
"v_cvt_pk_bf16_f32 v201, v202, v203\n\t"
"v_mfma_f32_16x16x32_bf16 v[76:79], a[132:135], a[172:175], v[76:79]\n\t"
"buffer_load_dwordx4 a[92:95], v248, s[8:11], s20 offen offset:2048 sc1\n\t"
"v_mfma_f32_16x16x32_bf16 v[60:63], a[120:123], a[156:159], v[60:63]\n\t"
"ds_read_b128 a[156:159], v216 offset:19456\n\t"
"v_mfma_f32_16x16x32_bf16 v[60:63], a[124:127], a[172:175], v[60:63]\n\t"
"v_cvt_pk_bf16_f32 v202, v204, v205\n\t"
"v_cvt_pk_bf16_f32 v203, v206, v207\n\t"
"v_mfma_f32_16x16x32_bf16 v[208:211], a[184:187], a[212:215], 0\n\t"
"buffer_load_dwordx4 a[84:87], v248, s[8:11], s20 offen offset:0 sc1\n\t"
"s_waitcnt vmcnt(15) lgkmcnt(5)\n\t"
"v_mfma_f32_16x16x32_bf16 v[96:99], a[0:3], a[144:147], v[96:99]\n\t"
"ds_read_b128 a[172:175], v216 offset:27648\n\t"
"v_mfma_f32_16x16x32_bf16 v[96:99], a[4:7], a[160:163], v[96:99]\n\t"
"ds_write_b128 v217, v[200:203] offset:0\n\t"
"v_mfma_f32_16x16x32_bf16 v[212:215], a[188:191], a[212:215], 0\n\t"
"s_mov_b32 s20, 933888\n\t"
"buffer_load_dwordx4 a[80:83], v248, s[8:11], s20 offen offset:2048 sc1\n\t"
"v_mfma_f32_16x16x32_bf16 v[112:115], a[8:11], a[144:147], v[112:115]\n\t"
"v_cvt_pk_bf16_f32 v208, v208, v209\n\t"
"v_mfma_f32_16x16x32_bf16 v[112:115], a[12:15], a[160:163], v[112:115]\n\t"
"v_cvt_pk_bf16_f32 v209, v210, v211\n\t"
"v_mfma_f32_16x16x32_bf16 v[128:131], a[16:19], a[144:147], v[128:131]\n\t"
"ds_read_b128 a[144:147], v216 offset:20480\n\t"
"v_mfma_f32_16x16x32_bf16 v[128:131], a[20:23], a[160:163], v[128:131]\n\t"
"buffer_load_dwordx4 a[72:75], v248, s[8:11], s20 offen offset:0 sc1\n\t"
"v_mfma_f32_16x16x32_bf16 v[192:195], a[192:195], a[212:215], 0\n\t"
"v_cvt_pk_bf16_f32 v210, v212, v213\n\t"
"v_cvt_pk_bf16_f32 v211, v214, v215\n\t"
"s_waitcnt lgkmcnt(6)\n\t"
"v_mfma_f32_16x16x32_bf16 v[132:135], a[16:19], a[148:151], v[132:135]\n\t"
"ds_read_b128 a[160:163], v216 offset:28672\n\t"
"v_mfma_f32_16x16x32_bf16 v[132:135], a[20:23], a[164:167], v[132:135]\n\t"
"v_mfma_f32_16x16x32_bf16 v[116:119], a[8:11], a[148:151], v[116:119]\n\t"
"v_mfma_f32_16x16x32_bf16 v[116:119], a[12:15], a[164:167], v[116:119]\n\t"
"buffer_load_dwordx4 a[76:79], v248, s[8:11], s20 offen offset:1024 sc1\n\t"
"v_mfma_f32_16x16x32_bf16 v[196:199], a[196:199], a[212:215], 0\n\t"
"ds_write_b128 v219, v[208:211] offset:0\n\t"
"v_mfma_f32_16x16x32_bf16 v[100:103], a[0:3], a[148:151], v[100:103]\n\t"
"v_cvt_pk_bf16_f32 v192, v192, v193\n\t"
"v_cvt_pk_bf16_f32 v193, v194, v195\n\t"
"v_mfma_f32_16x16x32_bf16 v[100:103], a[4:7], a[164:167], v[100:103]\n\t"
"s_mov_b32 s20, 1019904\n\t"
"buffer_load_dwordx4 a[112:115], v248, s[8:11], s20 offen offset:1024 sc1\n\t"
"v_mfma_f32_16x16x32_bf16 v[200:203], a[200:203], a[212:215], 0\n\t"
"ds_read_b128 a[148:151], v216 offset:21504\n\t"
"ds_read_b128 a[164:167], v216 offset:29696\n\t"
"s_waitcnt lgkmcnt(8)\n\t"
"v_mfma_f32_16x16x32_bf16 v[104:107], a[0:3], a[152:155], v[104:107]\n\t"
"v_cvt_pk_bf16_f32 v194, v196, v197\n\t"
"v_mfma_f32_16x16x32_bf16 v[104:107], a[4:7], a[168:171], v[104:107]\n\t"
"v_cvt_pk_bf16_f32 v195, v198, v199\n\t"
"v_mfma_f32_16x16x32_bf16 v[120:123], a[8:11], a[152:155], v[120:123]\n\t"
"v_mfma_f32_16x16x32_bf16 v[120:123], a[12:15], a[168:171], v[120:123]\n\t"
"buffer_load_dwordx4 a[116:119], v248, s[8:11], s20 offen offset:2048 sc1\n\t"
"v_mfma_f32_16x16x32_bf16 v[204:207], a[204:207], a[212:215], 0\n\t"
"buffer_load_dwordx4 a[108:111], v248, s[8:11], s20 offen offset:0 sc1\n\t"
"v_mfma_f32_16x16x32_bf16 v[136:139], a[16:19], a[152:155], v[136:139]\n\t"
"v_cvt_pk_bf16_f32 v200, v200, v201\n\t"
"v_cvt_pk_bf16_f32 v201, v202, v203\n\t"
"v_mfma_f32_16x16x32_bf16 v[136:139], a[20:23], a[168:171], v[136:139]\n\t"
"ds_read_b128 a[152:155], v216 offset:22528\n\t"
"s_waitcnt lgkmcnt(7)\n\t"
"v_mfma_f32_16x16x32_bf16 v[140:143], a[16:19], a[156:159], v[140:143]\n\t"
"ds_read_b128 a[168:171], v216 offset:30720\n\t"
"v_mfma_f32_16x16x32_bf16 v[140:143], a[20:23], a[172:175], v[140:143]\n\t"
"ds_write_b128 v217, v[192:195] offset:8192\n\t"
"v_mfma_f32_16x16x32_bf16 v[124:127], a[8:11], a[156:159], v[124:127]\n\t"
"v_cvt_pk_bf16_f32 v202, v204, v205\n\t"
"v_mfma_f32_16x16x32_bf16 v[124:127], a[12:15], a[172:175], v[124:127]\n\t"
"s_mov_b32 s20, 1007616\n\t"
"buffer_load_dwordx4 a[104:107], v248, s[8:11], s20 offen offset:2048 sc1\n\t"
"v_mfma_f32_16x16x32_bf16 v[108:111], a[0:3], a[156:159], v[108:111]\n\t"
"v_cvt_pk_bf16_f32 v203, v206, v207\n\t"
"v_mfma_f32_16x16x32_bf16 v[108:111], a[4:7], a[172:175], v[108:111]\n\t"
"ds_read_b128 a[156:159], v216 offset:23552\n\t"
"s_waitcnt vmcnt(16) lgkmcnt(7)\n\t"
"v_mfma_f32_16x16x32_bf16 v[144:147], a[24:27], a[144:147], v[144:147]\n\t"
"ds_read_b128 a[172:175], v216 offset:31744\n\t"
"v_mfma_f32_16x16x32_bf16 v[144:147], a[28:31], a[160:163], v[144:147]\n\t"
"ds_write_b128 v219, v[200:203] offset:8192\n\t"
"buffer_load_dwordx4 a[96:99], v248, s[8:11], s20 offen offset:0 sc1\n\t"
"v_mfma_f32_16x16x32_bf16 v[160:163], a[32:35], a[144:147], v[160:163]\n\t"
"v_mfma_f32_16x16x32_bf16 v[160:163], a[36:39], a[160:163], v[160:163]\n\t"
"v_mfma_f32_16x16x32_bf16 v[176:179], a[40:43], a[144:147], v[176:179]\n\t"
"v_mfma_f32_16x16x32_bf16 v[176:179], a[44:47], a[160:163], v[176:179]\n\t"
"buffer_load_dwordx4 a[100:103], v248, s[8:11], s20 offen offset:1024 sc1\n\t"
"s_waitcnt lgkmcnt(6)\n\t"
"v_mfma_f32_16x16x32_bf16 v[180:183], a[40:43], a[148:151], v[180:183]\n\t"
"v_mfma_f32_16x16x32_bf16 v[180:183], a[44:47], a[164:167], v[180:183]\n\t"
"s_mov_b32 s20, 1044480\n\t"
"buffer_load_dwordx4 a[136:139], v248, s[8:11], s20 offen offset:1024 sc1\n\t"
"v_mfma_f32_16x16x32_bf16 v[164:167], a[32:35], a[148:151], v[164:167]\n\t"
"v_mfma_f32_16x16x32_bf16 v[164:167], a[36:39], a[164:167], v[164:167]\n\t"
"buffer_load_dwordx4 a[140:143], v248, s[8:11], s20 offen offset:2048 sc1\n\t"
"v_mfma_f32_16x16x32_bf16 v[148:151], a[24:27], a[148:151], v[148:151]\n\t"
"v_mfma_f32_16x16x32_bf16 v[148:151], a[28:31], a[164:167], v[148:151]\n\t"
"s_waitcnt lgkmcnt(0)\n\t"
"s_barrier\n\t"
"v_mfma_f32_16x16x32_bf16 v[152:155], a[24:27], a[152:155], v[152:155]\n\t"
"ds_read_b128 a[144:147], v216 offset:0\n\t"
"v_add_u32 v249, 122368, v226\n\t"
"v_mfma_f32_16x16x32_bf16 v[152:155], a[28:31], a[168:171], v[152:155]\n\t"
"ds_read_b128 a[160:163], v216 offset:8192\n\t"
"ds_read_b128 a[148:151], v216 offset:1024\n\t"
"v_mfma_f32_16x16x32_bf16 v[168:171], a[32:35], a[152:155], v[168:171]\n\t"
"ds_read_b128 a[164:167], v216 offset:9216\n\t"
"v_xor_b32 v250, 32, v249\n\t"
"v_mfma_f32_16x16x32_bf16 v[168:171], a[36:39], a[168:171], v[168:171]\n\t"
"buffer_load_dwordx4 a[132:135], v248, s[8:11], s20 offen offset:0 sc1\n\t"
"v_mfma_f32_16x16x32_bf16 v[184:187], a[40:43], a[152:155], v[184:187]\n\t"
"v_xor_b32 v251, 64, v249\n\t"
"v_xor_b32 v252, 96, v249\n\t"
"v_mfma_f32_16x16x32_bf16 v[184:187], a[44:47], a[168:171], v[184:187]\n\t"
"s_mov_b32 s20, 1032192\n\t"
"buffer_load_dwordx4 a[128:131], v248, s[8:11], s20 offen offset:2048 sc1\n\t"
"v_mfma_f32_16x16x32_bf16 v[208:211], a[176:179], a[216:219], 0\n\t"
"buffer_load_dwordx4 a[120:123], v248, s[8:11], s20 offen offset:0 sc1\n\t"
"v_mfma_f32_16x16x32_bf16 v[188:191], a[40:43], a[156:159], v[188:191]\n\t"
"ds_read_b128 a[152:155], v216 offset:2048\n\t"
"v_mfma_f32_16x16x32_bf16 v[188:191], a[44:47], a[172:175], v[188:191]\n\t"
"buffer_load_dwordx4 a[124:127], v248, s[8:11], s20 offen offset:1024 sc1\n\t"
"v_mfma_f32_16x16x32_bf16 v[212:215], a[180:183], a[216:219], 0\n\t"
"ds_read_b128 a[168:171], v216 offset:10240\n\t"
"ds_read_b64_tr_b16 a[176:177], v249 offset:0\n\t"
"v_mfma_f32_16x16x32_bf16 v[172:175], a[32:35], a[156:159], v[172:175]\n\t"
"v_cvt_pk_bf16_f32 v208, v208, v209\n\t"
"v_cvt_pk_bf16_f32 v209, v210, v211\n\t"
"v_mfma_f32_16x16x32_bf16 v[172:175], a[36:39], a[172:175], v[172:175]\n\t"
"ds_read_b64_tr_b16 a[178:179], v249 offset:512\n\t"
"ds_read_b64_tr_b16 a[180:181], v250 offset:0\n\t"
"v_mfma_f32_16x16x32_bf16 v[156:159], a[24:27], a[156:159], v[156:159]\n\t"
"ds_read_b128 a[156:159], v216 offset:3072\n\t"
"v_mfma_f32_16x16x32_bf16 v[156:159], a[28:31], a[172:175], v[156:159]\n\t"
"s_mov_b32 s20, 503808\n\t"
"buffer_load_dwordx4 a[16:19], v248, s[8:11], s20 offen offset:1024 sc1\n\t"
"v_mfma_f32_16x16x32_bf16 v[192:195], a[184:187], a[216:219], 0\n\t"
"v_cvt_pk_bf16_f32 v210, v212, v213\n\t"
"v_cvt_pk_bf16_f32 v211, v214, v215\n\t"
"s_waitcnt vmcnt(19) lgkmcnt(8)\n\t"
"v_mfma_f32_16x16x32_bf16 v[0:3], a[48:51], a[144:147], v[0:3]\n\t"
"ds_read_b128 a[172:175], v216 offset:11264\n\t"
"v_mfma_f32_16x16x32_bf16 v[0:3], a[52:55], a[160:163], v[0:3]\n\t"
"buffer_load_dwordx4 a[20:23], v248, s[8:11], s20 offen offset:2048 sc1\n\t"
"v_mfma_f32_16x16x32_bf16 v[196:199], a[188:191], a[216:219], 0\n\t"
"ds_write_b128 v217, v[208:211] offset:16384\n\t"
"v_mfma_f32_16x16x32_bf16 v[16:19], a[56:59], a[144:147], v[16:19]\n\t"
"v_cvt_pk_bf16_f32 v192, v192, v193\n\t"
"v_cvt_pk_bf16_f32 v193, v194, v195\n\t"
"v_mfma_f32_16x16x32_bf16 v[16:19], a[60:63], a[160:163], v[16:19]\n\t"
"buffer_load_dwordx4 a[12:15], v248, s[8:11], s20 offen offset:0 sc1\n\t"
"v_mfma_f32_16x16x32_bf16 v[32:35], a[64:67], a[144:147], v[32:35]\n\t"
"ds_read_b128 a[144:147], v216 offset:4096\n\t"
"v_mfma_f32_16x16x32_bf16 v[32:35], a[68:71], a[160:163], v[32:35]\n\t"
"v_cvt_pk_bf16_f32 v194, v196, v197\n\t"
"v_cvt_pk_bf16_f32 v195, v198, v199\n\t"
"v_mfma_f32_16x16x32_bf16 v[200:203], a[192:195], a[216:219], 0\n\t"
"ds_read_b128 a[160:163], v216 offset:12288\n\t"
"ds_read_b64_tr_b16 a[182:183], v250 offset:512\n\t"
"s_waitcnt lgkmcnt(11)\n\t"
"v_mfma_f32_16x16x32_bf16 v[36:39], a[64:67], a[148:151], v[36:39]\n\t"
"ds_read_b64_tr_b16 a[184:185], v251 offset:2304\n\t"
"v_mfma_f32_16x16x32_bf16 v[36:39], a[68:71], a[164:167], v[36:39]\n\t"
"s_mov_b32 s20, 491520\n\t"
"buffer_load_dwordx4 a[8:11], v248, s[8:11], s20 offen offset:2048 sc1\n\t"
"v_mfma_f32_16x16x32_bf16 v[20:23], a[56:59], a[148:151], v[20:23]\n\t"
"ds_read_b64_tr_b16 a[186:187], v251 offset:2816\n\t"
"v_mfma_f32_16x16x32_bf16 v[20:23], a[60:63], a[164:167], v[20:23]\n\t"
"buffer_load_dwordx4 a[0:3], v248, s[8:11], s20 offen offset:0 sc1\n\t"
"v_mfma_f32_16x16x32_bf16 v[204:207], a[196:199], a[216:219], 0\n\t"
"ds_write_b128 v219, v[192:195] offset:16384\n\t"
"v_mfma_f32_16x16x32_bf16 v[4:7], a[48:51], a[148:151], v[4:7]\n\t"
"v_cvt_pk_bf16_f32 v200, v200, v201\n\t"
"v_cvt_pk_bf16_f32 v201, v202, v203\n\t"
"v_mfma_f32_16x16x32_bf16 v[4:7], a[52:55], a[164:167], v[4:7]\n\t"
"ds_read_b128 a[148:151], v216 offset:5120\n\t"
"ds_read_b128 a[164:167], v216 offset:13312\n\t"
"v_mfma_f32_16x16x32_bf16 v[208:211], a[200:203], a[216:219], 0\n\t"
"buffer_load_dwordx4 a[4:7], v248, s[8:11], s20 offen offset:1024 sc1\n\t"
"s_waitcnt lgkmcnt(14)\n\t"
"v_mfma_f32_16x16x32_bf16 v[8:11], a[48:51], a[152:155], v[8:11]\n\t"
"v_cvt_pk_bf16_f32 v202, v204, v205\n\t"
"v_cvt_pk_bf16_f32 v203, v206, v207\n\t"
"v_mfma_f32_16x16x32_bf16 v[8:11], a[52:55], a[168:171], v[8:11]\n\t"
"ds_write_b128 v217, v[200:203] offset:24576\n\t"
"v_mfma_f32_16x16x32_bf16 v[24:27], a[56:59], a[152:155], v[24:27]\n\t"
"ds_read_b64_tr_b16 a[188:189], v252 offset:2304\n\t"
"v_mfma_f32_16x16x32_bf16 v[24:27], a[60:63], a[168:171], v[24:27]\n\t"
"s_mov_b32 s20, 602112\n\t"
"buffer_load_dwordx4 a[40:43], v248, s[8:11], s20 offen offset:1024 sc1\n\t"
"v_mfma_f32_16x16x32_bf16 v[212:215], a[204:207], a[216:219], 0\n\t"
"v_cvt_pk_bf16_f32 v208, v208, v209\n\t"
"v_cvt_pk_bf16_f32 v209, v210, v211\n\t"
"v_mfma_f32_16x16x32_bf16 v[40:43], a[64:67], a[152:155], v[40:43]\n\t"
"ds_read_b64_tr_b16 a[190:191], v252 offset:2816\n\t"
"v_mfma_f32_16x16x32_bf16 v[40:43], a[68:71], a[168:171], v[40:43]\n\t"
"buffer_load_dwordx4 a[44:47], v248, s[8:11], s20 offen offset:2048 sc1\n\t"
"s_waitcnt lgkmcnt(12)\n\t"
"v_mfma_f32_16x16x32_bf16 v[44:47], a[64:67], a[156:159], v[44:47]\n\t"
"ds_read_b64_tr_b16 a[192:193], v251 offset:0\n\t"
"v_mfma_f32_16x16x32_bf16 v[44:47], a[68:71], a[172:175], v[44:47]\n\t"
"ds_read_b64_tr_b16 a[194:195], v251 offset:512\n\t"
"ds_read_b64_tr_b16 a[196:197], v252 offset:0\n\t"
"v_mfma_f32_16x16x32_bf16 v[28:31], a[56:59], a[156:159], v[28:31]\n\t"
"ds_read_b64_tr_b16 a[198:199], v252 offset:512\n\t"
"v_cvt_pk_bf16_f32 v210, v212, v213\n\t"
"v_mfma_f32_16x16x32_bf16 v[28:31], a[60:63], a[172:175], v[28:31]\n\t"
"buffer_load_dwordx4 a[36:39], v248, s[8:11], s20 offen offset:0 sc1\n\t"
"v_mfma_f32_16x16x32_bf16 v[12:15], a[48:51], a[156:159], v[12:15]\n\t"
"ds_read_b64_tr_b16 a[200:201], v249 offset:2304\n\t"
"v_cvt_pk_bf16_f32 v211, v214, v215\n\t"
"v_mfma_f32_16x16x32_bf16 v[12:15], a[52:55], a[172:175], v[12:15]\n\t"
"ds_write_b128 v219, v[208:211] offset:24576\n\t"
"s_waitcnt vmcnt(21) lgkmcnt(15)\n\t"
"v_mfma_f32_16x16x32_bf16 v[48:51], a[72:75], a[144:147], v[48:51]\n\t"
"ds_read_b64_tr_b16 a[202:203], v249 offset:2816\n\t"
"v_mfma_f32_16x16x32_bf16 v[48:51], a[76:79], a[160:163], v[48:51]\n\t"
"ds_read_b64_tr_b16 a[204:205], v250 offset:2304\n\t"
"ds_read_b64_tr_b16 a[206:207], v250 offset:2816\n\t"
"v_mfma_f32_16x16x32_bf16 v[64:67], a[80:83], a[144:147], v[64:67]\n\t"
"ds_read_b128 a[152:155], v216 offset:6144\n\t"
"v_mfma_f32_16x16x32_bf16 v[64:67], a[84:87], a[160:163], v[64:67]\n\t"
"s_mov_b32 s20, 589824\n\t"
"buffer_load_dwordx4 a[32:35], v248, s[8:11], s20 offen offset:2048 sc1\n\t"
"v_mfma_f32_16x16x32_bf16 v[80:83], a[88:91], a[144:147], v[80:83]\n\t"
"ds_read_b128 a[168:171], v216 offset:14336\n\t"
"v_mfma_f32_16x16x32_bf16 v[80:83], a[92:95], a[160:163], v[80:83]\n\t"
"ds_read_b128 a[156:159], v216 offset:7168\n\t"
"ds_read_b128 a[172:175], v216 offset:15360\n\t"
"s_waitcnt lgkmcnt(15)\n\t"
"v_mfma_f32_16x16x32_bf16 v[84:87], a[88:91], a[148:151], v[84:87]\n\t"
"v_mfma_f32_16x16x32_bf16 v[84:87], a[92:95], a[164:167], v[84:87]\n\t"
"buffer_load_dwordx4 a[24:27], v248, s[8:11], s20 offen offset:0 sc1\n\t"
"v_mfma_f32_16x16x32_bf16 v[68:71], a[80:83], a[148:151], v[68:71]\n\t"
"v_mfma_f32_16x16x32_bf16 v[68:71], a[84:87], a[164:167], v[68:71]\n\t"
"buffer_load_dwordx4 a[28:31], v248, s[8:11], s20 offen offset:1024 sc1\n\t"
"v_mfma_f32_16x16x32_bf16 v[52:55], a[72:75], a[148:151], v[52:55]\n\t"
"v_mfma_f32_16x16x32_bf16 v[52:55], a[76:79], a[164:167], v[52:55]\n\t"
"s_mov_b32 s20, 577536\n\t"
"buffer_load_dwordx4 a[64:67], v248, s[8:11], s20 offen offset:1024 sc1\n\t"
"s_waitcnt lgkmcnt(0)\n\t"
"s_barrier\n\t"
"v_mfma_f32_16x16x32_bf16 v[56:59], a[72:75], a[152:155], v[56:59]\n\t"
"ds_read_b128 a[144:147], v216 offset:16384\n\t"
"v_mfma_f32_16x16x32_bf16 v[56:59], a[76:79], a[168:171], v[56:59]\n\t"
"ds_read_b128 a[160:163], v216 offset:24576\n\t"
"ds_read_b128 a[148:151], v216 offset:17408\n\t"
"v_mfma_f32_16x16x32_bf16 v[72:75], a[80:83], a[152:155], v[72:75]\n\t"
"ds_read_b128 a[164:167], v216 offset:25600\n\t"
"v_mfma_f32_16x16x32_bf16 v[72:75], a[84:87], a[168:171], v[72:75]\n\t"
"buffer_load_dwordx4 a[68:71], v248, s[8:11], s20 offen offset:2048 sc1\n\t"
"v_mfma_f32_16x16x32_bf16 v[88:91], a[88:91], a[152:155], v[88:91]\n\t"
"ds_read_b128 a[240:243], v223 offset:39168\n\t"
"v_mfma_f32_16x16x32_bf16 v[88:91], a[92:95], a[168:171], v[88:91]\n\t"
"buffer_load_dwordx4 a[60:63], v248, s[8:11], s20 offen offset:0 sc1\n\t"
"v_mfma_f32_16x16x32_bf16 v[192:195], a[176:179], a[220:223], 0\n\t"
"ds_read_b128 a[152:155], v216 offset:18432\n\t"
"ds_read_b128 a[168:171], v216 offset:26624\n\t"
"v_mfma_f32_16x16x32_bf16 v[92:95], a[88:91], a[156:159], v[92:95]\n\t"
"ds_read_b64_tr_b16 a[176:177], v251 offset:20736\n\t"
"v_mfma_f32_16x16x32_bf16 v[92:95], a[92:95], a[172:175], v[92:95]\n\t"
"s_mov_b32 s20, 565248\n\t"
"buffer_load_dwordx4 a[56:59], v248, s[8:11], s20 offen offset:2048 sc1\n\t"
"v_mfma_f32_16x16x32_bf16 v[196:199], a[180:183], a[220:223], 0\n\t"
"ds_read_b64_tr_b16 a[178:179], v251 offset:21248\n\t"
"ds_read_b64_tr_b16 a[180:181], v252 offset:20736\n\t"
"v_mfma_f32_16x16x32_bf16 v[76:79], a[80:83], a[156:159], v[76:79]\n\t"
"v_cvt_pk_bf16_f32 v192, v192, v193\n\t"
"v_cvt_pk_bf16_f32 v193, v194, v195\n\t"
"v_mfma_f32_16x16x32_bf16 v[76:79], a[84:87], a[172:175], v[76:79]\n\t"
"buffer_load_dwordx4 a[48:51], v248, s[8:11], s20 offen offset:0 sc1\n\t"
"v_mfma_f32_16x16x32_bf16 v[60:63], a[72:75], a[156:159], v[60:63]\n\t"
"ds_read_b128 a[156:159], v216 offset:19456\n\t"
"v_mfma_f32_16x16x32_bf16 v[60:63], a[76:79], a[172:175], v[60:63]\n\t"
"buffer_load_dwordx4 a[52:55], v248, s[8:11], s20 offen offset:1024 sc1\n\t"
"v_mfma_f32_16x16x32_bf16 v[200:203], a[184:187], a[220:223], 0\n\t"
"s_mov_b32 s20, 675840\n\t"
"buffer_load_dwordx4 a[88:91], v248, s[8:11], s20 offen offset:1024 sc1\n\t"
"s_waitcnt vmcnt(25) lgkmcnt(9)\n\t"
"v_mfma_f32_16x16x32_bf16 v[96:99], a[96:99], a[144:147], v[96:99]\n\t"
"v_cvt_pk_bf16_f32 v194, v196, v197\n\t"
"v_cvt_pk_bf16_f32 v195, v198, v199\n\t"
"v_mfma_f32_16x16x32_bf16 v[96:99], a[100:103], a[160:163], v[96:99]\n\t"
"buffer_load_dwordx4 a[92:95], v248, s[8:11], s20 offen offset:2048 sc1\n\t"
"v_mfma_f32_16x16x32_bf16 v[204:207], a[188:191], a[220:223], 0\n\t"
"v_pk_add_f32 v[0:1], v[0:1], v[48:49]\n\t"
"v_pk_add_f32 v[2:3], v[2:3], v[50:51]\n\t"
"v_pk_fma_f32 v[48:49], v[48:49], s[38:39], v[0:1]\n\t"
"v_pk_fma_f32 v[50:51], v[50:51], s[38:39], v[2:3]\n\t"
"v_pk_add_f32 v[16:17], v[16:17], v[64:65]\n\t"
"v_pk_add_f32 v[18:19], v[18:19], v[66:67]\n\t"
"v_pk_fma_f32 v[64:65], v[64:65], s[38:39], v[16:17]\n\t"
"v_pk_fma_f32 v[66:67], v[66:67], s[38:39], v[18:19]\n\t"
"v_pk_add_f32 v[32:33], v[32:33], v[80:81]\n\t"
"v_pk_add_f32 v[34:35], v[34:35], v[82:83]\n\t"
"v_pk_fma_f32 v[80:81], v[80:81], s[38:39], v[32:33]\n\t"
"v_pk_fma_f32 v[82:83], v[82:83], s[38:39], v[34:35]\n\t"
"v_pk_add_f32 v[36:37], v[36:37], v[84:85]\n\t"
"v_pk_add_f32 v[38:39], v[38:39], v[86:87]\n\t"
"v_pk_fma_f32 v[84:85], v[84:85], s[38:39], v[36:37]\n\t"
"v_pk_fma_f32 v[86:87], v[86:87], s[38:39], v[38:39]\n\t"
"v_pk_add_f32 v[20:21], v[20:21], v[68:69]\n\t"
"v_pk_add_f32 v[22:23], v[22:23], v[70:71]\n\t"
"v_pk_fma_f32 v[68:69], v[68:69], s[38:39], v[20:21]\n\t"
"v_pk_fma_f32 v[70:71], v[70:71], s[38:39], v[22:23]\n\t"
"v_pk_add_f32 v[4:5], v[4:5], v[52:53]\n\t"
"v_pk_add_f32 v[6:7], v[6:7], v[54:55]\n\t"
"v_pk_fma_f32 v[52:53], v[52:53], s[38:39], v[4:5]\n\t"
"v_pk_fma_f32 v[54:55], v[54:55], s[38:39], v[6:7]\n\t"
"v_pk_add_f32 v[8:9], v[8:9], v[56:57]\n\t"
"v_pk_add_f32 v[10:11], v[10:11], v[58:59]\n\t"
"v_pk_fma_f32 v[56:57], v[56:57], s[38:39], v[8:9]\n\t"
"v_pk_fma_f32 v[58:59], v[58:59], s[38:39], v[10:11]\n\t"
"v_pk_add_f32 v[24:25], v[24:25], v[72:73]\n\t"
"v_pk_add_f32 v[26:27], v[26:27], v[74:75]\n\t"
"v_pk_fma_f32 v[72:73], v[72:73], s[38:39], v[24:25]\n\t"
"v_pk_fma_f32 v[74:75], v[74:75], s[38:39], v[26:27]\n\t"
"v_pk_add_f32 v[40:41], v[40:41], v[88:89]\n\t"
"v_pk_add_f32 v[42:43], v[42:43], v[90:91]\n\t"
"v_pk_fma_f32 v[88:89], v[88:89], s[38:39], v[40:41]\n\t"
"v_pk_fma_f32 v[90:91], v[90:91], s[38:39], v[42:43]\n\t"
"v_pk_add_f32 v[44:45], v[44:45], v[92:93]\n\t"
"v_pk_add_f32 v[46:47], v[46:47], v[94:95]\n\t"
"v_pk_fma_f32 v[92:93], v[92:93], s[38:39], v[44:45]\n\t"
"v_pk_fma_f32 v[94:95], v[94:95], s[38:39], v[46:47]\n\t"
"v_pk_add_f32 v[28:29], v[28:29], v[76:77]\n\t"
"v_pk_add_f32 v[30:31], v[30:31], v[78:79]\n\t"
"v_pk_fma_f32 v[76:77], v[76:77], s[38:39], v[28:29]\n\t"
"v_pk_fma_f32 v[78:79], v[78:79], s[38:39], v[30:31]\n\t"
"v_pk_add_f32 v[12:13], v[12:13], v[60:61]\n\t"
"v_pk_add_f32 v[14:15], v[14:15], v[62:63]\n\t"
"v_pk_fma_f32 v[60:61], v[60:61], s[38:39], v[12:13]\n\t"
"v_pk_fma_f32 v[62:63], v[62:63], s[38:39], v[14:15]\n\t"
"buffer_load_dwordx4 a[84:87], v248, s[8:11], s20 offen offset:0 sc1\n\t"
"v_mfma_f32_16x16x32_bf16 v[112:115], a[104:107], a[144:147], v[112:115]\n\t"
"ds_read_b128 a[172:175], v216 offset:27648\n\t"
"v_cvt_pk_bf16_f32 v200, v200, v201\n\t"
"v_mfma_f32_16x16x32_bf16 v[112:115], a[108:111], a[160:163], v[112:115]\n\t"
"s_mov_b32 s20, 663552\n\t"
"buffer_load_dwordx4 a[80:83], v248, s[8:11], s20 offen offset:2048 sc1\n\t"
"v_mfma_f32_16x16x32_bf16 v[128:131], a[112:115], a[144:147], v[128:131]\n\t"
"v_cvt_pk_bf16_f32 v201, v202, v203\n\t"
"ds_read_b128 a[144:147], v216 offset:20480\n\t"
"v_mfma_f32_16x16x32_bf16 v[128:131], a[116:119], a[160:163], v[128:131]\n\t"
"buffer_load_dwordx4 a[72:75], v248, s[8:11], s20 offen offset:0 sc1\n\t"
"v_mfma_f32_16x16x32_bf16 v[208:211], a[192:195], a[220:223], 0\n\t"
"ds_write_b128 v217, v[192:195] offset:0\n\t"
"s_waitcnt lgkmcnt(10)\n\t"
"v_mfma_f32_16x16x32_bf16 v[132:135], a[112:115], a[148:151], v[132:135]\n\t"
"v_cvt_pk_bf16_f32 v202, v204, v205\n\t"
"v_cvt_pk_bf16_f32 v203, v206, v207\n\t"
"v_mfma_f32_16x16x32_bf16 v[132:135], a[116:119], a[164:167], v[132:135]\n\t"
"buffer_load_dwordx4 a[76:79], v248, s[8:11], s20 offen offset:1024 sc1\n\t"
"v_mfma_f32_16x16x32_bf16 v[116:119], a[104:107], a[148:151], v[116:119]\n\t"
"ds_read_b128 a[160:163], v216 offset:28672\n\t"
"v_mfma_f32_16x16x32_bf16 v[116:119], a[108:111], a[164:167], v[116:119]\n\t"
"ds_write_b128 v219, v[200:203] offset:0\n\t"
"v_mfma_f32_16x16x32_bf16 v[212:215], a[196:199], a[220:223], 0\n\t"
"v_cvt_pk_bf16_f32 v208, v208, v209\n\t"
"v_cvt_pk_bf16_f32 v209, v210, v211\n\t"
"v_mfma_f32_16x16x32_bf16 v[100:103], a[96:99], a[148:151], v[100:103]\n\t"
"ds_read_b128 a[148:151], v216 offset:21504\n\t"
"v_mfma_f32_16x16x32_bf16 v[100:103], a[100:103], a[164:167], v[100:103]\n\t"
"ds_read_b128 a[164:167], v216 offset:29696\n\t"
"ds_read_b64_tr_b16 a[182:183], v252 offset:21248\n\t"
"v_mfma_f32_16x16x32_bf16 v[192:195], a[200:203], a[220:223], 0\n\t"
"ds_read_b64_tr_b16 a[184:185], v249 offset:23040\n\t"
"ds_read_b64_tr_b16 a[186:187], v249 offset:23552\n\t"
"s_waitcnt lgkmcnt(14)\n\t"
"v_mfma_f32_16x16x32_bf16 v[104:107], a[96:99], a[152:155], v[104:107]\n\t"
"v_cvt_pk_bf16_f32 v210, v212, v213\n\t"
"v_cvt_pk_bf16_f32 v211, v214, v215\n\t"
"v_mfma_f32_16x16x32_bf16 v[104:107], a[100:103], a[168:171], v[104:107]\n\t"
"ds_write_b128 v217, v[208:211] offset:8192\n\t"
"v_mfma_f32_16x16x32_bf16 v[120:123], a[104:107], a[152:155], v[120:123]\n\t"
"ds_read_b64_tr_b16 a[188:189], v250 offset:23040\n\t"
"v_mfma_f32_16x16x32_bf16 v[120:123], a[108:111], a[168:171], v[120:123]\n\t"
"v_cvt_pk_bf16_f32 v192, v192, v193\n\t"
"v_cvt_pk_bf16_f32 v193, v194, v195\n\t"
"v_mfma_f32_16x16x32_bf16 v[196:199], a[204:207], a[220:223], 0\n\t"
"ds_read_b64_tr_b16 a[190:191], v250 offset:23552\n\t"
"ds_read_b64_tr_b16 a[192:193], v249 offset:20736\n\t"
"v_mfma_f32_16x16x32_bf16 v[136:139], a[112:115], a[152:155], v[136:139]\n\t"
"ds_read_b64_tr_b16 a[194:195], v249 offset:21248\n\t"
"v_mfma_f32_16x16x32_bf16 v[136:139], a[116:119], a[168:171], v[136:139]\n\t"
"ds_read_b64_tr_b16 a[196:197], v250 offset:20736\n\t"
"ds_read_b64_tr_b16 a[198:199], v250 offset:21248\n\t"
"s_waitcnt lgkmcnt(15)\n\t"
"v_mfma_f32_16x16x32_bf16 v[140:143], a[112:115], a[156:159], v[140:143]\n\t"
"ds_read_b64_tr_b16 a[200:201], v251 offset:23040\n\t"
"v_mfma_f32_16x16x32_bf16 v[140:143], a[116:119], a[172:175], v[140:143]\n\t"
"s_mov_b32 s20, 897024\n\t"
"buffer_load_dwordx4 a[112:115], v248, s[8:11], s20 offen offset:1024 sc1\n\t"
"v_mfma_f32_16x16x32_bf16 v[124:127], a[104:107], a[156:159], v[124:127]\n\t"
"ds_read_b64_tr_b16 a[202:203], v251 offset:23552\n\t"
"v_cvt_pk_bf16_f32 v194, v196, v197\n\t"
"v_mfma_f32_16x16x32_bf16 v[124:127], a[108:111], a[172:175], v[124:127]\n\t"
"buffer_load_dwordx4 a[116:119], v248, s[8:11], s20 offen offset:2048 sc1\n\t"
"v_mfma_f32_16x16x32_bf16 v[108:111], a[96:99], a[156:159], v[108:111]\n\t"
"ds_read_b64_tr_b16 a[204:205], v252 offset:23040\n\t"
"v_cvt_pk_bf16_f32 v195, v198, v199\n\t"
"v_mfma_f32_16x16x32_bf16 v[108:111], a[100:103], a[172:175], v[108:111]\n\t"
"buffer_load_dwordx4 a[108:111], v248, s[8:11], s20 offen offset:0 sc1\n\t"
"s_waitcnt vmcnt(27) lgkmcnt(15)\n\t"
"v_mfma_f32_16x16x32_bf16 v[144:147], a[120:123], a[144:147], v[144:147]\n\t"
"ds_read_b64_tr_b16 a[206:207], v252 offset:23552\n\t"
"v_mfma_f32_16x16x32_bf16 v[144:147], a[124:127], a[160:163], v[144:147]\n\t"
"ds_write_b128 v219, v[192:195] offset:8192\n\t"
"s_mov_b32 s20, 884736\n\t"
"buffer_load_dwordx4 a[104:107], v248, s[8:11], s20 offen offset:2048 sc1\n\t"
"v_mfma_f32_16x16x32_bf16 v[160:163], a[128:131], a[144:147], v[160:163]\n\t"
"ds_read_b128 a[152:155], v216 offset:22528\n\t"
"v_mfma_f32_16x16x32_bf16 v[160:163], a[132:135], a[160:163], v[160:163]\n\t"
"buffer_load_dwordx4 a[96:99], v248, s[8:11], s20 offen offset:0 sc1\n\t"
"buffer_load_dwordx4 a[100:103], v248, s[8:11], s20 offen offset:1024 sc1\n\t"
"v_mfma_f32_16x16x32_bf16 v[176:179], a[136:139], a[144:147], v[176:179]\n\t"
"v_mfma_f32_16x16x32_bf16 v[176:179], a[140:143], a[160:163], v[176:179]\n\t"
"ds_read_b128 a[168:171], v216 offset:30720\n\t"
"ds_read_b128 a[156:159], v216 offset:23552\n\t"
"ds_read_b128 a[172:175], v216 offset:31744\n\t"
"ds_write_b128 v223, a[240:243] offset:20736\n\t"
"s_waitcnt lgkmcnt(15)\n\t"
"v_mfma_f32_16x16x32_bf16 v[180:183], a[136:139], a[148:151], v[180:183]\n\t"
"v_mfma_f32_16x16x32_bf16 v[180:183], a[140:143], a[164:167], v[180:183]\n\t"
"v_mfma_f32_16x16x32_bf16 v[164:167], a[128:131], a[148:151], v[164:167]\n\t"
"v_mfma_f32_16x16x32_bf16 v[164:167], a[132:135], a[164:167], v[164:167]\n\t"
"v_mfma_f32_16x16x32_bf16 v[148:151], a[120:123], a[148:151], v[148:151]\n\t"
"v_mfma_f32_16x16x32_bf16 v[148:151], a[124:127], a[164:167], v[148:151]\n\t"
"s_waitcnt lgkmcnt(0)\n\t"
"s_barrier\n\t"
"v_mfma_f32_16x16x32_bf16 v[152:155], a[120:123], a[152:155], v[152:155]\n\t"
"ds_read_b128 a[144:147], v216 offset:0\n\t"
"v_add_u32 v249, 39424, v226\n\t"
"v_mfma_f32_16x16x32_bf16 v[152:155], a[124:127], a[168:171], v[152:155]\n\t"
"ds_read_b128 a[160:163], v216 offset:8192\n\t"
"ds_read_b128 a[148:151], v216 offset:1024\n\t"
"v_mfma_f32_16x16x32_bf16 v[168:171], a[128:131], a[152:155], v[168:171]\n\t"
"ds_read_b128 a[164:167], v216 offset:9216\n\t"
"v_xor_b32 v250, 32, v249\n\t"
"v_mfma_f32_16x16x32_bf16 v[168:171], a[132:135], a[168:171], v[168:171]\n\t"
"v_xor_b32 v251, 64, v249\n\t"
"v_xor_b32 v252, 96, v249\n\t"
"v_mfma_f32_16x16x32_bf16 v[184:187], a[136:139], a[152:155], v[184:187]\n\t"
"ds_read_b128 a[240:243], v223 offset:59904\n\t"
"v_mfma_f32_16x16x32_bf16 v[184:187], a[140:143], a[168:171], v[184:187]\n\t"
"ds_read_b128 a[152:155], v216 offset:2048\n\t"
"ds_read_b128 a[168:171], v216 offset:10240\n\t"
"v_mfma_f32_16x16x32_bf16 v[200:203], a[176:179], a[220:223], 0\n\t"
"ds_read_b64_tr_b16 a[176:177], v249 offset:0\n\t"
"ds_read_b64_tr_b16 a[178:179], v249 offset:512\n\t"
"v_mfma_f32_16x16x32_bf16 v[188:191], a[136:139], a[156:159], v[188:191]\n\t"
"v_mfma_f32_16x16x32_bf16 v[188:191], a[140:143], a[172:175], v[188:191]\n\t"
"s_mov_b32 s20, 995328\n\t"
"buffer_load_dwordx4 a[136:139], v248, s[8:11], s20 offen offset:1024 sc1\n\t"
"v_mfma_f32_16x16x32_bf16 v[204:207], a[180:183], a[220:223], 0\n\t"
"buffer_load_dwordx4 a[140:143], v248, s[8:11], s20 offen offset:2048 sc1\n\t"
"v_mfma_f32_16x16x32_bf16 v[172:175], a[128:131], a[156:159], v[172:175]\n\t"
"v_cvt_pk_bf16_f32 v200, v200, v201\n\t"
"v_cvt_pk_bf16_f32 v201, v202, v203\n\t"
"v_mfma_f32_16x16x32_bf16 v[172:175], a[132:135], a[172:175], v[172:175]\n\t"
"buffer_load_dwordx4 a[132:135], v248, s[8:11], s20 offen offset:0 sc1\n\t"
"v_mfma_f32_16x16x32_bf16 v[156:159], a[120:123], a[156:159], v[156:159]\n\t"
"ds_read_b128 a[156:159], v216 offset:3072\n\t"
"v_mfma_f32_16x16x32_bf16 v[156:159], a[124:127], a[172:175], v[156:159]\n\t"
"s_mov_b32 s20, 983040\n\t"
"buffer_load_dwordx4 a[128:131], v248, s[8:11], s20 offen offset:2048 sc1\n\t"
"v_mfma_f32_16x16x32_bf16 v[208:211], a[184:187], a[220:223], 0\n\t"
"buffer_load_dwordx4 a[120:123], v248, s[8:11], s20 offen offset:0 sc1\n\t"
"s_waitcnt vmcnt(29) lgkmcnt(8)\n\t"
"v_mfma_f32_16x16x32_bf16 v[0:3], a[0:3], a[144:147], v[0:3]\n\t"
"v_cvt_pk_bf16_f32 v202, v204, v205\n\t"
"v_cvt_pk_bf16_f32 v203, v206, v207\n\t"
"v_mfma_f32_16x16x32_bf16 v[0:3], a[4:7], a[160:163], v[0:3]\n\t"
"ds_read_b128 a[172:175], v216 offset:11264\n\t"
"v_mfma_f32_16x16x32_bf16 v[212:215], a[188:191], a[220:223], 0\n\t"
"buffer_load_dwordx4 a[124:127], v248, s[8:11], s20 offen offset:1024 sc1\n\t"
"v_pk_add_f32 v[96:97], v[96:97], v[144:145]\n\t"
"v_pk_add_f32 v[98:99], v[98:99], v[146:147]\n\t"
"v_pk_fma_f32 v[144:145], v[144:145], s[38:39], v[96:97]\n\t"
"v_pk_fma_f32 v[146:147], v[146:147], s[38:39], v[98:99]\n\t"
"v_pk_add_f32 v[112:113], v[112:113], v[160:161]\n\t"
"v_pk_add_f32 v[114:115], v[114:115], v[162:163]\n\t"
"v_pk_fma_f32 v[160:161], v[160:161], s[38:39], v[112:113]\n\t"
"v_pk_fma_f32 v[162:163], v[162:163], s[38:39], v[114:115]\n\t"
"v_pk_add_f32 v[128:129], v[128:129], v[176:177]\n\t"
"v_pk_add_f32 v[130:131], v[130:131], v[178:179]\n\t"
"v_pk_fma_f32 v[176:177], v[176:177], s[38:39], v[128:129]\n\t"
"v_pk_fma_f32 v[178:179], v[178:179], s[38:39], v[130:131]\n\t"
"v_pk_add_f32 v[132:133], v[132:133], v[180:181]\n\t"
"v_pk_add_f32 v[134:135], v[134:135], v[182:183]\n\t"
"v_pk_fma_f32 v[180:181], v[180:181], s[38:39], v[132:133]\n\t"
"v_pk_fma_f32 v[182:183], v[182:183], s[38:39], v[134:135]\n\t"
"v_pk_add_f32 v[116:117], v[116:117], v[164:165]\n\t"
"v_pk_add_f32 v[118:119], v[118:119], v[166:167]\n\t"
"v_pk_fma_f32 v[164:165], v[164:165], s[38:39], v[116:117]\n\t"
"v_pk_fma_f32 v[166:167], v[166:167], s[38:39], v[118:119]\n\t"
"v_pk_add_f32 v[100:101], v[100:101], v[148:149]\n\t"
"v_pk_add_f32 v[102:103], v[102:103], v[150:151]\n\t"
"v_pk_fma_f32 v[148:149], v[148:149], s[38:39], v[100:101]\n\t"
"v_pk_fma_f32 v[150:151], v[150:151], s[38:39], v[102:103]\n\t"
"v_pk_add_f32 v[104:105], v[104:105], v[152:153]\n\t"
"v_pk_add_f32 v[106:107], v[106:107], v[154:155]\n\t"
"v_pk_fma_f32 v[152:153], v[152:153], s[38:39], v[104:105]\n\t"
"v_pk_fma_f32 v[154:155], v[154:155], s[38:39], v[106:107]\n\t"
"v_pk_add_f32 v[120:121], v[120:121], v[168:169]\n\t"
"v_pk_add_f32 v[122:123], v[122:123], v[170:171]\n\t"
"v_pk_fma_f32 v[168:169], v[168:169], s[38:39], v[120:121]\n\t"
"v_pk_fma_f32 v[170:171], v[170:171], s[38:39], v[122:123]\n\t"
"v_pk_add_f32 v[136:137], v[136:137], v[184:185]\n\t"
"v_pk_add_f32 v[138:139], v[138:139], v[186:187]\n\t"
"v_pk_fma_f32 v[184:185], v[184:185], s[38:39], v[136:137]\n\t"
"v_pk_fma_f32 v[186:187], v[186:187], s[38:39], v[138:139]\n\t"
"v_pk_add_f32 v[140:141], v[140:141], v[188:189]\n\t"
"v_pk_add_f32 v[142:143], v[142:143], v[190:191]\n\t"
"v_pk_fma_f32 v[188:189], v[188:189], s[38:39], v[140:141]\n\t"
"v_pk_fma_f32 v[190:191], v[190:191], s[38:39], v[142:143]\n\t"
"v_pk_add_f32 v[124:125], v[124:125], v[172:173]\n\t"
"v_pk_add_f32 v[126:127], v[126:127], v[174:175]\n\t"
"v_pk_fma_f32 v[172:173], v[172:173], s[38:39], v[124:125]\n\t"
"v_pk_fma_f32 v[174:175], v[174:175], s[38:39], v[126:127]\n\t"
"v_pk_add_f32 v[108:109], v[108:109], v[156:157]\n\t"
"v_pk_add_f32 v[110:111], v[110:111], v[158:159]\n\t"
"v_pk_fma_f32 v[156:157], v[156:157], s[38:39], v[108:109]\n\t"
"v_pk_fma_f32 v[158:159], v[158:159], s[38:39], v[110:111]\n\t"
"v_mfma_f32_16x16x32_bf16 v[16:19], a[8:11], a[144:147], v[16:19]\n\t"
"v_cvt_pk_bf16_f32 v208, v208, v209\n\t"
"v_cvt_pk_bf16_f32 v209, v210, v211\n\t"
"v_mfma_f32_16x16x32_bf16 v[16:19], a[12:15], a[160:163], v[16:19]\n\t"
"ds_read_b64_tr_b16 a[180:181], v250 offset:0\n\t"
"v_mfma_f32_16x16x32_bf16 v[32:35], a[16:19], a[144:147], v[32:35]\n\t"
"ds_read_b128 a[144:147], v216 offset:4096\n\t"
"v_mfma_f32_16x16x32_bf16 v[32:35], a[20:23], a[160:163], v[32:35]\n\t"
"ds_write_b128 v217, v[200:203] offset:16384\n\t"
"v_mfma_f32_16x16x32_bf16 v[192:195], a[192:195], a[220:223], 0\n\t"
"v_cvt_pk_bf16_f32 v210, v212, v213\n\t"
"v_cvt_pk_bf16_f32 v211, v214, v215\n\t"
"s_waitcnt lgkmcnt(10)\n\t"
"v_mfma_f32_16x16x32_bf16 v[36:39], a[16:19], a[148:151], v[36:39]\n\t"
"ds_read_b128 a[160:163], v216 offset:12288\n\t"
"v_mfma_f32_16x16x32_bf16 v[36:39], a[20:23], a[164:167], v[36:39]\n\t"
"ds_write_b128 v219, v[208:211] offset:16384\n\t"
"v_mfma_f32_16x16x32_bf16 v[20:23], a[8:11], a[148:151], v[20:23]\n\t"
"ds_read_b64_tr_b16 a[182:183], v250 offset:512\n\t"
"v_mfma_f32_16x16x32_bf16 v[20:23], a[12:15], a[164:167], v[20:23]\n\t"
"v_cvt_pk_bf16_f32 v192, v192, v193\n\t"
"v_cvt_pk_bf16_f32 v193, v194, v195\n\t"
"v_mfma_f32_16x16x32_bf16 v[196:199], a[196:199], a[220:223], 0\n\t"
"ds_read_b64_tr_b16 a[184:185], v251 offset:2304\n\t"
"ds_read_b64_tr_b16 a[186:187], v251 offset:2816\n\t"
"v_mfma_f32_16x16x32_bf16 v[4:7], a[0:3], a[148:151], v[4:7]\n\t"
"ds_read_b128 a[148:151], v216 offset:5120\n\t"
"v_mfma_f32_16x16x32_bf16 v[4:7], a[4:7], a[164:167], v[4:7]\n\t"
"ds_read_b128 a[164:167], v216 offset:13312\n\t"
"ds_read_b64_tr_b16 a[188:189], v252 offset:2304\n\t"
"v_mfma_f32_16x16x32_bf16 v[200:203], a[200:203], a[220:223], 0\n\t"
"ds_read_b64_tr_b16 a[190:191], v252 offset:2816\n\t"
"ds_read_b64_tr_b16 a[192:193], v251 offset:0\n\t"
"s_waitcnt lgkmcnt(15)\n\t"
"v_mfma_f32_16x16x32_bf16 v[8:11], a[0:3], a[152:155], v[8:11]\n\t"
"v_cvt_pk_bf16_f32 v194, v196, v197\n\t"
"v_cvt_pk_bf16_f32 v195, v198, v199\n\t"
"v_mfma_f32_16x16x32_bf16 v[8:11], a[4:7], a[168:171], v[8:11]\n\t"
"ds_write_b128 v217, v[192:195] offset:24576\n\t"
"v_mfma_f32_16x16x32_bf16 v[24:27], a[8:11], a[152:155], v[24:27]\n\t"
"ds_read_b64_tr_b16 a[194:195], v251 offset:512\n\t"
"v_mfma_f32_16x16x32_bf16 v[24:27], a[12:15], a[168:171], v[24:27]\n\t"
"v_cvt_pk_bf16_f32 v200, v200, v201\n\t"
"v_cvt_pk_bf16_f32 v201, v202, v203\n\t"
"v_mfma_f32_16x16x32_bf16 v[204:207], a[204:207], a[220:223], 0\n\t"
"ds_read_b64_tr_b16 a[196:197], v252 offset:0\n\t"
"ds_read_b64_tr_b16 a[198:199], v252 offset:512\n\t"
"v_mfma_f32_16x16x32_bf16 v[40:43], a[16:19], a[152:155], v[40:43]\n\t"
"ds_read_b64_tr_b16 a[200:201], v249 offset:2304\n\t"
"v_mfma_f32_16x16x32_bf16 v[40:43], a[20:23], a[168:171], v[40:43]\n\t"
"ds_read_b64_tr_b16 a[202:203], v249 offset:2816\n\t"
"ds_read_b64_tr_b16 a[204:205], v250 offset:2304\n\t"
"s_waitcnt lgkmcnt(15)\n\t"
"v_mfma_f32_16x16x32_bf16 v[44:47], a[16:19], a[156:159], v[44:47]\n\t"
"ds_read_b64_tr_b16 a[206:207], v250 offset:2816\n\t"
"v_mfma_f32_16x16x32_bf16 v[44:47], a[20:23], a[172:175], v[44:47]\n\t"
"s_mov_b32 s20, 798720\n\t"
"buffer_load_dwordx4 a[16:19], v248, s[8:11], s20 offen offset:1024 sc1\n\t"
"v_mfma_f32_16x16x32_bf16 v[28:31], a[8:11], a[156:159], v[28:31]\n\t"
"v_cvt_pk_bf16_f32 v202, v204, v205\n\t"
"v_cvt_pk_bf16_f32 v203, v206, v207\n\t"
"v_mfma_f32_16x16x32_bf16 v[28:31], a[12:15], a[172:175], v[28:31]\n\t"
"ds_read_b128 a[152:155], v216 offset:6144\n\t"
"v_mfma_f32_16x16x32_bf16 v[12:15], a[0:3], a[156:159], v[12:15]\n\t"
"ds_read_b128 a[168:171], v216 offset:14336\n\t"
"v_mfma_f32_16x16x32_bf16 v[12:15], a[4:7], a[172:175], v[12:15]\n\t"
"buffer_load_dwordx4 a[20:23], v248, s[8:11], s20 offen offset:2048 sc1\n\t"
"s_waitcnt vmcnt(26)\n\t"
"v_mfma_f32_16x16x32_bf16 v[96:99], a[24:27], a[144:147], v[96:99]\n\t"
"ds_read_b128 a[156:159], v216 offset:7168\n\t"
"v_mfma_f32_16x16x32_bf16 v[96:99], a[28:31], a[160:163], v[96:99]\n\t"
"buffer_load_dwordx4 a[12:15], v248, s[8:11], s20 offen offset:0 sc1\n\t"
"ds_write_b128 v219, v[200:203] offset:24576\n\t"
"s_mov_b32 s20, 786432\n\t"
"buffer_load_dwordx4 a[8:11], v248, s[8:11], s20 offen offset:2048 sc1\n\t"
"v_mfma_f32_16x16x32_bf16 v[112:115], a[32:35], a[144:147], v[112:115]\n\t"
"v_mfma_f32_16x16x32_bf16 v[112:115], a[36:39], a[160:163], v[112:115]\n\t"
"buffer_load_dwordx4 a[0:3], v248, s[8:11], s20 offen offset:0 sc1\n\t"
"buffer_load_dwordx4 a[4:7], v248, s[8:11], s20 offen offset:1024 sc1\n\t"
"v_mfma_f32_16x16x32_bf16 v[128:131], a[40:43], a[144:147], v[128:131]\n\t"
"v_mfma_f32_16x16x32_bf16 v[128:131], a[44:47], a[160:163], v[128:131]\n\t"
"ds_read_b128 a[172:175], v216 offset:15360\n\t"
"ds_write_b128 v223, a[240:243] offset:41472\n\t"
"s_waitcnt lgkmcnt(15)\n\t"
"v_mfma_f32_16x16x32_bf16 v[132:135], a[40:43], a[148:151], v[132:135]\n\t"
"v_mfma_f32_16x16x32_bf16 v[132:135], a[44:47], a[164:167], v[132:135]\n\t"
"v_mfma_f32_16x16x32_bf16 v[116:119], a[32:35], a[148:151], v[116:119]\n\t"
"v_mfma_f32_16x16x32_bf16 v[116:119], a[36:39], a[164:167], v[116:119]\n\t"
"v_mfma_f32_16x16x32_bf16 v[100:103], a[24:27], a[148:151], v[100:103]\n\t"
"v_mfma_f32_16x16x32_bf16 v[100:103], a[28:31], a[164:167], v[100:103]\n\t"
"s_waitcnt lgkmcnt(0)\n\t"
"s_barrier\n\t"
"v_mfma_f32_16x16x32_bf16 v[104:107], a[24:27], a[152:155], v[104:107]\n\t"
"ds_read_b128 a[144:147], v216 offset:16384\n\t"
"v_add_u32 v249, 39424, v227\n\t"
"v_mfma_f32_16x16x32_bf16 v[104:107], a[28:31], a[168:171], v[104:107]\n\t"
"ds_read_b128 a[160:163], v216 offset:24576\n\t"
"ds_read_b128 a[148:151], v216 offset:17408\n\t"
"s_add_u32 m0, s26, 124544\n\t"
"s_nop 0\n\t"
"buffer_load_dwordx4 v236, s[12:15], s22 offen offset:128 nt lds\n\t"
"v_mfma_f32_16x16x32_bf16 v[120:123], a[32:35], a[152:155], v[120:123]\n\t"
"ds_read_b128 a[164:167], v216 offset:25600\n\t"
"v_mfma_f32_16x16x32_bf16 v[120:123], a[36:39], a[168:171], v[120:123]\n\t"
"v_xor_b32 v250, 32, v249\n\t"
"v_mfma_f32_16x16x32_bf16 v[136:139], a[40:43], a[152:155], v[136:139]\n\t"
"v_xor_b32 v251, 64, v249\n\t"
"v_mfma_f32_16x16x32_bf16 v[136:139], a[44:47], a[168:171], v[136:139]\n\t"
"v_xor_b32 v252, 96, v249\n\t"
"ds_read_b128 a[152:155], v216 offset:18432\n\t"
"v_mfma_f32_16x16x32_bf16 v[208:211], a[176:179], a[220:223], 0\n\t"
"s_add_u32 m0, s26, 125568\n\t"
"s_nop 0\n\t"
"buffer_load_dwordx4 v237, s[12:15], s22 offen offset:128 nt lds\n\t"
"v_mfma_f32_16x16x32_bf16 v[140:143], a[40:43], a[156:159], v[140:143]\n\t"
"ds_read_b128 a[168:171], v216 offset:26624\n\t"
"v_mfma_f32_16x16x32_bf16 v[140:143], a[44:47], a[172:175], v[140:143]\n\t"
"ds_read_b64_tr_b16 a[176:177], v249 offset:0\n\t"
"v_mfma_f32_16x16x32_bf16 v[212:215], a[180:183], a[220:223], 0\n\t"
"ds_read_b64_tr_b16 a[178:179], v249 offset:512\n\t"
"ds_read_b64_tr_b16 a[180:181], v250 offset:0\n\t"
"v_mfma_f32_16x16x32_bf16 v[124:127], a[32:35], a[156:159], v[124:127]\n\t"
"v_cvt_pk_bf16_f32 v208, v208, v209\n\t"
"v_mfma_f32_16x16x32_bf16 v[124:127], a[36:39], a[172:175], v[124:127]\n\t"
"s_add_u32 m0, s26, 126592\n\t"
"s_nop 0\n\t"
"buffer_load_dwordx4 v238, s[12:15], s22 offen offset:128 nt lds\n\t"
"v_mfma_f32_16x16x32_bf16 v[108:111], a[24:27], a[156:159], v[108:111]\n\t"
"v_cvt_pk_bf16_f32 v209, v210, v211\n\t"
"v_mfma_f32_16x16x32_bf16 v[108:111], a[28:31], a[172:175], v[108:111]\n\t"
"v_cvt_pk_bf16_f32 v210, v212, v213\n\t"
"v_cvt_pk_bf16_f32 v211, v214, v215\n\t"
"v_mfma_f32_16x16x32_bf16 v[192:195], a[184:187], a[220:223], 0\n\t"
"ds_read_b128 a[156:159], v216 offset:19456\n\t"
"s_waitcnt vmcnt(27) lgkmcnt(8)\n\t"
"v_mfma_f32_16x16x32_bf16 v[48:51], a[48:51], a[144:147], v[48:51]\n\t"
"ds_read_b128 a[172:175], v216 offset:27648\n\t"
"v_mfma_f32_16x16x32_bf16 v[48:51], a[52:55], a[160:163], v[48:51]\n\t"
"ds_write_b128 v217, v[208:211] offset:0\n\t"
"v_mfma_f32_16x16x32_bf16 v[196:199], a[188:191], a[220:223], 0\n\t"
"s_add_u32 m0, s26, 127616\n\t"
"s_nop 0\n\t"
"buffer_load_dwordx4 v239, s[12:15], s22 offen offset:128 nt lds\n\t"
"v_mfma_f32_16x16x32_bf16 v[64:67], a[56:59], a[144:147], v[64:67]\n\t"
"v_cvt_pk_bf16_f32 v192, v192, v193\n\t"
"v_mfma_f32_16x16x32_bf16 v[64:67], a[60:63], a[160:163], v[64:67]\n\t"
"v_cvt_pk_bf16_f32 v193, v194, v195\n\t"
"ds_read_b64_tr_b16 a[182:183], v250 offset:512\n\t"
"v_mfma_f32_16x16x32_bf16 v[80:83], a[64:67], a[144:147], v[80:83]\n\t"
"ds_read_b128 a[144:147], v216 offset:20480\n\t"
"v_mfma_f32_16x16x32_bf16 v[80:83], a[68:71], a[160:163], v[80:83]\n\t"
"v_cvt_pk_bf16_f32 v194, v196, v197\n\t"
"v_cvt_pk_bf16_f32 v195, v198, v199\n\t"
"v_mfma_f32_16x16x32_bf16 v[200:203], a[192:195], a[220:223], 0\n\t"
"s_add_u32 m0, s26, 128640\n\t"
"s_mov_b64 exec, s[36:37]\n\t"
"buffer_load_dwordx4 v240, s[12:15], s22 offen offset:128 nt lds\n\t"
"s_mov_b64 exec, -1\n\t"
"s_waitcnt lgkmcnt(10)\n\t"
"v_mfma_f32_16x16x32_bf16 v[84:87], a[64:67], a[148:151], v[84:87]\n\t"
"ds_read_b128 a[160:163], v216 offset:28672\n\t"
"v_mfma_f32_16x16x32_bf16 v[84:87], a[68:71], a[164:167], v[84:87]\n\t"
"ds_read_b64_tr_b16 a[184:185], v251 offset:0\n\t"
"v_mfma_f32_16x16x32_bf16 v[68:71], a[56:59], a[148:151], v[68:71]\n\t"
"ds_read_b64_tr_b16 a[186:187], v251 offset:512\n\t"
"v_mfma_f32_16x16x32_bf16 v[68:71], a[60:63], a[164:167], v[68:71]\n\t"
"ds_write_b128 v219, v[192:195] offset:0\n\t"
"v_mfma_f32_16x16x32_bf16 v[204:207], a[196:199], a[220:223], 0\n\t"
"v_cvt_pk_bf16_f32 v200, v200, v201\n\t"
"v_cvt_pk_bf16_f32 v201, v202, v203\n\t"
"v_mfma_f32_16x16x32_bf16 v[52:55], a[48:51], a[148:151], v[52:55]\n\t"
"ds_read_b128 a[148:151], v216 offset:21504\n\t"
"v_mfma_f32_16x16x32_bf16 v[52:55], a[52:55], a[164:167], v[52:55]\n\t"
"s_add_u32 m0, s26, 145280\n\t"
"s_nop 0\n\t"
"buffer_load_dwordx4 v241, s[12:15], s22 offen offset:128 nt lds\n\t"
"v_mfma_f32_16x16x32_bf16 v[208:211], a[200:203], a[220:223], 0\n\t"
"ds_read_b128 a[164:167], v216 offset:29696\n\t"
"s_waitcnt lgkmcnt(14)\n\t"
"v_mfma_f32_16x16x32_bf16 v[56:59], a[48:51], a[152:155], v[56:59]\n\t"
"v_cvt_pk_bf16_f32 v202, v204, v205\n\t"
"v_cvt_pk_bf16_f32 v203, v206, v207\n\t"
"v_mfma_f32_16x16x32_bf16 v[56:59], a[52:55], a[168:171], v[56:59]\n\t"
"ds_read_b64_tr_b16 a[188:189], v252 offset:0\n\t"
"v_mfma_f32_16x16x32_bf16 v[72:75], a[56:59], a[152:155], v[72:75]\n\t"
"ds_read_b64_tr_b16 a[190:191], v252 offset:512\n\t"
"v_mfma_f32_16x16x32_bf16 v[72:75], a[60:63], a[168:171], v[72:75]\n\t"
"s_add_u32 m0, s26, 146304\n\t"
"s_nop 0\n\t"
"buffer_load_dwordx4 v242, s[12:15], s22 offen offset:128 nt lds\n\t"
"v_mfma_f32_16x16x32_bf16 v[212:215], a[204:207], a[220:223], 0\n\t"
"v_cvt_pk_bf16_f32 v208, v208, v209\n\t"
"v_cvt_pk_bf16_f32 v209, v210, v211\n\t"
"v_mfma_f32_16x16x32_bf16 v[88:91], a[64:67], a[152:155], v[88:91]\n\t"
"ds_read_b64_tr_b16 a[192:193], v249 offset:1152\n\t"
"v_mfma_f32_16x16x32_bf16 v[88:91], a[68:71], a[168:171], v[88:91]\n\t"
"ds_write_b128 v217, v[200:203] offset:8192\n\t"
"s_waitcnt lgkmcnt(13)\n\t"
"v_mfma_f32_16x16x32_bf16 v[92:95], a[64:67], a[156:159], v[92:95]\n\t"
"ds_read_b64_tr_b16 a[194:195], v249 offset:1664\n\t"
"v_mfma_f32_16x16x32_bf16 v[92:95], a[68:71], a[172:175], v[92:95]\n\t"
"s_add_u32 m0, s26, 147328\n\t"
"s_nop 0\n\t"
"buffer_load_dwordx4 v243, s[12:15], s22 offen offset:128 nt lds\n\t"
"v_mfma_f32_16x16x32_bf16 v[76:79], a[56:59], a[156:159], v[76:79]\n\t"
"ds_read_b64_tr_b16 a[196:197], v250 offset:1152\n\t"
"v_mfma_f32_16x16x32_bf16 v[76:79], a[60:63], a[172:175], v[76:79]\n\t"
"ds_read_b64_tr_b16 a[198:199], v250 offset:1664\n\t"
"ds_read_b64_tr_b16 a[200:201], v251 offset:1152\n\t"
"v_mfma_f32_16x16x32_bf16 v[60:63], a[48:51], a[156:159], v[60:63]\n\t"
"ds_read_b64_tr_b16 a[202:203], v251 offset:1664\n\t"
"v_mfma_f32_16x16x32_bf16 v[60:63], a[52:55], a[172:175], v[60:63]\n\t"
"ds_read_b64_tr_b16 a[204:205], v252 offset:1152\n\t"
"ds_read_b64_tr_b16 a[206:207], v252 offset:1664\n\t"
"s_waitcnt vmcnt(26) lgkmcnt(15)\n\t"
"v_mfma_f32_16x16x32_bf16 v[144:147], a[72:75], a[144:147], v[144:147]\n\t"
"v_cvt_pk_bf16_f32 v210, v212, v213\n\t"
"v_mfma_f32_16x16x32_bf16 v[144:147], a[76:79], a[160:163], v[144:147]\n\t"
"s_add_u32 m0, s26, 148352\n\t"
"s_nop 0\n\t"
"buffer_load_dwordx4 v244, s[12:15], s22 offen offset:128 nt lds\n\t"
"v_cvt_pk_bf16_f32 v211, v214, v215\n\t"
"v_mfma_f32_16x16x32_bf16 v[160:163], a[80:83], a[144:147], v[160:163]\n\t"
"v_mfma_f32_16x16x32_bf16 v[160:163], a[84:87], a[160:163], v[160:163]\n\t"
"ds_write_b128 v219, v[208:211] offset:8192\n\t"
"v_mfma_f32_16x16x32_bf16 v[176:179], a[88:91], a[144:147], v[176:179]\n\t"
"ds_read_b128 a[152:155], v216 offset:22528\n\t"
"v_mfma_f32_16x16x32_bf16 v[176:179], a[92:95], a[160:163], v[176:179]\n\t"
"ds_read_b128 a[168:171], v216 offset:30720\n\t"
"ds_read_b128 a[156:159], v216 offset:23552\n\t"
"ds_read_b128 a[172:175], v216 offset:31744\n\t"
"s_add_u32 m0, s26, 149376\n\t"
"s_mov_b64 exec, s[36:37]\n\t"
"buffer_load_dwordx4 v245, s[12:15], s22 offen offset:128 nt lds\n\t"
"s_mov_b64 exec, -1\n\t"
"s_waitcnt lgkmcnt(15)\n\t"
"v_mfma_f32_16x16x32_bf16 v[180:183], a[88:91], a[148:151], v[180:183]\n\t"
"v_mfma_f32_16x16x32_bf16 v[180:183], a[92:95], a[164:167], v[180:183]\n\t"
"v_mfma_f32_16x16x32_bf16 v[164:167], a[80:83], a[148:151], v[164:167]\n\t"
"v_mfma_f32_16x16x32_bf16 v[164:167], a[84:87], a[164:167], v[164:167]\n\t"
"v_mfma_f32_16x16x32_bf16 v[148:151], a[72:75], a[148:151], v[148:151]\n\t"
"v_mfma_f32_16x16x32_bf16 v[148:151], a[76:79], a[164:167], v[148:151]\n\t"
"s_waitcnt lgkmcnt(0)\n\t"
"s_barrier\n\t"
"v_mfma_f32_16x16x32_bf16 v[152:155], a[72:75], a[152:155], v[152:155]\n\t"
"ds_read_b128 a[144:147], v216 offset:0\n\t"
"v_add_u32 v249, 39424, v226\n\t"
"v_mfma_f32_16x16x32_bf16 v[152:155], a[76:79], a[168:171], v[152:155]\n\t"
"ds_read_b128 a[160:163], v216 offset:8192\n\t"
"ds_read_b128 a[148:151], v216 offset:1024\n\t"
"v_mfma_f32_16x16x32_bf16 v[168:171], a[80:83], a[152:155], v[168:171]\n\t"
"ds_read_b128 a[164:167], v216 offset:9216\n\t"
"v_mfma_f32_16x16x32_bf16 v[168:171], a[84:87], a[168:171], v[168:171]\n\t"
"v_xor_b32 v250, 32, v249\n\t"
"v_mfma_f32_16x16x32_bf16 v[184:187], a[88:91], a[152:155], v[184:187]\n\t"
"v_xor_b32 v251, 64, v249\n\t"
"v_mfma_f32_16x16x32_bf16 v[184:187], a[92:95], a[168:171], v[184:187]\n\t"
"v_xor_b32 v252, 96, v249\n\t"
"ds_read_b128 a[248:251], v222 offset:18432\n\t"
"v_mfma_f32_16x16x32_bf16 v[192:195], a[176:179], a[224:227], 0\n\t"
"ds_read_b128 a[152:155], v216 offset:2048\n\t"
"v_mfma_f32_16x16x32_bf16 v[188:191], a[88:91], a[156:159], v[188:191]\n\t"
"ds_read_b128 a[168:171], v216 offset:10240\n\t"
"v_mfma_f32_16x16x32_bf16 v[188:191], a[92:95], a[172:175], v[188:191]\n\t"
"ds_read_b64_tr_b16 a[176:177], v251 offset:20736\n\t"
"v_mfma_f32_16x16x32_bf16 v[196:199], a[180:183], a[224:227], 0\n\t"
"ds_read_b64_tr_b16 a[178:179], v251 offset:21248\n\t"
"v_mfma_f32_16x16x32_bf16 v[172:175], a[80:83], a[156:159], v[172:175]\n\t"
"v_cvt_pk_bf16_f32 v192, v192, v193\n\t"
"v_cvt_pk_bf16_f32 v193, v194, v195\n\t"
"v_mfma_f32_16x16x32_bf16 v[172:175], a[84:87], a[172:175], v[172:175]\n\t"
"ds_read_b64_tr_b16 a[180:181], v252 offset:20736\n\t"
"v_mfma_f32_16x16x32_bf16 v[156:159], a[72:75], a[156:159], v[156:159]\n\t"
"ds_read_b128 a[156:159], v216 offset:3072\n\t"
"v_mfma_f32_16x16x32_bf16 v[156:159], a[76:79], a[172:175], v[156:159]\n\t"
"v_cvt_pk_bf16_f32 v194, v196, v197\n\t"
"v_mfma_f32_16x16x32_bf16 v[200:203], a[184:187], a[224:227], 0\n\t"
"v_cvt_pk_bf16_f32 v195, v198, v199\n\t"
"s_waitcnt vmcnt(22) lgkmcnt(9)\n\t"
"v_mfma_f32_16x16x32_bf16 v[0:3], a[96:99], a[144:147], v[0:3]\n\t"
"ds_read_b128 a[172:175], v216 offset:11264\n\t"
"v_mfma_f32_16x16x32_bf16 v[0:3], a[100:103], a[160:163], v[0:3]\n\t"
"ds_write_b128 v221, v[192:195] offset:16384\n\t"
"v_mfma_f32_16x16x32_bf16 v[204:207], a[188:191], a[224:227], 0\n\t"
"ds_read_b64_tr_b16 a[182:183], v252 offset:21248\n\t"
"v_mfma_f32_16x16x32_bf16 v[16:19], a[104:107], a[144:147], v[16:19]\n\t"
"v_cvt_pk_bf16_f32 v200, v200, v201\n\t"
"v_mfma_f32_16x16x32_bf16 v[16:19], a[108:111], a[160:163], v[16:19]\n\t"
"v_cvt_pk_bf16_f32 v201, v202, v203\n\t"
"v_mfma_f32_16x16x32_bf16 v[32:35], a[112:115], a[144:147], v[32:35]\n\t"
"ds_read_b128 a[144:147], v216 offset:4096\n\t"
"v_mfma_f32_16x16x32_bf16 v[32:35], a[116:119], a[160:163], v[32:35]\n\t"
"v_cvt_pk_bf16_f32 v202, v204, v205\n\t"
"v_cvt_pk_bf16_f32 v203, v206, v207\n\t"
"v_mfma_f32_16x16x32_bf16 v[208:211], a[192:195], a[224:227], 0\n\t"
"ds_read_b128 a[160:163], v216 offset:12288\n\t"
"s_waitcnt lgkmcnt(12)\n\t"
"v_mfma_f32_16x16x32_bf16 v[36:39], a[112:115], a[148:151], v[36:39]\n\t"
"ds_read_b64_tr_b16 a[184:185], v249 offset:23040\n\t"
"v_mfma_f32_16x16x32_bf16 v[36:39], a[116:119], a[164:167], v[36:39]\n\t"
"ds_read_b64_tr_b16 a[186:187], v249 offset:23552\n\t"
"v_mfma_f32_16x16x32_bf16 v[20:23], a[104:107], a[148:151], v[20:23]\n\t"
"ds_read_b64_tr_b16 a[188:189], v250 offset:23040\n\t"
"v_mfma_f32_16x16x32_bf16 v[20:23], a[108:111], a[164:167], v[20:23]\n\t"
"ds_write_b128 v221, v[200:203] offset:24576\n\t"
"v_mfma_f32_16x16x32_bf16 v[212:215], a[196:199], a[224:227], 0\n\t"
"v_cvt_pk_bf16_f32 v208, v208, v209\n\t"
"v_mfma_f32_16x16x32_bf16 v[4:7], a[96:99], a[148:151], v[4:7]\n\t"
"v_cvt_pk_bf16_f32 v209, v210, v211\n\t"
"v_mfma_f32_16x16x32_bf16 v[4:7], a[100:103], a[164:167], v[4:7]\n\t"
"ds_read_b128 a[148:151], v216 offset:5120\n\t"
"v_mfma_f32_16x16x32_bf16 v[192:195], a[200:203], a[224:227], 0\n\t"
"ds_read_b128 a[164:167], v216 offset:13312\n\t"
"s_waitcnt lgkmcnt(15)\n\t"
"v_mfma_f32_16x16x32_bf16 v[8:11], a[96:99], a[152:155], v[8:11]\n\t"
"v_cvt_pk_bf16_f32 v210, v212, v213\n\t"
"v_cvt_pk_bf16_f32 v211, v214, v215\n\t"
"v_mfma_f32_16x16x32_bf16 v[8:11], a[100:103], a[168:171], v[8:11]\n\t"
"s_mov_b32 s20, 1093632\n\t"
"buffer_load_dwordx4 a[40:43], v248, s[8:11], s20 offen offset:1024 sc1\n\t"
"v_mfma_f32_16x16x32_bf16 v[24:27], a[104:107], a[152:155], v[24:27]\n\t"
"v_mfma_f32_16x16x32_bf16 v[24:27], a[108:111], a[168:171], v[24:27]\n\t"
"v_cvt_pk_bf16_f32 v192, v192, v193\n\t"
"v_mfma_f32_16x16x32_bf16 v[196:199], a[204:207], a[224:227], 0\n\t"
"v_cvt_pk_bf16_f32 v193, v194, v195\n\t"
"v_mfma_f32_16x16x32_bf16 v[40:43], a[112:115], a[152:155], v[40:43]\n\t"
"ds_read_b64_tr_b16 a[190:191], v250 offset:23552\n\t"
"v_mfma_f32_16x16x32_bf16 v[40:43], a[116:119], a[168:171], v[40:43]\n\t"
"ds_write_b128 v221, v[208:211] offset:20480\n\t"
"s_waitcnt lgkmcnt(12)\n\t"
"v_mfma_f32_16x16x32_bf16 v[44:47], a[112:115], a[156:159], v[44:47]\n\t"
"ds_read_b64_tr_b16 a[192:193], v249 offset:20736\n\t"
"v_mfma_f32_16x16x32_bf16 v[44:47], a[116:119], a[172:175], v[44:47]\n\t"
"ds_read_b64_tr_b16 a[194:195], v249 offset:21248\n\t"
"v_mfma_f32_16x16x32_bf16 v[28:31], a[104:107], a[156:159], v[28:31]\n\t"
"ds_read_b64_tr_b16 a[196:197], v250 offset:20736\n\t"
"v_mfma_f32_16x16x32_bf16 v[28:31], a[108:111], a[172:175], v[28:31]\n\t"
"ds_read_b64_tr_b16 a[198:199], v250 offset:21248\n\t"
"v_mfma_f32_16x16x32_bf16 v[12:15], a[96:99], a[156:159], v[12:15]\n\t"
"ds_read_b64_tr_b16 a[200:201], v251 offset:23040\n\t"
"v_cvt_pk_bf16_f32 v194, v196, v197\n\t"
"v_mfma_f32_16x16x32_bf16 v[12:15], a[100:103], a[172:175], v[12:15]\n\t"
"ds_read_b64_tr_b16 a[202:203], v251 offset:23552\n\t"
"s_waitcnt vmcnt(17) lgkmcnt(14)\n\t"
"v_mfma_f32_16x16x32_bf16 v[96:99], a[120:123], a[144:147], v[96:99]\n\t"
"ds_read_b64_tr_b16 a[204:205], v252 offset:23040\n\t"
"v_mfma_f32_16x16x32_bf16 v[96:99], a[124:127], a[160:163], v[96:99]\n\t"
"ds_read_b64_tr_b16 a[206:207], v252 offset:23552\n\t"
"v_cvt_pk_bf16_f32 v195, v198, v199\n\t"
"v_mfma_f32_16x16x32_bf16 v[112:115], a[128:131], a[144:147], v[112:115]\n\t"
"v_mfma_f32_16x16x32_bf16 v[112:115], a[132:135], a[160:163], v[112:115]\n\t"
"ds_write_b128 v221, v[192:195] offset:28672\n\t"
"v_mfma_f32_16x16x32_bf16 v[128:131], a[136:139], a[144:147], v[128:131]\n\t"
"ds_read_b128 a[152:155], v216 offset:6144\n\t"
"v_mfma_f32_16x16x32_bf16 v[128:131], a[140:143], a[160:163], v[128:131]\n\t"
"ds_read_b128 a[168:171], v216 offset:14336\n\t"
"ds_read_b128 a[156:159], v216 offset:7168\n\t"
"ds_read_b128 a[172:175], v216 offset:15360\n\t"
"s_waitcnt lgkmcnt(15)\n\t"
"v_mfma_f32_16x16x32_bf16 v[132:135], a[136:139], a[148:151], v[132:135]\n\t"
"v_mfma_f32_16x16x32_bf16 v[132:135], a[140:143], a[164:167], v[132:135]\n\t"
"v_mfma_f32_16x16x32_bf16 v[116:119], a[128:131], a[148:151], v[116:119]\n\t"
"v_mfma_f32_16x16x32_bf16 v[116:119], a[132:135], a[164:167], v[116:119]\n\t"
"v_mfma_f32_16x16x32_bf16 v[100:103], a[120:123], a[148:151], v[100:103]\n\t"
"v_mfma_f32_16x16x32_bf16 v[100:103], a[124:127], a[164:167], v[100:103]\n\t"
"s_waitcnt lgkmcnt(0)\n\t"
"s_barrier\n\t"
"v_mfma_f32_16x16x32_bf16 v[104:107], a[120:123], a[152:155], v[104:107]\n\t"
"ds_read_b128 a[144:147], v216 offset:16384\n\t"
"v_add_u32 v249, 39424, v227\n\t"
"v_mfma_f32_16x16x32_bf16 v[104:107], a[124:127], a[168:171], v[104:107]\n\t"
"ds_read_b128 a[160:163], v216 offset:24576\n\t"
"ds_read_b128 a[148:151], v216 offset:17408\n\t"
"v_mfma_f32_16x16x32_bf16 v[120:123], a[128:131], a[152:155], v[120:123]\n\t"
"ds_read_b128 a[164:167], v216 offset:25600\n\t"
"v_xor_b32 v250, 32, v249\n\t"
"v_mfma_f32_16x16x32_bf16 v[120:123], a[132:135], a[168:171], v[120:123]\n\t"
"buffer_load_dwordx4 a[44:47], v248, s[8:11], s20 offen offset:2048 sc1\n\t"
"v_mfma_f32_16x16x32_bf16 v[136:139], a[136:139], a[152:155], v[136:139]\n\t"
"v_xor_b32 v251, 64, v249\n\t"
"v_xor_b32 v252, 96, v249\n\t"
"v_mfma_f32_16x16x32_bf16 v[136:139], a[140:143], a[168:171], v[136:139]\n\t"
"buffer_load_dwordx4 a[36:39], v248, s[8:11], s20 offen offset:0 sc1\n\t"
"v_mfma_f32_16x16x32_bf16 v[200:203], a[176:179], a[220:223], 0\n\t"
"s_mov_b32 s20, 1081344\n\t"
"buffer_load_dwordx4 a[32:35], v248, s[8:11], s20 offen offset:2048 sc1\n\t"
"v_mfma_f32_16x16x32_bf16 v[140:143], a[136:139], a[156:159], v[140:143]\n\t"
"ds_read_b128 a[152:155], v216 offset:18432\n\t"
"v_mfma_f32_16x16x32_bf16 v[140:143], a[140:143], a[172:175], v[140:143]\n\t"
"buffer_load_dwordx4 a[24:27], v248, s[8:11], s20 offen offset:0 sc1\n\t"
"buffer_load_dwordx4 a[28:31], v248, s[8:11], s20 offen offset:1024 sc1\n\t"
"v_mfma_f32_16x16x32_bf16 v[204:207], a[180:183], a[220:223], 0\n\t"
"ds_read_b128 a[168:171], v216 offset:26624\n\t"
"ds_read_b64_tr_b16 a[176:177], v251 offset:20736\n\t"
"v_mfma_f32_16x16x32_bf16 v[124:127], a[128:131], a[156:159], v[124:127]\n\t"
"v_cvt_pk_bf16_f32 v200, v200, v201\n\t"
"v_cvt_pk_bf16_f32 v201, v202, v203\n\t"
"v_mfma_f32_16x16x32_bf16 v[124:127], a[132:135], a[172:175], v[124:127]\n\t"
"s_mov_b32 s20, 970752\n\t"
"buffer_load_dwordx4 a[64:67], v248, s[8:11], s20 offen offset:1024 sc1\n\t"
"v_mfma_f32_16x16x32_bf16 v[108:111], a[120:123], a[156:159], v[108:111]\n\t"
"ds_read_b128 a[156:159], v216 offset:19456\n\t"
"v_mfma_f32_16x16x32_bf16 v[108:111], a[124:127], a[172:175], v[108:111]\n\t"
"buffer_load_dwordx4 a[68:71], v248, s[8:11], s20 offen offset:2048 sc1\n\t"
"v_cvt_pk_bf16_f32 v202, v204, v205\n\t"
"v_cvt_pk_bf16_f32 v203, v206, v207\n\t"
"v_mfma_f32_16x16x32_bf16 v[208:211], a[184:187], a[220:223], 0\n\t"
"v_pk_add_f32 v[0:1], v[0:1], v[96:97]\n\t"
"v_pk_add_f32 v[2:3], v[2:3], v[98:99]\n\t"
"v_pk_fma_f32 v[96:97], v[96:97], s[38:39], v[0:1]\n\t"
"v_pk_fma_f32 v[98:99], v[98:99], s[38:39], v[2:3]\n\t"
"v_pk_add_f32 v[16:17], v[16:17], v[112:113]\n\t"
"v_pk_add_f32 v[18:19], v[18:19], v[114:115]\n\t"
"v_pk_fma_f32 v[112:113], v[112:113], s[38:39], v[16:17]\n\t"
"v_pk_fma_f32 v[114:115], v[114:115], s[38:39], v[18:19]\n\t"
"v_pk_add_f32 v[32:33], v[32:33], v[128:129]\n\t"
"v_pk_add_f32 v[34:35], v[34:35], v[130:131]\n\t"
"v_pk_fma_f32 v[128:129], v[128:129], s[38:39], v[32:33]\n\t"
"v_pk_fma_f32 v[130:131], v[130:131], s[38:39], v[34:35]\n\t"
"v_pk_add_f32 v[36:37], v[36:37], v[132:133]\n\t"
"v_pk_add_f32 v[38:39], v[38:39], v[134:135]\n\t"
"v_pk_fma_f32 v[132:133], v[132:133], s[38:39], v[36:37]\n\t"
"v_pk_fma_f32 v[134:135], v[134:135], s[38:39], v[38:39]\n\t"
"v_pk_add_f32 v[20:21], v[20:21], v[116:117]\n\t"
"v_pk_add_f32 v[22:23], v[22:23], v[118:119]\n\t"
"v_pk_fma_f32 v[116:117], v[116:117], s[38:39], v[20:21]\n\t"
"v_pk_fma_f32 v[118:119], v[118:119], s[38:39], v[22:23]\n\t"
"v_pk_add_f32 v[4:5], v[4:5], v[100:101]\n\t"
"v_pk_add_f32 v[6:7], v[6:7], v[102:103]\n\t"
"v_pk_fma_f32 v[100:101], v[100:101], s[38:39], v[4:5]\n\t"
"v_pk_fma_f32 v[102:103], v[102:103], s[38:39], v[6:7]\n\t"
"v_pk_add_f32 v[8:9], v[8:9], v[104:105]\n\t"
"v_pk_add_f32 v[10:11], v[10:11], v[106:107]\n\t"
"v_pk_fma_f32 v[104:105], v[104:105], s[38:39], v[8:9]\n\t"
"v_pk_fma_f32 v[106:107], v[106:107], s[38:39], v[10:11]\n\t"
"v_pk_add_f32 v[24:25], v[24:25], v[120:121]\n\t"
"v_pk_add_f32 v[26:27], v[26:27], v[122:123]\n\t"
"v_pk_fma_f32 v[120:121], v[120:121], s[38:39], v[24:25]\n\t"
"v_pk_fma_f32 v[122:123], v[122:123], s[38:39], v[26:27]\n\t"
"v_pk_add_f32 v[40:41], v[40:41], v[136:137]\n\t"
"v_pk_add_f32 v[42:43], v[42:43], v[138:139]\n\t"
"v_pk_fma_f32 v[136:137], v[136:137], s[38:39], v[40:41]\n\t"
"v_pk_fma_f32 v[138:139], v[138:139], s[38:39], v[42:43]\n\t"
"v_pk_add_f32 v[44:45], v[44:45], v[140:141]\n\t"
"v_pk_add_f32 v[46:47], v[46:47], v[142:143]\n\t"
"v_pk_fma_f32 v[140:141], v[140:141], s[38:39], v[44:45]\n\t"
"v_pk_fma_f32 v[142:143], v[142:143], s[38:39], v[46:47]\n\t"
"v_pk_add_f32 v[28:29], v[28:29], v[124:125]\n\t"
"v_pk_add_f32 v[30:31], v[30:31], v[126:127]\n\t"
"v_pk_fma_f32 v[124:125], v[124:125], s[38:39], v[28:29]\n\t"
"v_pk_fma_f32 v[126:127], v[126:127], s[38:39], v[30:31]\n\t"
"v_pk_add_f32 v[12:13], v[12:13], v[108:109]\n\t"
"v_pk_add_f32 v[14:15], v[14:15], v[110:111]\n\t"
"v_pk_fma_f32 v[108:109], v[108:109], s[38:39], v[12:13]\n\t"
"v_pk_fma_f32 v[110:111], v[110:111], s[38:39], v[14:15]\n\t"
"buffer_load_dwordx4 a[60:63], v248, s[8:11], s20 offen offset:0 sc1\n\t"
"ds_read_b128 a[172:175], v216 offset:27648\n\t"
"ds_read_b64_tr_b16 a[178:179], v251 offset:21248\n\t"
"s_waitcnt vmcnt(19) lgkmcnt(8)\n\t"
"v_mfma_f32_16x16x32_bf16 v[0:3], a[0:3], a[144:147], v[0:3]\n\t"
"ds_read_b64_tr_b16 a[180:181], v252 offset:20736\n\t"
"v_mfma_f32_16x16x32_bf16 v[0:3], a[4:7], a[160:163], v[0:3]\n\t"
"s_mov_b32 s20, 958464\n\t"
"buffer_load_dwordx4 a[56:59], v248, s[8:11], s20 offen offset:2048 sc1\n\t"
"buffer_load_dwordx4 a[48:51], v248, s[8:11], s20 offen offset:0 sc1\n\t"
"v_mfma_f32_16x16x32_bf16 v[212:215], a[188:191], a[220:223], 0\n\t"
"buffer_load_dwordx4 a[52:55], v248, s[8:11], s20 offen offset:1024 sc1\n\t"
"v_mfma_f32_16x16x32_bf16 v[16:19], a[8:11], a[144:147], v[16:19]\n\t"
"v_cvt_pk_bf16_f32 v208, v208, v209\n\t"
"v_cvt_pk_bf16_f32 v209, v210, v211\n\t"
"v_mfma_f32_16x16x32_bf16 v[16:19], a[12:15], a[160:163], v[16:19]\n\t"
"s_mov_b32 s20, 1069056\n\t"
"buffer_load_dwordx4 a[88:91], v248, s[8:11], s20 offen offset:1024 sc1\n\t"
"v_mfma_f32_16x16x32_bf16 v[32:35], a[16:19], a[144:147], v[32:35]\n\t"
"ds_read_b128 a[144:147], v216 offset:20480\n\t"
"v_mfma_f32_16x16x32_bf16 v[32:35], a[20:23], a[160:163], v[32:35]\n\t"
"buffer_load_dwordx4 a[92:95], v248, s[8:11], s20 offen offset:2048 sc1\n\t"
"ds_write_b128 v217, v[200:203] offset:0\n\t"
"v_mfma_f32_16x16x32_bf16 v[192:195], a[192:195], a[220:223], 0\n\t"
"v_cvt_pk_bf16_f32 v210, v212, v213\n\t"
"v_cvt_pk_bf16_f32 v211, v214, v215\n\t"
"s_waitcnt lgkmcnt(9)\n\t"
"v_mfma_f32_16x16x32_bf16 v[36:39], a[16:19], a[148:151], v[36:39]\n\t"
"ds_read_b128 a[160:163], v216 offset:28672\n\t"
"v_mfma_f32_16x16x32_bf16 v[36:39], a[20:23], a[164:167], v[36:39]\n\t"
"buffer_load_dwordx4 a[84:87], v248, s[8:11], s20 offen offset:0 sc1\n\t"
"ds_write_b128 v219, v[208:211] offset:0\n\t"
"v_mfma_f32_16x16x32_bf16 v[20:23], a[8:11], a[148:151], v[20:23]\n\t"
"v_cvt_pk_bf16_f32 v0, v0, v1\n\t"
"v_cvt_pk_bf16_f32 v1, v2, v3\n\t"
"v_mfma_f32_16x16x32_bf16 v[20:23], a[12:15], a[164:167], v[20:23]\n\t"
"ds_write_b64 v228, v[0:1] offset:0\n\t"
"v_cvt_pk_bf16_f32 v16, v16, v17\n\t"
"v_cvt_pk_bf16_f32 v17, v18, v19\n\t"
"v_mfma_f32_16x16x32_bf16 v[196:199], a[196:199], a[220:223], 0\n\t"
"ds_write_b64 v228, v[16:17] offset:8\n\t"
"v_mfma_f32_16x16x32_bf16 v[4:7], a[0:3], a[148:151], v[4:7]\n\t"
"v_cvt_pk_bf16_f32 v192, v192, v193\n\t"
"v_cvt_pk_bf16_f32 v193, v194, v195\n\t"
"v_mfma_f32_16x16x32_bf16 v[4:7], a[4:7], a[164:167], v[4:7]\n\t"
"s_mov_b32 s20, 1056768\n\t"
"buffer_load_dwordx4 a[80:83], v248, s[8:11], s20 offen offset:2048 sc1\n\t"
"v_mfma_f32_16x16x32_bf16 v[200:203], a[200:203], a[220:223], 0\n\t"
"v_cvt_pk_bf16_f32 v32, v32, v33\n\t"
"v_cvt_pk_bf16_f32 v33, v34, v35\n\t"
"s_waitcnt lgkmcnt(11)\n\t"
"v_mfma_f32_16x16x32_bf16 v[8:11], a[0:3], a[152:155], v[8:11]\n\t"
"ds_read_b128 a[148:151], v216 offset:21504\n\t"
"v_cvt_pk_bf16_f32 v194, v196, v197\n\t"
"v_mfma_f32_16x16x32_bf16 v[8:11], a[4:7], a[168:171], v[8:11]\n\t"
"ds_read_b128 a[164:167], v216 offset:29696\n\t"
"buffer_load_dwordx4 a[72:75], v248, s[8:11], s20 offen offset:0 sc1\n\t"
"v_cvt_pk_bf16_f32 v195, v198, v199\n\t"
"v_mfma_f32_16x16x32_bf16 v[24:27], a[8:11], a[152:155], v[24:27]\n\t"
"v_cvt_pk_bf16_f32 v4, v4, v5\n\t"
"v_cvt_pk_bf16_f32 v5, v6, v7\n\t"
"v_mfma_f32_16x16x32_bf16 v[24:27], a[12:15], a[168:171], v[24:27]\n\t"
"ds_write_b64 v228, v[32:33] offset:16\n\t"
"v_mfma_f32_16x16x32_bf16 v[204:207], a[204:207], a[220:223], 0\n\t"
"ds_write_b64 v228, v[4:5] offset:6400\n\t"
"v_mfma_f32_16x16x32_bf16 v[40:43], a[16:19], a[152:155], v[40:43]\n\t"
"v_cvt_pk_bf16_f32 v20, v20, v21\n\t"
"v_cvt_pk_bf16_f32 v21, v22, v23\n\t"
"v_mfma_f32_16x16x32_bf16 v[40:43], a[20:23], a[168:171], v[40:43]\n\t"
"buffer_load_dwordx4 a[76:79], v248, s[8:11], s20 offen offset:1024 sc1\n\t"
"v_cvt_pk_bf16_f32 v200, v200, v201\n\t"
"v_cvt_pk_bf16_f32 v201, v202, v203\n\t"
"s_waitcnt lgkmcnt(12)\n\t"
"v_mfma_f32_16x16x32_bf16 v[44:47], a[16:19], a[156:159], v[44:47]\n\t"
"v_cvt_pk_bf16_f32 v36, v36, v37\n\t"
"v_cvt_pk_bf16_f32 v37, v38, v39\n\t"
"v_mfma_f32_16x16x32_bf16 v[44:47], a[20:23], a[172:175], v[44:47]\n\t"
"ds_write_b64 v228, v[20:21] offset:6408\n\t"
"v_mfma_f32_16x16x32_bf16 v[28:31], a[8:11], a[156:159], v[28:31]\n\t"
"ds_read_b64_tr_b16 a[182:183], v252 offset:21248\n\t"
"v_cvt_pk_bf16_f32 v202, v204, v205\n\t"
"v_mfma_f32_16x16x32_bf16 v[28:31], a[12:15], a[172:175], v[28:31]\n\t"
"s_mov_b32 s20, 872448\n\t"
"buffer_load_dwordx4 a[112:115], v248, s[8:11], s20 offen offset:1024 sc1\n\t"
"ds_write_b128 v217, v[192:195] offset:8192\n\t"
"v_mfma_f32_16x16x32_bf16 v[12:15], a[0:3], a[156:159], v[12:15]\n\t"
"ds_read_b64_tr_b16 a[184:185], v249 offset:20736\n\t"
"v_cvt_pk_bf16_f32 v203, v206, v207\n\t"
"v_mfma_f32_16x16x32_bf16 v[12:15], a[4:7], a[172:175], v[12:15]\n\t"
"ds_write_b128 v219, v[200:203] offset:8192\n\t"
"s_waitcnt vmcnt(13) lgkmcnt(12)\n\t"
"v_mfma_f32_16x16x32_bf16 v[96:99], a[24:27], a[144:147], v[96:99]\n\t"
"ds_read_b64_tr_b16 a[186:187], v249 offset:21248\n\t"
"v_mfma_f32_16x16x32_bf16 v[96:99], a[28:31], a[160:163], v[96:99]\n\t"
"ds_read_b64_tr_b16 a[188:189], v250 offset:20736\n\t"
"ds_read_b64_tr_b16 a[190:191], v250 offset:21248\n\t"
"ds_read_b64_tr_b16 a[192:193], v251 offset:21888\n\t"
"ds_read_b64_tr_b16 a[194:195], v251 offset:22400\n\t"
"ds_read_b64_tr_b16 a[196:197], v252 offset:21888\n\t"
"ds_read_b64_tr_b16 a[198:199], v252 offset:22400\n\t"
"ds_read_b64_tr_b16 a[200:201], v249 offset:21888\n\t"
"ds_read_b64_tr_b16 a[202:203], v249 offset:22400\n\t"
"ds_read_b64_tr_b16 a[204:205], v250 offset:21888\n\t"
"ds_read_b64_tr_b16 a[206:207], v250 offset:22400\n\t"
"ds_write_b64 v228, v[36:37] offset:6416\n\t"
"buffer_load_dwordx4 a[116:119], v248, s[8:11], s20 offen offset:2048 sc1\n\t"
"v_mfma_f32_16x16x32_bf16 v[112:115], a[32:35], a[144:147], v[112:115]\n\t"
"v_mfma_f32_16x16x32_bf16 v[112:115], a[36:39], a[160:163], v[112:115]\n\t"
"v_mfma_f32_16x16x32_bf16 v[128:131], a[40:43], a[144:147], v[128:131]\n\t"
"ds_read_b128 a[152:155], v216 offset:22528\n\t"
"v_mfma_f32_16x16x32_bf16 v[128:131], a[44:47], a[160:163], v[128:131]\n\t"
"buffer_load_dwordx4 a[108:111], v248, s[8:11], s20 offen offset:0 sc1\n\t"
"ds_read_b128 a[168:171], v216 offset:30720\n\t"
"ds_read_b128 a[156:159], v216 offset:23552\n\t"
"ds_read_b128 a[172:175], v216 offset:31744\n\t"
"s_waitcnt lgkmcnt(15)\n\t"
"v_mfma_f32_16x16x32_bf16 v[132:135], a[40:43], a[148:151], v[132:135]\n\t"
"v_mfma_f32_16x16x32_bf16 v[132:135], a[44:47], a[164:167], v[132:135]\n\t"
"s_mov_b32 s20, 860160\n\t"
"buffer_load_dwordx4 a[104:107], v248, s[8:11], s20 offen offset:2048 sc1\n\t"
"v_mfma_f32_16x16x32_bf16 v[116:119], a[32:35], a[148:151], v[116:119]\n\t"
"v_mfma_f32_16x16x32_bf16 v[116:119], a[36:39], a[164:167], v[116:119]\n\t"
"buffer_load_dwordx4 a[96:99], v248, s[8:11], s20 offen offset:0 sc1\n\t"
"v_mfma_f32_16x16x32_bf16 v[100:103], a[24:27], a[148:151], v[100:103]\n\t"
"v_mfma_f32_16x16x32_bf16 v[100:103], a[28:31], a[164:167], v[100:103]\n\t"
"s_waitcnt lgkmcnt(0)\n\t"
"s_barrier\n\t"
"s_cmp_eq_u32 s21, 1\n\t"
"s_cbranch_scc1 Ltail_%=\n\t"
"v_mfma_f32_16x16x32_bf16 v[104:107], a[24:27], a[152:155], v[104:107]\n\t"
"ds_read_b128 a[144:147], v216 offset:0\n\t"
"v_add_u32 v249, 80896, v226\n\t"
"v_mfma_f32_16x16x32_bf16 v[104:107], a[28:31], a[168:171], v[104:107]\n\t"
"ds_read_b128 a[160:163], v216 offset:8192\n\t"
"ds_read_b128 a[148:151], v216 offset:1024\n\t"
"v_mfma_f32_16x16x32_bf16 v[120:123], a[32:35], a[152:155], v[120:123]\n\t"
"ds_read_b128 a[164:167], v216 offset:9216\n\t"
"v_xor_b32 v250, 32, v249\n\t"
"v_mfma_f32_16x16x32_bf16 v[120:123], a[36:39], a[168:171], v[120:123]\n\t"
"s_mov_b32 s20, 860160\n\t"
"buffer_load_dwordx4 a[100:103], v248, s[8:11], s20 offen offset:1024 sc1\n\t"
"v_mfma_f32_16x16x32_bf16 v[136:139], a[40:43], a[152:155], v[136:139]\n\t"
"v_xor_b32 v251, 64, v249\n\t"
"v_xor_b32 v252, 96, v249\n\t"
"v_mfma_f32_16x16x32_bf16 v[136:139], a[44:47], a[168:171], v[136:139]\n\t"
"s_mov_b32 s20, 1167360\n\t"
"buffer_load_dwordx4 a[136:139], v248, s[8:11], s20 offen offset:1024 sc1\n\t"
"v_mfma_f32_16x16x32_bf16 v[192:195], a[176:179], a[224:227], 0\n\t"
"buffer_load_dwordx4 a[140:143], v248, s[8:11], s20 offen offset:2048 sc1\n\t"
"v_mfma_f32_16x16x32_bf16 v[140:143], a[40:43], a[156:159], v[140:143]\n\t"
"ds_read_b128 a[240:243], v222 offset:39168\n\t"
"v_cvt_pk_bf16_f32 v96, v96, v97\n\t"
"v_mfma_f32_16x16x32_bf16 v[140:143], a[44:47], a[172:175], v[140:143]\n\t"
"v_cvt_pk_bf16_f32 v97, v98, v99\n\t"
"ds_read_b128 a[228:231], v229 offset:0\n\t"
"v_mfma_f32_16x16x32_bf16 v[196:199], a[180:183], a[224:227], 0\n\t"
"buffer_load_dwordx4 a[132:135], v248, s[8:11], s20 offen offset:0 sc1\n\t"
"ds_write_b64 v228, v[96:97] offset:12800\n\t"
"v_mfma_f32_16x16x32_bf16 v[124:127], a[32:35], a[156:159], v[124:127]\n\t"
"ds_read_b128 a[232:235], v230 offset:0\n\t"
"v_cvt_pk_bf16_f32 v192, v192, v193\n\t"
"v_mfma_f32_16x16x32_bf16 v[124:127], a[36:39], a[172:175], v[124:127]\n\t"
"ds_read_b128 a[152:155], v216 offset:2048\n\t"
"ds_read_b128 a[168:171], v216 offset:10240\n\t"
"v_mfma_f32_16x16x32_bf16 v[108:111], a[24:27], a[156:159], v[108:111]\n\t"
"v_cvt_pk_bf16_f32 v193, v194, v195\n\t"
"ds_read_b128 a[236:239], v231 offset:0\n\t"
"v_mfma_f32_16x16x32_bf16 v[108:111], a[28:31], a[172:175], v[108:111]\n\t"
"s_mov_b32 s20, 1155072\n\t"
"buffer_load_dwordx4 a[128:131], v248, s[8:11], s20 offen offset:2048 sc1\n\t"
"v_mfma_f32_16x16x32_bf16 v[200:203], a[184:187], a[224:227], 0\n\t"
"v_cvt_pk_bf16_f32 v112, v112, v113\n\t"
"v_cvt_pk_bf16_f32 v113, v114, v115\n\t"
"v_cvt_pk_bf16_f32 v194, v196, v197\n\t"
"v_cvt_pk_bf16_f32 v195, v198, v199\n\t"
"s_waitcnt vmcnt(16) lgkmcnt(9)\n\t"
"v_mfma_f32_16x16x32_bf16 v[48:51], a[48:51], a[144:147], v[48:51]\n\t"
"ds_read_b128 a[156:159], v216 offset:3072\n\t"
"v_cvt_pk_bf16_f32 v128, v128, v129\n\t"
"v_mfma_f32_16x16x32_bf16 v[48:51], a[52:55], a[160:163], v[48:51]\n\t"
"buffer_load_dwordx4 a[120:123], v248, s[8:11], s20 offen offset:0 sc1\n\t"
"v_mfma_f32_16x16x32_bf16 v[204:207], a[188:191], a[224:227], 0\n\t"
"ds_write_b64 v228, v[112:113] offset:12808\n\t"
"v_mfma_f32_16x16x32_bf16 v[64:67], a[56:59], a[144:147], v[64:67]\n\t"
"ds_read_b128 a[172:175], v216 offset:11264\n\t"
"v_cvt_pk_bf16_f32 v129, v130, v131\n\t"
"v_mfma_f32_16x16x32_bf16 v[64:67], a[60:63], a[160:163], v[64:67]\n\t"
"buffer_load_dwordx4 a[124:127], v248, s[8:11], s20 offen offset:1024 sc1\n\t"
"ds_write_b128 v221, v[192:195] offset:16384\n\t"
"v_mfma_f32_16x16x32_bf16 v[80:83], a[64:67], a[144:147], v[80:83]\n\t"
"v_cvt_pk_bf16_f32 v200, v200, v201\n\t"
"v_cvt_pk_bf16_f32 v201, v202, v203\n\t"
"v_mfma_f32_16x16x32_bf16 v[80:83], a[68:71], a[160:163], v[80:83]\n\t"
"ds_write_b64 v228, v[128:129] offset:12816\n\t"
"v_mfma_f32_16x16x32_bf16 v[208:211], a[192:195], a[224:227], 0\n\t"
"s_mov_b32 s20, 110592\n\t"
"buffer_load_dwordx4 a[16:19], v248, s[8:11], s20 offen offset:1024 sc1\n\t"
"s_waitcnt lgkmcnt(12)\n\t"
"v_mfma_f32_16x16x32_bf16 v[84:87], a[64:67], a[148:151], v[84:87]\n\t"
"v_cvt_pk_bf16_f32 v202, v204, v205\n\t"
"v_cvt_pk_bf16_f32 v203, v206, v207\n\t"
"v_mfma_f32_16x16x32_bf16 v[84:87], a[68:71], a[164:167], v[84:87]\n\t"
"s_add_u32 s28, s24, 0\n\t"
"s_waitcnt lgkmcnt(10)\n\t"
"buffer_store_dwordx4 a[228:231], v232, s[16:19], s28 offen nt\n\t"
"ds_read_b128 a[144:147], v216 offset:4096\n\t"
"ds_read_b128 a[160:163], v216 offset:12288\n\t"
"v_mfma_f32_16x16x32_bf16 v[68:71], a[56:59], a[148:151], v[68:71]\n\t"
"v_cvt_pk_bf16_f32 v100, v100, v101\n\t"
"v_cvt_pk_bf16_f32 v101, v102, v103\n\t"
"v_mfma_f32_16x16x32_bf16 v[68:71], a[60:63], a[164:167], v[68:71]\n\t"
"buffer_load_dwordx4 a[20:23], v248, s[8:11], s20 offen offset:2048 sc1\n\t"
"v_mfma_f32_16x16x32_bf16 v[212:215], a[196:199], a[224:227], 0\n\t"
"ds_write_b128 v221, v[200:203] offset:24576\n\t"
"v_mfma_f32_16x16x32_bf16 v[52:55], a[48:51], a[148:151], v[52:55]\n\t"
"v_cvt_pk_bf16_f32 v208, v208, v209\n\t"
"v_cvt_pk_bf16_f32 v209, v210, v211\n\t"
"v_mfma_f32_16x16x32_bf16 v[52:55], a[52:55], a[164:167], v[52:55]\n\t"
"ds_write_b64 v228, v[100:101] offset:19200\n\t"
"buffer_load_dwordx4 a[12:15], v248, s[8:11], s20 offen offset:0 sc1\n\t"
"v_mfma_f32_16x16x32_bf16 v[192:195], a[200:203], a[224:227], 0\n\t"
"ds_read_b128 a[148:151], v216 offset:5120\n\t"
"ds_read_b128 a[164:167], v216 offset:13312\n\t"
"s_waitcnt lgkmcnt(12)\n\t"
"v_mfma_f32_16x16x32_bf16 v[56:59], a[48:51], a[152:155], v[56:59]\n\t"
"v_cvt_pk_bf16_f32 v116, v116, v117\n\t"
"v_cvt_pk_bf16_f32 v117, v118, v119\n\t"
"v_mfma_f32_16x16x32_bf16 v[56:59], a[52:55], a[168:171], v[56:59]\n\t"
"v_cvt_pk_bf16_f32 v210, v212, v213\n\t"
"v_cvt_pk_bf16_f32 v211, v214, v215\n\t"
"v_mfma_f32_16x16x32_bf16 v[72:75], a[56:59], a[152:155], v[72:75]\n\t"
"v_cvt_pk_bf16_f32 v132, v132, v133\n\t"
"v_cvt_pk_bf16_f32 v133, v134, v135\n\t"
"v_mfma_f32_16x16x32_bf16 v[72:75], a[60:63], a[168:171], v[72:75]\n\t"
"ds_write_b64 v228, v[116:117] offset:19208\n\t"
"s_add_u32 s28, s24, 0\n\t"
"buffer_store_dwordx4 a[232:235], v233, s[16:19], s28 offen nt\n\t"
"v_mfma_f32_16x16x32_bf16 v[196:199], a[204:207], a[224:227], 0\n\t"
"v_cvt_pk_bf16_f32 v192, v192, v193\n\t"
"v_cvt_pk_bf16_f32 v193, v194, v195\n\t"
"v_mfma_f32_16x16x32_bf16 v[88:91], a[64:67], a[152:155], v[88:91]\n\t"
"ds_read_b64_tr_b16 a[176:177], v249 offset:0\n\t"
"v_mfma_f32_16x16x32_bf16 v[88:91], a[68:71], a[168:171], v[88:91]\n\t"
"s_mov_b32 s20, 98304\n\t"
"buffer_load_dwordx4 a[8:11], v248, s[8:11], s20 offen offset:2048 sc1\n\t"
"ds_write_b128 v221, v[208:211] offset:20480\n\t"
"s_waitcnt lgkmcnt(11)\n\t"
"v_mfma_f32_16x16x32_bf16 v[92:95], a[64:67], a[156:159], v[92:95]\n\t"
"ds_read_b64_tr_b16 a[178:179], v249 offset:512\n\t"
"v_mfma_f32_16x16x32_bf16 v[92:95], a[68:71], a[172:175], v[92:95]\n\t"
"ds_read_b64_tr_b16 a[180:181], v250 offset:0\n\t"
"ds_read_b64_tr_b16 a[182:183], v250 offset:512\n\t"
"ds_read_b64_tr_b16 a[184:185], v251 offset:2304\n\t"
"v_cvt_pk_bf16_f32 v194, v196, v197\n\t"
"v_mfma_f32_16x16x32_bf16 v[76:79], a[56:59], a[156:159], v[76:79]\n\t"
"ds_read_b64_tr_b16 a[186:187], v251 offset:2816\n\t"
"v_cvt_pk_bf16_f32 v195, v198, v199\n\t"
"v_mfma_f32_16x16x32_bf16 v[76:79], a[60:63], a[172:175], v[76:79]\n\t"
"buffer_load_dwordx4 a[0:3], v248, s[8:11], s20 offen offset:0 sc1\n\t"
"v_mfma_f32_16x16x32_bf16 v[60:63], a[48:51], a[156:159], v[60:63]\n\t"
"ds_read_b64_tr_b16 a[188:189], v252 offset:2304\n\t"
"v_mfma_f32_16x16x32_bf16 v[60:63], a[52:55], a[172:175], v[60:63]\n\t"
"ds_write_b128 v221, v[192:195] offset:28672\n\t"
"ds_read_b64_tr_b16 a[190:191], v252 offset:2816\n\t"
"ds_read_b64_tr_b16 a[192:193], v251 offset:0\n\t"
"s_waitcnt vmcnt(19) lgkmcnt(15)\n\t"
"v_mfma_f32_16x16x32_bf16 v[144:147], a[72:75], a[144:147], v[144:147]\n\t"
"ds_read_b64_tr_b16 a[194:195], v251 offset:512\n\t"
"v_mfma_f32_16x16x32_bf16 v[144:147], a[76:79], a[160:163], v[144:147]\n\t"
"ds_read_b64_tr_b16 a[196:197], v252 offset:0\n\t"
"ds_read_b64_tr_b16 a[198:199], v252 offset:512\n\t"
"ds_read_b64_tr_b16 a[200:201], v249 offset:2304\n\t"
"ds_read_b64_tr_b16 a[202:203], v249 offset:2816\n\t"
"ds_read_b64_tr_b16 a[204:205], v250 offset:2304\n\t"
"ds_read_b64_tr_b16 a[206:207], v250 offset:2816\n\t"
"ds_write_b64 v228, v[132:133] offset:19216\n\t"
"v_mfma_f32_16x16x32_bf16 v[160:163], a[80:83], a[144:147], v[160:163]\n\t"
"ds_read_b128 a[152:155], v216 offset:6144\n\t"
"v_mfma_f32_16x16x32_bf16 v[160:163], a[84:87], a[160:163], v[160:163]\n\t"
"buffer_load_dwordx4 a[4:7], v248, s[8:11], s20 offen offset:1024 sc1\n\t"
"v_mfma_f32_16x16x32_bf16 v[176:179], a[88:91], a[144:147], v[176:179]\n\t"
"ds_read_b128 a[168:171], v216 offset:14336\n\t"
"v_mfma_f32_16x16x32_bf16 v[176:179], a[92:95], a[160:163], v[176:179]\n\t"
"ds_read_b128 a[156:159], v216 offset:7168\n\t"
"ds_read_b128 a[172:175], v216 offset:15360\n\t"
"ds_write_b128 v222, a[240:243] offset:20736\n\t"
"s_waitcnt lgkmcnt(15)\n\t"
"v_mfma_f32_16x16x32_bf16 v[180:183], a[88:91], a[148:151], v[180:183]\n\t"
"v_mfma_f32_16x16x32_bf16 v[180:183], a[92:95], a[164:167], v[180:183]\n\t"
"s_mov_b32 s20, 208896\n\t"
"buffer_load_dwordx4 a[40:43], v248, s[8:11], s20 offen offset:1024 sc1\n\t"
"v_mfma_f32_16x16x32_bf16 v[164:167], a[80:83], a[148:151], v[164:167]\n\t"
"v_mfma_f32_16x16x32_bf16 v[164:167], a[84:87], a[164:167], v[164:167]\n\t"
"s_add_u32 s28, s24, 0\n\t"
"buffer_store_dwordx4 a[236:239], v234, s[16:19], s28 offen nt\n\t"
"buffer_load_dwordx4 a[44:47], v248, s[8:11], s20 offen offset:2048 sc1\n\t"
"v_mfma_f32_16x16x32_bf16 v[148:151], a[72:75], a[148:151], v[148:151]\n\t"
"v_mfma_f32_16x16x32_bf16 v[148:151], a[76:79], a[164:167], v[148:151]\n\t"
"buffer_load_dwordx4 a[36:39], v248, s[8:11], s20 offen offset:0 sc1\n\t"
"s_waitcnt lgkmcnt(0)\n\t"
"s_barrier\n\t"
"v_mfma_f32_16x16x32_bf16 v[152:155], a[72:75], a[152:155], v[152:155]\n\t"
"ds_read_b128 a[144:147], v216 offset:16384\n\t"
"v_add_u32 v249, 80896, v227\n\t"
"v_mfma_f32_16x16x32_bf16 v[152:155], a[76:79], a[168:171], v[152:155]\n\t"
"ds_read_b128 a[160:163], v216 offset:24576\n\t"
"ds_read_b128 a[148:151], v216 offset:17408\n\t"
"v_mfma_f32_16x16x32_bf16 v[168:171], a[80:83], a[152:155], v[168:171]\n\t"
"ds_read_b128 a[164:167], v216 offset:25600\n\t"
"v_xor_b32 v250, 32, v249\n\t"
"v_mfma_f32_16x16x32_bf16 v[168:171], a[84:87], a[168:171], v[168:171]\n\t"
"s_mov_b32 s20, 196608\n\t"
"buffer_load_dwordx4 a[32:35], v248, s[8:11], s20 offen offset:2048 sc1\n\t"
"v_mfma_f32_16x16x32_bf16 v[184:187], a[88:91], a[152:155], v[184:187]\n\t"
"v_xor_b32 v251, 64, v249\n\t"
"v_xor_b32 v252, 96, v249\n\t"
"v_mfma_f32_16x16x32_bf16 v[184:187], a[92:95], a[168:171], v[184:187]\n\t"
"buffer_load_dwordx4 a[24:27], v248, s[8:11], s20 offen offset:0 sc1\n\t"
"v_cvt_pk_bf16_f32 v104, v104, v105\n\t"
"v_cvt_pk_bf16_f32 v105, v106, v107\n\t"
"v_mfma_f32_16x16x32_bf16 v[200:203], a[176:179], a[220:223], 0\n\t"
"ds_write_b64 v228, v[104:105] offset:0\n\t"
"v_cvt_pk_bf16_f32 v120, v120, v121\n\t"
"v_cvt_pk_bf16_f32 v121, v122, v123\n\t"
"v_mfma_f32_16x16x32_bf16 v[188:191], a[88:91], a[156:159], v[188:191]\n\t"
"ds_read_b128 a[228:231], v229 offset:12800\n\t"
"v_cvt_pk_bf16_f32 v136, v136, v137\n\t"
"v_mfma_f32_16x16x32_bf16 v[188:191], a[92:95], a[172:175], v[188:191]\n\t"
"buffer_load_dwordx4 a[28:31], v248, s[8:11], s20 offen offset:1024 sc1\n\t"
"ds_write_b64 v228, v[120:121] offset:8\n\t"
"v_mfma_f32_16x16x32_bf16 v[204:207], a[180:183], a[220:223], 0\n\t"
"v_cvt_pk_bf16_f32 v137, v138, v139\n\t"
"ds_read_b128 a[232:235], v230 offset:12800\n\t"
"v_mfma_f32_16x16x32_bf16 v[172:175], a[80:83], a[156:159], v[172:175]\n\t"
"ds_read_b128 a[152:155], v216 offset:18432\n\t"
"v_cvt_pk_bf16_f32 v200, v200, v201\n\t"
"v_mfma_f32_16x16x32_bf16 v[172:175], a[84:87], a[172:175], v[172:175]\n\t"
"s_mov_b32 s20, 12288\n\t"
"buffer_load_dwordx4 a[64:67], v248, s[8:11], s20 offen offset:1024 sc1\n\t"
"ds_read_b128 a[168:171], v216 offset:26624\n\t"
"v_cvt_pk_bf16_f32 v201, v202, v203\n\t"
"v_mfma_f32_16x16x32_bf16 v[156:159], a[72:75], a[156:159], v[156:159]\n\t"
"ds_read_b128 a[236:239], v231 offset:12800\n\t"
"v_cvt_pk_bf16_f32 v108, v108, v109\n\t"
"v_mfma_f32_16x16x32_bf16 v[156:159], a[76:79], a[172:175], v[156:159]\n\t"
"ds_write_b64 v228, v[136:137] offset:16\n\t"
"buffer_load_dwordx4 a[68:71], v248, s[8:11], s20 offen offset:2048 sc1\n\t"
"v_mfma_f32_16x16x32_bf16 v[208:211], a[184:187], a[220:223], 0\n\t"
"v_pk_add_f32 v[48:49], v[48:49], v[144:145]\n\t"
"v_pk_add_f32 v[50:51], v[50:51], v[146:147]\n\t"
"v_pk_fma_f32 v[144:145], v[144:145], s[38:39], v[48:49]\n\t"
"v_pk_fma_f32 v[146:147], v[146:147], s[38:39], v[50:51]\n\t"
"v_pk_add_f32 v[64:65], v[64:65], v[160:161]\n\t"
"v_pk_add_f32 v[66:67], v[66:67], v[162:163]\n\t"
"v_pk_fma_f32 v[160:161], v[160:161], s[38:39], v[64:65]\n\t"
"v_pk_fma_f32 v[162:163], v[162:163], s[38:39], v[66:67]\n\t"
"v_pk_add_f32 v[80:81], v[80:81], v[176:177]\n\t"
"v_pk_add_f32 v[82:83], v[82:83], v[178:179]\n\t"
"v_pk_fma_f32 v[176:177], v[176:177], s[38:39], v[80:81]\n\t"
"v_pk_fma_f32 v[178:179], v[178:179], s[38:39], v[82:83]\n\t"
"v_pk_add_f32 v[84:85], v[84:85], v[180:181]\n\t"
"v_pk_add_f32 v[86:87], v[86:87], v[182:183]\n\t"
"v_pk_fma_f32 v[180:181], v[180:181], s[38:39], v[84:85]\n\t"
"v_pk_fma_f32 v[182:183], v[182:183], s[38:39], v[86:87]\n\t"
"v_pk_add_f32 v[68:69], v[68:69], v[164:165]\n\t"
"v_pk_add_f32 v[70:71], v[70:71], v[166:167]\n\t"
"v_pk_fma_f32 v[164:165], v[164:165], s[38:39], v[68:69]\n\t"
"v_pk_fma_f32 v[166:167], v[166:167], s[38:39], v[70:71]\n\t"
"v_pk_add_f32 v[52:53], v[52:53], v[148:149]\n\t"
"v_pk_add_f32 v[54:55], v[54:55], v[150:151]\n\t"
"v_pk_fma_f32 v[148:149], v[148:149], s[38:39], v[52:53]\n\t"
"v_pk_fma_f32 v[150:151], v[150:151], s[38:39], v[54:55]\n\t"
"v_pk_add_f32 v[56:57], v[56:57], v[152:153]\n\t"
"v_pk_add_f32 v[58:59], v[58:59], v[154:155]\n\t"
"v_pk_fma_f32 v[152:153], v[152:153], s[38:39], v[56:57]\n\t"
"v_pk_fma_f32 v[154:155], v[154:155], s[38:39], v[58:59]\n\t"
"v_pk_add_f32 v[72:73], v[72:73], v[168:169]\n\t"
"v_pk_add_f32 v[74:75], v[74:75], v[170:171]\n\t"
"v_pk_fma_f32 v[168:169], v[168:169], s[38:39], v[72:73]\n\t"
"v_pk_fma_f32 v[170:171], v[170:171], s[38:39], v[74:75]\n\t"
"v_pk_add_f32 v[88:89], v[88:89], v[184:185]\n\t"
"v_pk_add_f32 v[90:91], v[90:91], v[186:187]\n\t"
"v_pk_fma_f32 v[184:185], v[184:185], s[38:39], v[88:89]\n\t"
"v_pk_fma_f32 v[186:187], v[186:187], s[38:39], v[90:91]\n\t"
"v_pk_add_f32 v[92:93], v[92:93], v[188:189]\n\t"
"v_pk_add_f32 v[94:95], v[94:95], v[190:191]\n\t"
"v_pk_fma_f32 v[188:189], v[188:189], s[38:39], v[92:93]\n\t"
"v_pk_fma_f32 v[190:191], v[190:191], s[38:39], v[94:95]\n\t"
"v_pk_add_f32 v[76:77], v[76:77], v[172:173]\n\t"
"v_pk_add_f32 v[78:79], v[78:79], v[174:175]\n\t"
"v_pk_fma_f32 v[172:173], v[172:173], s[38:39], v[76:77]\n\t"
"v_pk_fma_f32 v[174:175], v[174:175], s[38:39], v[78:79]\n\t"
"v_pk_add_f32 v[60:61], v[60:61], v[156:157]\n\t"
"v_pk_add_f32 v[62:63], v[62:63], v[158:159]\n\t"
"v_pk_fma_f32 v[156:157], v[156:157], s[38:39], v[60:61]\n\t"
"v_pk_fma_f32 v[158:159], v[158:159], s[38:39], v[62:63]\n\t"
"v_cvt_pk_bf16_f32 v109, v110, v111\n\t"
"v_cvt_pk_bf16_f32 v202, v204, v205\n\t"
"s_waitcnt vmcnt(23) lgkmcnt(10)\n\t"
"v_mfma_f32_16x16x32_bf16 v[48:51], a[96:99], a[144:147], v[48:51]\n\t"
"v_cvt_pk_bf16_f32 v203, v206, v207\n\t"
"v_cvt_pk_bf16_f32 v124, v124, v125\n\t"
"v_mfma_f32_16x16x32_bf16 v[48:51], a[100:103], a[160:163], v[48:51]\n\t"
"buffer_load_dwordx4 a[60:63], v248, s[8:11], s20 offen offset:0 sc1\n\t"
"s_mov_b32 s20, 0\n\t"
"buffer_load_dwordx4 a[56:59], v248, s[8:11], s20 offen offset:2048 sc1\n\t"
"v_mfma_f32_16x16x32_bf16 v[212:215], a[188:191], a[220:223], 0\n\t"
"ds_write_b64 v228, v[108:109] offset:6400\n\t"
"buffer_load_dwordx4 a[48:51], v248, s[8:11], s20 offen offset:0 sc1\n\t"
"v_mfma_f32_16x16x32_bf16 v[64:67], a[104:107], a[144:147], v[64:67]\n\t"
"v_cvt_pk_bf16_f32 v125, v126, v127\n\t"
"ds_read_b128 a[156:159], v216 offset:19456\n\t"
"v_mfma_f32_16x16x32_bf16 v[64:67], a[108:111], a[160:163], v[64:67]\n\t"
"buffer_load_dwordx4 a[52:55], v248, s[8:11], s20 offen offset:1024 sc1\n\t"
"s_mov_b32 s20, 307200\n\t"
"buffer_load_dwordx4 a[88:91], v248, s[8:11], s20 offen offset:1024 sc1\n\t"
"v_mfma_f32_16x16x32_bf16 v[80:83], a[112:115], a[144:147], v[80:83]\n\t"
"ds_read_b128 a[172:175], v216 offset:27648\n\t"
"v_cvt_pk_bf16_f32 v140, v140, v141\n\t"
"v_mfma_f32_16x16x32_bf16 v[80:83], a[116:119], a[160:163], v[80:83]\n\t"
"ds_write_b64 v228, v[124:125] offset:6408\n\t"
"ds_write_b128 v217, v[200:203] offset:0\n\t"
"v_mfma_f32_16x16x32_bf16 v[192:195], a[192:195], a[220:223], 0\n\t"
"v_cvt_pk_bf16_f32 v141, v142, v143\n\t"
"v_cvt_pk_bf16_f32 v208, v208, v209\n\t"
"s_waitcnt lgkmcnt(13)\n\t"
"v_mfma_f32_16x16x32_bf16 v[84:87], a[112:115], a[148:151], v[84:87]\n\t"
"v_cvt_pk_bf16_f32 v209, v210, v211\n\t"
"v_cvt_pk_bf16_f32 v8, v8, v9\n\t"
"v_mfma_f32_16x16x32_bf16 v[84:87], a[116:119], a[164:167], v[84:87]\n\t"
"buffer_load_dwordx4 a[92:95], v248, s[8:11], s20 offen offset:2048 sc1\n\t"
"ds_write_b64 v228, v[140:141] offset:6416\n\t"
"v_mfma_f32_16x16x32_bf16 v[68:71], a[104:107], a[148:151], v[68:71]\n\t"
"v_cvt_pk_bf16_f32 v9, v10, v11\n\t"
"v_cvt_pk_bf16_f32 v210, v212, v213\n\t"
"v_mfma_f32_16x16x32_bf16 v[68:71], a[108:111], a[164:167], v[68:71]\n\t"
"s_add_u32 s28, s24, 196608\n\t"
"s_waitcnt lgkmcnt(12)\n\t"
"buffer_store_dwordx4 a[228:231], v232, s[16:19], s28 offen nt\n\t"
"buffer_load_dwordx4 a[84:87], v248, s[8:11], s20 offen offset:0 sc1\n\t"
"v_mfma_f32_16x16x32_bf16 v[196:199], a[196:199], a[220:223], 0\n\t"
"v_cvt_pk_bf16_f32 v211, v214, v215\n\t"
"ds_read_b128 a[144:147], v216 offset:20480\n\t"
"v_mfma_f32_16x16x32_bf16 v[52:55], a[96:99], a[148:151], v[52:55]\n\t"
"ds_read_b128 a[160:163], v216 offset:28672\n\t"
"v_cvt_pk_bf16_f32 v24, v24, v25\n\t"
"v_mfma_f32_16x16x32_bf16 v[52:55], a[100:103], a[164:167], v[52:55]\n\t"
"ds_write_b64 v228, v[8:9] offset:29696\n\t"
"v_cvt_pk_bf16_f32 v25, v26, v27\n\t"
"v_cvt_pk_bf16_f32 v192, v192, v193\n\t"
"v_mfma_f32_16x16x32_bf16 v[200:203], a[200:203], a[220:223], 0\n\t"
"s_mov_b32 s20, 294912\n\t"
"buffer_load_dwordx4 a[80:83], v248, s[8:11], s20 offen offset:2048 sc1\n\t"
"ds_write_b128 v219, v[208:211] offset:0\n\t"
"s_waitcnt lgkmcnt(12)\n\t"
"v_mfma_f32_16x16x32_bf16 v[56:59], a[96:99], a[152:155], v[56:59]\n\t"
"v_cvt_pk_bf16_f32 v193, v194, v195\n\t"
"v_cvt_pk_bf16_f32 v40, v40, v41\n\t"
"v_mfma_f32_16x16x32_bf16 v[56:59], a[100:103], a[168:171], v[56:59]\n\t"
"ds_write_b64 v228, v[24:25] offset:29704\n\t"
"v_cvt_pk_bf16_f32 v41, v42, v43\n\t"
"ds_read_b128 a[148:151], v216 offset:21504\n\t"
"v_mfma_f32_16x16x32_bf16 v[72:75], a[104:107], a[152:155], v[72:75]\n\t"
"ds_read_b128 a[164:167], v216 offset:29696\n\t"
"v_cvt_pk_bf16_f32 v12, v12, v13\n\t"
"v_mfma_f32_16x16x32_bf16 v[72:75], a[108:111], a[168:171], v[72:75]\n\t"
"buffer_load_dwordx4 a[72:75], v248, s[8:11], s20 offen offset:0 sc1\n\t"
"ds_write_b64 v228, v[40:41] offset:29712\n\t"
"v_mfma_f32_16x16x32_bf16 v[204:207], a[204:207], a[220:223], 0\n\t"
"v_cvt_pk_bf16_f32 v13, v14, v15\n\t"
"v_cvt_pk_bf16_f32 v194, v196, v197\n\t"
"v_mfma_f32_16x16x32_bf16 v[88:91], a[112:115], a[152:155], v[88:91]\n\t"
"v_cvt_pk_bf16_f32 v195, v198, v199\n\t"
"v_cvt_pk_bf16_f32 v28, v28, v29\n\t"
"v_mfma_f32_16x16x32_bf16 v[88:91], a[116:119], a[168:171], v[88:91]\n\t"
"ds_write_b64 v228, v[12:13] offset:36096\n\t"
"v_cvt_pk_bf16_f32 v29, v30, v31\n\t"
"v_cvt_pk_bf16_f32 v200, v200, v201\n\t"
"s_waitcnt lgkmcnt(12)\n\t"
"v_mfma_f32_16x16x32_bf16 v[92:95], a[112:115], a[156:159], v[92:95]\n\t"
"v_cvt_pk_bf16_f32 v201, v202, v203\n\t"
"v_cvt_pk_bf16_f32 v44, v44, v45\n\t"
"v_mfma_f32_16x16x32_bf16 v[92:95], a[116:119], a[172:175], v[92:95]\n\t"
"s_add_u32 s28, s24, 196608\n\t"
"buffer_store_dwordx4 a[232:235], v233, s[16:19], s28 offen nt\n\t"
"ds_write_b64 v228, v[28:29] offset:36104\n\t"
"v_mfma_f32_16x16x32_bf16 v[76:79], a[104:107], a[156:159], v[76:79]\n\t"
"v_cvt_pk_bf16_f32 v45, v46, v47\n\t"
"ds_read_b64_tr_b16 a[176:177], v249 offset:0\n\t"
"v_mfma_f32_16x16x32_bf16 v[76:79], a[108:111], a[172:175], v[76:79]\n\t"
"ds_write_b128 v217, v[192:195] offset:8192\n\t"
"buffer_load_dwordx4 a[76:79], v248, s[8:11], s20 offen offset:1024 sc1\n\t"
"v_mfma_f32_16x16x32_bf16 v[60:63], a[96:99], a[156:159], v[60:63]\n\t"
"ds_read_b64_tr_b16 a[178:179], v249 offset:512\n\t"
"v_cvt_pk_bf16_f32 v202, v204, v205\n\t"
"v_mfma_f32_16x16x32_bf16 v[60:63], a[100:103], a[172:175], v[60:63]\n\t"
"ds_read_b64_tr_b16 a[180:181], v250 offset:0\n\t"
"ds_read_b64_tr_b16 a[182:183], v250 offset:512\n\t"
"ds_read_b64_tr_b16 a[184:185], v251 offset:0\n\t"
"v_cvt_pk_bf16_f32 v203, v206, v207\n\t"
"s_waitcnt vmcnt(29) lgkmcnt(14)\n\t"
"v_mfma_f32_16x16x32_bf16 v[144:147], a[120:123], a[144:147], v[144:147]\n\t"
"ds_read_b64_tr_b16 a[186:187], v251 offset:512\n\t"
"v_mfma_f32_16x16x32_bf16 v[144:147], a[124:127], a[160:163], v[144:147]\n\t"
"ds_write_b128 v219, v[200:203] offset:8192\n\t"
"ds_read_b64_tr_b16 a[188:189], v252 offset:0\n\t"
"ds_read_b64_tr_b16 a[190:191], v252 offset:512\n\t"
"ds_read_b64_tr_b16 a[192:193], v249 offset:1152\n\t"
"ds_read_b64_tr_b16 a[194:195], v249 offset:1664\n\t"
"ds_read_b64_tr_b16 a[196:197], v250 offset:1152\n\t"
"ds_read_b64_tr_b16 a[198:199], v250 offset:1664\n\t"
"ds_read_b64_tr_b16 a[200:201], v251 offset:1152\n\t"
"ds_read_b64_tr_b16 a[202:203], v251 offset:1664\n\t"
"ds_read_b64_tr_b16 a[204:205], v252 offset:1152\n\t"
"ds_read_b64_tr_b16 a[206:207], v252 offset:1664\n\t"
"ds_write_b64 v228, v[44:45] offset:36112\n\t"
"v_mfma_f32_16x16x32_bf16 v[160:163], a[128:131], a[144:147], v[160:163]\n\t"
"v_mfma_f32_16x16x32_bf16 v[160:163], a[132:135], a[160:163], v[160:163]\n\t"
"s_mov_b32 s20, 405504\n\t"
"buffer_load_dwordx4 a[112:115], v248, s[8:11], s20 offen offset:1024 sc1\n\t"
"v_mfma_f32_16x16x32_bf16 v[176:179], a[136:139], a[144:147], v[176:179]\n\t"
"ds_read_b128 a[152:155], v216 offset:22528\n\t"
"v_mfma_f32_16x16x32_bf16 v[176:179], a[140:143], a[160:163], v[176:179]\n\t"
"ds_read_b128 a[168:171], v216 offset:30720\n\t"
"ds_read_b128 a[156:159], v216 offset:23552\n\t"
"ds_read_b128 a[172:175], v216 offset:31744\n\t"
"buffer_load_dwordx4 a[116:119], v248, s[8:11], s20 offen offset:2048 sc1\n\t"
"s_waitcnt lgkmcnt(15)\n\t"
"v_mfma_f32_16x16x32_bf16 v[180:183], a[136:139], a[148:151], v[180:183]\n\t"
"v_mfma_f32_16x16x32_bf16 v[180:183], a[140:143], a[164:167], v[180:183]\n\t"
"s_add_u32 s28, s24, 196608\n\t"
"buffer_store_dwordx4 a[236:239], v234, s[16:19], s28 offen nt\n\t"
"v_mfma_f32_16x16x32_bf16 v[164:167], a[128:131], a[148:151], v[164:167]\n\t"
"v_mfma_f32_16x16x32_bf16 v[164:167], a[132:135], a[164:167], v[164:167]\n\t"
"buffer_load_dwordx4 a[108:111], v248, s[8:11], s20 offen offset:0 sc1\n\t"
"s_mov_b32 s20, 393216\n\t"
"buffer_load_dwordx4 a[104:107], v248, s[8:11], s20 offen offset:2048 sc1\n\t"
"v_mfma_f32_16x16x32_bf16 v[148:151], a[120:123], a[148:151], v[148:151]\n\t"
"v_mfma_f32_16x16x32_bf16 v[148:151], a[124:127], a[164:167], v[148:151]\n\t"
"s_waitcnt lgkmcnt(0)\n\t"
"s_barrier\n\t"
"s_add_u32 s71, s71, 1\n\t"
"v_mov_b32 v215, s71\n\t"
"s_mov_b64 exec, 1\n\t"
"buffer_store_dword v215, off, s[8:11], s70 sc1\n\t"
"s_mov_b64 exec, -1\n\t"
"s_mov_b32 s25, s24\n\t"
"s_add_u32 s24, s24, 3145728\n\t"
"s_mov_b32 s23, s22\n\t"
"s_add_u32 s22, s22, 3145728\n\t"
"s_sub_u32 s21, s21, 1\n\t"
"s_cmp_eq_u32 s21, 1\n\t"
"s_cselect_b32 s22, 0x40000000, s22\n\t"
"v_mfma_f32_16x16x32_bf16 v[152:155], a[120:123], a[152:155], v[152:155]\n\t"
"ds_read_b128 a[144:147], v216 offset:0\n\t"
"v_add_u32 v249, 122368, v227\n\t"
"v_mfma_f32_16x16x32_bf16 v[152:155], a[124:127], a[168:171], v[152:155]\n\t"
"ds_read_b128 a[160:163], v216 offset:8192\n\t"
"ds_read_b128 a[148:151], v216 offset:1024\n\t"
"v_mfma_f32_16x16x32_bf16 v[168:171], a[128:131], a[152:155], v[168:171]\n\t"
"ds_read_b128 a[164:167], v216 offset:9216\n\t"
"v_xor_b32 v250, 32, v249\n\t"
"v_mfma_f32_16x16x32_bf16 v[168:171], a[132:135], a[168:171], v[168:171]\n\t"
"s_mov_b32 s20, 393216\n\t"
"buffer_load_dwordx4 a[96:99], v248, s[8:11], s20 offen offset:0 sc1\n\t"
"v_mfma_f32_16x16x32_bf16 v[184:187], a[136:139], a[152:155], v[184:187]\n\t"
"v_xor_b32 v251, 64, v249\n\t"
"v_xor_b32 v252, 96, v249\n\t"
"v_mfma_f32_16x16x32_bf16 v[184:187], a[140:143], a[168:171], v[184:187]\n\t"
"buffer_load_dwordx4 a[100:103], v248, s[8:11], s20 offen offset:1024 sc1\n\t"
"v_mfma_f32_16x16x32_bf16 v[192:195], a[176:179], a[224:227], 0\n\t"
"v_cvt_pk_bf16_f32 v48, v48, v49\n\t"
"v_cvt_pk_bf16_f32 v49, v50, v51\n\t"
"v_mfma_f32_16x16x32_bf16 v[188:191], a[136:139], a[156:159], v[188:191]\n\t"
"ds_read_b128 a[228:231], v229 offset:0\n\t"
"v_cvt_pk_bf16_f32 v64, v64, v65\n\t"
"v_mfma_f32_16x16x32_bf16 v[188:191], a[140:143], a[172:175], v[188:191]\n\t"
"s_mov_b32 s20, 700416\n\t"
"buffer_load_dwordx4 a[136:139], v248, s[8:11], s20 offen offset:1024 sc1\n\t"
"ds_write_b64 v228, v[48:49] offset:12800\n\t"
"v_mfma_f32_16x16x32_bf16 v[196:199], a[180:183], a[224:227], 0\n\t"
"buffer_load_dwordx4 a[140:143], v248, s[8:11], s20 offen offset:2048 sc1\n\t"
"v_mfma_f32_16x16x32_bf16 v[172:175], a[128:131], a[156:159], v[172:175]\n\t"
"ds_read_b128 a[232:235], v230 offset:0\n\t"
"v_cvt_pk_bf16_f32 v192, v192, v193\n\t"
"v_mfma_f32_16x16x32_bf16 v[172:175], a[132:135], a[172:175], v[172:175]\n\t"
"ds_read_b128 a[152:155], v216 offset:2048\n\t"
"ds_read_b128 a[168:171], v216 offset:10240\n\t"
"v_mfma_f32_16x16x32_bf16 v[156:159], a[120:123], a[156:159], v[156:159]\n\t"
"v_cvt_pk_bf16_f32 v193, v194, v195\n\t"
"ds_read_b128 a[236:239], v231 offset:0\n\t"
"v_mfma_f32_16x16x32_bf16 v[156:159], a[124:127], a[172:175], v[156:159]\n\t"
"buffer_load_dwordx4 a[132:135], v248, s[8:11], s20 offen offset:0 sc1\n\t"
"v_cvt_pk_bf16_f32 v65, v66, v67\n\t"
"v_cvt_pk_bf16_f32 v194, v196, v197\n\t"
"v_mfma_f32_16x16x32_bf16 v[200:203], a[184:187], a[224:227], 0\n\t"
"s_add_u32 s28, s25, 1769472\n\t"
"s_waitcnt lgkmcnt(5)\n\t"
"buffer_store_dwordx4 a[228:231], v232, s[16:19], s28 offen nt\n\t"
"s_waitcnt vmcnt(33)\n\t"
"v_mfma_f32_16x16x32_bf16 v[96:99], a[0:3], a[144:147], 0\n\t"
"v_cvt_pk_bf16_f32 v195, v198, v199\n\t"
"ds_read_b128 a[156:159], v216 offset:3072\n\t"
"v_mfma_f32_16x16x32_bf16 v[96:99], a[4:7], a[160:163], v[96:99]\n\t"
"ds_write_b64 v228, v[64:65] offset:12808\n\t"
"v_mfma_f32_16x16x32_bf16 v[204:207], a[188:191], a[224:227], 0\n\t"
"s_mov_b32 s20, 688128\n\t"
"buffer_load_dwordx4 a[128:131], v248, s[8:11], s20 offen offset:2048 sc1\n\t"
"ds_read_b128 a[172:175], v216 offset:11264\n\t"
"v_cvt_pk_bf16_f32 v80, v80, v81\n\t"
"v_mfma_f32_16x16x32_bf16 v[112:115], a[8:11], a[144:147], 0\n\t"
"v_cvt_pk_bf16_f32 v81, v82, v83\n\t"
"v_cvt_pk_bf16_f32 v200, v200, v201\n\t"
"v_mfma_f32_16x16x32_bf16 v[112:115], a[12:15], a[160:163], v[112:115]\n\t"
"s_add_u32 s28, s25, 1769472\n\t"
"s_waitcnt lgkmcnt(6)\n\t"
"buffer_store_dwordx4 a[232:235], v233, s[16:19], s28 offen nt\n\t"
"v_mfma_f32_16x16x32_bf16 v[128:131], a[16:19], a[144:147], 0\n\t"
"v_cvt_pk_bf16_f32 v201, v202, v203\n\t"
"ds_read_b128 a[144:147], v216 offset:4096\n\t"
"v_mfma_f32_16x16x32_bf16 v[128:131], a[20:23], a[160:163], v[128:131]\n\t"
"buffer_load_dwordx4 a[120:123], v248, s[8:11], s20 offen offset:0 sc1\n\t"
"v_mfma_f32_16x16x32_bf16 v[208:211], a[192:195], a[224:227], 0\n\t"
"ds_write_b128 v221, v[192:195] offset:16384\n\t"
"ds_write_b64 v228, v[80:81] offset:12816\n\t"
"v_mfma_f32_16x16x32_bf16 v[132:135], a[16:19], a[148:151], 0\n\t"
"v_cvt_pk_bf16_f32 v202, v204, v205\n\t"
"v_cvt_pk_bf16_f32 v203, v206, v207\n\t"
"v_mfma_f32_16x16x32_bf16 v[132:135], a[20:23], a[164:167], v[132:135]\n\t"
"buffer_load_dwordx4 a[124:127], v248, s[8:11], s20 offen offset:1024 sc1\n\t"
"v_mfma_f32_16x16x32_bf16 v[116:119], a[8:11], a[148:151], 0\n\t"
"ds_read_b128 a[160:163], v216 offset:12288\n\t"
"v_cvt_pk_bf16_f32 v52, v52, v53\n\t"
"v_mfma_f32_16x16x32_bf16 v[116:119], a[12:15], a[164:167], v[116:119]\n\t"
"s_add_u32 s28, s25, 1769472\n\t"
"s_waitcnt lgkmcnt(7)\n\t"
"buffer_store_dwordx4 a[236:239], v234, s[16:19], s28 offen nt\n\t"
"v_cvt_pk_bf16_f32 v53, v54, v55\n\t"
"v_cvt_pk_bf16_f32 v208, v208, v209\n\t"
"v_mfma_f32_16x16x32_bf16 v[212:215], a[196:199], a[224:227], 0\n\t"
"ds_write_b128 v221, v[200:203] offset:24576\n\t"
"v_mfma_f32_16x16x32_bf16 v[100:103], a[0:3], a[148:151], 0\n\t"
"v_cvt_pk_bf16_f32 v209, v210, v211\n\t"
"ds_read_b128 a[148:151], v216 offset:5120\n\t"
"v_mfma_f32_16x16x32_bf16 v[100:103], a[4:7], a[164:167], v[100:103]\n\t"
"ds_write_b64 v228, v[52:53] offset:19200\n\t"
"v_mfma_f32_16x16x32_bf16 v[192:195], a[200:203], a[224:227], 0\n\t"
"ds_read_b128 a[164:167], v216 offset:13312\n\t"
"v_cvt_pk_bf16_f32 v68, v68, v69\n\t"
"v_cvt_pk_bf16_f32 v69, v70, v71\n\t"
"ds_read_b128 a[228:231], v229 offset:29696\n\t"
"v_mfma_f32_16x16x32_bf16 v[104:107], a[0:3], a[152:155], 0\n\t"
"v_cvt_pk_bf16_f32 v210, v212, v213\n\t"
"v_cvt_pk_bf16_f32 v211, v214, v215\n\t"
"v_mfma_f32_16x16x32_bf16 v[104:107], a[4:7], a[168:171], v[104:107]\n\t"
"ds_write_b64 v228, v[68:69] offset:19208\n\t"
"v_mfma_f32_16x16x32_bf16 v[120:123], a[8:11], a[152:155], 0\n\t"
"ds_read_b128 a[232:235], v230 offset:29696\n\t"
"v_cvt_pk_bf16_f32 v84, v84, v85\n\t"
"v_mfma_f32_16x16x32_bf16 v[120:123], a[12:15], a[168:171], v[120:123]\n\t"
"v_cvt_pk_bf16_f32 v192, v192, v193\n\t"
"v_cvt_pk_bf16_f32 v193, v194, v195\n\t"
"v_mfma_f32_16x16x32_bf16 v[196:199], a[204:207], a[224:227], 0\n\t"
"ds_read_b128 a[236:239], v231 offset:29696\n\t"
"ds_write_b128 v221, v[208:211] offset:20480\n\t"
"v_cvt_pk_bf16_f32 v85, v86, v87\n\t"
"v_mfma_f32_16x16x32_bf16 v[136:139], a[16:19], a[152:155], 0\n\t"
"ds_read_b64_tr_b16 a[176:177], v249 offset:0\n\t"
"v_mfma_f32_16x16x32_bf16 v[136:139], a[20:23], a[168:171], v[136:139]\n\t"
"s_add_u32 s28, s25, 1572864\n\t"
"s_waitcnt lgkmcnt(5)\n\t"
"buffer_store_dwordx4 a[228:231], v232, s[16:19], s28 offen nt\n\t"
"ds_read_b64_tr_b16 a[178:179], v249 offset:512\n\t"
"ds_read_b64_tr_b16 a[180:181], v250 offset:0\n\t"
"v_mfma_f32_16x16x32_bf16 v[140:143], a[16:19], a[156:159], 0\n\t"
"ds_read_b64_tr_b16 a[182:183], v250 offset:512\n\t"
"v_mfma_f32_16x16x32_bf16 v[140:143], a[20:23], a[172:175], v[140:143]\n\t"
"s_mov_b32 s20, 184320\n\t"
"buffer_load_dwordx4 a[16:19], v248, s[8:11], s20 offen offset:1024 sc1\n\t"
"buffer_load_dwordx4 a[20:23], v248, s[8:11], s20 offen offset:2048 sc1\n\t"
"v_mfma_f32_16x16x32_bf16 v[124:127], a[8:11], a[156:159], 0\n\t"
"ds_read_b64_tr_b16 a[184:185], v251 offset:0\n\t"
"v_cvt_pk_bf16_f32 v194, v196, v197\n\t"
"v_mfma_f32_16x16x32_bf16 v[124:127], a[12:15], a[172:175], v[124:127]\n\t"
"ds_read_b64_tr_b16 a[186:187], v251 offset:512\n\t"
"ds_read_b64_tr_b16 a[188:189], v252 offset:0\n\t"
"v_mfma_f32_16x16x32_bf16 v[108:111], a[0:3], a[156:159], 0\n\t"
"ds_read_b64_tr_b16 a[190:191], v252 offset:512\n\t"
"v_cvt_pk_bf16_f32 v195, v198, v199\n\t"
"v_mfma_f32_16x16x32_bf16 v[108:111], a[4:7], a[172:175], v[108:111]\n\t"
"ds_write_b128 v221, v[192:195] offset:28672\n\t"
"s_waitcnt vmcnt(34)\n\t"
"v_mfma_f32_16x16x32_bf16 v[0:3], a[24:27], a[144:147], v[96:99]\n\t"
"ds_read_b64_tr_b16 a[192:193], v249 offset:1152\n\t"
"v_mfma_f32_16x16x32_bf16 v[0:3], a[28:31], a[160:163], v[0:3]\n\t"
"ds_read_b64_tr_b16 a[194:195], v249 offset:1664\n\t"
"ds_read_b64_tr_b16 a[196:197], v250 offset:1152\n\t"
"ds_read_b64_tr_b16 a[198:199], v250 offset:1664\n\t"
"ds_read_b64_tr_b16 a[200:201], v251 offset:1152\n\t"
"ds_read_b64_tr_b16 a[202:203], v251 offset:1664\n\t"
"ds_read_b64_tr_b16 a[204:205], v252 offset:1152\n\t"
"ds_read_b64_tr_b16 a[206:207], v252 offset:1664\n\t"
"ds_write_b64 v228, v[84:85] offset:19216\n\t"
"buffer_load_dwordx4 a[12:15], v248, s[8:11], s20 offen offset:0 sc1\n\t"
"v_mfma_f32_16x16x32_bf16 v[16:19], a[32:35], a[144:147], v[112:115]\n\t"
"ds_read_b128 a[152:155], v216 offset:6144\n\t"
"v_mfma_f32_16x16x32_bf16 v[16:19], a[36:39], a[160:163], v[16:19]\n\t"
"s_add_u32 s28, s25, 1572864\n\t"
"s_waitcnt lgkmcnt(15)\n\t"
"buffer_store_dwordx4 a[232:235], v233, s[16:19], s28 offen nt\n\t"
"v_mfma_f32_16x16x32_bf16 v[32:35], a[40:43], a[144:147], v[128:131]\n\t"
"ds_read_b128 a[168:171], v216 offset:14336\n\t"
"v_mfma_f32_16x16x32_bf16 v[32:35], a[44:47], a[160:163], v[32:35]\n\t"
"ds_read_b128 a[156:159], v216 offset:7168\n\t"
"ds_read_b128 a[172:175], v216 offset:15360\n\t"
"s_mov_b32 s20, 172032\n\t"
"buffer_load_dwordx4 a[8:11], v248, s[8:11], s20 offen offset:2048 sc1\n\t"
"v_mfma_f32_16x16x32_bf16 v[36:39], a[40:43], a[148:151], v[132:135]\n\t"
"v_mfma_f32_16x16x32_bf16 v[36:39], a[44:47], a[164:167], v[36:39]\n\t"
"buffer_load_dwordx4 a[0:3], v248, s[8:11], s20 offen offset:0 sc1\n\t"
"v_mfma_f32_16x16x32_bf16 v[20:23], a[32:35], a[148:151], v[116:119]\n\t"
"v_mfma_f32_16x16x32_bf16 v[20:23], a[36:39], a[164:167], v[20:23]\n\t"
"s_add_u32 s28, s25, 1572864\n\t"
"buffer_store_dwordx4 a[236:239], v234, s[16:19], s28 offen nt\n\t"
"buffer_load_dwordx4 a[4:7], v248, s[8:11], s20 offen offset:1024 sc1\n\t"
"v_mfma_f32_16x16x32_bf16 v[4:7], a[24:27], a[148:151], v[100:103]\n\t"
"v_mfma_f32_16x16x32_bf16 v[4:7], a[28:31], a[164:167], v[4:7]\n\t"
"s_waitcnt lgkmcnt(0)\n\t"
"s_barrier\n\t"
"v_mfma_f32_16x16x32_bf16 v[8:11], a[24:27], a[152:155], v[104:107]\n\t"
"ds_read_b128 a[144:147], v216 offset:16384\n\t"
"v_add_u32 v249, 80896, v226\n\t"
"v_mfma_f32_16x16x32_bf16 v[8:11], a[28:31], a[168:171], v[8:11]\n\t"
"ds_read_b128 a[160:163], v216 offset:24576\n\t"
"ds_read_b128 a[148:151], v216 offset:17408\n\t"
"v_mfma_f32_16x16x32_bf16 v[24:27], a[32:35], a[152:155], v[120:123]\n\t"
"ds_read_b128 a[164:167], v216 offset:25600\n\t"
"v_xor_b32 v250, 32, v249\n\t"
"v_mfma_f32_16x16x32_bf16 v[24:27], a[36:39], a[168:171], v[24:27]\n\t"
"v_xor_b32 v251, 64, v249\n\t"
"v_xor_b32 v252, 96, v249\n\t"
"v_mfma_f32_16x16x32_bf16 v[40:43], a[40:43], a[152:155], v[136:139]\n\t"
"v_cvt_pk_bf16_f32 v144, v144, v145\n\t"
"v_cvt_pk_bf16_f32 v145, v146, v147\n\t"
"v_mfma_f32_16x16x32_bf16 v[40:43], a[44:47], a[168:171], v[40:43]\n\t"
"ds_write_b64 v228, v[144:145] offset:0\n\t"
"v_mfma_f32_16x16x32_bf16 v[200:203], a[176:179], a[224:227], 0\n\t"
"v_cvt_pk_bf16_f32 v160, v160, v161\n\t"
"v_cvt_pk_bf16_f32 v161, v162, v163\n\t"
"ds_read_b128 a[228:231], v229 offset:12800\n\t"
"v_cvt_pk_bf16_f32 v176, v176, v177\n\t"
"v_mfma_f32_16x16x32_bf16 v[44:47], a[40:43], a[156:159], v[140:143]\n\t"
"v_cvt_pk_bf16_f32 v177, v178, v179\n\t"
"ds_read_b128 a[232:235], v230 offset:12800\n\t"
"v_mfma_f32_16x16x32_bf16 v[44:47], a[44:47], a[172:175], v[44:47]\n\t"
"s_mov_b32 s20, 282624\n\t"
"buffer_load_dwordx4 a[40:43], v248, s[8:11], s20 offen offset:1024 sc1\n\t"
"v_mfma_f32_16x16x32_bf16 v[204:207], a[180:183], a[224:227], 0\n\t"
"buffer_load_dwordx4 a[44:47], v248, s[8:11], s20 offen offset:2048 sc1\n\t"
"ds_write_b64 v228, v[160:161] offset:8\n\t"
"v_mfma_f32_16x16x32_bf16 v[28:31], a[32:35], a[156:159], v[124:127]\n\t"
"ds_read_b128 a[152:155], v216 offset:18432\n\t"
"v_cvt_pk_bf16_f32 v200, v200, v201\n\t"
"v_mfma_f32_16x16x32_bf16 v[28:31], a[36:39], a[172:175], v[28:31]\n\t"
"buffer_load_dwordx4 a[36:39], v248, s[8:11], s20 offen offset:0 sc1\n\t"
"v_mfma_f32_16x16x32_bf16 v[12:15], a[24:27], a[156:159], v[108:111]\n\t"
"ds_read_b128 a[168:171], v216 offset:26624\n\t"
"v_cvt_pk_bf16_f32 v201, v202, v203\n\t"
"v_mfma_f32_16x16x32_bf16 v[12:15], a[28:31], a[172:175], v[12:15]\n\t"
"s_mov_b32 s20, 270336\n\t"
"buffer_load_dwordx4 a[32:35], v248, s[8:11], s20 offen offset:2048 sc1\n\t"
"ds_write_b64 v228, v[176:177] offset:16\n\t"
"v_mfma_f32_16x16x32_bf16 v[208:211], a[184:187], a[224:227], 0\n\t"
"v_pk_fma_f32 v[96:97], v[96:97], s[38:39], v[0:1] neg_lo:[1,0,1] neg_hi:[1,0,1]\n\t"
"v_pk_fma_f32 v[98:99], v[98:99], s[38:39], v[2:3] neg_lo:[1,0,1] neg_hi:[1,0,1]\n\t"
"v_pk_fma_f32 v[112:113], v[112:113], s[38:39], v[16:17] neg_lo:[1,0,1] neg_hi:[1,0,1]\n\t"
"v_pk_fma_f32 v[114:115], v[114:115], s[38:39], v[18:19] neg_lo:[1,0,1] neg_hi:[1,0,1]\n\t"
"v_pk_fma_f32 v[128:129], v[128:129], s[38:39], v[32:33] neg_lo:[1,0,1] neg_hi:[1,0,1]\n\t"
"v_pk_fma_f32 v[130:131], v[130:131], s[38:39], v[34:35] neg_lo:[1,0,1] neg_hi:[1,0,1]\n\t"
"v_pk_fma_f32 v[132:133], v[132:133], s[38:39], v[36:37] neg_lo:[1,0,1] neg_hi:[1,0,1]\n\t"
"v_pk_fma_f32 v[134:135], v[134:135], s[38:39], v[38:39] neg_lo:[1,0,1] neg_hi:[1,0,1]\n\t"
"v_pk_fma_f32 v[116:117], v[116:117], s[38:39], v[20:21] neg_lo:[1,0,1] neg_hi:[1,0,1]\n\t"
"v_pk_fma_f32 v[118:119], v[118:119], s[38:39], v[22:23] neg_lo:[1,0,1] neg_hi:[1,0,1]\n\t"
"v_pk_fma_f32 v[100:101], v[100:101], s[38:39], v[4:5] neg_lo:[1,0,1] neg_hi:[1,0,1]\n\t"
"v_pk_fma_f32 v[102:103], v[102:103], s[38:39], v[6:7] neg_lo:[1,0,1] neg_hi:[1,0,1]\n\t"
"v_pk_fma_f32 v[104:105], v[104:105], s[38:39], v[8:9] neg_lo:[1,0,1] neg_hi:[1,0,1]\n\t"
"v_pk_fma_f32 v[106:107], v[106:107], s[38:39], v[10:11] neg_lo:[1,0,1] neg_hi:[1,0,1]\n\t"
"v_pk_fma_f32 v[120:121], v[120:121], s[38:39], v[24:25] neg_lo:[1,0,1] neg_hi:[1,0,1]\n\t"
"v_pk_fma_f32 v[122:123], v[122:123], s[38:39], v[26:27] neg_lo:[1,0,1] neg_hi:[1,0,1]\n\t"
"v_pk_fma_f32 v[136:137], v[136:137], s[38:39], v[40:41] neg_lo:[1,0,1] neg_hi:[1,0,1]\n\t"
"v_pk_fma_f32 v[138:139], v[138:139], s[38:39], v[42:43] neg_lo:[1,0,1] neg_hi:[1,0,1]\n\t"
"v_pk_fma_f32 v[140:141], v[140:141], s[38:39], v[44:45] neg_lo:[1,0,1] neg_hi:[1,0,1]\n\t"
"v_pk_fma_f32 v[142:143], v[142:143], s[38:39], v[46:47] neg_lo:[1,0,1] neg_hi:[1,0,1]\n\t"
"v_pk_fma_f32 v[124:125], v[124:125], s[38:39], v[28:29] neg_lo:[1,0,1] neg_hi:[1,0,1]\n\t"
"v_pk_fma_f32 v[126:127], v[126:127], s[38:39], v[30:31] neg_lo:[1,0,1] neg_hi:[1,0,1]\n\t"
"v_pk_fma_f32 v[108:109], v[108:109], s[38:39], v[12:13] neg_lo:[1,0,1] neg_hi:[1,0,1]\n\t"
"v_pk_fma_f32 v[110:111], v[110:111], s[38:39], v[14:15] neg_lo:[1,0,1] neg_hi:[1,0,1]\n\t"
"buffer_load_dwordx4 a[24:27], v248, s[8:11], s20 offen offset:0 sc1\n\t"
"s_waitcnt vmcnt(39) lgkmcnt(9)\n\t"
"v_mfma_f32_16x16x32_bf16 v[0:3], a[48:51], a[144:147], v[0:3]\n\t"
"ds_read_b128 a[236:239], v231 offset:12800\n\t"
"v_cvt_pk_bf16_f32 v148, v148, v149\n\t"
"v_mfma_f32_16x16x32_bf16 v[0:3], a[52:55], a[160:163], v[0:3]\n\t"
"v_cvt_pk_bf16_f32 v149, v150, v151\n\t"
"buffer_load_dwordx4 a[28:31], v248, s[8:11], s20 offen offset:1024 sc1\n\t"
"v_cvt_pk_bf16_f32 v202, v204, v205\n\t"
"v_mfma_f32_16x16x32_bf16 v[212:215], a[188:191], a[224:227], 0\n\t"
"ds_write_b64 v228, v[148:149] offset:6400\n\t"
"v_mfma_f32_16x16x32_bf16 v[16:19], a[56:59], a[144:147], v[16:19]\n\t"
"v_cvt_pk_bf16_f32 v203, v206, v207\n\t"
"v_cvt_pk_bf16_f32 v164, v164, v165\n\t"
"v_mfma_f32_16x16x32_bf16 v[16:19], a[60:63], a[160:163], v[16:19]\n\t"
"v_cvt_pk_bf16_f32 v165, v166, v167\n\t"
"ds_read_b128 a[156:159], v216 offset:19456\n\t"
"ds_read_b128 a[172:175], v216 offset:27648\n\t"
"v_cvt_pk_bf16_f32 v180, v180, v181\n\t"
"v_mfma_f32_16x16x32_bf16 v[32:35], a[64:67], a[144:147], v[32:35]\n\t"
"v_cvt_pk_bf16_f32 v181, v182, v183\n\t"
"v_cvt_pk_bf16_f32 v208, v208, v209\n\t"
"v_mfma_f32_16x16x32_bf16 v[32:35], a[68:71], a[160:163], v[32:35]\n\t"
"ds_write_b64 v228, v[164:165] offset:6408\n\t"
"ds_write_b128 v221, v[200:203] offset:0\n\t"
"v_mfma_f32_16x16x32_bf16 v[192:195], a[192:195], a[224:227], 0\n\t"
"v_cvt_pk_bf16_f32 v209, v210, v211\n\t"
"v_cvt_pk_bf16_f32 v152, v152, v153\n\t"
"s_waitcnt lgkmcnt(13)\n\t"
"v_mfma_f32_16x16x32_bf16 v[36:39], a[64:67], a[148:151], v[36:39]\n\t"
"v_cvt_pk_bf16_f32 v153, v154, v155\n\t"
"v_cvt_pk_bf16_f32 v210, v212, v213\n\t"
"v_mfma_f32_16x16x32_bf16 v[36:39], a[68:71], a[164:167], v[36:39]\n\t"
"ds_write_b64 v228, v[180:181] offset:6416\n\t"
"s_add_u32 s28, s25, 384\n\t"
"s_waitcnt lgkmcnt(12)\n\t"
"buffer_store_dwordx4 a[228:231], v232, s[16:19], s28 offen nt\n\t"
"v_mfma_f32_16x16x32_bf16 v[20:23], a[56:59], a[148:151], v[20:23]\n\t"
"v_cvt_pk_bf16_f32 v211, v214, v215\n\t"
"ds_read_b128 a[144:147], v216 offset:20480\n\t"
"v_mfma_f32_16x16x32_bf16 v[20:23], a[60:63], a[164:167], v[20:23]\n\t"
"ds_read_b128 a[160:163], v216 offset:28672\n\t"
"v_cvt_pk_bf16_f32 v168, v168, v169\n\t"
"v_mfma_f32_16x16x32_bf16 v[196:199], a[196:199], a[224:227], 0\n\t"
"ds_write_b64 v228, v[152:153] offset:29696\n\t"
"v_cvt_pk_bf16_f32 v169, v170, v171\n\t"
"v_cvt_pk_bf16_f32 v192, v192, v193\n\t"
"v_mfma_f32_16x16x32_bf16 v[4:7], a[48:51], a[148:151], v[4:7]\n\t"
"v_cvt_pk_bf16_f32 v193, v194, v195\n\t"
"v_cvt_pk_bf16_f32 v184, v184, v185\n\t"
"v_mfma_f32_16x16x32_bf16 v[4:7], a[52:55], a[164:167], v[4:7]\n\t"
"ds_write_b128 v221, v[208:211] offset:8192\n\t"
"v_mfma_f32_16x16x32_bf16 v[200:203], a[200:203], a[224:227], 0\n\t"
"ds_write_b64 v228, v[168:169] offset:29704\n\t"
"v_cvt_pk_bf16_f32 v185, v186, v187\n\t"
"ds_read_b128 a[148:151], v216 offset:21504\n\t"
"s_waitcnt lgkmcnt(14)\n\t"
"v_mfma_f32_16x16x32_bf16 v[8:11], a[48:51], a[152:155], v[8:11]\n\t"
"ds_read_b128 a[164:167], v216 offset:29696\n\t"
"v_cvt_pk_bf16_f32 v156, v156, v157\n\t"
"v_mfma_f32_16x16x32_bf16 v[8:11], a[52:55], a[168:171], v[8:11]\n\t"
"ds_write_b64 v228, v[184:185] offset:29712\n\t"
"v_mfma_f32_16x16x32_bf16 v[24:27], a[56:59], a[152:155], v[24:27]\n\t"
"v_cvt_pk_bf16_f32 v157, v158, v159\n\t"
"v_cvt_pk_bf16_f32 v194, v196, v197\n\t"
"v_mfma_f32_16x16x32_bf16 v[24:27], a[60:63], a[168:171], v[24:27]\n\t"
"v_cvt_pk_bf16_f32 v195, v198, v199\n\t"
"ds_write_b64 v228, v[156:157] offset:36096\n\t"
"v_cvt_pk_bf16_f32 v172, v172, v173\n\t"
"v_mfma_f32_16x16x32_bf16 v[204:207], a[204:207], a[224:227], 0\n\t"
"v_cvt_pk_bf16_f32 v173, v174, v175\n\t"
"v_cvt_pk_bf16_f32 v200, v200, v201\n\t"
"v_mfma_f32_16x16x32_bf16 v[40:43], a[64:67], a[152:155], v[40:43]\n\t"
"v_cvt_pk_bf16_f32 v201, v202, v203\n\t"
"v_cvt_pk_bf16_f32 v188, v188, v189\n\t"
"v_mfma_f32_16x16x32_bf16 v[40:43], a[68:71], a[168:171], v[40:43]\n\t"
"s_add_u32 s28, s25, 384\n\t"
"buffer_store_dwordx4 a[232:235], v233, s[16:19], s28 offen nt\n\t"
"ds_write_b64 v228, v[172:173] offset:36104\n\t"
"s_waitcnt lgkmcnt(13)\n\t"
"v_mfma_f32_16x16x32_bf16 v[44:47], a[64:67], a[156:159], v[44:47]\n\t"
"v_cvt_pk_bf16_f32 v189, v190, v191\n\t"
"ds_read_b64_tr_b16 a[176:177], v251 offset:20736\n\t"
"v_mfma_f32_16x16x32_bf16 v[44:47], a[68:71], a[172:175], v[44:47]\n\t"
"s_mov_b32 s20, 86016\n\t"
"buffer_load_dwordx4 a[64:67], v248, s[8:11], s20 offen offset:1024 sc1\n\t"
"ds_write_b128 v221, v[192:195] offset:4096\n\t"
"v_mfma_f32_16x16x32_bf16 v[28:31], a[56:59], a[156:159], v[28:31]\n\t"
"ds_read_b64_tr_b16 a[178:179], v251 offset:21248\n\t"
"v_cvt_pk_bf16_f32 v202, v204, v205\n\t"
"v_mfma_f32_16x16x32_bf16 v[28:31], a[60:63], a[172:175], v[28:31]\n\t"
"buffer_load_dwordx4 a[68:71], v248, s[8:11], s20 offen offset:2048 sc1\n\t"
"v_mfma_f32_16x16x32_bf16 v[12:15], a[48:51], a[156:159], v[12:15]\n\t"
"ds_read_b64_tr_b16 a[180:181], v252 offset:20736\n\t"
"v_cvt_pk_bf16_f32 v203, v206, v207\n\t"
"v_mfma_f32_16x16x32_bf16 v[12:15], a[52:55], a[172:175], v[12:15]\n\t"
"buffer_load_dwordx4 a[60:63], v248, s[8:11], s20 offen offset:0 sc1\n\t"
"ds_write_b128 v221, v[200:203] offset:12288\n\t"
"s_waitcnt vmcnt(37) lgkmcnt(13)\n\t"
"v_mfma_f32_16x16x32_bf16 v[96:99], a[72:75], a[144:147], v[96:99]\n\t"
"ds_read_b64_tr_b16 a[182:183], v252 offset:21248\n\t"
"v_mfma_f32_16x16x32_bf16 v[96:99], a[76:79], a[160:163], v[96:99]\n\t"
"ds_read_b64_tr_b16 a[184:185], v249 offset:23040\n\t"
"ds_read_b64_tr_b16 a[186:187], v249 offset:23552\n\t"
"ds_read_b64_tr_b16 a[188:189], v250 offset:23040\n\t"
"ds_read_b64_tr_b16 a[190:191], v250 offset:23552\n\t"
"ds_read_b64_tr_b16 a[192:193], v249 offset:20736\n\t"
"ds_read_b64_tr_b16 a[194:195], v249 offset:21248\n\t"
"ds_read_b64_tr_b16 a[196:197], v250 offset:20736\n\t"
"ds_read_b64_tr_b16 a[198:199], v250 offset:21248\n\t"
"ds_read_b64_tr_b16 a[200:201], v251 offset:23040\n\t"
"ds_read_b64_tr_b16 a[202:203], v251 offset:23552\n\t"
"ds_read_b64_tr_b16 a[204:205], v252 offset:23040\n\t"
"ds_read_b64_tr_b16 a[206:207], v252 offset:23552\n\t"
"ds_write_b64 v228, v[188:189] offset:36112\n\t"
"s_mov_b32 s20, 73728\n\t"
"buffer_load_dwordx4 a[56:59], v248, s[8:11], s20 offen offset:2048 sc1\n\t"
"v_mfma_f32_16x16x32_bf16 v[112:115], a[80:83], a[144:147], v[112:115]\n\t"
"v_mfma_f32_16x16x32_bf16 v[112:115], a[84:87], a[160:163], v[112:115]\n\t"
"buffer_load_dwordx4 a[48:51], v248, s[8:11], s20 offen offset:0 sc1\n\t"
"buffer_load_dwordx4 a[52:55], v248, s[8:11], s20 offen offset:1024 sc1\n\t"
"v_mfma_f32_16x16x32_bf16 v[128:131], a[88:91], a[144:147], v[128:131]\n\t"
"v_mfma_f32_16x16x32_bf16 v[128:131], a[92:95], a[160:163], v[128:131]\n\t"
"ds_read_b128 a[152:155], v216 offset:22528\n\t"
"ds_read_b128 a[168:171], v216 offset:30720\n\t"
"ds_read_b128 a[156:159], v216 offset:23552\n\t"
"ds_read_b128 a[172:175], v216 offset:31744\n\t"
"s_waitcnt lgkmcnt(15)\n\t"
"v_mfma_f32_16x16x32_bf16 v[132:135], a[88:91], a[148:151], v[132:135]\n\t"
"v_mfma_f32_16x16x32_bf16 v[132:135], a[92:95], a[164:167], v[132:135]\n\t"
"v_mfma_f32_16x16x32_bf16 v[116:119], a[80:83], a[148:151], v[116:119]\n\t"
"v_mfma_f32_16x16x32_bf16 v[116:119], a[84:87], a[164:167], v[116:119]\n\t"
"s_add_u32 s28, s25, 384\n\t"
"buffer_store_dwordx4 a[236:239], v234, s[16:19], s28 offen nt\n\t"
"v_mfma_f32_16x16x32_bf16 v[100:103], a[72:75], a[148:151], v[100:103]\n\t"
"v_mfma_f32_16x16x32_bf16 v[100:103], a[76:79], a[164:167], v[100:103]\n\t"
"s_waitcnt lgkmcnt(0)\n\t"
"s_barrier\n\t"
"v_mfma_f32_16x16x32_bf16 v[104:107], a[72:75], a[152:155], v[104:107]\n\t"
"ds_read_b128 a[144:147], v216 offset:0\n\t"
"v_add_u32 v249, 80896, v227\n\t"
"v_mfma_f32_16x16x32_bf16 v[104:107], a[76:79], a[168:171], v[104:107]\n\t"
"ds_read_b128 a[160:163], v216 offset:8192\n\t"
"ds_read_b128 a[148:151], v216 offset:1024\n\t"
"v_mfma_f32_16x16x32_bf16 v[120:123], a[80:83], a[152:155], v[120:123]\n\t"
"ds_read_b128 a[164:167], v216 offset:9216\n\t"
"v_xor_b32 v250, 32, v249\n\t"
"v_mfma_f32_16x16x32_bf16 v[120:123], a[84:87], a[168:171], v[120:123]\n\t"
"v_xor_b32 v251, 64, v249\n\t"
"v_xor_b32 v252, 96, v249\n\t"
"v_mfma_f32_16x16x32_bf16 v[136:139], a[88:91], a[152:155], v[136:139]\n\t"
"v_cvt_pk_bf16_f32 v56, v56, v57\n\t"
"v_cvt_pk_bf16_f32 v57, v58, v59\n\t"
"v_mfma_f32_16x16x32_bf16 v[136:139], a[92:95], a[168:171], v[136:139]\n\t"
"ds_read_b128 a[228:231], v229 offset:0\n\t"
"ds_read_b128 a[152:155], v216 offset:2048\n\t"
"v_mfma_f32_16x16x32_bf16 v[208:211], a[176:179], a[220:223], 0\n\t"
"ds_write_b64 v228, v[56:57] offset:12800\n\t"
"v_mfma_f32_16x16x32_bf16 v[140:143], a[88:91], a[156:159], v[140:143]\n\t"
"ds_read_b128 a[232:235], v230 offset:0\n\t"
"v_cvt_pk_bf16_f32 v72, v72, v73\n\t"
"v_mfma_f32_16x16x32_bf16 v[140:143], a[92:95], a[172:175], v[140:143]\n\t"
"s_mov_b32 s20, 380928\n\t"
"buffer_load_dwordx4 a[88:91], v248, s[8:11], s20 offen offset:1024 sc1\n\t"
"v_mfma_f32_16x16x32_bf16 v[212:215], a[180:183], a[220:223], 0\n\t"
"buffer_load_dwordx4 a[92:95], v248, s[8:11], s20 offen offset:2048 sc1\n\t"
"v_mfma_f32_16x16x32_bf16 v[124:127], a[80:83], a[156:159], v[124:127]\n\t"
"ds_read_b128 a[168:171], v216 offset:10240\n\t"
"v_cvt_pk_bf16_f32 v208, v208, v209\n\t"
"v_mfma_f32_16x16x32_bf16 v[124:127], a[84:87], a[172:175], v[124:127]\n\t"
"buffer_load_dwordx4 a[84:87], v248, s[8:11], s20 offen offset:0 sc1\n\t"
"v_cvt_pk_bf16_f32 v209, v210, v211\n\t"
"ds_read_b128 a[236:239], v231 offset:0\n\t"
"v_mfma_f32_16x16x32_bf16 v[108:111], a[72:75], a[156:159], v[108:111]\n\t"
"v_cvt_pk_bf16_f32 v73, v74, v75\n\t"
"ds_read_b128 a[156:159], v216 offset:3072\n\t"
"v_mfma_f32_16x16x32_bf16 v[108:111], a[76:79], a[172:175], v[108:111]\n\t"
"s_mov_b32 s20, 368640\n\t"
"buffer_load_dwordx4 a[80:83], v248, s[8:11], s20 offen offset:2048 sc1\n\t"
"v_mfma_f32_16x16x32_bf16 v[192:195], a[184:187], a[220:223], 0\n\t"
"buffer_load_dwordx4 a[72:75], v248, s[8:11], s20 offen offset:0 sc1\n\t"
"s_waitcnt vmcnt(38) lgkmcnt(9)\n\t"
"v_mfma_f32_16x16x32_bf16 v[0:3], a[96:99], a[144:147], v[0:3]\n\t"
"v_cvt_pk_bf16_f32 v210, v212, v213\n\t"
"v_cvt_pk_bf16_f32 v211, v214, v215\n\t"
"v_mfma_f32_16x16x32_bf16 v[0:3], a[100:103], a[160:163], v[0:3]\n\t"
"buffer_load_dwordx4 a[76:79], v248, s[8:11], s20 offen offset:1024 sc1\n\t"
"s_add_u32 s28, s25, 196992\n\t"
"s_waitcnt lgkmcnt(6)\n\t"
"buffer_store_dwordx4 a[228:231], v232, s[16:19], s28 offen nt\n\t"
"v_mfma_f32_16x16x32_bf16 v[196:199], a[188:191], a[220:223], 0\n\t"
"ds_write_b64 v228, v[72:73] offset:12808\n\t"
"v_mfma_f32_16x16x32_bf16 v[16:19], a[104:107], a[144:147], v[16:19]\n\t"
"ds_read_b128 a[172:175], v216 offset:11264\n\t"
"v_cvt_pk_bf16_f32 v88, v88, v89\n\t"
"v_mfma_f32_16x16x32_bf16 v[16:19], a[108:111], a[160:163], v[16:19]\n\t"
"s_add_u32 s28, s25, 196992\n\t"
"s_waitcnt lgkmcnt(5)\n\t"
"buffer_store_dwordx4 a[232:235], v233, s[16:19], s28 offen nt\n\t"
"v_mfma_f32_16x16x32_bf16 v[32:35], a[112:115], a[144:147], v[32:35]\n\t"
"v_cvt_pk_bf16_f32 v89, v90, v91\n\t"
"v_cvt_pk_bf16_f32 v192, v192, v193\n\t"
"v_mfma_f32_16x16x32_bf16 v[32:35], a[116:119], a[160:163], v[32:35]\n\t"
"ds_write_b128 v217, v[208:211] offset:16384\n\t"
"v_cvt_pk_bf16_f32 v193, v194, v195\n\t"
"v_cvt_pk_bf16_f32 v194, v196, v197\n\t"
"v_mfma_f32_16x16x32_bf16 v[200:203], a[192:195], a[220:223], 0\n\t"
"ds_write_b64 v228, v[88:89] offset:12816\n\t"
"v_mfma_f32_16x16x32_bf16 v[36:39], a[112:115], a[148:151], v[36:39]\n\t"
"v_cvt_pk_bf16_f32 v195, v198, v199\n\t"
"ds_read_b128 a[144:147], v216 offset:4096\n\t"
"v_mfma_f32_16x16x32_bf16 v[36:39], a[116:119], a[164:167], v[36:39]\n\t"
"ds_read_b128 a[160:163], v216 offset:12288\n\t"
"v_cvt_pk_bf16_f32 v60, v60, v61\n\t"
"v_mfma_f32_16x16x32_bf16 v[20:23], a[104:107], a[148:151], v[20:23]\n\t"
"v_cvt_pk_bf16_f32 v61, v62, v63\n\t"
"v_cvt_pk_bf16_f32 v76, v76, v77\n\t"
"v_mfma_f32_16x16x32_bf16 v[20:23], a[108:111], a[164:167], v[20:23]\n\t"
"s_add_u32 s28, s25, 196992\n\t"
"s_waitcnt lgkmcnt(7)\n\t"
"buffer_store_dwordx4 a[236:239], v234, s[16:19], s28 offen nt\n\t"
"ds_write_b128 v219, v[192:195] offset:16384\n\t"
"v_mfma_f32_16x16x32_bf16 v[204:207], a[196:199], a[220:223], 0\n\t"
"ds_write_b64 v228, v[60:61] offset:19200\n\t"
"v_mfma_f32_16x16x32_bf16 v[4:7], a[96:99], a[148:151], v[4:7]\n\t"
"v_cvt_pk_bf16_f32 v200, v200, v201\n\t"
"v_cvt_pk_bf16_f32 v201, v202, v203\n\t"
"v_mfma_f32_16x16x32_bf16 v[4:7], a[100:103], a[164:167], v[4:7]\n\t"
"ds_read_b128 a[148:151], v216 offset:5120\n\t"
"ds_read_b128 a[164:167], v216 offset:13312\n\t"
"v_mfma_f32_16x16x32_bf16 v[208:211], a[200:203], a[220:223], 0\n\t"
"v_cvt_pk_bf16_f32 v77, v78, v79\n\t"
"ds_read_b128 a[228:231], v229 offset:29696\n\t"
"v_mfma_f32_16x16x32_bf16 v[8:11], a[96:99], a[152:155], v[8:11]\n\t"
"v_cvt_pk_bf16_f32 v202, v204, v205\n\t"
"v_cvt_pk_bf16_f32 v203, v206, v207\n\t"
"v_mfma_f32_16x16x32_bf16 v[8:11], a[100:103], a[168:171], v[8:11]\n\t"
"ds_write_b64 v228, v[76:77] offset:19208\n\t"
"ds_read_b128 a[232:235], v230 offset:29696\n\t"
"ds_read_b128 a[236:239], v231 offset:29696\n\t"
"v_mfma_f32_16x16x32_bf16 v[24:27], a[104:107], a[152:155], v[24:27]\n\t"
"v_cvt_pk_bf16_f32 v92, v92, v93\n\t"
"v_cvt_pk_bf16_f32 v93, v94, v95\n\t"
"v_mfma_f32_16x16x32_bf16 v[24:27], a[108:111], a[168:171], v[24:27]\n\t"
"v_cvt_pk_bf16_f32 v208, v208, v209\n\t"
"v_cvt_pk_bf16_f32 v209, v210, v211\n\t"
"v_mfma_f32_16x16x32_bf16 v[212:215], a[204:207], a[220:223], 0\n\t"
"ds_write_b128 v217, v[200:203] offset:24576\n\t"
"v_mfma_f32_16x16x32_bf16 v[40:43], a[112:115], a[152:155], v[40:43]\n\t"
"ds_read_b64_tr_b16 a[176:177], v251 offset:20736\n\t"
"v_mfma_f32_16x16x32_bf16 v[40:43], a[116:119], a[168:171], v[40:43]\n\t"
"s_add_u32 s28, s25, 1769856\n\t"
"s_waitcnt lgkmcnt(5)\n\t"
"buffer_store_dwordx4 a[228:231], v232, s[16:19], s28 offen nt\n\t"
"ds_read_b64_tr_b16 a[178:179], v251 offset:21248\n\t"
"ds_read_b64_tr_b16 a[180:181], v252 offset:20736\n\t"
"v_mfma_f32_16x16x32_bf16 v[44:47], a[112:115], a[156:159], v[44:47]\n\t"
"ds_read_b64_tr_b16 a[182:183], v252 offset:21248\n\t"
"v_mfma_f32_16x16x32_bf16 v[44:47], a[116:119], a[172:175], v[44:47]\n\t"
"s_mov_b32 s20, 479232\n\t"
"buffer_load_dwordx4 a[112:115], v248, s[8:11], s20 offen offset:1024 sc1\n\t"
"buffer_load_dwordx4 a[116:119], v248, s[8:11], s20 offen offset:2048 sc1\n\t"
"v_mfma_f32_16x16x32_bf16 v[28:31], a[104:107], a[156:159], v[28:31]\n\t"
"ds_read_b64_tr_b16 a[184:185], v249 offset:20736\n\t"
"v_cvt_pk_bf16_f32 v210, v212, v213\n\t"
"v_mfma_f32_16x16x32_bf16 v[28:31], a[108:111], a[172:175], v[28:31]\n\t"
"buffer_load_dwordx4 a[108:111], v248, s[8:11], s20 offen offset:0 sc1\n\t"
"v_mfma_f32_16x16x32_bf16 v[12:15], a[96:99], a[156:159], v[12:15]\n\t"
"ds_read_b64_tr_b16 a[186:187], v249 offset:21248\n\t"
"v_cvt_pk_bf16_f32 v211, v214, v215\n\t"
"v_mfma_f32_16x16x32_bf16 v[12:15], a[100:103], a[172:175], v[12:15]\n\t"
"ds_write_b128 v219, v[208:211] offset:24576\n\t"
"ds_read_b64_tr_b16 a[188:189], v250 offset:20736\n\t"
"ds_read_b64_tr_b16 a[190:191], v250 offset:21248\n\t"
"s_waitcnt vmcnt(38)\n\t"
"v_mfma_f32_16x16x32_bf16 v[96:99], a[120:123], a[144:147], v[96:99]\n\t"
"ds_read_b64_tr_b16 a[192:193], v251 offset:21888\n\t"
"v_mfma_f32_16x16x32_bf16 v[96:99], a[124:127], a[160:163], v[96:99]\n\t"
"ds_read_b64_tr_b16 a[194:195], v251 offset:22400\n\t"
"ds_read_b64_tr_b16 a[196:197], v252 offset:21888\n\t"
"ds_read_b64_tr_b16 a[198:199], v252 offset:22400\n\t"
"ds_read_b64_tr_b16 a[200:201], v249 offset:21888\n\t"
"ds_read_b64_tr_b16 a[202:203], v249 offset:22400\n\t"
"ds_read_b64_tr_b16 a[204:205], v250 offset:21888\n\t"
"ds_read_b64_tr_b16 a[206:207], v250 offset:22400\n\t"
"ds_write_b64 v228, v[92:93] offset:19216\n\t"
"s_mov_b32 s20, 466944\n\t"
"buffer_load_dwordx4 a[104:107], v248, s[8:11], s20 offen offset:2048 sc1\n\t"
"v_mfma_f32_16x16x32_bf16 v[112:115], a[128:131], a[144:147], v[112:115]\n\t"
"v_mfma_f32_16x16x32_bf16 v[112:115], a[132:135], a[160:163], v[112:115]\n\t"
"buffer_load_dwordx4 a[96:99], v248, s[8:11], s20 offen offset:0 sc1\n\t"
"s_add_u32 s28, s25, 1769856\n\t"
"s_waitcnt lgkmcnt(15)\n\t"
"buffer_store_dwordx4 a[232:235], v233, s[16:19], s28 offen nt\n\t"
"buffer_load_dwordx4 a[100:103], v248, s[8:11], s20 offen offset:1024 sc1\n\t"
"v_mfma_f32_16x16x32_bf16 v[128:131], a[136:139], a[144:147], v[128:131]\n\t"
"v_mfma_f32_16x16x32_bf16 v[128:131], a[140:143], a[160:163], v[128:131]\n\t"
"ds_read_b128 a[152:155], v216 offset:6144\n\t"
"ds_read_b128 a[168:171], v216 offset:14336\n\t"
"ds_read_b128 a[156:159], v216 offset:7168\n\t"
"ds_read_b128 a[172:175], v216 offset:15360\n\t"
"v_mfma_f32_16x16x32_bf16 v[132:135], a[136:139], a[148:151], v[132:135]\n\t"
"v_mfma_f32_16x16x32_bf16 v[132:135], a[140:143], a[164:167], v[132:135]\n\t"
"v_mfma_f32_16x16x32_bf16 v[116:119], a[128:131], a[148:151], v[116:119]\n\t"
"v_mfma_f32_16x16x32_bf16 v[116:119], a[132:135], a[164:167], v[116:119]\n\t"
"s_add_u32 s28, s25, 1769856\n\t"
"buffer_store_dwordx4 a[236:239], v234, s[16:19], s28 offen nt\n\t"
"v_mfma_f32_16x16x32_bf16 v[100:103], a[120:123], a[148:151], v[100:103]\n\t"
"v_mfma_f32_16x16x32_bf16 v[100:103], a[124:127], a[164:167], v[100:103]\n\t"
"s_waitcnt lgkmcnt(0)\n\t"
"s_barrier\n\t"
"v_mfma_f32_16x16x32_bf16 v[104:107], a[120:123], a[152:155], v[104:107]\n\t"
"ds_read_b128 a[144:147], v216 offset:16384\n\t"
"v_add_u32 v249, 122368, v227\n\t"
"v_mfma_f32_16x16x32_bf16 v[104:107], a[124:127], a[168:171], v[104:107]\n\t"
"ds_read_b128 a[160:163], v216 offset:24576\n\t"
"ds_read_b128 a[148:151], v216 offset:17408\n\t"
"v_mfma_f32_16x16x32_bf16 v[120:123], a[128:131], a[152:155], v[120:123]\n\t"
"ds_read_b128 a[164:167], v216 offset:25600\n\t"
"v_xor_b32 v250, 32, v249\n\t"
"v_mfma_f32_16x16x32_bf16 v[120:123], a[132:135], a[168:171], v[120:123]\n\t"
"v_xor_b32 v251, 64, v249\n\t"
"v_xor_b32 v252, 96, v249\n\t"
"v_mfma_f32_16x16x32_bf16 v[136:139], a[136:139], a[152:155], v[136:139]\n\t"
"ds_read_b128 a[228:231], v229 offset:12800\n\t"
"v_mfma_f32_16x16x32_bf16 v[136:139], a[140:143], a[168:171], v[136:139]\n\t"
"ds_read_b128 a[152:155], v216 offset:18432\n\t"
"ds_read_b128 a[168:171], v216 offset:26624\n\t"
"v_mfma_f32_16x16x32_bf16 v[192:195], a[176:179], a[224:227], 0\n\t"
"ds_read_b128 a[232:235], v230 offset:12800\n\t"
"ds_read_b64_tr_b16 a[176:177], v251 offset:20736\n\t"
"v_mfma_f32_16x16x32_bf16 v[140:143], a[136:139], a[156:159], v[140:143]\n\t"
"ds_read_b64_tr_b16 a[178:179], v251 offset:21248\n\t"
"v_mfma_f32_16x16x32_bf16 v[140:143], a[140:143], a[172:175], v[140:143]\n\t"
"s_mov_b32 s20, 774144\n\t"
"buffer_load_dwordx4 a[136:139], v248, s[8:11], s20 offen offset:1024 sc1\n\t"
"v_mfma_f32_16x16x32_bf16 v[196:199], a[180:183], a[224:227], 0\n\t"
"buffer_load_dwordx4 a[140:143], v248, s[8:11], s20 offen offset:2048 sc1\n\t"
"v_mfma_f32_16x16x32_bf16 v[124:127], a[128:131], a[156:159], v[124:127]\n\t"
"v_cvt_pk_bf16_f32 v192, v192, v193\n\t"
"v_cvt_pk_bf16_f32 v193, v194, v195\n\t"
"v_mfma_f32_16x16x32_bf16 v[124:127], a[132:135], a[172:175], v[124:127]\n\t"
"buffer_load_dwordx4 a[132:135], v248, s[8:11], s20 offen offset:0 sc1\n\t"
"v_mfma_f32_16x16x32_bf16 v[108:111], a[120:123], a[156:159], v[108:111]\n\t"
"ds_read_b128 a[236:239], v231 offset:12800\n\t"
"v_mfma_f32_16x16x32_bf16 v[108:111], a[124:127], a[172:175], v[108:111]\n\t"
"s_mov_b32 s20, 761856\n\t"
"buffer_load_dwordx4 a[128:131], v248, s[8:11], s20 offen offset:2048 sc1\n\t"
"v_mfma_f32_16x16x32_bf16 v[200:203], a[184:187], a[224:227], 0\n\t"
"buffer_load_dwordx4 a[120:123], v248, s[8:11], s20 offen offset:0 sc1\n\t"
"s_waitcnt vmcnt(38) lgkmcnt(9)\n\t"
"v_mfma_f32_16x16x32_bf16 v[144:147], a[0:3], a[144:147], 0\n\t"
"v_cvt_pk_bf16_f32 v194, v196, v197\n\t"
"v_cvt_pk_bf16_f32 v195, v198, v199\n\t"
"v_mfma_f32_16x16x32_bf16 v[144:147], a[4:7], a[160:163], v[144:147]\n\t"
"buffer_load_dwordx4 a[124:127], v248, s[8:11], s20 offen offset:1024 sc1\n\t"
"v_mfma_f32_16x16x32_bf16 v[204:207], a[188:191], a[224:227], 0\n\t"
"ds_read_b128 a[156:159], v216 offset:19456\n\t"
"ds_read_b128 a[172:175], v216 offset:27648\n\t"
"v_mfma_f32_16x16x32_bf16 v[160:163], a[8:11], a[144:147], 0\n\t"
"v_cvt_pk_bf16_f32 v200, v200, v201\n\t"
"v_cvt_pk_bf16_f32 v201, v202, v203\n\t"
"v_mfma_f32_16x16x32_bf16 v[160:163], a[12:15], a[160:163], v[160:163]\n\t"
"ds_write_b128 v221, v[192:195] offset:0\n\t"
"v_mfma_f32_16x16x32_bf16 v[176:179], a[16:19], a[144:147], 0\n\t"
"ds_read_b128 a[144:147], v216 offset:20480\n\t"
"v_mfma_f32_16x16x32_bf16 v[176:179], a[20:23], a[160:163], v[176:179]\n\t"
"s_add_u32 s28, s25, 1573248\n\t"
"s_waitcnt lgkmcnt(10)\n\t"
"buffer_store_dwordx4 a[228:231], v232, s[16:19], s28 offen nt\n\t"
"v_mfma_f32_16x16x32_bf16 v[208:211], a[192:195], a[224:227], 0\n\t"
"v_cvt_pk_bf16_f32 v202, v204, v205\n\t"
"v_cvt_pk_bf16_f32 v203, v206, v207\n\t"
"v_mfma_f32_16x16x32_bf16 v[180:183], a[16:19], a[148:151], 0\n\t"
"ds_read_b128 a[160:163], v216 offset:28672\n\t"
"v_mfma_f32_16x16x32_bf16 v[180:183], a[20:23], a[164:167], v[180:183]\n\t"
"ds_write_b128 v221, v[200:203] offset:8192\n\t"
"v_mfma_f32_16x16x32_bf16 v[164:167], a[8:11], a[148:151], 0\n\t"
"ds_read_b64_tr_b16 a[180:181], v252 offset:20736\n\t"
"v_mfma_f32_16x16x32_bf16 v[164:167], a[12:15], a[164:167], v[164:167]\n\t"
"v_cvt_pk_bf16_f32 v208, v208, v209\n\t"
"v_cvt_pk_bf16_f32 v209, v210, v211\n\t"
"v_mfma_f32_16x16x32_bf16 v[212:215], a[196:199], a[224:227], 0\n\t"
"s_add_u32 s28, s25, 1573248\n\t"
"s_waitcnt lgkmcnt(10)\n\t"
"buffer_store_dwordx4 a[232:235], v233, s[16:19], s28 offen nt\n\t"
"v_mfma_f32_16x16x32_bf16 v[148:151], a[0:3], a[148:151], 0\n\t"
"ds_read_b128 a[148:151], v216 offset:21504\n\t"
"v_mfma_f32_16x16x32_bf16 v[148:151], a[4:7], a[164:167], v[148:151]\n\t"
"ds_read_b128 a[164:167], v216 offset:29696\n\t"
"ds_read_b64_tr_b16 a[182:183], v252 offset:21248\n\t"
"v_mfma_f32_16x16x32_bf16 v[192:195], a[200:203], a[224:227], 0\n\t"
"ds_read_b64_tr_b16 a[184:185], v249 offset:20736\n\t"
"ds_read_b64_tr_b16 a[186:187], v249 offset:21248\n\t"
"v_mfma_f32_16x16x32_bf16 v[152:155], a[0:3], a[152:155], 0\n\t"
"v_cvt_pk_bf16_f32 v210, v212, v213\n\t"
"v_cvt_pk_bf16_f32 v211, v214, v215\n\t"
"v_mfma_f32_16x16x32_bf16 v[152:155], a[4:7], a[168:171], v[152:155]\n\t"
"ds_write_b128 v221, v[208:211] offset:4096\n\t"
"v_mfma_f32_16x16x32_bf16 v[168:171], a[8:11], a[152:155], 0\n\t"
"ds_read_b64_tr_b16 a[188:189], v250 offset:20736\n\t"
"v_mfma_f32_16x16x32_bf16 v[168:171], a[12:15], a[168:171], v[168:171]\n\t"
"v_cvt_pk_bf16_f32 v192, v192, v193\n\t"
"v_cvt_pk_bf16_f32 v193, v194, v195\n\t"
"v_mfma_f32_16x16x32_bf16 v[196:199], a[204:207], a[224:227], 0\n\t"
"ds_read_b64_tr_b16 a[190:191], v250 offset:21248\n\t"
"ds_read_b64_tr_b16 a[192:193], v251 offset:21888\n\t"
"v_mfma_f32_16x16x32_bf16 v[184:187], a[16:19], a[152:155], 0\n\t"
"ds_read_b64_tr_b16 a[194:195], v251 offset:22400\n\t"
"v_mfma_f32_16x16x32_bf16 v[184:187], a[20:23], a[168:171], v[184:187]\n\t"
"ds_read_b64_tr_b16 a[196:197], v252 offset:21888\n\t"
"ds_read_b64_tr_b16 a[198:199], v252 offset:22400\n\t"
"s_waitcnt lgkmcnt(15)\n\t"
"v_mfma_f32_16x16x32_bf16 v[188:191], a[16:19], a[156:159], 0\n\t"
"ds_read_b64_tr_b16 a[200:201], v249 offset:21888\n\t"
"v_mfma_f32_16x16x32_bf16 v[188:191], a[20:23], a[172:175], v[188:191]\n\t"
"s_mov_b32 s20, 36864\n\t"
"buffer_load_dwordx4 a[16:19], v248, s[8:11], s20 offen offset:1024 sc1\n\t"
"v_mfma_f32_16x16x32_bf16 v[172:175], a[8:11], a[156:159], 0\n\t"
"ds_read_b64_tr_b16 a[202:203], v249 offset:22400\n\t"
"v_cvt_pk_bf16_f32 v194, v196, v197\n\t"
"v_mfma_f32_16x16x32_bf16 v[172:175], a[12:15], a[172:175], v[172:175]\n\t"
"buffer_load_dwordx4 a[20:23], v248, s[8:11], s20 offen offset:2048 sc1\n\t"
"v_mfma_f32_16x16x32_bf16 v[156:159], a[0:3], a[156:159], 0\n\t"
"ds_read_b64_tr_b16 a[204:205], v250 offset:21888\n\t"
"v_cvt_pk_bf16_f32 v195, v198, v199\n\t"
"v_mfma_f32_16x16x32_bf16 v[156:159], a[4:7], a[172:175], v[156:159]\n\t"
"buffer_load_dwordx4 a[12:15], v248, s[8:11], s20 offen offset:0 sc1\n\t"
"s_waitcnt vmcnt(38) lgkmcnt(15)\n\t"
"v_mfma_f32_16x16x32_bf16 v[48:51], a[24:27], a[144:147], v[144:147]\n\t"
"ds_read_b64_tr_b16 a[206:207], v250 offset:22400\n\t"
"v_mfma_f32_16x16x32_bf16 v[48:51], a[28:31], a[160:163], v[48:51]\n\t"
"ds_write_b128 v221, v[192:195] offset:12288\n\t"
"s_mov_b32 s20, 24576\n\t"
"buffer_load_dwordx4 a[8:11], v248, s[8:11], s20 offen offset:2048 sc1\n\t"
"v_mfma_f32_16x16x32_bf16 v[64:67], a[32:35], a[144:147], v[160:163]\n\t"
"ds_read_b128 a[152:155], v216 offset:22528\n\t"
"v_mfma_f32_16x16x32_bf16 v[64:67], a[36:39], a[160:163], v[64:67]\n\t"
"buffer_load_dwordx4 a[0:3], v248, s[8:11], s20 offen offset:0 sc1\n\t"
"buffer_load_dwordx4 a[4:7], v248, s[8:11], s20 offen offset:1024 sc1\n\t"
"v_mfma_f32_16x16x32_bf16 v[80:83], a[40:43], a[144:147], v[176:179]\n\t"
"ds_read_b128 a[168:171], v216 offset:30720\n\t"
"v_mfma_f32_16x16x32_bf16 v[80:83], a[44:47], a[160:163], v[80:83]\n\t"
"ds_read_b128 a[156:159], v216 offset:23552\n\t"
"ds_read_b128 a[172:175], v216 offset:31744\n\t"
"ds_write_b128 v222, a[248:251] offset:0\n\t"
"s_waitcnt lgkmcnt(15)\n\t"
"v_mfma_f32_16x16x32_bf16 v[84:87], a[40:43], a[148:151], v[180:183]\n\t"
"v_mfma_f32_16x16x32_bf16 v[84:87], a[44:47], a[164:167], v[84:87]\n\t"
"s_add_u32 s28, s25, 1573248\n\t"
"buffer_store_dwordx4 a[236:239], v234, s[16:19], s28 offen nt\n\t"
"v_mfma_f32_16x16x32_bf16 v[68:71], a[32:35], a[148:151], v[164:167]\n\t"
"v_mfma_f32_16x16x32_bf16 v[68:71], a[36:39], a[164:167], v[68:71]\n\t"
"v_mfma_f32_16x16x32_bf16 v[52:55], a[24:27], a[148:151], v[148:151]\n\t"
"v_mfma_f32_16x16x32_bf16 v[52:55], a[28:31], a[164:167], v[52:55]\n\t"
"s_waitcnt lgkmcnt(0)\n\t"
"s_barrier\n\t"
"v_mfma_f32_16x16x32_bf16 v[56:59], a[24:27], a[152:155], v[152:155]\n\t"
"ds_read_b128 a[144:147], v216 offset:0\n\t"
"v_add_u32 v249, 80896, v225\n\t"
"v_mfma_f32_16x16x32_bf16 v[56:59], a[28:31], a[168:171], v[56:59]\n\t"
"ds_read_b128 a[160:163], v216 offset:8192\n\t"
"ds_read_b128 a[148:151], v216 offset:1024\n\t"
"v_mfma_f32_16x16x32_bf16 v[72:75], a[32:35], a[152:155], v[168:171]\n\t"
"ds_read_b128 a[164:167], v216 offset:9216\n\t"
"v_xor_b32 v250, 32, v249\n\t"
"v_mfma_f32_16x16x32_bf16 v[72:75], a[36:39], a[168:171], v[72:75]\n\t"
"v_xor_b32 v251, 64, v249\n\t"
"v_xor_b32 v252, 96, v249\n\t"
"v_mfma_f32_16x16x32_bf16 v[88:91], a[40:43], a[152:155], v[184:187]\n\t"
"ds_read_b128 a[152:155], v216 offset:2048\n\t"
"v_mfma_f32_16x16x32_bf16 v[88:91], a[44:47], a[168:171], v[88:91]\n\t"
"ds_read_b128 a[168:171], v216 offset:10240\n\t"
"v_mfma_f32_16x16x32_bf16 v[200:203], a[176:179], a[224:227], 0\n\t"
"ds_read_b64_tr_b16 a[176:177], v249 offset:0\n\t"
"ds_read_b64_tr_b16 a[178:179], v249 offset:512\n\t"
"v_mfma_f32_16x16x32_bf16 v[92:95], a[40:43], a[156:159], v[188:191]\n\t"
"v_mfma_f32_16x16x32_bf16 v[92:95], a[44:47], a[172:175], v[92:95]\n\t"
"s_mov_b32 s20, 61440\n\t"
"buffer_load_dwordx4 a[40:43], v248, s[8:11], s20 offen offset:1024 sc1\n\t"
"v_mfma_f32_16x16x32_bf16 v[204:207], a[180:183], a[224:227], 0\n\t"
"buffer_load_dwordx4 a[44:47], v248, s[8:11], s20 offen offset:2048 sc1\n\t"
"v_mfma_f32_16x16x32_bf16 v[76:79], a[32:35], a[156:159], v[172:175]\n\t"
"v_cvt_pk_bf16_f32 v200, v200, v201\n\t"
"v_cvt_pk_bf16_f32 v201, v202, v203\n\t"
"v_mfma_f32_16x16x32_bf16 v[76:79], a[36:39], a[172:175], v[76:79]\n\t"
"buffer_load_dwordx4 a[36:39], v248, s[8:11], s20 offen offset:0 sc1\n\t"
"v_mfma_f32_16x16x32_bf16 v[60:63], a[24:27], a[156:159], v[156:159]\n\t"
"ds_read_b128 a[156:159], v216 offset:3072\n\t"
"v_mfma_f32_16x16x32_bf16 v[60:63], a[28:31], a[172:175], v[60:63]\n\t"
"s_mov_b32 s20, 49152\n\t"
"buffer_load_dwordx4 a[32:35], v248, s[8:11], s20 offen offset:2048 sc1\n\t"
"v_mfma_f32_16x16x32_bf16 v[208:211], a[184:187], a[224:227], 0\n\t"
"v_pk_fma_f32 v[144:145], v[144:145], s[38:39], v[48:49] neg_lo:[1,0,1] neg_hi:[1,0,1]\n\t"
"v_pk_fma_f32 v[146:147], v[146:147], s[38:39], v[50:51] neg_lo:[1,0,1] neg_hi:[1,0,1]\n\t"
"v_pk_fma_f32 v[160:161], v[160:161], s[38:39], v[64:65] neg_lo:[1,0,1] neg_hi:[1,0,1]\n\t"
"v_pk_fma_f32 v[162:163], v[162:163], s[38:39], v[66:67] neg_lo:[1,0,1] neg_hi:[1,0,1]\n\t"
"v_pk_fma_f32 v[176:177], v[176:177], s[38:39], v[80:81] neg_lo:[1,0,1] neg_hi:[1,0,1]\n\t"
"v_pk_fma_f32 v[178:179], v[178:179], s[38:39], v[82:83] neg_lo:[1,0,1] neg_hi:[1,0,1]\n\t"
"v_pk_fma_f32 v[180:181], v[180:181], s[38:39], v[84:85] neg_lo:[1,0,1] neg_hi:[1,0,1]\n\t"
"v_pk_fma_f32 v[182:183], v[182:183], s[38:39], v[86:87] neg_lo:[1,0,1] neg_hi:[1,0,1]\n\t"
"v_pk_fma_f32 v[164:165], v[164:165], s[38:39], v[68:69] neg_lo:[1,0,1] neg_hi:[1,0,1]\n\t"
"v_pk_fma_f32 v[166:167], v[166:167], s[38:39], v[70:71] neg_lo:[1,0,1] neg_hi:[1,0,1]\n\t"
"v_pk_fma_f32 v[148:149], v[148:149], s[38:39], v[52:53] neg_lo:[1,0,1] neg_hi:[1,0,1]\n\t"
"v_pk_fma_f32 v[150:151], v[150:151], s[38:39], v[54:55] neg_lo:[1,0,1] neg_hi:[1,0,1]\n\t"
"v_pk_fma_f32 v[152:153], v[152:153], s[38:39], v[56:57] neg_lo:[1,0,1] neg_hi:[1,0,1]\n\t"
"v_pk_fma_f32 v[154:155], v[154:155], s[38:39], v[58:59] neg_lo:[1,0,1] neg_hi:[1,0,1]\n\t"
"v_pk_fma_f32 v[168:169], v[168:169], s[38:39], v[72:73] neg_lo:[1,0,1] neg_hi:[1,0,1]\n\t"
"v_pk_fma_f32 v[170:171], v[170:171], s[38:39], v[74:75] neg_lo:[1,0,1] neg_hi:[1,0,1]\n\t"
"v_pk_fma_f32 v[184:185], v[184:185], s[38:39], v[88:89] neg_lo:[1,0,1] neg_hi:[1,0,1]\n\t"
"v_pk_fma_f32 v[186:187], v[186:187], s[38:39], v[90:91] neg_lo:[1,0,1] neg_hi:[1,0,1]\n\t"
"v_pk_fma_f32 v[188:189], v[188:189], s[38:39], v[92:93] neg_lo:[1,0,1] neg_hi:[1,0,1]\n\t"
"v_pk_fma_f32 v[190:191], v[190:191], s[38:39], v[94:95] neg_lo:[1,0,1] neg_hi:[1,0,1]\n\t"
"v_pk_fma_f32 v[172:173], v[172:173], s[38:39], v[76:77] neg_lo:[1,0,1] neg_hi:[1,0,1]\n\t"
"v_pk_fma_f32 v[174:175], v[174:175], s[38:39], v[78:79] neg_lo:[1,0,1] neg_hi:[1,0,1]\n\t"
"v_pk_fma_f32 v[156:157], v[156:157], s[38:39], v[60:61] neg_lo:[1,0,1] neg_hi:[1,0,1]\n\t"
"v_pk_fma_f32 v[158:159], v[158:159], s[38:39], v[62:63] neg_lo:[1,0,1] neg_hi:[1,0,1]\n\t"
"buffer_load_dwordx4 a[24:27], v248, s[8:11], s20 offen offset:0 sc1\n\t"
"s_waitcnt vmcnt(39) lgkmcnt(7)\n\t"
"v_mfma_f32_16x16x32_bf16 v[48:51], a[48:51], a[144:147], v[48:51]\n\t"
"v_cvt_pk_bf16_f32 v202, v204, v205\n\t"
"v_cvt_pk_bf16_f32 v203, v206, v207\n\t"
"v_mfma_f32_16x16x32_bf16 v[48:51], a[52:55], a[160:163], v[48:51]\n\t"
"ds_read_b128 a[172:175], v216 offset:11264\n\t"
"v_mfma_f32_16x16x32_bf16 v[212:215], a[188:191], a[224:227], 0\n\t"
"buffer_load_dwordx4 a[28:31], v248, s[8:11], s20 offen offset:1024 sc1\n\t"
"v_mfma_f32_16x16x32_bf16 v[64:67], a[56:59], a[144:147], v[64:67]\n\t"
"v_cvt_pk_bf16_f32 v208, v208, v209\n\t"
"v_cvt_pk_bf16_f32 v209, v210, v211\n\t"
"v_mfma_f32_16x16x32_bf16 v[64:67], a[60:63], a[160:163], v[64:67]\n\t"
"ds_read_b64_tr_b16 a[180:181], v250 offset:0\n\t"
"v_mfma_f32_16x16x32_bf16 v[80:83], a[64:67], a[144:147], v[80:83]\n\t"
"ds_read_b128 a[144:147], v216 offset:4096\n\t"
"v_mfma_f32_16x16x32_bf16 v[80:83], a[68:71], a[160:163], v[80:83]\n\t"
"ds_write_b128 v221, v[200:203] offset:16384\n\t"
"v_mfma_f32_16x16x32_bf16 v[192:195], a[192:195], a[224:227], 0\n\t"
"v_cvt_pk_bf16_f32 v210, v212, v213\n\t"
"v_cvt_pk_bf16_f32 v211, v214, v215\n\t"
"s_waitcnt lgkmcnt(9)\n\t"
"v_mfma_f32_16x16x32_bf16 v[84:87], a[64:67], a[148:151], v[84:87]\n\t"
"ds_read_b128 a[160:163], v216 offset:12288\n\t"
"v_mfma_f32_16x16x32_bf16 v[84:87], a[68:71], a[164:167], v[84:87]\n\t"
"ds_write_b128 v221, v[208:211] offset:24576\n\t"
"v_mfma_f32_16x16x32_bf16 v[68:71], a[56:59], a[148:151], v[68:71]\n\t"
"ds_read_b64_tr_b16 a[182:183], v250 offset:512\n\t"
"v_mfma_f32_16x16x32_bf16 v[68:71], a[60:63], a[164:167], v[68:71]\n\t"
"v_cvt_pk_bf16_f32 v192, v192, v193\n\t"
"v_cvt_pk_bf16_f32 v193, v194, v195\n\t"
"v_mfma_f32_16x16x32_bf16 v[196:199], a[196:199], a[224:227], 0\n\t"
"ds_read_b64_tr_b16 a[184:185], v251 offset:2304\n\t"
"ds_read_b64_tr_b16 a[186:187], v251 offset:2816\n\t"
"v_mfma_f32_16x16x32_bf16 v[52:55], a[48:51], a[148:151], v[52:55]\n\t"
"ds_read_b128 a[148:151], v216 offset:5120\n\t"
"v_mfma_f32_16x16x32_bf16 v[52:55], a[52:55], a[164:167], v[52:55]\n\t"
"ds_read_b128 a[164:167], v216 offset:13312\n\t"
"ds_read_b64_tr_b16 a[188:189], v252 offset:2304\n\t"
"v_mfma_f32_16x16x32_bf16 v[200:203], a[200:203], a[224:227], 0\n\t"
"ds_read_b64_tr_b16 a[190:191], v252 offset:2816\n\t"
"ds_read_b64_tr_b16 a[192:193], v251 offset:0\n\t"
"s_waitcnt lgkmcnt(15)\n\t"
"v_mfma_f32_16x16x32_bf16 v[56:59], a[48:51], a[152:155], v[56:59]\n\t"
"v_cvt_pk_bf16_f32 v194, v196, v197\n\t"
"v_mfma_f32_16x16x32_bf16 v[56:59], a[52:55], a[168:171], v[56:59]\n\t"
"v_cvt_pk_bf16_f32 v195, v198, v199\n\t"
"ds_read_b64_tr_b16 a[194:195], v251 offset:512\n\t"
"v_mfma_f32_16x16x32_bf16 v[72:75], a[56:59], a[152:155], v[72:75]\n\t"
"ds_read_b64_tr_b16 a[196:197], v252 offset:0\n\t"
"v_mfma_f32_16x16x32_bf16 v[72:75], a[60:63], a[168:171], v[72:75]\n\t"
"v_cvt_pk_bf16_f32 v200, v200, v201\n\t"
"v_cvt_pk_bf16_f32 v201, v202, v203\n\t"
"v_mfma_f32_16x16x32_bf16 v[204:207], a[204:207], a[224:227], 0\n\t"
"ds_write_b128 v221, v[192:195] offset:20480\n\t"
"v_mfma_f32_16x16x32_bf16 v[88:91], a[64:67], a[152:155], v[88:91]\n\t"
"ds_read_b64_tr_b16 a[198:199], v252 offset:512\n\t"
"v_mfma_f32_16x16x32_bf16 v[88:91], a[68:71], a[168:171], v[88:91]\n\t"
"ds_read_b64_tr_b16 a[200:201], v249 offset:2304\n\t"
"ds_read_b64_tr_b16 a[202:203], v249 offset:2816\n\t"
"s_waitcnt lgkmcnt(15)\n\t"
"v_mfma_f32_16x16x32_bf16 v[92:95], a[64:67], a[156:159], v[92:95]\n\t"
"ds_read_b64_tr_b16 a[204:205], v250 offset:2304\n\t"
"v_mfma_f32_16x16x32_bf16 v[92:95], a[68:71], a[172:175], v[92:95]\n\t"
"s_mov_b32 s20, 331776\n\t"
"buffer_load_dwordx4 a[64:67], v248, s[8:11], s20 offen offset:1024 sc1\n\t"
"v_mfma_f32_16x16x32_bf16 v[76:79], a[56:59], a[156:159], v[76:79]\n\t"
"ds_read_b64_tr_b16 a[206:207], v250 offset:2816\n\t"
"v_mfma_f32_16x16x32_bf16 v[76:79], a[60:63], a[172:175], v[76:79]\n\t"
"buffer_load_dwordx4 a[68:71], v248, s[8:11], s20 offen offset:2048 sc1\n\t"
"v_mfma_f32_16x16x32_bf16 v[60:63], a[48:51], a[156:159], v[60:63]\n\t"
"v_cvt_pk_bf16_f32 v202, v204, v205\n\t"
"v_cvt_pk_bf16_f32 v203, v206, v207\n\t"
"v_mfma_f32_16x16x32_bf16 v[60:63], a[52:55], a[172:175], v[60:63]\n\t"
"ds_read_b128 a[152:155], v216 offset:6144\n\t"
"s_waitcnt vmcnt(35)\n\t"
"v_mfma_f32_16x16x32_bf16 v[144:147], a[72:75], a[144:147], v[144:147]\n\t"
"ds_read_b128 a[168:171], v216 offset:14336\n\t"
"v_mfma_f32_16x16x32_bf16 v[144:147], a[76:79], a[160:163], v[144:147]\n\t"
"buffer_load_dwordx4 a[60:63], v248, s[8:11], s20 offen offset:0 sc1\n\t"
"ds_write_b128 v221, v[200:203] offset:28672\n\t"
"s_mov_b32 s20, 319488\n\t"
"buffer_load_dwordx4 a[56:59], v248, s[8:11], s20 offen offset:2048 sc1\n\t"
"v_mfma_f32_16x16x32_bf16 v[160:163], a[80:83], a[144:147], v[160:163]\n\t"
"v_mfma_f32_16x16x32_bf16 v[160:163], a[84:87], a[160:163], v[160:163]\n\t"
"buffer_load_dwordx4 a[48:51], v248, s[8:11], s20 offen offset:0 sc1\n\t"
"buffer_load_dwordx4 a[52:55], v248, s[8:11], s20 offen offset:1024 sc1\n\t"
"v_mfma_f32_16x16x32_bf16 v[176:179], a[88:91], a[144:147], v[176:179]\n\t"
"v_mfma_f32_16x16x32_bf16 v[176:179], a[92:95], a[160:163], v[176:179]\n\t"
"ds_read_b128 a[156:159], v216 offset:7168\n\t"
"ds_read_b128 a[172:175], v216 offset:15360\n\t"
"s_waitcnt lgkmcnt(15)\n\t"
"v_mfma_f32_16x16x32_bf16 v[180:183], a[88:91], a[148:151], v[180:183]\n\t"
"v_mfma_f32_16x16x32_bf16 v[180:183], a[92:95], a[164:167], v[180:183]\n\t"
"v_mfma_f32_16x16x32_bf16 v[164:167], a[80:83], a[148:151], v[164:167]\n\t"
"v_mfma_f32_16x16x32_bf16 v[164:167], a[84:87], a[164:167], v[164:167]\n\t"
"v_mfma_f32_16x16x32_bf16 v[148:151], a[72:75], a[148:151], v[148:151]\n\t"
"v_mfma_f32_16x16x32_bf16 v[148:151], a[76:79], a[164:167], v[148:151]\n\t"
"s_waitcnt lgkmcnt(0)\n\t"
"s_barrier\n\t"
"s_branch Lunit_%=\n\t"
"Lfb_%=:\n\t"
"v_mbcnt_lo_u32_b32 v162, -1, 0\n\t"
"v_mbcnt_hi_u32_b32 v162, -1, v162\n\t"
"v_and_b32 v161, 3, v162\n\t"
"v_lshlrev_b32 v163, 2, v162\n\t"
"v_and_or_b32 v161, v163, 16, v161\n\t"
"v_mul_u32_u24 v161, 0xc0, v161\n\t"
"v_lshrrev_b32 v163, 5, v162\n\t"
"v_mul_u32_u24 v163, 12, v163\n\t"
"v_bfe_u32 v164, v162, 3, 2\n\t"
"v_add3_u32 v161, v161, v163, v164\n\t"
"v_lshlrev_b32 v161, 1, v161\n\t"
"v_lshlrev_b32 v162, 1, v162\n\t"
"v_mov_b32 v152, 0\n\t"
"v_mov_b32 v153, 0\n\t"
"v_mov_b32 v154, 0\n\t"
"v_mov_b32 v155, 0\n\t"
"v_mov_b32 v156, 0\n\t"
"v_mov_b32 v157, 0\n\t"
"v_mov_b32 v158, 0\n\t"
"v_mov_b32 v159, 0\n\t"
"v_mov_b32 v160, 0\n\t"
"s_mov_b32 s67, 0\n\t"
"Lfb0_%=:\n\t"
"s_mov_b32 s69, 0\n\t"
"Lfb1_%=:\n\t"
"s_lshr_b32 s60, s69, 5\n\t"
"s_mul_i32 s56, s60, 12288\n\t"
"s_bfe_u32 s61, s69, 0x20001\n\t"
"s_mul_i32 s61, s61, 1536\n\t"
"s_add_u32 s56, s56, s61\n\t"
"s_bitcmp1_b32 s69, 0\n\t"
"s_cselect_b32 s61, 48, 0\n\t"
"s_add_u32 s56, s56, s61\n\t"
"s_bfe_u32 s62, s69, 0x20003\n\t"
"s_mul_i32 s61, s62, 96\n\t"
"s_add_u32 s56, s56, s61\n\t"
"s_lshl3_add_u32 s56, s67, s56\n\t"
"s_lshr_b32 s60, s69, 5\n\t"
"s_bfe_u32 s62, s69, 0x20003\n\t"
"s_and_b32 s61, s60, 1\n\t"
"s_lshl1_add_u32 s61, s67, s61\n\t"
"s_cmp_ge_u32 s61, 3\n\t"
"s_cselect_b32 s63, 9, 0\n\t"
"s_add_u32 s61, s61, s63\n\t"
"s_mul_i32 s62, s62, 3\n\t"
"s_add_u32 s61, s61, s62\n\t"
"s_lshr_b32 s60, s60, 1\n\t"
"s_mul_i32 s62, s60, 384\n\t"
"s_add_u32 s61, s61, s62\n\t"
"s_lshl_b32 s57, s61, 10\n\t"
"s_and_b32 s61, s69, 7\n\t"
"s_lshl_b32 s61, s61, 7\n\t"
"s_add_u32 s57, s57, s61\n\t"
"s_cmp_lt_u32 s60, 1\n\t"
"s_cselect_b32 s58, 0x3e000000, 0.5\n\t"
"s_cmp_lt_u32 s60, 2\n\t"
"s_cselect_b32 s59, 0x3e800000, 1.0\n\t"
"s_add_u32 s60, s56, 0\n\t"
"buffer_load_short_d16_hi v152, v161, s[52:55], s60 offen\n\t"
"s_add_u32 s60, s56, 73728\n\t"
"buffer_load_short_d16_hi v153, v161, s[52:55], s60 offen\n\t"
"s_add_u32 s60, s56, 147456\n\t"
"buffer_load_short_d16_hi v154, v161, s[52:55], s60 offen\n\t"
"s_add_u32 s60, s56, 221184\n\t"
"buffer_load_short_d16_hi v155, v161, s[52:55], s60 offen\n\t"
"s_add_u32 s60, s56, 294912\n\t"
"buffer_load_short_d16_hi v156, v161, s[52:55], s60 offen\n\t"
"s_add_u32 s60, s56, 368640\n\t"
"buffer_load_short_d16_hi v157, v161, s[52:55], s60 offen\n\t"
"s_add_u32 s60, s56, 442368\n\t"
"buffer_load_short_d16_hi v158, v161, s[52:55], s60 offen\n\t"
"s_add_u32 s60, s56, 516096\n\t"
"buffer_load_short_d16_hi v159, v161, s[52:55], s60 offen\n\t"
"s_add_u32 s60, s56, 589824\n\t"
"buffer_load_short_d16_hi v160, v161, s[52:55], s60 offen\n\t"
"s_waitcnt vmcnt(2)\n\t"
"v_add_f32 v168, v152, v158\n\t"
"s_waitcnt vmcnt(1)\n\t"
"v_add_f32 v169, v153, v159\n\t"
"s_waitcnt vmcnt(0)\n\t"
"v_add_f32 v170, v154, v160\n\t"
"v_add_f32 v171, v168, v155\n\t"
"v_sub_f32 v174, v168, v155\n\t"
"v_add_f32 v172, v169, v156\n\t"
"v_sub_f32 v175, v169, v156\n\t"
"v_add_f32 v173, v170, v157\n\t"
"v_sub_f32 v176, v170, v157\n\t"
"v_add_f32 v177, v152, v154\n\t"
"v_add_f32 v178, v171, v173\n\t"
"v_add_f32 v179, v174, v176\n\t"
"v_add_f32 v180, v158, v160\n\t"
"v_add_f32 v182, v177, v153\n\t"
"v_sub_f32 v183, v177, v153\n\t"
"v_add_f32 v184, v178, v172\n\t"
"v_sub_f32 v185, v178, v172\n\t"
"v_add_f32 v186, v179, v175\n\t"
"v_sub_f32 v187, v179, v175\n\t"
"v_add_f32 v188, v180, v159\n\t"
"v_sub_f32 v189, v180, v159\n\t"
"v_mul_f32 v190, s59, v152\n\t"
"v_mul_f32 v191, 0x3e800000, v182\n\t"
"v_mul_f32 v192, 0x3e800000, v183\n\t"
"v_mul_f32 v193, -s59, v154\n\t"
"v_mul_f32 v194, s58, v171\n\t"
"v_mul_f32 v195, 0x3e800000, v184\n\t"
"v_mul_f32 v196, 0x3e800000, v185\n\t"
"v_mul_f32 v197, -s58, v173\n\t"
"v_mul_f32 v198, s58, v174\n\t"
"v_mul_f32 v199, 0x3e800000, v186\n\t"
"v_mul_f32 v200, 0x3e800000, v187\n\t"
"v_mul_f32 v201, -s58, v176\n\t"
"v_mul_f32 v202, -s59, v158\n\t"
"v_mul_f32 v203, 0xbe800000, v188\n\t"
"v_mul_f32 v204, 0xbe800000, v189\n\t"
"v_mul_f32 v205, s59, v160\n\t"
"v_cvt_pk_bf16_f32 v206, v190, v191\n\t"
"v_cvt_pk_bf16_f32 v207, v192, v193\n\t"
"v_cvt_pk_bf16_f32 v208, v194, v195\n\t"
"v_cvt_pk_bf16_f32 v209, v196, v197\n\t"
"v_cvt_pk_bf16_f32 v210, v198, v199\n\t"
"v_cvt_pk_bf16_f32 v211, v200, v201\n\t"
"v_cvt_pk_bf16_f32 v212, v202, v203\n\t"
"v_cvt_pk_bf16_f32 v213, v204, v205\n\t"
"s_add_u32 s60, s57, 0\n\t"
"buffer_store_short v206, v162, s[8:11], s60 offen sc1\n\t"
"s_add_u32 s60, s57, 24576\n\t"
"buffer_store_short_d16_hi v206, v162, s[8:11], s60 offen sc1\n\t"
"s_add_u32 s60, s57, 49152\n\t"
"buffer_store_short v207, v162, s[8:11], s60 offen sc1\n\t"
"s_add_u32 s60, s57, 73728\n\t"
"buffer_store_short_d16_hi v207, v162, s[8:11], s60 offen sc1\n\t"
"s_add_u32 s60, s57, 98304\n\t"
"buffer_store_short v208, v162, s[8:11], s60 offen sc1\n\t"
"s_add_u32 s60, s57, 122880\n\t"
"buffer_store_short_d16_hi v208, v162, s[8:11], s60 offen sc1\n\t"
"s_add_u32 s60, s57, 147456\n\t"
"buffer_store_short v209, v162, s[8:11], s60 offen sc1\n\t"
"s_add_u32 s60, s57, 172032\n\t"
"buffer_store_short_d16_hi v209, v162, s[8:11], s60 offen sc1\n\t"
"s_add_u32 s60, s57, 196608\n\t"
"buffer_store_short v210, v162, s[8:11], s60 offen sc1\n\t"
"s_add_u32 s60, s57, 221184\n\t"
"buffer_store_short_d16_hi v210, v162, s[8:11], s60 offen sc1\n\t"
"s_add_u32 s60, s57, 245760\n\t"
"buffer_store_short v211, v162, s[8:11], s60 offen sc1\n\t"
"s_add_u32 s60, s57, 270336\n\t"
"buffer_store_short_d16_hi v211, v162, s[8:11], s60 offen sc1\n\t"
"s_add_u32 s60, s57, 294912\n\t"
"buffer_store_short v212, v162, s[8:11], s60 offen sc1\n\t"
"s_add_u32 s60, s57, 319488\n\t"
"buffer_store_short_d16_hi v212, v162, s[8:11], s60 offen sc1\n\t"
"s_add_u32 s60, s57, 344064\n\t"
"buffer_store_short v213, v162, s[8:11], s60 offen sc1\n\t"
"s_add_u32 s60, s57, 368640\n\t"
"buffer_store_short_d16_hi v213, v162, s[8:11], s60 offen sc1\n\t"
"s_waitcnt vmcnt(0)\n\t"
"s_add_u32 s69, s69, 1\n\t"
"s_cmp_lt_u32 s69, 192\n\t"
"s_cbranch_scc1 Lfb1_%=\n\t"
"s_add_u32 s67, s67, 1\n\t"
"s_cmp_lt_u32 s67, 3\n\t"
"s_cbranch_scc1 Lfb0_%=\n\t"
"s_branch Lgo_%=\n\t"
"Ltail_%=:\n\t"
"v_mfma_f32_16x16x32_bf16 v[104:107], a[24:27], a[152:155], v[104:107]\n\t"
"ds_read_b128 a[144:147], v216 offset:0\n\t"
"v_cvt_pk_bf16_f32 v8, v8, v9\n\t"
"v_mfma_f32_16x16x32_bf16 v[104:107], a[28:31], a[168:171], v[104:107]\n\t"
"ds_read_b128 a[160:163], v216 offset:8192\n\t"
"ds_read_b128 a[148:151], v216 offset:1024\n\t"
"v_mfma_f32_16x16x32_bf16 v[120:123], a[32:35], a[152:155], v[120:123]\n\t"
"ds_read_b128 a[164:167], v216 offset:9216\n\t"
"v_cvt_pk_bf16_f32 v9, v10, v11\n\t"
"v_mfma_f32_16x16x32_bf16 v[120:123], a[36:39], a[168:171], v[120:123]\n\t"
"s_mov_b32 s20, 860160\n\t"
"buffer_load_dwordx4 a[100:103], v248, s[8:11], s20 offen offset:1024 sc1\n\t"
"v_mfma_f32_16x16x32_bf16 v[136:139], a[40:43], a[152:155], v[136:139]\n\t"
"v_cvt_pk_bf16_f32 v24, v24, v25\n\t"
"v_cvt_pk_bf16_f32 v25, v26, v27\n\t"
"v_mfma_f32_16x16x32_bf16 v[136:139], a[44:47], a[168:171], v[136:139]\n\t"
"s_mov_b32 s20, 1167360\n\t"
"buffer_load_dwordx4 a[136:139], v248, s[8:11], s20 offen offset:1024 sc1\n\t"
"v_mfma_f32_16x16x32_bf16 v[192:195], a[176:179], a[224:227], 0\n\t"
"buffer_load_dwordx4 a[140:143], v248, s[8:11], s20 offen offset:2048 sc1\n\t"
"v_mfma_f32_16x16x32_bf16 v[140:143], a[40:43], a[156:159], v[140:143]\n\t"
"v_cvt_pk_bf16_f32 v40, v40, v41\n\t"
"v_cvt_pk_bf16_f32 v41, v42, v43\n\t"
"v_mfma_f32_16x16x32_bf16 v[140:143], a[44:47], a[172:175], v[140:143]\n\t"
"ds_write_b64 v228, v[8:9] offset:12800\n\t"
"ds_write_b64 v228, v[24:25] offset:12808\n\t"
"v_mfma_f32_16x16x32_bf16 v[196:199], a[180:183], a[224:227], 0\n\t"
"buffer_load_dwordx4 a[132:135], v248, s[8:11], s20 offen offset:0 sc1\n\t"
"v_mfma_f32_16x16x32_bf16 v[124:127], a[32:35], a[156:159], v[124:127]\n\t"
"ds_read_b128 a[228:231], v229 offset:0\n\t"
"v_cvt_pk_bf16_f32 v12, v12, v13\n\t"
"v_mfma_f32_16x16x32_bf16 v[124:127], a[36:39], a[172:175], v[124:127]\n\t"
"ds_write_b64 v228, v[40:41] offset:12816\n\t"
"v_cvt_pk_bf16_f32 v13, v14, v15\n\t"
"ds_read_b128 a[232:235], v230 offset:0\n\t"
"v_mfma_f32_16x16x32_bf16 v[108:111], a[24:27], a[156:159], v[108:111]\n\t"
"v_cvt_pk_bf16_f32 v28, v28, v29\n\t"
"v_cvt_pk_bf16_f32 v29, v30, v31\n\t"
"v_mfma_f32_16x16x32_bf16 v[108:111], a[28:31], a[172:175], v[108:111]\n\t"
"ds_write_b64 v228, v[12:13] offset:19200\n\t"
"v_mfma_f32_16x16x32_bf16 v[200:203], a[184:187], a[224:227], 0\n\t"
"ds_read_b128 a[152:155], v216 offset:2048\n\t"
"ds_read_b128 a[168:171], v216 offset:10240\n\t"
"s_mov_b32 s20, 1155072\n\t"
"buffer_load_dwordx4 a[128:131], v248, s[8:11], s20 offen offset:2048 sc1\n\t"
"s_waitcnt vmcnt(16) lgkmcnt(10)\n\t"
"v_mfma_f32_16x16x32_bf16 v[48:51], a[48:51], a[144:147], v[48:51]\n\t"
"v_cvt_pk_bf16_f32 v192, v192, v193\n\t"
"v_cvt_pk_bf16_f32 v193, v194, v195\n\t"
"v_mfma_f32_16x16x32_bf16 v[48:51], a[52:55], a[160:163], v[48:51]\n\t"
"ds_read_b128 a[236:239], v231 offset:0\n\t"
"ds_write_b64 v228, v[28:29] offset:19208\n\t"
"v_cvt_pk_bf16_f32 v44, v44, v45\n\t"
"v_cvt_pk_bf16_f32 v45, v46, v47\n\t"
"v_mfma_f32_16x16x32_bf16 v[204:207], a[188:191], a[224:227], 0\n\t"
"buffer_load_dwordx4 a[120:123], v248, s[8:11], s20 offen offset:0 sc1\n\t"
"v_mfma_f32_16x16x32_bf16 v[64:67], a[56:59], a[144:147], v[64:67]\n\t"
"v_cvt_pk_bf16_f32 v96, v96, v97\n\t"
"v_cvt_pk_bf16_f32 v97, v98, v99\n\t"
"v_mfma_f32_16x16x32_bf16 v[64:67], a[60:63], a[160:163], v[64:67]\n\t"
"ds_write_b64 v228, v[44:45] offset:19216\n\t"
"v_cvt_pk_bf16_f32 v194, v196, v197\n\t"
"v_cvt_pk_bf16_f32 v195, v198, v199\n\t"
"v_mfma_f32_16x16x32_bf16 v[80:83], a[64:67], a[144:147], v[80:83]\n\t"
"v_cvt_pk_bf16_f32 v112, v112, v113\n\t"
"v_cvt_pk_bf16_f32 v113, v114, v115\n\t"
"v_mfma_f32_16x16x32_bf16 v[80:83], a[68:71], a[160:163], v[80:83]\n\t"
"ds_write_b64 v228, v[96:97] offset:25600\n\t"
"v_mfma_f32_16x16x32_bf16 v[208:211], a[192:195], a[224:227], 0\n\t"
"ds_read_b128 a[156:159], v216 offset:3072\n\t"
"ds_read_b128 a[172:175], v216 offset:11264\n\t"
"buffer_load_dwordx4 a[124:127], v248, s[8:11], s20 offen offset:1024 sc1\n\t"
"ds_write_b64 v228, v[112:113] offset:25608\n\t"
"s_waitcnt lgkmcnt(15)\n\t"
"v_mfma_f32_16x16x32_bf16 v[84:87], a[64:67], a[148:151], v[84:87]\n\t"
"v_cvt_pk_bf16_f32 v128, v128, v129\n\t"
"v_cvt_pk_bf16_f32 v129, v130, v131\n\t"
"v_mfma_f32_16x16x32_bf16 v[84:87], a[68:71], a[164:167], v[84:87]\n\t"
"ds_write_b128 v221, v[192:195] offset:16384\n\t"
"v_cvt_pk_bf16_f32 v200, v200, v201\n\t"
"v_cvt_pk_bf16_f32 v201, v202, v203\n\t"
"v_mfma_f32_16x16x32_bf16 v[68:71], a[56:59], a[148:151], v[68:71]\n\t"
"v_cvt_pk_bf16_f32 v100, v100, v101\n\t"
"v_cvt_pk_bf16_f32 v101, v102, v103\n\t"
"v_mfma_f32_16x16x32_bf16 v[68:71], a[60:63], a[164:167], v[68:71]\n\t"
"ds_write_b64 v228, v[128:129] offset:25616\n\t"
"v_mfma_f32_16x16x32_bf16 v[212:215], a[196:199], a[224:227], 0\n\t"
"v_cvt_pk_bf16_f32 v202, v204, v205\n\t"
"v_cvt_pk_bf16_f32 v203, v206, v207\n\t"
"ds_write_b64 v228, v[100:101] offset:32000\n\t"
"v_cvt_pk_bf16_f32 v116, v116, v117\n\t"
"v_cvt_pk_bf16_f32 v117, v118, v119\n\t"
"v_mfma_f32_16x16x32_bf16 v[52:55], a[48:51], a[148:151], v[52:55]\n\t"
"ds_read_b128 a[144:147], v216 offset:4096\n\t"
"v_mfma_f32_16x16x32_bf16 v[52:55], a[52:55], a[164:167], v[52:55]\n\t"
"ds_read_b128 a[160:163], v216 offset:12288\n\t"
"s_add_u32 s28, s24, 0\n\t"
"s_waitcnt lgkmcnt(15)\n\t"
"buffer_store_dwordx4 a[228:231], v232, s[16:19], s28 offen nt\n\t"
"ds_write_b64 v228, v[116:117] offset:32008\n\t"
"v_cvt_pk_bf16_f32 v132, v132, v133\n\t"
"v_cvt_pk_bf16_f32 v133, v134, v135\n\t"
"v_mfma_f32_16x16x32_bf16 v[192:195], a[200:203], a[224:227], 0\n\t"
"s_waitcnt lgkmcnt(13)\n\t"
"v_mfma_f32_16x16x32_bf16 v[56:59], a[48:51], a[152:155], v[56:59]\n\t"
"v_cvt_pk_bf16_f32 v104, v104, v105\n\t"
"v_cvt_pk_bf16_f32 v105, v106, v107\n\t"
"v_mfma_f32_16x16x32_bf16 v[56:59], a[52:55], a[168:171], v[56:59]\n\t"
"ds_write_b128 v221, v[200:203] offset:24576\n\t"
"ds_write_b64 v228, v[132:133] offset:32016\n\t"
"v_cvt_pk_bf16_f32 v208, v208, v209\n\t"
"v_cvt_pk_bf16_f32 v209, v210, v211\n\t"
"v_mfma_f32_16x16x32_bf16 v[72:75], a[56:59], a[152:155], v[72:75]\n\t"
"v_cvt_pk_bf16_f32 v120, v120, v121\n\t"
"v_cvt_pk_bf16_f32 v121, v122, v123\n\t"
"v_mfma_f32_16x16x32_bf16 v[72:75], a[60:63], a[168:171], v[72:75]\n\t"
"ds_write_b64 v228, v[104:105] offset:38400\n\t"
"ds_read_b128 a[148:151], v216 offset:5120\n\t"
"ds_read_b128 a[164:167], v216 offset:13312\n\t"
"v_mfma_f32_16x16x32_bf16 v[196:199], a[204:207], a[224:227], 0\n\t"
"ds_write_b64 v228, v[120:121] offset:38408\n\t"
"v_cvt_pk_bf16_f32 v136, v136, v137\n\t"
"v_cvt_pk_bf16_f32 v137, v138, v139\n\t"
"v_mfma_f32_16x16x32_bf16 v[88:91], a[64:67], a[152:155], v[88:91]\n\t"
"v_mfma_f32_16x16x32_bf16 v[88:91], a[68:71], a[168:171], v[88:91]\n\t"
"v_cvt_pk_bf16_f32 v210, v212, v213\n\t"
"v_cvt_pk_bf16_f32 v211, v214, v215\n\t"
"ds_write_b64 v228, v[136:137] offset:38416\n\t"
"v_cvt_pk_bf16_f32 v108, v108, v109\n\t"
"v_cvt_pk_bf16_f32 v109, v110, v111\n\t"
"s_waitcnt lgkmcnt(14)\n\t"
"v_mfma_f32_16x16x32_bf16 v[92:95], a[64:67], a[156:159], v[92:95]\n\t"
"v_mfma_f32_16x16x32_bf16 v[92:95], a[68:71], a[172:175], v[92:95]\n\t"
"ds_write_b64 v228, v[108:109] offset:44800\n\t"
"v_cvt_pk_bf16_f32 v124, v124, v125\n\t"
"v_cvt_pk_bf16_f32 v125, v126, v127\n\t"
"v_mfma_f32_16x16x32_bf16 v[76:79], a[56:59], a[156:159], v[76:79]\n\t"
"v_cvt_pk_bf16_f32 v192, v192, v193\n\t"
"v_cvt_pk_bf16_f32 v193, v194, v195\n\t"
"v_mfma_f32_16x16x32_bf16 v[76:79], a[60:63], a[172:175], v[76:79]\n\t"
"ds_write_b128 v221, v[208:211] offset:20480\n\t"
"ds_write_b64 v228, v[124:125] offset:44808\n\t"
"v_mfma_f32_16x16x32_bf16 v[60:63], a[48:51], a[156:159], v[60:63]\n\t"
"v_cvt_pk_bf16_f32 v140, v140, v141\n\t"
"v_cvt_pk_bf16_f32 v141, v142, v143\n\t"
"v_mfma_f32_16x16x32_bf16 v[60:63], a[52:55], a[172:175], v[60:63]\n\t"
"s_waitcnt vmcnt(13) lgkmcnt(11)\n\t"
"v_mfma_f32_16x16x32_bf16 v[144:147], a[72:75], a[144:147], v[144:147]\n\t"
"v_mfma_f32_16x16x32_bf16 v[144:147], a[76:79], a[160:163], v[144:147]\n\t"
"s_add_u32 s28, s24, 0\n\t"
"buffer_store_dwordx4 a[232:235], v233, s[16:19], s28 offen nt\n\t"
"ds_write_b64 v228, v[140:141] offset:44816\n\t"
"v_cvt_pk_bf16_f32 v194, v196, v197\n\t"
"v_cvt_pk_bf16_f32 v195, v198, v199\n\t"
"v_mfma_f32_16x16x32_bf16 v[160:163], a[80:83], a[144:147], v[160:163]\n\t"
"v_mfma_f32_16x16x32_bf16 v[160:163], a[84:87], a[160:163], v[160:163]\n\t"
"ds_write_b128 v221, v[192:195] offset:28672\n\t"
"v_mfma_f32_16x16x32_bf16 v[176:179], a[88:91], a[144:147], v[176:179]\n\t"
"v_mfma_f32_16x16x32_bf16 v[176:179], a[92:95], a[160:163], v[176:179]\n\t"
"ds_read_b128 a[152:155], v216 offset:6144\n\t"
"ds_read_b128 a[168:171], v216 offset:14336\n\t"
"ds_read_b128 a[156:159], v216 offset:7168\n\t"
"ds_read_b128 a[172:175], v216 offset:15360\n\t"
"s_waitcnt lgkmcnt(11)\n\t"
"v_mfma_f32_16x16x32_bf16 v[180:183], a[88:91], a[148:151], v[180:183]\n\t"
"v_mfma_f32_16x16x32_bf16 v[180:183], a[92:95], a[164:167], v[180:183]\n\t"
"v_mfma_f32_16x16x32_bf16 v[164:167], a[80:83], a[148:151], v[164:167]\n\t"
"v_mfma_f32_16x16x32_bf16 v[164:167], a[84:87], a[164:167], v[164:167]\n\t"
"s_add_u32 s28, s24, 0\n\t"
"buffer_store_dwordx4 a[236:239], v234, s[16:19], s28 offen nt\n\t"
"v_mfma_f32_16x16x32_bf16 v[148:151], a[72:75], a[148:151], v[148:151]\n\t"
"v_mfma_f32_16x16x32_bf16 v[148:151], a[76:79], a[164:167], v[148:151]\n\t"
"s_waitcnt lgkmcnt(0)\n\t"
"s_barrier\n\t"
"v_mfma_f32_16x16x32_bf16 v[152:155], a[72:75], a[152:155], v[152:155]\n\t"
"ds_read_b128 a[144:147], v216 offset:16384\n\t"
"v_mfma_f32_16x16x32_bf16 v[152:155], a[76:79], a[168:171], v[152:155]\n\t"
"ds_read_b128 a[160:163], v216 offset:24576\n\t"
"ds_read_b128 a[148:151], v216 offset:17408\n\t"
"v_mfma_f32_16x16x32_bf16 v[168:171], a[80:83], a[152:155], v[168:171]\n\t"
"ds_read_b128 a[164:167], v216 offset:25600\n\t"
"v_mfma_f32_16x16x32_bf16 v[168:171], a[84:87], a[168:171], v[168:171]\n\t"
"ds_read_b128 a[0:3], v229 offset:12800\n\t"
"ds_read_b128 a[4:7], v230 offset:12800\n\t"
"ds_read_b128 a[8:11], v231 offset:12800\n\t"
"v_mfma_f32_16x16x32_bf16 v[184:187], a[88:91], a[152:155], v[184:187]\n\t"
"ds_read_b128 a[152:155], v216 offset:18432\n\t"
"v_mfma_f32_16x16x32_bf16 v[184:187], a[92:95], a[168:171], v[184:187]\n\t"
"ds_read_b128 a[168:171], v216 offset:26624\n\t"
"ds_read_b128 a[12:15], v229 offset:25600\n\t"
"ds_read_b128 a[16:19], v230 offset:25600\n\t"
"v_mfma_f32_16x16x32_bf16 v[188:191], a[88:91], a[156:159], v[188:191]\n\t"
"ds_read_b128 a[20:23], v231 offset:25600\n\t"
"v_mfma_f32_16x16x32_bf16 v[188:191], a[92:95], a[172:175], v[188:191]\n\t"
"ds_read_b128 a[24:27], v229 offset:38400\n\t"
"ds_read_b128 a[28:31], v230 offset:38400\n\t"
"ds_read_b128 a[32:35], v231 offset:38400\n\t"
"v_mfma_f32_16x16x32_bf16 v[172:175], a[80:83], a[156:159], v[172:175]\n\t"
"v_mfma_f32_16x16x32_bf16 v[172:175], a[84:87], a[172:175], v[172:175]\n\t"
"s_add_u32 s28, s24, 1572864\n\t"
"s_waitcnt lgkmcnt(10)\n\t"
"buffer_store_dwordx4 a[0:3], v232, s[16:19], s28 offen nt\n\t"
"s_add_u32 s28, s24, 1572864\n\t"
"s_waitcnt lgkmcnt(9)\n\t"
"buffer_store_dwordx4 a[4:7], v233, s[16:19], s28 offen nt\n\t"
"v_mfma_f32_16x16x32_bf16 v[156:159], a[72:75], a[156:159], v[156:159]\n\t"
"ds_read_b128 a[156:159], v216 offset:19456\n\t"
"v_mfma_f32_16x16x32_bf16 v[156:159], a[76:79], a[172:175], v[156:159]\n\t"
"v_pk_add_f32 v[48:49], v[48:49], v[144:145]\n\t"
"v_pk_add_f32 v[50:51], v[50:51], v[146:147]\n\t"
"v_pk_fma_f32 v[144:145], v[144:145], s[38:39], v[48:49]\n\t"
"v_pk_fma_f32 v[146:147], v[146:147], s[38:39], v[50:51]\n\t"
"v_pk_add_f32 v[64:65], v[64:65], v[160:161]\n\t"
"v_pk_add_f32 v[66:67], v[66:67], v[162:163]\n\t"
"v_pk_fma_f32 v[160:161], v[160:161], s[38:39], v[64:65]\n\t"
"v_pk_fma_f32 v[162:163], v[162:163], s[38:39], v[66:67]\n\t"
"v_pk_add_f32 v[80:81], v[80:81], v[176:177]\n\t"
"v_pk_add_f32 v[82:83], v[82:83], v[178:179]\n\t"
"v_pk_fma_f32 v[176:177], v[176:177], s[38:39], v[80:81]\n\t"
"v_pk_fma_f32 v[178:179], v[178:179], s[38:39], v[82:83]\n\t"
"v_pk_add_f32 v[84:85], v[84:85], v[180:181]\n\t"
"v_pk_add_f32 v[86:87], v[86:87], v[182:183]\n\t"
"v_pk_fma_f32 v[180:181], v[180:181], s[38:39], v[84:85]\n\t"
"v_pk_fma_f32 v[182:183], v[182:183], s[38:39], v[86:87]\n\t"
"v_pk_add_f32 v[68:69], v[68:69], v[164:165]\n\t"
"v_pk_add_f32 v[70:71], v[70:71], v[166:167]\n\t"
"v_pk_fma_f32 v[164:165], v[164:165], s[38:39], v[68:69]\n\t"
"v_pk_fma_f32 v[166:167], v[166:167], s[38:39], v[70:71]\n\t"
"v_pk_add_f32 v[52:53], v[52:53], v[148:149]\n\t"
"v_pk_add_f32 v[54:55], v[54:55], v[150:151]\n\t"
"v_pk_fma_f32 v[148:149], v[148:149], s[38:39], v[52:53]\n\t"
"v_pk_fma_f32 v[150:151], v[150:151], s[38:39], v[54:55]\n\t"
"v_pk_add_f32 v[56:57], v[56:57], v[152:153]\n\t"
"v_pk_add_f32 v[58:59], v[58:59], v[154:155]\n\t"
"v_pk_fma_f32 v[152:153], v[152:153], s[38:39], v[56:57]\n\t"
"v_pk_fma_f32 v[154:155], v[154:155], s[38:39], v[58:59]\n\t"
"v_pk_add_f32 v[72:73], v[72:73], v[168:169]\n\t"
"v_pk_add_f32 v[74:75], v[74:75], v[170:171]\n\t"
"v_pk_fma_f32 v[168:169], v[168:169], s[38:39], v[72:73]\n\t"
"v_pk_fma_f32 v[170:171], v[170:171], s[38:39], v[74:75]\n\t"
"v_pk_add_f32 v[88:89], v[88:89], v[184:185]\n\t"
"v_pk_add_f32 v[90:91], v[90:91], v[186:187]\n\t"
"v_pk_fma_f32 v[184:185], v[184:185], s[38:39], v[88:89]\n\t"
"v_pk_fma_f32 v[186:187], v[186:187], s[38:39], v[90:91]\n\t"
"v_pk_add_f32 v[92:93], v[92:93], v[188:189]\n\t"
"v_pk_add_f32 v[94:95], v[94:95], v[190:191]\n\t"
"v_pk_fma_f32 v[188:189], v[188:189], s[38:39], v[92:93]\n\t"
"v_pk_fma_f32 v[190:191], v[190:191], s[38:39], v[94:95]\n\t"
"v_pk_add_f32 v[76:77], v[76:77], v[172:173]\n\t"
"v_pk_add_f32 v[78:79], v[78:79], v[174:175]\n\t"
"v_pk_fma_f32 v[172:173], v[172:173], s[38:39], v[76:77]\n\t"
"v_pk_fma_f32 v[174:175], v[174:175], s[38:39], v[78:79]\n\t"
"v_pk_add_f32 v[60:61], v[60:61], v[156:157]\n\t"
"v_pk_add_f32 v[62:63], v[62:63], v[158:159]\n\t"
"v_pk_fma_f32 v[156:157], v[156:157], s[38:39], v[60:61]\n\t"
"v_pk_fma_f32 v[158:159], v[158:159], s[38:39], v[62:63]\n\t"
"ds_read_b128 a[172:175], v216 offset:27648\n\t"
"s_add_u32 s28, s24, 1572864\n\t"
"s_waitcnt lgkmcnt(10)\n\t"
"buffer_store_dwordx4 a[8:11], v234, s[16:19], s28 offen nt\n\t"
"s_waitcnt vmcnt(12)\n\t"
"v_mfma_f32_16x16x32_bf16 v[48:51], a[96:99], a[144:147], v[48:51]\n\t"
"v_mfma_f32_16x16x32_bf16 v[48:51], a[100:103], a[160:163], v[48:51]\n\t"
"v_mfma_f32_16x16x32_bf16 v[64:67], a[104:107], a[144:147], v[64:67]\n\t"
"v_mfma_f32_16x16x32_bf16 v[64:67], a[108:111], a[160:163], v[64:67]\n\t"
"v_mfma_f32_16x16x32_bf16 v[80:83], a[112:115], a[144:147], v[80:83]\n\t"
"ds_read_b128 a[144:147], v216 offset:20480\n\t"
"v_mfma_f32_16x16x32_bf16 v[80:83], a[116:119], a[160:163], v[80:83]\n\t"
"ds_read_b128 a[160:163], v216 offset:28672\n\t"
"v_mfma_f32_16x16x32_bf16 v[84:87], a[112:115], a[148:151], v[84:87]\n\t"
"v_mfma_f32_16x16x32_bf16 v[84:87], a[116:119], a[164:167], v[84:87]\n\t"
"v_mfma_f32_16x16x32_bf16 v[68:71], a[104:107], a[148:151], v[68:71]\n\t"
"v_mfma_f32_16x16x32_bf16 v[68:71], a[108:111], a[164:167], v[68:71]\n\t"
"v_cvt_pk_bf16_f32 v48, v48, v49\n\t"
"v_cvt_pk_bf16_f32 v49, v50, v51\n\t"
"v_cvt_pk_bf16_f32 v64, v64, v65\n\t"
"v_cvt_pk_bf16_f32 v65, v66, v67\n\t"
"v_mfma_f32_16x16x32_bf16 v[52:55], a[96:99], a[148:151], v[52:55]\n\t"
"ds_read_b128 a[148:151], v216 offset:21504\n\t"
"v_cvt_pk_bf16_f32 v80, v80, v81\n\t"
"v_mfma_f32_16x16x32_bf16 v[52:55], a[100:103], a[164:167], v[52:55]\n\t"
"ds_read_b128 a[164:167], v216 offset:29696\n\t"
"s_add_u32 s28, s24, 196608\n\t"
"s_waitcnt lgkmcnt(11)\n\t"
"buffer_store_dwordx4 a[12:15], v232, s[16:19], s28 offen nt\n\t"
"v_cvt_pk_bf16_f32 v81, v82, v83\n\t"
"v_mfma_f32_16x16x32_bf16 v[56:59], a[96:99], a[152:155], v[56:59]\n\t"
"v_mfma_f32_16x16x32_bf16 v[56:59], a[100:103], a[168:171], v[56:59]\n\t"
"s_add_u32 s28, s24, 196608\n\t"
"s_waitcnt lgkmcnt(10)\n\t"
"buffer_store_dwordx4 a[16:19], v233, s[16:19], s28 offen nt\n\t"
"ds_write_b64 v228, v[48:49] offset:0\n\t"
"v_mfma_f32_16x16x32_bf16 v[72:75], a[104:107], a[152:155], v[72:75]\n\t"
"v_mfma_f32_16x16x32_bf16 v[72:75], a[108:111], a[168:171], v[72:75]\n\t"
"v_cvt_pk_bf16_f32 v52, v52, v53\n\t"
"v_cvt_pk_bf16_f32 v53, v54, v55\n\t"
"v_cvt_pk_bf16_f32 v68, v68, v69\n\t"
"v_cvt_pk_bf16_f32 v69, v70, v71\n\t"
"v_mfma_f32_16x16x32_bf16 v[88:91], a[112:115], a[152:155], v[88:91]\n\t"
"ds_read_b128 a[152:155], v216 offset:22528\n\t"
"v_cvt_pk_bf16_f32 v84, v84, v85\n\t"
"v_mfma_f32_16x16x32_bf16 v[88:91], a[116:119], a[168:171], v[88:91]\n\t"
"ds_read_b128 a[168:171], v216 offset:30720\n\t"
"v_cvt_pk_bf16_f32 v85, v86, v87\n\t"
"ds_write_b64 v228, v[64:65] offset:8\n\t"
"s_waitcnt lgkmcnt(8)\n\t"
"v_mfma_f32_16x16x32_bf16 v[92:95], a[112:115], a[156:159], v[92:95]\n\t"
"v_mfma_f32_16x16x32_bf16 v[92:95], a[116:119], a[172:175], v[92:95]\n\t"
"ds_write_b64 v228, v[80:81] offset:16\n\t"
"ds_write_b64 v228, v[52:53] offset:6400\n\t"
"v_mfma_f32_16x16x32_bf16 v[76:79], a[104:107], a[156:159], v[76:79]\n\t"
"v_mfma_f32_16x16x32_bf16 v[76:79], a[108:111], a[172:175], v[76:79]\n\t"
"v_cvt_pk_bf16_f32 v56, v56, v57\n\t"
"v_cvt_pk_bf16_f32 v57, v58, v59\n\t"
"v_cvt_pk_bf16_f32 v72, v72, v73\n\t"
"v_cvt_pk_bf16_f32 v73, v74, v75\n\t"
"v_mfma_f32_16x16x32_bf16 v[60:63], a[96:99], a[156:159], v[60:63]\n\t"
"v_cvt_pk_bf16_f32 v88, v88, v89\n\t"
"v_cvt_pk_bf16_f32 v89, v90, v91\n\t"
"v_mfma_f32_16x16x32_bf16 v[60:63], a[100:103], a[172:175], v[60:63]\n\t"
"ds_write_b64 v228, v[68:69] offset:6408\n\t"
"ds_write_b64 v228, v[84:85] offset:6416\n\t"
"s_waitcnt vmcnt(8) lgkmcnt(10)\n\t"
"v_mfma_f32_16x16x32_bf16 v[144:147], a[120:123], a[144:147], v[144:147]\n\t"
"ds_read_b128 a[156:159], v216 offset:23552\n\t"
"v_mfma_f32_16x16x32_bf16 v[144:147], a[124:127], a[160:163], v[144:147]\n\t"
"ds_write_b64 v228, v[56:57] offset:51200\n\t"
"ds_write_b64 v228, v[72:73] offset:51208\n\t"
"ds_write_b64 v228, v[88:89] offset:51216\n\t"
"v_mfma_f32_16x16x32_bf16 v[160:163], a[128:131], a[144:147], v[160:163]\n\t"
"ds_read_b128 a[172:175], v216 offset:31744\n\t"
"v_mfma_f32_16x16x32_bf16 v[160:163], a[132:135], a[160:163], v[160:163]\n\t"
"s_add_u32 s28, s24, 196608\n\t"
"buffer_store_dwordx4 a[20:23], v234, s[16:19], s28 offen nt\n\t"
"v_cvt_pk_bf16_f32 v60, v60, v61\n\t"
"v_cvt_pk_bf16_f32 v61, v62, v63\n\t"
"v_cvt_pk_bf16_f32 v76, v76, v77\n\t"
"v_cvt_pk_bf16_f32 v77, v78, v79\n\t"
"v_cvt_pk_bf16_f32 v92, v92, v93\n\t"
"v_cvt_pk_bf16_f32 v93, v94, v95\n\t"
"v_mfma_f32_16x16x32_bf16 v[176:179], a[136:139], a[144:147], v[176:179]\n\t"
"v_mfma_f32_16x16x32_bf16 v[176:179], a[140:143], a[160:163], v[176:179]\n\t"
"ds_write_b64 v228, v[60:61] offset:57600\n\t"
"ds_write_b64 v228, v[76:77] offset:57608\n\t"
"ds_write_b64 v228, v[92:93] offset:57616\n\t"
"s_waitcnt lgkmcnt(15)\n\t"
"v_mfma_f32_16x16x32_bf16 v[180:183], a[136:139], a[148:151], v[180:183]\n\t"
"v_mfma_f32_16x16x32_bf16 v[180:183], a[140:143], a[164:167], v[180:183]\n\t"
"s_add_u32 s28, s24, 1769472\n\t"
"buffer_store_dwordx4 a[24:27], v232, s[16:19], s28 offen nt\n\t"
"s_add_u32 s28, s24, 1769472\n\t"
"buffer_store_dwordx4 a[28:31], v233, s[16:19], s28 offen nt\n\t"
"v_mfma_f32_16x16x32_bf16 v[164:167], a[128:131], a[148:151], v[164:167]\n\t"
"v_mfma_f32_16x16x32_bf16 v[164:167], a[132:135], a[164:167], v[164:167]\n\t"
"v_cvt_pk_bf16_f32 v144, v144, v145\n\t"
"v_cvt_pk_bf16_f32 v145, v146, v147\n\t"
"v_cvt_pk_bf16_f32 v160, v160, v161\n\t"
"v_cvt_pk_bf16_f32 v161, v162, v163\n\t"
"v_mfma_f32_16x16x32_bf16 v[148:151], a[120:123], a[148:151], v[148:151]\n\t"
"v_cvt_pk_bf16_f32 v176, v176, v177\n\t"
"v_cvt_pk_bf16_f32 v177, v178, v179\n\t"
"v_mfma_f32_16x16x32_bf16 v[148:151], a[124:127], a[164:167], v[148:151]\n\t"
"s_add_u32 s28, s24, 1769472\n\t"
"buffer_store_dwordx4 a[32:35], v234, s[16:19], s28 offen nt\n\t"
"s_waitcnt lgkmcnt(0)\n\t"
"s_barrier\n\t"
"v_mfma_f32_16x16x32_bf16 v[152:155], a[120:123], a[152:155], v[152:155]\n\t"
"ds_read_b128 a[36:39], v229 offset:0\n\t"
"v_mfma_f32_16x16x32_bf16 v[152:155], a[124:127], a[168:171], v[152:155]\n\t"
"ds_write_b64 v228, v[144:145] offset:12800\n\t"
"ds_write_b64 v228, v[160:161] offset:12808\n\t"
"ds_write_b64 v228, v[176:177] offset:12816\n\t"
"v_mfma_f32_16x16x32_bf16 v[168:171], a[128:131], a[152:155], v[168:171]\n\t"
"ds_read_b128 a[40:43], v230 offset:0\n\t"
"v_mfma_f32_16x16x32_bf16 v[168:171], a[132:135], a[168:171], v[168:171]\n\t"
"v_cvt_pk_bf16_f32 v148, v148, v149\n\t"
"v_cvt_pk_bf16_f32 v149, v150, v151\n\t"
"v_cvt_pk_bf16_f32 v164, v164, v165\n\t"
"v_cvt_pk_bf16_f32 v165, v166, v167\n\t"
"v_cvt_pk_bf16_f32 v180, v180, v181\n\t"
"v_cvt_pk_bf16_f32 v181, v182, v183\n\t"
"v_mfma_f32_16x16x32_bf16 v[184:187], a[136:139], a[152:155], v[184:187]\n\t"
"ds_read_b128 a[44:47], v231 offset:0\n\t"
"v_mfma_f32_16x16x32_bf16 v[184:187], a[140:143], a[168:171], v[184:187]\n\t"
"ds_write_b64 v228, v[148:149] offset:19200\n\t"
"ds_write_b64 v228, v[164:165] offset:19208\n\t"
"ds_write_b64 v228, v[180:181] offset:19216\n\t"
"v_mfma_f32_16x16x32_bf16 v[188:191], a[136:139], a[156:159], v[188:191]\n\t"
"ds_read_b128 a[48:51], v229 offset:51200\n\t"
"v_mfma_f32_16x16x32_bf16 v[188:191], a[140:143], a[172:175], v[188:191]\n\t"
"s_add_u32 s28, s24, 384\n\t"
"s_waitcnt lgkmcnt(9)\n\t"
"buffer_store_dwordx4 a[36:39], v232, s[16:19], s28 offen nt\n\t"
"ds_read_b128 a[52:55], v230 offset:51200\n\t"
"ds_read_b128 a[56:59], v231 offset:51200\n\t"
"v_mfma_f32_16x16x32_bf16 v[172:175], a[128:131], a[156:159], v[172:175]\n\t"
"v_mfma_f32_16x16x32_bf16 v[172:175], a[132:135], a[172:175], v[172:175]\n\t"
"v_cvt_pk_bf16_f32 v152, v152, v153\n\t"
"v_cvt_pk_bf16_f32 v153, v154, v155\n\t"
"v_cvt_pk_bf16_f32 v168, v168, v169\n\t"
"v_cvt_pk_bf16_f32 v169, v170, v171\n\t"
"v_cvt_pk_bf16_f32 v184, v184, v185\n\t"
"v_cvt_pk_bf16_f32 v185, v186, v187\n\t"
"v_mfma_f32_16x16x32_bf16 v[156:159], a[120:123], a[156:159], v[156:159]\n\t"
"v_mfma_f32_16x16x32_bf16 v[156:159], a[124:127], a[172:175], v[156:159]\n\t"
"ds_write_b64 v228, v[152:153] offset:25600\n\t"
"ds_write_b64 v228, v[168:169] offset:25608\n\t"
"ds_write_b64 v228, v[184:185] offset:25616\n\t"
"s_add_u32 s28, s24, 384\n\t"
"s_waitcnt lgkmcnt(10)\n\t"
"buffer_store_dwordx4 a[40:43], v233, s[16:19], s28 offen nt\n\t"
"s_add_u32 s28, s24, 384\n\t"
"s_waitcnt lgkmcnt(9)\n\t"
"buffer_store_dwordx4 a[44:47], v234, s[16:19], s28 offen nt\n\t"
"s_add_u32 s28, s24, 1573248\n\t"
"s_waitcnt lgkmcnt(5)\n\t"
"buffer_store_dwordx4 a[48:51], v232, s[16:19], s28 offen nt\n\t"
"s_add_u32 s28, s24, 1573248\n\t"
"s_waitcnt lgkmcnt(4)\n\t"
"buffer_store_dwordx4 a[52:55], v233, s[16:19], s28 offen nt\n\t"
"s_add_u32 s28, s24, 1573248\n\t"
"s_waitcnt lgkmcnt(3)\n\t"
"buffer_store_dwordx4 a[56:59], v234, s[16:19], s28 offen nt\n\t"
"v_cvt_pk_bf16_f32 v156, v156, v157\n\t"
"v_cvt_pk_bf16_f32 v157, v158, v159\n\t"
"ds_write_b64 v228, v[156:157] offset:32000\n\t"
"v_cvt_pk_bf16_f32 v172, v172, v173\n\t"
"v_cvt_pk_bf16_f32 v173, v174, v175\n\t"
"ds_write_b64 v228, v[172:173] offset:32008\n\t"
"v_cvt_pk_bf16_f32 v188, v188, v189\n\t"
"v_cvt_pk_bf16_f32 v189, v190, v191\n\t"
"ds_write_b64 v228, v[188:189] offset:32016\n\t"
"s_waitcnt lgkmcnt(0)\n\t"
"s_barrier\n\t"
"ds_read_b128 a[60:63], v229 offset:12800\n\t"
"ds_read_b128 a[64:67], v230 offset:12800\n\t"
"ds_read_b128 a[68:71], v231 offset:12800\n\t"
"ds_read_b128 a[72:75], v229 offset:25600\n\t"
"ds_read_b128 a[76:79], v230 offset:25600\n\t"
"ds_read_b128 a[80:83], v231 offset:25600\n\t"
"s_add_u32 s28, s24, 196992\n\t"
"s_waitcnt lgkmcnt(5)\n\t"
"buffer_store_dwordx4 a[60:63], v232, s[16:19], s28 offen nt\n\t"
"s_add_u32 s28, s24, 196992\n\t"
"s_waitcnt lgkmcnt(4)\n\t"
"buffer_store_dwordx4 a[64:67], v233, s[16:19], s28 offen nt\n\t"
"s_add_u32 s28, s24, 196992\n\t"
"s_waitcnt lgkmcnt(3)\n\t"
"buffer_store_dwordx4 a[68:71], v234, s[16:19], s28 offen nt\n\t"
"s_add_u32 s28, s24, 1769856\n\t"
"s_waitcnt lgkmcnt(2)\n\t"
"buffer_store_dwordx4 a[72:75], v232, s[16:19], s28 offen nt\n\t"
"s_add_u32 s28, s24, 1769856\n\t"
"s_waitcnt lgkmcnt(1)\n\t"
"buffer_store_dwordx4 a[76:79], v233, s[16:19], s28 offen nt\n\t"
"s_add_u32 s28, s24, 1769856\n\t"
"s_waitcnt lgkmcnt(0)\n\t"
"buffer_store_dwordx4 a[80:83], v234, s[16:19], s28 offen nt\n\t"
"s_waitcnt vmcnt(0)\n\t"
"Lwino_end:\n\t"
  : "+{v205}"(tid)
  : "s"(__builtin_amdgcn_kernarg_segment_ptr()), "s"(blockIdx.x), "s"((unsigned)(size_t)(__attribute__((address_space(3))) char*)smem)
  : "v0", "v1", "v2", "v3", "v4", "v5", "v6", "v7", "v8", "v9", "v10", "v11", "v12", "v13", "v14", "v15", "v16", "v17", "v18", "v19", "v20", "v21", "v22", "v23", "v24", "v25", "v26", "v27", "v28", "v29", "v30", "v31", "v32", "v33", "v34", "v35", "v36", "v37", "v38", "v39", "v40", "v41", "v42", "v43", "v44", "v45", "v46", "v47", "v48", "v49", "v50", "v51", "v52", "v53", "v54", "v55", "v56", "v57", "v58", "v59", "v60", "v61", "v62", "v63", "v64", "v65", "v66", "v67", "v68", "v69", "v70", "v71", "v72", "v73", "v74", "v75", "v76", "v77", "v78", "v79", "v80", "v81", "v82", "v83", "v84", "v85", "v86", "v87", "v88", "v89", "v90", "v91", "v92", "v93", "v94", "v95", "v96", "v97", "v98", "v99", "v100", "v101", "v102", "v103", "v104", "v105", "v106", "v107", "v108", "v109", "v110", "v111", "v112", "v113", "v114", "v115", "v116", "v117", "v118", "v119", "v120", "v121", "v122", "v123", "v124", "v125", "v126", "v127", "v128", "v129", "v130", "v131", "v132", "v133", "v134", "v135", "v136", "v137", "v138", "v139", "v140", "v141", "v142", "v143", "v144", "v145", "v146", "v147", "v148", "v149", "v150", "v151", "v152", "v153", "v154", "v155", "v156", "v157", "v158", "v159", "v160", "v161", "v162", "v163", "v164", "v165", "v166", "v167", "v168", "v169", "v170", "v171", "v172", "v173", "v174", "v175", "v176", "v177", "v178", "v179", "v180", "v181", "v182", "v183", "v184", "v185", "v186", "v187", "v188", "v189", "v190", "v191", "v192", "v193", "v194", "v195", "v196", "v197", "v198", "v199", "v200", "v201", "v202", "v203", "v204", "v206", "v207", "v208", "v209", "v210", "v211", "v212", "v213", "v214", "v215", "v216", "v217", "v218", "v219", "v220", "v221", "v222", "v223", "v224", "v225", "v226", "v227", "v228", "v229", "v230", "v231", "v232", "v233", "v234", "v235", "v236", "v237", "v238", "v239", "v240", "v241", "v242", "v243", "v244", "v245", "v246", "v247", "v248", "v249", "v250", "v251", "v252", "v253", "v254", "a0", "a1", "a2", "a3", "a4", "a5", "a6", "a7", "a8", "a9", "a10", "a11", "a12", "a13", "a14", "a15", "a16", "a17", "a18", "a19", "a20", "a21", "a22", "a23", "a24", "a25", "a26", "a27", "a28", "a29", "a30", "a31", "a32", "a33", "a34", "a35", "a36", "a37", "a38", "a39", "a40", "a41", "a42", "a43", "a44", "a45", "a46", "a47", "a48", "a49", "a50", "a51", "a52", "a53", "a54", "a55", "a56", "a57", "a58", "a59", "a60", "a61", "a62", "a63", "a64", "a65", "a66", "a67", "a68", "a69", "a70", "a71", "a72", "a73", "a74", "a75", "a76", "a77", "a78", "a79", "a80", "a81", "a82", "a83", "a84", "a85", "a86", "a87", "a88", "a89", "a90", "a91", "a92", "a93", "a94", "a95", "a96", "a97", "a98", "a99", "a100", "a101", "a102", "a103", "a104", "a105", "a106", "a107", "a108", "a109", "a110", "a111", "a112", "a113", "a114", "a115", "a116", "a117", "a118", "a119", "a120", "a121", "a122", "a123", "a124", "a125", "a126", "a127", "a128", "a129", "a130", "a131", "a132", "a133", "a134", "a135", "a136", "a137", "a138", "a139", "a140", "a141", "a142", "a143", "a144", "a145", "a146", "a147", "a148", "a149", "a150", "a151", "a152", "a153", "a154", "a155", "a156", "a157", "a158", "a159", "a160", "a161", "a162", "a163", "a164", "a165", "a166", "a167", "a168", "a169", "a170", "a171", "a172", "a173", "a174", "a175", "a176", "a177", "a178", "a179", "a180", "a181", "a182", "a183", "a184", "a185", "a186", "a187", "a188", "a189", "a190", "a191", "a192", "a193", "a194", "a195", "a196", "a197", "a198", "a199", "a200", "a201", "a202", "a203", "a204", "a205", "a206", "a207", "a208", "a209", "a210", "a211", "a212", "a213", "a214", "a215", "a216", "a217", "a218", "a219", "a220", "a221", "a222", "a223", "a224", "a225", "a226", "a227", "a228", "a229", "a230", "a231", "a232", "a233", "a234", "a235", "a236", "a237", "a238", "a239", "a240", "a241", "a242", "a243", "a244", "a245", "a246", "a247", "a248", "a249", "a250", "a251", "a252", "a253", "a254", "a255", "s6", "s7", "s8", "s9", "s10", "s11", "s12", "s13", "s14", "s15", "s16", "s17", "s18", "s19", "s20", "s21", "s22", "s23", "s24", "s25", "s26", "s27", "s28", "s29", "s30", "s31", "s32", "s33", "s34", "s35", "s36", "s37", "s38", "s39", "s40", "s41", "s42", "s43", "s44", "s45", "s46", "s47", "s48", "s49", "s50", "s51", "s52", "s53", "s54", "s55", "s56", "s57", "s58", "s59", "s60", "s61", "s62", "s63", "s64", "s65", "s66", "s67", "s68", "s69", "s70", "s71", "s72", "s73", "s74", "s75", "s76", "s77", "s78", "s79", "s80", "s81", "s82", "s83", "scc", "vcc", "memory");
}

extern "C" void kernel_launch(void* const* d_in, const int* in_sizes, int n_in, void* d_out, int out_size, void* d_ws,
                              size_t ws_size, hipStream_t stream) {
  const __hip_bfloat16* x = (const __hip_bfloat16*)d_in[0];
  const __hip_bfloat16* w = (const __hip_bfloat16*)d_in[1];
  __hip_bfloat16* Uq = (__hip_bfloat16*)d_ws;
  wino_main_kernel<<<256, 256, 0, stream>>>((const char*)x, (char*)Uq, (char*)d_out, (const char*)w);
}
```

```cpp
#include <hip/hip_runtime.h>
#include <hip/hip_bf16.h>
#include <stdint.h>

#define IMG_W 512
#define CH 192
#define PXB 384
#define ROWB (IMG_W * PXB)
#define U_BYTES 1179648

__global__ __launch_bounds__(256, 1) void wino_main_kernel(const char* __restrict__ x, char* __restrict__ Uq, char* __restrict__ out, const char* __restrict__ wt) {
  __shared__ __attribute__((aligned(1024))) char smem[163840];
  unsigned tid = threadIdx.x;
  asm volatile(
"s_load_dwordx8 s[76:83], %1, 0x0\n\t"
"s_mov_b32 s66, %2\n\t"
"s_getpc_b64 s[6:7]\n\t"
"Lwino_pc:\n\t"
"s_bfe_u32 s42, %2, 0x50003\n\t"
"s_lshl_b32 s42, s42, 12\n\t"
"v_lshl_add_u32 v203, v205, 4, s42\n\t"
"v_min_u32 v203, (Lwino_end-Lwino_pc-16)&-16, v203\n\t"
"global_load_dwordx4 v[196:199], v203, s[6:7]\n\t"
"v_and_b32 v204, 63, v205\n\t"
"v_readfirstlane_b32 s73, v205\n\t"
"v_lshrrev_b32 v120, 3, v204\n\t"
"v_and_b32 v121, 7, v204\n\t"
"s_lshr_b32 s73, s73, 6\n\t"
"s_lshr_b32 s56, s66, 5\n\t"
"s_mul_i32 s56, s56, 12288\n\t"
"s_bfe_u32 s61, s66, 0x20001\n\t"
"s_mul_i32 s61, s61, 1536\n\t"
"s_add_u32 s56, s56, s61\n\t"
"s_bitcmp1_b32 s66, 0\n\t"
"s_cselect_b32 s61, 48, 0\n\t"
"s_add_u32 s56, s56, s61\n\t"
"s_bfe_u32 s62, s66, 0x20003\n\t"
"s_mul_i32 s61, s62, 96\n\t"
"s_add_u32 s56, s56, s61\n\t"
"s_cmp_lt_u32 s66, 192\n\t"
"s_cselect_b32 s56, s56, 2.0\n\t"
"v_bfe_u32 v42, v204, 2, 2\n\t"
"v_and_or_b32 v42, v204, 16, v42\n\t"
"v_mul_u32_u24 v42, 0x180, v42\n\t"
"v_lshrrev_b32 v44, 5, v204\n\t"
"v_mul_u32_u24 v43, 0x12000, v44\n\t"
"v_and_b32 v45, 3, v204\n\t"
"v_min_u32 v41, 2, v45\n\t"
"v_lshl_add_u32 v41, v41, 4, v42\n\t"
"v_add_u32 v41, v41, v43\n\t"
"v_lshlrev_b32 v43, 2, v204\n\t"
"v_and_or_b32 v45, v43, 16, v45\n\t"
"v_mul_u32_u24 v45, 0x180, v45\n\t"
"v_lshl_add_u32 v44, v44, 3, v120\n\t"
"v_lshl_add_u32 v45, v44, 1, v45\n\t"
"v_mov_b32 v40, 0\n\t"
"s_mul_i32 s61, s73, 147456\n\t"
"s_add_u32 s61, s61, s56\n\t"
"s_lshl3_add_u32 s62, s73, s56\n\t"
"s_add_u32 s62, s62, 589824\n\t"
"s_lshl_b32 s64, s73, 10\n\t"
"s_and_b32 s60, s66, 7\n\t"
"s_lshr_b32 s65, s66, 3\n\t"
"s_mul_i32 s24, s60, 12582912\n\t"
"s_mul_i32 s63, s65, 6144\n\t"
"s_add_u32 s24, s24, s63\n\t"
"s_add_u32 s23, s24, 196224\n\t"
"s_mul_i32 s63, s73, 786432\n\t"
"s_mul_i32 s26, s73, 4608\n\t"
"s_mov_b32 s48, 189696\n\t"
"s_cmp_eq_u32 s65, 0\n\t"
"s_cselect_b64 s[40:41], 0xff, 0\n\t"
"s_cselect_b64 s[46:47], -1, 0\n\t"
"v_mul_u32_u24 v122, 0x300, v120\n\t"
"v_xor_b32 v123, 4, v121\n\t"
"v_add_u32 v122, s63, v122\n\t"
"v_and_b32 v124, 2, v120\n\t"
"v_xor_b32 v124, v124, v123\n\t"
"v_lshl_add_u32 v128, v124, 4, v122\n\t"
"v_cndmask_b32 v236, v128, 2.0, s[40:41]\n\t"
"v_add_u32 v122, 194816, v122\n\t"
"v_add_u32 v124, -1, v120\n\t"
"v_ashrrev_i32 v125, 31, v124\n\t"
"v_sub_u32 v124, v124, v125\n\t"
"v_and_b32 v124, 2, v124\n\t"
"v_xor_b32 v124, v124, v123\n\t"
"v_mad_i32_i24 v125, v125, s48, v122\n\t"
"v_lshl_add_u32 v129, v124, 4, v125\n\t"
"s_lshl_b64 s[40:41], s[40:41], 8\n\t"
"v_cndmask_b32 v237, v129, 2.0, s[40:41]\n\t"
"v_add_u32 v122, 194816, v122\n\t"
"v_add_u32 v124, -2, v120\n\t"
"v_ashrrev_i32 v125, 31, v124\n\t"
"v_sub_u32 v124, v124, v125\n\t"
"v_and_b32 v124, 2, v124\n\t"
"v_and_or_b32 v124, v125, 4, v124\n\t"
"v_xor_b32 v124, v124, v121\n\t"
"v_mad_i32_i24 v125, v125, s48, v122\n\t"
"v_lshl_add_u32 v130, v124, 4, v125\n\t"
"s_lshl_b64 s[40:41], s[40:41], 8\n\t"
"v_cndmask_b32 v238, v130, 2.0, s[40:41]\n\t"
"v_add_u32 v122, 194816, v122\n\t"
"v_add_u32 v124, -3, v120\n\t"
"v_ashrrev_i32 v125, 31, v124\n\t"
"v_sub_u32 v124, v124, v125\n\t"
"v_and_b32 v124, 2, v124\n\t"
"v_xor_b32 v124, v124, v121\n\t"
"v_mad_i32_i24 v125, v125, s48, v122\n\t"
"v_lshl_add_u32 v131, v124, 4, v125\n\t"
"s_lshl_b64 s[40:41], s[40:41], 8\n\t"
"v_cndmask_b32 v239, v131, 2.0, s[40:41]\n\t"
"v_add_u32 v124, 5, v120\n\t"
"v_and_b32 v124, 2, v124\n\t"
"v_xor_b32 v124, v124, v121\n\t"
"v_add_u32 v122, 2560, v122\n\t"
"v_lshl_add_u32 v240, v124, 4, v122\n\t"
"s_mov_b32 s37, -1\n\t"
"s_mov_b32 s36, 0\n\t"
"s_mul_i32 s67, s73, 48\n\t"
"s_add_u32 s67, s67, 118784\n\t"
"s_mov_b32 s10, 0x200000\n\t"
"s_mov_b32 s11, 0x20000\n\t"
"v_min_u32 v189, 47, v204\n\t"
"v_add_u32 v189, s67, v189\n\t"
"v_mul_u32_u24 v189, 16, v189\n\t"
"s_mov_b32 s54, 663552\n\t"
"s_mov_b32 s55, 0x20000\n\t"
"s_mov_b32 s14, 100663296\n\t"
"s_mov_b32 s15, 0x20000\n\t"
"s_add_u32 m0, s64, 0\n\t"
"s_waitcnt lgkmcnt(0)\n\t"
"s_mov_b32 s52, s82\n\t"
"s_and_b32 s53, s83, 0xffff\n\t"
"buffer_load_dwordx4 v41, s[52:55], s61 offen lds\n\t"
"buffer_load_short_d16_hi v40, v45, s[52:55], s62 offen\n\t"
"s_mov_b32 s8, s78\n\t"
"s_and_b32 s9, s79, 0xffff\n\t"
"buffer_load_dword v188, v189, s[8:11], 0 offen sc1\n\t"
"s_mov_b32 s12, s76\n\t"
"s_and_b32 s13, s77, 0xffff\n\t"
"s_add_u32 m0, s26, 83200\n\t"
"s_nop 0\n\t"
"buffer_load_dwordx4 v236, s[12:15], s23 offen offset:0 nt lds\n\t"
"buffer_load_dwordx4 v237, s[12:15], s23 offen offset:1024 nt lds\n\t"
"buffer_load_dwordx4 v238, s[12:15], s23 offen offset:2048 nt lds\n\t"
"buffer_load_dwordx4 v239, s[12:15], s23 offen offset:3072 nt lds\n\t"
"s_mov_b64 exec, s[36:37]\n\t"
"buffer_load_dwordx4 v240, s[12:15], s23 offen offset:3584 nt lds\n\t"
"s_mov_b64 exec, -1\n\t"
"s_mul_i32 s49, s73, 36\n\t"
"s_mul_i32 s43, s73, 576\n\t"
"s_sub_u32 s42, s23, 203520\n\t"
"s_mov_b32 s44, -1\n\t"
"s_mov_b32 s45, 15\n\t"
"v_add_u32 v124, s49, v204\n\t"
"v_lshrrev_b32 v119, 3, v124\n\t"
"v_and_b32 v124, 7, v124\n\t"
"v_mul_u32_u24 v127, 0x300, v119\n\t"
"v_cmp_gt_u32 vcc, 9, v119\n\t"
"v_add_u32 v119, -9, v119\n\t"
"v_ashrrev_i32 v125, 31, v119\n\t"
"v_add_u32 v127, s42, v127\n\t"
"v_mad_i32_i24 v127, v125, s48, v127\n\t"
"v_and_b32 v125, 9, v125\n\t"
"v_add_u32 v119, v119, v125\n\t"
"v_and_b32 v125, 2, v119\n\t"
"v_xor_b32 v124, v124, v125\n\t"
"v_lshl_add_u32 v133, v124, 4, v127\n\t"
"s_cmp_eq_u32 s60, 0\n\t"
"s_cselect_b64 s[50:51], vcc, 0\n\t"
"v_cmp_eq_u32 vcc, 0, v119\n\t"
"v_cndmask_b32 v246, v133, 2.0, s[50:51]\n\t"
"s_and_b64 vcc, vcc, s[46:47]\n\t"
"v_cndmask_b32 v246, v246, 2.0, vcc\n\t"
"s_add_u32 m0, s43, 80896\n\t"
"s_mov_b64 exec, s[44:45]\n\t"
"buffer_load_dwordx4 v246, s[12:15], 0 offen offset:0 nt lds\n\t"
"s_mov_b64 exec, -1\n\t"
"s_mul_i32 s70, s66, 16\n\t"
"s_add_u32 s70, s70, 1900544\n\t"
"s_lshl_b32 s61, s73, 8\n\t"
"s_or_b32 s61, s61, s66\n\t"
"s_cmp_lt_u32 s61, 192\n\t"
"s_cselect_b32 s70, s70, 2.0\n\t"
"s_lshr_b32 s61, s66, 3\n\t"
"s_lshl2_add_u32 s61, s61, s73\n\t"
"s_lshl_b32 s60, s61, 8\n\t"
"s_cmp_lt_u32 s61, 96\n\t"
"s_cselect_b32 s60, s60, 2.0\n\t"
"v_lshrrev_b32 v118, 1, v204\n\t"
"v_mul_u32_u24 v118, 220928, v118\n\t"
"v_lshl_add_u32 v118, v204, 7, v118\n\t"
"s_mov_b64 exec, 63\n\t"
"buffer_load_dword v118, v118, s[52:55], s60 offen\n\t"
"s_mov_b64 exec, -1\n\t"
"buffer_load_dword v187, off, s[8:11], s70 sc1\n\t"
"v_add_u32 v107, 0, v204\n\t"
"v_mul_u32_u24 v104, 0xab, v107\n\t"
"v_lshrrev_b32 v104, 10, v104\n\t"
"v_mul_u32_u24 v104, 0x120, v104\n\t"
"v_lshl_add_u32 v104, v107, 4, v104\n\t"
"v_add_u32 v107, 64, v204\n\t"
"v_mul_u32_u24 v105, 0xab, v107\n\t"
"v_lshrrev_b32 v105, 10, v105\n\t"
"v_mul_u32_u24 v105, 0x120, v105\n\t"
"v_lshl_add_u32 v105, v107, 4, v105\n\t"
"v_add_u32 v107, 128, v204\n\t"
"v_mul_u32_u24 v106, 0xab, v107\n\t"
"v_lshrrev_b32 v106, 10, v106\n\t"
"v_mul_u32_u24 v106, 0x120, v106\n\t"
"v_lshl_add_u32 v106, v107, 4, v106\n\t"
"s_mul_i32 s74, s73, 6144\n\t"
"s_mul_i32 s72, s73, 96\n\t"
"s_cmp_lt_u32 s73, 2\n\t"
"s_cselect_b32 s75, 0, 65280\n\t"
"s_add_u32 s75, s75, s74\n\t"
"s_add_u32 s75, s75, 65536\n\t"
"s_lshr_b32 s60, s66, 5\n\t"
"s_bfe_u32 s62, s66, 0x20003\n\t"
"s_and_b32 s61, s60, 1\n\t"
"s_lshl1_add_u32 s61, s73, s61\n\t"
"s_mul_i32 s62, s62, 288\n\t"
"s_add_u32 s61, s61, s62\n\t"
"s_lshr_b32 s60, s60, 1\n\t"
"s_mul_i32 s62, s60, 96\n\t"
"s_add_u32 s61, s61, s62\n\t"
"s_lshl_b32 s57, s61, 10\n\t"
"s_and_b32 s61, s66, 7\n\t"
"s_lshl_b32 s61, s61, 7\n\t"
"s_add_u32 s57, s57, s61\n\t"
"s_cmp_lt_u32 s60, 1\n\t"
"s_cselect_b32 s58, 0x3e000000, 0.5\n\t"
"s_cmp_lt_u32 s60, 2\n\t"
"s_cselect_b32 s59, 0x3e800000, 1.0\n\t"
"s_cmp_lt_u32 s73, 3\n\t"
"s_cselect_b32 s67, s66, 0xff\n\t"
"s_cmp_lt_u32 s67, 192\n\t"
"s_cselect_b32 s57, s57, 2.0\n\t"
"s_lshl_b32 s60, s73, 3\n\t"
"s_add_u32 s60, s60, 0\n\t"
"s_lshl_b32 s61, s73, 11\n\t"
"s_add_u32 s61, s61, 4096\n\t"
"v_lshl_add_u32 v46, v121, 6, s60\n\t"
"v_lshl_add_u32 v46, v44, 1, v46\n\t"
"v_lshl_add_u32 v42, v204, 1, s61\n\t"
"v_lshl_add_u32 v43, v204, 4, s61\n\t"
"v_mul_u32_u24 v44, 6144, v120\n\t"
"v_lshl_add_u32 v44, v121, 4, v44\n\t"
"v_mov_b32 v32, 0\n\t"
"v_mov_b32 v33, 0\n\t"
"v_mov_b32 v34, 0\n\t"
"v_mov_b32 v35, 0\n\t"
"v_mov_b32 v36, 0\n\t"
"v_mov_b32 v37, 0\n\t"
"v_mov_b32 v38, 0\n\t"
"v_mov_b32 v39, 0\n\t"
"s_waitcnt vmcnt(9)\n\t"
"s_barrier\n\t"
"ds_read_u16_d16_hi v32, v46 offset:0\n\t"
"ds_read_u16_d16_hi v33, v46 offset:512\n\t"
"ds_read_u16_d16_hi v34, v46 offset:1024\n\t"
"ds_read_u16_d16_hi v35, v46 offset:1536\n\t"
"ds_read_u16_d16_hi v36, v46 offset:2048\n\t"
"ds_read_u16_d16_hi v37, v46 offset:2560\n\t"
"ds_read_u16_d16_hi v38, v46 offset:3072\n\t"
"ds_read_u16_d16_hi v39, v46 offset:3584\n\t"
"s_waitcnt lgkmcnt(1)\n\t"
"v_add_f32 v48, v32, v38\n\t"
"s_waitcnt lgkmcnt(0)\n\t"
"v_add_f32 v49, v33, v39\n\t"
"v_add_f32 v50, v34, v40\n\t"
"v_add_f32 v51, v48, v35\n\t"
"v_sub_f32 v54, v48, v35\n\t"
"v_add_f32 v52, v49, v36\n\t"
"v_sub_f32 v55, v49, v36\n\t"
"v_add_f32 v53, v50, v37\n\t"
"v_sub_f32 v56, v50, v37\n\t"
"v_add_f32 v57, v32, v34\n\t"
"v_add_f32 v58, v51, v53\n\t"
"v_add_f32 v59, v54, v56\n\t"
"v_add_f32 v60, v38, v40\n\t"
"v_add_f32 v62, v57, v33\n\t"
"v_sub_f32 v63, v57, v33\n\t"
"v_add_f32 v64, v58, v52\n\t"
"v_sub_f32 v65, v58, v52\n\t"
"v_add_f32 v66, v59, v55\n\t"
"v_sub_f32 v67, v59, v55\n\t"
"v_add_f32 v68, v60, v39\n\t"
"v_sub_f32 v69, v60, v39\n\t"
"v_mul_f32 v70, s59, v32\n\t"
"v_mul_f32 v71, 0x3e800000, v62\n\t"
"v_mul_f32 v72, 0x3e800000, v63\n\t"
"v_mul_f32 v73, -s59, v34\n\t"
"v_mul_f32 v74, s58, v51\n\t"
"v_mul_f32 v75, 0x3e800000, v64\n\t"
"v_mul_f32 v76, 0x3e800000, v65\n\t"
"v_mul_f32 v77, -s58, v53\n\t"
"v_mul_f32 v78, s58, v54\n\t"
"v_mul_f32 v79, 0x3e800000, v66\n\t"
"v_mul_f32 v80, 0x3e800000, v67\n\t"
"v_mul_f32 v81, -s58, v56\n\t"
"v_mul_f32 v82, -s59, v38\n\t"
"v_mul_f32 v83, 0xbe800000, v68\n\t"
"v_mul_f32 v84, 0xbe800000, v69\n\t"
"v_mul_f32 v85, s59, v40\n\t"
"v_cvt_pk_bf16_f32 v86, v70, v71\n\t"
"v_cvt_pk_bf16_f32 v87, v72, v73\n\t"
"v_cvt_pk_bf16_f32 v88, v74, v75\n\t"
"v_cvt_pk_bf16_f32 v89, v76, v77\n\t"
"v_cvt_pk_bf16_f32 v90, v78, v79\n\t"
"v_cvt_pk_bf16_f32 v91, v80, v81\n\t"
"v_cvt_pk_bf16_f32 v92, v82, v83\n\t"
"v_cvt_pk_bf16_f32 v93, v84, v85\n\t"
"ds_write_b16 v42, v86 offset:0\n\t"
"ds_write_b16_d16_hi v42, v86 offset:128\n\t"
"ds_write_b16 v42, v87 offset:256\n\t"
"ds_write_b16_d16_hi v42, v87 offset:384\n\t"
"ds_write_b16 v42, v88 offset:512\n\t"
"ds_write_b16_d16_hi v42, v88 offset:640\n\t"
"ds_write_b16 v42, v89 offset:768\n\t"
"ds_write_b16_d16_hi v42, v89 offset:896\n\t"
"ds_write_b16 v42, v90 offset:1024\n\t"
"ds_write_b16_d16_hi v42, v90 offset:1152\n\t"
"ds_write_b16 v42, v91 offset:1280\n\t"
"ds_write_b16_d16_hi v42, v91 offset:1408\n\t"
"ds_write_b16 v42, v92 offset:1536\n\t"
"ds_write_b16_d16_hi v42, v92 offset:1664\n\t"
"ds_write_b16 v42, v93 offset:1792\n\t"
"ds_write_b16_d16_hi v42, v93 offset:1920\n\t"
"ds_read_b128 v[96:99], v43 offset:0\n\t"
"ds_read_b128 v[100:103], v43 offset:1024\n\t"
"s_add_u32 s60, s57, 0\n\t"
"s_waitcnt lgkmcnt(1)\n\t"
"buffer_store_dwordx4 v[96:99], v44, s[8:11], s60 offen sc1\n\t"
"s_add_u32 s60, s57, 49152\n\t"
"s_waitcnt lgkmcnt(0)\n\t"
"buffer_store_dwordx4 v[100:103], v44, s[8:11], s60 offen sc1\n\t"
"v_and_b32 v122, 15, v204\n\t"
"v_lshrrev_b32 v123, 4, v204\n\t"
"v_bfe_u32 v124, v204, 2, 2\n\t"
"v_and_b32 v125, 3, v204\n\t"
"v_lshlrev_b32 v125, 3, v125\n\t"
"s_mov_b32 s60, 0x1be4\n\t"
"s_lshl_b32 s61, s73, 10\n\t"
"s_mov_b32 s62, 512\n\t"
"s_add_u32 s63, s26, 1152\n\t"
"v_and_b32 v126, 14, v204\n\t"
"v_bfe_u32 v126, s60, v126, 2\n\t"
"v_xor_b32 v126, v126, v123\n\t"
"v_lshlrev_b32 v126, 4, v126\n\t"
"v_lshl_add_u32 v216, v122, 6, v126\n\t"
"v_add_u32 v221, s61, v216\n\t"
"s_add_u32 m0, s74, 16384\n\t"
"s_add_u32 s27, s72, 0\n\t"
"buffer_load_dwordx4 v104, s[52:55], s27 offen lds\n\t"
"v_lshrrev_b32 v126, 1, v121\n\t"
"v_xor_b32 v126, v126, v123\n\t"
"v_lshl_add_u32 v126, v126, 4, s61\n\t"
"v_lshl_add_u32 v126, v121, 6, v126\n\t"
"v_and_b32 v127, 8, v204\n\t"
"v_lshl_add_u32 v217, v127, 9, v126\n\t"
"v_xad_u32 v219, v217, 48, s62\n\t"
"v_sub_u32 v126, 4, v124\n\t"
"v_cmp_gt_u32 vcc, 32, v204\n\t"
"v_mov_b32 v135, 0x3f80bf80\n\t"
"s_nop 0\n\t"
"v_cndmask_b32 v126, v126, v124, vcc\n\t"
"v_and_b32 v127, 2, v126\n\t"
"v_lshl_add_u32 v127, v127, 4, v125\n\t"
"v_lshl_add_u32 v127, v126, 7, v127\n\t"
"s_add_u32 m0, s74, 17408\n\t"
"s_add_u32 s27, s72, 0\n\t"
"buffer_load_dwordx4 v105, s[52:55], s27 offen lds\n\t"
"v_and_b32 v126, 1, v123\n\t"
"v_mul_u32_u24 v126, 1216, v126\n\t"
"v_add3_u32 v226, v126, v127, s63\n\t"
"v_min_u32 v126, 2, v124\n\t"
"v_cmp_gt_u32 vcc, 48, v204\n\t"
"s_nop 1\n\t"
"v_cndmask_b32 v126, v126, v123, vcc\n\t"
"v_and_b32 v134, 1, v126\n\t"
"v_cndmask_b32 v127, 4, v124, vcc\n\t"
"v_lshl_add_u32 v134, v134, 6, v125\n\t"
"v_lshl_add_u32 v134, v127, 7, v134\n\t"
"v_and_b32 v127, 2, v127\n\t"
"v_lshl_add_u32 v134, v127, 4, v134\n\t"
"v_mul_u32_u24 v126, 2304, v126\n\t"
"v_add3_u32 v227, v126, v134, s26\n\t"
"s_add_u32 m0, s74, 18432\n\t"
"s_add_u32 s27, s72, 0\n\t"
"buffer_load_dwordx4 v106, s[52:55], s27 offen lds\n\t"
"v_and_b32 v126, 24, v204\n\t"
"v_cmp_eq_u32 vcc, 8, v126\n\t"
"v_mov_b32 v134, 0xbf803f80\n\t"
"s_nop 0\n\t"
"v_cndmask_b32 v134, v134, v135, vcc\n\t"
"v_lshrrev_b32 v135, 16, v134\n\t"
"v_cmp_eq_u32 vcc, 7, v121\n\t"
"s_nop 1\n\t"
"v_cndmask_b32 v135, 0, v135, vcc\n\t"
"v_cmp_gt_u32 vcc, 32, v204\n\t"
"s_mov_b32 s48, 384\n\t"
"s_cmp_eq_u32 s65, 31\n\t"
"v_cndmask_b32 v134, v135, v134, vcc\n\t"
"v_cndmask_b32 v136, 4, v121, vcc\n\t"
"v_mov_b32 v135, 0\n\t"
"v_and_b32 v137, 1, v136\n\t"
"v_lshlrev_b32 v137, 4, v137\n\t"
"v_lshrrev_b32 v136, 1, v136\n\t"
"v_lshlrev_b64 v[134:135], v137, v[134:135]\n\t"
"v_cmp_eq_u32 vcc, 0, v136\n\t"
"s_cselect_b64 s[40:41], 0xff, 0\n\t"
"s_cselect_b64 s[46:47], -1, 0\n\t"
"s_add_u32 m0, s74, 19456\n\t"
"s_add_u32 s27, s72, 12288\n\t"
"buffer_load_dwordx4 v104, s[52:55], s27 offen lds\n\t"
"v_cndmask_b32 v140, 0, v134, vcc\n\t"
"v_cndmask_b32 v141, 0, v135, vcc\n\t"
"v_cmp_eq_u32 vcc, 1, v136\n\t"
"v_accvgpr_write_b32 a220, v140\n\t"
"v_xad_u32 v241, v128, 64, s48\n\t"
"v_cndmask_b32 v141, v141, v134, vcc\n\t"
"v_cndmask_b32 v142, 0, v135, vcc\n\t"
"v_cmp_eq_u32 vcc, 2, v136\n\t"
"s_add_u32 m0, s74, 20480\n\t"
"s_add_u32 s27, s72, 12288\n\t"
"buffer_load_dwordx4 v105, s[52:55], s27 offen lds\n\t"
"v_accvgpr_write_b32 a221, v141\n\t"
"v_xad_u32 v242, v129, 64, s48\n\t"
"v_cndmask_b32 v142, v142, v134, vcc\n\t"
"v_cndmask_b32 v143, 0, v135, vcc\n\t"
"v_cmp_eq_u32 vcc, 3, v136\n\t"
"v_accvgpr_write_b32 a222, v142\n\t"
"v_cndmask_b32 v242, v242, 2.0, s[40:41]\n\t"
"v_cndmask_b32 v143, v143, v134, vcc\n\t"
"v_accvgpr_write_b32 a223, v143\n\t"
"s_lshl_b64 s[40:41], s[40:41], 8\n\t"
"v_xad_u32 v243, v130, 64, s48\n\t"
"v_cndmask_b32 v243, v243, 2.0, s[40:41]\n\t"
"s_lshl_b64 s[40:41], s[40:41], 8\n\t"
"v_xad_u32 v244, v131, 64, s48\n\t"
"s_add_u32 m0, s74, 21504\n\t"
"s_add_u32 s27, s72, 12288\n\t"
"buffer_load_dwordx4 v106, s[52:55], s27 offen lds\n\t"
"v_cndmask_b32 v244, v244, 2.0, s[40:41]\n\t"
"s_lshl_b64 s[40:41], s[40:41], 40\n\t"
"v_xad_u32 v245, v240, 64, s48\n\t"
"v_cndmask_b32 v245, v245, 2.0, s[40:41]\n\t"
"v_cmp_eq_u32 vcc, 8, v119\n\t"
"v_xad_u32 v247, v133, 64, s48\n\t"
"s_and_b64 vcc, vcc, s[46:47]\n\t"
"v_cndmask_b32 v247, v247, 2.0, s[50:51]\n\t"
"v_cndmask_b32 v247, v247, 2.0, vcc\n\t"
"v_bfe_u32 v126, v204, 3, 1\n\t"
"v_lshrrev_b32 v127, 4, v204\n\t"
"v_sub_u32 v127, v127, v126\n\t"
"v_mov_b32 v134, 0xbf803f80\n\t"
"v_mov_b32 v135, 0x3f80bf80\n\t"
"v_cmp_eq_u32 vcc, 0, v127\n\t"
"s_add_u32 m0, s74, 40960\n\t"
"s_add_u32 s27, s72, 442368\n\t"
"buffer_load_dwordx4 v104, s[52:55], s27 offen lds\n\t"
"v_add_u32 v126, 4, v126\n\t"
"v_cndmask_b32 v134, 0, v134, vcc\n\t"
"v_cmp_eq_u32 vcc, 1, v127\n\t"
"s_nop 1\n\t"
"v_cndmask_b32 v134, v134, v135, vcc\n\t"
"v_cmp_eq_u32 vcc, 7, v121\n\t"
"s_nop 1\n\t"
"v_cndmask_b32 v135, 0, v135, vcc\n\t"
"v_cmp_gt_u32 vcc, 48, v204\n\t"
"s_nop 1\n\t"
"v_cndmask_b32 v134, v135, v134, vcc\n\t"
"v_mov_b32 v135, 0\n\t"
"v_cndmask_b32 v136, v126, v121, vcc\n\t"
"v_and_b32 v137, 1, v136\n\t"
"v_lshlrev_b32 v137, 4, v137\n\t"
"v_lshrrev_b32 v136, 1, v136\n\t"
"v_lshlrev_b64 v[134:135], v137, v[134:135]\n\t"
"s_add_u32 m0, s74, 41984\n\t"
"s_add_u32 s27, s72, 442368\n\t"
"buffer_load_dwordx4 v105, s[52:55], s27 offen lds\n\t"
"v_cmp_eq_u32 vcc, 0, v136\n\t"
"s_mov_b32 s16, s80\n\t"
"s_and_b32 s17, s81, 0xffff\n\t"
"v_cndmask_b32 v140, 0, v134, vcc\n\t"
"v_cndmask_b32 v141, 0, v135, vcc\n\t"
"v_cmp_eq_u32 vcc, 1, v136\n\t"
"v_accvgpr_write_b32 a224, v140\n\t"
"v_and_b32 v144, 0xfefffeff, v140\n\t"
"v_cndmask_b32 v141, v141, v134, vcc\n\t"
"v_cndmask_b32 v142, 0, v135, vcc\n\t"
"v_cmp_eq_u32 vcc, 2, v136\n\t"
"v_accvgpr_write_b32 a225, v141\n\t"
"v_xor_b32 v148, 0x80008000, v144\n\t"
"v_cndmask_b32 v142, v142, v134, vcc\n\t"
"s_add_u32 m0, s74, 43008\n\t"
"s_add_u32 s27, s72, 442368\n\t"
"buffer_load_dwordx4 v106, s[52:55], s27 offen lds\n\t"
"v_cndmask_b32 v143, 0, v135, vcc\n\t"
"v_cmp_eq_u32 vcc, 3, v136\n\t"
"v_accvgpr_write_b32 a226, v142\n\t"
"v_and_b32 v145, 0xfefffeff, v141\n\t"
"v_cndmask_b32 v143, v143, v134, vcc\n\t"
"v_accvgpr_write_b32 a227, v143\n\t"
"v_xor_b32 v149, 0x80008000, v145\n\t"
"v_and_b32 v146, 0xfefffeff, v142\n\t"
"v_xor_b32 v150, 0x80008000, v146\n\t"
"v_and_b32 v147, 0xfefffeff, v143\n\t"
"v_xor_b32 v151, 0x80008000, v147\n\t"
"s_mov_b32 s18, 100663296\n\t"
"s_mov_b32 s19, 0x20000\n\t"
"s_add_u32 s22, s23, 3145728\n\t"
"s_add_u32 m0, s74, 44032\n\t"
"s_add_u32 s27, s72, 454656\n\t"
"buffer_load_dwordx4 v104, s[52:55], s27 offen lds\n\t"
"s_mov_b32 s21, 4\n\t"
"s_mov_b32 s38, -2.0\n\t"
"s_mov_b32 s39, -2.0\n\t"
"s_mov_b32 s68, 3000\n\t"
"s_mul_i32 s61, s73, 294912\n\t"
"v_lshl_add_u32 v248, v204, 4, s61\n\t"
"v_mul_u32_u24 v107, 24, v204\n\t"
"v_add_u32 v47, s75, v107\n\t"
"v_add_u32 v107, s74, v107\n\t"
"v_lshrrev_b32 v112, 4, v204\n\t"
"v_bfe_u32 v113, v204, 2, 2\n\t"
"v_cmp_eq_u32 vcc, v112, v113\n\t"
"v_lshlrev_b32 v112, 4, v204\n\t"
"v_mov_b32 v113, 0x3e00\n\t"
"s_add_u32 m0, s74, 45056\n\t"
"s_add_u32 s27, s72, 454656\n\t"
"buffer_load_dwordx4 v105, s[52:55], s27 offen lds\n\t"
"v_lshlrev_b32 v113, v112, v113\n\t"
"v_cndmask_b32 v113, 0, v113, vcc\n\t"
"v_and_b32 v112, 2, v204\n\t"
"v_cmp_eq_u32 vcc, 0, v112\n\t"
"v_mov_b32 v108, 0\n\t"
"v_mov_b32 v109, 0\n\t"
"v_cndmask_b32 v110, 0, v113, vcc\n\t"
"v_cndmask_b32 v111, v113, 0, vcc\n\t"
"v_xor_b32 v114, 0x80008000, v110\n\t"
"v_xor_b32 v115, 0x80008000, v111\n\t"
"v_mov_b32 v112, 0\n\t"
"v_mov_b32 v113, 0\n\t"
"v_mov_b32 v116, 0\n\t"
"v_mov_b32 v117, 0\n\t"
"s_add_u32 m0, s74, 46080\n\t"
"s_add_u32 s27, s72, 454656\n\t"
"buffer_load_dwordx4 v106, s[52:55], s27 offen lds\n\t"
"s_waitcnt vmcnt(16)\n\t"
"s_waitcnt vmcnt(12)\n\t"
"v_readfirstlane_b32 s71, v187\n\t"
"s_barrier\n\t"
"s_add_u32 s71, s71, 1\n\t"
"v_mov_b32 v215, s71\n\t"
"s_mov_b64 exec, 1\n\t"
"buffer_store_dword v215, off, s[8:11], s70 sc1\n\t"
"s_mov_b64 exec, -1\n\t"
"v_add_u32 v226, 39424, v226\n\t"
"v_xor_b32 v218, 32, v226\n\t"
"v_xor_b32 v220, 64, v226\n\t"
"v_xor_b32 v255, 96, v226\n\t"
"ds_read_b64_tr_b16 a[176:177], v226 offset:41472\n\t"
"ds_read_b64_tr_b16 a[178:179], v226 offset:41984\n\t"
"ds_read_b64_tr_b16 a[180:181], v218 offset:41472\n\t"
"ds_read_b64_tr_b16 a[182:183], v218 offset:41984\n\t"
"s_add_u32 m0, s75, 0\n\t"
"s_add_u32 s27, s72, 221184\n\t"
"buffer_load_dwordx4 v104, s[52:55], s27 offen lds\n\t"
"ds_read_b64_tr_b16 a[184:185], v220 offset:43776\n\t"
"ds_read_b64_tr_b16 a[186:187], v220 offset:44288\n\t"
"ds_read_b64_tr_b16 a[188:189], v255 offset:43776\n\t"
"ds_read_b64_tr_b16 a[190:191], v255 offset:44288\n\t"
"s_add_u32 m0, s75, 1024\n\t"
"s_add_u32 s27, s72, 221184\n\t"
"buffer_load_dwordx4 v105, s[52:55], s27 offen lds\n\t"
"ds_read_b64_tr_b16 a[192:193], v220 offset:41472\n\t"
"ds_read_b64_tr_b16 a[194:195], v220 offset:41984\n\t"
"ds_read_b64_tr_b16 a[196:197], v255 offset:41472\n\t"
"ds_read_b64_tr_b16 a[198:199], v255 offset:41984\n\t"
"s_add_u32 m0, s75, 2048\n\t"
"s_add_u32 s27, s72, 221184\n\t"
"buffer_load_dwordx4 v106, s[52:55], s27 offen lds\n\t"
"ds_read_b64_tr_b16 a[200:201], v226 offset:43776\n\t"
"ds_read_b64_tr_b16 a[202:203], v226 offset:44288\n\t"
"ds_read_b64_tr_b16 a[204:205], v218 offset:43776\n\t"
"ds_read_b64_tr_b16 a[206:207], v218 offset:44288\n\t"
"s_add_u32 m0, s75, 3072\n\t"
"s_add_u32 s27, s72, 233472\n\t"
"buffer_load_dwordx4 v104, s[52:55], s27 offen lds\n\t"
"s_waitcnt lgkmcnt(14)\n\t"
"v_mfma_f32_16x16x32_bf16 v[0:3], a[176:179], a[220:223], 0\n\t"
"s_waitcnt lgkmcnt(12)\n\t"
"v_mfma_f32_16x16x32_bf16 v[4:7], a[180:183], a[220:223], 0\n\t"
"s_waitcnt lgkmcnt(10)\n\t"
"v_mfma_f32_16x16x32_bf16 v[8:11], a[184:187], a[220:223], 0\n\t"
"s_waitcnt lgkmcnt(8)\n\t"
"v_mfma_f32_16x16x32_bf16 v[12:15], a[188:191], a[220:223], 0\n\t"
"s_add_u32 m0, s75, 4096\n\t"
"s_add_u32 s27, s72, 233472\n\t"
"buffer_load_dwordx4 v105, s[52:55], s27 offen lds\n\t"
"s_add_u32 m0, s75, 5120\n\t"
"s_add_u32 s27, s72, 233472\n\t"
"buffer_load_dwordx4 v106, s[52:55], s27 offen lds\n\t"
"s_waitcnt lgkmcnt(6)\n\t"
"v_mfma_f32_16x16x32_bf16 v[16:19], a[192:195], a[220:223], 0\n\t"
"s_waitcnt lgkmcnt(4)\n\t"
"v_mfma_f32_16x16x32_bf16 v[20:23], a[196:199], a[220:223], 0\n\t"
"s_waitcnt lgkmcnt(2)\n\t"
"v_mfma_f32_16x16x32_bf16 v[24:27], a[200:203], a[220:223], 0\n\t"
"s_waitcnt lgkmcnt(0)\n\t"
"v_mfma_f32_16x16x32_bf16 v[28:31], a[204:207], a[220:223], 0\n\t"
"s_add_u32 m0, s26, 124544\n\t"
"s_nop 0\n\t"
"buffer_load_dwordx4 v236, s[12:15], s23 offen offset:128 nt lds\n\t"
"buffer_load_dwordx4 v237, s[12:15], s23 offen offset:1152 nt lds\n\t"
"v_cvt_pk_bf16_f32 v0, v0, v1\n\t"
"v_cvt_pk_bf16_f32 v1, v2, v3\n\t"
"v_cvt_pk_bf16_f32 v2, v4, v5\n\t"
"v_cvt_pk_bf16_f32 v3, v6, v7\n\t"
"ds_write_b128 v217, v[0:3] offset:0\n\t"
"v_cvt_pk_bf16_f32 v8, v8, v9\n\t"
"v_cvt_pk_bf16_f32 v9, v10, v11\n\t"
"v_cvt_pk_bf16_f32 v10, v12, v13\n\t"
"v_cvt_pk_bf16_f32 v11, v14, v15\n\t"
"ds_write_b128 v219, v[8:11] offset:0\n\t"
"v_cvt_pk_bf16_f32 v16, v16, v17\n\t"
"v_cvt_pk_bf16_f32 v17, v18, v19\n\t"
"v_cvt_pk_bf16_f32 v18, v20, v21\n\t"
"v_cvt_pk_bf16_f32 v19, v22, v23\n\t"
"ds_write_b128 v217, v[16:19] offset:8192\n\t"
"v_cvt_pk_bf16_f32 v24, v24, v25\n\t"
"v_cvt_pk_bf16_f32 v25, v26, v27\n\t"
"v_cvt_pk_bf16_f32 v26, v28, v29\n\t"
"v_cvt_pk_bf16_f32 v27, v30, v31\n\t"
"ds_write_b128 v219, v[24:27] offset:8192\n\t"
"v_add_u32 v249, 80896, v227\n\t"
"v_xor_b32 v250, 32, v249\n\t"
"v_xor_b32 v251, 64, v249\n\t"
"v_xor_b32 v252, 96, v249\n\t"
"ds_read_b64_tr_b16 a[176:177], v249 offset:0\n\t"
"ds_read_b64_tr_b16 a[178:179], v249 offset:512\n\t"
"ds_read_b64_tr_b16 a[180:181], v250 offset:0\n\t"
"ds_read_b64_tr_b16 a[182:183], v250 offset:512\n\t"
"buffer_load_dwordx4 v238, s[12:15], s23 offen offset:2176 nt lds\n\t"
"ds_read_b64_tr_b16 a[184:185], v251 offset:0\n\t"
"ds_read_b64_tr_b16 a[186:187], v251 offset:512\n\t"
"ds_read_b64_tr_b16 a[188:189], v252 offset:0\n\t"
"ds_read_b64_tr_b16 a[190:191], v252 offset:512\n\t"
"buffer_load_dwordx4 v239, s[12:15], s23 offen offset:3200 nt lds\n\t"
"ds_read_b64_tr_b16 a[192:193], v249 offset:1152\n\t"
"ds_read_b64_tr_b16 a[194:195], v249 offset:1664\n\t"
"ds_read_b64_tr_b16 a[196:197], v250 offset:1152\n\t"
"ds_read_b64_tr_b16 a[198:199], v250 offset:1664\n\t"
"s_mov_b64 exec, s[36:37]\n\t"
"buffer_load_dwordx4 v240, s[12:15], s23 offen offset:3712 nt lds\n\t"
"s_mov_b64 exec, -1\n\t"
"ds_read_b64_tr_b16 a[200:201], v251 offset:1152\n\t"
"ds_read_b64_tr_b16 a[202:203], v251 offset:1664\n\t"
"ds_read_b64_tr_b16 a[204:205], v252 offset:1152\n\t"
"ds_read_b64_tr_b16 a[206:207], v252 offset:1664\n\t"
"s_add_u32 m0, s43, 122240\n\t"
"s_mov_b64 exec, s[44:45]\n\t"
"buffer_load_dwordx4 v246, s[12:15], 0 offen offset:128 nt lds\n\t"
"s_mov_b64 exec, -1\n\t"
"s_waitcnt vmcnt(19)\n\t"
"ds_read_b64_tr_b16 a[48:49], v107 offset:16384\n\t"
"ds_read_b64_tr_b16 a[50:51], v107 offset:17920\n\t"
"ds_read_b64_tr_b16 a[52:53], v107 offset:19456\n\t"
"ds_read_b64_tr_b16 a[54:55], v107 offset:20992\n\t"
"ds_read_b64_tr_b16 a[56:57], v107 offset:16392\n\t"
"ds_read_b64_tr_b16 a[58:59], v107 offset:17928\n\t"
"ds_read_b64_tr_b16 a[60:61], v107 offset:19464\n\t"
"ds_read_b64_tr_b16 a[62:63], v107 offset:21000\n\t"
"ds_read_b64_tr_b16 a[64:65], v107 offset:16400\n\t"
"ds_read_b64_tr_b16 a[66:67], v107 offset:17936\n\t"
"ds_read_b64_tr_b16 a[68:69], v107 offset:19472\n\t"
"ds_read_b64_tr_b16 a[70:71], v107 offset:21008\n\t"
"s_waitcnt vmcnt(13)\n\t"
"ds_read_b64_tr_b16 a[72:73], v107 offset:40960\n\t"
"ds_read_b64_tr_b16 a[74:75], v107 offset:42496\n\t"
"ds_read_b64_tr_b16 a[76:77], v107 offset:44032\n\t"
"ds_read_b64_tr_b16 a[78:79], v107 offset:45568\n\t"
"ds_read_b64_tr_b16 a[80:81], v107 offset:40968\n\t"
"ds_read_b64_tr_b16 a[82:83], v107 offset:42504\n\t"
"ds_read_b64_tr_b16 a[84:85], v107 offset:44040\n\t"
"ds_read_b64_tr_b16 a[86:87], v107 offset:45576\n\t"
"ds_read_b64_tr_b16 a[88:89], v107 offset:40976\n\t"
"ds_read_b64_tr_b16 a[90:91], v107 offset:42512\n\t"
"ds_read_b64_tr_b16 a[92:93], v107 offset:44048\n\t"
"ds_read_b64_tr_b16 a[94:95], v107 offset:45584\n\t"
"s_waitcnt lgkmcnt(15)\n\t"
"v_mfma_f32_16x16x32_bf16 v[0:3], v[110:113], a[48:51], 0\n\t"
"s_waitcnt lgkmcnt(10)\n\t"
"v_mfma_f32_16x16x32_bf16 v[0:3], v[110:113], a[72:75], v[0:3]\n\t"
"v_mfma_f32_16x16x32_bf16 v[8:11], v[108:111], a[48:51], 0\n\t"
"v_mfma_f32_16x16x32_bf16 v[8:11], v[108:111], a[72:75], v[8:11]\n\t"
"s_add_u32 m0, s26, 103936\n\t"
"s_nop 0\n\t"
"buffer_load_dwordx4 v241, s[12:15], s23 offen offset:0 nt lds\n\t"
"v_mfma_f32_16x16x32_bf16 v[16:19], v[110:113], a[52:55], 0\n\t"
"s_waitcnt lgkmcnt(8)\n\t"
"v_mfma_f32_16x16x32_bf16 v[16:19], v[110:113], a[76:79], v[16:19]\n\t"
"v_mfma_f32_16x16x32_bf16 v[24:27], v[108:111], a[52:55], 0\n\t"
"v_mfma_f32_16x16x32_bf16 v[24:27], v[108:111], a[76:79], v[24:27]\n\t"
"buffer_load_dwordx4 v242, s[12:15], s23 offen offset:1024 nt lds\n\t"
"s_waitcnt vmcnt(8)\n\t"
"ds_read_b64_tr_b16 a[0:1], v47 offset:0\n\t"
"ds_read_b64_tr_b16 a[2:3], v47 offset:1536\n\t"
"ds_read_b64_tr_b16 a[4:5], v47 offset:3072\n\t"
"ds_read_b64_tr_b16 a[6:7], v47 offset:4608\n\t"
"ds_read_b64_tr_b16 a[8:9], v47 offset:8\n\t"
"ds_read_b64_tr_b16 a[10:11], v47 offset:1544\n\t"
"ds_read_b64_tr_b16 a[12:13], v47 offset:3080\n\t"
"ds_read_b64_tr_b16 a[14:15], v47 offset:4616\n\t"
"ds_read_b64_tr_b16 a[16:17], v47 offset:16\n\t"
"ds_read_b64_tr_b16 a[18:19], v47 offset:1552\n\t"
"ds_read_b64_tr_b16 a[20:21], v47 offset:3088\n\t"
"ds_read_b64_tr_b16 a[22:23], v47 offset:4624\n\t"
"v_mfma_f32_16x16x32_bf16 v[32:35], v[110:113], a[56:59], 0\n\t"
"s_waitcnt lgkmcnt(15)\n\t"
"v_mfma_f32_16x16x32_bf16 v[32:35], v[110:113], a[80:83], v[32:35]\n\t"
"v_mfma_f32_16x16x32_bf16 v[96:99], v[108:111], a[56:59], 0\n\t"
"v_mfma_f32_16x16x32_bf16 v[96:99], v[108:111], a[80:83], v[96:99]\n\t"
"buffer_load_dwordx4 v243, s[12:15], s23 offen offset:2048 nt lds\n\t"
"v_mfma_f32_16x16x32_bf16 v[120:123], v[110:113], a[60:63], 0\n\t"
"v_mfma_f32_16x16x32_bf16 v[120:123], v[110:113], a[84:87], v[120:123]\n\t"
"v_mfma_f32_16x16x32_bf16 v[128:131], v[108:111], a[60:63], 0\n\t"
"v_mfma_f32_16x16x32_bf16 v[128:131], v[108:111], a[84:87], v[128:131]\n\t"
"buffer_load_dwordx4 v244, s[12:15], s23 offen offset:3072 nt lds\n\t"
"v_mfma_f32_16x16x32_bf16 v[136:139], v[110:113], a[64:67], 0\n\t"
"s_waitcnt lgkmcnt(14)\n\t"
"v_mfma_f32_16x16x32_bf16 v[136:139], v[110:113], a[88:91], v[136:139]\n\t"
"v_mfma_f32_16x16x32_bf16 v[152:155], v[108:111], a[64:67], 0\n\t"
"v_mfma_f32_16x16x32_bf16 v[152:155], v[108:111], a[88:91], v[152:155]\n\t"
"s_mov_b64 exec, s[36:37]\n\t"
"buffer_load_dwordx4 v245, s[12:15], s23 offen offset:3584 nt lds\n\t"
"s_mov_b64 exec, -1\n\t"
"v_mfma_f32_16x16x32_bf16 v[160:163], v[110:113], a[68:71], 0\n\t"
"s_waitcnt lgkmcnt(12)\n\t"
"v_mfma_f32_16x16x32_bf16 v[160:163], v[110:113], a[92:95], v[160:163]\n\t"
"v_mfma_f32_16x16x32_bf16 v[168:171], v[108:111], a[68:71], 0\n\t"
"v_mfma_f32_16x16x32_bf16 v[168:171], v[108:111], a[92:95], v[168:171]\n\t"
"s_add_u32 m0, s43, 101632\n\t"
"s_mov_b64 exec, s[44:45]\n\t"
"buffer_load_dwordx4 v247, s[12:15], 0 offen offset:0 nt lds\n\t"
"s_mov_b64 exec, -1\n\t"
"s_waitcnt lgkmcnt(10)\n\t"
"v_mfma_f32_16x16x32_bf16 v[4:7], v[110:113], a[0:3], v[0:3]\n\t"
"v_mfma_f32_16x16x32_bf16 v[0:3], v[114:117], a[0:3], v[0:3]\n\t"
"v_mfma_f32_16x16x32_bf16 v[12:15], v[108:111], a[0:3], v[8:11]\n\t"
"v_mfma_f32_16x16x32_bf16 v[8:11], v[112:115], a[0:3], v[8:11]\n\t"
"s_waitcnt lgkmcnt(8)\n\t"
"v_mfma_f32_16x16x32_bf16 v[20:23], v[110:113], a[4:7], v[16:19]\n\t"
"v_mfma_f32_16x16x32_bf16 v[16:19], v[114:117], a[4:7], v[16:19]\n\t"
"v_mfma_f32_16x16x32_bf16 v[28:31], v[108:111], a[4:7], v[24:27]\n\t"
"s_nop 0\n\t"
"v_cvt_pk_bf16_f32 v48, v4, v5\n\t"
"v_cvt_pk_bf16_f32 v49, v6, v7\n\t"
"v_mfma_f32_16x16x32_bf16 v[24:27], v[112:115], a[4:7], v[24:27]\n\t"
"v_cvt_pk_bf16_f32 v72, v0, v1\n\t"
"v_cvt_pk_bf16_f32 v73, v2, v3\n\t"
"s_waitcnt lgkmcnt(6)\n\t"
"v_mfma_f32_16x16x32_bf16 v[36:39], v[110:113], a[8:11], v[32:35]\n\t"
"v_cvt_pk_bf16_f32 v50, v12, v13\n\t"
"v_cvt_pk_bf16_f32 v51, v14, v15\n\t"
"v_mfma_f32_16x16x32_bf16 v[32:35], v[114:117], a[8:11], v[32:35]\n\t"
"v_cvt_pk_bf16_f32 v74, v8, v9\n\t"
"v_cvt_pk_bf16_f32 v75, v10, v11\n\t"
"v_mfma_f32_16x16x32_bf16 v[100:103], v[108:111], a[8:11], v[96:99]\n\t"
"v_cvt_pk_bf16_f32 v52, v20, v21\n\t"
"v_cvt_pk_bf16_f32 v53, v22, v23\n\t"
"v_mfma_f32_16x16x32_bf16 v[96:99], v[112:115], a[8:11], v[96:99]\n\t"
"v_cvt_pk_bf16_f32 v76, v16, v17\n\t"
"v_cvt_pk_bf16_f32 v77, v18, v19\n\t"
"s_waitcnt lgkmcnt(4)\n\t"
"v_mfma_f32_16x16x32_bf16 v[124:127], v[110:113], a[12:15], v[120:123]\n\t"
"v_cvt_pk_bf16_f32 v54, v28, v29\n\t"
"v_cvt_pk_bf16_f32 v55, v30, v31\n\t"
"v_mfma_f32_16x16x32_bf16 v[120:123], v[114:117], a[12:15], v[120:123]\n\t"
"v_cvt_pk_bf16_f32 v78, v24, v25\n\t"
"v_cvt_pk_bf16_f32 v79, v26, v27\n\t"
"v_mfma_f32_16x16x32_bf16 v[132:135], v[108:111], a[12:15], v[128:131]\n\t"
"v_cvt_pk_bf16_f32 v56, v36, v37\n\t"
"v_cvt_pk_bf16_f32 v57, v38, v39\n\t"
"v_mfma_f32_16x16x32_bf16 v[128:131], v[112:115], a[12:15], v[128:131]\n\t"
"v_cvt_pk_bf16_f32 v80, v32, v33\n\t"
"v_cvt_pk_bf16_f32 v81, v34, v35\n\t"
"s_waitcnt lgkmcnt(2)\n\t"
"v_mfma_f32_16x16x32_bf16 v[140:143], v[110:113], a[16:19], v[136:139]\n\t"
"v_cvt_pk_bf16_f32 v58, v100, v101\n\t"
"v_cvt_pk_bf16_f32 v59, v102, v103\n\t"
"v_mfma_f32_16x16x32_bf16 v[136:139], v[114:117], a[16:19], v[136:139]\n\t"
"v_cvt_pk_bf16_f32 v82, v96, v97\n\t"
"v_cvt_pk_bf16_f32 v83, v98, v99\n\t"
"v_mfma_f32_16x16x32_bf16 v[156:159], v[108:111], a[16:19], v[152:155]\n\t"
"v_cvt_pk_bf16_f32 v60, v124, v125\n\t"
"v_cvt_pk_bf16_f32 v61, v126, v127\n\t"
"v_mfma_f32_16x16x32_bf16 v[152:155], v[112:115], a[16:19], v[152:155]\n\t"
"v_cvt_pk_bf16_f32 v84, v120, v121\n\t"
"v_cvt_pk_bf16_f32 v85, v122, v123\n\t"
"s_waitcnt lgkmcnt(0)\n\t"
"v_mfma_f32_16x16x32_bf16 v[164:167], v[110:113], a[20:23], v[160:163]\n\t"
"v_cvt_pk_bf16_f32 v62, v132, v133\n\t"
"v_cvt_pk_bf16_f32 v63, v134, v135\n\t"
"v_mfma_f32_16x16x32_bf16 v[160:163], v[114:117], a[20:23], v[160:163]\n\t"
"v_cvt_pk_bf16_f32 v86, v128, v129\n\t"
"v_cvt_pk_bf16_f32 v87, v130, v131\n\t"
"v_mfma_f32_16x16x32_bf16 v[172:175], v[108:111], a[20:23], v[168:171]\n\t"
"v_cvt_pk_bf16_f32 v64, v140, v141\n\t"
"v_cvt_pk_bf16_f32 v65, v142, v143\n\t"
"v_mfma_f32_16x16x32_bf16 v[168:171], v[112:115], a[20:23], v[168:171]\n\t"
"v_cvt_pk_bf16_f32 v88, v136, v137\n\t"
"v_cvt_pk_bf16_f32 v89, v138, v139\n\t"
"v_cvt_pk_bf16_f32 v66, v156, v157\n\t"
"v_cvt_pk_bf16_f32 v67, v158, v159\n\t"
"v_cvt_pk_bf16_f32 v90, v152, v153\n\t"
"v_cvt_pk_bf16_f32 v91, v154, v155\n\t"
"v_cvt_pk_bf16_f32 v68, v164, v165\n\t"
"v_cvt_pk_bf16_f32 v69, v166, v167\n\t"
"v_cvt_pk_bf16_f32 v92, v160, v161\n\t"
"v_cvt_pk_bf16_f32 v93, v162, v163\n\t"
"v_cvt_pk_bf16_f32 v70, v172, v173\n\t"
"v_cvt_pk_bf16_f32 v71, v174, v175\n\t"
"v_cvt_pk_bf16_f32 v94, v168, v169\n\t"
"v_cvt_pk_bf16_f32 v95, v170, v171\n\t"
"s_barrier\n\t"
"v_mfma_f32_16x16x32_bf16 v[192:195], a[176:179], v[144:147], 0\n\t"
"ds_read_b128 a[144:147], v216 offset:0\n\t"
"v_mfma_f32_16x16x32_bf16 v[196:199], a[180:183], v[144:147], 0\n\t"
"ds_read_b128 a[160:163], v216 offset:8192\n\t"
"ds_read_b128 a[148:151], v216 offset:1024\n\t"
"v_mfma_f32_16x16x32_bf16 v[200:203], a[184:187], v[144:147], 0\n\t"
"ds_read_b128 a[164:167], v216 offset:9216\n\t"
"ds_read_b128 a[152:155], v216 offset:2048\n\t"
"v_mfma_f32_16x16x32_bf16 v[204:207], a[188:191], v[144:147], 0\n\t"
"ds_read_b128 a[168:171], v216 offset:10240\n\t"
"ds_read_b128 a[156:159], v216 offset:3072\n\t"
"v_add_u32 v235, 4096, v248\n\t"
"v_mbcnt_lo_u32_b32 v176, -1, 0\n\t"
"v_mfma_f32_16x16x32_bf16 v[208:211], a[192:195], v[148:151], 0\n\t"
"ds_read_b128 a[172:175], v216 offset:11264\n\t"
"v_cvt_pk_bf16_f32 v192, v192, v193\n\t"
"v_mfma_f32_16x16x32_bf16 v[212:215], a[196:199], v[148:151], 0\n\t"
"v_cvt_pk_bf16_f32 v193, v194, v195\n\t"
"v_cvt_pk_bf16_f32 v194, v196, v197\n\t"
"v_cvt_pk_bf16_f32 v195, v198, v199\n\t"
"v_mbcnt_hi_u32_b32 v176, -1, v176\n\t"
"s_waitcnt lgkmcnt(6)\n\t"
"v_mfma_f32_16x16x32_bf16 v[96:99], v[48:51], a[144:147], 0\n\t"
"v_cvt_pk_bf16_f32 v200, v200, v201\n\t"
"v_cvt_pk_bf16_f32 v201, v202, v203\n\t"
"v_mfma_f32_16x16x32_bf16 v[96:99], v[52:55], a[160:163], v[96:99]\n\t"
"v_cvt_pk_bf16_f32 v202, v204, v205\n\t"
"v_cvt_pk_bf16_f32 v203, v206, v207\n\t"
"ds_write_b128 v221, v[192:195] offset:16384\n\t"
"v_mfma_f32_16x16x32_bf16 v[112:115], v[56:59], a[144:147], 0\n\t"
"v_cvt_pk_bf16_f32 v208, v208, v209\n\t"
"v_cvt_pk_bf16_f32 v209, v210, v211\n\t"
"v_mfma_f32_16x16x32_bf16 v[112:115], v[60:63], a[160:163], v[112:115]\n\t"
"v_cvt_pk_bf16_f32 v210, v212, v213\n\t"
"v_cvt_pk_bf16_f32 v211, v214, v215\n\t"
"ds_write_b128 v221, v[200:203] offset:24576\n\t"
"v_mfma_f32_16x16x32_bf16 v[128:131], v[64:67], a[144:147], 0\n\t"
"ds_read_b128 a[144:147], v216 offset:4096\n\t"
"v_and_b32 v153, 7, v176\n\t"
"v_mfma_f32_16x16x32_bf16 v[128:131], v[68:71], a[160:163], v[128:131]\n\t"
"buffer_load_dword v190, v189, s[8:11], 0 offen sc1\n\t"
"s_waitcnt lgkmcnt(7)\n\t"
"v_mfma_f32_16x16x32_bf16 v[132:135], v[64:67], a[148:151], 0\n\t"
"ds_read_b128 a[160:163], v216 offset:12288\n\t"
"v_and_b32 v158, 24, v176\n\t"
"v_mfma_f32_16x16x32_bf16 v[132:135], v[68:71], a[164:167], v[132:135]\n\t"
"ds_write_b128 v221, v[208:211] offset:20480\n\t"
"v_cmp_eq_u32 vcc, 8, v158\n\t"
"v_mov_b32 v166, 0x3f80\n\t"
"v_mfma_f32_16x16x32_bf16 v[116:119], v[56:59], a[148:151], 0\n\t"
"v_mov_b32 v167, 0xbf80\n\t"
"v_cndmask_b32 v166, v166, v167, vcc\n\t"
"v_mfma_f32_16x16x32_bf16 v[116:119], v[60:63], a[164:167], v[116:119]\n\t"
"v_mov_b32 v168, v153\n\t"
"v_mov_b32 v167, 0\n\t"
"v_and_b32 v169, 1, v168\n\t"
"v_lshlrev_b32 v169, 4, v169\n\t"
"s_waitcnt vmcnt(7)\n\t"
"s_barrier\n\t"
"v_mfma_f32_16x16x32_bf16 v[192:195], a[200:203], v[148:151], 0\n\t"
"ds_read_b64_tr_b16 a[176:177], v249 offset:41472\n\t"
"ds_read_b64_tr_b16 a[178:179], v249 offset:41984\n\t"
"v_lshrrev_b32 v168, 1, v168\n\t"
"v_lshlrev_b64 v[166:167], v169, v[166:167]\n\t"
"v_mfma_f32_16x16x32_bf16 v[100:103], v[48:51], a[148:151], 0\n\t"
"ds_read_b128 a[148:151], v216 offset:5120\n\t"
"v_cmp_eq_u32 vcc, 0, v168\n\t"
"v_mfma_f32_16x16x32_bf16 v[100:103], v[52:55], a[164:167], v[100:103]\n\t"
"ds_read_b128 a[164:167], v216 offset:13312\n\t"
"ds_read_b64_tr_b16 a[180:181], v250 offset:41472\n\t"
"v_cndmask_b32 v172, 0, v166, vcc\n\t"
"v_accvgpr_write_b32 a212, v172\n\t"
"s_waitcnt lgkmcnt(12)\n\t"
"v_mfma_f32_16x16x32_bf16 v[104:107], v[48:51], a[152:155], 0\n\t"
"ds_read_b64_tr_b16 a[182:183], v250 offset:41984\n\t"
"v_cndmask_b32 v173, 0, v167, vcc\n\t"
"v_mfma_f32_16x16x32_bf16 v[104:107], v[52:55], a[168:171], v[104:107]\n\t"
"v_cvt_pk_bf16_f32 v192, v192, v193\n\t"
"v_cvt_pk_bf16_f32 v193, v194, v195\n\t"
"v_mfma_f32_16x16x32_bf16 v[196:199], a[204:207], v[148:151], 0\n\t"
"ds_read_b64_tr_b16 a[184:185], v251 offset:41472\n\t"
"ds_read_b64_tr_b16 a[186:187], v251 offset:41984\n\t"
"v_cmp_eq_u32 vcc, 1, v168\n\t"
"s_nop 1\n\t"
"v_cndmask_b32 v173, v173, v166, vcc\n\t"
"v_mfma_f32_16x16x32_bf16 v[120:123], v[56:59], a[152:155], 0\n\t"
"ds_read_b64_tr_b16 a[188:189], v252 offset:41472\n\t"
"v_accvgpr_write_b32 a213, v173\n\t"
"v_mfma_f32_16x16x32_bf16 v[120:123], v[60:63], a[168:171], v[120:123]\n\t"
"ds_read_b64_tr_b16 a[190:191], v252 offset:41984\n\t"
"ds_read_b64_tr_b16 a[192:193], v249 offset:42624\n\t"
"v_cndmask_b32 v174, 0, v167, vcc\n\t"
"v_cmp_eq_u32 vcc, 2, v168\n\t"
"v_mfma_f32_16x16x32_bf16 v[136:139], v[64:67], a[152:155], 0\n\t"
"ds_read_b64_tr_b16 a[194:195], v249 offset:43136\n\t"
"v_cndmask_b32 v174, v174, v166, vcc\n\t"
"v_mfma_f32_16x16x32_bf16 v[136:139], v[68:71], a[168:171], v[136:139]\n\t"
"v_cvt_pk_bf16_f32 v194, v196, v197\n\t"
"v_cvt_pk_bf16_f32 v195, v198, v199\n\t"
"ds_read_b64_tr_b16 a[196:197], v250 offset:42624\n\t"
"ds_read_b64_tr_b16 a[198:199], v250 offset:43136\n\t"
"s_waitcnt lgkmcnt(15)\n\t"
"v_mfma_f32_16x16x32_bf16 v[140:143], v[64:67], a[156:159], 0\n\t"
"ds_read_b64_tr_b16 a[200:201], v251 offset:42624\n\t"
"v_accvgpr_write_b32 a214, v174\n\t"
"v_mfma_f32_16x16x32_bf16 v[140:143], v[68:71], a[172:175], v[140:143]\n\t"
"ds_write_b128 v221, v[192:195] offset:28672\n\t"
"ds_read_b64_tr_b16 a[202:203], v251 offset:43136\n\t"
"v_cndmask_b32 v175, 0, v167, vcc\n\t"
"v_mfma_f32_16x16x32_bf16 v[124:127], v[56:59], a[156:159], 0\n\t"
"ds_read_b64_tr_b16 a[204:205], v252 offset:42624\n\t"
"v_cmp_eq_u32 vcc, 3, v168\n\t"
"v_mfma_f32_16x16x32_bf16 v[124:127], v[60:63], a[172:175], v[124:127]\n\t"
"ds_read_b64_tr_b16 a[206:207], v252 offset:43136\n\t"
"ds_read_b128 a[152:155], v216 offset:6144\n\t"
"v_cndmask_b32 v175, v175, v166, vcc\n\t"
"v_accvgpr_write_b32 a215, v175\n\t"
"v_mfma_f32_16x16x32_bf16 v[108:111], v[48:51], a[156:159], 0\n\t"
"ds_read_b128 a[168:171], v216 offset:14336\n\t"
"v_cmp_gt_u32 vcc, 32, v176\n\t"
"v_mfma_f32_16x16x32_bf16 v[108:111], v[52:55], a[172:175], v[108:111]\n\t"
"buffer_load_dword v191, v189, s[8:11], 0 offen sc1\n\t"
"v_mfma_f32_16x16x32_bf16 v[0:3], v[72:75], a[144:147], v[96:99]\n\t"
"ds_read_b128 a[156:159], v216 offset:7168\n\t"
"v_lshlrev_b32 v166, 8, v172\n\t"
"v_mfma_f32_16x16x32_bf16 v[0:3], v[76:79], a[160:163], v[0:3]\n\t"
"ds_read_b128 a[172:175], v216 offset:15360\n\t"
"v_and_b32 v166, 0x80008000, v166\n\t"
"v_xor_b32 v166, v166, v172\n\t"
"v_cndmask_b32 v167, v166, v172, vcc\n\t"
"v_mfma_f32_16x16x32_bf16 v[16:19], v[80:83], a[144:147], v[112:115]\n\t"
"v_cndmask_b32 v166, v172, v166, vcc\n\t"
"v_accvgpr_write_b32 a208, v167\n\t"
"v_mfma_f32_16x16x32_bf16 v[16:19], v[84:87], a[160:163], v[16:19]\n\t"
"v_accvgpr_write_b32 a216, v166\n\t"
"v_lshlrev_b32 v166, 8, v173\n\t"
"v_and_b32 v166, 0x80008000, v166\n\t"
"v_xor_b32 v166, v166, v173\n\t"
"v_mfma_f32_16x16x32_bf16 v[32:35], v[88:91], a[144:147], v[128:131]\n\t"
"v_cndmask_b32 v167, v166, v173, vcc\n\t"
"v_cndmask_b32 v166, v173, v166, vcc\n\t"
"v_mfma_f32_16x16x32_bf16 v[32:35], v[92:95], a[160:163], v[32:35]\n\t"
"v_accvgpr_write_b32 a209, v167\n\t"
"v_accvgpr_write_b32 a217, v166\n\t"
"v_lshlrev_b32 v166, 8, v174\n\t"
"v_and_b32 v166, 0x80008000, v166\n\t"
"s_waitcnt lgkmcnt(15)\n\t"
"v_mfma_f32_16x16x32_bf16 v[36:39], v[88:91], a[148:151], v[132:135]\n\t"
"v_xor_b32 v166, v166, v174\n\t"
"v_cndmask_b32 v167, v166, v174, vcc\n\t"
"v_mfma_f32_16x16x32_bf16 v[36:39], v[92:95], a[164:167], v[36:39]\n\t"
"v_cndmask_b32 v166, v174, v166, vcc\n\t"
"v_accvgpr_write_b32 a210, v167\n\t"
"v_accvgpr_write_b32 a218, v166\n\t"
"v_lshlrev_b32 v166, 8, v175\n\t"
"v_mfma_f32_16x16x32_bf16 v[20:23], v[80:83], a[148:151], v[116:119]\n\t"
"v_and_b32 v166, 0x80008000, v166\n\t"
"v_xor_b32 v166, v166, v175\n\t"
"v_mfma_f32_16x16x32_bf16 v[20:23], v[84:87], a[164:167], v[20:23]\n\t"
"v_cndmask_b32 v167, v166, v175, vcc\n\t"
"v_cndmask_b32 v166, v175, v166, vcc\n\t"
"v_accvgpr_write_b32 a211, v167\n\t"
"v_accvgpr_write_b32 a219, v166\n\t"
"v_mfma_f32_16x16x32_bf16 v[4:7], v[72:75], a[148:151], v[100:103]\n\t"
"v_mfma_f32_16x16x32_bf16 v[4:7], v[76:79], a[164:167], v[4:7]\n\t"
"s_waitcnt lgkmcnt(0)\n\t"
"s_waitcnt vmcnt(1)\n\t"
"v_cmp_eq_u32 vcc, v190, v188\n\t"
"s_nop 4\n\t"
"s_cmp_eq_u64 vcc, 0\n\t"
"s_cbranch_scc1 Lgo_%=\n\t"
"s_sub_u32 s68, s68, 1\n\t"
"s_waitcnt vmcnt(0)\n\t"
"Lchk_%=:\n\t"
"v_cmp_eq_u32 vcc, v191, v188\n\t"
"s_nop 4\n\t"
"s_cmp_eq_u64 vcc, 0\n\t"
"s_cbranch_scc1 Lgo_%=\n\t"
"s_sub_u32 s68, s68, 1\n\t"
"s_cmp_eq_u32 s68, 0\n\t"
"s_cbranch_scc1 Lfb_%=\n\t"
"buffer_load_dword v191, v189, s[8:11], 0 offen sc1\n\t"
"s_waitcnt vmcnt(0)\n\t"
"s_branch Lchk_%=\n\t"
"Lgo_%=:\n\t"
"s_barrier\n\t"
"v_mfma_f32_16x16x32_bf16 v[8:11], v[72:75], a[152:155], v[104:107]\n\t"
"ds_read_b128 a[144:147], v216 offset:16384\n\t"
"v_mfma_f32_16x16x32_bf16 v[8:11], v[76:79], a[168:171], v[8:11]\n\t"
"ds_read_b128 a[160:163], v216 offset:24576\n\t"
"ds_read_b128 a[148:151], v216 offset:17408\n\t"
"v_mfma_f32_16x16x32_bf16 v[24:27], v[80:83], a[152:155], v[120:123]\n\t"
"ds_read_b128 a[164:167], v216 offset:25600\n\t"
"v_mbcnt_lo_u32_b32 v176, -1, 0\n\t"
"v_mfma_f32_16x16x32_bf16 v[24:27], v[84:87], a[168:171], v[24:27]\n\t"
"s_mov_b32 s54, 98304\n\t"
"buffer_load_dwordx4 a[96:99], v248, s[8:11], s54 offen offset:0 sc1\n\t"
"buffer_load_dwordx4 a[100:103], v248, s[8:11], s54 offen offset:1024 sc1\n\t"
"v_mfma_f32_16x16x32_bf16 v[40:43], v[88:91], a[152:155], v[136:139]\n\t"
"ds_read_b128 a[152:155], v216 offset:18432\n\t"
"v_mbcnt_hi_u32_b32 v176, -1, v176\n\t"
"v_mfma_f32_16x16x32_bf16 v[40:43], v[92:95], a[168:171], v[40:43]\n\t"
"buffer_load_dwordx4 a[104:107], v248, s[8:11], s54 offen offset:2048 sc1\n\t"
"buffer_load_dwordx4 a[108:111], v248, s[8:11], s54 offen offset:3072 sc1\n\t"
"v_mfma_f32_16x16x32_bf16 v[200:203], a[176:179], a[224:227], 0\n\t"
"buffer_load_dwordx4 a[112:115], v235, s[8:11], s54 offen offset:0 sc1\n\t"
"v_mfma_f32_16x16x32_bf16 v[44:47], v[88:91], a[156:159], v[140:143]\n\t"
"ds_read_b128 a[168:171], v216 offset:26624\n\t"
"v_and_b32 v154, 15, v176\n\t"
"v_mfma_f32_16x16x32_bf16 v[44:47], v[92:95], a[172:175], v[44:47]\n\t"
"buffer_load_dwordx4 a[116:119], v235, s[8:11], s54 offen offset:1024 sc1\n\t"
"s_mov_b32 s66, 172032\n\t"
"buffer_load_dwordx4 a[120:123], v248, s[8:11], s66 offen offset:0 sc1\n\t"
"v_mfma_f32_16x16x32_bf16 v[204:207], a[180:183], a[224:227], 0\n\t"
"buffer_load_dwordx4 a[124:127], v248, s[8:11], s66 offen offset:1024 sc1\n\t"
"buffer_load_dwordx4 a[128:131], v248, s[8:11], s66 offen offset:2048 sc1\n\t"
"v_mfma_f32_16x16x32_bf16 v[28:31], v[80:83], a[156:159], v[124:127]\n\t"
"v_cvt_pk_bf16_f32 v200, v200, v201\n\t"
"v_cvt_pk_bf16_f32 v201, v202, v203\n\t"
"v_mfma_f32_16x16x32_bf16 v[28:31], v[84:87], a[172:175], v[28:31]\n\t"
"buffer_load_dwordx4 a[132:135], v248, s[8:11], s66 offen offset:3072 sc1\n\t"
"buffer_load_dwordx4 a[136:139], v235, s[8:11], s66 offen offset:0 sc1\n\t"
"v_mfma_f32_16x16x32_bf16 v[12:15], v[72:75], a[156:159], v[108:111]\n\t"
"ds_read_b128 a[156:159], v216 offset:19456\n\t"
"v_lshrrev_b32 v155, 4, v176\n\t"
"v_mfma_f32_16x16x32_bf16 v[12:15], v[76:79], a[172:175], v[12:15]\n\t"
"buffer_load_dwordx4 a[140:143], v235, s[8:11], s66 offen offset:1024 sc1\n\t"
"v_mfma_f32_16x16x32_bf16 v[208:211], a[184:187], a[224:227], 0\n\t"
"v_pk_fma_f32 v[96:97], v[96:97], s[38:39], v[0:1] neg_lo:[1,0,1] neg_hi:[1,0,1]\n\t"
"v_pk_fma_f32 v[98:99], v[98:99], s[38:39], v[2:3] neg_lo:[1,0,1] neg_hi:[1,0,1]\n\t"
"v_pk_fma_f32 v[112:113], v[112:113], s[38:39], v[16:17] neg_lo:[1,0,1] neg_hi:[1,0,1]\n\t"
"v_pk_fma_f32 v[114:115], v[114:115], s[38:39], v[18:19] neg_lo:[1,0,1] neg_hi:[1,0,1]\n\t"
"v_pk_fma_f32 v[128:129], v[128:129], s[38:39], v[32:33] neg_lo:[1,0,1] neg_hi:[1,0,1]\n\t"
"v_pk_fma_f32 v[130:131], v[130:131], s[38:39], v[34:35] neg_lo:[1,0,1] neg_hi:[1,0,1]\n\t"
"v_pk_fma_f32 v[132:133], v[132:133], s[38:39], v[36:37] neg_lo:[1,0,1] neg_hi:[1,0,1]\n\t"
"v_pk_fma_f32 v[134:135], v[134:135], s[38:39], v[38:39] neg_lo:[1,0,1] neg_hi:[1,0,1]\n\t"
"v_pk_fma_f32 v[116:117], v[116:117], s[38:39], v[20:21] neg_lo:[1,0,1] neg_hi:[1,0,1]\n\t"
"v_pk_fma_f32 v[118:119], v[118:119], s[38:39], v[22:23] neg_lo:[1,0,1] neg_hi:[1,0,1]\n\t"
"v_pk_fma_f32 v[100:101], v[100:101], s[38:39], v[4:5] neg_lo:[1,0,1] neg_hi:[1,0,1]\n\t"
"v_pk_fma_f32 v[102:103], v[102:103], s[38:39], v[6:7] neg_lo:[1,0,1] neg_hi:[1,0,1]\n\t"
"v_pk_fma_f32 v[104:105], v[104:105], s[38:39], v[8:9] neg_lo:[1,0,1] neg_hi:[1,0,1]\n\t"
"v_pk_fma_f32 v[106:107], v[106:107], s[38:39], v[10:11] neg_lo:[1,0,1] neg_hi:[1,0,1]\n\t"
"v_pk_fma_f32 v[120:121], v[120:121], s[38:39], v[24:25] neg_lo:[1,0,1] neg_hi:[1,0,1]\n\t"
"v_pk_fma_f32 v[122:123], v[122:123], s[38:39], v[26:27] neg_lo:[1,0,1] neg_hi:[1,0,1]\n\t"
"v_pk_fma_f32 v[136:137], v[136:137], s[38:39], v[40:41] neg_lo:[1,0,1] neg_hi:[1,0,1]\n\t"
"v_pk_fma_f32 v[138:139], v[138:139], s[38:39], v[42:43] neg_lo:[1,0,1] neg_hi:[1,0,1]\n\t"
"v_pk_fma_f32 v[140:141], v[140:141], s[38:39], v[44:45] neg_lo:[1,0,1] neg_hi:[1,0,1]\n\t"
"v_pk_fma_f32 v[142:143], v[142:143], s[38:39], v[46:47] neg_lo:[1,0,1] neg_hi:[1,0,1]\n\t"
"v_pk_fma_f32 v[124:125], v[124:125], s[38:39], v[28:29] neg_lo:[1,0,1] neg_hi:[1,0,1]\n\t"
"v_pk_fma_f32 v[126:127], v[126:127], s[38:39], v[30:31] neg_lo:[1,0,1] neg_hi:[1,0,1]\n\t"
"v_pk_fma_f32 v[108:109], v[108:109], s[38:39], v[12:13] neg_lo:[1,0,1] neg_hi:[1,0,1]\n\t"
"v_pk_fma_f32 v[110:111], v[110:111], s[38:39], v[14:15] neg_lo:[1,0,1] neg_hi:[1,0,1]\n\t"
"s_mov_b32 s47, 40960\n\t"
"buffer_load_dwordx4 a[0:3], v248, s[8:11], s47 offen offset:2048 sc1\n\t"
"v_cvt_pk_bf16_f32 v202, v204, v205\n\t"
"v_cvt_pk_bf16_f32 v203, v206, v207\n\t"
"s_waitcnt lgkmcnt(5)\n\t"
"v_mfma_f32_16x16x32_bf16 v[0:3], a[48:51], a[144:147], v[0:3]\n\t"
"ds_read_b128 a[172:175], v216 offset:27648\n\t"
"v_bfe_u32 v156, v176, 2, 2\n\t"
"v_mfma_f32_16x16x32_bf16 v[0:3], a[52:55], a[160:163], v[0:3]\n\t"
"buffer_load_dwordx4 a[4:7], v248, s[8:11], s47 offen offset:3072 sc1\n\t"
"ds_write_b128 v221, v[200:203] offset:0\n\t"
"v_mfma_f32_16x16x32_bf16 v[212:215], a[188:191], a[224:227], 0\n\t"
"v_and_b32 v157, 3, v176\n\t"
"v_lshlrev_b32 v157, 3, v157\n\t"
"v_mfma_f32_16x16x32_bf16 v[16:19], a[56:59], a[144:147], v[16:19]\n\t"
"v_cvt_pk_bf16_f32 v208, v208, v209\n\t"
"v_cvt_pk_bf16_f32 v209, v210, v211\n\t"
"v_mfma_f32_16x16x32_bf16 v[16:19], a[60:63], a[160:163], v[16:19]\n\t"
"buffer_load_dwordx4 a[8:11], v235, s[8:11], s47 offen offset:0 sc1\n\t"
"s_add_u32 s60, s43, 39424\n\t"
"s_add_u32 s61, s26, 20800\n\t"
"v_mfma_f32_16x16x32_bf16 v[32:35], a[64:67], a[144:147], v[32:35]\n\t"
"ds_read_b128 a[144:147], v216 offset:20480\n\t"
"s_mul_i32 s62, s73, 96\n\t"
"v_mfma_f32_16x16x32_bf16 v[32:35], a[68:71], a[160:163], v[32:35]\n\t"
"v_cvt_pk_bf16_f32 v210, v212, v213\n\t"
"v_cvt_pk_bf16_f32 v211, v214, v215\n\t"
"ds_read_b128 a[160:163], v216 offset:28672\n\t"
"s_add_u32 s62, s62, 32768\n\t"
"v_mfma_f32_16x16x32_bf16 v[192:195], a[192:195], a[224:227], 0\n\t"
"ds_write_b128 v221, v[208:211] offset:8192\n\t"
"v_min_u32 v158, 35, v176\n\t"
"v_lshl_add_u32 v222, v158, 4, s60\n\t"
"s_waitcnt lgkmcnt(8)\n\t"
"v_mfma_f32_16x16x32_bf16 v[36:39], a[64:67], a[148:151], v[36:39]\n\t"
"v_add_u32 v223, 62208, v222\n\t"
"v_and_b32 v158, 1, v155\n\t"
"v_mfma_f32_16x16x32_bf16 v[36:39], a[68:71], a[164:167], v[36:39]\n\t"
"buffer_load_dwordx4 a[12:15], v235, s[8:11], s47 offen offset:1024 sc1\n\t"
"v_add_u32 v159, v156, v158\n\t"
"v_lshl_add_u32 v166, v159, 7, v157\n\t"
"v_mfma_f32_16x16x32_bf16 v[20:23], a[56:59], a[148:151], v[20:23]\n\t"
"v_and_b32 v159, 2, v159\n\t"
"v_lshl_add_u32 v166, v159, 4, v166\n\t"
"v_mfma_f32_16x16x32_bf16 v[20:23], a[60:63], a[164:167], v[20:23]\n\t"
"buffer_load_dwordx4 a[16:19], v235, s[8:11], s47 offen offset:2048 sc1\n\t"
"v_mfma_f32_16x16x32_bf16 v[196:199], a[196:199], a[224:227], 0\n\t"
"v_cvt_pk_bf16_f32 v192, v192, v193\n\t"
"v_cvt_pk_bf16_f32 v193, v194, v195\n\t"
"v_mul_u32_u24 v159, 20736, v158\n\t"
"v_sub_u32 v166, v166, v159\n\t"
"s_barrier\n\t"
"v_mfma_f32_16x16x32_bf16 v[4:7], a[48:51], a[148:151], v[4:7]\n\t"
"ds_read_b128 a[148:151], v216 offset:21504\n\t"
"v_lshrrev_b32 v159, 5, v176\n\t"
"v_mfma_f32_16x16x32_bf16 v[4:7], a[52:55], a[164:167], v[4:7]\n\t"
"ds_read_b128 a[164:167], v216 offset:29696\n\t"
"ds_read_b64_tr_b16 a[176:177], v220 offset:62208\n\t"
"v_xor_b32 v158, v158, v159\n\t"
"v_lshlrev_b32 v158, 6, v158\n\t"
"v_mfma_f32_16x16x32_bf16 v[200:203], a[200:203], a[224:227], 0\n\t"
"buffer_load_dwordx4 a[20:23], v235, s[8:11], s47 offen offset:3072 sc1\n\t"
"s_waitcnt lgkmcnt(9)\n\t"
"v_mfma_f32_16x16x32_bf16 v[8:11], a[48:51], a[152:155], v[8:11]\n\t"
"v_cvt_pk_bf16_f32 v194, v196, v197\n\t"
"v_cvt_pk_bf16_f32 v195, v198, v199\n\t"
"v_mfma_f32_16x16x32_bf16 v[8:11], a[52:55], a[168:171], v[8:11]\n\t"
"ds_write_b128 v221, v[192:195] offset:4096\n\t"
"ds_read_b64_tr_b16 a[178:179], v220 offset:62720\n\t"
"v_sub_u32 v166, v166, v158\n\t"
"v_mfma_f32_16x16x32_bf16 v[24:27], a[56:59], a[152:155], v[24:27]\n\t"
"ds_read_b64_tr_b16 a[180:181], v255 offset:62208\n\t"
"v_mul_u32_u24 v159, 1152, v159\n\t"
"v_mfma_f32_16x16x32_bf16 v[24:27], a[60:63], a[168:171], v[24:27]\n\t"
"s_add_u32 m0, s43, 142976\n\t"
"s_mov_b64 exec, s[44:45]\n\t"
"buffer_load_dwordx4 v247, s[12:15], 0 offen offset:128 nt lds\n\t"
"s_mov_b64 exec, -1\n\t"
"v_cvt_pk_bf16_f32 v200, v200, v201\n\t"
"v_cvt_pk_bf16_f32 v201, v202, v203\n\t"
"v_mfma_f32_16x16x32_bf16 v[204:207], a[204:207], a[224:227], 0\n\t"
"ds_read_b64_tr_b16 a[182:183], v255 offset:62720\n\t"
"ds_read_b64_tr_b16 a[184:185], v226 offset:64512\n\t"
"v_add3_u32 v166, v166, v159, s61\n\t"
"v_add_u32 v224, 1152, v166\n\t"
"v_mfma_f32_16x16x32_bf16 v[40:43], a[64:67], a[152:155], v[40:43]\n\t"
"ds_read_b64_tr_b16 a[186:187], v226 offset:65024\n\t"
"v_add_u32 v225, v159, v166\n\t"
"v_mfma_f32_16x16x32_bf16 v[40:43], a[68:71], a[168:171], v[40:43]\n\t"
"ds_read_b64_tr_b16 a[188:189], v218 offset:64512\n\t"
"ds_read_b64_tr_b16 a[190:191], v218 offset:65024\n\t"
"v_mul_u32_u24 v158, 400, v154\n\t"
"v_mul_u32_u24 v159, 24, v155\n\t"
"s_waitcnt lgkmcnt(15)\n\t"
"v_mfma_f32_16x16x32_bf16 v[44:47], a[64:67], a[156:159], v[44:47]\n\t"
"ds_read_b64_tr_b16 a[192:193], v226 offset:62208\n\t"
"v_add3_u32 v228, v158, v159, s62\n\t"
"v_mfma_f32_16x16x32_bf16 v[44:47], a[68:71], a[172:175], v[44:47]\n\t"
"ds_read_b64_tr_b16 a[194:195], v226 offset:62720\n\t"
"v_cvt_pk_bf16_f32 v202, v204, v205\n\t"
"v_mfma_f32_16x16x32_bf16 v[28:31], a[56:59], a[156:159], v[28:31]\n\t"
"ds_read_b64_tr_b16 a[196:197], v218 offset:62208\n\t"
"v_cvt_pk_bf16_f32 v203, v206, v207\n\t"
"v_mfma_f32_16x16x32_bf16 v[28:31], a[60:63], a[172:175], v[28:31]\n\t"
"ds_write_b128 v221, v[200:203] offset:12288\n\t"
"ds_read_b64_tr_b16 a[198:199], v218 offset:62720\n\t"
"s_mul_i32 s60, s73, 3200\n\t"
"v_mfma_f32_16x16x32_bf16 v[12:15], a[48:51], a[156:159], v[12:15]\n\t"
"ds_read_b64_tr_b16 a[200:201], v220 offset:64512\n\t"
"s_add_u32 s60, s60, 32768\n\t"
"v_mfma_f32_16x16x32_bf16 v[12:15], a[52:55], a[172:175], v[12:15]\n\t"
"ds_read_b64_tr_b16 a[202:203], v220 offset:65024\n\t"
"ds_read_b64_tr_b16 a[204:205], v255 offset:64512\n\t"
"s_add_u32 m0, s26, 145280\n\t"
"s_nop 0\n\t"
"buffer_load_dwordx4 v241, s[12:15], s23 offen offset:128 nt lds\n\t"
"s_waitcnt lgkmcnt(15)\n\t"
"v_mfma_f32_16x16x32_bf16 v[96:99], a[72:75], a[144:147], v[96:99]\n\t"
"ds_read_b64_tr_b16 a[206:207], v255 offset:65024\n\t"
"s_and_b32 s61, s73, 2\n\t"
"v_mfma_f32_16x16x32_bf16 v[96:99], a[76:79], a[160:163], v[96:99]\n\t"
"ds_read_b128 a[152:155], v216 offset:22528\n\t"
"ds_read_b128 a[168:171], v216 offset:30720\n\t"
"s_lshl_b32 s61, s61, 7\n\t"
"s_add_u32 s60, s60, s61\n\t"
"v_mfma_f32_16x16x32_bf16 v[112:115], a[80:83], a[144:147], v[112:115]\n\t"
"ds_read_b128 a[156:159], v216 offset:23552\n\t"
"s_mul_i32 s61, s73, 393216\n\t"
"v_mfma_f32_16x16x32_bf16 v[112:115], a[84:87], a[160:163], v[112:115]\n\t"
"ds_read_b128 a[172:175], v216 offset:31744\n\t"
"v_add_u32 v158, 0, v176\n\t"
"v_mul_u32_u24 v159, 0xab, v158\n\t"
"v_lshrrev_b32 v159, 12, v159\n\t"
"v_mfma_f32_16x16x32_bf16 v[128:131], a[88:91], a[144:147], v[128:131]\n\t"
"v_add_lshl_u32 v166, v158, v159, 4\n\t"
"v_add_u32 v229, s60, v166\n\t"
"v_mfma_f32_16x16x32_bf16 v[128:131], a[92:95], a[160:163], v[128:131]\n\t"
"v_mad_u32_u24 v158, v159, 24, v158\n\t"
"v_lshl_add_u32 v232, v158, 4, s61\n\t"
"v_add_u32 v158, 64, v176\n\t"
"v_mul_u32_u24 v159, 0xab, v158\n\t"
"v_mfma_f32_16x16x32_bf16 v[132:135], a[88:91], a[148:151], v[132:135]\n\t"
"v_lshrrev_b32 v159, 12, v159\n\t"
"v_add_lshl_u32 v166, v158, v159, 4\n\t"
"v_mfma_f32_16x16x32_bf16 v[132:135], a[92:95], a[164:167], v[132:135]\n\t"
"v_add_u32 v230, s60, v166\n\t"
"v_mad_u32_u24 v158, v159, 24, v158\n\t"
"v_lshl_add_u32 v233, v158, 4, s61\n\t"
"v_add_u32 v158, 128, v176\n\t"
"v_mfma_f32_16x16x32_bf16 v[116:119], a[80:83], a[148:151], v[116:119]\n\t"
"v_mul_u32_u24 v159, 0xab, v158\n\t"
"v_lshrrev_b32 v159, 12, v159\n\t"
"v_mfma_f32_16x16x32_bf16 v[116:119], a[84:87], a[164:167], v[116:119]\n\t"
"v_add_lshl_u32 v166, v158, v159, 4\n\t"
"v_add_u32 v231, s60, v166\n\t"
"v_mad_u32_u24 v158, v159, 24, v158\n\t"
"v_lshl_add_u32 v234, v158, 4, s61\n\t"
"v_mfma_f32_16x16x32_bf16 v[100:103], a[72:75], a[148:151], v[100:103]\n\t"
"v_mfma_f32_16x16x32_bf16 v[100:103], a[76:79], a[164:167], v[100:103]\n\t"
"s_waitcnt lgkmcnt(0)\n\t"
"s_add_u32 s71, s71, 1\n\t"
"v_mov_b32 v215, s71\n\t"
"s_mov_b64 exec, 1\n\t"
"buffer_store_dword v215, off, s[8:11], s70 sc1\n\t"
"s_mov_b64 exec, -1\n\t"
"s_barrier\n\t"
"v_mfma_f32_16x16x32_bf16 v[104:107], a[72:75], a[152:155], v[104:107]\n\t"
"ds_read_b128 a[144:147], v216 offset:0\n\t"
"v_mfma_f32_16x16x32_bf16 v[104:107], a[76:79], a[168:171], v[104:107]\n\t"
"ds_read_b128 a[160:163], v216 offset:8192\n\t"
"ds_read_b128 a[148:151], v216 offset:1024\n\t"
"v_mfma_f32_16x16x32_bf16 v[120:123], a[80:83], a[152:155], v[120:123]\n\t"
"ds_read_b128 a[164:167], v216 offset:9216\n\t"
"v_mfma_f32_16x16x32_bf16 v[120:123], a[84:87], a[168:171], v[120:123]\n\t"
"buffer_load_dwordx4 v242, s[12:15], s23 offen offset:1152 nt lds\n\t"
"v_mfma_f32_16x16x32_bf16 v[136:139], a[88:91], a[152:155], v[136:139]\n\t"
"ds_read_b128 a[152:155], v216 offset:2048\n\t"
"v_mfma_f32_16x16x32_bf16 v[136:139], a[92:95], a[168:171], v[136:139]\n\t"
"ds_read_b128 a[168:171], v216 offset:10240\n\t"
"s_mov_b32 s40, 8192\n\t"
"v_mfma_f32_16x16x32_bf16 v[208:211], a[176:179], a[220:223], 0\n\t"
"ds_read_b64_tr_b16 a[176:177], v251 offset:20736\n\t"
"v_mfma_f32_16x16x32_bf16 v[140:143], a[88:91], a[156:159], v[140:143]\n\t"
"ds_read_b64_tr_b16 a[178:179], v251 offset:21248\n\t"
"v_mfma_f32_16x16x32_bf16 v[140:143], a[92:95], a[172:175], v[140:143]\n\t"
"s_mov_b32 s41, 16384\n\t"
"v_mfma_f32_16x16x32_bf16 v[212:215], a[180:183], a[220:223], 0\n\t"
"ds_read_b64_tr_b16 a[180:181], v252 offset:20736\n\t"
"v_mfma_f32_16x16x32_bf16 v[124:127], a[80:83], a[156:159], v[124:127]\n\t"
"v_cvt_pk_bf16_f32 v208, v208, v209\n\t"
"v_cvt_pk_bf16_f32 v209, v210, v211\n\t"
"v_mfma_f32_16x16x32_bf16 v[124:127], a[84:87], a[172:175], v[124:127]\n\t"
"ds_read_b64_tr_b16 a[182:183], v252 offset:21248\n\t"
"v_mfma_f32_16x16x32_bf16 v[108:111], a[72:75], a[156:159], v[108:111]\n\t"
"ds_read_b128 a[156:159], v216 offset:3072\n\t"
"v_mfma_f32_16x16x32_bf16 v[108:111], a[76:79], a[172:175], v[108:111]\n\t"
"v_cvt_pk_bf16_f32 v210, v212, v213\n\t"
"v_cvt_pk_bf16_f32 v211, v214, v215\n\t"
"s_mov_b32 s42, 24576\n\t"
"v_mfma_f32_16x16x32_bf16 v[192:195], a[184:187], a[220:223], 0\n\t"
"ds_read_b128 a[172:175], v216 offset:11264\n\t"
"s_waitcnt vmcnt(20) lgkmcnt(10)\n\t"
"v_mfma_f32_16x16x32_bf16 v[0:3], a[96:99], a[144:147], v[0:3]\n\t"
"ds_read_b64_tr_b16 a[184:185], v249 offset:20736\n\t"
"v_mfma_f32_16x16x32_bf16 v[0:3], a[100:103], a[160:163], v[0:3]\n\t"
"ds_write_b128 v217, v[208:211] offset:16384\n\t"
"v_mfma_f32_16x16x32_bf16 v[196:199], a[188:191], a[220:223], 0\n\t"
"ds_read_b64_tr_b16 a[186:187], v249 offset:21248\n\t"
"s_mov_b32 s46, 32768\n\t"
"s_waitcnt vmcnt(18)\n\t"
"v_mfma_f32_16x16x32_bf16 v[16:19], a[104:107], a[144:147], v[16:19]\n\t"
"v_cvt_pk_bf16_f32 v192, v192, v193\n\t"
"v_cvt_pk_bf16_f32 v193, v194, v195\n\t"
"v_mfma_f32_16x16x32_bf16 v[16:19], a[108:111], a[160:163], v[16:19]\n\t"
"ds_read_b64_tr_b16 a[188:189], v250 offset:20736\n\t"
"s_waitcnt vmcnt(16)\n\t"
"v_mfma_f32_16x16x32_bf16 v[32:35], a[112:115], a[144:147], v[32:35]\n\t"
"ds_read_b128 a[144:147], v216 offset:4096\n\t"
"v_mfma_f32_16x16x32_bf16 v[32:35], a[116:119], a[160:163], v[32:35]\n\t"
"buffer_load_dwordx4 v243, s[12:15], s23 offen offset:2176 nt lds\n\t"
"s_mov_b32 s48, 49152\n\t"
"v_mfma_f32_16x16x32_bf16 v[200:203], a[192:195], a[220:223], 0\n\t"
"v_cvt_pk_bf16_f32 v194, v196, v197\n\t"
"v_cvt_pk_bf16_f32 v195, v198, v199\n\t"
"s_waitcnt lgkmcnt(13)\n\t"
"v_mfma_f32_16x16x32_bf16 v[36:39], a[112:115], a[148:151], v[36:39]\n\t"
"ds_read_b128 a[160:163], v216 offset:12288\n\t"
"v_mfma_f32_16x16x32_bf16 v[36:39], a[116:119], a[164:167], v[36:39]\n\t"
"ds_write_b128 v219, v[192:195] offset:16384\n\t"
"v_mfma_f32_16x16x32_bf16 v[20:23], a[104:107], a[148:151], v[20:23]\n\t"
"ds_read_b64_tr_b16 a[190:191], v250 offset:21248\n\t"
"v_mfma_f32_16x16x32_bf16 v[20:23], a[108:111], a[164:167], v[20:23]\n\t"
"v_cvt_pk_bf16_f32 v200, v200, v201\n\t"
"v_cvt_pk_bf16_f32 v201, v202, v203\n\t"
"s_mov_b32 s49, 57344\n\t"
"v_mfma_f32_16x16x32_bf16 v[204:207], a[196:199], a[220:223], 0\n\t"
"ds_read_b64_tr_b16 a[192:193], v251 offset:21888\n\t"
"v_mfma_f32_16x16x32_bf16 v[4:7], a[96:99], a[148:151], v[4:7]\n\t"
"ds_read_b128 a[148:151], v216 offset:5120\n\t"
"v_mfma_f32_16x16x32_bf16 v[4:7], a[100:103], a[164:167], v[4:7]\n\t"
"ds_read_b128 a[164:167], v216 offset:13312\n\t"
"v_mfma_f32_16x16x32_bf16 v[208:211], a[200:203], a[220:223], 0\n\t"
"ds_read_b64_tr_b16 a[194:195], v251 offset:22400\n\t"
"s_mov_b32 s50, 65536\n\t"
"s_waitcnt lgkmcnt(15)\n\t"
"v_mfma_f32_16x16x32_bf16 v[8:11], a[96:99], a[152:155], v[8:11]\n\t"
"v_cvt_pk_bf16_f32 v202, v204, v205\n\t"
"v_cvt_pk_bf16_f32 v203, v206, v207\n\t"
"v_mfma_f32_16x16x32_bf16 v[8:11], a[100:103], a[168:171], v[8:11]\n\t"
"ds_write_b128 v217, v[200:203] offset:24576\n\t"
"v_mfma_f32_16x16x32_bf16 v[24:27], a[104:107], a[152:155], v[24:27]\n\t"
"ds_read_b64_tr_b16 a[196:197], v252 offset:21888\n\t"
"v_mfma_f32_16x16x32_bf16 v[24:27], a[108:111], a[168:171], v[24:27]\n\t"
"v_cvt_pk_bf16_f32 v208, v208, v209\n\t"
"s_mov_b32 s51, 73728\n\t"
"v_mfma_f32_16x16x32_bf16 v[212:215], a[204:207], a[220:223], 0\n\t"
"v_cvt_pk_bf16_f32 v209, v210, v211\n\t"
"ds_read_b64_tr_b16 a[198:199], v252 offset:22400\n\t"
"v_mfma_f32_16x16x32_bf16 v[40:43], a[112:115], a[152:155], v[40:43]\n\t"
"ds_read_b64_tr_b16 a[200:201], v249 offset:21888\n\t"
"v_mfma_f32_16x16x32_bf16 v[40:43], a[116:119], a[168:171], v[40:43]\n\t"
"ds_read_b64_tr_b16 a[202:203], v249 offset:22400\n\t"
"s_waitcnt lgkmcnt(15)\n\t"
"v_mfma_f32_16x16x32_bf16 v[44:47], a[112:115], a[156:159], v[44:47]\n\t"
"ds_read_b64_tr_b16 a[204:205], v250 offset:21888\n\t"
"v_mfma_f32_16x16x32_bf16 v[44:47], a[116:119], a[172:175], v[44:47]\n\t"
"buffer_load_dwordx4 v244, s[12:15], s23 offen offset:3200 nt lds\n\t"
"s_mov_b32 s52, 81920\n\t"
"v_mfma_f32_16x16x32_bf16 v[28:31], a[104:107], a[156:159], v[28:31]\n\t"
"ds_read_b64_tr_b16 a[206:207], v250 offset:22400\n\t"
"v_mfma_f32_16x16x32_bf16 v[28:31], a[108:111], a[172:175], v[28:31]\n\t"
"v_cvt_pk_bf16_f32 v210, v212, v213\n\t"
"v_mfma_f32_16x16x32_bf16 v[12:15], a[96:99], a[156:159], v[12:15]\n\t"
"v_cvt_pk_bf16_f32 v211, v214, v215\n\t"
"ds_read_b128 a[152:155], v216 offset:6144\n\t"
"v_mfma_f32_16x16x32_bf16 v[12:15], a[100:103], a[172:175], v[12:15]\n\t"
"s_mov_b64 exec, s[36:37]\n\t"
"buffer_load_dwordx4 v245, s[12:15], s23 offen offset:3712 nt lds\n\t"
"s_mov_b64 exec, -1\n\t"
"s_mov_b32 s53, 90112\n\t"
"s_waitcnt vmcnt(17) lgkmcnt(14)\n\t"
"v_mfma_f32_16x16x32_bf16 v[96:99], a[120:123], a[144:147], v[96:99]\n\t"
"v_mfma_f32_16x16x32_bf16 v[96:99], a[124:127], a[160:163], v[96:99]\n\t"
"ds_write_b128 v219, v[208:211] offset:24576\n\t"
"s_waitcnt vmcnt(15)\n\t"
"v_mfma_f32_16x16x32_bf16 v[112:115], a[128:131], a[144:147], v[112:115]\n\t"
"ds_read_b128 a[168:171], v216 offset:14336\n\t"
"v_mfma_f32_16x16x32_bf16 v[112:115], a[132:135], a[160:163], v[112:115]\n\t"
"ds_read_b128 a[156:159], v216 offset:7168\n\t"
"s_mov_b32 s55, 106496\n\t"
"s_waitcnt vmcnt(13)\n\t"
"v_mfma_f32_16x16x32_bf16 v[128:131], a[136:139], a[144:147], v[128:131]\n\t"
"ds_read_b128 a[172:175], v216 offset:15360\n\t"
"v_mfma_f32_16x16x32_bf16 v[128:131], a[140:143], a[160:163], v[128:131]\n\t"
"buffer_load_dwordx4 a[24:27], v248, s[8:11], s50 offen offset:2048 sc1\n\t"
"s_waitcnt lgkmcnt(13)\n\t"
"v_mfma_f32_16x16x32_bf16 v[132:135], a[136:139], a[148:151], v[132:135]\n\t"
"v_mfma_f32_16x16x32_bf16 v[132:135], a[140:143], a[164:167], v[132:135]\n\t"
"v_mfma_f32_16x16x32_bf16 v[116:119], a[128:131], a[148:151], v[116:119]\n\t"
"v_mfma_f32_16x16x32_bf16 v[116:119], a[132:135], a[164:167], v[116:119]\n\t"
"buffer_load_dwordx4 a[28:31], v248, s[8:11], s50 offen offset:3072 sc1\n\t"
"v_mfma_f32_16x16x32_bf16 v[100:103], a[120:123], a[148:151], v[100:103]\n\t"
"v_mfma_f32_16x16x32_bf16 v[100:103], a[124:127], a[164:167], v[100:103]\n\t"
"s_waitcnt lgkmcnt(0)\n\t"
"s_barrier\n\t"
"v_mfma_f32_16x16x32_bf16 v[104:107], a[120:123], a[152:155], v[104:107]\n\t"
"ds_read_b128 a[144:147], v216 offset:16384\n\t"
"v_mfma_f32_16x16x32_bf16 v[104:107], a[124:127], a[168:171], v[104:107]\n\t"
"ds_read_b128 a[160:163], v216 offset:24576\n\t"
"ds_read_b128 a[148:151], v216 offset:17408\n\t"
"v_mfma_f32_16x16x32_bf16 v[120:123], a[128:131], a[152:155], v[120:123]\n\t"
"ds_read_b128 a[164:167], v216 offset:25600\n\t"
"v_mfma_f32_16x16x32_bf16 v[120:123], a[132:135], a[168:171], v[120:123]\n\t"
"buffer_load_dwordx4 a[32:35], v235, s[8:11], s50 offen offset:0 sc1\n\t"
"v_mfma_f32_16x16x32_bf16 v[136:139], a[136:139], a[152:155], v[136:139]\n\t"
"ds_read_b128 a[152:155], v216 offset:18432\n\t"
"v_mfma_f32_16x16x32_bf16 v[136:139], a[140:143], a[168:171], v[136:139]\n\t"
"buffer_load_dwordx4 a[36:39], v235, s[8:11], s50 offen offset:1024 sc1\n\t"
"s_mov_b32 s56, 114688\n\t"
"v_mfma_f32_16x16x32_bf16 v[192:195], a[176:179], a[224:227], 0\n\t"
"buffer_load_dwordx4 a[40:43], v235, s[8:11], s50 offen offset:2048 sc1\n\t"
"v_mfma_f32_16x16x32_bf16 v[140:143], a[136:139], a[156:159], v[140:143]\n\t"
"ds_read_b128 a[168:171], v216 offset:26624\n\t"
"v_mfma_f32_16x16x32_bf16 v[140:143], a[140:143], a[172:175], v[140:143]\n\t"
"buffer_load_dwordx4 a[44:47], v235, s[8:11], s50 offen offset:3072 sc1\n\t"
"s_mov_b32 s57, 122880\n\t"
"v_mfma_f32_16x16x32_bf16 v[196:199], a[180:183], a[224:227], 0\n\t"
"buffer_load_dwordx4 a[48:51], v248, s[8:11], s41 offen offset:2048 sc1\n\t"
"v_mfma_f32_16x16x32_bf16 v[124:127], a[128:131], a[156:159], v[124:127]\n\t"
"v_cvt_pk_bf16_f32 v192, v192, v193\n\t"
"v_cvt_pk_bf16_f32 v193, v194, v195\n\t"
"v_mfma_f32_16x16x32_bf16 v[124:127], a[132:135], a[172:175], v[124:127]\n\t"
"buffer_load_dwordx4 a[52:55], v248, s[8:11], s41 offen offset:3072 sc1\n\t"
"v_mfma_f32_16x16x32_bf16 v[108:111], a[120:123], a[156:159], v[108:111]\n\t"
"ds_read_b128 a[156:159], v216 offset:19456\n\t"
"v_mfma_f32_16x16x32_bf16 v[108:111], a[124:127], a[172:175], v[108:111]\n\t"
"v_cvt_pk_bf16_f32 v194, v196, v197\n\t"
"v_cvt_pk_bf16_f32 v195, v198, v199\n\t"
"s_mov_b32 s58, 131072\n\t"
"v_mfma_f32_16x16x32_bf16 v[200:203], a[184:187], a[224:227], 0\n\t"
"ds_read_b128 a[172:175], v216 offset:27648\n\t"
"s_waitcnt vmcnt(19) lgkmcnt(6)\n\t"
"v_mfma_f32_16x16x32_bf16 v[144:147], a[0:3], a[144:147], 0\n\t"
"v_mfma_f32_16x16x32_bf16 v[144:147], a[4:7], a[160:163], v[144:147]\n\t"
"buffer_load_dwordx4 a[56:59], v235, s[8:11], s41 offen offset:0 sc1\n\t"
"ds_write_b128 v221, v[192:195] offset:0\n\t"
"v_mfma_f32_16x16x32_bf16 v[204:207], a[188:191], a[224:227], 0\n\t"
"s_mov_b32 s59, 139264\n\t"
"s_waitcnt vmcnt(18)\n\t"
"v_mfma_f32_16x16x32_bf16 v[160:163], a[8:11], a[144:147], 0\n\t"
"v_cvt_pk_bf16_f32 v200, v200, v201\n\t"
"v_cvt_pk_bf16_f32 v201, v202, v203\n\t"
"v_mfma_f32_16x16x32_bf16 v[160:163], a[12:15], a[160:163], v[160:163]\n\t"
"buffer_load_dwordx4 a[60:63], v235, s[8:11], s41 offen offset:1024 sc1\n\t"
"s_waitcnt vmcnt(17)\n\t"
"v_mfma_f32_16x16x32_bf16 v[176:179], a[16:19], a[144:147], 0\n\t"
"ds_read_b128 a[144:147], v216 offset:20480\n\t"
"v_mfma_f32_16x16x32_bf16 v[176:179], a[20:23], a[160:163], v[176:179]\n\t"
"v_cvt_pk_bf16_f32 v202, v204, v205\n\t"
"v_cvt_pk_bf16_f32 v203, v206, v207\n\t"
"ds_read_b128 a[160:163], v216 offset:28672\n\t"
"s_mov_b32 s63, 147456\n\t"
"v_mfma_f32_16x16x32_bf16 v[208:211], a[192:195], a[224:227], 0\n\t"
"buffer_load_dwordx4 a[64:67], v235, s[8:11], s41 offen offset:2048 sc1\n\t"
"ds_write_b128 v221, v[200:203] offset:8192\n\t"
"s_waitcnt lgkmcnt(8)\n\t"
"v_mfma_f32_16x16x32_bf16 v[180:183], a[16:19], a[148:151], 0\n\t"
"v_mfma_f32_16x16x32_bf16 v[180:183], a[20:23], a[164:167], v[180:183]\n\t"
"v_mfma_f32_16x16x32_bf16 v[164:167], a[8:11], a[148:151], 0\n\t"
"v_mfma_f32_16x16x32_bf16 v[164:167], a[12:15], a[164:167], v[164:167]\n\t"
"buffer_load_dwordx4 a[68:71], v235, s[8:11], s41 offen offset:3072 sc1\n\t"
"v_cvt_pk_bf16_f32 v208, v208, v209\n\t"
"v_cvt_pk_bf16_f32 v209, v210, v211\n\t"
"s_mov_b32 s64, 155648\n\t"
"v_mfma_f32_16x16x32_bf16 v[212:215], a[196:199], a[224:227], 0\n\t"
"s_waitcnt vmcnt(12)\n\t"
"s_barrier\n\t"
"v_mfma_f32_16x16x32_bf16 v[148:151], a[0:3], a[148:151], 0\n\t"
"ds_read_b128 a[148:151], v216 offset:21504\n\t"
"v_mfma_f32_16x16x32_bf16 v[148:151], a[4:7], a[164:167], v[148:151]\n\t"
"buffer_load_dwordx4 a[72:75], v248, s[8:11], s53 offen offset:2048 sc1\n\t"
"buffer_load_dwordx4 a[76:79], v248, s[8:11], s53 offen offset:3072 sc1\n\t"
"v_mfma_f32_16x16x32_bf16 v[192:195], a[200:203], a[224:227], 0\n\t"
"buffer_load_dwordx4 a[80:83], v235, s[8:11], s53 offen offset:0 sc1\n\t"
"ds_read_b128 a[164:167], v216 offset:29696\n\t"
"ds_read_b64_tr_b16 a[176:177], v251 offset:62208\n\t"
"s_mov_b32 s65, 163840\n\t"
"s_waitcnt lgkmcnt(9)\n\t"
"v_mfma_f32_16x16x32_bf16 v[152:155], a[0:3], a[152:155], 0\n\t"
"v_cvt_pk_bf16_f32 v210, v212, v213\n\t"
"v_cvt_pk_bf16_f32 v211, v214, v215\n\t"
"v_mfma_f32_16x16x32_bf16 v[152:155], a[4:7], a[168:171], v[152:155]\n\t"
"buffer_load_dwordx4 a[84:87], v235, s[8:11], s53 offen offset:1024 sc1\n\t"
"ds_write_b128 v221, v[208:211] offset:4096\n\t"
"v_mfma_f32_16x16x32_bf16 v[168:171], a[8:11], a[152:155], 0\n\t"
"ds_read_b64_tr_b16 a[178:179], v251 offset:62720\n\t"
"v_mfma_f32_16x16x32_bf16 v[168:171], a[12:15], a[168:171], v[168:171]\n\t"
"buffer_load_dwordx4 a[88:91], v235, s[8:11], s53 offen offset:2048 sc1\n\t"
"s_mov_b32 s67, 180224\n\t"
"v_mfma_f32_16x16x32_bf16 v[196:199], a[204:207], a[224:227], 0\n\t"
"v_cvt_pk_bf16_f32 v192, v192, v193\n\t"
"v_cvt_pk_bf16_f32 v193, v194, v195\n\t"
"v_mfma_f32_16x16x32_bf16 v[184:187], a[16:19], a[152:155], 0\n\t"
"ds_read_b64_tr_b16 a[180:181], v252 offset:62208\n\t"
"v_mfma_f32_16x16x32_bf16 v[184:187], a[20:23], a[168:171], v[184:187]\n\t"
"buffer_load_dwordx4 a[92:95], v235, s[8:11], s53 offen offset:3072 sc1\n\t"
"s_waitcnt lgkmcnt(10)\n\t"
"v_mfma_f32_16x16x32_bf16 v[188:191], a[16:19], a[156:159], 0\n\t"
"ds_read_b64_tr_b16 a[182:183], v252 offset:62720\n\t"
"v_mfma_f32_16x16x32_bf16 v[188:191], a[20:23], a[172:175], v[188:191]\n\t"
"ds_read_b64_tr_b16 a[184:185], v249 offset:62208\n\t"
"v_cvt_pk_bf16_f32 v194, v196, v197\n\t"
"s_mov_b32 s68, 188416\n\t"
"v_mfma_f32_16x16x32_bf16 v[172:175], a[8:11], a[156:159], 0\n\t"
"ds_read_b64_tr_b16 a[186:187], v249 offset:62720\n\t"
"v_cvt_pk_bf16_f32 v195, v198, v199\n\t"
"v_mfma_f32_16x16x32_bf16 v[172:175], a[12:15], a[172:175], v[172:175]\n\t"
"buffer_load_dwordx4 a[96:99], v248, s[8:11], s56 offen offset:2048 sc1\n\t"
"v_mfma_f32_16x16x32_bf16 v[156:159], a[0:3], a[156:159], 0\n\t"
"ds_read_b64_tr_b16 a[188:189], v250 offset:62208\n\t"
"v_mfma_f32_16x16x32_bf16 v[156:159], a[4:7], a[172:175], v[156:159]\n\t"
"ds_write_b128 v221, v[192:195] offset:12288\n\t"
"ds_read_b64_tr_b16 a[190:191], v250 offset:62720\n\t"
"s_mov_b32 s69, 196608\n\t"
"s_waitcnt vmcnt(17) lgkmcnt(13)\n\t"
"v_mfma_f32_16x16x32_bf16 v[48:51], a[24:27], a[144:147], v[144:147]\n\t"
"ds_read_b64_tr_b16 a[192:193], v251 offset:63360\n\t"
"v_mfma_f32_16x16x32_bf16 v[48:51], a[28:31], a[160:163], v[48:51]\n\t"
"ds_read_b64_tr_b16 a[194:195], v251 offset:63872\n\t"
"ds_read_b64_tr_b16 a[196:197], v252 offset:63360\n\t"
"ds_read_b64_tr_b16 a[198:199], v252 offset:63872\n\t"
"ds_read_b64_tr_b16 a[200:201], v249 offset:63360\n\t"
"ds_read_b64_tr_b16 a[202:203], v249 offset:63872\n\t"
"ds_read_b64_tr_b16 a[204:205], v250 offset:63360\n\t"
"ds_read_b64_tr_b16 a[206:207], v250 offset:63872\n\t"
"s_waitcnt vmcnt(15)\n\t"
"v_mfma_f32_16x16x32_bf16 v[64:67], a[32:35], a[144:147], v[160:163]\n\t"
"ds_read_b128 a[152:155], v216 offset:22528\n\t"
"v_mfma_f32_16x16x32_bf16 v[64:67], a[36:39], a[160:163], v[64:67]\n\t"
"buffer_load_dwordx4 a[100:103], v248, s[8:11], s56 offen offset:3072 sc1\n\t"
"s_mov_b32 s72, 204800\n\t"
"s_waitcnt vmcnt(14)\n\t"
"v_mfma_f32_16x16x32_bf16 v[80:83], a[40:43], a[144:147], v[176:179]\n\t"
"ds_read_b128 a[168:171], v216 offset:30720\n\t"
"v_mfma_f32_16x16x32_bf16 v[80:83], a[44:47], a[160:163], v[80:83]\n\t"
"buffer_load_dwordx4 a[104:107], v235, s[8:11], s56 offen offset:0 sc1\n\t"
"s_waitcnt lgkmcnt(15)\n\t"
"v_mfma_f32_16x16x32_bf16 v[84:87], a[40:43], a[148:151], v[180:183]\n\t"
"ds_read_b128 a[156:159], v216 offset:23552\n\t"
"v_mfma_f32_16x16x32_bf16 v[84:87], a[44:47], a[164:167], v[84:87]\n\t"
"ds_read_b128 a[172:175], v216 offset:31744\n\t"
"v_mfma_f32_16x16x32_bf16 v[68:71], a[32:35], a[148:151], v[164:167]\n\t"
"v_mfma_f32_16x16x32_bf16 v[68:71], a[36:39], a[164:167], v[68:71]\n\t"
"buffer_load_dwordx4 a[108:111], v235, s[8:11], s56 offen offset:1024 sc1\n\t"
"v_mfma_f32_16x16x32_bf16 v[52:55], a[24:27], a[148:151], v[148:151]\n\t"
"v_mfma_f32_16x16x32_bf16 v[52:55], a[28:31], a[164:167], v[52:55]\n\t"
"buffer_load_dwordx4 a[112:115], v235, s[8:11], s56 offen offset:2048 sc1\n\t"
"s_waitcnt lgkmcnt(0)\n\t"
"s_add_u32 s71, s71, 1\n\t"
"v_mov_b32 v215, s71\n\t"
"s_mov_b64 exec, 1\n\t"
"buffer_store_dword v215, off, s[8:11], s70 sc1\n\t"
"s_mov_b64 exec, -1\n\t"
"s_barrier\n\t"
"v_mfma_f32_16x16x32_bf16 v[56:59], a[24:27], a[152:155], v[152:155]\n\t"
"ds_read_b128 a[144:147], v216 offset:0\n\t"
"v_add_u32 v249, 80896, v225\n\t"
"v_mfma_f32_16x16x32_bf16 v[56:59], a[28:31], a[168:171], v[56:59]\n\t"
"ds_read_b128 a[160:163], v216 offset:8192\n\t"
"ds_read_b128 a[148:151], v216 offset:1024\n\t"
"v_mfma_f32_16x16x32_bf16 v[72:75], a[32:35], a[152:155], v[168:171]\n\t"
"ds_read_b128 a[164:167], v216 offset:9216\n\t"
"v_xor_b32 v250, 32, v249\n\t"
"v_mfma_f32_16x16x32_bf16 v[72:75], a[36:39], a[168:171], v[72:75]\n\t"
"buffer_load_dwordx4 a[116:119], v235, s[8:11], s56 offen offset:3072 sc1\n\t"
"v_mfma_f32_16x16x32_bf16 v[88:91], a[40:43], a[152:155], v[184:187]\n\t"
"v_xor_b32 v251, 64, v249\n\t"
"v_xor_b32 v252, 96, v249\n\t"
"v_mfma_f32_16x16x32_bf16 v[88:91], a[44:47], a[168:171], v[88:91]\n\t"
"buffer_load_dwordx4 a[120:123], v248, s[8:11], s68 offen offset:2048 sc1\n\t"
"s_mov_b32 s74, 212992\n\t"
"v_mfma_f32_16x16x32_bf16 v[200:203], a[176:179], a[224:227], 0\n\t"
"ds_read_b128 a[152:155], v216 offset:2048\n\t"
"v_mfma_f32_16x16x32_bf16 v[92:95], a[40:43], a[156:159], v[188:191]\n\t"
"ds_read_b128 a[168:171], v216 offset:10240\n\t"
"v_mfma_f32_16x16x32_bf16 v[92:95], a[44:47], a[172:175], v[92:95]\n\t"
"ds_read_b64_tr_b16 a[176:177], v249 offset:0\n\t"
"s_mov_b32 s75, 221184\n\t"
"v_mfma_f32_16x16x32_bf16 v[204:207], a[180:183], a[224:227], 0\n\t"
"buffer_load_dwordx4 a[124:127], v248, s[8:11], s68 offen offset:3072 sc1\n\t"
"ds_read_b64_tr_b16 a[178:179], v249 offset:512\n\t"
"ds_read_b64_tr_b16 a[180:181], v250 offset:0\n\t"
"v_mfma_f32_16x16x32_bf16 v[76:79], a[32:35], a[156:159], v[172:175]\n\t"
"v_cvt_pk_bf16_f32 v200, v200, v201\n\t"
"v_cvt_pk_bf16_f32 v201, v202, v203\n\t"
"v_mfma_f32_16x16x32_bf16 v[76:79], a[36:39], a[172:175], v[76:79]\n\t"
"ds_read_b64_tr_b16 a[182:183], v250 offset:512\n\t"
"v_mfma_f32_16x16x32_bf16 v[60:63], a[24:27], a[156:159], v[156:159]\n\t"
"ds_read_b128 a[156:159], v216 offset:3072\n\t"
"v_mfma_f32_16x16x32_bf16 v[60:63], a[28:31], a[172:175], v[60:63]\n\t"
"buffer_load_dwordx4 a[128:131], v235, s[8:11], s68 offen offset:0 sc1\n\t"
"v_cvt_pk_bf16_f32 v202, v204, v205\n\t"
"v_cvt_pk_bf16_f32 v203, v206, v207\n\t"
"s_mov_b32 s76, 229376\n\t"
"v_mfma_f32_16x16x32_bf16 v[208:211], a[184:187], a[224:227], 0\n\t"
"v_pk_fma_f32 v[144:145], v[144:145], s[38:39], v[48:49] neg_lo:[1,0,1] neg_hi:[1,0,1]\n\t"
"v_pk_fma_f32 v[146:147], v[146:147], s[38:39], v[50:51] neg_lo:[1,0,1] neg_hi:[1,0,1]\n\t"
"v_pk_fma_f32 v[160:161], v[160:161], s[38:39], v[64:65] neg_lo:[1,0,1] neg_hi:[1,0,1]\n\t"
"v_pk_fma_f32 v[162:163], v[162:163], s[38:39], v[66:67] neg_lo:[1,0,1] neg_hi:[1,0,1]\n\t"
"v_pk_fma_f32 v[176:177], v[176:177], s[38:39], v[80:81] neg_lo:[1,0,1] neg_hi:[1,0,1]\n\t"
"v_pk_fma_f32 v[178:179], v[178:179], s[38:39], v[82:83] neg_lo:[1,0,1] neg_hi:[1,0,1]\n\t"
"v_pk_fma_f32 v[180:181], v[180:181], s[38:39], v[84:85] neg_lo:[1,0,1] neg_hi:[1,0,1]\n\t"
"v_pk_fma_f32 v[182:183], v[182:183], s[38:39], v[86:87] neg_lo:[1,0,1] neg_hi:[1,0,1]\n\t"
"v_pk_fma_f32 v[164:165], v[164:165], s[38:39], v[68:69] neg_lo:[1,0,1] neg_hi:[1,0,1]\n\t"
"v_pk_fma_f32 v[166:167], v[166:167], s[38:39], v[70:71] neg_lo:[1,0,1] neg_hi:[1,0,1]\n\t"
"v_pk_fma_f32 v[148:149], v[148:149], s[38:39], v[52:53] neg_lo:[1,0,1] neg_hi:[1,0,1]\n\t"
"v_pk_fma_f32 v[150:151], v[150:151], s[38:39], v[54:55] neg_lo:[1,0,1] neg_hi:[1,0,1]\n\t"
"v_pk_fma_f32 v[152:153], v[152:153], s[38:39], v[56:57] neg_lo:[1,0,1] neg_hi:[1,0,1]\n\t"
"v_pk_fma_f32 v[154:155], v[154:155], s[38:39], v[58:59] neg_lo:[1,0,1] neg_hi:[1,0,1]\n\t"
"v_pk_fma_f32 v[168:169], v[168:169], s[38:39], v[72:73] neg_lo:[1,0,1] neg_hi:[1,0,1]\n\t"
"v_pk_fma_f32 v[170:171], v[170:171], s[38:39], v[74:75] neg_lo:[1,0,1] neg_hi:[1,0,1]\n\t"
"v_pk_fma_f32 v[184:185], v[184:185], s[38:39], v[88:89] neg_lo:[1,0,1] neg_hi:[1,0,1]\n\t"
"v_pk_fma_f32 v[186:187], v[186:187], s[38:39], v[90:91] neg_lo:[1,0,1] neg_hi:[1,0,1]\n\t"
"v_pk_fma_f32 v[188:189], v[188:189], s[38:39], v[92:93] neg_lo:[1,0,1] neg_hi:[1,0,1]\n\t"
"v_pk_fma_f32 v[190:191], v[190:191], s[38:39], v[94:95] neg_lo:[1,0,1] neg_hi:[1,0,1]\n\t"
"v_pk_fma_f32 v[172:173], v[172:173], s[38:39], v[76:77] neg_lo:[1,0,1] neg_hi:[1,0,1]\n\t"
"v_pk_fma_f32 v[174:175], v[174:175], s[38:39], v[78:79] neg_lo:[1,0,1] neg_hi:[1,0,1]\n\t"
"v_pk_fma_f32 v[156:157], v[156:157], s[38:39], v[60:61] neg_lo:[1,0,1] neg_hi:[1,0,1]\n\t"
"v_pk_fma_f32 v[158:159], v[158:159], s[38:39], v[62:63] neg_lo:[1,0,1] neg_hi:[1,0,1]\n\t"
"ds_read_b128 a[172:175], v216 offset:11264\n\t"
"s_waitcnt vmcnt(20) lgkmcnt(10)\n\t"
"v_mfma_f32_16x16x32_bf16 v[48:51], a[48:51], a[144:147], v[48:51]\n\t"
"ds_read_b64_tr_b16 a[184:185], v251 offset:2304\n\t"
"v_mfma_f32_16x16x32_bf16 v[48:51], a[52:55], a[160:163], v[48:51]\n\t"
"buffer_load_dwordx4 a[132:135], v235, s[8:11], s68 offen offset:1024 sc1\n\t"
"ds_write_b128 v221, v[200:203] offset:16384\n\t"
"v_mfma_f32_16x16x32_bf16 v[212:215], a[188:191], a[224:227], 0\n\t"
"ds_read_b64_tr_b16 a[186:187], v251 offset:2816\n\t"
"s_waitcnt vmcnt(19)\n\t"
"v_mfma_f32_16x16x32_bf16 v[64:67], a[56:59], a[144:147], v[64:67]\n\t"
"v_cvt_pk_bf16_f32 v208, v208, v209\n\t"
"v_cvt_pk_bf16_f32 v209, v210, v211\n\t"
"v_mfma_f32_16x16x32_bf16 v[64:67], a[60:63], a[160:163], v[64:67]\n\t"
"buffer_load_dwordx4 a[136:139], v235, s[8:11], s68 offen offset:2048 sc1\n\t"
"ds_read_b64_tr_b16 a[188:189], v252 offset:2304\n\t"
"ds_read_b64_tr_b16 a[190:191], v252 offset:2816\n\t"
"s_mov_b32 s77, 237568\n\t"
"s_waitcnt vmcnt(18)\n\t"
"v_mfma_f32_16x16x32_bf16 v[80:83], a[64:67], a[144:147], v[80:83]\n\t"
"ds_read_b128 a[144:147], v216 offset:4096\n\t"
"v_mfma_f32_16x16x32_bf16 v[80:83], a[68:71], a[160:163], v[80:83]\n\t"
"v_cvt_pk_bf16_f32 v210, v212, v213\n\t"
"v_cvt_pk_bf16_f32 v211, v214, v215\n\t"
"ds_read_b128 a[160:163], v216 offset:12288\n\t"
"v_mfma_f32_16x16x32_bf16 v[192:195], a[192:195], a[224:227], 0\n\t"
"ds_write_b128 v221, v[208:211] offset:24576\n\t"
"s_waitcnt lgkmcnt(15)\n\t"
"v_mfma_f32_16x16x32_bf16 v[84:87], a[64:67], a[148:151], v[84:87]\n\t"
"ds_read_b64_tr_b16 a[192:193], v251 offset:0\n\t"
"v_mfma_f32_16x16x32_bf16 v[84:87], a[68:71], a[164:167], v[84:87]\n\t"
"buffer_load_dwordx4 a[140:143], v235, s[8:11], s68 offen offset:3072 sc1\n\t"
"ds_read_b64_tr_b16 a[194:195], v251 offset:512\n\t"
"s_mov_b32 s78, 245760\n\t"
"v_mfma_f32_16x16x32_bf16 v[68:71], a[56:59], a[148:151], v[68:71]\n\t"
"v_mfma_f32_16x16x32_bf16 v[68:71], a[60:63], a[164:167], v[68:71]\n\t"
"buffer_load_dwordx4 a[0:3], v235, s[8:11], 0 offen offset:2048 sc1\n\t"
"v_cvt_pk_bf16_f32 v192, v192, v193\n\t"
"v_cvt_pk_bf16_f32 v193, v194, v195\n\t"
"v_mfma_f32_16x16x32_bf16 v[196:199], a[196:199], a[224:227], 0\n\t"
"ds_read_b64_tr_b16 a[196:197], v252 offset:0\n\t"
"ds_read_b64_tr_b16 a[198:199], v252 offset:512\n\t"
"v_mfma_f32_16x16x32_bf16 v[52:55], a[48:51], a[148:151], v[52:55]\n\t"
"ds_read_b128 a[148:151], v216 offset:5120\n\t"
"v_mfma_f32_16x16x32_bf16 v[52:55], a[52:55], a[164:167], v[52:55]\n\t"
"ds_read_b128 a[164:167], v216 offset:13312\n\t"
"s_mov_b32 s79, 253952\n\t"
"v_mfma_f32_16x16x32_bf16 v[200:203], a[200:203], a[224:227], 0\n\t"
"buffer_load_dwordx4 a[4:7], v235, s[8:11], 0 offen offset:3072 sc1\n\t"
"ds_read_b64_tr_b16 a[200:201], v249 offset:2304\n\t"
"ds_read_b64_tr_b16 a[202:203], v249 offset:2816\n\t"
"s_waitcnt lgkmcnt(15)\n\t"
"v_mfma_f32_16x16x32_bf16 v[56:59], a[48:51], a[152:155], v[56:59]\n\t"
"v_cvt_pk_bf16_f32 v194, v196, v197\n\t"
"v_cvt_pk_bf16_f32 v195, v198, v199\n\t"
"v_mfma_f32_16x16x32_bf16 v[56:59], a[52:55], a[168:171], v[56:59]\n\t"
"ds_write_b128 v221, v[192:195] offset:20480\n\t"
"s_mov_b32 s80, 262144\n\t"
"v_mfma_f32_16x16x32_bf16 v[72:75], a[56:59], a[152:155], v[72:75]\n\t"
"v_mfma_f32_16x16x32_bf16 v[72:75], a[60:63], a[168:171], v[72:75]\n\t"
"buffer_load_dwordx4 a[8:11], v248, s[8:11], s40 offen offset:0 sc1\n\t"
"v_cvt_pk_bf16_f32 v200, v200, v201\n\t"
"v_cvt_pk_bf16_f32 v201, v202, v203\n\t"
"v_mfma_f32_16x16x32_bf16 v[204:207], a[204:207], a[224:227], 0\n\t"
"ds_read_b64_tr_b16 a[204:205], v250 offset:2304\n\t"
"ds_read_b64_tr_b16 a[206:207], v250 offset:2816\n\t"
"v_mfma_f32_16x16x32_bf16 v[88:91], a[64:67], a[152:155], v[88:91]\n\t"
"ds_read_b128 a[152:155], v216 offset:6144\n\t"
"v_mfma_f32_16x16x32_bf16 v[88:91], a[68:71], a[168:171], v[88:91]\n\t"
"buffer_load_dwordx4 a[12:15], v248, s[8:11], s40 offen offset:1024 sc1\n\t"
"ds_read_b128 a[168:171], v216 offset:14336\n\t"
"s_mov_b32 s81, 270336\n\t"
"v_mfma_f32_16x16x32_bf16 v[92:95], a[64:67], a[156:159], v[92:95]\n\t"
"v_mfma_f32_16x16x32_bf16 v[92:95], a[68:71], a[172:175], v[92:95]\n\t"
"buffer_load_dwordx4 a[16:19], v248, s[8:11], s40 offen offset:2048 sc1\n\t"
"buffer_load_dwordx4 a[20:23], v248, s[8:11], s40 offen offset:3072 sc1\n\t"
"v_mfma_f32_16x16x32_bf16 v[76:79], a[56:59], a[156:159], v[76:79]\n\t"
"v_cvt_pk_bf16_f32 v202, v204, v205\n\t"
"v_cvt_pk_bf16_f32 v203, v206, v207\n\t"
"v_mfma_f32_16x16x32_bf16 v[76:79], a[60:63], a[172:175], v[76:79]\n\t"
"buffer_load_dwordx4 a[24:27], v235, s[8:11], s40 offen offset:0 sc1\n\t"
"buffer_load_dwordx4 a[28:31], v235, s[8:11], s40 offen offset:1024 sc1\n\t"
"v_mfma_f32_16x16x32_bf16 v[60:63], a[48:51], a[156:159], v[60:63]\n\t"
"ds_read_b128 a[156:159], v216 offset:7168\n\t"
"v_mfma_f32_16x16x32_bf16 v[60:63], a[52:55], a[172:175], v[60:63]\n\t"
"ds_write_b128 v221, v[200:203] offset:28672\n\t"
"buffer_load_dwordx4 a[32:35], v235, s[8:11], s40 offen offset:2048 sc1\n\t"
"s_mov_b32 s82, 278528\n\t"
"s_waitcnt vmcnt(26) lgkmcnt(15)\n\t"
"v_mfma_f32_16x16x32_bf16 v[144:147], a[72:75], a[144:147], v[144:147]\n\t"
"ds_read_b128 a[172:175], v216 offset:15360\n\t"
"v_mfma_f32_16x16x32_bf16 v[144:147], a[76:79], a[160:163], v[144:147]\n\t"
"buffer_load_dwordx4 a[36:39], v235, s[8:11], s40 offen offset:3072 sc1\n\t"
"buffer_load_dwordx4 a[40:43], v248, s[8:11], s41 offen offset:0 sc1\n\t"
"s_waitcnt vmcnt(26)\n\t"
"v_mfma_f32_16x16x32_bf16 v[160:163], a[80:83], a[144:147], v[160:163]\n\t"
"v_mfma_f32_16x16x32_bf16 v[160:163], a[84:87], a[160:163], v[160:163]\n\t"
"buffer_load_dwordx4 a[44:47], v248, s[8:11], s41 offen offset:1024 sc1\n\t"
"buffer_load_dwordx4 a[48:51], v235, s[8:11], s51 offen offset:2048 sc1\n\t"
"s_mov_b32 s83, 286720\n\t"
"s_waitcnt vmcnt(26)\n\t"
"v_mfma_f32_16x16x32_bf16 v[176:179], a[88:91], a[144:147], v[176:179]\n\t"
"v_mfma_f32_16x16x32_bf16 v[176:179], a[92:95], a[160:163], v[176:179]\n\t"
"buffer_load_dwordx4 a[52:55], v235, s[8:11], s51 offen offset:3072 sc1\n\t"
"buffer_load_dwordx4 a[56:59], v248, s[8:11], s52 offen offset:0 sc1\n\t"
"s_waitcnt lgkmcnt(10)\n\t"
"v_mfma_f32_16x16x32_bf16 v[180:183], a[88:91], a[148:151], v[180:183]\n\t"
"v_mfma_f32_16x16x32_bf16 v[180:183], a[92:95], a[164:167], v[180:183]\n\t"
"buffer_load_dwordx4 a[60:63], v248, s[8:11], s52 offen offset:1024 sc1\n\t"
"buffer_load_dwordx4 a[64:67], v248, s[8:11], s52 offen offset:2048 sc1\n\t"
"v_mfma_f32_16x16x32_bf16 v[164:167], a[80:83], a[148:151], v[164:167]\n\t"
"v_mfma_f32_16x16x32_bf16 v[164:167], a[84:87], a[164:167], v[164:167]\n\t"
"buffer_load_dwordx4 a[68:71], v248, s[8:11], s52 offen offset:3072 sc1\n\t"
"s_add_u32 m0, s43, 39168\n\t"
"s_mov_b64 exec, s[44:45]\n\t"
"buffer_load_dwordx4 v246, s[12:15], 0 offen offset:256 nt lds\n\t"
"s_mov_b64 exec, -1\n\t"
"v_mfma_f32_16x16x32_bf16 v[148:151], a[72:75], a[148:151], v[148:151]\n\t"
"v_mfma_f32_16x16x32_bf16 v[148:151], a[76:79], a[164:167], v[148:151]\n\t"
"s_add_u32 m0, s43, 59904\n\t"
"s_mov_b64 exec, s[44:45]\n\t"
"buffer_load_dwordx4 v247, s[12:15], 0 offen offset:256 nt lds\n\t"
"s_mov_b64 exec, -1\n\t"
"v_add_u32 v246, 8, v228\n\t"
"v_add_u32 v247, 16, v228\n\t"
"s_waitcnt lgkmcnt(0)\n\t"
"s_barrier\n\t"
"Lunit_%=:\n\t"
"v_mfma_f32_16x16x32_bf16 v[152:155], a[72:75], a[152:155], v[152:155]\n\t"
"ds_read_b128 a[144:147], v216 offset:16384\n\t"
"v_mfma_f32_16x16x32_bf16 v[152:155], a[76:79], a[168:171], v[152:155]\n\t"
"s_add_u32 m0, s26, 41472\n\t"
"ds_read_b128 a[160:163], v216 offset:24576\n\t"
"ds_read_b128 a[148:151], v216 offset:17408\n\t"
"buffer_load_dwordx4 v236, s[12:15], s23 offen offset:256 nt lds\n\t"
"v_mfma_f32_16x16x32_bf16 v[168:171], a[80:83], a[152:155], v[168:171]\n\t"
"ds_read_b128 a[164:167], v216 offset:25600\n\t"
"v_mfma_f32_16x16x32_bf16 v[168:171], a[84:87], a[168:171], v[168:171]\n\t"
"v_mfma_f32_16x16x32_bf16 v[184:187], a[88:91], a[152:155], v[184:187]\n\t"
"ds_read_b128 a[152:155], v216 offset:18432\n\t"
"v_mfma_f32_16x16x32_bf16 v[184:187], a[92:95], a[168:171], v[184:187]\n\t"
"ds_read_b128 a[168:171], v216 offset:26624\n\t"
"v_mfma_f32_16x16x32_bf16 v[192:195], a[176:179], a[208:211], 0\n\t"
"buffer_load_dwordx4 v237, s[12:15], s23 offen offset:1280 nt lds\n\t"
"v_mfma_f32_16x16x32_bf16 v[188:191], a[88:91], a[156:159], v[188:191]\n\t"
"ds_read_b64_tr_b16 a[176:177], v249 offset:1152\n\t"
"v_mfma_f32_16x16x32_bf16 v[188:191], a[92:95], a[172:175], v[188:191]\n\t"
"ds_read_b64_tr_b16 a[178:179], v249 offset:1664\n\t"
"v_mfma_f32_16x16x32_bf16 v[196:199], a[180:183], a[208:211], 0\n\t"
"ds_read_b64_tr_b16 a[180:181], v250 offset:1152\n\t"
"v_mfma_f32_16x16x32_bf16 v[172:175], a[80:83], a[156:159], v[172:175]\n\t"
"v_cvt_pk_bf16_f32 v192, v192, v193\n\t"
"v_cvt_pk_bf16_f32 v193, v194, v195\n\t"
"v_mfma_f32_16x16x32_bf16 v[172:175], a[84:87], a[172:175], v[172:175]\n\t"
"buffer_load_dwordx4 v238, s[12:15], s23 offen offset:2304 nt lds\n\t"
"v_mfma_f32_16x16x32_bf16 v[156:159], a[72:75], a[156:159], v[156:159]\n\t"
"ds_read_b128 a[156:159], v216 offset:19456\n\t"
"v_mfma_f32_16x16x32_bf16 v[156:159], a[76:79], a[172:175], v[156:159]\n\t"
"v_cvt_pk_bf16_f32 v194, v196, v197\n\t"
"v_cvt_pk_bf16_f32 v195, v198, v199\n\t"
"v_mfma_f32_16x16x32_bf16 v[200:203], a[184:187], a[208:211], 0\n\t"
"ds_read_b128 a[172:175], v216 offset:27648\n\t"
"s_waitcnt vmcnt(31) lgkmcnt(6)\n\t"
"v_mfma_f32_16x16x32_bf16 v[48:51], a[96:99], a[144:147], v[48:51]\n\t"
"ds_read_b64_tr_b16 a[182:183], v250 offset:1664\n\t"
"v_mfma_f32_16x16x32_bf16 v[48:51], a[100:103], a[160:163], v[48:51]\n\t"
"ds_write_b128 v217, v[192:195] offset:0\n\t"
"v_mfma_f32_16x16x32_bf16 v[204:207], a[188:191], a[208:211], 0\n\t"
"buffer_load_dwordx4 v239, s[12:15], s23 offen offset:3328 nt lds\n\t"
"v_mfma_f32_16x16x32_bf16 v[64:67], a[104:107], a[144:147], v[64:67]\n\t"
"v_cvt_pk_bf16_f32 v200, v200, v201\n\t"
"v_cvt_pk_bf16_f32 v201, v202, v203\n\t"
"v_mfma_f32_16x16x32_bf16 v[64:67], a[108:111], a[160:163], v[64:67]\n\t"
"ds_read_b64_tr_b16 a[184:185], v251 offset:3456\n\t"
"s_waitcnt vmcnt(30) lgkmcnt(5)\n\t"
"v_mfma_f32_16x16x32_bf16 v[80:83], a[112:115], a[144:147], v[80:83]\n\t"
"ds_read_b128 a[144:147], v216 offset:20480\n\t"
"v_mfma_f32_16x16x32_bf16 v[80:83], a[116:119], a[160:163], v[80:83]\n\t"
"v_cvt_pk_bf16_f32 v202, v204, v205\n\t"
"v_cvt_pk_bf16_f32 v203, v206, v207\n\t"
"v_mfma_f32_16x16x32_bf16 v[208:211], a[192:195], a[208:211], 0\n\t"
"s_mov_b64 exec, s[36:37]\n\t"
"buffer_load_dwordx4 v240, s[12:15], s23 offen offset:3840 nt lds\n\t"
"s_mov_b64 exec, -1\n\t"
"s_add_u32 m0, s26, 62208\n\t"
"v_mfma_f32_16x16x32_bf16 v[84:87], a[112:115], a[148:151], v[84:87]\n\t"
"ds_read_b128 a[160:163], v216 offset:28672\n\t"
"v_mfma_f32_16x16x32_bf16 v[84:87], a[116:119], a[164:167], v[84:87]\n\t"
"ds_write_b128 v219, v[200:203] offset:0\n\t"
"v_mfma_f32_16x16x32_bf16 v[68:71], a[104:107], a[148:151], v[68:71]\n\t"
"ds_read_b64_tr_b16 a[186:187], v251 offset:3968\n\t"
"v_mfma_f32_16x16x32_bf16 v[68:71], a[108:111], a[164:167], v[68:71]\n\t"
"v_cvt_pk_bf16_f32 v208, v208, v209\n\t"
"v_cvt_pk_bf16_f32 v209, v210, v211\n\t"
"v_mfma_f32_16x16x32_bf16 v[212:215], a[196:199], a[208:211], 0\n\t"
"ds_read_b64_tr_b16 a[188:189], v252 offset:3456\n\t"
"v_mfma_f32_16x16x32_bf16 v[52:55], a[96:99], a[148:151], v[52:55]\n\t"
"ds_read_b128 a[148:151], v216 offset:21504\n\t"
"v_mfma_f32_16x16x32_bf16 v[52:55], a[100:103], a[164:167], v[52:55]\n\t"
"buffer_load_dwordx4 v241, s[12:15], s23 offen offset:256 nt lds\n\t"
"v_mfma_f32_16x16x32_bf16 v[192:195], a[200:203], a[208:211], 0\n\t"
"ds_read_b128 a[164:167], v216 offset:29696\n\t"
"v_mfma_f32_16x16x32_bf16 v[56:59], a[96:99], a[152:155], v[56:59]\n\t"
"v_cvt_pk_bf16_f32 v210, v212, v213\n\t"
"v_cvt_pk_bf16_f32 v211, v214, v215\n\t"
"v_mfma_f32_16x16x32_bf16 v[56:59], a[100:103], a[168:171], v[56:59]\n\t"
"ds_write_b128 v217, v[208:211] offset:8192\n\t"
"v_mfma_f32_16x16x32_bf16 v[72:75], a[104:107], a[152:155], v[72:75]\n\t"
"ds_read_b64_tr_b16 a[190:191], v252 offset:3968\n\t"
"v_mfma_f32_16x16x32_bf16 v[72:75], a[108:111], a[168:171], v[72:75]\n\t"
"buffer_load_dwordx4 v242, s[12:15], s23 offen offset:1280 nt lds\n\t"
"v_mfma_f32_16x16x32_bf16 v[196:199], a[204:207], a[208:211], 0\n\t"
"v_cvt_pk_bf16_f32 v192, v192, v193\n\t"
"v_mfma_f32_16x16x32_bf16 v[88:91], a[112:115], a[152:155], v[88:91]\n\t"
"v_cvt_pk_bf16_f32 v193, v194, v195\n\t"
"ds_read_b64_tr_b16 a[192:193], v251 offset:1152\n\t"
"v_mfma_f32_16x16x32_bf16 v[88:91], a[116:119], a[168:171], v[88:91]\n\t"
"ds_read_b64_tr_b16 a[194:195], v251 offset:1664\n\t"
"s_waitcnt lgkmcnt(6)\n\t"
"v_mfma_f32_16x16x32_bf16 v[92:95], a[112:115], a[156:159], v[92:95]\n\t"
"ds_read_b64_tr_b16 a[196:197], v252 offset:1152\n\t"
"v_mfma_f32_16x16x32_bf16 v[92:95], a[116:119], a[172:175], v[92:95]\n\t"
"buffer_load_dwordx4 v243, s[12:15], s23 offen offset:2304 nt lds\n\t"
"v_mfma_f32_16x16x32_bf16 v[76:79], a[104:107], a[156:159], v[76:79]\n\t"
"ds_read_b64_tr_b16 a[198:199], v252 offset:1664\n\t"
"v_mfma_f32_16x16x32_bf16 v[76:79], a[108:111], a[172:175], v[76:79]\n\t"
"ds_read_b64_tr_b16 a[200:201], v249 offset:3456\n\t"
"v_cvt_pk_bf16_f32 v194, v196, v197\n\t"
"v_mfma_f32_16x16x32_bf16 v[60:63], a[96:99], a[156:159], v[60:63]\n\t"
"ds_read_b64_tr_b16 a[202:203], v249 offset:3968\n\t"
"v_mfma_f32_16x16x32_bf16 v[60:63], a[100:103], a[172:175], v[60:63]\n\t"
"ds_read_b64_tr_b16 a[204:205], v250 offset:3456\n\t"
"v_cvt_pk_bf16_f32 v195, v198, v199\n\t"
"s_waitcnt vmcnt(29) lgkmcnt(7)\n\t"
"v_mfma_f32_16x16x32_bf16 v[144:147], a[120:123], a[144:147], v[144:147]\n\t"
"ds_read_b64_tr_b16 a[206:207], v250 offset:3968\n\t"
"v_mfma_f32_16x16x32_bf16 v[144:147], a[124:127], a[160:163], v[144:147]\n\t"
"ds_write_b128 v219, v[192:195] offset:8192\n\t"
"buffer_load_dwordx4 v244, s[12:15], s23 offen offset:3328 nt lds\n\t"
"v_mfma_f32_16x16x32_bf16 v[160:163], a[128:131], a[144:147], v[160:163]\n\t"
"v_mfma_f32_16x16x32_bf16 v[160:163], a[132:135], a[160:163], v[160:163]\n\t"
"v_pk_add_f32 v[0:1], v[0:1], v[48:49]\n\t"
"v_pk_add_f32 v[2:3], v[2:3], v[50:51]\n\t"
"v_pk_fma_f32 v[48:49], v[48:49], s[38:39], v[0:1]\n\t"
"v_pk_fma_f32 v[50:51], v[50:51], s[38:39], v[2:3]\n\t"
"v_pk_add_f32 v[16:17], v[16:17], v[64:65]\n\t"
"v_pk_add_f32 v[18:19], v[18:19], v[66:67]\n\t"
"v_pk_fma_f32 v[64:65], v[64:65], s[38:39], v[16:17]\n\t"
"v_pk_fma_f32 v[66:67], v[66:67], s[38:39], v[18:19]\n\t"
"v_pk_add_f32 v[32:33], v[32:33], v[80:81]\n\t"
"v_pk_add_f32 v[34:35], v[34:35], v[82:83]\n\t"
"v_pk_fma_f32 v[80:81], v[80:81], s[38:39], v[32:33]\n\t"
"v_pk_fma_f32 v[82:83], v[82:83], s[38:39], v[34:35]\n\t"
"v_pk_add_f32 v[36:37], v[36:37], v[84:85]\n\t"
"v_pk_add_f32 v[38:39], v[38:39], v[86:87]\n\t"
"v_pk_fma_f32 v[84:85], v[84:85], s[38:39], v[36:37]\n\t"
"v_pk_fma_f32 v[86:87], v[86:87], s[38:39], v[38:39]\n\t"
"v_pk_add_f32 v[20:21], v[20:21], v[68:69]\n\t"
"v_pk_add_f32 v[22:23], v[22:23], v[70:71]\n\t"
"v_pk_fma_f32 v[68:69], v[68:69], s[38:39], v[20:21]\n\t"
"v_pk_fma_f32 v[70:71], v[70:71], s[38:39], v[22:23]\n\t"
"v_pk_add_f32 v[4:5], v[4:5], v[52:53]\n\t"
"v_pk_add_f32 v[6:7], v[6:7], v[54:55]\n\t"
"v_pk_fma_f32 v[52:53], v[52:53], s[38:39], v[4:5]\n\t"
"v_pk_fma_f32 v[54:55], v[54:55], s[38:39], v[6:7]\n\t"
"v_pk_add_f32 v[8:9], v[8:9], v[56:57]\n\t"
"v_pk_add_f32 v[10:11], v[10:11], v[58:59]\n\t"
"v_pk_fma_f32 v[56:57], v[56:57], s[38:39], v[8:9]\n\t"
"v_pk_fma_f32 v[58:59], v[58:59], s[38:39], v[10:11]\n\t"
"v_pk_add_f32 v[24:25], v[24:25], v[72:73]\n\t"
"v_pk_add_f32 v[26:27], v[26:27], v[74:75]\n\t"
"v_pk_fma_f32 v[72:73], v[72:73], s[38:39], v[24:25]\n\t"
"v_pk_fma_f32 v[74:75], v[74:75], s[38:39], v[26:27]\n\t"
"v_pk_add_f32 v[40:41], v[40:41], v[88:89]\n\t"
"v_pk_add_f32 v[42:43], v[42:43], v[90:91]\n\t"
"v_pk_fma_f32 v[88:89], v[88:89], s[38:39], v[40:41]\n\t"
"v_pk_fma_f32 v[90:91], v[90:91], s[38:39], v[42:43]\n\t"
"v_pk_add_f32 v[44:45], v[44:45], v[92:93]\n\t"
"v_pk_add_f32 v[46:47], v[46:47], v[94:95]\n\t"
"v_pk_fma_f32 v[92:93], v[92:93], s[38:39], v[44:45]\n\t"
"v_pk_fma_f32 v[94:95], v[94:95], s[38:39], v[46:47]\n\t"
"v_pk_add_f32 v[28:29], v[28:29], v[76:77]\n\t"
"v_pk_add_f32 v[30:31], v[30:31], v[78:79]\n\t"
"v_pk_fma_f32 v[76:77], v[76:77], s[38:39], v[28:29]\n\t"
"v_pk_fma_f32 v[78:79], v[78:79], s[38:39], v[30:31]\n\t"
"v_pk_add_f32 v[12:13], v[12:13], v[60:61]\n\t"
"v_pk_add_f32 v[14:15], v[14:15], v[62:63]\n\t"
"v_pk_fma_f32 v[60:61], v[60:61], s[38:39], v[12:13]\n\t"
"v_pk_fma_f32 v[62:63], v[62:63], s[38:39], v[14:15]\n\t"
"s_waitcnt vmcnt(29) lgkmcnt(7)\n\t"
"v_mfma_f32_16x16x32_bf16 v[176:179], a[136:139], a[144:147], v[176:179]\n\t"
"ds_read_b128 a[152:155], v216 offset:22528\n\t"
"v_mfma_f32_16x16x32_bf16 v[176:179], a[140:143], a[160:163], v[176:179]\n\t"
"s_mov_b64 exec, s[36:37]\n\t"
"buffer_load_dwordx4 v245, s[12:15], s23 offen offset:3840 nt lds\n\t"
"s_mov_b64 exec, -1\n\t"
"v_mfma_f32_16x16x32_bf16 v[180:183], a[136:139], a[148:151], v[180:183]\n\t"
"ds_read_b128 a[168:171], v216 offset:30720\n\t"
"v_mfma_f32_16x16x32_bf16 v[180:183], a[140:143], a[164:167], v[180:183]\n\t"
"ds_read_b128 a[156:159], v216 offset:23552\n\t"
"ds_read_b128 a[172:175], v216 offset:31744\n\t"
"v_mfma_f32_16x16x32_bf16 v[164:167], a[128:131], a[148:151], v[164:167]\n\t"
"v_mfma_f32_16x16x32_bf16 v[164:167], a[132:135], a[164:167], v[164:167]\n\t"
"v_mfma_f32_16x16x32_bf16 v[148:151], a[120:123], a[148:151], v[148:151]\n\t"
"v_mfma_f32_16x16x32_bf16 v[148:151], a[124:127], a[164:167], v[148:151]\n\t"
"s_waitcnt lgkmcnt(0)\n\t"
"s_barrier\n\t"
"v_mfma_f32_16x16x32_bf16 v[152:155], a[120:123], a[152:155], v[152:155]\n\t"
"ds_read_b128 a[144:147], v216 offset:0\n\t"
"v_mfma_f32_16x16x32_bf16 v[152:155], a[124:127], a[168:171], v[152:155]\n\t"
"ds_read_b128 a[160:163], v216 offset:8192\n\t"
"ds_read_b128 a[148:151], v216 offset:1024\n\t"
"v_mfma_f32_16x16x32_bf16 v[168:171], a[128:131], a[152:155], v[168:171]\n\t"
"ds_read_b128 a[164:167], v216 offset:9216\n\t"
"v_mfma_f32_16x16x32_bf16 v[168:171], a[132:135], a[168:171], v[168:171]\n\t"
"v_mfma_f32_16x16x32_bf16 v[184:187], a[136:139], a[152:155], v[184:187]\n\t"
"ds_read_b128 a[152:155], v216 offset:2048\n\t"
"v_mfma_f32_16x16x32_bf16 v[184:187], a[140:143], a[168:171], v[184:187]\n\t"
"ds_read_b128 a[168:171], v216 offset:10240\n\t"
"v_mfma_f32_16x16x32_bf16 v[200:203], a[176:179], a[208:211], 0\n\t"
"ds_read_b64_tr_b16 a[176:177], v249 offset:41472\n\t"
"v_mfma_f32_16x16x32_bf16 v[188:191], a[136:139], a[156:159], v[188:191]\n\t"
"ds_read_b64_tr_b16 a[178:179], v249 offset:41984\n\t"
"v_mfma_f32_16x16x32_bf16 v[188:191], a[140:143], a[172:175], v[188:191]\n\t"
"v_mfma_f32_16x16x32_bf16 v[204:207], a[180:183], a[208:211], 0\n\t"
"ds_read_b64_tr_b16 a[180:181], v250 offset:41472\n\t"
"v_mfma_f32_16x16x32_bf16 v[172:175], a[128:131], a[156:159], v[172:175]\n\t"
"s_nop 0\n\t"
"v_cvt_pk_bf16_f32 v200, v200, v201\n\t"
"v_cvt_pk_bf16_f32 v201, v202, v203\n\t"
"v_mfma_f32_16x16x32_bf16 v[172:175], a[132:135], a[172:175], v[172:175]\n\t"
"ds_read_b64_tr_b16 a[182:183], v250 offset:41984\n\t"
"v_mfma_f32_16x16x32_bf16 v[156:159], a[120:123], a[156:159], v[156:159]\n\t"
"ds_read_b128 a[156:159], v216 offset:3072\n\t"
"v_mfma_f32_16x16x32_bf16 v[156:159], a[124:127], a[172:175], v[156:159]\n\t"
"buffer_load_dwordx4 a[88:91], v248, s[8:11], s53 offen offset:0 sc1\n\t"
"v_cvt_pk_bf16_f32 v202, v204, v205\n\t"
"v_cvt_pk_bf16_f32 v203, v206, v207\n\t"
"v_mfma_f32_16x16x32_bf16 v[208:211], a[184:187], a[208:211], 0\n\t"
"ds_read_b128 a[172:175], v216 offset:11264\n\t"
"s_waitcnt vmcnt(24) lgkmcnt(7)\n\t"
"v_mfma_f32_16x16x32_bf16 v[0:3], a[0:3], a[144:147], v[0:3]\n\t"
"ds_read_b64_tr_b16 a[184:185], v251 offset:43776\n\t"
"v_mfma_f32_16x16x32_bf16 v[0:3], a[4:7], a[160:163], v[0:3]\n\t"
"ds_write_b128 v217, v[200:203] offset:16384\n\t"
"v_mfma_f32_16x16x32_bf16 v[212:215], a[188:191], a[208:211], 0\n\t"
"v_pk_add_f32 v[96:97], v[96:97], v[144:145]\n\t"
"v_pk_add_f32 v[98:99], v[98:99], v[146:147]\n\t"
"v_pk_fma_f32 v[144:145], v[144:145], s[38:39], v[96:97]\n\t"
"v_pk_fma_f32 v[146:147], v[146:147], s[38:39], v[98:99]\n\t"
"v_pk_add_f32 v[112:113], v[112:113], v[160:161]\n\t"
"v_pk_add_f32 v[114:115], v[114:115], v[162:163]\n\t"
"v_pk_fma_f32 v[160:161], v[160:161], s[38:39], v[112:113]\n\t"
"v_pk_fma_f32 v[162:163], v[162:163], s[38:39], v[114:115]\n\t"
"v_pk_add_f32 v[128:129], v[128:129], v[176:177]\n\t"
"v_pk_add_f32 v[130:131], v[130:131], v[178:179]\n\t"
"v_pk_fma_f32 v[176:177], v[176:177], s[38:39], v[128:129]\n\t"
"v_pk_fma_f32 v[178:179], v[178:179], s[38:39], v[130:131]\n\t"
"v_pk_add_f32 v[132:133], v[132:133], v[180:181]\n\t"
"v_pk_add_f32 v[134:135], v[134:135], v[182:183]\n\t"
"v_pk_fma_f32 v[180:181], v[180:181], s[38:39], v[132:133]\n\t"
"v_pk_fma_f32 v[182:183], v[182:183], s[38:39], v[134:135]\n\t"
"v_pk_add_f32 v[116:117], v[116:117], v[164:165]\n\t"
"v_pk_add_f32 v[118:119], v[118:119], v[166:167]\n\t"
"v_pk_fma_f32 v[164:165], v[164:165], s[38:39], v[116:117]\n\t"
"v_pk_fma_f32 v[166:167], v[166:167], s[38:39], v[118:119]\n\t"
"v_pk_add_f32 v[100:101], v[100:101], v[148:149]\n\t"
"v_pk_add_f32 v[102:103], v[102:103], v[150:151]\n\t"
"v_pk_fma_f32 v[148:149], v[148:149], s[38:39], v[100:101]\n\t"
"v_pk_fma_f32 v[150:151], v[150:151], s[38:39], v[102:103]\n\t"
"v_pk_add_f32 v[104:105], v[104:105], v[152:153]\n\t"
"v_pk_add_f32 v[106:107], v[106:107], v[154:155]\n\t"
"v_pk_fma_f32 v[152:153], v[152:153], s[38:39], v[104:105]\n\t"
"v_pk_fma_f32 v[154:155], v[154:155], s[38:39], v[106:107]\n\t"
"v_pk_add_f32 v[120:121], v[120:121], v[168:169]\n\t"
"v_pk_add_f32 v[122:123], v[122:123], v[170:171]\n\t"
"v_pk_fma_f32 v[168:169], v[168:169], s[38:39], v[120:121]\n\t"
"v_pk_fma_f32 v[170:171], v[170:171], s[38:39], v[122:123]\n\t"
"v_pk_add_f32 v[136:137], v[136:137], v[184:185]\n\t"
"v_pk_add_f32 v[138:139], v[138:139], v[186:187]\n\t"
"v_pk_fma_f32 v[184:185], v[184:185], s[38:39], v[136:137]\n\t"
"v_pk_fma_f32 v[186:187], v[186:187], s[38:39], v[138:139]\n\t"
"v_pk_add_f32 v[140:141], v[140:141], v[188:189]\n\t"
"v_pk_add_f32 v[142:143], v[142:143], v[190:191]\n\t"
"v_pk_fma_f32 v[188:189], v[188:189], s[38:39], v[140:141]\n\t"
"v_pk_fma_f32 v[190:191], v[190:191], s[38:39], v[142:143]\n\t"
"v_pk_add_f32 v[124:125], v[124:125], v[172:173]\n\t"
"v_pk_add_f32 v[126:127], v[126:127], v[174:175]\n\t"
"v_pk_fma_f32 v[172:173], v[172:173], s[38:39], v[124:125]\n\t"
"v_pk_fma_f32 v[174:175], v[174:175], s[38:39], v[126:127]\n\t"
"v_pk_add_f32 v[108:109], v[108:109], v[156:157]\n\t"
"v_pk_add_f32 v[110:111], v[110:111], v[158:159]\n\t"
"v_pk_fma_f32 v[156:157], v[156:157], s[38:39], v[108:109]\n\t"
"v_pk_fma_f32 v[158:159], v[158:159], s[38:39], v[110:111]\n\t"
"ds_read_b64_tr_b16 a[186:187], v251 offset:44288\n\t"
"v_mfma_f32_16x16x32_bf16 v[16:19], a[8:11], a[144:147], v[16:19]\n\t"
"v_cvt_pk_bf16_f32 v208, v208, v209\n\t"
"v_cvt_pk_bf16_f32 v209, v210, v211\n\t"
"v_mfma_f32_16x16x32_bf16 v[16:19], a[12:15], a[160:163], v[16:19]\n\t"
"ds_read_b64_tr_b16 a[188:189], v252 offset:43776\n\t"
"v_mfma_f32_16x16x32_bf16 v[32:35], a[16:19], a[144:147], v[32:35]\n\t"
"ds_read_b128 a[144:147], v216 offset:4096\n\t"
"v_mfma_f32_16x16x32_bf16 v[32:35], a[20:23], a[160:163], v[32:35]\n\t"
"v_cvt_pk_bf16_f32 v210, v212, v213\n\t"
"v_cvt_pk_bf16_f32 v211, v214, v215\n\t"
"v_mfma_f32_16x16x32_bf16 v[192:195], a[192:195], a[208:211], 0\n\t"
"ds_read_b128 a[160:163], v216 offset:12288\n\t"
"v_mfma_f32_16x16x32_bf16 v[36:39], a[16:19], a[148:151], v[36:39]\n\t"
"ds_read_b64_tr_b16 a[190:191], v252 offset:44288\n\t"
"v_mfma_f32_16x16x32_bf16 v[36:39], a[20:23], a[164:167], v[36:39]\n\t"
"ds_write_b128 v219, v[208:211] offset:16384\n\t"
"v_mfma_f32_16x16x32_bf16 v[20:23], a[8:11], a[148:151], v[20:23]\n\t"
"ds_read_b64_tr_b16 a[192:193], v251 offset:41472\n\t"
"v_mfma_f32_16x16x32_bf16 v[20:23], a[12:15], a[164:167], v[20:23]\n\t"
"v_cvt_pk_bf16_f32 v192, v192, v193\n\t"
"v_cvt_pk_bf16_f32 v193, v194, v195\n\t"
"v_mfma_f32_16x16x32_bf16 v[196:199], a[196:199], a[208:211], 0\n\t"
"ds_read_b64_tr_b16 a[194:195], v251 offset:41984\n\t"
"v_mfma_f32_16x16x32_bf16 v[4:7], a[0:3], a[148:151], v[4:7]\n\t"
"ds_read_b128 a[148:151], v216 offset:5120\n\t"
"v_mfma_f32_16x16x32_bf16 v[4:7], a[4:7], a[164:167], v[4:7]\n\t"
"ds_read_b128 a[164:167], v216 offset:13312\n\t"
"v_mfma_f32_16x16x32_bf16 v[200:203], a[200:203], a[208:211], 0\n\t"
"ds_read_b64_tr_b16 a[196:197], v252 offset:41472\n\t"
"s_waitcnt lgkmcnt(8)\n\t"
"v_mfma_f32_16x16x32_bf16 v[8:11], a[0:3], a[152:155], v[8:11]\n\t"
"v_cvt_pk_bf16_f32 v194, v196, v197\n\t"
"v_cvt_pk_bf16_f32 v195, v198, v199\n\t"
"v_mfma_f32_16x16x32_bf16 v[8:11], a[4:7], a[168:171], v[8:11]\n\t"
"buffer_load_dwordx4 a[92:95], v248, s[8:11], s53 offen offset:1024 sc1\n\t"
"v_mfma_f32_16x16x32_bf16 v[24:27], a[8:11], a[152:155], v[24:27]\n\t"
"ds_read_b64_tr_b16 a[198:199], v252 offset:41984\n\t"
"v_mfma_f32_16x16x32_bf16 v[24:27], a[12:15], a[168:171], v[24:27]\n\t"
"v_cvt_pk_bf16_f32 v200, v200, v201\n\t"
"v_cvt_pk_bf16_f32 v201, v202, v203\n\t"
"v_mfma_f32_16x16x32_bf16 v[204:207], a[204:207], a[208:211], 0\n\t"
"buffer_load_dwordx4 a[84:87], v235, s[8:11], s52 offen offset:3072 sc1\n\t"
"v_mfma_f32_16x16x32_bf16 v[40:43], a[16:19], a[152:155], v[40:43]\n\t"
"ds_read_b64_tr_b16 a[200:201], v249 offset:43776\n\t"
"v_mfma_f32_16x16x32_bf16 v[40:43], a[20:23], a[168:171], v[40:43]\n\t"
"ds_write_b128 v217, v[192:195] offset:24576\n\t"
"v_mfma_f32_16x16x32_bf16 v[44:47], a[16:19], a[156:159], v[44:47]\n\t"
"ds_read_b64_tr_b16 a[202:203], v249 offset:44288\n\t"
"v_mfma_f32_16x16x32_bf16 v[44:47], a[20:23], a[172:175], v[44:47]\n\t"
"buffer_load_dwordx4 a[80:83], v235, s[8:11], s52 offen offset:2048 sc1\n\t"
"v_mfma_f32_16x16x32_bf16 v[28:31], a[8:11], a[156:159], v[28:31]\n\t"
"ds_read_b64_tr_b16 a[204:205], v250 offset:43776\n\t"
"v_cvt_pk_bf16_f32 v202, v204, v205\n\t"
"v_mfma_f32_16x16x32_bf16 v[28:31], a[12:15], a[172:175], v[28:31]\n\t"
"ds_read_b64_tr_b16 a[206:207], v250 offset:44288\n\t"
"v_cvt_pk_bf16_f32 v203, v206, v207\n\t"
"v_mfma_f32_16x16x32_bf16 v[12:15], a[0:3], a[156:159], v[12:15]\n\t"
"ds_read_b128 a[152:155], v216 offset:6144\n\t"
"v_mfma_f32_16x16x32_bf16 v[12:15], a[4:7], a[172:175], v[12:15]\n\t"
"ds_write_b128 v219, v[200:203] offset:24576\n\t"
"s_waitcnt vmcnt(20) lgkmcnt(7)\n\t"
"v_mfma_f32_16x16x32_bf16 v[48:51], a[24:27], a[144:147], v[48:51]\n\t"
"ds_read_b128 a[168:171], v216 offset:14336\n\t"
"v_mfma_f32_16x16x32_bf16 v[48:51], a[28:31], a[160:163], v[48:51]\n\t"
"buffer_load_dwordx4 a[72:75], v235, s[8:11], s52 offen offset:0 sc1\n\t"
"v_mfma_f32_16x16x32_bf16 v[64:67], a[32:35], a[144:147], v[64:67]\n\t"
"ds_read_b128 a[156:159], v216 offset:7168\n\t"
"v_mfma_f32_16x16x32_bf16 v[64:67], a[36:39], a[160:163], v[64:67]\n\t"
"buffer_load_dwordx4 a[76:79], v235, s[8:11], s52 offen offset:1024 sc1\n\t"
"v_mfma_f32_16x16x32_bf16 v[80:83], a[40:43], a[144:147], v[80:83]\n\t"
"ds_read_b128 a[172:175], v216 offset:15360\n\t"
"v_mfma_f32_16x16x32_bf16 v[80:83], a[44:47], a[160:163], v[80:83]\n\t"
"v_mfma_f32_16x16x32_bf16 v[84:87], a[40:43], a[148:151], v[84:87]\n\t"
"v_mfma_f32_16x16x32_bf16 v[84:87], a[44:47], a[164:167], v[84:87]\n\t"
"buffer_load_dwordx4 a[112:115], v248, s[8:11], s55 offen offset:2048 sc1\n\t"
"v_mfma_f32_16x16x32_bf16 v[68:71], a[32:35], a[148:151], v[68:71]\n\t"
"v_mfma_f32_16x16x32_bf16 v[68:71], a[36:39], a[164:167], v[68:71]\n\t"
"buffer_load_dwordx4 a[116:119], v248, s[8:11], s55 offen offset:3072 sc1\n\t"
"v_mfma_f32_16x16x32_bf16 v[52:55], a[24:27], a[148:151], v[52:55]\n\t"
"v_mfma_f32_16x16x32_bf16 v[52:55], a[28:31], a[164:167], v[52:55]\n\t"
"s_waitcnt lgkmcnt(0)\n\t"
"s_barrier\n\t"
"v_mfma_f32_16x16x32_bf16 v[56:59], a[24:27], a[152:155], v[56:59]\n\t"
"ds_read_b128 a[144:147], v216 offset:16384\n\t"
"v_mfma_f32_16x16x32_bf16 v[56:59], a[28:31], a[168:171], v[56:59]\n\t"
"ds_read_b128 a[160:163], v216 offset:24576\n\t"
"ds_read_b128 a[148:151], v216 offset:17408\n\t"
"v_mfma_f32_16x16x32_bf16 v[72:75], a[32:35], a[152:155], v[72:75]\n\t"
"ds_read_b128 a[164:167], v216 offset:25600\n\t"
"v_mfma_f32_16x16x32_bf16 v[72:75], a[36:39], a[168:171], v[72:75]\n\t"
"buffer_load_dwordx4 a[108:111], v248, s[8:11], s55 offen offset:1024 sc1\n\t"
"v_mfma_f32_16x16x32_bf16 v[88:91], a[40:43], a[152:155], v[88:91]\n\t"
"ds_read_b128 a[152:155], v216 offset:18432\n\t"
"v_mfma_f32_16x16x32_bf16 v[88:91], a[44:47], a[168:171], v[88:91]\n\t"
"buffer_load_dwordx4 a[104:107], v248, s[8:11], s55 offen offset:0 sc1\n\t"
"v_mfma_f32_16x16x32_bf16 v[208:211], a[176:179], a[208:211], 0\n\t"
"buffer_load_dwordx4 a[96:99], v235, s[8:11], s54 offen offset:2048 sc1\n\t"
"v_mfma_f32_16x16x32_bf16 v[92:95], a[40:43], a[156:159], v[92:95]\n\t"
"ds_read_b128 a[168:171], v216 offset:26624\n\t"
"v_mfma_f32_16x16x32_bf16 v[92:95], a[44:47], a[172:175], v[92:95]\n\t"
"buffer_load_dwordx4 a[100:103], v235, s[8:11], s54 offen offset:3072 sc1\n\t"
"v_mfma_f32_16x16x32_bf16 v[212:215], a[180:183], a[208:211], 0\n\t"
"ds_read_b64_tr_b16 a[176:177], v249 offset:42624\n\t"
"v_mfma_f32_16x16x32_bf16 v[76:79], a[32:35], a[156:159], v[76:79]\n\t"
"v_cvt_pk_bf16_f32 v208, v208, v209\n\t"
"v_cvt_pk_bf16_f32 v209, v210, v211\n\t"
"v_mfma_f32_16x16x32_bf16 v[76:79], a[36:39], a[172:175], v[76:79]\n\t"
"ds_read_b64_tr_b16 a[178:179], v249 offset:43136\n\t"
"v_mfma_f32_16x16x32_bf16 v[60:63], a[24:27], a[156:159], v[60:63]\n\t"
"ds_read_b128 a[156:159], v216 offset:19456\n\t"
"v_mfma_f32_16x16x32_bf16 v[60:63], a[28:31], a[172:175], v[60:63]\n\t"
"buffer_load_dwordx4 a[136:139], v248, s[8:11], s56 offen offset:0 sc1\n\t"
"v_mfma_f32_16x16x32_bf16 v[192:195], a[184:187], a[208:211], 0\n\t"
"v_cvt_pk_bf16_f32 v210, v212, v213\n\t"
"v_cvt_pk_bf16_f32 v211, v214, v215\n\t"
"s_waitcnt vmcnt(23) lgkmcnt(4)\n\t"
"v_mfma_f32_16x16x32_bf16 v[96:99], a[48:51], a[144:147], v[96:99]\n\t"
"ds_read_b128 a[172:175], v216 offset:27648\n\t"
"v_mfma_f32_16x16x32_bf16 v[96:99], a[52:55], a[160:163], v[96:99]\n\t"
"buffer_load_dwordx4 a[140:143], v248, s[8:11], s56 offen offset:1024 sc1\n\t"
"v_mfma_f32_16x16x32_bf16 v[196:199], a[188:191], a[208:211], 0\n\t"
"ds_write_b128 v217, v[208:211] offset:0\n\t"
"v_mfma_f32_16x16x32_bf16 v[112:115], a[56:59], a[144:147], v[112:115]\n\t"
"v_cvt_pk_bf16_f32 v192, v192, v193\n\t"
"v_cvt_pk_bf16_f32 v193, v194, v195\n\t"
"v_mfma_f32_16x16x32_bf16 v[112:115], a[60:63], a[160:163], v[112:115]\n\t"
"buffer_load_dwordx4 a[132:135], v235, s[8:11], s55 offen offset:3072 sc1\n\t"
"v_mfma_f32_16x16x32_bf16 v[128:131], a[64:67], a[144:147], v[128:131]\n\t"
"ds_read_b128 a[144:147], v216 offset:20480\n\t"
"v_mfma_f32_16x16x32_bf16 v[128:131], a[68:71], a[160:163], v[128:131]\n\t"
"v_cvt_pk_bf16_f32 v194, v196, v197\n\t"
"v_cvt_pk_bf16_f32 v195, v198, v199\n\t"
"v_mfma_f32_16x16x32_bf16 v[200:203], a[192:195], a[208:211], 0\n\t"
"buffer_load_dwordx4 a[128:131], v235, s[8:11], s55 offen offset:2048 sc1\n\t"
"v_mfma_f32_16x16x32_bf16 v[132:135], a[64:67], a[148:151], v[132:135]\n\t"
"ds_read_b128 a[160:163], v216 offset:28672\n\t"
"v_mfma_f32_16x16x32_bf16 v[132:135], a[68:71], a[164:167], v[132:135]\n\t"
"ds_write_b128 v219, v[192:195] offset:0\n\t"
"v_mfma_f32_16x16x32_bf16 v[116:119], a[56:59], a[148:151], v[116:119]\n\t"
"ds_read_b64_tr_b16 a[180:181], v250 offset:42624\n\t"
"v_mfma_f32_16x16x32_bf16 v[116:119], a[60:63], a[164:167], v[116:119]\n\t"
"buffer_load_dwordx4 a[120:123], v235, s[8:11], s55 offen offset:0 sc1\n\t"
"v_mfma_f32_16x16x32_bf16 v[204:207], a[196:199], a[208:211], 0\n\t"
"v_cvt_pk_bf16_f32 v200, v200, v201\n\t"
"v_cvt_pk_bf16_f32 v201, v202, v203\n\t"
"v_mfma_f32_16x16x32_bf16 v[100:103], a[48:51], a[148:151], v[100:103]\n\t"
"ds_read_b128 a[148:151], v216 offset:21504\n\t"
"v_mfma_f32_16x16x32_bf16 v[100:103], a[52:55], a[164:167], v[100:103]\n\t"
"buffer_load_dwordx4 a[124:127], v235, s[8:11], s55 offen offset:1024 sc1\n\t"
"v_mfma_f32_16x16x32_bf16 v[208:211], a[200:203], a[208:211], 0\n\t"
"ds_read_b128 a[164:167], v216 offset:29696\n\t"
"s_waitcnt lgkmcnt(5)\n\t"
"v_mfma_f32_16x16x32_bf16 v[104:107], a[48:51], a[152:155], v[104:107]\n\t"
"v_cvt_pk_bf16_f32 v202, v204, v205\n\t"
"v_cvt_pk_bf16_f32 v203, v206, v207\n\t"
"v_mfma_f32_16x16x32_bf16 v[104:107], a[52:55], a[168:171], v[104:107]\n\t"
"ds_write_b128 v217, v[200:203] offset:8192\n\t"
"v_mfma_f32_16x16x32_bf16 v[120:123], a[56:59], a[152:155], v[120:123]\n\t"
"ds_read_b64_tr_b16 a[182:183], v250 offset:43136\n\t"
"v_mfma_f32_16x16x32_bf16 v[120:123], a[60:63], a[168:171], v[120:123]\n\t"
"buffer_load_dwordx4 a[16:19], v248, s[8:11], s67 offen offset:2048 sc1\n\t"
"v_mfma_f32_16x16x32_bf16 v[212:215], a[204:207], a[208:211], 0\n\t"
"v_cvt_pk_bf16_f32 v208, v208, v209\n\t"
"v_cvt_pk_bf16_f32 v209, v210, v211\n\t"
"v_mfma_f32_16x16x32_bf16 v[136:139], a[64:67], a[152:155], v[136:139]\n\t"
"ds_read_b64_tr_b16 a[184:185], v251 offset:44928\n\t"
"v_mfma_f32_16x16x32_bf16 v[136:139], a[68:71], a[168:171], v[136:139]\n\t"
"buffer_load_dwordx4 a[20:23], v248, s[8:11], s67 offen offset:3072 sc1\n\t"
"v_mfma_f32_16x16x32_bf16 v[140:143], a[64:67], a[156:159], v[140:143]\n\t"
"ds_read_b64_tr_b16 a[186:187], v251 offset:45440\n\t"
"v_mfma_f32_16x16x32_bf16 v[140:143], a[68:71], a[172:175], v[140:143]\n\t"
"ds_read_b64_tr_b16 a[188:189], v252 offset:44928\n\t"
"v_cvt_pk_bf16_f32 v210, v212, v213\n\t"
"v_mfma_f32_16x16x32_bf16 v[124:127], a[56:59], a[156:159], v[124:127]\n\t"
"ds_read_b64_tr_b16 a[190:191], v252 offset:45440\n\t"
"v_cvt_pk_bf16_f32 v211, v214, v215\n\t"
"v_mfma_f32_16x16x32_bf16 v[124:127], a[60:63], a[172:175], v[124:127]\n\t"
"buffer_load_dwordx4 a[12:15], v248, s[8:11], s67 offen offset:1024 sc1\n\t"
"v_mfma_f32_16x16x32_bf16 v[108:111], a[48:51], a[156:159], v[108:111]\n\t"
"ds_read_b64_tr_b16 a[192:193], v251 offset:42624\n\t"
"v_mfma_f32_16x16x32_bf16 v[108:111], a[52:55], a[172:175], v[108:111]\n\t"
"ds_write_b128 v219, v[208:211] offset:8192\n\t"
"s_waitcnt vmcnt(15) lgkmcnt(6)\n\t"
"v_mfma_f32_16x16x32_bf16 v[144:147], a[72:75], a[144:147], v[144:147]\n\t"
"ds_read_b64_tr_b16 a[194:195], v251 offset:43136\n\t"
"v_mfma_f32_16x16x32_bf16 v[144:147], a[76:79], a[160:163], v[144:147]\n\t"
"ds_read_b64_tr_b16 a[196:197], v252 offset:42624\n\t"
"ds_read_b64_tr_b16 a[198:199], v252 offset:43136\n\t"
"ds_read_b64_tr_b16 a[200:201], v249 offset:44928\n\t"
"ds_read_b64_tr_b16 a[202:203], v249 offset:45440\n\t"
"ds_read_b64_tr_b16 a[204:205], v250 offset:44928\n\t"
"ds_read_b64_tr_b16 a[206:207], v250 offset:45440\n\t"
"v_mfma_f32_16x16x32_bf16 v[160:163], a[80:83], a[144:147], v[160:163]\n\t"
"ds_read_b128 a[152:155], v216 offset:22528\n\t"
"v_mfma_f32_16x16x32_bf16 v[160:163], a[84:87], a[160:163], v[160:163]\n\t"
"buffer_load_dwordx4 a[8:11], v248, s[8:11], s67 offen offset:0 sc1\n\t"
"v_mfma_f32_16x16x32_bf16 v[176:179], a[88:91], a[144:147], v[176:179]\n\t"
"ds_read_b128 a[168:171], v216 offset:30720\n\t"
"v_mfma_f32_16x16x32_bf16 v[176:179], a[92:95], a[160:163], v[176:179]\n\t"
"buffer_load_dwordx4 a[0:3], v235, s[8:11], s66 offen offset:2048 sc1\n\t"
"v_mfma_f32_16x16x32_bf16 v[180:183], a[88:91], a[148:151], v[180:183]\n\t"
"ds_read_b128 a[156:159], v216 offset:23552\n\t"
"v_mfma_f32_16x16x32_bf16 v[180:183], a[92:95], a[164:167], v[180:183]\n\t"
"ds_read_b128 a[172:175], v216 offset:31744\n\t"
"v_mfma_f32_16x16x32_bf16 v[164:167], a[80:83], a[148:151], v[164:167]\n\t"
"v_mfma_f32_16x16x32_bf16 v[164:167], a[84:87], a[164:167], v[164:167]\n\t"
"buffer_load_dwordx4 a[4:7], v235, s[8:11], s66 offen offset:3072 sc1\n\t"
"v_mfma_f32_16x16x32_bf16 v[148:151], a[72:75], a[148:151], v[148:151]\n\t"
"v_mfma_f32_16x16x32_bf16 v[148:151], a[76:79], a[164:167], v[148:151]\n\t"
"buffer_load_dwordx4 a[40:43], v248, s[8:11], s68 offen offset:0 sc1\n\t"
"s_waitcnt lgkmcnt(0)\n\t"
"s_barrier\n\t"
"v_mfma_f32_16x16x32_bf16 v[152:155], a[72:75], a[152:155], v[152:155]\n\t"
"ds_read_b128 a[144:147], v216 offset:0\n\t"
"v_add_u32 v249, 39424, v225\n\t"
"v_mfma_f32_16x16x32_bf16 v[152:155], a[76:79], a[168:171], v[152:155]\n\t"
"ds_read_b128 a[160:163], v216 offset:8192\n\t"
"ds_read_b128 a[148:151], v216 offset:1024\n\t"
"v_mfma_f32_16x16x32_bf16 v[168:171], a[80:83], a[152:155], v[168:171]\n\t"
"ds_read_b128 a[164:167], v216 offset:9216\n\t"
"v_xor_b32 v250, 32, v249\n\t"
"v_mfma_f32_16x16x32_bf16 v[168:171], a[84:87], a[168:171], v[168:171]\n\t"
"buffer_load_dwordx4 a[44:47], v248, s[8:11], s68 offen offset:1024 sc1\n\t"
"v_mfma_f32_16x16x32_bf16 v[184:187], a[88:91], a[152:155], v[184:187]\n\t"
"v_xor_b32 v251, 64, v249\n\t"
"v_xor_b32 v252, 96, v249\n\t"
"v_mfma_f32_16x16x32_bf16 v[184:187], a[92:95], a[168:171], v[184:187]\n\t"
"buffer_load_dwordx4 a[36:39], v235, s[8:11], s67 offen offset:3072 sc1\n\t"
"v_mfma_f32_16x16x32_bf16 v[192:195], a[176:179], a[208:211], 0\n\t"
"buffer_load_dwordx4 a[32:35], v235, s[8:11], s67 offen offset:2048 sc1\n\t"
"v_mfma_f32_16x16x32_bf16 v[188:191], a[88:91], a[156:159], v[188:191]\n\t"
"ds_read_b128 a[152:155], v216 offset:2048\n\t"
"v_mfma_f32_16x16x32_bf16 v[188:191], a[92:95], a[172:175], v[188:191]\n\t"
"ds_read_b128 a[168:171], v216 offset:10240\n\t"
"v_mfma_f32_16x16x32_bf16 v[196:199], a[180:183], a[208:211], 0\n\t"
"ds_read_b64_tr_b16 a[176:177], v249 offset:0\n\t"
"v_mfma_f32_16x16x32_bf16 v[172:175], a[80:83], a[156:159], v[172:175]\n\t"
"v_cvt_pk_bf16_f32 v192, v192, v193\n\t"
"v_cvt_pk_bf16_f32 v193, v194, v195\n\t"
"v_mfma_f32_16x16x32_bf16 v[172:175], a[84:87], a[172:175], v[172:175]\n\t"
"buffer_load_dwordx4 a[24:27], v235, s[8:11], s67 offen offset:0 sc1\n\t"
"v_mfma_f32_16x16x32_bf16 v[156:159], a[72:75], a[156:159], v[156:159]\n\t"
"ds_read_b128 a[156:159], v216 offset:3072\n\t"
"v_mfma_f32_16x16x32_bf16 v[156:159], a[76:79], a[172:175], v[156:159]\n\t"
"buffer_load_dwordx4 a[28:31], v235, s[8:11], s67 offen offset:1024 sc1\n\t"
"v_mfma_f32_16x16x32_bf16 v[200:203], a[184:187], a[208:211], 0\n\t"
"v_cvt_pk_bf16_f32 v194, v196, v197\n\t"
"v_cvt_pk_bf16_f32 v195, v198, v199\n\t"
"s_waitcnt vmcnt(18) lgkmcnt(4)\n\t"
"v_mfma_f32_16x16x32_bf16 v[0:3], a[96:99], a[144:147], v[0:3]\n\t"
"ds_read_b128 a[172:175], v216 offset:11264\n\t"
"v_mfma_f32_16x16x32_bf16 v[0:3], a[100:103], a[160:163], v[0:3]\n\t"
"ds_write_b128 v217, v[192:195] offset:16384\n\t"
"v_mfma_f32_16x16x32_bf16 v[204:207], a[188:191], a[208:211], 0\n\t"
"buffer_load_dwordx4 a[64:67], v248, s[8:11], s72 offen offset:2048 sc1\n\t"
"v_mfma_f32_16x16x32_bf16 v[16:19], a[104:107], a[144:147], v[16:19]\n\t"
"v_cvt_pk_bf16_f32 v200, v200, v201\n\t"
"v_cvt_pk_bf16_f32 v201, v202, v203\n\t"
"v_mfma_f32_16x16x32_bf16 v[16:19], a[108:111], a[160:163], v[16:19]\n\t"
"ds_read_b64_tr_b16 a[178:179], v249 offset:512\n\t"
"v_mfma_f32_16x16x32_bf16 v[32:35], a[112:115], a[144:147], v[32:35]\n\t"
"ds_read_b128 a[144:147], v216 offset:4096\n\t"
"v_mfma_f32_16x16x32_bf16 v[32:35], a[116:119], a[160:163], v[32:35]\n\t"
"buffer_load_dwordx4 a[68:71], v248, s[8:11], s72 offen offset:3072 sc1\n\t"
"v_mfma_f32_16x16x32_bf16 v[208:211], a[192:195], a[208:211], 0\n\t"
"v_cvt_pk_bf16_f32 v202, v204, v205\n\t"
"v_cvt_pk_bf16_f32 v203, v206, v207\n\t"
"v_mfma_f32_16x16x32_bf16 v[36:39], a[112:115], a[148:151], v[36:39]\n\t"
"ds_read_b128 a[160:163], v216 offset:12288\n\t"
"v_mfma_f32_16x16x32_bf16 v[36:39], a[116:119], a[164:167], v[36:39]\n\t"
"buffer_load_dwordx4 a[60:63], v248, s[8:11], s72 offen offset:1024 sc1\n\t"
"v_mfma_f32_16x16x32_bf16 v[20:23], a[104:107], a[148:151], v[20:23]\n\t"
"ds_read_b64_tr_b16 a[180:181], v250 offset:0\n\t"
"v_mfma_f32_16x16x32_bf16 v[20:23], a[108:111], a[164:167], v[20:23]\n\t"
"ds_write_b128 v219, v[200:203] offset:16384\n\t"
"v_mfma_f32_16x16x32_bf16 v[212:215], a[196:199], a[208:211], 0\n\t"
"v_cvt_pk_bf16_f32 v208, v208, v209\n\t"
"v_cvt_pk_bf16_f32 v209, v210, v211\n\t"
"v_mfma_f32_16x16x32_bf16 v[4:7], a[96:99], a[148:151], v[4:7]\n\t"
"ds_read_b128 a[148:151], v216 offset:5120\n\t"
"v_mfma_f32_16x16x32_bf16 v[4:7], a[100:103], a[164:167], v[4:7]\n\t"
"buffer_load_dwordx4 a[56:59], v248, s[8:11], s72 offen offset:0 sc1\n\t"
"v_mfma_f32_16x16x32_bf16 v[192:195], a[200:203], a[208:211], 0\n\t"
"ds_read_b128 a[164:167], v216 offset:13312\n\t"
"s_waitcnt lgkmcnt(5)\n\t"
"v_mfma_f32_16x16x32_bf16 v[8:11], a[96:99], a[152:155], v[8:11]\n\t"
"v_cvt_pk_bf16_f32 v210, v212, v213\n\t"
"v_cvt_pk_bf16_f32 v211, v214, v215\n\t"
"v_mfma_f32_16x16x32_bf16 v[8:11], a[100:103], a[168:171], v[8:11]\n\t"
"buffer_load_dwordx4 a[48:51], v235, s[8:11], s69 offen offset:2048 sc1\n\t"
"v_mfma_f32_16x16x32_bf16 v[24:27], a[104:107], a[152:155], v[24:27]\n\t"
"ds_read_b64_tr_b16 a[182:183], v250 offset:512\n\t"
"v_mfma_f32_16x16x32_bf16 v[24:27], a[108:111], a[168:171], v[24:27]\n\t"
"v_cvt_pk_bf16_f32 v192, v192, v193\n\t"
"v_cvt_pk_bf16_f32 v193, v194, v195\n\t"
"v_mfma_f32_16x16x32_bf16 v[196:199], a[204:207], a[208:211], 0\n\t"
"buffer_load_dwordx4 a[52:55], v235, s[8:11], s69 offen offset:3072 sc1\n\t"
"v_mfma_f32_16x16x32_bf16 v[40:43], a[112:115], a[152:155], v[40:43]\n\t"
"ds_read_b64_tr_b16 a[184:185], v251 offset:2304\n\t"
"v_mfma_f32_16x16x32_bf16 v[40:43], a[116:119], a[168:171], v[40:43]\n\t"
"ds_write_b128 v217, v[208:211] offset:24576\n\t"
"v_mfma_f32_16x16x32_bf16 v[44:47], a[112:115], a[156:159], v[44:47]\n\t"
"ds_read_b64_tr_b16 a[186:187], v251 offset:2816\n\t"
"v_mfma_f32_16x16x32_bf16 v[44:47], a[116:119], a[172:175], v[44:47]\n\t"
"buffer_load_dwordx4 a[88:91], v248, s[8:11], s74 offen offset:0 sc1\n\t"
"v_mfma_f32_16x16x32_bf16 v[28:31], a[104:107], a[156:159], v[28:31]\n\t"
"ds_read_b64_tr_b16 a[188:189], v252 offset:2304\n\t"
"v_cvt_pk_bf16_f32 v194, v196, v197\n\t"
"v_mfma_f32_16x16x32_bf16 v[28:31], a[108:111], a[172:175], v[28:31]\n\t"
"ds_read_b64_tr_b16 a[190:191], v252 offset:2816\n\t"
"v_cvt_pk_bf16_f32 v195, v198, v199\n\t"
"v_mfma_f32_16x16x32_bf16 v[12:15], a[96:99], a[156:159], v[12:15]\n\t"
"ds_read_b64_tr_b16 a[192:193], v251 offset:0\n\t"
"v_mfma_f32_16x16x32_bf16 v[12:15], a[100:103], a[172:175], v[12:15]\n\t"
"ds_write_b128 v219, v[192:195] offset:24576\n\t"
"s_waitcnt vmcnt(19) lgkmcnt(7)\n\t"
"v_mfma_f32_16x16x32_bf16 v[48:51], a[120:123], a[144:147], v[48:51]\n\t"
"ds_read_b64_tr_b16 a[194:195], v251 offset:512\n\t"
"v_mfma_f32_16x16x32_bf16 v[48:51], a[124:127], a[160:163], v[48:51]\n\t"
"ds_read_b64_tr_b16 a[196:197], v252 offset:0\n\t"
"ds_read_b64_tr_b16 a[198:199], v252 offset:512\n\t"
"ds_read_b64_tr_b16 a[200:201], v249 offset:2304\n\t"
"ds_read_b64_tr_b16 a[202:203], v249 offset:2816\n\t"
"ds_read_b64_tr_b16 a[204:205], v250 offset:2304\n\t"
"ds_read_b64_tr_b16 a[206:207], v250 offset:2816\n\t"
"buffer_load_dwordx4 a[92:95], v248, s[8:11], s74 offen offset:1024 sc1\n\t"
"v_mfma_f32_16x16x32_bf16 v[64:67], a[128:131], a[144:147], v[64:67]\n\t"
"ds_read_b128 a[152:155], v216 offset:6144\n\t"
"v_mfma_f32_16x16x32_bf16 v[64:67], a[132:135], a[160:163], v[64:67]\n\t"
"buffer_load_dwordx4 a[84:87], v235, s[8:11], s72 offen offset:3072 sc1\n\t"
"v_mfma_f32_16x16x32_bf16 v[80:83], a[136:139], a[144:147], v[80:83]\n\t"
"ds_read_b128 a[168:171], v216 offset:14336\n\t"
"v_mfma_f32_16x16x32_bf16 v[80:83], a[140:143], a[160:163], v[80:83]\n\t"
"ds_read_b128 a[156:159], v216 offset:7168\n\t"
"v_mfma_f32_16x16x32_bf16 v[84:87], a[136:139], a[148:151], v[84:87]\n\t"
"ds_read_b128 a[172:175], v216 offset:15360\n\t"
"v_mfma_f32_16x16x32_bf16 v[84:87], a[140:143], a[164:167], v[84:87]\n\t"
"buffer_load_dwordx4 a[80:83], v235, s[8:11], s72 offen offset:2048 sc1\n\t"
"v_mfma_f32_16x16x32_bf16 v[68:71], a[128:131], a[148:151], v[68:71]\n\t"
"v_mfma_f32_16x16x32_bf16 v[68:71], a[132:135], a[164:167], v[68:71]\n\t"
"buffer_load_dwordx4 a[72:75], v235, s[8:11], s72 offen offset:0 sc1\n\t"
"v_mfma_f32_16x16x32_bf16 v[52:55], a[120:123], a[148:151], v[52:55]\n\t"
"v_mfma_f32_16x16x32_bf16 v[52:55], a[124:127], a[164:167], v[52:55]\n\t"
"s_waitcnt lgkmcnt(0)\n\t"
"s_barrier\n\t"
"v_mfma_f32_16x16x32_bf16 v[56:59], a[120:123], a[152:155], v[56:59]\n\t"
"ds_read_b128 a[144:147], v216 offset:16384\n\t"
"v_mfma_f32_16x16x32_bf16 v[56:59], a[124:127], a[168:171], v[56:59]\n\t"
"ds_read_b128 a[160:163], v216 offset:24576\n\t"
"ds_read_b128 a[148:151], v216 offset:17408\n\t"
"v_mfma_f32_16x16x32_bf16 v[72:75], a[128:131], a[152:155], v[72:75]\n\t"
"ds_read_b128 a[164:167], v216 offset:25600\n\t"
"v_mfma_f32_16x16x32_bf16 v[72:75], a[132:135], a[168:171], v[72:75]\n\t"
"buffer_load_dwordx4 a[76:79], v235, s[8:11], s72 offen offset:1024 sc1\n\t"
"v_mfma_f32_16x16x32_bf16 v[88:91], a[136:139], a[152:155], v[88:91]\n\t"
"ds_read_b128 a[152:155], v216 offset:18432\n\t"
"v_mfma_f32_16x16x32_bf16 v[88:91], a[140:143], a[168:171], v[88:91]\n\t"
"buffer_load_dwordx4 a[112:115], v248, s[8:11], s82 offen offset:2048 sc1\n\t"
"v_mfma_f32_16x16x32_bf16 v[200:203], a[176:179], a[208:211], 0\n\t"
"buffer_load_dwordx4 a[116:119], v248, s[8:11], s82 offen offset:3072 sc1\n\t"
"v_mfma_f32_16x16x32_bf16 v[92:95], a[136:139], a[156:159], v[92:95]\n\t"
"ds_read_b128 a[168:171], v216 offset:26624\n\t"
"v_mfma_f32_16x16x32_bf16 v[92:95], a[140:143], a[172:175], v[92:95]\n\t"
"buffer_load_dwordx4 a[108:111], v248, s[8:11], s82 offen offset:1024 sc1\n\t"
"v_mfma_f32_16x16x32_bf16 v[204:207], a[180:183], a[208:211], 0\n\t"
"ds_read_b64_tr_b16 a[176:177], v249 offset:1152\n\t"
"v_mfma_f32_16x16x32_bf16 v[76:79], a[128:131], a[156:159], v[76:79]\n\t"
"v_cvt_pk_bf16_f32 v200, v200, v201\n\t"
"v_cvt_pk_bf16_f32 v201, v202, v203\n\t"
"v_mfma_f32_16x16x32_bf16 v[76:79], a[132:135], a[172:175], v[76:79]\n\t"
"ds_read_b64_tr_b16 a[178:179], v249 offset:1664\n\t"
"v_mfma_f32_16x16x32_bf16 v[60:63], a[120:123], a[156:159], v[60:63]\n\t"
"ds_read_b128 a[156:159], v216 offset:19456\n\t"
"v_mfma_f32_16x16x32_bf16 v[60:63], a[124:127], a[172:175], v[60:63]\n\t"
"buffer_load_dwordx4 a[104:107], v248, s[8:11], s82 offen offset:0 sc1\n\t"
"v_mfma_f32_16x16x32_bf16 v[208:211], a[184:187], a[208:211], 0\n\t"
"v_cvt_pk_bf16_f32 v202, v204, v205\n\t"
"v_cvt_pk_bf16_f32 v203, v206, v207\n\t"
"s_waitcnt vmcnt(22) lgkmcnt(4)\n\t"
"v_mfma_f32_16x16x32_bf16 v[96:99], a[0:3], a[144:147], v[96:99]\n\t"
"ds_read_b128 a[172:175], v216 offset:27648\n\t"
"v_mfma_f32_16x16x32_bf16 v[96:99], a[4:7], a[160:163], v[96:99]\n\t"
"buffer_load_dwordx4 a[96:99], v235, s[8:11], s81 offen offset:2048 sc1\n\t"
"v_mfma_f32_16x16x32_bf16 v[212:215], a[188:191], a[208:211], 0\n\t"
"ds_write_b128 v217, v[200:203] offset:0\n\t"
"v_mfma_f32_16x16x32_bf16 v[112:115], a[8:11], a[144:147], v[112:115]\n\t"
"v_cvt_pk_bf16_f32 v208, v208, v209\n\t"
"v_cvt_pk_bf16_f32 v209, v210, v211\n\t"
"v_mfma_f32_16x16x32_bf16 v[112:115], a[12:15], a[160:163], v[112:115]\n\t"
"buffer_load_dwordx4 a[100:103], v235, s[8:11], s81 offen offset:3072 sc1\n\t"
"v_mfma_f32_16x16x32_bf16 v[128:131], a[16:19], a[144:147], v[128:131]\n\t"
"ds_read_b128 a[144:147], v216 offset:20480\n\t"
"v_mfma_f32_16x16x32_bf16 v[128:131], a[20:23], a[160:163], v[128:131]\n\t"
"v_cvt_pk_bf16_f32 v210, v212, v213\n\t"
"v_cvt_pk_bf16_f32 v211, v214, v215\n\t"
"v_mfma_f32_16x16x32_bf16 v[192:195], a[192:195], a[208:211], 0\n\t"
"ds_read_b128 a[160:163], v216 offset:28672\n\t"
"v_mfma_f32_16x16x32_bf16 v[132:135], a[16:19], a[148:151], v[132:135]\n\t"
"ds_read_b64_tr_b16 a[180:181], v250 offset:1152\n\t"
"v_mfma_f32_16x16x32_bf16 v[132:135], a[20:23], a[164:167], v[132:135]\n\t"
"buffer_load_dwordx4 a[136:139], v248, s[8:11], s83 offen offset:0 sc1\n\t"
"v_mfma_f32_16x16x32_bf16 v[116:119], a[8:11], a[148:151], v[116:119]\n\t"
"ds_read_b64_tr_b16 a[182:183], v250 offset:1664\n\t"
"v_mfma_f32_16x16x32_bf16 v[116:119], a[12:15], a[164:167], v[116:119]\n\t"
"buffer_load_dwordx4 a[140:143], v248, s[8:11], s83 offen offset:1024 sc1\n\t"
"v_mfma_f32_16x16x32_bf16 v[196:199], a[196:199], a[208:211], 0\n\t"
"ds_write_b128 v219, v[208:211] offset:0\n\t"
"v_mfma_f32_16x16x32_bf16 v[100:103], a[0:3], a[148:151], v[100:103]\n\t"
"v_cvt_pk_bf16_f32 v192, v192, v193\n\t"
"v_cvt_pk_bf16_f32 v193, v194, v195\n\t"
"v_mfma_f32_16x16x32_bf16 v[100:103], a[4:7], a[164:167], v[100:103]\n\t"
"ds_read_b128 a[148:151], v216 offset:21504\n\t"
"v_mfma_f32_16x16x32_bf16 v[200:203], a[200:203], a[208:211], 0\n\t"
"buffer_load_dwordx4 a[132:135], v235, s[8:11], s82 offen offset:3072 sc1\n\t"
"s_waitcnt lgkmcnt(5)\n\t"
"v_mfma_f32_16x16x32_bf16 v[104:107], a[0:3], a[152:155], v[104:107]\n\t"
"ds_read_b128 a[164:167], v216 offset:29696\n\t"
"v_cvt_pk_bf16_f32 v194, v196, v197\n\t"
"v_mfma_f32_16x16x32_bf16 v[104:107], a[4:7], a[168:171], v[104:107]\n\t"
"v_cvt_pk_bf16_f32 v195, v198, v199\n\t"
"ds_read_b64_tr_b16 a[184:185], v251 offset:3456\n\t"
"v_mfma_f32_16x16x32_bf16 v[120:123], a[8:11], a[152:155], v[120:123]\n\t"
"ds_read_b64_tr_b16 a[186:187], v251 offset:3968\n\t"
"v_mfma_f32_16x16x32_bf16 v[120:123], a[12:15], a[168:171], v[120:123]\n\t"
"buffer_load_dwordx4 a[128:131], v235, s[8:11], s82 offen offset:2048 sc1\n\t"
"v_mfma_f32_16x16x32_bf16 v[204:207], a[204:207], a[208:211], 0\n\t"
"v_cvt_pk_bf16_f32 v200, v200, v201\n\t"
"v_cvt_pk_bf16_f32 v201, v202, v203\n\t"
"v_mfma_f32_16x16x32_bf16 v[136:139], a[16:19], a[152:155], v[136:139]\n\t"
"ds_read_b64_tr_b16 a[188:189], v252 offset:3456\n\t"
"v_mfma_f32_16x16x32_bf16 v[136:139], a[20:23], a[168:171], v[136:139]\n\t"
"buffer_load_dwordx4 a[120:123], v235, s[8:11], s82 offen offset:0 sc1\n\t"
"v_mfma_f32_16x16x32_bf16 v[140:143], a[16:19], a[156:159], v[140:143]\n\t"
"ds_read_b64_tr_b16 a[190:191], v252 offset:3968\n\t"
"v_mfma_f32_16x16x32_bf16 v[140:143], a[20:23], a[172:175], v[140:143]\n\t"
"ds_write_b128 v217, v[192:195] offset:8192\n\t"
"v_mfma_f32_16x16x32_bf16 v[124:127], a[8:11], a[156:159], v[124:127]\n\t"
"ds_read_b64_tr_b16 a[192:193], v251 offset:1152\n\t"
"v_cvt_pk_bf16_f32 v202, v204, v205\n\t"
"v_mfma_f32_16x16x32_bf16 v[124:127], a[12:15], a[172:175], v[124:127]\n\t"
"buffer_load_dwordx4 a[124:127], v235, s[8:11], s82 offen offset:1024 sc1\n\t"
"v_mfma_f32_16x16x32_bf16 v[108:111], a[0:3], a[156:159], v[108:111]\n\t"
"ds_read_b64_tr_b16 a[194:195], v251 offset:1664\n\t"
"v_cvt_pk_bf16_f32 v203, v206, v207\n\t"
"v_mfma_f32_16x16x32_bf16 v[108:111], a[4:7], a[172:175], v[108:111]\n\t"
"ds_write_b128 v219, v[200:203] offset:8192\n\t"
"s_waitcnt vmcnt(24) lgkmcnt(6)\n\t"
"v_mfma_f32_16x16x32_bf16 v[144:147], a[24:27], a[144:147], v[144:147]\n\t"
"ds_read_b64_tr_b16 a[196:197], v252 offset:1152\n\t"
"v_mfma_f32_16x16x32_bf16 v[144:147], a[28:31], a[160:163], v[144:147]\n\t"
"ds_read_b64_tr_b16 a[198:199], v252 offset:1664\n\t"
"ds_read_b64_tr_b16 a[200:201], v249 offset:3456\n\t"
"ds_read_b64_tr_b16 a[202:203], v249 offset:3968\n\t"
"ds_read_b64_tr_b16 a[204:205], v250 offset:3456\n\t"
"ds_read_b64_tr_b16 a[206:207], v250 offset:3968\n\t"
"v_mfma_f32_16x16x32_bf16 v[160:163], a[32:35], a[144:147], v[160:163]\n\t"
"ds_read_b128 a[152:155], v216 offset:22528\n\t"
"v_mfma_f32_16x16x32_bf16 v[160:163], a[36:39], a[160:163], v[160:163]\n\t"
"buffer_load_dwordx4 a[16:19], v248, s[8:11], s46 offen offset:2048 sc1\n\t"
"v_mfma_f32_16x16x32_bf16 v[176:179], a[40:43], a[144:147], v[176:179]\n\t"
"ds_read_b128 a[168:171], v216 offset:30720\n\t"
"v_mfma_f32_16x16x32_bf16 v[176:179], a[44:47], a[160:163], v[176:179]\n\t"
"ds_read_b128 a[156:159], v216 offset:23552\n\t"
"v_mfma_f32_16x16x32_bf16 v[180:183], a[40:43], a[148:151], v[180:183]\n\t"
"ds_read_b128 a[172:175], v216 offset:31744\n\t"
"v_mfma_f32_16x16x32_bf16 v[180:183], a[44:47], a[164:167], v[180:183]\n\t"
"buffer_load_dwordx4 a[20:23], v248, s[8:11], s46 offen offset:3072 sc1\n\t"
"v_mfma_f32_16x16x32_bf16 v[164:167], a[32:35], a[148:151], v[164:167]\n\t"
"v_mfma_f32_16x16x32_bf16 v[164:167], a[36:39], a[164:167], v[164:167]\n\t"
"buffer_load_dwordx4 a[12:15], v248, s[8:11], s46 offen offset:1024 sc1\n\t"
"v_mfma_f32_16x16x32_bf16 v[148:151], a[24:27], a[148:151], v[148:151]\n\t"
"v_mfma_f32_16x16x32_bf16 v[148:151], a[28:31], a[164:167], v[148:151]\n\t"
"buffer_load_dwordx4 a[8:11], v248, s[8:11], s46 offen offset:0 sc1\n\t"
"s_waitcnt lgkmcnt(0)\n\t"
"s_barrier\n\t"
"v_mfma_f32_16x16x32_bf16 v[152:155], a[24:27], a[152:155], v[152:155]\n\t"
"ds_read_b128 a[144:147], v216 offset:0\n\t"
"v_add_u32 v249, 80896, v224\n\t"
"v_mfma_f32_16x16x32_bf16 v[152:155], a[28:31], a[168:171], v[152:155]\n\t"
"ds_read_b128 a[160:163], v216 offset:8192\n\t"
"ds_read_b128 a[148:151], v216 offset:1024\n\t"
"v_mfma_f32_16x16x32_bf16 v[168:171], a[32:35], a[152:155], v[168:171]\n\t"
"ds_read_b128 a[164:167], v216 offset:9216\n\t"
"v_xor_b32 v250, 32, v249\n\t"
"v_mfma_f32_16x16x32_bf16 v[168:171], a[36:39], a[168:171], v[168:171]\n\t"
"buffer_load_dwordx4 a[0:3], v235, s[8:11], s42 offen offset:2048 sc1\n\t"
"v_mfma_f32_16x16x32_bf16 v[184:187], a[40:43], a[152:155], v[184:187]\n\t"
"v_xor_b32 v251, 64, v249\n\t"
"v_xor_b32 v252, 96, v249\n\t"
"v_mfma_f32_16x16x32_bf16 v[184:187], a[44:47], a[168:171], v[184:187]\n\t"
"ds_read_b128 a[152:155], v216 offset:2048\n\t"
"v_mfma_f32_16x16x32_bf16 v[208:211], a[176:179], a[208:211], 0\n\t"
"buffer_load_dwordx4 a[4:7], v235, s[8:11], s42 offen offset:3072 sc1\n\t"
"v_mfma_f32_16x16x32_bf16 v[188:191], a[40:43], a[156:159], v[188:191]\n\t"
"ds_read_b128 a[168:171], v216 offset:10240\n\t"
"v_mfma_f32_16x16x32_bf16 v[188:191], a[44:47], a[172:175], v[188:191]\n\t"
"buffer_load_dwordx4 a[40:43], v248, s[8:11], s47 offen offset:0 sc1\n\t"
"v_mfma_f32_16x16x32_bf16 v[212:215], a[180:183], a[208:211], 0\n\t"
"ds_read_b64_tr_b16 a[176:177], v249 offset:0\n\t"
"v_mfma_f32_16x16x32_bf16 v[172:175], a[32:35], a[156:159], v[172:175]\n\t"
"v_cvt_pk_bf16_f32 v208, v208, v209\n\t"
"v_cvt_pk_bf16_f32 v209, v210, v211\n\t"
"v_mfma_f32_16x16x32_bf16 v[172:175], a[36:39], a[172:175], v[172:175]\n\t"
"buffer_load_dwordx4 a[44:47], v248, s[8:11], s47 offen offset:1024 sc1\n\t"
"v_mfma_f32_16x16x32_bf16 v[156:159], a[24:27], a[156:159], v[156:159]\n\t"
"ds_read_b128 a[156:159], v216 offset:3072\n\t"
"v_mfma_f32_16x16x32_bf16 v[156:159], a[28:31], a[172:175], v[156:159]\n\t"
"v_cvt_pk_bf16_f32 v210, v212, v213\n\t"
"v_cvt_pk_bf16_f32 v211, v214, v215\n\t"
"v_mfma_f32_16x16x32_bf16 v[192:195], a[184:187], a[208:211], 0\n\t"
"buffer_load_dwordx4 a[36:39], v235, s[8:11], s46 offen offset:3072 sc1\n\t"
"s_waitcnt vmcnt(27) lgkmcnt(4)\n\t"
"v_mfma_f32_16x16x32_bf16 v[0:3], a[48:51], a[144:147], v[0:3]\n\t"
"ds_read_b128 a[172:175], v216 offset:11264\n\t"
"v_mfma_f32_16x16x32_bf16 v[0:3], a[52:55], a[160:163], v[0:3]\n\t"
"ds_write_b128 v217, v[208:211] offset:16384\n\t"
"v_mfma_f32_16x16x32_bf16 v[196:199], a[188:191], a[208:211], 0\n\t"
"buffer_load_dwordx4 a[32:35], v235, s[8:11], s46 offen offset:2048 sc1\n\t"
"v_mfma_f32_16x16x32_bf16 v[16:19], a[56:59], a[144:147], v[16:19]\n\t"
"v_cvt_pk_bf16_f32 v192, v192, v193\n\t"
"v_cvt_pk_bf16_f32 v193, v194, v195\n\t"
"v_mfma_f32_16x16x32_bf16 v[16:19], a[60:63], a[160:163], v[16:19]\n\t"
"ds_read_b64_tr_b16 a[178:179], v249 offset:512\n\t"
"v_mfma_f32_16x16x32_bf16 v[32:35], a[64:67], a[144:147], v[32:35]\n\t"
"ds_read_b128 a[144:147], v216 offset:4096\n\t"
"v_mfma_f32_16x16x32_bf16 v[32:35], a[68:71], a[160:163], v[32:35]\n\t"
"buffer_load_dwordx4 a[24:27], v235, s[8:11], s46 offen offset:0 sc1\n\t"
"v_mfma_f32_16x16x32_bf16 v[200:203], a[192:195], a[208:211], 0\n\t"
"v_cvt_pk_bf16_f32 v194, v196, v197\n\t"
"v_cvt_pk_bf16_f32 v195, v198, v199\n\t"
"v_mfma_f32_16x16x32_bf16 v[36:39], a[64:67], a[148:151], v[36:39]\n\t"
"ds_read_b128 a[160:163], v216 offset:12288\n\t"
"v_mfma_f32_16x16x32_bf16 v[36:39], a[68:71], a[164:167], v[36:39]\n\t"
"ds_write_b128 v219, v[192:195] offset:16384\n\t"
"v_mfma_f32_16x16x32_bf16 v[20:23], a[56:59], a[148:151], v[20:23]\n\t"
"ds_read_b64_tr_b16 a[180:181], v250 offset:0\n\t"
"v_mfma_f32_16x16x32_bf16 v[20:23], a[60:63], a[164:167], v[20:23]\n\t"
"buffer_load_dwordx4 a[28:31], v235, s[8:11], s46 offen offset:1024 sc1\n\t"
"v_mfma_f32_16x16x32_bf16 v[204:207], a[196:199], a[208:211], 0\n\t"
"v_cvt_pk_bf16_f32 v200, v200, v201\n\t"
"v_cvt_pk_bf16_f32 v201, v202, v203\n\t"
"v_mfma_f32_16x16x32_bf16 v[4:7], a[48:51], a[148:151], v[4:7]\n\t"
"ds_read_b128 a[148:151], v216 offset:5120\n\t"
"v_mfma_f32_16x16x32_bf16 v[4:7], a[52:55], a[164:167], v[4:7]\n\t"
"ds_read_b128 a[164:167], v216 offset:13312\n\t"
"v_mfma_f32_16x16x32_bf16 v[208:211], a[200:203], a[208:211], 0\n\t"
"ds_read_b64_tr_b16 a[182:183], v250 offset:512\n\t"
"s_waitcnt lgkmcnt(6)\n\t"
"v_mfma_f32_16x16x32_bf16 v[8:11], a[48:51], a[152:155], v[8:11]\n\t"
"v_cvt_pk_bf16_f32 v202, v204, v205\n\t"
"v_cvt_pk_bf16_f32 v203, v206, v207\n\t"
"v_mfma_f32_16x16x32_bf16 v[8:11], a[52:55], a[168:171], v[8:11]\n\t"
"ds_write_b128 v217, v[200:203] offset:24576\n\t"
"v_mfma_f32_16x16x32_bf16 v[24:27], a[56:59], a[152:155], v[24:27]\n\t"
"ds_read_b64_tr_b16 a[184:185], v251 offset:2304\n\t"
"v_mfma_f32_16x16x32_bf16 v[24:27], a[60:63], a[168:171], v[24:27]\n\t"
"v_cvt_pk_bf16_f32 v208, v208, v209\n\t"
"v_cvt_pk_bf16_f32 v209, v210, v211\n\t"
"v_mfma_f32_16x16x32_bf16 v[212:215], a[204:207], a[208:211], 0\n\t"
"ds_read_b64_tr_b16 a[186:187], v251 offset:2816\n\t"
"v_mfma_f32_16x16x32_bf16 v[40:43], a[64:67], a[152:155], v[40:43]\n\t"
"ds_read_b64_tr_b16 a[188:189], v252 offset:2304\n\t"
"v_mfma_f32_16x16x32_bf16 v[40:43], a[68:71], a[168:171], v[40:43]\n\t"
"ds_read_b64_tr_b16 a[190:191], v252 offset:2816\n\t"
"v_mfma_f32_16x16x32_bf16 v[44:47], a[64:67], a[156:159], v[44:47]\n\t"
"ds_read_b64_tr_b16 a[192:193], v251 offset:0\n\t"
"v_mfma_f32_16x16x32_bf16 v[44:47], a[68:71], a[172:175], v[44:47]\n\t"
"buffer_load_dwordx4 a[64:67], v248, s[8:11], s49 offen offset:2048 sc1\n\t"
"v_mfma_f32_16x16x32_bf16 v[28:31], a[56:59], a[156:159], v[28:31]\n\t"
"ds_read_b64_tr_b16 a[194:195], v251 offset:512\n\t"
"v_cvt_pk_bf16_f32 v210, v212, v213\n\t"
"v_mfma_f32_16x16x32_bf16 v[28:31], a[60:63], a[172:175], v[28:31]\n\t"
"buffer_load_dwordx4 a[68:71], v248, s[8:11], s49 offen offset:3072 sc1\n\t"
"v_mfma_f32_16x16x32_bf16 v[12:15], a[48:51], a[156:159], v[12:15]\n\t"
"ds_read_b64_tr_b16 a[196:197], v252 offset:0\n\t"
"v_cvt_pk_bf16_f32 v211, v214, v215\n\t"
"v_mfma_f32_16x16x32_bf16 v[12:15], a[52:55], a[172:175], v[12:15]\n\t"
"buffer_load_dwordx4 a[60:63], v248, s[8:11], s49 offen offset:1024 sc1\n\t"
"s_waitcnt vmcnt(27) lgkmcnt(6)\n\t"
"v_mfma_f32_16x16x32_bf16 v[48:51], a[72:75], a[144:147], v[48:51]\n\t"
"ds_read_b64_tr_b16 a[198:199], v252 offset:512\n\t"
"v_mfma_f32_16x16x32_bf16 v[48:51], a[76:79], a[160:163], v[48:51]\n\t"
"ds_write_b128 v219, v[208:211] offset:24576\n\t"
"ds_read_b64_tr_b16 a[200:201], v249 offset:2304\n\t"
"ds_read_b64_tr_b16 a[202:203], v249 offset:2816\n\t"
"ds_read_b64_tr_b16 a[204:205], v250 offset:2304\n\t"
"ds_read_b64_tr_b16 a[206:207], v250 offset:2816\n\t"
"buffer_load_dwordx4 a[56:59], v248, s[8:11], s49 offen offset:0 sc1\n\t"
"v_mfma_f32_16x16x32_bf16 v[64:67], a[80:83], a[144:147], v[64:67]\n\t"
"v_mfma_f32_16x16x32_bf16 v[64:67], a[84:87], a[160:163], v[64:67]\n\t"
"buffer_load_dwordx4 a[48:51], v235, s[8:11], s48 offen offset:2048 sc1\n\t"
"v_mfma_f32_16x16x32_bf16 v[80:83], a[88:91], a[144:147], v[80:83]\n\t"
"ds_read_b128 a[152:155], v216 offset:6144\n\t"
"v_mfma_f32_16x16x32_bf16 v[80:83], a[92:95], a[160:163], v[80:83]\n\t"
"buffer_load_dwordx4 a[52:55], v235, s[8:11], s48 offen offset:3072 sc1\n\t"
"v_mfma_f32_16x16x32_bf16 v[84:87], a[88:91], a[148:151], v[84:87]\n\t"
"ds_read_b128 a[168:171], v216 offset:14336\n\t"
"v_mfma_f32_16x16x32_bf16 v[84:87], a[92:95], a[164:167], v[84:87]\n\t"
"ds_read_b128 a[156:159], v216 offset:7168\n\t"
"ds_read_b128 a[172:175], v216 offset:15360\n\t"
"v_mfma_f32_16x16x32_bf16 v[68:71], a[80:83], a[148:151], v[68:71]\n\t"
"v_mfma_f32_16x16x32_bf16 v[68:71], a[84:87], a[164:167], v[68:71]\n\t"
"v_mfma_f32_16x16x32_bf16 v[52:55], a[72:75], a[148:151], v[52:55]\n\t"
"v_mfma_f32_16x16x32_bf16 v[52:55], a[76:79], a[164:167], v[52:55]\n\t"
"s_waitcnt lgkmcnt(0)\n\t"
"s_barrier\n\t"
"v_mfma_f32_16x16x32_bf16 v[56:59], a[72:75], a[152:155], v[56:59]\n\t"
"ds_read_b128 a[144:147], v216 offset:16384\n\t"
"v_mfma_f32_16x16x32_bf16 v[56:59], a[76:79], a[168:171], v[56:59]\n\t"
"ds_read_b128 a[160:163], v216 offset:24576\n\t"
"ds_read_b128 a[148:151], v216 offset:17408\n\t"
"v_mfma_f32_16x16x32_bf16 v[72:75], a[80:83], a[152:155], v[72:75]\n\t"
"ds_read_b128 a[164:167], v216 offset:25600\n\t"
"v_mfma_f32_16x16x32_bf16 v[72:75], a[84:87], a[168:171], v[72:75]\n\t"
"ds_read_b128 a[240:243], v222 offset:59904\n\t"
"v_mfma_f32_16x16x32_bf16 v[88:91], a[88:91], a[152:155], v[88:91]\n\t"
"ds_read_b128 a[244:247], v223 offset:18432\n\t"
"v_mfma_f32_16x16x32_bf16 v[88:91], a[92:95], a[168:171], v[88:91]\n\t"
"ds_read_b128 a[152:155], v216 offset:18432\n\t"
"v_mfma_f32_16x16x32_bf16 v[192:195], a[176:179], a[212:215], 0\n\t"
"ds_read_b128 a[168:171], v216 offset:26624\n\t"
"v_mfma_f32_16x16x32_bf16 v[92:95], a[88:91], a[156:159], v[92:95]\n\t"
"v_mfma_f32_16x16x32_bf16 v[92:95], a[92:95], a[172:175], v[92:95]\n\t"
"buffer_load_dwordx4 a[88:91], v248, s[8:11], s50 offen offset:0 sc1\n\t"
"v_mfma_f32_16x16x32_bf16 v[196:199], a[180:183], a[212:215], 0\n\t"
"buffer_load_dwordx4 a[92:95], v248, s[8:11], s50 offen offset:1024 sc1\n\t"
"v_mfma_f32_16x16x32_bf16 v[76:79], a[80:83], a[156:159], v[76:79]\n\t"
"s_nop 0\n\t"
"v_cvt_pk_bf16_f32 v192, v192, v193\n\t"
"v_cvt_pk_bf16_f32 v193, v194, v195\n\t"
"v_mfma_f32_16x16x32_bf16 v[76:79], a[84:87], a[172:175], v[76:79]\n\t"
"buffer_load_dwordx4 a[84:87], v235, s[8:11], s49 offen offset:3072 sc1\n\t"
"v_mfma_f32_16x16x32_bf16 v[60:63], a[72:75], a[156:159], v[60:63]\n\t"
"ds_read_b128 a[156:159], v216 offset:19456\n\t"
"v_mfma_f32_16x16x32_bf16 v[60:63], a[76:79], a[172:175], v[60:63]\n\t"
"buffer_load_dwordx4 a[80:83], v235, s[8:11], s49 offen offset:2048 sc1\n\t"
"v_mfma_f32_16x16x32_bf16 v[200:203], a[184:187], a[212:215], 0\n\t"
"buffer_load_dwordx4 a[72:75], v235, s[8:11], s49 offen offset:0 sc1\n\t"
"s_waitcnt vmcnt(29) lgkmcnt(3)\n\t"
"v_mfma_f32_16x16x32_bf16 v[96:99], a[96:99], a[144:147], v[96:99]\n\t"
"v_cvt_pk_bf16_f32 v194, v196, v197\n\t"
"v_cvt_pk_bf16_f32 v195, v198, v199\n\t"
"v_mfma_f32_16x16x32_bf16 v[96:99], a[100:103], a[160:163], v[96:99]\n\t"
"ds_read_b128 a[172:175], v216 offset:27648\n\t"
"v_mfma_f32_16x16x32_bf16 v[204:207], a[188:191], a[212:215], 0\n\t"
"buffer_load_dwordx4 a[76:79], v235, s[8:11], s49 offen offset:1024 sc1\n\t"
"v_mfma_f32_16x16x32_bf16 v[112:115], a[104:107], a[144:147], v[112:115]\n\t"
"v_cvt_pk_bf16_f32 v200, v200, v201\n\t"
"v_mfma_f32_16x16x32_bf16 v[112:115], a[108:111], a[160:163], v[112:115]\n\t"
"v_cvt_pk_bf16_f32 v201, v202, v203\n\t"
"v_mfma_f32_16x16x32_bf16 v[128:131], a[112:115], a[144:147], v[128:131]\n\t"
"ds_read_b128 a[144:147], v216 offset:20480\n\t"
"v_mfma_f32_16x16x32_bf16 v[128:131], a[116:119], a[160:163], v[128:131]\n\t"
"ds_write_b128 v217, v[192:195] offset:0\n\t"
"v_mfma_f32_16x16x32_bf16 v[208:211], a[192:195], a[212:215], 0\n\t"
"v_cvt_pk_bf16_f32 v202, v204, v205\n\t"
"v_mfma_f32_16x16x32_bf16 v[132:135], a[112:115], a[148:151], v[132:135]\n\t"
"v_cvt_pk_bf16_f32 v203, v206, v207\n\t"
"v_mfma_f32_16x16x32_bf16 v[132:135], a[116:119], a[164:167], v[132:135]\n\t"
"ds_read_b128 a[160:163], v216 offset:28672\n\t"
"v_mfma_f32_16x16x32_bf16 v[116:119], a[104:107], a[148:151], v[116:119]\n\t"
"v_mfma_f32_16x16x32_bf16 v[116:119], a[108:111], a[164:167], v[116:119]\n\t"
"ds_write_b128 v219, v[200:203] offset:0\n\t"
"v_mfma_f32_16x16x32_bf16 v[212:215], a[196:199], a[212:215], 0\n\t"
"v_cvt_pk_bf16_f32 v208, v208, v209\n\t"
"v_cvt_pk_bf16_f32 v209, v210, v211\n\t"
"v_mfma_f32_16x16x32_bf16 v[100:103], a[96:99], a[148:151], v[100:103]\n\t"
"ds_read_b128 a[148:151], v216 offset:21504\n\t"
"v_mfma_f32_16x16x32_bf16 v[100:103], a[100:103], a[164:167], v[100:103]\n\t"
"ds_read_b128 a[164:167], v216 offset:29696\n\t"
"v_mfma_f32_16x16x32_bf16 v[192:195], a[200:203], a[212:215], 0\n\t"
"ds_write_b128 v222, a[240:243] offset:41472\n\t"
"s_waitcnt lgkmcnt(6)\n\t"
"v_mfma_f32_16x16x32_bf16 v[104:107], a[96:99], a[152:155], v[104:107]\n\t"
"v_cvt_pk_bf16_f32 v210, v212, v213\n\t"
"v_cvt_pk_bf16_f32 v211, v214, v215\n\t"
"v_mfma_f32_16x16x32_bf16 v[104:107], a[100:103], a[168:171], v[104:107]\n\t"
"v_mfma_f32_16x16x32_bf16 v[120:123], a[104:107], a[152:155], v[120:123]\n\t"
"v_mfma_f32_16x16x32_bf16 v[120:123], a[108:111], a[168:171], v[120:123]\n\t"
"v_cvt_pk_bf16_f32 v192, v192, v193\n\t"
"v_cvt_pk_bf16_f32 v193, v194, v195\n\t"
"v_mfma_f32_16x16x32_bf16 v[196:199], a[204:207], a[212:215], 0\n\t"
"ds_write_b128 v217, v[208:211] offset:8192\n\t"
"v_mfma_f32_16x16x32_bf16 v[136:139], a[112:115], a[152:155], v[136:139]\n\t"
"ds_read_b128 a[152:155], v216 offset:22528\n\t"
"v_mfma_f32_16x16x32_bf16 v[136:139], a[116:119], a[168:171], v[136:139]\n\t"
"ds_read_b128 a[168:171], v216 offset:30720\n\t"
"v_mfma_f32_16x16x32_bf16 v[140:143], a[112:115], a[156:159], v[140:143]\n\t"
"v_mfma_f32_16x16x32_bf16 v[140:143], a[116:119], a[172:175], v[140:143]\n\t"
"buffer_load_dwordx4 a[112:115], v248, s[8:11], s58 offen offset:2048 sc1\n\t"
"v_mfma_f32_16x16x32_bf16 v[124:127], a[104:107], a[156:159], v[124:127]\n\t"
"v_cvt_pk_bf16_f32 v194, v196, v197\n\t"
"v_cvt_pk_bf16_f32 v195, v198, v199\n\t"
"v_mfma_f32_16x16x32_bf16 v[124:127], a[108:111], a[172:175], v[124:127]\n\t"
"buffer_load_dwordx4 a[116:119], v248, s[8:11], s58 offen offset:3072 sc1\n\t"
"v_mfma_f32_16x16x32_bf16 v[108:111], a[96:99], a[156:159], v[108:111]\n\t"
"ds_read_b128 a[156:159], v216 offset:23552\n\t"
"v_mfma_f32_16x16x32_bf16 v[108:111], a[100:103], a[172:175], v[108:111]\n\t"
"buffer_load_dwordx4 a[108:111], v248, s[8:11], s58 offen offset:1024 sc1\n\t"
"s_waitcnt vmcnt(27) lgkmcnt(4)\n\t"
"v_mfma_f32_16x16x32_bf16 v[144:147], a[120:123], a[144:147], v[144:147]\n\t"
"ds_read_b128 a[172:175], v216 offset:31744\n\t"
"v_mfma_f32_16x16x32_bf16 v[144:147], a[124:127], a[160:163], v[144:147]\n\t"
"ds_write_b128 v219, v[192:195] offset:8192\n\t"
"buffer_load_dwordx4 a[104:107], v248, s[8:11], s58 offen offset:0 sc1\n\t"
"v_mfma_f32_16x16x32_bf16 v[160:163], a[128:131], a[144:147], v[160:163]\n\t"
"v_mfma_f32_16x16x32_bf16 v[160:163], a[132:135], a[160:163], v[160:163]\n\t"
"buffer_load_dwordx4 a[96:99], v235, s[8:11], s57 offen offset:2048 sc1\n\t"
"buffer_load_dwordx4 a[100:103], v235, s[8:11], s57 offen offset:3072 sc1\n\t"
"v_pk_add_f32 v[0:1], v[0:1], v[96:97]\n\t"
"v_pk_add_f32 v[2:3], v[2:3], v[98:99]\n\t"
"v_pk_fma_f32 v[96:97], v[96:97], s[38:39], v[0:1]\n\t"
"v_pk_fma_f32 v[98:99], v[98:99], s[38:39], v[2:3]\n\t"
"v_pk_add_f32 v[16:17], v[16:17], v[112:113]\n\t"
"v_pk_add_f32 v[18:19], v[18:19], v[114:115]\n\t"
"v_pk_fma_f32 v[112:113], v[112:113], s[38:39], v[16:17]\n\t"
"v_pk_fma_f32 v[114:115], v[114:115], s[38:39], v[18:19]\n\t"
"v_pk_add_f32 v[32:33], v[32:33], v[128:129]\n\t"
"v_pk_add_f32 v[34:35], v[34:35], v[130:131]\n\t"
"v_pk_fma_f32 v[128:129], v[128:129], s[38:39], v[32:33]\n\t"
"v_pk_fma_f32 v[130:131], v[130:131], s[38:39], v[34:35]\n\t"
"v_pk_add_f32 v[36:37], v[36:37], v[132:133]\n\t"
"v_pk_add_f32 v[38:39], v[38:39], v[134:135]\n\t"
"v_pk_fma_f32 v[132:133], v[132:133], s[38:39], v[36:37]\n\t"
"v_pk_fma_f32 v[134:135], v[134:135], s[38:39], v[38:39]\n\t"
"v_pk_add_f32 v[20:21], v[20:21], v[116:117]\n\t"
"v_pk_add_f32 v[22:23], v[22:23], v[118:119]\n\t"
"v_pk_fma_f32 v[116:117], v[116:117], s[38:39], v[20:21]\n\t"
"v_pk_fma_f32 v[118:119], v[118:119], s[38:39], v[22:23]\n\t"
"v_pk_add_f32 v[4:5], v[4:5], v[100:101]\n\t"
"v_pk_add_f32 v[6:7], v[6:7], v[102:103]\n\t"
"v_pk_fma_f32 v[100:101], v[100:101], s[38:39], v[4:5]\n\t"
"v_pk_fma_f32 v[102:103], v[102:103], s[38:39], v[6:7]\n\t"
"v_pk_add_f32 v[8:9], v[8:9], v[104:105]\n\t"
"v_pk_add_f32 v[10:11], v[10:11], v[106:107]\n\t"
"v_pk_fma_f32 v[104:105], v[104:105], s[38:39], v[8:9]\n\t"
"v_pk_fma_f32 v[106:107], v[106:107], s[38:39], v[10:11]\n\t"
"v_pk_add_f32 v[24:25], v[24:25], v[120:121]\n\t"
"v_pk_add_f32 v[26:27], v[26:27], v[122:123]\n\t"
"v_pk_fma_f32 v[120:121], v[120:121], s[38:39], v[24:25]\n\t"
"v_pk_fma_f32 v[122:123], v[122:123], s[38:39], v[26:27]\n\t"
"v_pk_add_f32 v[40:41], v[40:41], v[136:137]\n\t"
"v_pk_add_f32 v[42:43], v[42:43], v[138:139]\n\t"
"v_pk_fma_f32 v[136:137], v[136:137], s[38:39], v[40:41]\n\t"
"v_pk_fma_f32 v[138:139], v[138:139], s[38:39], v[42:43]\n\t"
"v_pk_add_f32 v[44:45], v[44:45], v[140:141]\n\t"
"v_pk_add_f32 v[46:47], v[46:47], v[142:143]\n\t"
"v_pk_fma_f32 v[140:141], v[140:141], s[38:39], v[44:45]\n\t"
"v_pk_fma_f32 v[142:143], v[142:143], s[38:39], v[46:47]\n\t"
"v_pk_add_f32 v[28:29], v[28:29], v[124:125]\n\t"
"v_pk_add_f32 v[30:31], v[30:31], v[126:127]\n\t"
"v_pk_fma_f32 v[124:125], v[124:125], s[38:39], v[28:29]\n\t"
"v_pk_fma_f32 v[126:127], v[126:127], s[38:39], v[30:31]\n\t"
"v_pk_add_f32 v[12:13], v[12:13], v[108:109]\n\t"
"v_pk_add_f32 v[14:15], v[14:15], v[110:111]\n\t"
"v_pk_fma_f32 v[108:109], v[108:109], s[38:39], v[12:13]\n\t"
"v_pk_fma_f32 v[110:111], v[110:111], s[38:39], v[14:15]\n\t"
"v_mfma_f32_16x16x32_bf16 v[176:179], a[136:139], a[144:147], v[176:179]\n\t"
"v_mfma_f32_16x16x32_bf16 v[176:179], a[140:143], a[160:163], v[176:179]\n\t"
"v_mfma_f32_16x16x32_bf16 v[180:183], a[136:139], a[148:151], v[180:183]\n\t"
"v_mfma_f32_16x16x32_bf16 v[180:183], a[140:143], a[164:167], v[180:183]\n\t"
"ds_write_b128 v223, a[244:247] offset:0\n\t"
"v_mfma_f32_16x16x32_bf16 v[164:167], a[128:131], a[148:151], v[164:167]\n\t"
"v_mfma_f32_16x16x32_bf16 v[164:167], a[132:135], a[164:167], v[164:167]\n\t"
"v_mfma_f32_16x16x32_bf16 v[148:151], a[120:123], a[148:151], v[148:151]\n\t"
"v_mfma_f32_16x16x32_bf16 v[148:151], a[124:127], a[164:167], v[148:151]\n\t"
"s_waitcnt lgkmcnt(0)\n\t"
"s_barrier\n\t"
"v_mfma_f32_16x16x32_bf16 v[152:155], a[120:123], a[152:155], v[152:155]\n\t"
"ds_read_b128 a[144:147], v216 offset:0\n\t"
"v_mfma_f32_16x16x32_bf16 v[152:155], a[124:127], a[168:171], v[152:155]\n\t"
"s_add_u32 m0, s26, 83200\n\t"
"ds_read_b128 a[160:163], v216 offset:8192\n\t"
"ds_read_b128 a[148:151], v216 offset:1024\n\t"
"buffer_load_dwordx4 v236, s[12:15], s22 offen offset:0 nt lds\n\t"
"v_mfma_f32_16x16x32_bf16 v[168:171], a[128:131], a[152:155], v[168:171]\n\t"
"ds_read_b128 a[164:167], v216 offset:9216\n\t"
"v_mfma_f32_16x16x32_bf16 v[168:171], a[132:135], a[168:171], v[168:171]\n\t"
"v_mfma_f32_16x16x32_bf16 v[184:187], a[136:139], a[152:155], v[184:187]\n\t"
"ds_read_b128 a[152:155], v216 offset:2048\n\t"
"v_mfma_f32_16x16x32_bf16 v[184:187], a[140:143], a[168:171], v[184:187]\n\t"
"ds_read_b128 a[168:171], v216 offset:10240\n\t"
"v_mfma_f32_16x16x32_bf16 v[200:203], a[176:179], a[216:219], 0\n\t"
"buffer_load_dwordx4 v237, s[12:15], s22 offen offset:1024 nt lds\n\t"
"v_mfma_f32_16x16x32_bf16 v[188:191], a[136:139], a[156:159], v[188:191]\n\t"
"ds_read_b64_tr_b16 a[176:177], v249 offset:41472\n\t"
"v_mfma_f32_16x16x32_bf16 v[188:191], a[140:143], a[172:175], v[188:191]\n\t"
"ds_read_b64_tr_b16 a[178:179], v249 offset:41984\n\t"
"v_mfma_f32_16x16x32_bf16 v[204:207], a[180:183], a[216:219], 0\n\t"
"ds_read_b64_tr_b16 a[180:181], v250 offset:41472\n\t"
"v_mfma_f32_16x16x32_bf16 v[172:175], a[128:131], a[156:159], v[172:175]\n\t"
"v_cvt_pk_bf16_f32 v200, v200, v201\n\t"
"v_cvt_pk_bf16_f32 v201, v202, v203\n\t"
"v_mfma_f32_16x16x32_bf16 v[172:175], a[132:135], a[172:175], v[172:175]\n\t"
"buffer_load_dwordx4 v238, s[12:15], s22 offen offset:2048 nt lds\n\t"
"v_mfma_f32_16x16x32_bf16 v[156:159], a[120:123], a[156:159], v[156:159]\n\t"
"ds_read_b128 a[156:159], v216 offset:3072\n\t"
"v_mfma_f32_16x16x32_bf16 v[156:159], a[124:127], a[172:175], v[156:159]\n\t"
"v_cvt_pk_bf16_f32 v202, v204, v205\n\t"
"v_cvt_pk_bf16_f32 v203, v206, v207\n\t"
"v_mfma_f32_16x16x32_bf16 v[208:211], a[184:187], a[216:219], 0\n\t"
"ds_read_b128 a[172:175], v216 offset:11264\n\t"
"s_waitcnt vmcnt(27) lgkmcnt(6)\n\t"
"v_mfma_f32_16x16x32_bf16 v[0:3], a[0:3], a[144:147], v[0:3]\n\t"
"ds_read_b64_tr_b16 a[182:183], v250 offset:41984\n\t"
"v_mfma_f32_16x16x32_bf16 v[0:3], a[4:7], a[160:163], v[0:3]\n\t"
"ds_write_b128 v217, v[200:203] offset:16384\n\t"
"v_mfma_f32_16x16x32_bf16 v[212:215], a[188:191], a[216:219], 0\n\t"
"buffer_load_dwordx4 v239, s[12:15], s22 offen offset:3072 nt lds\n\t"
"v_pk_add_f32 v[48:49], v[48:49], v[144:145]\n\t"
"v_pk_add_f32 v[50:51], v[50:51], v[146:147]\n\t"
"v_pk_fma_f32 v[144:145], v[144:145], s[38:39], v[48:49]\n\t"
"v_pk_fma_f32 v[146:147], v[146:147], s[38:39], v[50:51]\n\t"
"v_pk_add_f32 v[64:65], v[64:65], v[160:161]\n\t"
"v_pk_add_f32 v[66:67], v[66:67], v[162:163]\n\t"
"v_pk_fma_f32 v[160:161], v[160:161], s[38:39], v[64:65]\n\t"
"v_pk_fma_f32 v[162:163], v[162:163], s[38:39], v[66:67]\n\t"
"v_pk_add_f32 v[80:81], v[80:81], v[176:177]\n\t"
"v_pk_add_f32 v[82:83], v[82:83], v[178:179]\n\t"
"v_pk_fma_f32 v[176:177], v[176:177], s[38:39], v[80:81]\n\t"
"v_pk_fma_f32 v[178:179], v[178:179], s[38:39], v[82:83]\n\t"
"v_pk_add_f32 v[84:85], v[84:85], v[180:181]\n\t"
"v_pk_add_f32 v[86:87], v[86:87], v[182:183]\n\t"
"v_pk_fma_f32 v[180:181], v[180:181], s[38:39], v[84:85]\n\t"
"v_pk_fma_f32 v[182:183], v[182:183], s[38:39], v[86:87]\n\t"
"v_pk_add_f32 v[68:69], v[68:69], v[164:165]\n\t"
"v_pk_add_f32 v[70:71], v[70:71], v[166:167]\n\t"
"v_pk_fma_f32 v[164:165], v[164:165], s[38:39], v[68:69]\n\t"
"v_pk_fma_f32 v[166:167], v[166:167], s[38:39], v[70:71]\n\t"
"v_pk_add_f32 v[52:53], v[52:53], v[148:149]\n\t"
"v_pk_add_f32 v[54:55], v[54:55], v[150:151]\n\t"
"v_pk_fma_f32 v[148:149], v[148:149], s[38:39], v[52:53]\n\t"
"v_pk_fma_f32 v[150:151], v[150:151], s[38:39], v[54:55]\n\t"
"v_pk_add_f32 v[56:57], v[56:57], v[152:153]\n\t"
"v_pk_add_f32 v[58:59], v[58:59], v[154:155]\n\t"
"v_pk_fma_f32 v[152:153], v[152:153], s[38:39], v[56:57]\n\t"
"v_pk_fma_f32 v[154:155], v[154:155], s[38:39], v[58:59]\n\t"
"v_pk_add_f32 v[72:73], v[72:73], v[168:169]\n\t"
"v_pk_add_f32 v[74:75], v[74:75], v[170:171]\n\t"
"v_pk_fma_f32 v[168:169], v[168:169], s[38:39], v[72:73]\n\t"
"v_pk_fma_f32 v[170:171], v[170:171], s[38:39], v[74:75]\n\t"
"v_pk_add_f32 v[88:89], v[88:89], v[184:185]\n\t"
"v_pk_add_f32 v[90:91], v[90:91], v[186:187]\n\t"
"v_pk_fma_f32 v[184:185], v[184:185], s[38:39], v[88:89]\n\t"
"v_pk_fma_f32 v[186:187], v[186:187], s[38:39], v[90:91]\n\t"
"v_pk_add_f32 v[92:93], v[92:93], v[188:189]\n\t"
"v_pk_add_f32 v[94:95], v[94:95], v[190:191]\n\t"
"v_pk_fma_f32 v[188:189], v[188:189], s[38:39], v[92:93]\n\t"
"v_pk_fma_f32 v[190:191], v[190:191], s[38:39], v[94:95]\n\t"
"v_pk_add_f32 v[76:77], v[76:77], v[172:173]\n\t"
"v_pk_add_f32 v[78:79], v[78:79], v[174:175]\n\t"
"v_pk_fma_f32 v[172:173], v[172:173], s[38:39], v[76:77]\n\t"
"v_pk_fma_f32 v[174:175], v[174:175], s[38:39], v[78:79]\n\t"
"v_pk_add_f32 v[60:61], v[60:61], v[156:157]\n\t"
"v_pk_add_f32 v[62:63], v[62:63], v[158:159]\n\t"
"v_pk_fma_f32 v[156:157], v[156:157], s[38:39], v[60:61]\n\t"
"v_pk_fma_f32 v[158:159], v[158:159], s[38:39], v[62:63]\n\t"
"v_mfma_f32_16x16x32_bf16 v[16:19], a[8:11], a[144:147], v[16:19]\n\t"
"v_cvt_pk_bf16_f32 v208, v208, v209\n\t"
"v_cvt_pk_bf16_f32 v209, v210, v211\n\t"
"v_mfma_f32_16x16x32_bf16 v[16:19], a[12:15], a[160:163], v[16:19]\n\t"
"ds_read_b64_tr_b16 a[184:185], v251 offset:43776\n\t"
"v_mfma_f32_16x16x32_bf16 v[32:35], a[16:19], a[144:147], v[32:35]\n\t"
"ds_read_b128 a[144:147], v216 offset:4096\n\t"
"v_mfma_f32_16x16x32_bf16 v[32:35], a[20:23], a[160:163], v[32:35]\n\t"
"v_cvt_pk_bf16_f32 v210, v212, v213\n\t"
"v_cvt_pk_bf16_f32 v211, v214, v215\n\t"
"v_mfma_f32_16x16x32_bf16 v[192:195], a[192:195], a[216:219], 0\n\t"
"s_mov_b64 exec, s[36:37]\n\t"
"buffer_load_dwordx4 v240, s[12:15], s22 offen offset:3584 nt lds\n\t"
"s_mov_b64 exec, -1\n\t"
"s_add_u32 m0, s26, 103936\n\t"
"v_mfma_f32_16x16x32_bf16 v[36:39], a[16:19], a[148:151], v[36:39]\n\t"
"ds_read_b128 a[160:163], v216 offset:12288\n\t"
"v_mfma_f32_16x16x32_bf16 v[36:39], a[20:23], a[164:167], v[36:39]\n\t"
"ds_write_b128 v219, v[208:211] offset:16384\n\t"
"v_mfma_f32_16x16x32_bf16 v[20:23], a[8:11], a[148:151], v[20:23]\n\t"
"ds_read_b64_tr_b16 a[186:187], v251 offset:44288\n\t"
"v_mfma_f32_16x16x32_bf16 v[20:23], a[12:15], a[164:167], v[20:23]\n\t"
"v_cvt_pk_bf16_f32 v192, v192, v193\n\t"
"v_cvt_pk_bf16_f32 v193, v194, v195\n\t"
"v_mfma_f32_16x16x32_bf16 v[196:199], a[196:199], a[216:219], 0\n\t"
"ds_read_b64_tr_b16 a[188:189], v252 offset:43776\n\t"
"v_mfma_f32_16x16x32_bf16 v[4:7], a[0:3], a[148:151], v[4:7]\n\t"
"ds_read_b128 a[148:151], v216 offset:5120\n\t"
"v_mfma_f32_16x16x32_bf16 v[4:7], a[4:7], a[164:167], v[4:7]\n\t"
"buffer_load_dwordx4 v241, s[12:15], s22 offen offset:0 nt lds\n\t"
"v_mfma_f32_16x16x32_bf16 v[200:203], a[200:203], a[216:219], 0\n\t"
"ds_read_b128 a[164:167], v216 offset:13312\n\t"
"s_waitcnt lgkmcnt(6)\n\t"
"v_mfma_f32_16x16x32_bf16 v[8:11], a[0:3], a[152:155], v[8:11]\n\t"
"v_cvt_pk_bf16_f32 v194, v196, v197\n\t"
"v_cvt_pk_bf16_f32 v195, v198, v199\n\t"
"v_mfma_f32_16x16x32_bf16 v[8:11], a[4:7], a[168:171], v[8:11]\n\t"
"ds_write_b128 v217, v[192:195] offset:24576\n\t"
"v_mfma_f32_16x16x32_bf16 v[24:27], a[8:11], a[152:155], v[24:27]\n\t"
"ds_read_b64_tr_b16 a[190:191], v252 offset:44288\n\t"
"v_mfma_f32_16x16x32_bf16 v[24:27], a[12:15], a[168:171], v[24:27]\n\t"
"buffer_load_dwordx4 v242, s[12:15], s22 offen offset:1024 nt lds\n\t"
"v_mfma_f32_16x16x32_bf16 v[204:207], a[204:207], a[216:219], 0\n\t"
"v_cvt_pk_bf16_f32 v200, v200, v201\n\t"
"v_mfma_f32_16x16x32_bf16 v[40:43], a[16:19], a[152:155], v[40:43]\n\t"
"v_cvt_pk_bf16_f32 v201, v202, v203\n\t"
"ds_read_b64_tr_b16 a[192:193], v251 offset:41472\n\t"
"v_mfma_f32_16x16x32_bf16 v[40:43], a[20:23], a[168:171], v[40:43]\n\t"
"ds_read_b64_tr_b16 a[194:195], v251 offset:41984\n\t"
"v_mfma_f32_16x16x32_bf16 v[44:47], a[16:19], a[156:159], v[44:47]\n\t"
"ds_read_b64_tr_b16 a[196:197], v252 offset:41472\n\t"
"v_mfma_f32_16x16x32_bf16 v[44:47], a[20:23], a[172:175], v[44:47]\n\t"
"buffer_load_dwordx4 v243, s[12:15], s22 offen offset:2048 nt lds\n\t"
"v_mfma_f32_16x16x32_bf16 v[28:31], a[8:11], a[156:159], v[28:31]\n\t"
"ds_read_b64_tr_b16 a[198:199], v252 offset:41984\n\t"
"v_mfma_f32_16x16x32_bf16 v[28:31], a[12:15], a[172:175], v[28:31]\n\t"
"ds_read_b64_tr_b16 a[200:201], v249 offset:43776\n\t"
"v_cvt_pk_bf16_f32 v202, v204, v205\n\t"
"v_mfma_f32_16x16x32_bf16 v[12:15], a[0:3], a[156:159], v[12:15]\n\t"
"ds_read_b64_tr_b16 a[202:203], v249 offset:44288\n\t"
"v_mfma_f32_16x16x32_bf16 v[12:15], a[4:7], a[172:175], v[12:15]\n\t"
"ds_read_b64_tr_b16 a[204:205], v250 offset:43776\n\t"
"v_cvt_pk_bf16_f32 v203, v206, v207\n\t"
"s_waitcnt vmcnt(26) lgkmcnt(7)\n\t"
"v_mfma_f32_16x16x32_bf16 v[48:51], a[24:27], a[144:147], v[48:51]\n\t"
"ds_read_b64_tr_b16 a[206:207], v250 offset:44288\n\t"
"v_mfma_f32_16x16x32_bf16 v[48:51], a[28:31], a[160:163], v[48:51]\n\t"
"ds_write_b128 v219, v[200:203] offset:24576\n\t"
"buffer_load_dwordx4 v244, s[12:15], s22 offen offset:3072 nt lds\n\t"
"v_mfma_f32_16x16x32_bf16 v[64:67], a[32:35], a[144:147], v[64:67]\n\t"
"v_mfma_f32_16x16x32_bf16 v[64:67], a[36:39], a[160:163], v[64:67]\n\t"
"v_mfma_f32_16x16x32_bf16 v[80:83], a[40:43], a[144:147], v[80:83]\n\t"
"ds_read_b128 a[152:155], v216 offset:6144\n\t"
"v_mfma_f32_16x16x32_bf16 v[80:83], a[44:47], a[160:163], v[80:83]\n\t"
"s_mov_b64 exec, s[36:37]\n\t"
"buffer_load_dwordx4 v245, s[12:15], s22 offen offset:3584 nt lds\n\t"
"s_mov_b64 exec, -1\n\t"
"v_mfma_f32_16x16x32_bf16 v[84:87], a[40:43], a[148:151], v[84:87]\n\t"
"ds_read_b128 a[168:171], v216 offset:14336\n\t"
"v_mfma_f32_16x16x32_bf16 v[84:87], a[44:47], a[164:167], v[84:87]\n\t"
"ds_read_b128 a[156:159], v216 offset:7168\n\t"
"ds_read_b128 a[172:175], v216 offset:15360\n\t"
"v_mfma_f32_16x16x32_bf16 v[68:71], a[32:35], a[148:151], v[68:71]\n\t"
"v_mfma_f32_16x16x32_bf16 v[68:71], a[36:39], a[164:167], v[68:71]\n\t"
"v_mfma_f32_16x16x32_bf16 v[52:55], a[24:27], a[148:151], v[52:55]\n\t"
"v_mfma_f32_16x16x32_bf16 v[52:55], a[28:31], a[164:167], v[52:55]\n\t"
"s_waitcnt lgkmcnt(0)\n\t"
"s_barrier\n\t"
"v_mfma_f32_16x16x32_bf16 v[56:59], a[24:27], a[152:155], v[56:59]\n\t"
"ds_read_b128 a[144:147], v216 offset:16384\n\t"
"v_mfma_f32_16x16x32_bf16 v[56:59], a[28:31], a[168:171], v[56:59]\n\t"
"ds_read_b128 a[160:163], v216 offset:24576\n\t"
"ds_read_b128 a[148:151], v216 offset:17408\n\t"
"v_mfma_f32_16x16x32_bf16 v[72:75], a[32:35], a[152:155], v[72:75]\n\t"
"ds_read_b128 a[164:167], v216 offset:25600\n\t"
"v_mfma_f32_16x16x32_bf16 v[72:75], a[36:39], a[168:171], v[72:75]\n\t"
"v_mfma_f32_16x16x32_bf16 v[88:91], a[40:43], a[152:155], v[88:91]\n\t"
"ds_read_b128 a[152:155], v216 offset:18432\n\t"
"v_mfma_f32_16x16x32_bf16 v[88:91], a[44:47], a[168:171], v[88:91]\n\t"
"ds_read_b128 a[168:171], v216 offset:26624\n\t"
"v_mfma_f32_16x16x32_bf16 v[208:211], a[176:179], a[212:215], 0\n\t"
"v_mfma_f32_16x16x32_bf16 v[92:95], a[40:43], a[156:159], v[92:95]\n\t"
"v_mfma_f32_16x16x32_bf16 v[92:95], a[44:47], a[172:175], v[92:95]\n\t"
"v_mfma_f32_16x16x32_bf16 v[212:215], a[180:183], a[212:215], 0\n\t"
"v_mfma_f32_16x16x32_bf16 v[76:79], a[32:35], a[156:159], v[76:79]\n\t"
"s_nop 3\n\t"
"v_cvt_pk_bf16_f32 v208, v208, v209\n\t"
"v_cvt_pk_bf16_f32 v209, v210, v211\n\t"
"v_mfma_f32_16x16x32_bf16 v[76:79], a[36:39], a[172:175], v[76:79]\n\t"
"v_mfma_f32_16x16x32_bf16 v[60:63], a[24:27], a[156:159], v[60:63]\n\t"
"ds_read_b128 a[156:159], v216 offset:19456\n\t"
"v_mfma_f32_16x16x32_bf16 v[60:63], a[28:31], a[172:175], v[60:63]\n\t"
"buffer_load_dwordx4 a[136:139], v248, s[8:11], s59 offen offset:0 sc1\n\t"
"v_mfma_f32_16x16x32_bf16 v[192:195], a[184:187], a[212:215], 0\n\t"
"v_cvt_pk_bf16_f32 v210, v212, v213\n\t"
"v_cvt_pk_bf16_f32 v211, v214, v215\n\t"
"s_waitcnt vmcnt(23) lgkmcnt(2)\n\t"
"v_mfma_f32_16x16x32_bf16 v[96:99], a[48:51], a[144:147], v[96:99]\n\t"
"ds_read_b128 a[172:175], v216 offset:27648\n\t"
"v_mfma_f32_16x16x32_bf16 v[96:99], a[52:55], a[160:163], v[96:99]\n\t"
"ds_write_b128 v217, v[208:211] offset:0\n\t"
"v_mfma_f32_16x16x32_bf16 v[196:199], a[188:191], a[212:215], 0\n\t"
"v_mfma_f32_16x16x32_bf16 v[112:115], a[56:59], a[144:147], v[112:115]\n\t"
"v_cvt_pk_bf16_f32 v192, v192, v193\n\t"
"v_cvt_pk_bf16_f32 v193, v194, v195\n\t"
"v_mfma_f32_16x16x32_bf16 v[112:115], a[60:63], a[160:163], v[112:115]\n\t"
"v_mfma_f32_16x16x32_bf16 v[128:131], a[64:67], a[144:147], v[128:131]\n\t"
"ds_read_b128 a[144:147], v216 offset:20480\n\t"
"v_mfma_f32_16x16x32_bf16 v[128:131], a[68:71], a[160:163], v[128:131]\n\t"
"s_nop 0\n\t"
"v_cvt_pk_bf16_f32 v194, v196, v197\n\t"
"v_cvt_pk_bf16_f32 v195, v198, v199\n\t"
"v_mfma_f32_16x16x32_bf16 v[200:203], a[192:195], a[212:215], 0\n\t"
"ds_read_b128 a[160:163], v216 offset:28672\n\t"
"v_mfma_f32_16x16x32_bf16 v[132:135], a[64:67], a[148:151], v[132:135]\n\t"
"v_mfma_f32_16x16x32_bf16 v[132:135], a[68:71], a[164:167], v[132:135]\n\t"
"ds_write_b128 v219, v[192:195] offset:0\n\t"
"v_mfma_f32_16x16x32_bf16 v[116:119], a[56:59], a[148:151], v[116:119]\n\t"
"v_mfma_f32_16x16x32_bf16 v[116:119], a[60:63], a[164:167], v[116:119]\n\t"
"s_nop 1\n\t"
"v_cvt_pk_bf16_f32 v200, v200, v201\n\t"
"v_cvt_pk_bf16_f32 v201, v202, v203\n\t"
"v_mfma_f32_16x16x32_bf16 v[204:207], a[196:199], a[212:215], 0\n\t"
"v_mfma_f32_16x16x32_bf16 v[100:103], a[48:51], a[148:151], v[100:103]\n\t"
"ds_read_b128 a[148:151], v216 offset:21504\n\t"
"v_mfma_f32_16x16x32_bf16 v[100:103], a[52:55], a[164:167], v[100:103]\n\t"
"ds_read_b128 a[164:167], v216 offset:29696\n\t"
"v_mfma_f32_16x16x32_bf16 v[208:211], a[200:203], a[212:215], 0\n\t"
"s_waitcnt lgkmcnt(4)\n\t"
"v_mfma_f32_16x16x32_bf16 v[104:107], a[48:51], a[152:155], v[104:107]\n\t"
"s_nop 0\n\t"
"v_cvt_pk_bf16_f32 v202, v204, v205\n\t"
"v_cvt_pk_bf16_f32 v203, v206, v207\n\t"
"v_mfma_f32_16x16x32_bf16 v[104:107], a[52:55], a[168:171], v[104:107]\n\t"
"ds_write_b128 v217, v[200:203] offset:8192\n\t"
"v_mfma_f32_16x16x32_bf16 v[120:123], a[56:59], a[152:155], v[120:123]\n\t"
"v_mfma_f32_16x16x32_bf16 v[120:123], a[60:63], a[168:171], v[120:123]\n\t"
"v_cvt_pk_bf16_f32 v208, v208, v209\n\t"
"v_cvt_pk_bf16_f32 v209, v210, v211\n\t"
"v_mfma_f32_16x16x32_bf16 v[212:215], a[204:207], a[212:215], 0\n\t"
"v_mfma_f32_16x16x32_bf16 v[136:139], a[64:67], a[152:155], v[136:139]\n\t"
"ds_read_b128 a[152:155], v216 offset:22528\n\t"
"v_mfma_f32_16x16x32_bf16 v[136:139], a[68:71], a[168:171], v[136:139]\n\t"
"ds_read_b128 a[168:171], v216 offset:30720\n\t"
"v_mfma_f32_16x16x32_bf16 v[140:143], a[64:67], a[156:159], v[140:143]\n\t"
"v_mfma_f32_16x16x32_bf16 v[140:143], a[68:71], a[172:175], v[140:143]\n\t"
"s_nop 1\n\t"
"v_cvt_pk_bf16_f32 v210, v212, v213\n\t"
"v_cvt_pk_bf16_f32 v211, v214, v215\n\t"
"v_mfma_f32_16x16x32_bf16 v[124:127], a[56:59], a[156:159], v[124:127]\n\t"
"v_mfma_f32_16x16x32_bf16 v[124:127], a[60:63], a[172:175], v[124:127]\n\t"
"ds_write_b128 v219, v[208:211] offset:8192\n\t"
"v_mfma_f32_16x16x32_bf16 v[108:111], a[48:51], a[156:159], v[108:111]\n\t"
"ds_read_b128 a[156:159], v216 offset:23552\n\t"
"v_mfma_f32_16x16x32_bf16 v[108:111], a[52:55], a[172:175], v[108:111]\n\t"
"ds_read_b128 a[172:175], v216 offset:31744\n\t"
"s_waitcnt vmcnt(17) lgkmcnt(5)\n\t"
"v_mfma_f32_16x16x32_bf16 v[144:147], a[72:75], a[144:147], v[144:147]\n\t"
"v_mfma_f32_16x16x32_bf16 v[144:147], a[76:79], a[160:163], v[144:147]\n\t"
"buffer_load_dwordx4 a[140:143], v248, s[8:11], s59 offen offset:1024 sc1\n\t"
"v_mfma_f32_16x16x32_bf16 v[160:163], a[80:83], a[144:147], v[160:163]\n\t"
"v_mfma_f32_16x16x32_bf16 v[160:163], a[84:87], a[160:163], v[160:163]\n\t"
"v_mfma_f32_16x16x32_bf16 v[176:179], a[88:91], a[144:147], v[176:179]\n\t"
"v_mfma_f32_16x16x32_bf16 v[176:179], a[92:95], a[160:163], v[176:179]\n\t"
"buffer_load_dwordx4 a[132:135], v235, s[8:11], s58 offen offset:3072 sc1\n\t"
"v_mfma_f32_16x16x32_bf16 v[180:183], a[88:91], a[148:151], v[180:183]\n\t"
"v_mfma_f32_16x16x32_bf16 v[180:183], a[92:95], a[164:167], v[180:183]\n\t"
"v_mfma_f32_16x16x32_bf16 v[164:167], a[80:83], a[148:151], v[164:167]\n\t"
"v_mfma_f32_16x16x32_bf16 v[164:167], a[84:87], a[164:167], v[164:167]\n\t"
"buffer_load_dwordx4 a[128:131], v235, s[8:11], s58 offen offset:2048 sc1\n\t"
"v_mfma_f32_16x16x32_bf16 v[148:151], a[72:75], a[148:151], v[148:151]\n\t"
"v_mfma_f32_16x16x32_bf16 v[148:151], a[76:79], a[164:167], v[148:151]\n\t"
"buffer_load_dwordx4 a[120:123], v235, s[8:11], s58 offen offset:0 sc1\n\t"
"s_waitcnt lgkmcnt(0)\n\t"
"s_barrier\n\t"
"v_mfma_f32_16x16x32_bf16 v[152:155], a[72:75], a[152:155], v[152:155]\n\t"
"ds_read_b128 a[144:147], v216 offset:0\n\t"
"v_add_u32 v249, 39424, v224\n\t"
"v_mfma_f32_16x16x32_bf16 v[152:155], a[76:79], a[168:171], v[152:155]\n\t"
"ds_read_b128 a[160:163], v216 offset:8192\n\t"
"ds_read_b128 a[148:151], v216 offset:1024\n\t"
"v_mfma_f32_16x16x32_bf16 v[168:171], a[80:83], a[152:155], v[168:171]\n\t"
"ds_read_b128 a[164:167], v216 offset:9216\n\t"
"v_xor_b32 v250, 32, v249\n\t"
"v_mfma_f32_16x16x32_bf16 v[168:171], a[84:87], a[168:171], v[168:171]\n\t"
"buffer_load_dwordx4 a[124:127], v235, s[8:11], s58 offen offset:1024 sc1\n\t"
"v_mfma_f32_16x16x32_bf16 v[184:187], a[88:91], a[152:155], v[184:187]\n\t"
"v_xor_b32 v251, 64, v249\n\t"
"v_xor_b32 v252, 96, v249\n\t"
"v_mfma_f32_16x16x32_bf16 v[184:187], a[92:95], a[168:171], v[184:187]\n\t"
"buffer_load_dwordx4 a[16:19], v248, s[8:11], s64 offen offset:2048 sc1\n\t"
"v_mfma_f32_16x16x32_bf16 v[192:195], a[176:179], a[216:219], 0\n\t"
"ds_read_b128 a[152:155], v216 offset:2048\n\t"
"v_mfma_f32_16x16x32_bf16 v[188:191], a[88:91], a[156:159], v[188:191]\n\t"
"ds_read_b128 a[168:171], v216 offset:10240\n\t"
"v_mfma_f32_16x16x32_bf16 v[188:191], a[92:95], a[172:175], v[188:191]\n\t"
"buffer_load_dwordx4 a[20:23], v248, s[8:11], s64 offen offset:3072 sc1\n\t"
"v_mfma_f32_16x16x32_bf16 v[196:199], a[180:183], a[216:219], 0\n\t"
"ds_read_b64_tr_b16 a[176:177], v249 offset:0\n\t"
"v_mfma_f32_16x16x32_bf16 v[172:175], a[80:83], a[156:159], v[172:175]\n\t"
"v_cvt_pk_bf16_f32 v192, v192, v193\n\t"
"v_cvt_pk_bf16_f32 v193, v194, v195\n\t"
"v_mfma_f32_16x16x32_bf16 v[172:175], a[84:87], a[172:175], v[172:175]\n\t"
"buffer_load_dwordx4 a[12:15], v248, s[8:11], s64 offen offset:1024 sc1\n\t"
"v_mfma_f32_16x16x32_bf16 v[156:159], a[72:75], a[156:159], v[156:159]\n\t"
"ds_read_b128 a[156:159], v216 offset:3072\n\t"
"v_mfma_f32_16x16x32_bf16 v[156:159], a[76:79], a[172:175], v[156:159]\n\t"
"v_cvt_pk_bf16_f32 v194, v196, v197\n\t"
"v_cvt_pk_bf16_f32 v195, v198, v199\n\t"
"v_mfma_f32_16x16x32_bf16 v[200:203], a[184:187], a[216:219], 0\n\t"
"buffer_load_dwordx4 a[8:11], v248, s[8:11], s64 offen offset:0 sc1\n\t"
"s_waitcnt vmcnt(20) lgkmcnt(4)\n\t"
"v_mfma_f32_16x16x32_bf16 v[0:3], a[96:99], a[144:147], v[0:3]\n\t"
"ds_read_b128 a[172:175], v216 offset:11264\n\t"
"v_mfma_f32_16x16x32_bf16 v[0:3], a[100:103], a[160:163], v[0:3]\n\t"
"ds_write_b128 v217, v[192:195] offset:16384\n\t"
"v_mfma_f32_16x16x32_bf16 v[204:207], a[188:191], a[216:219], 0\n\t"
"ds_read_b64_tr_b16 a[178:179], v249 offset:512\n\t"
"v_mfma_f32_16x16x32_bf16 v[16:19], a[104:107], a[144:147], v[16:19]\n\t"
"v_cvt_pk_bf16_f32 v200, v200, v201\n\t"
"v_cvt_pk_bf16_f32 v201, v202, v203\n\t"
"v_mfma_f32_16x16x32_bf16 v[16:19], a[108:111], a[160:163], v[16:19]\n\t"
"buffer_load_dwordx4 a[0:3], v235, s[8:11], s63 offen offset:2048 sc1\n\t"
"v_mfma_f32_16x16x32_bf16 v[32:35], a[112:115], a[144:147], v[32:35]\n\t"
"ds_read_b128 a[144:147], v216 offset:4096\n\t"
"v_mfma_f32_16x16x32_bf16 v[32:35], a[116:119], a[160:163], v[32:35]\n\t"
"buffer_load_dwordx4 a[4:7], v235, s[8:11], s63 offen offset:3072 sc1\n\t"
"v_mfma_f32_16x16x32_bf16 v[208:211], a[192:195], a[216:219], 0\n\t"
"v_cvt_pk_bf16_f32 v202, v204, v205\n\t"
"v_cvt_pk_bf16_f32 v203, v206, v207\n\t"
"v_mfma_f32_16x16x32_bf16 v[36:39], a[112:115], a[148:151], v[36:39]\n\t"
"ds_read_b128 a[160:163], v216 offset:12288\n\t"
"v_mfma_f32_16x16x32_bf16 v[36:39], a[116:119], a[164:167], v[36:39]\n\t"
"ds_write_b128 v219, v[200:203] offset:16384\n\t"
"v_mfma_f32_16x16x32_bf16 v[20:23], a[104:107], a[148:151], v[20:23]\n\t"
"ds_read_b64_tr_b16 a[180:181], v250 offset:0\n\t"
"v_mfma_f32_16x16x32_bf16 v[20:23], a[108:111], a[164:167], v[20:23]\n\t"
"buffer_load_dwordx4 a[40:43], v248, s[8:11], s65 offen offset:0 sc1\n\t"
"v_mfma_f32_16x16x32_bf16 v[212:215], a[196:199], a[216:219], 0\n\t"
"v_cvt_pk_bf16_f32 v208, v208, v209\n\t"
"v_cvt_pk_bf16_f32 v209, v210, v211\n\t"
"v_mfma_f32_16x16x32_bf16 v[4:7], a[96:99], a[148:151], v[4:7]\n\t"
"ds_read_b128 a[148:151], v216 offset:5120\n\t"
"v_mfma_f32_16x16x32_bf16 v[4:7], a[100:103], a[164:167], v[4:7]\n\t"
"buffer_load_dwordx4 a[44:47], v248, s[8:11], s65 offen offset:1024 sc1\n\t"
"v_mfma_f32_16x16x32_bf16 v[192:195], a[200:203], a[216:219], 0\n\t"
"ds_read_b128 a[164:167], v216 offset:13312\n\t"
"s_waitcnt lgkmcnt(5)\n\t"
"v_mfma_f32_16x16x32_bf16 v[8:11], a[96:99], a[152:155], v[8:11]\n\t"
"v_cvt_pk_bf16_f32 v210, v212, v213\n\t"
"v_cvt_pk_bf16_f32 v211, v214, v215\n\t"
"v_mfma_f32_16x16x32_bf16 v[8:11], a[100:103], a[168:171], v[8:11]\n\t"
"buffer_load_dwordx4 a[36:39], v235, s[8:11], s64 offen offset:3072 sc1\n\t"
"v_mfma_f32_16x16x32_bf16 v[24:27], a[104:107], a[152:155], v[24:27]\n\t"
"ds_read_b64_tr_b16 a[182:183], v250 offset:512\n\t"
"v_mfma_f32_16x16x32_bf16 v[24:27], a[108:111], a[168:171], v[24:27]\n\t"
"v_cvt_pk_bf16_f32 v192, v192, v193\n\t"
"v_cvt_pk_bf16_f32 v193, v194, v195\n\t"
"ds_write_b128 v217, v[208:211] offset:24576\n\t"
"v_mfma_f32_16x16x32_bf16 v[196:199], a[204:207], a[216:219], 0\n\t"
"ds_read_b64_tr_b16 a[184:185], v251 offset:2304\n\t"
"v_mfma_f32_16x16x32_bf16 v[40:43], a[112:115], a[152:155], v[40:43]\n\t"
"ds_read_b64_tr_b16 a[186:187], v251 offset:2816\n\t"
"v_mfma_f32_16x16x32_bf16 v[40:43], a[116:119], a[168:171], v[40:43]\n\t"
"buffer_load_dwordx4 a[32:35], v235, s[8:11], s64 offen offset:2048 sc1\n\t"
"v_mfma_f32_16x16x32_bf16 v[44:47], a[112:115], a[156:159], v[44:47]\n\t"
"ds_read_b64_tr_b16 a[188:189], v252 offset:2304\n\t"
"v_mfma_f32_16x16x32_bf16 v[44:47], a[116:119], a[172:175], v[44:47]\n\t"
"buffer_load_dwordx4 a[24:27], v235, s[8:11], s64 offen offset:0 sc1\n\t"
"v_mfma_f32_16x16x32_bf16 v[28:31], a[104:107], a[156:159], v[28:31]\n\t"
"ds_read_b64_tr_b16 a[190:191], v252 offset:2816\n\t"
"v_cvt_pk_bf16_f32 v194, v196, v197\n\t"
"v_mfma_f32_16x16x32_bf16 v[28:31], a[108:111], a[172:175], v[28:31]\n\t"
"ds_read_b64_tr_b16 a[192:193], v251 offset:0\n\t"
"v_cvt_pk_bf16_f32 v195, v198, v199\n\t"
"v_mfma_f32_16x16x32_bf16 v[12:15], a[96:99], a[156:159], v[12:15]\n\t"
"ds_read_b64_tr_b16 a[194:195], v251 offset:512\n\t"
"v_mfma_f32_16x16x32_bf16 v[12:15], a[100:103], a[172:175], v[12:15]\n\t"
"ds_write_b128 v219, v[192:195] offset:24576\n\t"
"ds_read_b64_tr_b16 a[196:197], v252 offset:0\n\t"
"s_waitcnt vmcnt(11) lgkmcnt(9)\n\t"
"v_mfma_f32_16x16x32_bf16 v[48:51], a[120:123], a[144:147], v[48:51]\n\t"
"ds_read_b64_tr_b16 a[198:199], v252 offset:512\n\t"
"v_mfma_f32_16x16x32_bf16 v[48:51], a[124:127], a[160:163], v[48:51]\n\t"
"ds_read_b64_tr_b16 a[200:201], v249 offset:2304\n\t"
"ds_read_b64_tr_b16 a[202:203], v249 offset:2816\n\t"
"ds_read_b64_tr_b16 a[204:205], v250 offset:2304\n\t"
"ds_read_b64_tr_b16 a[206:207], v250 offset:2816\n\t"
"v_mfma_f32_16x16x32_bf16 v[64:67], a[128:131], a[144:147], v[64:67]\n\t"
"ds_read_b128 a[152:155], v216 offset:6144\n\t"
"v_mfma_f32_16x16x32_bf16 v[64:67], a[132:135], a[160:163], v[64:67]\n\t"
"buffer_load_dwordx4 a[28:31], v235, s[8:11], s64 offen offset:1024 sc1\n\t"
"v_mfma_f32_16x16x32_bf16 v[80:83], a[136:139], a[144:147], v[80:83]\n\t"
"ds_read_b128 a[168:171], v216 offset:14336\n\t"
"v_mfma_f32_16x16x32_bf16 v[80:83], a[140:143], a[160:163], v[80:83]\n\t"
"buffer_load_dwordx4 a[64:67], v248, s[8:11], s76 offen offset:2048 sc1\n\t"
"v_mfma_f32_16x16x32_bf16 v[84:87], a[136:139], a[148:151], v[84:87]\n\t"
"ds_read_b128 a[156:159], v216 offset:7168\n\t"
"v_mfma_f32_16x16x32_bf16 v[84:87], a[140:143], a[164:167], v[84:87]\n\t"
"ds_read_b128 a[172:175], v216 offset:15360\n\t"
"buffer_load_dwordx4 a[68:71], v248, s[8:11], s76 offen offset:3072 sc1\n\t"
"v_mfma_f32_16x16x32_bf16 v[68:71], a[128:131], a[148:151], v[68:71]\n\t"
"v_mfma_f32_16x16x32_bf16 v[68:71], a[132:135], a[164:167], v[68:71]\n\t"
"buffer_load_dwordx4 a[60:63], v248, s[8:11], s76 offen offset:1024 sc1\n\t"
"v_mfma_f32_16x16x32_bf16 v[52:55], a[120:123], a[148:151], v[52:55]\n\t"
"v_mfma_f32_16x16x32_bf16 v[52:55], a[124:127], a[164:167], v[52:55]\n\t"
"buffer_load_dwordx4 a[56:59], v248, s[8:11], s76 offen offset:0 sc1\n\t"
"s_waitcnt lgkmcnt(0)\n\t"
"s_barrier\n\t"
"v_mfma_f32_16x16x32_bf16 v[56:59], a[120:123], a[152:155], v[56:59]\n\t"
"ds_read_b128 a[144:147], v216 offset:16384\n\t"
"v_mfma_f32_16x16x32_bf16 v[56:59], a[124:127], a[168:171], v[56:59]\n\t"
"ds_read_b128 a[160:163], v216 offset:24576\n\t"
"ds_read_b128 a[148:151], v216 offset:17408\n\t"
"v_mfma_f32_16x16x32_bf16 v[72:75], a[128:131], a[152:155], v[72:75]\n\t"
"ds_read_b128 a[164:167], v216 offset:25600\n\t"
"v_mfma_f32_16x16x32_bf16 v[72:75], a[132:135], a[168:171], v[72:75]\n\t"
"buffer_load_dwordx4 a[48:51], v235, s[8:11], s75 offen offset:2048 sc1\n\t"
"v_mfma_f32_16x16x32_bf16 v[88:91], a[136:139], a[152:155], v[88:91]\n\t"
"ds_read_b128 a[152:155], v216 offset:18432\n\t"
"v_mfma_f32_16x16x32_bf16 v[88:91], a[140:143], a[168:171], v[88:91]\n\t"
"buffer_load_dwordx4 a[52:55], v235, s[8:11], s75 offen offset:3072 sc1\n\t"
"v_mfma_f32_16x16x32_bf16 v[200:203], a[176:179], a[212:215], 0\n\t"
"buffer_load_dwordx4 a[88:91], v248, s[8:11], s77 offen offset:0 sc1\n\t"
"v_mfma_f32_16x16x32_bf16 v[92:95], a[136:139], a[156:159], v[92:95]\n\t"
"ds_read_b128 a[168:171], v216 offset:26624\n\t"
"v_mfma_f32_16x16x32_bf16 v[92:95], a[140:143], a[172:175], v[92:95]\n\t"
"v_mfma_f32_16x16x32_bf16 v[204:207], a[180:183], a[212:215], 0\n\t"
"v_mfma_f32_16x16x32_bf16 v[76:79], a[128:131], a[156:159], v[76:79]\n\t"
"s_nop 1\n\t"
"v_cvt_pk_bf16_f32 v200, v200, v201\n\t"
"v_cvt_pk_bf16_f32 v201, v202, v203\n\t"
"v_mfma_f32_16x16x32_bf16 v[76:79], a[132:135], a[172:175], v[76:79]\n\t"
"buffer_load_dwordx4 a[92:95], v248, s[8:11], s77 offen offset:1024 sc1\n\t"
"v_mfma_f32_16x16x32_bf16 v[60:63], a[120:123], a[156:159], v[60:63]\n\t"
"ds_read_b128 a[156:159], v216 offset:19456\n\t"
"v_mfma_f32_16x16x32_bf16 v[60:63], a[124:127], a[172:175], v[60:63]\n\t"
"v_cvt_pk_bf16_f32 v202, v204, v205\n\t"
"v_cvt_pk_bf16_f32 v203, v206, v207\n\t"
"v_mfma_f32_16x16x32_bf16 v[208:211], a[184:187], a[212:215], 0\n\t"
"buffer_load_dwordx4 a[84:87], v235, s[8:11], s76 offen offset:3072 sc1\n\t"
"s_waitcnt vmcnt(15) lgkmcnt(2)\n\t"
"v_mfma_f32_16x16x32_bf16 v[96:99], a[0:3], a[144:147], v[96:99]\n\t"
"ds_read_b128 a[172:175], v216 offset:27648\n\t"
"v_mfma_f32_16x16x32_bf16 v[96:99], a[4:7], a[160:163], v[96:99]\n\t"
"ds_write_b128 v217, v[200:203] offset:0\n\t"
"v_mfma_f32_16x16x32_bf16 v[212:215], a[188:191], a[212:215], 0\n\t"
"buffer_load_dwordx4 a[80:83], v235, s[8:11], s76 offen offset:2048 sc1\n\t"
"v_mfma_f32_16x16x32_bf16 v[112:115], a[8:11], a[144:147], v[112:115]\n\t"
"v_cvt_pk_bf16_f32 v208, v208, v209\n\t"
"v_mfma_f32_16x16x32_bf16 v[112:115], a[12:15], a[160:163], v[112:115]\n\t"
"v_cvt_pk_bf16_f32 v209, v210, v211\n\t"
"v_mfma_f32_16x16x32_bf16 v[128:131], a[16:19], a[144:147], v[128:131]\n\t"
"ds_read_b128 a[144:147], v216 offset:20480\n\t"
"v_mfma_f32_16x16x32_bf16 v[128:131], a[20:23], a[160:163], v[128:131]\n\t"
"buffer_load_dwordx4 a[72:75], v235, s[8:11], s76 offen offset:0 sc1\n\t"
"v_mfma_f32_16x16x32_bf16 v[192:195], a[192:195], a[212:215], 0\n\t"
"v_cvt_pk_bf16_f32 v210, v212, v213\n\t"
"v_cvt_pk_bf16_f32 v211, v214, v215\n\t"
"v_mfma_f32_16x16x32_bf16 v[132:135], a[16:19], a[148:151], v[132:135]\n\t"
"ds_read_b128 a[160:163], v216 offset:28672\n\t"
"v_mfma_f32_16x16x32_bf16 v[132:135], a[20:23], a[164:167], v[132:135]\n\t"
"v_mfma_f32_16x16x32_bf16 v[116:119], a[8:11], a[148:151], v[116:119]\n\t"
"v_mfma_f32_16x16x32_bf16 v[116:119], a[12:15], a[164:167], v[116:119]\n\t"
"buffer_load_dwordx4 a[76:79], v235, s[8:11], s76 offen offset:1024 sc1\n\t"
"v_mfma_f32_16x16x32_bf16 v[196:199], a[196:199], a[212:215], 0\n\t"
"ds_write_b128 v219, v[208:211] offset:0\n\t"
"v_mfma_f32_16x16x32_bf16 v[100:103], a[0:3], a[148:151], v[100:103]\n\t"
"v_cvt_pk_bf16_f32 v192, v192, v193\n\t"
"v_cvt_pk_bf16_f32 v193, v194, v195\n\t"
"v_mfma_f32_16x16x32_bf16 v[100:103], a[4:7], a[164:167], v[100:103]\n\t"
"buffer_load_dwordx4 a[112:115], v248, s[8:11], s79 offen offset:2048 sc1\n\t"
"v_mfma_f32_16x16x32_bf16 v[200:203], a[200:203], a[212:215], 0\n\t"
"ds_read_b128 a[148:151], v216 offset:21504\n\t"
"s_waitcnt lgkmcnt(3)\n\t"
"v_mfma_f32_16x16x32_bf16 v[104:107], a[0:3], a[152:155], v[104:107]\n\t"
"ds_read_b128 a[164:167], v216 offset:29696\n\t"
"v_cvt_pk_bf16_f32 v194, v196, v197\n\t"
"v_mfma_f32_16x16x32_bf16 v[104:107], a[4:7], a[168:171], v[104:107]\n\t"
"v_cvt_pk_bf16_f32 v195, v198, v199\n\t"
"v_mfma_f32_16x16x32_bf16 v[120:123], a[8:11], a[152:155], v[120:123]\n\t"
"v_mfma_f32_16x16x32_bf16 v[120:123], a[12:15], a[168:171], v[120:123]\n\t"
"buffer_load_dwordx4 a[116:119], v248, s[8:11], s79 offen offset:3072 sc1\n\t"
"v_mfma_f32_16x16x32_bf16 v[204:207], a[204:207], a[212:215], 0\n\t"
"buffer_load_dwordx4 a[108:111], v248, s[8:11], s79 offen offset:1024 sc1\n\t"
"v_mfma_f32_16x16x32_bf16 v[136:139], a[16:19], a[152:155], v[136:139]\n\t"
"v_cvt_pk_bf16_f32 v200, v200, v201\n\t"
"v_cvt_pk_bf16_f32 v201, v202, v203\n\t"
"v_mfma_f32_16x16x32_bf16 v[136:139], a[20:23], a[168:171], v[136:139]\n\t"
"ds_read_b128 a[152:155], v216 offset:22528\n\t"
"v_mfma_f32_16x16x32_bf16 v[140:143], a[16:19], a[156:159], v[140:143]\n\t"
"ds_read_b128 a[168:171], v216 offset:30720\n\t"
"v_mfma_f32_16x16x32_bf16 v[140:143], a[20:23], a[172:175], v[140:143]\n\t"
"ds_write_b128 v217, v[192:195] offset:8192\n\t"
"v_mfma_f32_16x16x32_bf16 v[124:127], a[8:11], a[156:159], v[124:127]\n\t"
"v_cvt_pk_bf16_f32 v202, v204, v205\n\t"
"v_mfma_f32_16x16x32_bf16 v[124:127], a[12:15], a[172:175], v[124:127]\n\t"
"buffer_load_dwordx4 a[104:107], v248, s[8:11], s79 offen offset:0 sc1\n\t"
"v_mfma_f32_16x16x32_bf16 v[108:111], a[0:3], a[156:159], v[108:111]\n\t"
"v_cvt_pk_bf16_f32 v203, v206, v207\n\t"
"v_mfma_f32_16x16x32_bf16 v[108:111], a[4:7], a[172:175], v[108:111]\n\t"
"ds_read_b128 a[156:159], v216 offset:23552\n\t"
"s_waitcnt vmcnt(16) lgkmcnt(4)\n\t"
"v_mfma_f32_16x16x32_bf16 v[144:147], a[24:27], a[144:147], v[144:147]\n\t"
"ds_read_b128 a[172:175], v216 offset:31744\n\t"
"v_mfma_f32_16x16x32_bf16 v[144:147], a[28:31], a[160:163], v[144:147]\n\t"
"ds_write_b128 v219, v[200:203] offset:8192\n\t"
"buffer_load_dwordx4 a[96:99], v235, s[8:11], s78 offen offset:2048 sc1\n\t"
"v_mfma_f32_16x16x32_bf16 v[160:163], a[32:35], a[144:147], v[160:163]\n\t"
"v_mfma_f32_16x16x32_bf16 v[160:163], a[36:39], a[160:163], v[160:163]\n\t"
"v_mfma_f32_16x16x32_bf16 v[176:179], a[40:43], a[144:147], v[176:179]\n\t"
"v_mfma_f32_16x16x32_bf16 v[176:179], a[44:47], a[160:163], v[176:179]\n\t"
"buffer_load_dwordx4 a[100:103], v235, s[8:11], s78 offen offset:3072 sc1\n\t"
"v_mfma_f32_16x16x32_bf16 v[180:183], a[40:43], a[148:151], v[180:183]\n\t"
"v_mfma_f32_16x16x32_bf16 v[180:183], a[44:47], a[164:167], v[180:183]\n\t"
"buffer_load_dwordx4 a[136:139], v248, s[8:11], s80 offen offset:0 sc1\n\t"
"v_mfma_f32_16x16x32_bf16 v[164:167], a[32:35], a[148:151], v[164:167]\n\t"
"v_mfma_f32_16x16x32_bf16 v[164:167], a[36:39], a[164:167], v[164:167]\n\t"
"buffer_load_dwordx4 a[140:143], v248, s[8:11], s80 offen offset:1024 sc1\n\t"
"v_mfma_f32_16x16x32_bf16 v[148:151], a[24:27], a[148:151], v[148:151]\n\t"
"v_mfma_f32_16x16x32_bf16 v[148:151], a[28:31], a[164:167], v[148:151]\n\t"
"s_waitcnt lgkmcnt(0)\n\t"
"s_barrier\n\t"
"v_mfma_f32_16x16x32_bf16 v[152:155], a[24:27], a[152:155], v[152:155]\n\t"
"ds_read_b128 a[144:147], v216 offset:0\n\t"
"v_add_u32 v226, 0xa200, v226\n\t"
"v_mfma_f32_16x16x32_bf16 v[152:155], a[28:31], a[168:171], v[152:155]\n\t"
"ds_read_b128 a[160:163], v216 offset:8192\n\t"
"ds_read_b128 a[148:151], v216 offset:1024\n\t"
"v_mfma_f32_16x16x32_bf16 v[168:171], a[32:35], a[152:155], v[168:171]\n\t"
"ds_read_b128 a[164:167], v216 offset:9216\n\t"
"v_add_u32 v218, 0xa200, v218\n\t"
"v_mfma_f32_16x16x32_bf16 v[168:171], a[36:39], a[168:171], v[168:171]\n\t"
"buffer_load_dwordx4 a[132:135], v235, s[8:11], s79 offen offset:3072 sc1\n\t"
"v_mfma_f32_16x16x32_bf16 v[184:187], a[40:43], a[152:155], v[184:187]\n\t"
"v_add_u32 v220, 0xa200, v220\n\t"
"v_add_u32 v255, 0xa200, v255\n\t"
"v_mfma_f32_16x16x32_bf16 v[184:187], a[44:47], a[168:171], v[184:187]\n\t"
"buffer_load_dwordx4 a[128:131], v235, s[8:11], s79 offen offset:2048 sc1\n\t"
"v_mfma_f32_16x16x32_bf16 v[208:211], a[176:179], a[216:219], 0\n\t"
"buffer_load_dwordx4 a[120:123], v235, s[8:11], s79 offen offset:0 sc1\n\t"
"v_mfma_f32_16x16x32_bf16 v[188:191], a[40:43], a[156:159], v[188:191]\n\t"
"ds_read_b128 a[152:155], v216 offset:2048\n\t"
"v_mfma_f32_16x16x32_bf16 v[188:191], a[44:47], a[172:175], v[188:191]\n\t"
"buffer_load_dwordx4 a[124:127], v235, s[8:11], s79 offen offset:1024 sc1\n\t"
"v_mfma_f32_16x16x32_bf16 v[212:215], a[180:183], a[216:219], 0\n\t"
"ds_read_b128 a[168:171], v216 offset:10240\n\t"
"v_mfma_f32_16x16x32_bf16 v[172:175], a[32:35], a[156:159], v[172:175]\n\t"
"v_cvt_pk_bf16_f32 v208, v208, v209\n\t"
"v_cvt_pk_bf16_f32 v209, v210, v211\n\t"
"v_mfma_f32_16x16x32_bf16 v[172:175], a[36:39], a[172:175], v[172:175]\n\t"
"ds_read_b64_tr_b16 a[176:177], v226 offset:41472\n\t"
"v_mfma_f32_16x16x32_bf16 v[156:159], a[24:27], a[156:159], v[156:159]\n\t"
"ds_read_b128 a[156:159], v216 offset:3072\n\t"
"v_mfma_f32_16x16x32_bf16 v[156:159], a[28:31], a[172:175], v[156:159]\n\t"
"buffer_load_dwordx4 a[16:19], v235, s[8:11], s57 offen offset:0 sc1\n\t"
"v_mfma_f32_16x16x32_bf16 v[192:195], a[184:187], a[216:219], 0\n\t"
"v_cvt_pk_bf16_f32 v210, v212, v213\n\t"
"v_cvt_pk_bf16_f32 v211, v214, v215\n\t"
"s_waitcnt vmcnt(19) lgkmcnt(4)\n\t"
"v_mfma_f32_16x16x32_bf16 v[0:3], a[48:51], a[144:147], v[0:3]\n\t"
"ds_read_b128 a[172:175], v216 offset:11264\n\t"
"v_mfma_f32_16x16x32_bf16 v[0:3], a[52:55], a[160:163], v[0:3]\n\t"
"buffer_load_dwordx4 a[20:23], v235, s[8:11], s57 offen offset:1024 sc1\n\t"
"v_mfma_f32_16x16x32_bf16 v[196:199], a[188:191], a[216:219], 0\n\t"
"ds_write_b128 v217, v[208:211] offset:16384\n\t"
"v_mfma_f32_16x16x32_bf16 v[16:19], a[56:59], a[144:147], v[16:19]\n\t"
"v_cvt_pk_bf16_f32 v192, v192, v193\n\t"
"v_cvt_pk_bf16_f32 v193, v194, v195\n\t"
"v_mfma_f32_16x16x32_bf16 v[16:19], a[60:63], a[160:163], v[16:19]\n\t"
"buffer_load_dwordx4 a[12:15], v248, s[8:11], s57 offen offset:3072 sc1\n\t"
"v_mfma_f32_16x16x32_bf16 v[32:35], a[64:67], a[144:147], v[32:35]\n\t"
"ds_read_b128 a[144:147], v216 offset:4096\n\t"
"v_mfma_f32_16x16x32_bf16 v[32:35], a[68:71], a[160:163], v[32:35]\n\t"
"v_cvt_pk_bf16_f32 v194, v196, v197\n\t"
"v_cvt_pk_bf16_f32 v195, v198, v199\n\t"
"v_mfma_f32_16x16x32_bf16 v[200:203], a[192:195], a[216:219], 0\n\t"
"ds_read_b128 a[160:163], v216 offset:12288\n\t"
"v_mfma_f32_16x16x32_bf16 v[36:39], a[64:67], a[148:151], v[36:39]\n\t"
"ds_read_b64_tr_b16 a[178:179], v226 offset:41984\n\t"
"v_mfma_f32_16x16x32_bf16 v[36:39], a[68:71], a[164:167], v[36:39]\n\t"
"buffer_load_dwordx4 a[8:11], v248, s[8:11], s57 offen offset:2048 sc1\n\t"
"v_mfma_f32_16x16x32_bf16 v[20:23], a[56:59], a[148:151], v[20:23]\n\t"
"ds_read_b64_tr_b16 a[180:181], v218 offset:41472\n\t"
"v_mfma_f32_16x16x32_bf16 v[20:23], a[60:63], a[164:167], v[20:23]\n\t"
"buffer_load_dwordx4 a[0:3], v248, s[8:11], s57 offen offset:0 sc1\n\t"
"v_mfma_f32_16x16x32_bf16 v[204:207], a[196:199], a[216:219], 0\n\t"
"ds_write_b128 v219, v[192:195] offset:16384\n\t"
"v_mfma_f32_16x16x32_bf16 v[4:7], a[48:51], a[148:151], v[4:7]\n\t"
"v_cvt_pk_bf16_f32 v200, v200, v201\n\t"
"v_cvt_pk_bf16_f32 v201, v202, v203\n\t"
"v_mfma_f32_16x16x32_bf16 v[4:7], a[52:55], a[164:167], v[4:7]\n\t"
"ds_read_b128 a[148:151], v216 offset:5120\n\t"
"v_mfma_f32_16x16x32_bf16 v[208:211], a[200:203], a[216:219], 0\n\t"
"buffer_load_dwordx4 a[4:7], v248, s[8:11], s57 offen offset:1024 sc1\n\t"
"s_waitcnt lgkmcnt(5)\n\t"
"v_mfma_f32_16x16x32_bf16 v[8:11], a[48:51], a[152:155], v[8:11]\n\t"
"ds_read_b128 a[164:167], v216 offset:13312\n\t"
"v_cvt_pk_bf16_f32 v202, v204, v205\n\t"
"v_mfma_f32_16x16x32_bf16 v[8:11], a[52:55], a[168:171], v[8:11]\n\t"
"v_cvt_pk_bf16_f32 v203, v206, v207\n\t"
"ds_read_b64_tr_b16 a[182:183], v218 offset:41984\n\t"
"v_mfma_f32_16x16x32_bf16 v[24:27], a[56:59], a[152:155], v[24:27]\n\t"
"ds_read_b64_tr_b16 a[184:185], v220 offset:43776\n\t"
"v_mfma_f32_16x16x32_bf16 v[24:27], a[60:63], a[168:171], v[24:27]\n\t"
"buffer_load_dwordx4 a[40:43], v235, s[8:11], s63 offen offset:0 sc1\n\t"
"v_mfma_f32_16x16x32_bf16 v[212:215], a[204:207], a[216:219], 0\n\t"
"v_cvt_pk_bf16_f32 v208, v208, v209\n\t"
"v_cvt_pk_bf16_f32 v209, v210, v211\n\t"
"v_mfma_f32_16x16x32_bf16 v[40:43], a[64:67], a[152:155], v[40:43]\n\t"
"ds_read_b64_tr_b16 a[186:187], v220 offset:44288\n\t"
"v_mfma_f32_16x16x32_bf16 v[40:43], a[68:71], a[168:171], v[40:43]\n\t"
"buffer_load_dwordx4 a[44:47], v235, s[8:11], s63 offen offset:1024 sc1\n\t"
"v_mfma_f32_16x16x32_bf16 v[44:47], a[64:67], a[156:159], v[44:47]\n\t"
"ds_read_b64_tr_b16 a[188:189], v255 offset:43776\n\t"
"v_mfma_f32_16x16x32_bf16 v[44:47], a[68:71], a[172:175], v[44:47]\n\t"
"ds_write_b128 v217, v[200:203] offset:24576\n\t"
"v_mfma_f32_16x16x32_bf16 v[28:31], a[56:59], a[156:159], v[28:31]\n\t"
"ds_read_b64_tr_b16 a[190:191], v255 offset:44288\n\t"
"v_cvt_pk_bf16_f32 v210, v212, v213\n\t"
"v_mfma_f32_16x16x32_bf16 v[28:31], a[60:63], a[172:175], v[28:31]\n\t"
"buffer_load_dwordx4 a[36:39], v248, s[8:11], s63 offen offset:3072 sc1\n\t"
"v_mfma_f32_16x16x32_bf16 v[12:15], a[48:51], a[156:159], v[12:15]\n\t"
"ds_read_b64_tr_b16 a[192:193], v220 offset:41472\n\t"
"v_cvt_pk_bf16_f32 v211, v214, v215\n\t"
"v_mfma_f32_16x16x32_bf16 v[12:15], a[52:55], a[172:175], v[12:15]\n\t"
"ds_write_b128 v219, v[208:211] offset:24576\n\t"
"s_waitcnt vmcnt(21) lgkmcnt(6)\n\t"
"v_mfma_f32_16x16x32_bf16 v[48:51], a[72:75], a[144:147], v[48:51]\n\t"
"ds_read_b64_tr_b16 a[194:195], v220 offset:41984\n\t"
"v_mfma_f32_16x16x32_bf16 v[48:51], a[76:79], a[160:163], v[48:51]\n\t"
"ds_read_b64_tr_b16 a[196:197], v255 offset:41472\n\t"
"ds_read_b64_tr_b16 a[198:199], v255 offset:41984\n\t"
"ds_read_b64_tr_b16 a[200:201], v226 offset:43776\n\t"
"ds_read_b64_tr_b16 a[202:203], v226 offset:44288\n\t"
"ds_read_b64_tr_b16 a[204:205], v218 offset:43776\n\t"
"ds_read_b64_tr_b16 a[206:207], v218 offset:44288\n\t"
"v_mfma_f32_16x16x32_bf16 v[64:67], a[80:83], a[144:147], v[64:67]\n\t"
"ds_read_b128 a[152:155], v216 offset:6144\n\t"
"v_mfma_f32_16x16x32_bf16 v[64:67], a[84:87], a[160:163], v[64:67]\n\t"
"buffer_load_dwordx4 a[32:35], v248, s[8:11], s63 offen offset:2048 sc1\n\t"
"v_mfma_f32_16x16x32_bf16 v[80:83], a[88:91], a[144:147], v[80:83]\n\t"
"ds_read_b128 a[168:171], v216 offset:14336\n\t"
"v_mfma_f32_16x16x32_bf16 v[80:83], a[92:95], a[160:163], v[80:83]\n\t"
"ds_read_b128 a[156:159], v216 offset:7168\n\t"
"v_mfma_f32_16x16x32_bf16 v[84:87], a[88:91], a[148:151], v[84:87]\n\t"
"ds_read_b128 a[172:175], v216 offset:15360\n\t"
"v_mfma_f32_16x16x32_bf16 v[84:87], a[92:95], a[164:167], v[84:87]\n\t"
"buffer_load_dwordx4 a[24:27], v248, s[8:11], s63 offen offset:0 sc1\n\t"
"v_mfma_f32_16x16x32_bf16 v[68:71], a[80:83], a[148:151], v[68:71]\n\t"
"v_mfma_f32_16x16x32_bf16 v[68:71], a[84:87], a[164:167], v[68:71]\n\t"
"buffer_load_dwordx4 a[28:31], v248, s[8:11], s63 offen offset:1024 sc1\n\t"
"v_mfma_f32_16x16x32_bf16 v[52:55], a[72:75], a[148:151], v[52:55]\n\t"
"v_mfma_f32_16x16x32_bf16 v[52:55], a[76:79], a[164:167], v[52:55]\n\t"
"buffer_load_dwordx4 a[64:67], v235, s[8:11], s59 offen offset:2048 sc1\n\t"
"s_waitcnt lgkmcnt(0)\n\t"
"s_barrier\n\t"
"v_mfma_f32_16x16x32_bf16 v[56:59], a[72:75], a[152:155], v[56:59]\n\t"
"ds_read_b128 a[144:147], v216 offset:16384\n\t"
"v_mfma_f32_16x16x32_bf16 v[56:59], a[76:79], a[168:171], v[56:59]\n\t"
"ds_read_b128 a[160:163], v216 offset:24576\n\t"
"ds_read_b128 a[148:151], v216 offset:17408\n\t"
"v_mfma_f32_16x16x32_bf16 v[72:75], a[80:83], a[152:155], v[72:75]\n\t"
"ds_read_b128 a[164:167], v216 offset:25600\n\t"
"v_mfma_f32_16x16x32_bf16 v[72:75], a[84:87], a[168:171], v[72:75]\n\t"
"buffer_load_dwordx4 a[68:71], v235, s[8:11], s59 offen offset:3072 sc1\n\t"
"v_mfma_f32_16x16x32_bf16 v[88:91], a[88:91], a[152:155], v[88:91]\n\t"
"ds_read_b128 a[240:243], v223 offset:39168\n\t"
"v_mfma_f32_16x16x32_bf16 v[88:91], a[92:95], a[168:171], v[88:91]\n\t"
"buffer_load_dwordx4 a[60:63], v235, s[8:11], s59 offen offset:1024 sc1\n\t"
"v_mfma_f32_16x16x32_bf16 v[192:195], a[176:179], a[220:223], 0\n\t"
"ds_read_b128 a[152:155], v216 offset:18432\n\t"
"v_mfma_f32_16x16x32_bf16 v[92:95], a[88:91], a[156:159], v[92:95]\n\t"
"ds_read_b128 a[168:171], v216 offset:26624\n\t"
"v_mfma_f32_16x16x32_bf16 v[92:95], a[92:95], a[172:175], v[92:95]\n\t"
"buffer_load_dwordx4 a[56:59], v235, s[8:11], s59 offen offset:0 sc1\n\t"
"v_mfma_f32_16x16x32_bf16 v[196:199], a[180:183], a[220:223], 0\n\t"
"ds_read_b64_tr_b16 a[176:177], v220 offset:62208\n\t"
"v_mfma_f32_16x16x32_bf16 v[76:79], a[80:83], a[156:159], v[76:79]\n\t"
"v_cvt_pk_bf16_f32 v192, v192, v193\n\t"
"v_cvt_pk_bf16_f32 v193, v194, v195\n\t"
"v_mfma_f32_16x16x32_bf16 v[76:79], a[84:87], a[172:175], v[76:79]\n\t"
"buffer_load_dwordx4 a[48:51], v248, s[8:11], s59 offen offset:2048 sc1\n\t"
"v_mfma_f32_16x16x32_bf16 v[60:63], a[72:75], a[156:159], v[60:63]\n\t"
"ds_read_b128 a[156:159], v216 offset:19456\n\t"
"v_mfma_f32_16x16x32_bf16 v[60:63], a[76:79], a[172:175], v[60:63]\n\t"
"buffer_load_dwordx4 a[52:55], v248, s[8:11], s59 offen offset:3072 sc1\n\t"
"v_mfma_f32_16x16x32_bf16 v[200:203], a[184:187], a[220:223], 0\n\t"
"buffer_load_dwordx4 a[88:91], v235, s[8:11], s65 offen offset:2048 sc1\n\t"
"s_waitcnt vmcnt(25) lgkmcnt(4)\n\t"
"v_mfma_f32_16x16x32_bf16 v[96:99], a[96:99], a[144:147], v[96:99]\n\t"
"v_cvt_pk_bf16_f32 v194, v196, v197\n\t"
"v_cvt_pk_bf16_f32 v195, v198, v199\n\t"
"v_mfma_f32_16x16x32_bf16 v[96:99], a[100:103], a[160:163], v[96:99]\n\t"
"buffer_load_dwordx4 a[92:95], v235, s[8:11], s65 offen offset:3072 sc1\n\t"
"v_mfma_f32_16x16x32_bf16 v[204:207], a[188:191], a[220:223], 0\n\t"
"v_pk_add_f32 v[0:1], v[0:1], v[48:49]\n\t"
"v_pk_add_f32 v[2:3], v[2:3], v[50:51]\n\t"
"v_pk_fma_f32 v[48:49], v[48:49], s[38:39], v[0:1]\n\t"
"v_pk_fma_f32 v[50:51], v[50:51], s[38:39], v[2:3]\n\t"
"v_pk_add_f32 v[16:17], v[16:17], v[64:65]\n\t"
"v_pk_add_f32 v[18:19], v[18:19], v[66:67]\n\t"
"v_pk_fma_f32 v[64:65], v[64:65], s[38:39], v[16:17]\n\t"
"v_pk_fma_f32 v[66:67], v[66:67], s[38:39], v[18:19]\n\t"
"v_pk_add_f32 v[32:33], v[32:33], v[80:81]\n\t"
"v_pk_add_f32 v[34:35], v[34:35], v[82:83]\n\t"
"v_pk_fma_f32 v[80:81], v[80:81], s[38:39], v[32:33]\n\t"
"v_pk_fma_f32 v[82:83], v[82:83], s[38:39], v[34:35]\n\t"
"v_pk_add_f32 v[36:37], v[36:37], v[84:85]\n\t"
"v_pk_add_f32 v[38:39], v[38:39], v[86:87]\n\t"
"v_pk_fma_f32 v[84:85], v[84:85], s[38:39], v[36:37]\n\t"
"v_pk_fma_f32 v[86:87], v[86:87], s[38:39], v[38:39]\n\t"
"v_pk_add_f32 v[20:21], v[20:21], v[68:69]\n\t"
"v_pk_add_f32 v[22:23], v[22:23], v[70:71]\n\t"
"v_pk_fma_f32 v[68:69], v[68:69], s[38:39], v[20:21]\n\t"
"v_pk_fma_f32 v[70:71], v[70:71], s[38:39], v[22:23]\n\t"
"v_pk_add_f32 v[4:5], v[4:5], v[52:53]\n\t"
"v_pk_add_f32 v[6:7], v[6:7], v[54:55]\n\t"
"v_pk_fma_f32 v[52:53], v[52:53], s[38:39], v[4:5]\n\t"
"v_pk_fma_f32 v[54:55], v[54:55], s[38:39], v[6:7]\n\t"
"v_pk_add_f32 v[8:9], v[8:9], v[56:57]\n\t"
"v_pk_add_f32 v[10:11], v[10:11], v[58:59]\n\t"
"v_pk_fma_f32 v[56:57], v[56:57], s[38:39], v[8:9]\n\t"
"v_pk_fma_f32 v[58:59], v[58:59], s[38:39], v[10:11]\n\t"
"v_pk_add_f32 v[24:25], v[24:25], v[72:73]\n\t"
"v_pk_add_f32 v[26:27], v[26:27], v[74:75]\n\t"
"v_pk_fma_f32 v[72:73], v[72:73], s[38:39], v[24:25]\n\t"
"v_pk_fma_f32 v[74:75], v[74:75], s[38:39], v[26:27]\n\t"
"v_pk_add_f32 v[40:41], v[40:41], v[88:89]\n\t"
"v_pk_add_f32 v[42:43], v[42:43], v[90:91]\n\t"
"v_pk_fma_f32 v[88:89], v[88:89], s[38:39], v[40:41]\n\t"
"v_pk_fma_f32 v[90:91], v[90:91], s[38:39], v[42:43]\n\t"
"v_pk_add_f32 v[44:45], v[44:45], v[92:93]\n\t"
"v_pk_add_f32 v[46:47], v[46:47], v[94:95]\n\t"
"v_pk_fma_f32 v[92:93], v[92:93], s[38:39], v[44:45]\n\t"
"v_pk_fma_f32 v[94:95], v[94:95], s[38:39], v[46:47]\n\t"
"v_pk_add_f32 v[28:29], v[28:29], v[76:77]\n\t"
"v_pk_add_f32 v[30:31], v[30:31], v[78:79]\n\t"
"v_pk_fma_f32 v[76:77], v[76:77], s[38:39], v[28:29]\n\t"
"v_pk_fma_f32 v[78:79], v[78:79], s[38:39], v[30:31]\n\t"
"v_pk_add_f32 v[12:13], v[12:13], v[60:61]\n\t"
"v_pk_add_f32 v[14:15], v[14:15], v[62:63]\n\t"
"v_pk_fma_f32 v[60:61], v[60:61], s[38:39], v[12:13]\n\t"
"v_pk_fma_f32 v[62:63], v[62:63], s[38:39], v[14:15]\n\t"
"buffer_load_dwordx4 a[84:87], v235, s[8:11], s65 offen offset:1024 sc1\n\t"
"v_mfma_f32_16x16x32_bf16 v[112:115], a[104:107], a[144:147], v[112:115]\n\t"
"ds_read_b128 a[172:175], v216 offset:27648\n\t"
"v_cvt_pk_bf16_f32 v200, v200, v201\n\t"
"v_mfma_f32_16x16x32_bf16 v[112:115], a[108:111], a[160:163], v[112:115]\n\t"
"buffer_load_dwordx4 a[80:83], v235, s[8:11], s65 offen offset:0 sc1\n\t"
"v_mfma_f32_16x16x32_bf16 v[128:131], a[112:115], a[144:147], v[128:131]\n\t"
"v_cvt_pk_bf16_f32 v201, v202, v203\n\t"
"ds_read_b128 a[144:147], v216 offset:20480\n\t"
"v_mfma_f32_16x16x32_bf16 v[128:131], a[116:119], a[160:163], v[128:131]\n\t"
"buffer_load_dwordx4 a[72:75], v248, s[8:11], s65 offen offset:2048 sc1\n\t"
"v_mfma_f32_16x16x32_bf16 v[208:211], a[192:195], a[220:223], 0\n\t"
"ds_write_b128 v217, v[192:195] offset:0\n\t"
"v_mfma_f32_16x16x32_bf16 v[132:135], a[112:115], a[148:151], v[132:135]\n\t"
"v_cvt_pk_bf16_f32 v202, v204, v205\n\t"
"v_cvt_pk_bf16_f32 v203, v206, v207\n\t"
"v_mfma_f32_16x16x32_bf16 v[132:135], a[116:119], a[164:167], v[132:135]\n\t"
"buffer_load_dwordx4 a[76:79], v248, s[8:11], s65 offen offset:3072 sc1\n\t"
"v_mfma_f32_16x16x32_bf16 v[116:119], a[104:107], a[148:151], v[116:119]\n\t"
"ds_read_b128 a[160:163], v216 offset:28672\n\t"
"v_mfma_f32_16x16x32_bf16 v[116:119], a[108:111], a[164:167], v[116:119]\n\t"
"ds_write_b128 v219, v[200:203] offset:0\n\t"
"v_mfma_f32_16x16x32_bf16 v[212:215], a[196:199], a[220:223], 0\n\t"
"v_cvt_pk_bf16_f32 v208, v208, v209\n\t"
"v_cvt_pk_bf16_f32 v209, v210, v211\n\t"
"v_mfma_f32_16x16x32_bf16 v[100:103], a[96:99], a[148:151], v[100:103]\n\t"
"ds_read_b128 a[148:151], v216 offset:21504\n\t"
"v_mfma_f32_16x16x32_bf16 v[100:103], a[100:103], a[164:167], v[100:103]\n\t"
"ds_read_b128 a[164:167], v216 offset:29696\n\t"
"v_mfma_f32_16x16x32_bf16 v[192:195], a[200:203], a[220:223], 0\n\t"
"ds_read_b64_tr_b16 a[178:179], v220 offset:62720\n\t"
"s_waitcnt lgkmcnt(6)\n\t"
"v_mfma_f32_16x16x32_bf16 v[104:107], a[96:99], a[152:155], v[104:107]\n\t"
"v_cvt_pk_bf16_f32 v210, v212, v213\n\t"
"v_cvt_pk_bf16_f32 v211, v214, v215\n\t"
"v_mfma_f32_16x16x32_bf16 v[104:107], a[100:103], a[168:171], v[104:107]\n\t"
"ds_write_b128 v217, v[208:211] offset:8192\n\t"
"v_mfma_f32_16x16x32_bf16 v[120:123], a[104:107], a[152:155], v[120:123]\n\t"
"ds_read_b64_tr_b16 a[180:181], v255 offset:62208\n\t"
"v_mfma_f32_16x16x32_bf16 v[120:123], a[108:111], a[168:171], v[120:123]\n\t"
"v_cvt_pk_bf16_f32 v192, v192, v193\n\t"
"v_cvt_pk_bf16_f32 v193, v194, v195\n\t"
"v_mfma_f32_16x16x32_bf16 v[196:199], a[204:207], a[220:223], 0\n\t"
"ds_read_b64_tr_b16 a[182:183], v255 offset:62720\n\t"
"v_mfma_f32_16x16x32_bf16 v[136:139], a[112:115], a[152:155], v[136:139]\n\t"
"ds_read_b64_tr_b16 a[184:185], v226 offset:64512\n\t"
"v_mfma_f32_16x16x32_bf16 v[136:139], a[116:119], a[168:171], v[136:139]\n\t"
"ds_read_b64_tr_b16 a[186:187], v226 offset:65024\n\t"
"ds_read_b64_tr_b16 a[188:189], v218 offset:64512\n\t"
"v_mfma_f32_16x16x32_bf16 v[140:143], a[112:115], a[156:159], v[140:143]\n\t"
"ds_read_b64_tr_b16 a[190:191], v218 offset:65024\n\t"
"v_mfma_f32_16x16x32_bf16 v[140:143], a[116:119], a[172:175], v[140:143]\n\t"
"buffer_load_dwordx4 a[112:115], v235, s[8:11], s75 offen offset:0 sc1\n\t"
"buffer_load_dwordx4 a[116:119], v235, s[8:11], s75 offen offset:1024 sc1\n\t"
"v_mfma_f32_16x16x32_bf16 v[124:127], a[104:107], a[156:159], v[124:127]\n\t"
"ds_read_b64_tr_b16 a[192:193], v226 offset:62208\n\t"
"v_cvt_pk_bf16_f32 v194, v196, v197\n\t"
"v_mfma_f32_16x16x32_bf16 v[124:127], a[108:111], a[172:175], v[124:127]\n\t"
"buffer_load_dwordx4 a[108:111], v248, s[8:11], s75 offen offset:3072 sc1\n\t"
"v_mfma_f32_16x16x32_bf16 v[108:111], a[96:99], a[156:159], v[108:111]\n\t"
"ds_read_b64_tr_b16 a[194:195], v226 offset:62720\n\t"
"v_cvt_pk_bf16_f32 v195, v198, v199\n\t"
"v_mfma_f32_16x16x32_bf16 v[108:111], a[100:103], a[172:175], v[108:111]\n\t"
"ds_write_b128 v219, v[192:195] offset:8192\n\t"
"s_waitcnt vmcnt(27) lgkmcnt(8)\n\t"
"v_mfma_f32_16x16x32_bf16 v[144:147], a[120:123], a[144:147], v[144:147]\n\t"
"ds_read_b64_tr_b16 a[196:197], v218 offset:62208\n\t"
"v_mfma_f32_16x16x32_bf16 v[144:147], a[124:127], a[160:163], v[144:147]\n\t"
"ds_read_b64_tr_b16 a[198:199], v218 offset:62720\n\t"
"ds_read_b64_tr_b16 a[200:201], v220 offset:64512\n\t"
"ds_read_b64_tr_b16 a[202:203], v220 offset:65024\n\t"
"ds_read_b64_tr_b16 a[204:205], v255 offset:64512\n\t"
"ds_read_b64_tr_b16 a[206:207], v255 offset:65024\n\t"
"buffer_load_dwordx4 a[104:107], v248, s[8:11], s75 offen offset:2048 sc1\n\t"
"v_mfma_f32_16x16x32_bf16 v[160:163], a[128:131], a[144:147], v[160:163]\n\t"
"ds_read_b128 a[152:155], v216 offset:22528\n\t"
"v_mfma_f32_16x16x32_bf16 v[160:163], a[132:135], a[160:163], v[160:163]\n\t"
"buffer_load_dwordx4 a[96:99], v248, s[8:11], s75 offen offset:0 sc1\n\t"
"buffer_load_dwordx4 a[100:103], v248, s[8:11], s75 offen offset:1024 sc1\n\t"
"v_mfma_f32_16x16x32_bf16 v[176:179], a[136:139], a[144:147], v[176:179]\n\t"
"v_mfma_f32_16x16x32_bf16 v[176:179], a[140:143], a[160:163], v[176:179]\n\t"
"ds_read_b128 a[168:171], v216 offset:30720\n\t"
"v_mfma_f32_16x16x32_bf16 v[180:183], a[136:139], a[148:151], v[180:183]\n\t"
"ds_read_b128 a[156:159], v216 offset:23552\n\t"
"v_mfma_f32_16x16x32_bf16 v[180:183], a[140:143], a[164:167], v[180:183]\n\t"
"ds_read_b128 a[172:175], v216 offset:31744\n\t"
"ds_write_b128 v223, a[240:243] offset:20736\n\t"
"v_mfma_f32_16x16x32_bf16 v[164:167], a[128:131], a[148:151], v[164:167]\n\t"
"v_mfma_f32_16x16x32_bf16 v[164:167], a[132:135], a[164:167], v[164:167]\n\t"
"v_mfma_f32_16x16x32_bf16 v[148:151], a[120:123], a[148:151], v[148:151]\n\t"
"v_mfma_f32_16x16x32_bf16 v[148:151], a[124:127], a[164:167], v[148:151]\n\t"
"s_waitcnt lgkmcnt(0)\n\t"
"s_barrier\n\t"
"v_mfma_f32_16x16x32_bf16 v[152:155], a[120:123], a[152:155], v[152:155]\n\t"
"ds_read_b128 a[144:147], v216 offset:0\n\t"
"v_add_u32 v226, 0xffff5e00, v226\n\t"
"v_mfma_f32_16x16x32_bf16 v[152:155], a[124:127], a[168:171], v[152:155]\n\t"
"ds_read_b128 a[160:163], v216 offset:8192\n\t"
"ds_read_b128 a[148:151], v216 offset:1024\n\t"
"v_mfma_f32_16x16x32_bf16 v[168:171], a[128:131], a[152:155], v[168:171]\n\t"
"ds_read_b128 a[164:167], v216 offset:9216\n\t"
"v_add_u32 v218, 0xffff5e00, v218\n\t"
"v_mfma_f32_16x16x32_bf16 v[168:171], a[132:135], a[168:171], v[168:171]\n\t"
"v_add_u32 v220, 0xffff5e00, v220\n\t"
"v_add_u32 v255, 0xffff5e00, v255\n\t"
"v_mfma_f32_16x16x32_bf16 v[184:187], a[136:139], a[152:155], v[184:187]\n\t"
"ds_read_b128 a[240:243], v223 offset:59904\n\t"
"v_mfma_f32_16x16x32_bf16 v[184:187], a[140:143], a[168:171], v[184:187]\n\t"
"ds_read_b128 a[152:155], v216 offset:2048\n\t"
"v_mfma_f32_16x16x32_bf16 v[200:203], a[176:179], a[220:223], 0\n\t"
"ds_read_b128 a[168:171], v216 offset:10240\n\t"
"v_mfma_f32_16x16x32_bf16 v[188:191], a[136:139], a[156:159], v[188:191]\n\t"
"ds_read_b64_tr_b16 a[176:177], v226 offset:0\n\t"
"v_mfma_f32_16x16x32_bf16 v[188:191], a[140:143], a[172:175], v[188:191]\n\t"
"buffer_load_dwordx4 a[136:139], v235, s[8:11], s78 offen offset:0 sc1\n\t"
"v_mfma_f32_16x16x32_bf16 v[204:207], a[180:183], a[220:223], 0\n\t"
"buffer_load_dwordx4 a[140:143], v235, s[8:11], s78 offen offset:1024 sc1\n\t"
"v_mfma_f32_16x16x32_bf16 v[172:175], a[128:131], a[156:159], v[172:175]\n\t"
"v_cvt_pk_bf16_f32 v200, v200, v201\n\t"
"v_cvt_pk_bf16_f32 v201, v202, v203\n\t"
"v_mfma_f32_16x16x32_bf16 v[172:175], a[132:135], a[172:175], v[172:175]\n\t"
"buffer_load_dwordx4 a[132:135], v248, s[8:11], s78 offen offset:3072 sc1\n\t"
"v_mfma_f32_16x16x32_bf16 v[156:159], a[120:123], a[156:159], v[156:159]\n\t"
"ds_read_b128 a[156:159], v216 offset:3072\n\t"
"v_mfma_f32_16x16x32_bf16 v[156:159], a[124:127], a[172:175], v[156:159]\n\t"
"buffer_load_dwordx4 a[128:131], v248, s[8:11], s78 offen offset:2048 sc1\n\t"
"v_mfma_f32_16x16x32_bf16 v[208:211], a[184:187], a[220:223], 0\n\t"
"buffer_load_dwordx4 a[120:123], v248, s[8:11], s78 offen offset:0 sc1\n\t"
"s_waitcnt vmcnt(29) lgkmcnt(4)\n\t"
"v_mfma_f32_16x16x32_bf16 v[0:3], a[0:3], a[144:147], v[0:3]\n\t"
"v_cvt_pk_bf16_f32 v202, v204, v205\n\t"
"v_cvt_pk_bf16_f32 v203, v206, v207\n\t"
"v_mfma_f32_16x16x32_bf16 v[0:3], a[4:7], a[160:163], v[0:3]\n\t"
"buffer_load_dwordx4 a[124:127], v248, s[8:11], s78 offen offset:1024 sc1\n\t"
"v_mfma_f32_16x16x32_bf16 v[212:215], a[188:191], a[220:223], 0\n\t"
"v_pk_add_f32 v[96:97], v[96:97], v[144:145]\n\t"
"v_pk_add_f32 v[98:99], v[98:99], v[146:147]\n\t"
"v_pk_fma_f32 v[144:145], v[144:145], s[38:39], v[96:97]\n\t"
"v_pk_fma_f32 v[146:147], v[146:147], s[38:39], v[98:99]\n\t"
"v_pk_add_f32 v[112:113], v[112:113], v[160:161]\n\t"
"v_pk_add_f32 v[114:115], v[114:115], v[162:163]\n\t"
"v_pk_fma_f32 v[160:161], v[160:161], s[38:39], v[112:113]\n\t"
"v_pk_fma_f32 v[162:163], v[162:163], s[38:39], v[114:115]\n\t"
"v_pk_add_f32 v[128:129], v[128:129], v[176:177]\n\t"
"v_pk_add_f32 v[130:131], v[130:131], v[178:179]\n\t"
"v_pk_fma_f32 v[176:177], v[176:177], s[38:39], v[128:129]\n\t"
"v_pk_fma_f32 v[178:179], v[178:179], s[38:39], v[130:131]\n\t"
"v_pk_add_f32 v[132:133], v[132:133], v[180:181]\n\t"
"v_pk_add_f32 v[134:135], v[134:135], v[182:183]\n\t"
"v_pk_fma_f32 v[180:181], v[180:181], s[38:39], v[132:133]\n\t"
"v_pk_fma_f32 v[182:183], v[182:183], s[38:39], v[134:135]\n\t"
"v_pk_add_f32 v[116:117], v[116:117], v[164:165]\n\t"
"v_pk_add_f32 v[118:119], v[118:119], v[166:167]\n\t"
"v_pk_fma_f32 v[164:165], v[164:165], s[38:39], v[116:117]\n\t"
"v_pk_fma_f32 v[166:167], v[166:167], s[38:39], v[118:119]\n\t"
"v_pk_add_f32 v[100:101], v[100:101], v[148:149]\n\t"
"v_pk_add_f32 v[102:103], v[102:103], v[150:151]\n\t"
"v_pk_fma_f32 v[148:149], v[148:149], s[38:39], v[100:101]\n\t"
"v_pk_fma_f32 v[150:151], v[150:151], s[38:39], v[102:103]\n\t"
"v_pk_add_f32 v[104:105], v[104:105], v[152:153]\n\t"
"v_pk_add_f32 v[106:107], v[106:107], v[154:155]\n\t"
"v_pk_fma_f32 v[152:153], v[152:153], s[38:39], v[104:105]\n\t"
"v_pk_fma_f32 v[154:155], v[154:155], s[38:39], v[106:107]\n\t"
"v_pk_add_f32 v[120:121], v[120:121], v[168:169]\n\t"
"v_pk_add_f32 v[122:123], v[122:123], v[170:171]\n\t"
"v_pk_fma_f32 v[168:169], v[168:169], s[38:39], v[120:121]\n\t"
"v_pk_fma_f32 v[170:171], v[170:171], s[38:39], v[122:123]\n\t"
"v_pk_add_f32 v[136:137], v[136:137], v[184:185]\n\t"
"v_pk_add_f32 v[138:139], v[138:139], v[186:187]\n\t"
"v_pk_fma_f32 v[184:185], v[184:185], s[38:39], v[136:137]\n\t"
"v_pk_fma_f32 v[186:187], v[186:187], s[38:39], v[138:139]\n\t"
"v_pk_add_f32 v[140:141], v[140:141], v[188:189]\n\t"
"v_pk_add_f32 v[142:143], v[142:143], v[190:191]\n\t"
"v_pk_fma_f32 v[188:189], v[188:189], s[38:39], v[140:141]\n\t"
"v_pk_fma_f32 v[190:191], v[190:191], s[38:39], v[142:143]\n\t"
"v_pk_add_f32 v[124:125], v[124:125], v[172:173]\n\t"
"v_pk_add_f32 v[126:127], v[126:127], v[174:175]\n\t"
"v_pk_fma_f32 v[172:173], v[172:173], s[38:39], v[124:125]\n\t"
"v_pk_fma_f32 v[174:175], v[174:175], s[38:39], v[126:127]\n\t"
"v_pk_add_f32 v[108:109], v[108:109], v[156:157]\n\t"
"v_pk_add_f32 v[110:111], v[110:111], v[158:159]\n\t"
"v_pk_fma_f32 v[156:157], v[156:157], s[38:39], v[108:109]\n\t"
"v_pk_fma_f32 v[158:159], v[158:159], s[38:39], v[110:111]\n\t"
"ds_read_b128 a[172:175], v216 offset:11264\n\t"
"v_mfma_f32_16x16x32_bf16 v[16:19], a[8:11], a[144:147], v[16:19]\n\t"
"v_cvt_pk_bf16_f32 v208, v208, v209\n\t"
"v_cvt_pk_bf16_f32 v209, v210, v211\n\t"
"v_mfma_f32_16x16x32_bf16 v[16:19], a[12:15], a[160:163], v[16:19]\n\t"
"ds_write_b128 v217, v[200:203] offset:16384\n\t"
"v_mfma_f32_16x16x32_bf16 v[32:35], a[16:19], a[144:147], v[32:35]\n\t"
"ds_read_b128 a[144:147], v216 offset:4096\n\t"
"v_mfma_f32_16x16x32_bf16 v[32:35], a[20:23], a[160:163], v[32:35]\n\t"
"v_cvt_pk_bf16_f32 v210, v212, v213\n\t"
"v_cvt_pk_bf16_f32 v211, v214, v215\n\t"
"v_mfma_f32_16x16x32_bf16 v[192:195], a[192:195], a[220:223], 0\n\t"
"ds_read_b128 a[160:163], v216 offset:12288\n\t"
"v_mfma_f32_16x16x32_bf16 v[36:39], a[16:19], a[148:151], v[36:39]\n\t"
"ds_read_b64_tr_b16 a[178:179], v226 offset:512\n\t"
"v_mfma_f32_16x16x32_bf16 v[36:39], a[20:23], a[164:167], v[36:39]\n\t"
"ds_write_b128 v219, v[208:211] offset:16384\n\t"
"v_mfma_f32_16x16x32_bf16 v[20:23], a[8:11], a[148:151], v[20:23]\n\t"
"ds_read_b64_tr_b16 a[180:181], v218 offset:0\n\t"
"v_mfma_f32_16x16x32_bf16 v[20:23], a[12:15], a[164:167], v[20:23]\n\t"
"v_cvt_pk_bf16_f32 v192, v192, v193\n\t"
"v_cvt_pk_bf16_f32 v193, v194, v195\n\t"
"v_mfma_f32_16x16x32_bf16 v[196:199], a[196:199], a[220:223], 0\n\t"
"ds_read_b64_tr_b16 a[182:183], v218 offset:512\n\t"
"v_mfma_f32_16x16x32_bf16 v[4:7], a[0:3], a[148:151], v[4:7]\n\t"
"ds_read_b128 a[148:151], v216 offset:5120\n\t"
"v_mfma_f32_16x16x32_bf16 v[4:7], a[4:7], a[164:167], v[4:7]\n\t"
"ds_read_b128 a[164:167], v216 offset:13312\n\t"
"v_mfma_f32_16x16x32_bf16 v[200:203], a[200:203], a[220:223], 0\n\t"
"ds_read_b64_tr_b16 a[184:185], v220 offset:2304\n\t"
"s_waitcnt lgkmcnt(8)\n\t"
"v_mfma_f32_16x16x32_bf16 v[8:11], a[0:3], a[152:155], v[8:11]\n\t"
"v_cvt_pk_bf16_f32 v194, v196, v197\n\t"
"v_cvt_pk_bf16_f32 v195, v198, v199\n\t"
"v_mfma_f32_16x16x32_bf16 v[8:11], a[4:7], a[168:171], v[8:11]\n\t"
"ds_write_b128 v217, v[192:195] offset:24576\n\t"
"v_mfma_f32_16x16x32_bf16 v[24:27], a[8:11], a[152:155], v[24:27]\n\t"
"ds_read_b64_tr_b16 a[186:187], v220 offset:2816\n\t"
"v_mfma_f32_16x16x32_bf16 v[24:27], a[12:15], a[168:171], v[24:27]\n\t"
"v_cvt_pk_bf16_f32 v200, v200, v201\n\t"
"v_cvt_pk_bf16_f32 v201, v202, v203\n\t"
"v_mfma_f32_16x16x32_bf16 v[204:207], a[204:207], a[220:223], 0\n\t"
"ds_read_b64_tr_b16 a[188:189], v255 offset:2304\n\t"
"v_mfma_f32_16x16x32_bf16 v[40:43], a[16:19], a[152:155], v[40:43]\n\t"
"ds_read_b64_tr_b16 a[190:191], v255 offset:2816\n\t"
"v_mfma_f32_16x16x32_bf16 v[40:43], a[20:23], a[168:171], v[40:43]\n\t"
"ds_read_b64_tr_b16 a[192:193], v220 offset:0\n\t"
"v_mfma_f32_16x16x32_bf16 v[44:47], a[16:19], a[156:159], v[44:47]\n\t"
"ds_read_b64_tr_b16 a[194:195], v220 offset:512\n\t"
"v_mfma_f32_16x16x32_bf16 v[44:47], a[20:23], a[172:175], v[44:47]\n\t"
"buffer_load_dwordx4 a[16:19], v235, s[8:11], s69 offen offset:0 sc1\n\t"
"buffer_load_dwordx4 a[20:23], v235, s[8:11], s69 offen offset:1024 sc1\n\t"
"v_mfma_f32_16x16x32_bf16 v[28:31], a[8:11], a[156:159], v[28:31]\n\t"
"ds_read_b64_tr_b16 a[196:197], v255 offset:0\n\t"
"v_cvt_pk_bf16_f32 v202, v204, v205\n\t"
"v_mfma_f32_16x16x32_bf16 v[28:31], a[12:15], a[172:175], v[28:31]\n\t"
"buffer_load_dwordx4 a[12:15], v248, s[8:11], s69 offen offset:3072 sc1\n\t"
"v_mfma_f32_16x16x32_bf16 v[12:15], a[0:3], a[156:159], v[12:15]\n\t"
"ds_read_b64_tr_b16 a[198:199], v255 offset:512\n\t"
"v_cvt_pk_bf16_f32 v203, v206, v207\n\t"
"v_mfma_f32_16x16x32_bf16 v[12:15], a[4:7], a[172:175], v[12:15]\n\t"
"ds_write_b128 v219, v[200:203] offset:24576\n\t"
"s_waitcnt vmcnt(27) lgkmcnt(7)\n\t"
"v_mfma_f32_16x16x32_bf16 v[96:99], a[24:27], a[144:147], v[96:99]\n\t"
"ds_read_b64_tr_b16 a[200:201], v226 offset:2304\n\t"
"v_mfma_f32_16x16x32_bf16 v[96:99], a[28:31], a[160:163], v[96:99]\n\t"
"ds_read_b64_tr_b16 a[202:203], v226 offset:2816\n\t"
"ds_read_b64_tr_b16 a[204:205], v218 offset:2304\n\t"
"ds_read_b64_tr_b16 a[206:207], v218 offset:2816\n\t"
"buffer_load_dwordx4 a[8:11], v248, s[8:11], s69 offen offset:2048 sc1\n\t"
"v_mfma_f32_16x16x32_bf16 v[112:115], a[32:35], a[144:147], v[112:115]\n\t"
"ds_read_b128 a[152:155], v216 offset:6144\n\t"
"v_mfma_f32_16x16x32_bf16 v[112:115], a[36:39], a[160:163], v[112:115]\n\t"
"buffer_load_dwordx4 a[0:3], v248, s[8:11], s69 offen offset:0 sc1\n\t"
"buffer_load_dwordx4 a[4:7], v248, s[8:11], s69 offen offset:1024 sc1\n\t"
"v_mfma_f32_16x16x32_bf16 v[128:131], a[40:43], a[144:147], v[128:131]\n\t"
"v_mfma_f32_16x16x32_bf16 v[128:131], a[44:47], a[160:163], v[128:131]\n\t"
"ds_read_b128 a[168:171], v216 offset:14336\n\t"
"v_mfma_f32_16x16x32_bf16 v[132:135], a[40:43], a[148:151], v[132:135]\n\t"
"ds_read_b128 a[156:159], v216 offset:7168\n\t"
"v_mfma_f32_16x16x32_bf16 v[132:135], a[44:47], a[164:167], v[132:135]\n\t"
"ds_read_b128 a[172:175], v216 offset:15360\n\t"
"ds_write_b128 v223, a[240:243] offset:41472\n\t"
"v_mfma_f32_16x16x32_bf16 v[116:119], a[32:35], a[148:151], v[116:119]\n\t"
"v_mfma_f32_16x16x32_bf16 v[116:119], a[36:39], a[164:167], v[116:119]\n\t"
"v_mfma_f32_16x16x32_bf16 v[100:103], a[24:27], a[148:151], v[100:103]\n\t"
"v_mfma_f32_16x16x32_bf16 v[100:103], a[28:31], a[164:167], v[100:103]\n\t"
"s_waitcnt lgkmcnt(0)\n\t"
"s_barrier\n\t"
"v_mfma_f32_16x16x32_bf16 v[104:107], a[24:27], a[152:155], v[104:107]\n\t"
"ds_read_b128 a[144:147], v216 offset:16384\n\t"
"v_add_u32 v249, 39424, v227\n\t"
"v_mfma_f32_16x16x32_bf16 v[104:107], a[28:31], a[168:171], v[104:107]\n\t"
"s_add_u32 m0, s26, 124544\n\t"
"ds_read_b128 a[160:163], v216 offset:24576\n\t"
"ds_read_b128 a[148:151], v216 offset:17408\n\t"
"buffer_load_dwordx4 v236, s[12:15], s22 offen offset:128 nt lds\n\t"
"v_mfma_f32_16x16x32_bf16 v[120:123], a[32:35], a[152:155], v[120:123]\n\t"
"ds_read_b128 a[164:167], v216 offset:25600\n\t"
"v_mfma_f32_16x16x32_bf16 v[120:123], a[36:39], a[168:171], v[120:123]\n\t"
"v_xor_b32 v250, 32, v249\n\t"
"v_mfma_f32_16x16x32_bf16 v[136:139], a[40:43], a[152:155], v[136:139]\n\t"
"v_xor_b32 v251, 64, v249\n\t"
"v_mfma_f32_16x16x32_bf16 v[136:139], a[44:47], a[168:171], v[136:139]\n\t"
"v_xor_b32 v252, 96, v249\n\t"
"ds_read_b128 a[152:155], v216 offset:18432\n\t"
"v_mfma_f32_16x16x32_bf16 v[208:211], a[176:179], a[220:223], 0\n\t"
"buffer_load_dwordx4 v237, s[12:15], s22 offen offset:1152 nt lds\n\t"
"v_mfma_f32_16x16x32_bf16 v[140:143], a[40:43], a[156:159], v[140:143]\n\t"
"ds_read_b128 a[168:171], v216 offset:26624\n\t"
"v_mfma_f32_16x16x32_bf16 v[140:143], a[44:47], a[172:175], v[140:143]\n\t"
"ds_read_b64_tr_b16 a[176:177], v249 offset:0\n\t"
"v_mfma_f32_16x16x32_bf16 v[212:215], a[180:183], a[220:223], 0\n\t"
"ds_read_b64_tr_b16 a[178:179], v249 offset:512\n\t"
"v_mfma_f32_16x16x32_bf16 v[124:127], a[32:35], a[156:159], v[124:127]\n\t"
"v_cvt_pk_bf16_f32 v208, v208, v209\n\t"
"v_cvt_pk_bf16_f32 v209, v210, v211\n\t"
"v_mfma_f32_16x16x32_bf16 v[124:127], a[36:39], a[172:175], v[124:127]\n\t"
"buffer_load_dwordx4 v238, s[12:15], s22 offen offset:2176 nt lds\n\t"
"v_mfma_f32_16x16x32_bf16 v[108:111], a[24:27], a[156:159], v[108:111]\n\t"
"ds_read_b128 a[156:159], v216 offset:19456\n\t"
"v_mfma_f32_16x16x32_bf16 v[108:111], a[28:31], a[172:175], v[108:111]\n\t"
"v_cvt_pk_bf16_f32 v210, v212, v213\n\t"
"v_cvt_pk_bf16_f32 v211, v214, v215\n\t"
"v_mfma_f32_16x16x32_bf16 v[192:195], a[184:187], a[220:223], 0\n\t"
"ds_read_b128 a[172:175], v216 offset:27648\n\t"
"s_waitcnt vmcnt(27) lgkmcnt(6)\n\t"
"v_mfma_f32_16x16x32_bf16 v[48:51], a[48:51], a[144:147], v[48:51]\n\t"
"ds_read_b64_tr_b16 a[180:181], v250 offset:0\n\t"
"v_mfma_f32_16x16x32_bf16 v[48:51], a[52:55], a[160:163], v[48:51]\n\t"
"ds_write_b128 v217, v[208:211] offset:0\n\t"
"v_mfma_f32_16x16x32_bf16 v[196:199], a[188:191], a[220:223], 0\n\t"
"buffer_load_dwordx4 v239, s[12:15], s22 offen offset:3200 nt lds\n\t"
"v_mfma_f32_16x16x32_bf16 v[64:67], a[56:59], a[144:147], v[64:67]\n\t"
"v_cvt_pk_bf16_f32 v192, v192, v193\n\t"
"v_mfma_f32_16x16x32_bf16 v[64:67], a[60:63], a[160:163], v[64:67]\n\t"
"v_cvt_pk_bf16_f32 v193, v194, v195\n\t"
"ds_read_b64_tr_b16 a[182:183], v250 offset:512\n\t"
"v_mfma_f32_16x16x32_bf16 v[80:83], a[64:67], a[144:147], v[80:83]\n\t"
"ds_read_b128 a[144:147], v216 offset:20480\n\t"
"v_mfma_f32_16x16x32_bf16 v[80:83], a[68:71], a[160:163], v[80:83]\n\t"
"v_cvt_pk_bf16_f32 v194, v196, v197\n\t"
"v_cvt_pk_bf16_f32 v195, v198, v199\n\t"
"v_mfma_f32_16x16x32_bf16 v[200:203], a[192:195], a[220:223], 0\n\t"
"s_mov_b64 exec, s[36:37]\n\t"
"buffer_load_dwordx4 v240, s[12:15], s22 offen offset:3712 nt lds\n\t"
"s_mov_b64 exec, -1\n\t"
"s_add_u32 m0, s26, 145280\n\t"
"v_mfma_f32_16x16x32_bf16 v[84:87], a[64:67], a[148:151], v[84:87]\n\t"
"ds_read_b128 a[160:163], v216 offset:28672\n\t"
"v_mfma_f32_16x16x32_bf16 v[84:87], a[68:71], a[164:167], v[84:87]\n\t"
"ds_read_b64_tr_b16 a[184:185], v251 offset:0\n\t"
"v_mfma_f32_16x16x32_bf16 v[68:71], a[56:59], a[148:151], v[68:71]\n\t"
"ds_read_b64_tr_b16 a[186:187], v251 offset:512\n\t"
"v_mfma_f32_16x16x32_bf16 v[68:71], a[60:63], a[164:167], v[68:71]\n\t"
"ds_write_b128 v219, v[192:195] offset:0\n\t"
"v_mfma_f32_16x16x32_bf16 v[204:207], a[196:199], a[220:223], 0\n\t"
"v_cvt_pk_bf16_f32 v200, v200, v201\n\t"
"v_cvt_pk_bf16_f32 v201, v202, v203\n\t"
"v_mfma_f32_16x16x32_bf16 v[52:55], a[48:51], a[148:151], v[52:55]\n\t"
"ds_read_b128 a[148:151], v216 offset:21504\n\t"
"v_mfma_f32_16x16x32_bf16 v[52:55], a[52:55], a[164:167], v[52:55]\n\t"
"buffer_load_dwordx4 v241, s[12:15], s22 offen offset:128 nt lds\n\t"
"v_mfma_f32_16x16x32_bf16 v[208:211], a[200:203], a[220:223], 0\n\t"
"ds_read_b128 a[164:167], v216 offset:29696\n\t"
"s_waitcnt lgkmcnt(6)\n\t"
"v_mfma_f32_16x16x32_bf16 v[56:59], a[48:51], a[152:155], v[56:59]\n\t"
"v_cvt_pk_bf16_f32 v202, v204, v205\n\t"
"v_cvt_pk_bf16_f32 v203, v206, v207\n\t"
"v_mfma_f32_16x16x32_bf16 v[56:59], a[52:55], a[168:171], v[56:59]\n\t"
"ds_read_b64_tr_b16 a[188:189], v252 offset:0\n\t"
"v_mfma_f32_16x16x32_bf16 v[72:75], a[56:59], a[152:155], v[72:75]\n\t"
"ds_read_b64_tr_b16 a[190:191], v252 offset:512\n\t"
"v_mfma_f32_16x16x32_bf16 v[72:75], a[60:63], a[168:171], v[72:75]\n\t"
"buffer_load_dwordx4 v242, s[12:15], s22 offen offset:1152 nt lds\n\t"
"v_mfma_f32_16x16x32_bf16 v[212:215], a[204:207], a[220:223], 0\n\t"
"v_cvt_pk_bf16_f32 v208, v208, v209\n\t"
"v_cvt_pk_bf16_f32 v209, v210, v211\n\t"
"v_mfma_f32_16x16x32_bf16 v[88:91], a[64:67], a[152:155], v[88:91]\n\t"
"ds_read_b64_tr_b16 a[192:193], v249 offset:1152\n\t"
"v_mfma_f32_16x16x32_bf16 v[88:91], a[68:71], a[168:171], v[88:91]\n\t"
"ds_write_b128 v217, v[200:203] offset:8192\n\t"
"v_mfma_f32_16x16x32_bf16 v[92:95], a[64:67], a[156:159], v[92:95]\n\t"
"ds_read_b64_tr_b16 a[194:195], v249 offset:1664\n\t"
"v_mfma_f32_16x16x32_bf16 v[92:95], a[68:71], a[172:175], v[92:95]\n\t"
"buffer_load_dwordx4 v243, s[12:15], s22 offen offset:2176 nt lds\n\t"
"v_mfma_f32_16x16x32_bf16 v[76:79], a[56:59], a[156:159], v[76:79]\n\t"
"ds_read_b64_tr_b16 a[196:197], v250 offset:1152\n\t"
"v_mfma_f32_16x16x32_bf16 v[76:79], a[60:63], a[172:175], v[76:79]\n\t"
"ds_read_b64_tr_b16 a[198:199], v250 offset:1664\n\t"
"v_cvt_pk_bf16_f32 v210, v212, v213\n\t"
"v_mfma_f32_16x16x32_bf16 v[60:63], a[48:51], a[156:159], v[60:63]\n\t"
"ds_read_b64_tr_b16 a[200:201], v251 offset:1152\n\t"
"v_mfma_f32_16x16x32_bf16 v[60:63], a[52:55], a[172:175], v[60:63]\n\t"
"ds_read_b64_tr_b16 a[202:203], v251 offset:1664\n\t"
"v_cvt_pk_bf16_f32 v211, v214, v215\n\t"
"s_waitcnt vmcnt(26) lgkmcnt(7)\n\t"
"v_mfma_f32_16x16x32_bf16 v[144:147], a[72:75], a[144:147], v[144:147]\n\t"
"ds_read_b64_tr_b16 a[204:205], v252 offset:1152\n\t"
"v_mfma_f32_16x16x32_bf16 v[144:147], a[76:79], a[160:163], v[144:147]\n\t"
"ds_write_b128 v219, v[208:211] offset:8192\n\t"
"ds_read_b64_tr_b16 a[206:207], v252 offset:1664\n\t"
"buffer_load_dwordx4 v244, s[12:15], s22 offen offset:3200 nt lds\n\t"
"v_mfma_f32_16x16x32_bf16 v[160:163], a[80:83], a[144:147], v[160:163]\n\t"
"v_mfma_f32_16x16x32_bf16 v[160:163], a[84:87], a[160:163], v[160:163]\n\t"
"v_mfma_f32_16x16x32_bf16 v[176:179], a[88:91], a[144:147], v[176:179]\n\t"
"ds_read_b128 a[152:155], v216 offset:22528\n\t"
"v_mfma_f32_16x16x32_bf16 v[176:179], a[92:95], a[160:163], v[176:179]\n\t"
"s_mov_b64 exec, s[36:37]\n\t"
"buffer_load_dwordx4 v245, s[12:15], s22 offen offset:3712 nt lds\n\t"
"s_mov_b64 exec, -1\n\t"
"v_mfma_f32_16x16x32_bf16 v[180:183], a[88:91], a[148:151], v[180:183]\n\t"
"ds_read_b128 a[168:171], v216 offset:30720\n\t"
"v_mfma_f32_16x16x32_bf16 v[180:183], a[92:95], a[164:167], v[180:183]\n\t"
"ds_read_b128 a[156:159], v216 offset:23552\n\t"
"ds_read_b128 a[172:175], v216 offset:31744\n\t"
"v_mfma_f32_16x16x32_bf16 v[164:167], a[80:83], a[148:151], v[164:167]\n\t"
"v_mfma_f32_16x16x32_bf16 v[164:167], a[84:87], a[164:167], v[164:167]\n\t"
"v_mfma_f32_16x16x32_bf16 v[148:151], a[72:75], a[148:151], v[148:151]\n\t"
"v_mfma_f32_16x16x32_bf16 v[148:151], a[76:79], a[164:167], v[148:151]\n\t"
"s_waitcnt lgkmcnt(0)\n\t"
"s_barrier\n\t"
"v_mfma_f32_16x16x32_bf16 v[152:155], a[72:75], a[152:155], v[152:155]\n\t"
"ds_read_b128 a[144:147], v216 offset:0\n\t"
"v_mfma_f32_16x16x32_bf16 v[152:155], a[76:79], a[168:171], v[152:155]\n\t"
"ds_read_b128 a[160:163], v216 offset:8192\n\t"
"ds_read_b128 a[148:151], v216 offset:1024\n\t"
"v_mfma_f32_16x16x32_bf16 v[168:171], a[80:83], a[152:155], v[168:171]\n\t"
"ds_read_b128 a[164:167], v216 offset:9216\n\t"
"v_mfma_f32_16x16x32_bf16 v[168:171], a[84:87], a[168:171], v[168:171]\n\t"
"ds_read_b128 a[248:251], v222 offset:18432\n\t"
"v_mfma_f32_16x16x32_bf16 v[184:187], a[88:91], a[152:155], v[184:187]\n\t"
"ds_read_b128 a[152:155], v216 offset:2048\n\t"
"v_mfma_f32_16x16x32_bf16 v[184:187], a[92:95], a[168:171], v[184:187]\n\t"
"ds_read_b128 a[168:171], v216 offset:10240\n\t"
"v_mfma_f32_16x16x32_bf16 v[192:195], a[176:179], a[224:227], 0\n\t"
"ds_read_b64_tr_b16 a[176:177], v220 offset:20736\n\t"
"v_mfma_f32_16x16x32_bf16 v[188:191], a[88:91], a[156:159], v[188:191]\n\t"
"ds_read_b64_tr_b16 a[178:179], v220 offset:21248\n\t"
"v_mfma_f32_16x16x32_bf16 v[188:191], a[92:95], a[172:175], v[188:191]\n\t"
"v_mfma_f32_16x16x32_bf16 v[196:199], a[180:183], a[224:227], 0\n\t"
"ds_read_b64_tr_b16 a[180:181], v255 offset:20736\n\t"
"v_mfma_f32_16x16x32_bf16 v[172:175], a[80:83], a[156:159], v[172:175]\n\t"
"s_nop 0\n\t"
"v_cvt_pk_bf16_f32 v192, v192, v193\n\t"
"v_cvt_pk_bf16_f32 v193, v194, v195\n\t"
"v_mfma_f32_16x16x32_bf16 v[172:175], a[84:87], a[172:175], v[172:175]\n\t"
"ds_read_b64_tr_b16 a[182:183], v255 offset:21248\n\t"
"v_mfma_f32_16x16x32_bf16 v[156:159], a[72:75], a[156:159], v[156:159]\n\t"
"ds_read_b128 a[156:159], v216 offset:3072\n\t"
"v_mfma_f32_16x16x32_bf16 v[156:159], a[76:79], a[172:175], v[156:159]\n\t"
"v_cvt_pk_bf16_f32 v194, v196, v197\n\t"
"v_cvt_pk_bf16_f32 v195, v198, v199\n\t"
"v_mfma_f32_16x16x32_bf16 v[200:203], a[184:187], a[224:227], 0\n\t"
"ds_read_b128 a[172:175], v216 offset:11264\n\t"
"s_waitcnt vmcnt(22) lgkmcnt(7)\n\t"
"v_mfma_f32_16x16x32_bf16 v[0:3], a[96:99], a[144:147], v[0:3]\n\t"
"ds_read_b64_tr_b16 a[184:185], v226 offset:23040\n\t"
"v_mfma_f32_16x16x32_bf16 v[0:3], a[100:103], a[160:163], v[0:3]\n\t"
"ds_write_b128 v221, v[192:195] offset:16384\n\t"
"v_mfma_f32_16x16x32_bf16 v[204:207], a[188:191], a[224:227], 0\n\t"
"ds_read_b64_tr_b16 a[186:187], v226 offset:23552\n\t"
"v_mfma_f32_16x16x32_bf16 v[16:19], a[104:107], a[144:147], v[16:19]\n\t"
"v_cvt_pk_bf16_f32 v200, v200, v201\n\t"
"v_cvt_pk_bf16_f32 v201, v202, v203\n\t"
"v_mfma_f32_16x16x32_bf16 v[16:19], a[108:111], a[160:163], v[16:19]\n\t"
"ds_read_b64_tr_b16 a[188:189], v218 offset:23040\n\t"
"v_mfma_f32_16x16x32_bf16 v[32:35], a[112:115], a[144:147], v[32:35]\n\t"
"ds_read_b128 a[144:147], v216 offset:4096\n\t"
"v_mfma_f32_16x16x32_bf16 v[32:35], a[116:119], a[160:163], v[32:35]\n\t"
"v_cvt_pk_bf16_f32 v202, v204, v205\n\t"
"v_mfma_f32_16x16x32_bf16 v[208:211], a[192:195], a[224:227], 0\n\t"
"v_cvt_pk_bf16_f32 v203, v206, v207\n\t"
"v_mfma_f32_16x16x32_bf16 v[36:39], a[112:115], a[148:151], v[36:39]\n\t"
"ds_read_b128 a[160:163], v216 offset:12288\n\t"
"v_mfma_f32_16x16x32_bf16 v[36:39], a[116:119], a[164:167], v[36:39]\n\t"
"ds_read_b64_tr_b16 a[190:191], v218 offset:23552\n\t"
"v_mfma_f32_16x16x32_bf16 v[20:23], a[104:107], a[148:151], v[20:23]\n\t"
"ds_read_b64_tr_b16 a[192:193], v226 offset:20736\n\t"
"v_mfma_f32_16x16x32_bf16 v[20:23], a[108:111], a[164:167], v[20:23]\n\t"
"ds_write_b128 v221, v[200:203] offset:24576\n\t"
"v_mfma_f32_16x16x32_bf16 v[212:215], a[196:199], a[224:227], 0\n\t"
"v_cvt_pk_bf16_f32 v208, v208, v209\n\t"
"v_mfma_f32_16x16x32_bf16 v[4:7], a[96:99], a[148:151], v[4:7]\n\t"
"v_cvt_pk_bf16_f32 v209, v210, v211\n\t"
"v_mfma_f32_16x16x32_bf16 v[4:7], a[100:103], a[164:167], v[4:7]\n\t"
"ds_read_b128 a[148:151], v216 offset:5120\n\t"
"v_mfma_f32_16x16x32_bf16 v[192:195], a[200:203], a[224:227], 0\n\t"
"ds_read_b128 a[164:167], v216 offset:13312\n\t"
"s_waitcnt lgkmcnt(6)\n\t"
"v_mfma_f32_16x16x32_bf16 v[8:11], a[96:99], a[152:155], v[8:11]\n\t"
"v_cvt_pk_bf16_f32 v210, v212, v213\n\t"
"v_mfma_f32_16x16x32_bf16 v[8:11], a[100:103], a[168:171], v[8:11]\n\t"
"buffer_load_dwordx4 a[40:43], v235, s[8:11], s81 offen offset:0 sc1\n\t"
"v_mfma_f32_16x16x32_bf16 v[24:27], a[104:107], a[152:155], v[24:27]\n\t"
"v_mfma_f32_16x16x32_bf16 v[24:27], a[108:111], a[168:171], v[24:27]\n\t"
"v_cvt_pk_bf16_f32 v211, v214, v215\n\t"
"v_mfma_f32_16x16x32_bf16 v[196:199], a[204:207], a[224:227], 0\n\t"
"v_cvt_pk_bf16_f32 v192, v192, v193\n\t"
"v_cvt_pk_bf16_f32 v193, v194, v195\n\t"
"v_mfma_f32_16x16x32_bf16 v[40:43], a[112:115], a[152:155], v[40:43]\n\t"
"ds_read_b64_tr_b16 a[194:195], v226 offset:21248\n\t"
"v_mfma_f32_16x16x32_bf16 v[40:43], a[116:119], a[168:171], v[40:43]\n\t"
"ds_read_b64_tr_b16 a[196:197], v218 offset:20736\n\t"
"v_mfma_f32_16x16x32_bf16 v[44:47], a[112:115], a[156:159], v[44:47]\n\t"
"ds_read_b64_tr_b16 a[198:199], v218 offset:21248\n\t"
"v_mfma_f32_16x16x32_bf16 v[44:47], a[116:119], a[172:175], v[44:47]\n\t"
"ds_read_b64_tr_b16 a[200:201], v220 offset:23040\n\t"
"v_mfma_f32_16x16x32_bf16 v[28:31], a[104:107], a[156:159], v[28:31]\n\t"
"ds_read_b64_tr_b16 a[202:203], v220 offset:23552\n\t"
"v_mfma_f32_16x16x32_bf16 v[28:31], a[108:111], a[172:175], v[28:31]\n\t"
"ds_write_b128 v221, v[208:211] offset:20480\n\t"
"v_mfma_f32_16x16x32_bf16 v[12:15], a[96:99], a[156:159], v[12:15]\n\t"
"v_mfma_f32_16x16x32_bf16 v[12:15], a[100:103], a[172:175], v[12:15]\n\t"
"ds_read_b64_tr_b16 a[204:205], v255 offset:23040\n\t"
"s_waitcnt vmcnt(17) lgkmcnt(7)\n\t"
"v_mfma_f32_16x16x32_bf16 v[96:99], a[120:123], a[144:147], v[96:99]\n\t"
"ds_read_b64_tr_b16 a[206:207], v255 offset:23552\n\t"
"v_cvt_pk_bf16_f32 v194, v196, v197\n\t"
"v_mfma_f32_16x16x32_bf16 v[96:99], a[124:127], a[160:163], v[96:99]\n\t"
"v_cvt_pk_bf16_f32 v195, v198, v199\n\t"
"v_mfma_f32_16x16x32_bf16 v[112:115], a[128:131], a[144:147], v[112:115]\n\t"
"ds_read_b128 a[152:155], v216 offset:6144\n\t"
"v_mfma_f32_16x16x32_bf16 v[112:115], a[132:135], a[160:163], v[112:115]\n\t"
"ds_write_b128 v221, v[192:195] offset:28672\n\t"
"v_mfma_f32_16x16x32_bf16 v[128:131], a[136:139], a[144:147], v[128:131]\n\t"
"v_mfma_f32_16x16x32_bf16 v[128:131], a[140:143], a[160:163], v[128:131]\n\t"
"ds_read_b128 a[168:171], v216 offset:14336\n\t"
"v_mfma_f32_16x16x32_bf16 v[132:135], a[136:139], a[148:151], v[132:135]\n\t"
"ds_read_b128 a[156:159], v216 offset:7168\n\t"
"v_mfma_f32_16x16x32_bf16 v[132:135], a[140:143], a[164:167], v[132:135]\n\t"
"ds_read_b128 a[172:175], v216 offset:15360\n\t"
"v_mfma_f32_16x16x32_bf16 v[116:119], a[128:131], a[148:151], v[116:119]\n\t"
"v_mfma_f32_16x16x32_bf16 v[116:119], a[132:135], a[164:167], v[116:119]\n\t"
"v_mfma_f32_16x16x32_bf16 v[100:103], a[120:123], a[148:151], v[100:103]\n\t"
"v_mfma_f32_16x16x32_bf16 v[100:103], a[124:127], a[164:167], v[100:103]\n\t"
"s_waitcnt lgkmcnt(0)\n\t"
"s_barrier\n\t"
"v_mfma_f32_16x16x32_bf16 v[104:107], a[120:123], a[152:155], v[104:107]\n\t"
"ds_read_b128 a[144:147], v216 offset:16384\n\t"
"v_mfma_f32_16x16x32_bf16 v[104:107], a[124:127], a[168:171], v[104:107]\n\t"
"ds_read_b128 a[160:163], v216 offset:24576\n\t"
"ds_read_b128 a[148:151], v216 offset:17408\n\t"
"v_mfma_f32_16x16x32_bf16 v[120:123], a[128:131], a[152:155], v[120:123]\n\t"
"ds_read_b128 a[164:167], v216 offset:25600\n\t"
"v_mfma_f32_16x16x32_bf16 v[120:123], a[132:135], a[168:171], v[120:123]\n\t"
"buffer_load_dwordx4 a[44:47], v235, s[8:11], s81 offen offset:1024 sc1\n\t"
"v_mfma_f32_16x16x32_bf16 v[136:139], a[136:139], a[152:155], v[136:139]\n\t"
"ds_read_b128 a[152:155], v216 offset:18432\n\t"
"v_mfma_f32_16x16x32_bf16 v[136:139], a[140:143], a[168:171], v[136:139]\n\t"
"buffer_load_dwordx4 a[36:39], v248, s[8:11], s81 offen offset:3072 sc1\n\t"
"buffer_load_dwordx4 a[32:35], v248, s[8:11], s81 offen offset:2048 sc1\n\t"
"v_mfma_f32_16x16x32_bf16 v[200:203], a[176:179], a[220:223], 0\n\t"
"buffer_load_dwordx4 a[24:27], v248, s[8:11], s81 offen offset:0 sc1\n\t"
"v_mfma_f32_16x16x32_bf16 v[140:143], a[136:139], a[156:159], v[140:143]\n\t"
"ds_read_b128 a[168:171], v216 offset:26624\n\t"
"v_mfma_f32_16x16x32_bf16 v[140:143], a[140:143], a[172:175], v[140:143]\n\t"
"buffer_load_dwordx4 a[28:31], v248, s[8:11], s81 offen offset:1024 sc1\n\t"
"ds_read_b64_tr_b16 a[176:177], v251 offset:20736\n\t"
"ds_read_b64_tr_b16 a[178:179], v251 offset:21248\n\t"
"v_mfma_f32_16x16x32_bf16 v[204:207], a[180:183], a[220:223], 0\n\t"
"ds_read_b64_tr_b16 a[180:181], v252 offset:20736\n\t"
"v_mfma_f32_16x16x32_bf16 v[124:127], a[128:131], a[156:159], v[124:127]\n\t"
"v_cvt_pk_bf16_f32 v200, v200, v201\n\t"
"v_cvt_pk_bf16_f32 v201, v202, v203\n\t"
"v_mfma_f32_16x16x32_bf16 v[124:127], a[132:135], a[172:175], v[124:127]\n\t"
"buffer_load_dwordx4 a[64:67], v235, s[8:11], s77 offen offset:2048 sc1\n\t"
"v_mfma_f32_16x16x32_bf16 v[108:111], a[120:123], a[156:159], v[108:111]\n\t"
"ds_read_b128 a[156:159], v216 offset:19456\n\t"
"v_mfma_f32_16x16x32_bf16 v[108:111], a[124:127], a[172:175], v[108:111]\n\t"
"buffer_load_dwordx4 a[68:71], v235, s[8:11], s77 offen offset:3072 sc1\n\t"
"v_cvt_pk_bf16_f32 v202, v204, v205\n\t"
"v_cvt_pk_bf16_f32 v203, v206, v207\n\t"
"v_mfma_f32_16x16x32_bf16 v[208:211], a[184:187], a[220:223], 0\n\t"
"v_pk_add_f32 v[0:1], v[0:1], v[96:97]\n\t"
"v_pk_add_f32 v[2:3], v[2:3], v[98:99]\n\t"
"v_pk_fma_f32 v[96:97], v[96:97], s[38:39], v[0:1]\n\t"
"v_pk_fma_f32 v[98:99], v[98:99], s[38:39], v[2:3]\n\t"
"v_pk_add_f32 v[16:17], v[16:17], v[112:113]\n\t"
"v_pk_add_f32 v[18:19], v[18:19], v[114:115]\n\t"
"v_pk_fma_f32 v[112:113], v[112:113], s[38:39], v[16:17]\n\t"
"v_pk_fma_f32 v[114:115], v[114:115], s[38:39], v[18:19]\n\t"
"v_pk_add_f32 v[32:33], v[32:33], v[128:129]\n\t"
"v_pk_add_f32 v[34:35], v[34:35], v[130:131]\n\t"
"v_pk_fma_f32 v[128:129], v[128:129], s[38:39], v[32:33]\n\t"
"v_pk_fma_f32 v[130:131], v[130:131], s[38:39], v[34:35]\n\t"
"v_pk_add_f32 v[36:37], v[36:37], v[132:133]\n\t"
"v_pk_add_f32 v[38:39], v[38:39], v[134:135]\n\t"
"v_pk_fma_f32 v[132:133], v[132:133], s[38:39], v[36:37]\n\t"
"v_pk_fma_f32 v[134:135], v[134:135], s[38:39], v[38:39]\n\t"
"v_pk_add_f32 v[20:21], v[20:21], v[116:117]\n\t"
"v_pk_add_f32 v[22:23], v[22:23], v[118:119]\n\t"
"v_pk_fma_f32 v[116:117], v[116:117], s[38:39], v[20:21]\n\t"
"v_pk_fma_f32 v[118:119], v[118:119], s[38:39], v[22:23]\n\t"
"v_pk_add_f32 v[4:5], v[4:5], v[100:101]\n\t"
"v_pk_add_f32 v[6:7], v[6:7], v[102:103]\n\t"
"v_pk_fma_f32 v[100:101], v[100:101], s[38:39], v[4:5]\n\t"
"v_pk_fma_f32 v[102:103], v[102:103], s[38:39], v[6:7]\n\t"
"v_pk_add_f32 v[8:9], v[8:9], v[104:105]\n\t"
"v_pk_add_f32 v[10:11], v[10:11], v[106:107]\n\t"
"v_pk_fma_f32 v[104:105], v[104:105], s[38:39], v[8:9]\n\t"
"v_pk_fma_f32 v[106:107], v[106:107], s[38:39], v[10:11]\n\t"
"v_pk_add_f32 v[24:25], v[24:25], v[120:121]\n\t"
"v_pk_add_f32 v[26:27], v[26:27], v[122:123]\n\t"
"v_pk_fma_f32 v[120:121], v[120:121], s[38:39], v[24:25]\n\t"
"v_pk_fma_f32 v[122:123], v[122:123], s[38:39], v[26:27]\n\t"
"v_pk_add_f32 v[40:41], v[40:41], v[136:137]\n\t"
"v_pk_add_f32 v[42:43], v[42:43], v[138:139]\n\t"
"v_pk_fma_f32 v[136:137], v[136:137], s[38:39], v[40:41]\n\t"
"v_pk_fma_f32 v[138:139], v[138:139], s[38:39], v[42:43]\n\t"
"v_pk_add_f32 v[44:45], v[44:45], v[140:141]\n\t"
"v_pk_add_f32 v[46:47], v[46:47], v[142:143]\n\t"
"v_pk_fma_f32 v[140:141], v[140:141], s[38:39], v[44:45]\n\t"
"v_pk_fma_f32 v[142:143], v[142:143], s[38:39], v[46:47]\n\t"
"v_pk_add_f32 v[28:29], v[28:29], v[124:125]\n\t"
"v_pk_add_f32 v[30:31], v[30:31], v[126:127]\n\t"
"v_pk_fma_f32 v[124:125], v[124:125], s[38:39], v[28:29]\n\t"
"v_pk_fma_f32 v[126:127], v[126:127], s[38:39], v[30:31]\n\t"
"v_pk_add_f32 v[12:13], v[12:13], v[108:109]\n\t"
"v_pk_add_f32 v[14:15], v[14:15], v[110:111]\n\t"
"v_pk_fma_f32 v[108:109], v[108:109], s[38:39], v[12:13]\n\t"
"v_pk_fma_f32 v[110:111], v[110:111], s[38:39], v[14:15]\n\t"
"buffer_load_dwordx4 a[60:63], v235, s[8:11], s77 offen offset:1024 sc1\n\t"
"s_waitcnt vmcnt(19) lgkmcnt(5)\n\t"
"v_mfma_f32_16x16x32_bf16 v[0:3], a[0:3], a[144:147], v[0:3]\n\t"
"ds_read_b128 a[172:175], v216 offset:27648\n\t"
"v_mfma_f32_16x16x32_bf16 v[0:3], a[4:7], a[160:163], v[0:3]\n\t"
"buffer_load_dwordx4 a[56:59], v235, s[8:11], s77 offen offset:0 sc1\n\t"
"buffer_load_dwordx4 a[48:51], v248, s[8:11], s77 offen offset:2048 sc1\n\t"
"v_mfma_f32_16x16x32_bf16 v[212:215], a[188:191], a[220:223], 0\n\t"
"buffer_load_dwordx4 a[52:55], v248, s[8:11], s77 offen offset:3072 sc1\n\t"
"v_mfma_f32_16x16x32_bf16 v[16:19], a[8:11], a[144:147], v[16:19]\n\t"
"v_cvt_pk_bf16_f32 v208, v208, v209\n\t"
"v_cvt_pk_bf16_f32 v209, v210, v211\n\t"
"v_mfma_f32_16x16x32_bf16 v[16:19], a[12:15], a[160:163], v[16:19]\n\t"
"buffer_load_dwordx4 a[88:91], v235, s[8:11], s80 offen offset:2048 sc1\n\t"
"v_mfma_f32_16x16x32_bf16 v[32:35], a[16:19], a[144:147], v[32:35]\n\t"
"ds_read_b128 a[144:147], v216 offset:20480\n\t"
"v_mfma_f32_16x16x32_bf16 v[32:35], a[20:23], a[160:163], v[32:35]\n\t"
"buffer_load_dwordx4 a[92:95], v235, s[8:11], s80 offen offset:3072 sc1\n\t"
"ds_write_b128 v217, v[200:203] offset:0\n\t"
"v_mfma_f32_16x16x32_bf16 v[192:195], a[192:195], a[220:223], 0\n\t"
"v_cvt_pk_bf16_f32 v210, v212, v213\n\t"
"v_cvt_pk_bf16_f32 v211, v214, v215\n\t"
"v_mfma_f32_16x16x32_bf16 v[36:39], a[16:19], a[148:151], v[36:39]\n\t"
"ds_read_b128 a[160:163], v216 offset:28672\n\t"
"v_mfma_f32_16x16x32_bf16 v[36:39], a[20:23], a[164:167], v[36:39]\n\t"
"buffer_load_dwordx4 a[84:87], v235, s[8:11], s80 offen offset:1024 sc1\n\t"
"ds_write_b128 v219, v[208:211] offset:0\n\t"
"v_mfma_f32_16x16x32_bf16 v[20:23], a[8:11], a[148:151], v[20:23]\n\t"
"v_cvt_pk_bf16_f32 v0, v0, v1\n\t"
"v_cvt_pk_bf16_f32 v1, v2, v3\n\t"
"v_mfma_f32_16x16x32_bf16 v[20:23], a[12:15], a[164:167], v[20:23]\n\t"
"v_cvt_pk_bf16_f32 v16, v16, v17\n\t"
"v_cvt_pk_bf16_f32 v17, v18, v19\n\t"
"v_mfma_f32_16x16x32_bf16 v[196:199], a[196:199], a[220:223], 0\n\t"
"v_cvt_pk_bf16_f32 v192, v192, v193\n\t"
"v_cvt_pk_bf16_f32 v193, v194, v195\n\t"
"v_mfma_f32_16x16x32_bf16 v[4:7], a[0:3], a[148:151], v[4:7]\n\t"
"v_cvt_pk_bf16_f32 v32, v32, v33\n\t"
"v_cvt_pk_bf16_f32 v33, v34, v35\n\t"
"v_mfma_f32_16x16x32_bf16 v[4:7], a[4:7], a[164:167], v[4:7]\n\t"
"buffer_load_dwordx4 a[80:83], v235, s[8:11], s80 offen offset:0 sc1\n\t"
"v_mfma_f32_16x16x32_bf16 v[200:203], a[200:203], a[220:223], 0\n\t"
"ds_read_b128 a[148:151], v216 offset:21504\n\t"
"ds_read_b128 a[164:167], v216 offset:29696\n\t"
"s_waitcnt lgkmcnt(5)\n\t"
"v_mfma_f32_16x16x32_bf16 v[8:11], a[0:3], a[152:155], v[8:11]\n\t"
"v_cvt_pk_bf16_f32 v194, v196, v197\n\t"
"v_cvt_pk_bf16_f32 v195, v198, v199\n\t"
"v_mfma_f32_16x16x32_bf16 v[8:11], a[4:7], a[168:171], v[8:11]\n\t"
"buffer_load_dwordx4 a[72:75], v248, s[8:11], s80 offen offset:2048 sc1\n\t"
"ds_write_b128 v217, v[192:195] offset:8192\n\t"
"v_mfma_f32_16x16x32_bf16 v[24:27], a[8:11], a[152:155], v[24:27]\n\t"
"v_cvt_pk_bf16_f32 v4, v4, v5\n\t"
"v_cvt_pk_bf16_f32 v5, v6, v7\n\t"
"v_mfma_f32_16x16x32_bf16 v[24:27], a[12:15], a[168:171], v[24:27]\n\t"
"ds_write2st64_b64 v228, v[0:1], v[4:5] offset0:0 offset1:13\n\t"
"v_mfma_f32_16x16x32_bf16 v[204:207], a[204:207], a[220:223], 0\n\t"
"buffer_load_dwordx4 a[76:79], v248, s[8:11], s80 offen offset:3072 sc1\n\t"
"v_mfma_f32_16x16x32_bf16 v[40:43], a[16:19], a[152:155], v[40:43]\n\t"
"v_cvt_pk_bf16_f32 v20, v20, v21\n\t"
"v_cvt_pk_bf16_f32 v21, v22, v23\n\t"
"v_mfma_f32_16x16x32_bf16 v[40:43], a[20:23], a[168:171], v[40:43]\n\t"
"v_cvt_pk_bf16_f32 v200, v200, v201\n\t"
"v_cvt_pk_bf16_f32 v201, v202, v203\n\t"
"v_mfma_f32_16x16x32_bf16 v[44:47], a[16:19], a[156:159], v[44:47]\n\t"
"v_cvt_pk_bf16_f32 v36, v36, v37\n\t"
"v_cvt_pk_bf16_f32 v37, v38, v39\n\t"
"v_mfma_f32_16x16x32_bf16 v[44:47], a[20:23], a[172:175], v[44:47]\n\t"
"ds_write2st64_b64 v246, v[16:17], v[20:21] offset0:0 offset1:13\n\t"
"buffer_load_dwordx4 a[112:115], v235, s[8:11], s74 offen offset:2048 sc1\n\t"
"v_mfma_f32_16x16x32_bf16 v[28:31], a[8:11], a[156:159], v[28:31]\n\t"
"ds_read_b64_tr_b16 a[182:183], v252 offset:21248\n\t"
"v_cvt_pk_bf16_f32 v202, v204, v205\n\t"
"v_mfma_f32_16x16x32_bf16 v[28:31], a[12:15], a[172:175], v[28:31]\n\t"
"ds_read_b64_tr_b16 a[184:185], v249 offset:20736\n\t"
"v_cvt_pk_bf16_f32 v203, v206, v207\n\t"
"v_mfma_f32_16x16x32_bf16 v[12:15], a[0:3], a[156:159], v[12:15]\n\t"
"ds_read_b64_tr_b16 a[186:187], v249 offset:21248\n\t"
"v_mfma_f32_16x16x32_bf16 v[12:15], a[4:7], a[172:175], v[12:15]\n\t"
"ds_write_b128 v219, v[200:203] offset:8192\n\t"
"s_waitcnt vmcnt(13) lgkmcnt(6)\n\t"
"v_mfma_f32_16x16x32_bf16 v[96:99], a[24:27], a[144:147], v[96:99]\n\t"
"ds_read_b64_tr_b16 a[188:189], v250 offset:20736\n\t"
"v_mfma_f32_16x16x32_bf16 v[96:99], a[28:31], a[160:163], v[96:99]\n\t"
"ds_read_b64_tr_b16 a[190:191], v250 offset:21248\n\t"
"ds_read_b64_tr_b16 a[192:193], v251 offset:21888\n\t"
"ds_read_b64_tr_b16 a[194:195], v251 offset:22400\n\t"
"ds_read_b64_tr_b16 a[196:197], v252 offset:21888\n\t"
"ds_read_b64_tr_b16 a[198:199], v252 offset:22400\n\t"
"ds_read_b64_tr_b16 a[200:201], v249 offset:21888\n\t"
"ds_read_b64_tr_b16 a[202:203], v249 offset:22400\n\t"
"ds_read_b64_tr_b16 a[204:205], v250 offset:21888\n\t"
"ds_read_b64_tr_b16 a[206:207], v250 offset:22400\n\t"
"ds_write2st64_b64 v247, v[32:33], v[36:37] offset0:0 offset1:13\n\t"
"buffer_load_dwordx4 a[116:119], v235, s[8:11], s74 offen offset:3072 sc1\n\t"
"v_mfma_f32_16x16x32_bf16 v[112:115], a[32:35], a[144:147], v[112:115]\n\t"
"v_mfma_f32_16x16x32_bf16 v[112:115], a[36:39], a[160:163], v[112:115]\n\t"
"v_mfma_f32_16x16x32_bf16 v[128:131], a[40:43], a[144:147], v[128:131]\n\t"
"ds_read_b128 a[152:155], v216 offset:22528\n\t"
"v_mfma_f32_16x16x32_bf16 v[128:131], a[44:47], a[160:163], v[128:131]\n\t"
"buffer_load_dwordx4 a[108:111], v235, s[8:11], s74 offen offset:1024 sc1\n\t"
"v_mfma_f32_16x16x32_bf16 v[132:135], a[40:43], a[148:151], v[132:135]\n\t"
"ds_read_b128 a[168:171], v216 offset:30720\n\t"
"v_mfma_f32_16x16x32_bf16 v[132:135], a[44:47], a[164:167], v[132:135]\n\t"
"ds_read_b128 a[156:159], v216 offset:23552\n\t"
"ds_read_b128 a[172:175], v216 offset:31744\n\t"
"v_mfma_f32_16x16x32_bf16 v[116:119], a[32:35], a[148:151], v[116:119]\n\t"
"v_mfma_f32_16x16x32_bf16 v[116:119], a[36:39], a[164:167], v[116:119]\n\t"
"buffer_load_dwordx4 a[104:107], v235, s[8:11], s74 offen offset:0 sc1\n\t"
"buffer_load_dwordx4 a[96:99], v248, s[8:11], s74 offen offset:2048 sc1\n\t"
"v_mfma_f32_16x16x32_bf16 v[100:103], a[24:27], a[148:151], v[100:103]\n\t"
"v_mfma_f32_16x16x32_bf16 v[100:103], a[28:31], a[164:167], v[100:103]\n\t"
"s_waitcnt lgkmcnt(0)\n\t"
"s_barrier\n\t"
"s_cmp_eq_u32 s21, 1\n\t"
"s_cbranch_scc1 Ltail_%=\n\t"
"v_mfma_f32_16x16x32_bf16 v[104:107], a[24:27], a[152:155], v[104:107]\n\t"
"ds_read_b128 a[144:147], v216 offset:0\n\t"
"v_cvt_pk_bf16_f32 v96, v96, v97\n\t"
"v_mfma_f32_16x16x32_bf16 v[104:107], a[28:31], a[168:171], v[104:107]\n\t"
"ds_read_b128 a[160:163], v216 offset:8192\n\t"
"ds_read_b128 a[148:151], v216 offset:1024\n\t"
"v_mfma_f32_16x16x32_bf16 v[120:123], a[32:35], a[152:155], v[120:123]\n\t"
"ds_read_b128 a[164:167], v216 offset:9216\n\t"
"v_cvt_pk_bf16_f32 v97, v98, v99\n\t"
"v_mfma_f32_16x16x32_bf16 v[120:123], a[36:39], a[168:171], v[120:123]\n\t"
"buffer_load_dwordx4 a[100:103], v248, s[8:11], s74 offen offset:3072 sc1\n\t"
"v_mfma_f32_16x16x32_bf16 v[136:139], a[40:43], a[152:155], v[136:139]\n\t"
"ds_read_b128 a[240:243], v222 offset:39168\n\t"
"v_cvt_pk_bf16_f32 v100, v100, v101\n\t"
"v_mfma_f32_16x16x32_bf16 v[136:139], a[44:47], a[168:171], v[136:139]\n\t"
"buffer_load_dwordx4 a[136:139], v235, s[8:11], s83 offen offset:2048 sc1\n\t"
"v_mfma_f32_16x16x32_bf16 v[192:195], a[176:179], a[224:227], 0\n\t"
"buffer_load_dwordx4 a[140:143], v235, s[8:11], s83 offen offset:3072 sc1\n\t"
"v_mfma_f32_16x16x32_bf16 v[140:143], a[40:43], a[156:159], v[140:143]\n\t"
"ds_read_b128 a[228:231], v229 offset:0\n\t"
"v_cvt_pk_bf16_f32 v101, v102, v103\n\t"
"v_mfma_f32_16x16x32_bf16 v[140:143], a[44:47], a[172:175], v[140:143]\n\t"
"buffer_load_dwordx4 a[132:135], v235, s[8:11], s83 offen offset:1024 sc1\n\t"
"v_mfma_f32_16x16x32_bf16 v[196:199], a[180:183], a[224:227], 0\n\t"
"ds_read_b128 a[232:235], v230 offset:0\n\t"
"v_cvt_pk_bf16_f32 v112, v112, v113\n\t"
"v_mfma_f32_16x16x32_bf16 v[124:127], a[32:35], a[156:159], v[124:127]\n\t"
"ds_read_b128 a[152:155], v216 offset:2048\n\t"
"v_cvt_pk_bf16_f32 v192, v192, v193\n\t"
"v_mfma_f32_16x16x32_bf16 v[124:127], a[36:39], a[172:175], v[124:127]\n\t"
"ds_read_b128 a[168:171], v216 offset:10240\n\t"
"v_cvt_pk_bf16_f32 v193, v194, v195\n\t"
"v_mfma_f32_16x16x32_bf16 v[108:111], a[24:27], a[156:159], v[108:111]\n\t"
"ds_read_b128 a[236:239], v231 offset:0\n\t"
"v_cvt_pk_bf16_f32 v113, v114, v115\n\t"
"v_mfma_f32_16x16x32_bf16 v[108:111], a[28:31], a[172:175], v[108:111]\n\t"
"buffer_load_dwordx4 a[128:131], v235, s[8:11], s83 offen offset:0 sc1\n\t"
"v_mfma_f32_16x16x32_bf16 v[200:203], a[184:187], a[224:227], 0\n\t"
"ds_write2st64_b64 v228, v[96:97], v[100:101] offset0:26 offset1:39\n\t"
"s_waitcnt vmcnt(16) lgkmcnt(6)\n\t"
"v_mfma_f32_16x16x32_bf16 v[48:51], a[48:51], a[144:147], v[48:51]\n\t"
"v_cvt_pk_bf16_f32 v194, v196, v197\n\t"
"v_cvt_pk_bf16_f32 v195, v198, v199\n\t"
"v_mfma_f32_16x16x32_bf16 v[48:51], a[52:55], a[160:163], v[48:51]\n\t"
"buffer_load_dwordx4 a[120:123], v248, s[8:11], s83 offen offset:2048 sc1\n\t"
"v_mfma_f32_16x16x32_bf16 v[204:207], a[188:191], a[224:227], 0\n\t"
"ds_read_b128 a[156:159], v216 offset:3072\n\t"
"ds_read_b128 a[172:175], v216 offset:11264\n\t"
"s_add_u32 s28, s24, 0x0\n\t"
"s_waitcnt lgkmcnt(7)\n\t"
"buffer_store_dwordx4 a[228:231], v232, s[16:19], s28 offen offset:0 nt\n\t"
"v_mfma_f32_16x16x32_bf16 v[64:67], a[56:59], a[144:147], v[64:67]\n\t"
"v_cvt_pk_bf16_f32 v200, v200, v201\n\t"
"v_cvt_pk_bf16_f32 v201, v202, v203\n\t"
"v_mfma_f32_16x16x32_bf16 v[64:67], a[60:63], a[160:163], v[64:67]\n\t"
"buffer_load_dwordx4 a[124:127], v248, s[8:11], s83 offen offset:3072 sc1\n\t"
"v_mfma_f32_16x16x32_bf16 v[80:83], a[64:67], a[144:147], v[80:83]\n\t"
"ds_read_b128 a[144:147], v216 offset:4096\n\t"
"v_cvt_pk_bf16_f32 v116, v116, v117\n\t"
"v_mfma_f32_16x16x32_bf16 v[80:83], a[68:71], a[160:163], v[80:83]\n\t"
"ds_write_b128 v221, v[192:195] offset:16384\n\t"
"v_mfma_f32_16x16x32_bf16 v[208:211], a[192:195], a[224:227], 0\n\t"
"buffer_load_dwordx4 a[16:19], v235, s[8:11], s42 offen offset:0 sc1\n\t"
"v_mfma_f32_16x16x32_bf16 v[84:87], a[64:67], a[148:151], v[84:87]\n\t"
"v_cvt_pk_bf16_f32 v202, v204, v205\n\t"
"v_cvt_pk_bf16_f32 v203, v206, v207\n\t"
"v_mfma_f32_16x16x32_bf16 v[84:87], a[68:71], a[164:167], v[84:87]\n\t"
"ds_read_b128 a[160:163], v216 offset:12288\n\t"
"v_cvt_pk_bf16_f32 v117, v118, v119\n\t"
"v_mfma_f32_16x16x32_bf16 v[68:71], a[56:59], a[148:151], v[68:71]\n\t"
"v_cvt_pk_bf16_f32 v128, v128, v129\n\t"
"v_cvt_pk_bf16_f32 v129, v130, v131\n\t"
"v_mfma_f32_16x16x32_bf16 v[68:71], a[60:63], a[164:167], v[68:71]\n\t"
"buffer_load_dwordx4 a[20:23], v235, s[8:11], s42 offen offset:1024 sc1\n\t"
"ds_write_b128 v221, v[200:203] offset:24576\n\t"
"v_mfma_f32_16x16x32_bf16 v[212:215], a[196:199], a[224:227], 0\n\t"
"ds_write2st64_b64 v246, v[112:113], v[116:117] offset0:26 offset1:39\n\t"
"v_mfma_f32_16x16x32_bf16 v[52:55], a[48:51], a[148:151], v[52:55]\n\t"
"v_cvt_pk_bf16_f32 v208, v208, v209\n\t"
"v_cvt_pk_bf16_f32 v209, v210, v211\n\t"
"v_mfma_f32_16x16x32_bf16 v[52:55], a[52:55], a[164:167], v[52:55]\n\t"
"buffer_load_dwordx4 a[12:15], v248, s[8:11], s42 offen offset:3072 sc1\n\t"
"v_mfma_f32_16x16x32_bf16 v[192:195], a[200:203], a[224:227], 0\n\t"
"ds_read_b128 a[148:151], v216 offset:5120\n\t"
"v_cvt_pk_bf16_f32 v132, v132, v133\n\t"
"s_waitcnt lgkmcnt(5)\n\t"
"v_mfma_f32_16x16x32_bf16 v[56:59], a[48:51], a[152:155], v[56:59]\n\t"
"ds_read_b128 a[164:167], v216 offset:13312\n\t"
"v_cvt_pk_bf16_f32 v210, v212, v213\n\t"
"v_mfma_f32_16x16x32_bf16 v[56:59], a[52:55], a[168:171], v[56:59]\n\t"
"v_cvt_pk_bf16_f32 v211, v214, v215\n\t"
"v_cvt_pk_bf16_f32 v133, v134, v135\n\t"
"v_mfma_f32_16x16x32_bf16 v[72:75], a[56:59], a[152:155], v[72:75]\n\t"
"ds_read_b64_tr_b16 a[176:177], v226 offset:41472\n\t"
"v_mfma_f32_16x16x32_bf16 v[72:75], a[60:63], a[168:171], v[72:75]\n\t"
"buffer_store_dwordx4 a[232:235], v233, s[16:19], s28 offen offset:0 nt\n\t"
"v_cvt_pk_bf16_f32 v192, v192, v193\n\t"
"v_cvt_pk_bf16_f32 v193, v194, v195\n\t"
"v_mfma_f32_16x16x32_bf16 v[196:199], a[204:207], a[224:227], 0\n\t"
"ds_write_b128 v221, v[208:211] offset:20480\n\t"
"v_mfma_f32_16x16x32_bf16 v[88:91], a[64:67], a[152:155], v[88:91]\n\t"
"ds_read_b64_tr_b16 a[178:179], v226 offset:41984\n\t"
"v_mfma_f32_16x16x32_bf16 v[88:91], a[68:71], a[168:171], v[88:91]\n\t"
"buffer_load_dwordx4 a[8:11], v248, s[8:11], s42 offen offset:2048 sc1\n\t"
"ds_read_b64_tr_b16 a[180:181], v218 offset:41472\n\t"
"ds_read_b64_tr_b16 a[182:183], v218 offset:41984\n\t"
"v_mfma_f32_16x16x32_bf16 v[92:95], a[64:67], a[156:159], v[92:95]\n\t"
"ds_read_b64_tr_b16 a[184:185], v220 offset:43776\n\t"
"v_mfma_f32_16x16x32_bf16 v[92:95], a[68:71], a[172:175], v[92:95]\n\t"
"ds_read_b64_tr_b16 a[186:187], v220 offset:44288\n\t"
"ds_read_b64_tr_b16 a[188:189], v255 offset:43776\n\t"
"v_cvt_pk_bf16_f32 v194, v196, v197\n\t"
"v_cvt_pk_bf16_f32 v195, v198, v199\n\t"
"v_mfma_f32_16x16x32_bf16 v[76:79], a[56:59], a[156:159], v[76:79]\n\t"
"ds_read_b64_tr_b16 a[190:191], v255 offset:44288\n\t"
"v_mfma_f32_16x16x32_bf16 v[76:79], a[60:63], a[172:175], v[76:79]\n\t"
"buffer_load_dwordx4 a[0:3], v248, s[8:11], s42 offen offset:0 sc1\n\t"
"v_mfma_f32_16x16x32_bf16 v[60:63], a[48:51], a[156:159], v[60:63]\n\t"
"ds_read_b64_tr_b16 a[192:193], v220 offset:41472\n\t"
"v_mfma_f32_16x16x32_bf16 v[60:63], a[52:55], a[172:175], v[60:63]\n\t"
"ds_write_b128 v221, v[192:195] offset:28672\n\t"
"ds_read_b64_tr_b16 a[194:195], v220 offset:41984\n\t"
"s_waitcnt vmcnt(19) lgkmcnt(11)\n\t"
"v_mfma_f32_16x16x32_bf16 v[144:147], a[72:75], a[144:147], v[144:147]\n\t"
"ds_read_b64_tr_b16 a[196:197], v255 offset:41472\n\t"
"v_mfma_f32_16x16x32_bf16 v[144:147], a[76:79], a[160:163], v[144:147]\n\t"
"ds_read_b64_tr_b16 a[198:199], v255 offset:41984\n\t"
"ds_read_b64_tr_b16 a[200:201], v226 offset:43776\n\t"
"ds_read_b64_tr_b16 a[202:203], v226 offset:44288\n\t"
"ds_read_b64_tr_b16 a[204:205], v218 offset:43776\n\t"
"ds_read_b64_tr_b16 a[206:207], v218 offset:44288\n\t"
"ds_write2st64_b64 v247, v[128:129], v[132:133] offset0:26 offset1:39\n\t"
"v_mfma_f32_16x16x32_bf16 v[160:163], a[80:83], a[144:147], v[160:163]\n\t"
"ds_read_b128 a[152:155], v216 offset:6144\n\t"
"v_mfma_f32_16x16x32_bf16 v[160:163], a[84:87], a[160:163], v[160:163]\n\t"
"buffer_load_dwordx4 a[4:7], v248, s[8:11], s42 offen offset:1024 sc1\n\t"
"v_mfma_f32_16x16x32_bf16 v[176:179], a[88:91], a[144:147], v[176:179]\n\t"
"ds_read_b128 a[168:171], v216 offset:14336\n\t"
"v_mfma_f32_16x16x32_bf16 v[176:179], a[92:95], a[160:163], v[176:179]\n\t"
"ds_read_b128 a[156:159], v216 offset:7168\n\t"
"v_mfma_f32_16x16x32_bf16 v[180:183], a[88:91], a[148:151], v[180:183]\n\t"
"ds_read_b128 a[172:175], v216 offset:15360\n\t"
"v_mfma_f32_16x16x32_bf16 v[180:183], a[92:95], a[164:167], v[180:183]\n\t"
"buffer_load_dwordx4 a[40:43], v235, s[8:11], s48 offen offset:0 sc1\n\t"
"ds_write_b128 v222, a[240:243] offset:20736\n\t"
"v_mfma_f32_16x16x32_bf16 v[164:167], a[80:83], a[148:151], v[164:167]\n\t"
"v_mfma_f32_16x16x32_bf16 v[164:167], a[84:87], a[164:167], v[164:167]\n\t"
"buffer_store_dwordx4 a[236:239], v234, s[16:19], s28 offen offset:0 nt\n\t"
"buffer_load_dwordx4 a[44:47], v235, s[8:11], s48 offen offset:1024 sc1\n\t"
"v_mfma_f32_16x16x32_bf16 v[148:151], a[72:75], a[148:151], v[148:151]\n\t"
"v_mfma_f32_16x16x32_bf16 v[148:151], a[76:79], a[164:167], v[148:151]\n\t"
"buffer_load_dwordx4 a[36:39], v248, s[8:11], s48 offen offset:3072 sc1\n\t"
"s_waitcnt lgkmcnt(0)\n\t"
"s_barrier\n\t"
"v_mfma_f32_16x16x32_bf16 v[152:155], a[72:75], a[152:155], v[152:155]\n\t"
"ds_read_b128 a[144:147], v216 offset:16384\n\t"
"v_add_u32 v249, 80896, v227\n\t"
"v_mfma_f32_16x16x32_bf16 v[152:155], a[76:79], a[168:171], v[152:155]\n\t"
"ds_read_b128 a[160:163], v216 offset:24576\n\t"
"ds_read_b128 a[148:151], v216 offset:17408\n\t"
"v_mfma_f32_16x16x32_bf16 v[168:171], a[80:83], a[152:155], v[168:171]\n\t"
"ds_read_b128 a[164:167], v216 offset:25600\n\t"
"v_xor_b32 v250, 32, v249\n\t"
"v_mfma_f32_16x16x32_bf16 v[168:171], a[84:87], a[168:171], v[168:171]\n\t"
"buffer_load_dwordx4 a[32:35], v248, s[8:11], s48 offen offset:2048 sc1\n\t"
"v_mfma_f32_16x16x32_bf16 v[184:187], a[88:91], a[152:155], v[184:187]\n\t"
"v_xor_b32 v251, 64, v249\n\t"
"v_xor_b32 v252, 96, v249\n\t"
"v_mfma_f32_16x16x32_bf16 v[184:187], a[92:95], a[168:171], v[184:187]\n\t"
"buffer_load_dwordx4 a[24:27], v248, s[8:11], s48 offen offset:0 sc1\n\t"
"v_cvt_pk_bf16_f32 v104, v104, v105\n\t"
"v_cvt_pk_bf16_f32 v105, v106, v107\n\t"
"v_mfma_f32_16x16x32_bf16 v[200:203], a[176:179], a[220:223], 0\n\t"
"v_cvt_pk_bf16_f32 v108, v108, v109\n\t"
"v_cvt_pk_bf16_f32 v109, v110, v111\n\t"
"v_mfma_f32_16x16x32_bf16 v[188:191], a[88:91], a[156:159], v[188:191]\n\t"
"ds_read_b128 a[228:231], v229 offset:13312\n\t"
"v_cvt_pk_bf16_f32 v120, v120, v121\n\t"
"v_mfma_f32_16x16x32_bf16 v[188:191], a[92:95], a[172:175], v[188:191]\n\t"
"buffer_load_dwordx4 a[28:31], v248, s[8:11], s48 offen offset:1024 sc1\n\t"
"ds_write2st64_b64 v228, v[104:105], v[108:109] offset0:0 offset1:13\n\t"
"v_mfma_f32_16x16x32_bf16 v[204:207], a[180:183], a[220:223], 0\n\t"
"v_cvt_pk_bf16_f32 v121, v122, v123\n\t"
"ds_read_b128 a[232:235], v230 offset:13312\n\t"
"v_mfma_f32_16x16x32_bf16 v[172:175], a[80:83], a[156:159], v[172:175]\n\t"
"ds_read_b128 a[152:155], v216 offset:18432\n\t"
"v_cvt_pk_bf16_f32 v200, v200, v201\n\t"
"v_mfma_f32_16x16x32_bf16 v[172:175], a[84:87], a[172:175], v[172:175]\n\t"
"buffer_load_dwordx4 a[64:67], v235, s[8:11], 0 offen offset:0 sc1\n\t"
"ds_read_b128 a[168:171], v216 offset:26624\n\t"
"v_cvt_pk_bf16_f32 v201, v202, v203\n\t"
"v_mfma_f32_16x16x32_bf16 v[156:159], a[72:75], a[156:159], v[156:159]\n\t"
"ds_read_b128 a[236:239], v231 offset:13312\n\t"
"v_cvt_pk_bf16_f32 v124, v124, v125\n\t"
"v_mfma_f32_16x16x32_bf16 v[156:159], a[76:79], a[172:175], v[156:159]\n\t"
"buffer_load_dwordx4 a[68:71], v235, s[8:11], 0 offen offset:1024 sc1\n\t"
"v_mfma_f32_16x16x32_bf16 v[208:211], a[184:187], a[220:223], 0\n\t"
"v_pk_add_f32 v[48:49], v[48:49], v[144:145]\n\t"
"v_pk_add_f32 v[50:51], v[50:51], v[146:147]\n\t"
"v_pk_fma_f32 v[144:145], v[144:145], s[38:39], v[48:49]\n\t"
"v_pk_fma_f32 v[146:147], v[146:147], s[38:39], v[50:51]\n\t"
"v_pk_add_f32 v[64:65], v[64:65], v[160:161]\n\t"
"v_pk_add_f32 v[66:67], v[66:67], v[162:163]\n\t"
"v_pk_fma_f32 v[160:161], v[160:161], s[38:39], v[64:65]\n\t"
"v_pk_fma_f32 v[162:163], v[162:163], s[38:39], v[66:67]\n\t"
"v_pk_add_f32 v[80:81], v[80:81], v[176:177]\n\t"
"v_pk_add_f32 v[82:83], v[82:83], v[178:179]\n\t"
"v_pk_fma_f32 v[176:177], v[176:177], s[38:39], v[80:81]\n\t"
"v_pk_fma_f32 v[178:179], v[178:179], s[38:39], v[82:83]\n\t"
"v_pk_add_f32 v[84:85], v[84:85], v[180:181]\n\t"
"v_pk_add_f32 v[86:87], v[86:87], v[182:183]\n\t"
"v_pk_fma_f32 v[180:181], v[180:181], s[38:39], v[84:85]\n\t"
"v_pk_fma_f32 v[182:183], v[182:183], s[38:39], v[86:87]\n\t"
"v_pk_add_f32 v[68:69], v[68:69], v[164:165]\n\t"
"v_pk_add_f32 v[70:71], v[70:71], v[166:167]\n\t"
"v_pk_fma_f32 v[164:165], v[164:165], s[38:39], v[68:69]\n\t"
"v_pk_fma_f32 v[166:167], v[166:167], s[38:39], v[70:71]\n\t"
"v_pk_add_f32 v[52:53], v[52:53], v[148:149]\n\t"
"v_pk_add_f32 v[54:55], v[54:55], v[150:151]\n\t"
"v_pk_fma_f32 v[148:149], v[148:149], s[38:39], v[52:53]\n\t"
"v_pk_fma_f32 v[150:151], v[150:151], s[38:39], v[54:55]\n\t"
"v_pk_add_f32 v[56:57], v[56:57], v[152:153]\n\t"
"v_pk_add_f32 v[58:59], v[58:59], v[154:155]\n\t"
"v_pk_fma_f32 v[152:153], v[152:153], s[38:39], v[56:57]\n\t"
"v_pk_fma_f32 v[154:155], v[154:155], s[38:39], v[58:59]\n\t"
"v_pk_add_f32 v[72:73], v[72:73], v[168:169]\n\t"
"v_pk_add_f32 v[74:75], v[74:75], v[170:171]\n\t"
"v_pk_fma_f32 v[168:169], v[168:169], s[38:39], v[72:73]\n\t"
"v_pk_fma_f32 v[170:171], v[170:171], s[38:39], v[74:75]\n\t"
"v_pk_add_f32 v[88:89], v[88:89], v[184:185]\n\t"
"v_pk_add_f32 v[90:91], v[90:91], v[186:187]\n\t"
"v_pk_fma_f32 v[184:185], v[184:185], s[38:39], v[88:89]\n\t"
"v_pk_fma_f32 v[186:187], v[186:187], s[38:39], v[90:91]\n\t"
"v_pk_add_f32 v[92:93], v[92:93], v[188:189]\n\t"
"v_pk_add_f32 v[94:95], v[94:95], v[190:191]\n\t"
"v_pk_fma_f32 v[188:189], v[188:189], s[38:39], v[92:93]\n\t"
"v_pk_fma_f32 v[190:191], v[190:191], s[38:39], v[94:95]\n\t"
"v_pk_add_f32 v[76:77], v[76:77], v[172:173]\n\t"
"v_pk_add_f32 v[78:79], v[78:79], v[174:175]\n\t"
"v_pk_fma_f32 v[172:173], v[172:173], s[38:39], v[76:77]\n\t"
"v_pk_fma_f32 v[174:175], v[174:175], s[38:39], v[78:79]\n\t"
"v_pk_add_f32 v[60:61], v[60:61], v[156:157]\n\t"
"v_pk_add_f32 v[62:63], v[62:63], v[158:159]\n\t"
"v_pk_fma_f32 v[156:157], v[156:157], s[38:39], v[60:61]\n\t"
"v_pk_fma_f32 v[158:159], v[158:159], s[38:39], v[62:63]\n\t"
"v_cvt_pk_bf16_f32 v125, v126, v127\n\t"
"buffer_load_dwordx4 a[60:63], v248, s[8:11], 0 offen offset:3072 sc1\n\t"
"v_cvt_pk_bf16_f32 v202, v204, v205\n\t"
"s_waitcnt vmcnt(24) lgkmcnt(6)\n\t"
"v_mfma_f32_16x16x32_bf16 v[48:51], a[96:99], a[144:147], v[48:51]\n\t"
"v_cvt_pk_bf16_f32 v203, v206, v207\n\t"
"v_cvt_pk_bf16_f32 v136, v136, v137\n\t"
"v_mfma_f32_16x16x32_bf16 v[48:51], a[100:103], a[160:163], v[48:51]\n\t"
"buffer_load_dwordx4 a[56:59], v248, s[8:11], 0 offen offset:2048 sc1\n\t"
"v_mfma_f32_16x16x32_bf16 v[212:215], a[188:191], a[220:223], 0\n\t"
"ds_write2st64_b64 v246, v[120:121], v[124:125] offset0:0 offset1:13\n\t"
"buffer_load_dwordx4 a[48:51], v248, s[8:11], 0 offen offset:0 sc1\n\t"
"v_mfma_f32_16x16x32_bf16 v[64:67], a[104:107], a[144:147], v[64:67]\n\t"
"v_cvt_pk_bf16_f32 v137, v138, v139\n\t"
"ds_read_b128 a[156:159], v216 offset:19456\n\t"
"v_mfma_f32_16x16x32_bf16 v[64:67], a[108:111], a[160:163], v[64:67]\n\t"
"buffer_load_dwordx4 a[52:55], v248, s[8:11], 0 offen offset:1024 sc1\n\t"
"v_mfma_f32_16x16x32_bf16 v[80:83], a[112:115], a[144:147], v[80:83]\n\t"
"ds_read_b128 a[172:175], v216 offset:27648\n\t"
"v_cvt_pk_bf16_f32 v140, v140, v141\n\t"
"v_mfma_f32_16x16x32_bf16 v[80:83], a[116:119], a[160:163], v[80:83]\n\t"
"buffer_load_dwordx4 a[88:91], v235, s[8:11], s51 offen offset:0 sc1\n\t"
"ds_write_b128 v217, v[200:203] offset:0\n\t"
"v_mfma_f32_16x16x32_bf16 v[192:195], a[192:195], a[220:223], 0\n\t"
"v_cvt_pk_bf16_f32 v141, v142, v143\n\t"
"buffer_load_dwordx4 a[92:95], v235, s[8:11], s51 offen offset:1024 sc1\n\t"
"v_cvt_pk_bf16_f32 v208, v208, v209\n\t"
"v_mfma_f32_16x16x32_bf16 v[84:87], a[112:115], a[148:151], v[84:87]\n\t"
"v_cvt_pk_bf16_f32 v209, v210, v211\n\t"
"v_cvt_pk_bf16_f32 v8, v8, v9\n\t"
"v_mfma_f32_16x16x32_bf16 v[84:87], a[116:119], a[164:167], v[84:87]\n\t"
"ds_write2st64_b64 v247, v[136:137], v[140:141] offset0:0 offset1:13\n\t"
"v_mfma_f32_16x16x32_bf16 v[68:71], a[104:107], a[148:151], v[68:71]\n\t"
"v_cvt_pk_bf16_f32 v9, v10, v11\n\t"
"v_cvt_pk_bf16_f32 v210, v212, v213\n\t"
"v_mfma_f32_16x16x32_bf16 v[68:71], a[108:111], a[164:167], v[68:71]\n\t"
"s_add_u32 s20, s24, 0x30000\n\t"
"s_waitcnt lgkmcnt(5)\n\t"
"buffer_store_dwordx4 a[228:231], v232, s[16:19], s20 offen offset:0 nt\n\t"
"buffer_load_dwordx4 a[84:87], v248, s[8:11], s51 offen offset:3072 sc1\n\t"
"v_mfma_f32_16x16x32_bf16 v[196:199], a[196:199], a[220:223], 0\n\t"
"v_cvt_pk_bf16_f32 v211, v214, v215\n\t"
"ds_read_b128 a[144:147], v216 offset:20480\n\t"
"v_mfma_f32_16x16x32_bf16 v[52:55], a[96:99], a[148:151], v[52:55]\n\t"
"ds_read_b128 a[160:163], v216 offset:28672\n\t"
"v_cvt_pk_bf16_f32 v12, v12, v13\n\t"
"v_mfma_f32_16x16x32_bf16 v[52:55], a[100:103], a[164:167], v[52:55]\n\t"
"v_cvt_pk_bf16_f32 v13, v14, v15\n\t"
"buffer_load_dwordx4 a[80:83], v248, s[8:11], s51 offen offset:2048 sc1\n\t"
"v_cvt_pk_bf16_f32 v192, v192, v193\n\t"
"v_mfma_f32_16x16x32_bf16 v[200:203], a[200:203], a[220:223], 0\n\t"
"ds_write_b128 v219, v[208:211] offset:0\n\t"
"v_mfma_f32_16x16x32_bf16 v[56:59], a[96:99], a[152:155], v[56:59]\n\t"
"v_cvt_pk_bf16_f32 v193, v194, v195\n\t"
"v_cvt_pk_bf16_f32 v24, v24, v25\n\t"
"v_mfma_f32_16x16x32_bf16 v[56:59], a[100:103], a[168:171], v[56:59]\n\t"
"ds_write2st64_b64 v228, v[8:9], v[12:13] offset0:58 offset1:71\n\t"
"v_cvt_pk_bf16_f32 v25, v26, v27\n\t"
"ds_read_b128 a[148:151], v216 offset:21504\n\t"
"v_mfma_f32_16x16x32_bf16 v[72:75], a[104:107], a[152:155], v[72:75]\n\t"
"ds_read_b128 a[164:167], v216 offset:29696\n\t"
"v_cvt_pk_bf16_f32 v28, v28, v29\n\t"
"v_mfma_f32_16x16x32_bf16 v[72:75], a[108:111], a[168:171], v[72:75]\n\t"
"buffer_load_dwordx4 a[72:75], v248, s[8:11], s51 offen offset:0 sc1\n\t"
"v_cvt_pk_bf16_f32 v29, v30, v31\n\t"
"v_cvt_pk_bf16_f32 v194, v196, v197\n\t"
"v_mfma_f32_16x16x32_bf16 v[204:207], a[204:207], a[220:223], 0\n\t"
"v_cvt_pk_bf16_f32 v195, v198, v199\n\t"
"v_cvt_pk_bf16_f32 v40, v40, v41\n\t"
"v_mfma_f32_16x16x32_bf16 v[88:91], a[112:115], a[152:155], v[88:91]\n\t"
"v_cvt_pk_bf16_f32 v41, v42, v43\n\t"
"v_cvt_pk_bf16_f32 v200, v200, v201\n\t"
"v_mfma_f32_16x16x32_bf16 v[88:91], a[116:119], a[168:171], v[88:91]\n\t"
"ds_write2st64_b64 v246, v[24:25], v[28:29] offset0:58 offset1:71\n\t"
"buffer_store_dwordx4 a[232:235], v233, s[16:19], s20 offen offset:0 nt\n\t"
"s_waitcnt lgkmcnt(6)\n\t"
"v_mfma_f32_16x16x32_bf16 v[92:95], a[112:115], a[156:159], v[92:95]\n\t"
"v_cvt_pk_bf16_f32 v201, v202, v203\n\t"
"v_cvt_pk_bf16_f32 v44, v44, v45\n\t"
"v_mfma_f32_16x16x32_bf16 v[92:95], a[116:119], a[172:175], v[92:95]\n\t"
"ds_write_b128 v217, v[192:195] offset:8192\n\t"
"v_mfma_f32_16x16x32_bf16 v[76:79], a[104:107], a[156:159], v[76:79]\n\t"
"v_cvt_pk_bf16_f32 v45, v46, v47\n\t"
"ds_read_b64_tr_b16 a[176:177], v249 offset:0\n\t"
"v_mfma_f32_16x16x32_bf16 v[76:79], a[108:111], a[172:175], v[76:79]\n\t"
"buffer_load_dwordx4 a[76:79], v248, s[8:11], s51 offen offset:1024 sc1\n\t"
"ds_read_b64_tr_b16 a[178:179], v249 offset:512\n\t"
"ds_read_b64_tr_b16 a[180:181], v250 offset:0\n\t"
"v_mfma_f32_16x16x32_bf16 v[60:63], a[96:99], a[156:159], v[60:63]\n\t"
"ds_read_b64_tr_b16 a[182:183], v250 offset:512\n\t"
"v_cvt_pk_bf16_f32 v202, v204, v205\n\t"
"v_mfma_f32_16x16x32_bf16 v[60:63], a[100:103], a[172:175], v[60:63]\n\t"
"ds_read_b64_tr_b16 a[184:185], v251 offset:0\n\t"
"ds_read_b64_tr_b16 a[186:187], v251 offset:512\n\t"
"v_cvt_pk_bf16_f32 v203, v206, v207\n\t"
"s_waitcnt vmcnt(28) lgkmcnt(8)\n\t"
"v_mfma_f32_16x16x32_bf16 v[144:147], a[120:123], a[144:147], v[144:147]\n\t"
"ds_read_b64_tr_b16 a[188:189], v252 offset:0\n\t"
"v_mfma_f32_16x16x32_bf16 v[144:147], a[124:127], a[160:163], v[144:147]\n\t"
"ds_write_b128 v219, v[200:203] offset:8192\n\t"
"ds_read_b64_tr_b16 a[190:191], v252 offset:512\n\t"
"ds_read_b64_tr_b16 a[192:193], v249 offset:1152\n\t"
"ds_read_b64_tr_b16 a[194:195], v249 offset:1664\n\t"
"ds_read_b64_tr_b16 a[196:197], v250 offset:1152\n\t"
"ds_read_b64_tr_b16 a[198:199], v250 offset:1664\n\t"
"ds_read_b64_tr_b16 a[200:201], v251 offset:1152\n\t"
"ds_read_b64_tr_b16 a[202:203], v251 offset:1664\n\t"
"ds_read_b64_tr_b16 a[204:205], v252 offset:1152\n\t"
"ds_read_b64_tr_b16 a[206:207], v252 offset:1664\n\t"
"ds_write2st64_b64 v247, v[40:41], v[44:45] offset0:58 offset1:71\n\t"
"v_mfma_f32_16x16x32_bf16 v[160:163], a[128:131], a[144:147], v[160:163]\n\t"
"v_mfma_f32_16x16x32_bf16 v[160:163], a[132:135], a[160:163], v[160:163]\n\t"
"buffer_load_dwordx4 a[112:115], v235, s[8:11], s54 offen offset:0 sc1\n\t"
"v_mfma_f32_16x16x32_bf16 v[176:179], a[136:139], a[144:147], v[176:179]\n\t"
"ds_read_b128 a[152:155], v216 offset:22528\n\t"
"v_mfma_f32_16x16x32_bf16 v[176:179], a[140:143], a[160:163], v[176:179]\n\t"
"buffer_load_dwordx4 a[116:119], v235, s[8:11], s54 offen offset:1024 sc1\n\t"
"v_mfma_f32_16x16x32_bf16 v[180:183], a[136:139], a[148:151], v[180:183]\n\t"
"ds_read_b128 a[168:171], v216 offset:30720\n\t"
"v_mfma_f32_16x16x32_bf16 v[180:183], a[140:143], a[164:167], v[180:183]\n\t"
"ds_read_b128 a[156:159], v216 offset:23552\n\t"
"ds_read_b128 a[172:175], v216 offset:31744\n\t"
"v_mfma_f32_16x16x32_bf16 v[164:167], a[128:131], a[148:151], v[164:167]\n\t"
"v_mfma_f32_16x16x32_bf16 v[164:167], a[132:135], a[164:167], v[164:167]\n\t"
"buffer_store_dwordx4 a[236:239], v234, s[16:19], s20 offen offset:0 nt\n\t"
"buffer_load_dwordx4 a[108:111], v248, s[8:11], s54 offen offset:3072 sc1\n\t"
"v_mfma_f32_16x16x32_bf16 v[148:151], a[120:123], a[148:151], v[148:151]\n\t"
"v_mfma_f32_16x16x32_bf16 v[148:151], a[124:127], a[164:167], v[148:151]\n\t"
"buffer_load_dwordx4 a[104:107], v248, s[8:11], s54 offen offset:2048 sc1\n\t"
"s_waitcnt lgkmcnt(0)\n\t"
"s_barrier\n\t"
"s_add_u32 s71, s71, 1\n\t"
"v_mov_b32 v215, s71\n\t"
"s_mov_b64 exec, 1\n\t"
"buffer_store_dword v215, off, s[8:11], s70 sc1\n\t"
"s_mov_b64 exec, -1\n\t"
"s_add_u32 s24, s24, 3145728\n\t"
"s_mov_b32 s23, s22\n\t"
"s_add_u32 s22, s22, 3145728\n\t"
"s_sub_u32 s21, s21, 1\n\t"
"s_cmp_eq_u32 s21, 1\n\t"
"s_cselect_b32 s22, 0x40000000, s22\n\t"
"v_mfma_f32_16x16x32_bf16 v[152:155], a[120:123], a[152:155], v[152:155]\n\t"
"ds_read_b128 a[144:147], v216 offset:0\n\t"
"v_cvt_pk_bf16_f32 v48, v48, v49\n\t"
"v_mfma_f32_16x16x32_bf16 v[152:155], a[124:127], a[168:171], v[152:155]\n\t"
"ds_read_b128 a[160:163], v216 offset:8192\n\t"
"ds_read_b128 a[148:151], v216 offset:1024\n\t"
"v_mfma_f32_16x16x32_bf16 v[168:171], a[128:131], a[152:155], v[168:171]\n\t"
"ds_read_b128 a[164:167], v216 offset:9216\n\t"
"v_cvt_pk_bf16_f32 v49, v50, v51\n\t"
"v_mfma_f32_16x16x32_bf16 v[168:171], a[132:135], a[168:171], v[168:171]\n\t"
"buffer_load_dwordx4 a[96:99], v248, s[8:11], s54 offen offset:0 sc1\n\t"
"v_mfma_f32_16x16x32_bf16 v[184:187], a[136:139], a[152:155], v[184:187]\n\t"
"ds_read_b128 a[228:231], v229 offset:0\n\t"
"v_cvt_pk_bf16_f32 v52, v52, v53\n\t"
"v_mfma_f32_16x16x32_bf16 v[184:187], a[140:143], a[168:171], v[184:187]\n\t"
"buffer_load_dwordx4 a[100:103], v248, s[8:11], s54 offen offset:1024 sc1\n\t"
"v_mfma_f32_16x16x32_bf16 v[192:195], a[176:179], a[224:227], 0\n\t"
"ds_read_b128 a[232:235], v230 offset:0\n\t"
"v_cvt_pk_bf16_f32 v53, v54, v55\n\t"
"v_mfma_f32_16x16x32_bf16 v[188:191], a[136:139], a[156:159], v[188:191]\n\t"
"ds_read_b128 a[152:155], v216 offset:2048\n\t"
"v_cvt_pk_bf16_f32 v64, v64, v65\n\t"
"v_mfma_f32_16x16x32_bf16 v[188:191], a[140:143], a[172:175], v[188:191]\n\t"
"buffer_load_dwordx4 a[136:139], v235, s[8:11], s66 offen offset:0 sc1\n\t"
"v_mfma_f32_16x16x32_bf16 v[196:199], a[180:183], a[224:227], 0\n\t"
"buffer_load_dwordx4 a[140:143], v235, s[8:11], s66 offen offset:1024 sc1\n\t"
"v_mfma_f32_16x16x32_bf16 v[172:175], a[128:131], a[156:159], v[172:175]\n\t"
"ds_read_b128 a[168:171], v216 offset:10240\n\t"
"v_cvt_pk_bf16_f32 v192, v192, v193\n\t"
"v_mfma_f32_16x16x32_bf16 v[172:175], a[132:135], a[172:175], v[172:175]\n\t"
"v_cvt_pk_bf16_f32 v193, v194, v195\n\t"
"ds_read_b128 a[236:239], v231 offset:0\n\t"
"v_mfma_f32_16x16x32_bf16 v[156:159], a[120:123], a[156:159], v[156:159]\n\t"
"ds_read_b128 a[156:159], v216 offset:3072\n\t"
"v_cvt_pk_bf16_f32 v65, v66, v67\n\t"
"v_mfma_f32_16x16x32_bf16 v[156:159], a[124:127], a[172:175], v[156:159]\n\t"
"buffer_load_dwordx4 a[132:135], v248, s[8:11], s66 offen offset:3072 sc1\n\t"
"v_mfma_f32_16x16x32_bf16 v[200:203], a[184:187], a[224:227], 0\n\t"
"s_add_u32 s25, s24, 0xffeb0000\n\t"
"s_waitcnt lgkmcnt(5)\n\t"
"buffer_store_dwordx4 a[228:231], v232, s[16:19], s25 offen offset:0 nt\n\t"
"s_waitcnt vmcnt(33)\n\t"
"v_mfma_f32_16x16x32_bf16 v[96:99], a[0:3], a[144:147], 0\n\t"
"v_cvt_pk_bf16_f32 v194, v196, v197\n\t"
"v_cvt_pk_bf16_f32 v195, v198, v199\n\t"
"v_mfma_f32_16x16x32_bf16 v[96:99], a[4:7], a[160:163], v[96:99]\n\t"
"ds_write2st64_b64 v228, v[48:49], v[52:53] offset0:26 offset1:39\n\t"
"buffer_load_dwordx4 a[128:131], v248, s[8:11], s66 offen offset:2048 sc1\n\t"
"v_mfma_f32_16x16x32_bf16 v[204:207], a[188:191], a[224:227], 0\n\t"
"ds_read_b128 a[172:175], v216 offset:11264\n\t"
"v_cvt_pk_bf16_f32 v68, v68, v69\n\t"
"v_mfma_f32_16x16x32_bf16 v[112:115], a[8:11], a[144:147], 0\n\t"
"v_cvt_pk_bf16_f32 v200, v200, v201\n\t"
"v_cvt_pk_bf16_f32 v201, v202, v203\n\t"
"v_mfma_f32_16x16x32_bf16 v[112:115], a[12:15], a[160:163], v[112:115]\n\t"
"s_waitcnt lgkmcnt(5)\n\t"
"buffer_store_dwordx4 a[232:235], v233, s[16:19], s25 offen offset:0 nt\n\t"
"v_mfma_f32_16x16x32_bf16 v[128:131], a[16:19], a[144:147], 0\n\t"
"ds_read_b128 a[144:147], v216 offset:4096\n\t"
"v_cvt_pk_bf16_f32 v69, v70, v71\n\t"
"v_mfma_f32_16x16x32_bf16 v[128:131], a[20:23], a[160:163], v[128:131]\n\t"
"buffer_load_dwordx4 a[120:123], v248, s[8:11], s66 offen offset:0 sc1\n\t"
"v_mfma_f32_16x16x32_bf16 v[208:211], a[192:195], a[224:227], 0\n\t"
"ds_write_b128 v221, v[192:195] offset:16384\n\t"
"v_cvt_pk_bf16_f32 v202, v204, v205\n\t"
"v_cvt_pk_bf16_f32 v203, v206, v207\n\t"
"v_mfma_f32_16x16x32_bf16 v[132:135], a[16:19], a[148:151], 0\n\t"
"ds_read_b128 a[160:163], v216 offset:12288\n\t"
"v_cvt_pk_bf16_f32 v80, v80, v81\n\t"
"v_mfma_f32_16x16x32_bf16 v[132:135], a[20:23], a[164:167], v[132:135]\n\t"
"buffer_load_dwordx4 a[124:127], v248, s[8:11], s66 offen offset:1024 sc1\n\t"
"v_mfma_f32_16x16x32_bf16 v[116:119], a[8:11], a[148:151], 0\n\t"
"v_cvt_pk_bf16_f32 v81, v82, v83\n\t"
"v_cvt_pk_bf16_f32 v84, v84, v85\n\t"
"v_mfma_f32_16x16x32_bf16 v[116:119], a[12:15], a[164:167], v[116:119]\n\t"
"s_waitcnt lgkmcnt(4)\n\t"
"buffer_store_dwordx4 a[236:239], v234, s[16:19], s25 offen offset:0 nt\n\t"
"v_mfma_f32_16x16x32_bf16 v[212:215], a[196:199], a[224:227], 0\n\t"
"ds_write_b128 v221, v[200:203] offset:24576\n\t"
"v_mfma_f32_16x16x32_bf16 v[100:103], a[0:3], a[148:151], 0\n\t"
"v_cvt_pk_bf16_f32 v208, v208, v209\n\t"
"v_cvt_pk_bf16_f32 v209, v210, v211\n\t"
"v_mfma_f32_16x16x32_bf16 v[100:103], a[4:7], a[164:167], v[100:103]\n\t"
"ds_write2st64_b64 v246, v[64:65], v[68:69] offset0:26 offset1:39\n\t"
"v_mfma_f32_16x16x32_bf16 v[192:195], a[200:203], a[224:227], 0\n\t"
"ds_read_b128 a[148:151], v216 offset:5120\n\t"
"ds_read_b128 a[164:167], v216 offset:13312\n\t"
"v_cvt_pk_bf16_f32 v85, v86, v87\n\t"
"v_mfma_f32_16x16x32_bf16 v[104:107], a[0:3], a[152:155], 0\n\t"
"ds_read_b128 a[228:231], v229 offset:29696\n\t"
"v_cvt_pk_bf16_f32 v210, v212, v213\n\t"
"v_mfma_f32_16x16x32_bf16 v[104:107], a[4:7], a[168:171], v[104:107]\n\t"
"v_cvt_pk_bf16_f32 v211, v214, v215\n\t"
"ds_read_b128 a[232:235], v230 offset:29696\n\t"
"v_mfma_f32_16x16x32_bf16 v[120:123], a[8:11], a[152:155], 0\n\t"
"ds_read_b128 a[236:239], v231 offset:29696\n\t"
"v_mfma_f32_16x16x32_bf16 v[120:123], a[12:15], a[168:171], v[120:123]\n\t"
"v_cvt_pk_bf16_f32 v192, v192, v193\n\t"
"v_cvt_pk_bf16_f32 v193, v194, v195\n\t"
"ds_write_b128 v221, v[208:211] offset:20480\n\t"
"v_mfma_f32_16x16x32_bf16 v[196:199], a[204:207], a[224:227], 0\n\t"
"ds_read_b64_tr_b16 a[176:177], v249 offset:41472\n\t"
"v_mfma_f32_16x16x32_bf16 v[136:139], a[16:19], a[152:155], 0\n\t"
"ds_read_b64_tr_b16 a[178:179], v249 offset:41984\n\t"
"v_mfma_f32_16x16x32_bf16 v[136:139], a[20:23], a[168:171], v[136:139]\n\t"
"s_add_u32 s27, s24, 0xffe80000\n\t"
"s_waitcnt lgkmcnt(5)\n\t"
"buffer_store_dwordx4 a[228:231], v232, s[16:19], s27 offen offset:0 nt\n\t"
"ds_read_b64_tr_b16 a[180:181], v250 offset:41472\n\t"
"ds_read_b64_tr_b16 a[182:183], v250 offset:41984\n\t"
"v_mfma_f32_16x16x32_bf16 v[140:143], a[16:19], a[156:159], 0\n\t"
"ds_read_b64_tr_b16 a[184:185], v251 offset:41472\n\t"
"v_mfma_f32_16x16x32_bf16 v[140:143], a[20:23], a[172:175], v[140:143]\n\t"
"buffer_load_dwordx4 a[16:19], v235, s[8:11], s47 offen offset:2048 sc1\n\t"
"buffer_load_dwordx4 a[20:23], v235, s[8:11], s47 offen offset:3072 sc1\n\t"
"v_mfma_f32_16x16x32_bf16 v[124:127], a[8:11], a[156:159], 0\n\t"
"ds_read_b64_tr_b16 a[186:187], v251 offset:41984\n\t"
"v_cvt_pk_bf16_f32 v194, v196, v197\n\t"
"v_mfma_f32_16x16x32_bf16 v[124:127], a[12:15], a[172:175], v[124:127]\n\t"
"ds_read_b64_tr_b16 a[188:189], v252 offset:41472\n\t"
"ds_read_b64_tr_b16 a[190:191], v252 offset:41984\n\t"
"v_cvt_pk_bf16_f32 v195, v198, v199\n\t"
"v_mfma_f32_16x16x32_bf16 v[108:111], a[0:3], a[156:159], 0\n\t"
"ds_read_b64_tr_b16 a[192:193], v249 offset:42624\n\t"
"v_mfma_f32_16x16x32_bf16 v[108:111], a[4:7], a[172:175], v[108:111]\n\t"
"ds_write_b128 v221, v[192:195] offset:28672\n\t"
"ds_read_b64_tr_b16 a[194:195], v249 offset:43136\n\t"
"s_waitcnt vmcnt(34) lgkmcnt(12)\n\t"
"v_mfma_f32_16x16x32_bf16 v[0:3], a[24:27], a[144:147], v[96:99]\n\t"
"ds_read_b64_tr_b16 a[196:197], v250 offset:42624\n\t"
"v_mfma_f32_16x16x32_bf16 v[0:3], a[28:31], a[160:163], v[0:3]\n\t"
"ds_read_b64_tr_b16 a[198:199], v250 offset:43136\n\t"
"ds_read_b64_tr_b16 a[200:201], v251 offset:42624\n\t"
"ds_read_b64_tr_b16 a[202:203], v251 offset:43136\n\t"
"ds_read_b64_tr_b16 a[204:205], v252 offset:42624\n\t"
"ds_read_b64_tr_b16 a[206:207], v252 offset:43136\n\t"
"ds_write2st64_b64 v247, v[80:81], v[84:85] offset0:26 offset1:39\n\t"
"buffer_load_dwordx4 a[12:15], v235, s[8:11], s47 offen offset:1024 sc1\n\t"
"v_mfma_f32_16x16x32_bf16 v[16:19], a[32:35], a[144:147], v[112:115]\n\t"
"ds_read_b128 a[152:155], v216 offset:6144\n\t"
"v_mfma_f32_16x16x32_bf16 v[16:19], a[36:39], a[160:163], v[16:19]\n\t"
"buffer_store_dwordx4 a[232:235], v233, s[16:19], s27 offen offset:0 nt\n\t"
"v_mfma_f32_16x16x32_bf16 v[32:35], a[40:43], a[144:147], v[128:131]\n\t"
"ds_read_b128 a[168:171], v216 offset:14336\n\t"
"v_mfma_f32_16x16x32_bf16 v[32:35], a[44:47], a[160:163], v[32:35]\n\t"
"buffer_load_dwordx4 a[8:11], v235, s[8:11], s47 offen offset:0 sc1\n\t"
"v_mfma_f32_16x16x32_bf16 v[36:39], a[40:43], a[148:151], v[132:135]\n\t"
"ds_read_b128 a[156:159], v216 offset:7168\n\t"
"v_mfma_f32_16x16x32_bf16 v[36:39], a[44:47], a[164:167], v[36:39]\n\t"
"ds_read_b128 a[172:175], v216 offset:15360\n\t"
"v_mfma_f32_16x16x32_bf16 v[20:23], a[32:35], a[148:151], v[116:119]\n\t"
"v_mfma_f32_16x16x32_bf16 v[20:23], a[36:39], a[164:167], v[20:23]\n\t"
"buffer_load_dwordx4 a[0:3], v248, s[8:11], s47 offen offset:2048 sc1\n\t"
"buffer_store_dwordx4 a[236:239], v234, s[16:19], s27 offen offset:0 nt\n\t"
"v_mfma_f32_16x16x32_bf16 v[4:7], a[24:27], a[148:151], v[100:103]\n\t"
"v_mfma_f32_16x16x32_bf16 v[4:7], a[28:31], a[164:167], v[4:7]\n\t"
"buffer_load_dwordx4 a[4:7], v248, s[8:11], s47 offen offset:3072 sc1\n\t"
"s_waitcnt lgkmcnt(0)\n\t"
"s_barrier\n\t"
"v_mfma_f32_16x16x32_bf16 v[8:11], a[24:27], a[152:155], v[104:107]\n\t"
"ds_read_b128 a[144:147], v216 offset:16384\n\t"
"v_cvt_pk_bf16_f32 v144, v144, v145\n\t"
"v_mfma_f32_16x16x32_bf16 v[8:11], a[28:31], a[168:171], v[8:11]\n\t"
"ds_read_b128 a[160:163], v216 offset:24576\n\t"
"ds_read_b128 a[148:151], v216 offset:17408\n\t"
"v_mfma_f32_16x16x32_bf16 v[24:27], a[32:35], a[152:155], v[120:123]\n\t"
"ds_read_b128 a[164:167], v216 offset:25600\n\t"
"v_cvt_pk_bf16_f32 v145, v146, v147\n\t"
"v_mfma_f32_16x16x32_bf16 v[24:27], a[36:39], a[168:171], v[24:27]\n\t"
"v_cvt_pk_bf16_f32 v148, v148, v149\n\t"
"v_cvt_pk_bf16_f32 v149, v150, v151\n\t"
"v_mfma_f32_16x16x32_bf16 v[40:43], a[40:43], a[152:155], v[136:139]\n\t"
"ds_read_b128 a[228:231], v229 offset:13312\n\t"
"v_cvt_pk_bf16_f32 v160, v160, v161\n\t"
"v_mfma_f32_16x16x32_bf16 v[40:43], a[44:47], a[168:171], v[40:43]\n\t"
"ds_write2st64_b64 v228, v[144:145], v[148:149] offset0:0 offset1:13\n\t"
"v_mfma_f32_16x16x32_bf16 v[200:203], a[176:179], a[224:227], 0\n\t"
"v_cvt_pk_bf16_f32 v161, v162, v163\n\t"
"ds_read_b128 a[232:235], v230 offset:13312\n\t"
"v_mfma_f32_16x16x32_bf16 v[44:47], a[40:43], a[156:159], v[140:143]\n\t"
"ds_read_b128 a[152:155], v216 offset:18432\n\t"
"v_cvt_pk_bf16_f32 v164, v164, v165\n\t"
"v_mfma_f32_16x16x32_bf16 v[44:47], a[44:47], a[172:175], v[44:47]\n\t"
"buffer_load_dwordx4 a[40:43], v235, s[8:11], s50 offen offset:2048 sc1\n\t"
"v_mfma_f32_16x16x32_bf16 v[204:207], a[180:183], a[224:227], 0\n\t"
"buffer_load_dwordx4 a[44:47], v235, s[8:11], s50 offen offset:3072 sc1\n\t"
"v_mfma_f32_16x16x32_bf16 v[28:31], a[32:35], a[156:159], v[124:127]\n\t"
"ds_read_b128 a[168:171], v216 offset:26624\n\t"
"v_cvt_pk_bf16_f32 v200, v200, v201\n\t"
"v_mfma_f32_16x16x32_bf16 v[28:31], a[36:39], a[172:175], v[28:31]\n\t"
"buffer_load_dwordx4 a[36:39], v235, s[8:11], s50 offen offset:1024 sc1\n\t"
"v_cvt_pk_bf16_f32 v201, v202, v203\n\t"
"ds_read_b128 a[236:239], v231 offset:13312\n\t"
"v_mfma_f32_16x16x32_bf16 v[12:15], a[24:27], a[156:159], v[108:111]\n\t"
"v_cvt_pk_bf16_f32 v165, v166, v167\n\t"
"v_cvt_pk_bf16_f32 v176, v176, v177\n\t"
"v_mfma_f32_16x16x32_bf16 v[12:15], a[28:31], a[172:175], v[12:15]\n\t"
"buffer_load_dwordx4 a[32:35], v235, s[8:11], s50 offen offset:0 sc1\n\t"
"v_mfma_f32_16x16x32_bf16 v[208:211], a[184:187], a[224:227], 0\n\t"
"v_pk_fma_f32 v[96:97], v[96:97], s[38:39], v[0:1] neg_lo:[1,0,1] neg_hi:[1,0,1]\n\t"
"v_pk_fma_f32 v[98:99], v[98:99], s[38:39], v[2:3] neg_lo:[1,0,1] neg_hi:[1,0,1]\n\t"
"v_pk_fma_f32 v[112:113], v[112:113], s[38:39], v[16:17] neg_lo:[1,0,1] neg_hi:[1,0,1]\n\t"
"v_pk_fma_f32 v[114:115], v[114:115], s[38:39], v[18:19] neg_lo:[1,0,1] neg_hi:[1,0,1]\n\t"
"v_pk_fma_f32 v[128:129], v[128:129], s[38:39], v[32:33] neg_lo:[1,0,1] neg_hi:[1,0,1]\n\t"
"v_pk_fma_f32 v[130:131], v[130:131], s[38:39], v[34:35] neg_lo:[1,0,1] neg_hi:[1,0,1]\n\t"
"v_pk_fma_f32 v[132:133], v[132:133], s[38:39], v[36:37] neg_lo:[1,0,1] neg_hi:[1,0,1]\n\t"
"v_pk_fma_f32 v[134:135], v[134:135], s[38:39], v[38:39] neg_lo:[1,0,1] neg_hi:[1,0,1]\n\t"
"v_pk_fma_f32 v[116:117], v[116:117], s[38:39], v[20:21] neg_lo:[1,0,1] neg_hi:[1,0,1]\n\t"
"v_pk_fma_f32 v[118:119], v[118:119], s[38:39], v[22:23] neg_lo:[1,0,1] neg_hi:[1,0,1]\n\t"
"v_pk_fma_f32 v[100:101], v[100:101], s[38:39], v[4:5] neg_lo:[1,0,1] neg_hi:[1,0,1]\n\t"
"v_pk_fma_f32 v[102:103], v[102:103], s[38:39], v[6:7] neg_lo:[1,0,1] neg_hi:[1,0,1]\n\t"
"v_pk_fma_f32 v[104:105], v[104:105], s[38:39], v[8:9] neg_lo:[1,0,1] neg_hi:[1,0,1]\n\t"
"v_pk_fma_f32 v[106:107], v[106:107], s[38:39], v[10:11] neg_lo:[1,0,1] neg_hi:[1,0,1]\n\t"
"v_pk_fma_f32 v[120:121], v[120:121], s[38:39], v[24:25] neg_lo:[1,0,1] neg_hi:[1,0,1]\n\t"
"v_pk_fma_f32 v[122:123], v[122:123], s[38:39], v[26:27] neg_lo:[1,0,1] neg_hi:[1,0,1]\n\t"
"v_pk_fma_f32 v[136:137], v[136:137], s[38:39], v[40:41] neg_lo:[1,0,1] neg_hi:[1,0,1]\n\t"
"v_pk_fma_f32 v[138:139], v[138:139], s[38:39], v[42:43] neg_lo:[1,0,1] neg_hi:[1,0,1]\n\t"
"v_pk_fma_f32 v[140:141], v[140:141], s[38:39], v[44:45] neg_lo:[1,0,1] neg_hi:[1,0,1]\n\t"
"v_pk_fma_f32 v[142:143], v[142:143], s[38:39], v[46:47] neg_lo:[1,0,1] neg_hi:[1,0,1]\n\t"
"v_pk_fma_f32 v[124:125], v[124:125], s[38:39], v[28:29] neg_lo:[1,0,1] neg_hi:[1,0,1]\n\t"
"v_pk_fma_f32 v[126:127], v[126:127], s[38:39], v[30:31] neg_lo:[1,0,1] neg_hi:[1,0,1]\n\t"
"v_pk_fma_f32 v[108:109], v[108:109], s[38:39], v[12:13] neg_lo:[1,0,1] neg_hi:[1,0,1]\n\t"
"v_pk_fma_f32 v[110:111], v[110:111], s[38:39], v[14:15] neg_lo:[1,0,1] neg_hi:[1,0,1]\n\t"
"buffer_load_dwordx4 a[24:27], v248, s[8:11], s50 offen offset:2048 sc1\n\t"
"s_waitcnt vmcnt(39) lgkmcnt(5)\n\t"
"v_mfma_f32_16x16x32_bf16 v[0:3], a[48:51], a[144:147], v[0:3]\n\t"
"v_cvt_pk_bf16_f32 v202, v204, v205\n\t"
"v_cvt_pk_bf16_f32 v203, v206, v207\n\t"
"v_mfma_f32_16x16x32_bf16 v[0:3], a[52:55], a[160:163], v[0:3]\n\t"
"buffer_load_dwordx4 a[28:31], v248, s[8:11], s50 offen offset:3072 sc1\n\t"
"ds_write2st64_b64 v246, v[160:161], v[164:165] offset0:0 offset1:13\n\t"
"v_mfma_f32_16x16x32_bf16 v[212:215], a[188:191], a[224:227], 0\n\t"
"v_cvt_pk_bf16_f32 v177, v178, v179\n\t"
"ds_read_b128 a[156:159], v216 offset:19456\n\t"
"v_mfma_f32_16x16x32_bf16 v[16:19], a[56:59], a[144:147], v[16:19]\n\t"
"ds_read_b128 a[172:175], v216 offset:27648\n\t"
"v_cvt_pk_bf16_f32 v180, v180, v181\n\t"
"v_mfma_f32_16x16x32_bf16 v[16:19], a[60:63], a[160:163], v[16:19]\n\t"
"ds_write_b128 v221, v[200:203] offset:0\n\t"
"v_mfma_f32_16x16x32_bf16 v[32:35], a[64:67], a[144:147], v[32:35]\n\t"
"v_cvt_pk_bf16_f32 v181, v182, v183\n\t"
"v_cvt_pk_bf16_f32 v208, v208, v209\n\t"
"v_mfma_f32_16x16x32_bf16 v[32:35], a[68:71], a[160:163], v[32:35]\n\t"
"v_cvt_pk_bf16_f32 v209, v210, v211\n\t"
"v_cvt_pk_bf16_f32 v152, v152, v153\n\t"
"v_mfma_f32_16x16x32_bf16 v[192:195], a[192:195], a[224:227], 0\n\t"
"ds_write2st64_b64 v247, v[176:177], v[180:181] offset0:0 offset1:13\n\t"
"buffer_store_dwordx4 a[228:231], v232, s[16:19], s28 offen offset:384 nt\n\t"
"v_mfma_f32_16x16x32_bf16 v[36:39], a[64:67], a[148:151], v[36:39]\n\t"
"v_cvt_pk_bf16_f32 v153, v154, v155\n\t"
"v_cvt_pk_bf16_f32 v210, v212, v213\n\t"
"v_mfma_f32_16x16x32_bf16 v[36:39], a[68:71], a[164:167], v[36:39]\n\t"
"v_cvt_pk_bf16_f32 v211, v214, v215\n\t"
"ds_read_b128 a[144:147], v216 offset:20480\n\t"
"v_mfma_f32_16x16x32_bf16 v[20:23], a[56:59], a[148:151], v[20:23]\n\t"
"ds_read_b128 a[160:163], v216 offset:28672\n\t"
"v_cvt_pk_bf16_f32 v156, v156, v157\n\t"
"v_mfma_f32_16x16x32_bf16 v[20:23], a[60:63], a[164:167], v[20:23]\n\t"
"v_cvt_pk_bf16_f32 v157, v158, v159\n\t"
"v_cvt_pk_bf16_f32 v192, v192, v193\n\t"
"v_mfma_f32_16x16x32_bf16 v[196:199], a[196:199], a[224:227], 0\n\t"
"ds_write_b128 v221, v[208:211] offset:8192\n\t"
"ds_write2st64_b64 v228, v[152:153], v[156:157] offset0:58 offset1:71\n\t"
"v_mfma_f32_16x16x32_bf16 v[4:7], a[48:51], a[148:151], v[4:7]\n\t"
"v_cvt_pk_bf16_f32 v193, v194, v195\n\t"
"v_cvt_pk_bf16_f32 v168, v168, v169\n\t"
"v_mfma_f32_16x16x32_bf16 v[4:7], a[52:55], a[164:167], v[4:7]\n\t"
"v_cvt_pk_bf16_f32 v169, v170, v171\n\t"
"ds_read_b128 a[148:151], v216 offset:21504\n\t"
"v_mfma_f32_16x16x32_bf16 v[200:203], a[200:203], a[224:227], 0\n\t"
"ds_read_b128 a[164:167], v216 offset:29696\n\t"
"v_cvt_pk_bf16_f32 v172, v172, v173\n\t"
"s_waitcnt lgkmcnt(7)\n\t"
"v_mfma_f32_16x16x32_bf16 v[8:11], a[48:51], a[152:155], v[8:11]\n\t"
"v_cvt_pk_bf16_f32 v173, v174, v175\n\t"
"v_cvt_pk_bf16_f32 v194, v196, v197\n\t"
"v_mfma_f32_16x16x32_bf16 v[8:11], a[52:55], a[168:171], v[8:11]\n\t"
"v_cvt_pk_bf16_f32 v195, v198, v199\n\t"
"v_cvt_pk_bf16_f32 v184, v184, v185\n\t"
"v_mfma_f32_16x16x32_bf16 v[24:27], a[56:59], a[152:155], v[24:27]\n\t"
"v_cvt_pk_bf16_f32 v185, v186, v187\n\t"
"v_cvt_pk_bf16_f32 v188, v188, v189\n\t"
"v_mfma_f32_16x16x32_bf16 v[24:27], a[60:63], a[168:171], v[24:27]\n\t"
"ds_write2st64_b64 v246, v[168:169], v[172:173] offset0:58 offset1:71\n\t"
"buffer_store_dwordx4 a[232:235], v233, s[16:19], s28 offen offset:384 nt\n\t"
"v_mfma_f32_16x16x32_bf16 v[204:207], a[204:207], a[224:227], 0\n\t"
"v_cvt_pk_bf16_f32 v200, v200, v201\n\t"
"v_cvt_pk_bf16_f32 v201, v202, v203\n\t"
"v_mfma_f32_16x16x32_bf16 v[40:43], a[64:67], a[152:155], v[40:43]\n\t"
"v_cvt_pk_bf16_f32 v189, v190, v191\n\t"
"ds_read_b64_tr_b16 a[176:177], v220 offset:62208\n\t"
"v_mfma_f32_16x16x32_bf16 v[40:43], a[68:71], a[168:171], v[40:43]\n\t"
"ds_write_b128 v221, v[192:195] offset:4096\n\t"
"v_mfma_f32_16x16x32_bf16 v[44:47], a[64:67], a[156:159], v[44:47]\n\t"
"ds_read_b64_tr_b16 a[178:179], v220 offset:62720\n\t"
"v_mfma_f32_16x16x32_bf16 v[44:47], a[68:71], a[172:175], v[44:47]\n\t"
"buffer_load_dwordx4 a[64:67], v235, s[8:11], s41 offen offset:2048 sc1\n\t"
"buffer_load_dwordx4 a[68:71], v235, s[8:11], s41 offen offset:3072 sc1\n\t"
"v_mfma_f32_16x16x32_bf16 v[28:31], a[56:59], a[156:159], v[28:31]\n\t"
"ds_read_b64_tr_b16 a[180:181], v255 offset:62208\n\t"
"v_cvt_pk_bf16_f32 v202, v204, v205\n\t"
"v_mfma_f32_16x16x32_bf16 v[28:31], a[60:63], a[172:175], v[28:31]\n\t"
"buffer_load_dwordx4 a[60:63], v235, s[8:11], s41 offen offset:1024 sc1\n\t"
"ds_read_b64_tr_b16 a[182:183], v255 offset:62720\n\t"
"ds_read_b64_tr_b16 a[184:185], v226 offset:64512\n\t"
"v_mfma_f32_16x16x32_bf16 v[12:15], a[48:51], a[156:159], v[12:15]\n\t"
"ds_read_b64_tr_b16 a[186:187], v226 offset:65024\n\t"
"v_cvt_pk_bf16_f32 v203, v206, v207\n\t"
"v_mfma_f32_16x16x32_bf16 v[12:15], a[52:55], a[172:175], v[12:15]\n\t"
"ds_write_b128 v221, v[200:203] offset:12288\n\t"
"s_waitcnt vmcnt(37) lgkmcnt(9)\n\t"
"v_mfma_f32_16x16x32_bf16 v[96:99], a[72:75], a[144:147], v[96:99]\n\t"
"ds_read_b64_tr_b16 a[188:189], v218 offset:64512\n\t"
"v_mfma_f32_16x16x32_bf16 v[96:99], a[76:79], a[160:163], v[96:99]\n\t"
"ds_read_b64_tr_b16 a[190:191], v218 offset:65024\n\t"
"ds_read_b64_tr_b16 a[192:193], v226 offset:62208\n\t"
"ds_read_b64_tr_b16 a[194:195], v226 offset:62720\n\t"
"ds_read_b64_tr_b16 a[196:197], v218 offset:62208\n\t"
"ds_read_b64_tr_b16 a[198:199], v218 offset:62720\n\t"
"ds_read_b64_tr_b16 a[200:201], v220 offset:64512\n\t"
"ds_read_b64_tr_b16 a[202:203], v220 offset:65024\n\t"
"ds_read_b64_tr_b16 a[204:205], v255 offset:64512\n\t"
"ds_read_b64_tr_b16 a[206:207], v255 offset:65024\n\t"
"ds_write2st64_b64 v247, v[184:185], v[188:189] offset0:58 offset1:71\n\t"
"buffer_load_dwordx4 a[56:59], v235, s[8:11], s41 offen offset:0 sc1\n\t"
"v_mfma_f32_16x16x32_bf16 v[112:115], a[80:83], a[144:147], v[112:115]\n\t"
"v_mfma_f32_16x16x32_bf16 v[112:115], a[84:87], a[160:163], v[112:115]\n\t"
"buffer_load_dwordx4 a[48:51], v248, s[8:11], s41 offen offset:2048 sc1\n\t"
"buffer_load_dwordx4 a[52:55], v248, s[8:11], s41 offen offset:3072 sc1\n\t"
"v_mfma_f32_16x16x32_bf16 v[128:131], a[88:91], a[144:147], v[128:131]\n\t"
"v_mfma_f32_16x16x32_bf16 v[128:131], a[92:95], a[160:163], v[128:131]\n\t"
"v_mfma_f32_16x16x32_bf16 v[132:135], a[88:91], a[148:151], v[132:135]\n\t"
"ds_read_b128 a[152:155], v216 offset:22528\n\t"
"v_mfma_f32_16x16x32_bf16 v[132:135], a[92:95], a[164:167], v[132:135]\n\t"
"ds_read_b128 a[168:171], v216 offset:30720\n\t"
"ds_read_b128 a[156:159], v216 offset:23552\n\t"
"ds_read_b128 a[172:175], v216 offset:31744\n\t"
"v_mfma_f32_16x16x32_bf16 v[116:119], a[80:83], a[148:151], v[116:119]\n\t"
"v_mfma_f32_16x16x32_bf16 v[116:119], a[84:87], a[164:167], v[116:119]\n\t"
"buffer_store_dwordx4 a[236:239], v234, s[16:19], s28 offen offset:384 nt\n\t"
"v_mfma_f32_16x16x32_bf16 v[100:103], a[72:75], a[148:151], v[100:103]\n\t"
"v_mfma_f32_16x16x32_bf16 v[100:103], a[76:79], a[164:167], v[100:103]\n\t"
"s_waitcnt lgkmcnt(0)\n\t"
"s_barrier\n\t"
"v_mfma_f32_16x16x32_bf16 v[104:107], a[72:75], a[152:155], v[104:107]\n\t"
"ds_read_b128 a[144:147], v216 offset:0\n\t"
"v_cvt_pk_bf16_f32 v56, v56, v57\n\t"
"v_mfma_f32_16x16x32_bf16 v[104:107], a[76:79], a[168:171], v[104:107]\n\t"
"ds_read_b128 a[160:163], v216 offset:8192\n\t"
"ds_read_b128 a[148:151], v216 offset:1024\n\t"
"v_mfma_f32_16x16x32_bf16 v[120:123], a[80:83], a[152:155], v[120:123]\n\t"
"ds_read_b128 a[164:167], v216 offset:9216\n\t"
"v_cvt_pk_bf16_f32 v57, v58, v59\n\t"
"v_mfma_f32_16x16x32_bf16 v[120:123], a[84:87], a[168:171], v[120:123]\n\t"
"v_cvt_pk_bf16_f32 v60, v60, v61\n\t"
"v_cvt_pk_bf16_f32 v61, v62, v63\n\t"
"v_mfma_f32_16x16x32_bf16 v[136:139], a[88:91], a[152:155], v[136:139]\n\t"
"ds_read_b128 a[228:231], v229 offset:0\n\t"
"v_cvt_pk_bf16_f32 v72, v72, v73\n\t"
"v_mfma_f32_16x16x32_bf16 v[136:139], a[92:95], a[168:171], v[136:139]\n\t"
"ds_read_b128 a[152:155], v216 offset:2048\n\t"
"v_cvt_pk_bf16_f32 v73, v74, v75\n\t"
"v_mfma_f32_16x16x32_bf16 v[208:211], a[176:179], a[220:223], 0\n\t"
"ds_read_b128 a[232:235], v230 offset:0\n\t"
"v_cvt_pk_bf16_f32 v76, v76, v77\n\t"
"v_mfma_f32_16x16x32_bf16 v[140:143], a[88:91], a[156:159], v[140:143]\n\t"
"ds_read_b128 a[168:171], v216 offset:10240\n\t"
"v_cvt_pk_bf16_f32 v77, v78, v79\n\t"
"v_mfma_f32_16x16x32_bf16 v[140:143], a[92:95], a[172:175], v[140:143]\n\t"
"buffer_load_dwordx4 a[88:91], v235, s[8:11], s53 offen offset:2048 sc1\n\t"
"v_mfma_f32_16x16x32_bf16 v[212:215], a[180:183], a[220:223], 0\n\t"
"buffer_load_dwordx4 a[92:95], v235, s[8:11], s53 offen offset:3072 sc1\n\t"
"v_mfma_f32_16x16x32_bf16 v[124:127], a[80:83], a[156:159], v[124:127]\n\t"
"v_cvt_pk_bf16_f32 v208, v208, v209\n\t"
"v_cvt_pk_bf16_f32 v209, v210, v211\n\t"
"v_mfma_f32_16x16x32_bf16 v[124:127], a[84:87], a[172:175], v[124:127]\n\t"
"buffer_load_dwordx4 a[84:87], v235, s[8:11], s53 offen offset:1024 sc1\n\t"
"v_mfma_f32_16x16x32_bf16 v[108:111], a[72:75], a[156:159], v[108:111]\n\t"
"ds_read_b128 a[236:239], v231 offset:0\n\t"
"v_cvt_pk_bf16_f32 v88, v88, v89\n\t"
"v_mfma_f32_16x16x32_bf16 v[108:111], a[76:79], a[172:175], v[108:111]\n\t"
"buffer_load_dwordx4 a[80:83], v235, s[8:11], s53 offen offset:0 sc1\n\t"
"v_mfma_f32_16x16x32_bf16 v[192:195], a[184:187], a[220:223], 0\n\t"
"buffer_load_dwordx4 a[72:75], v248, s[8:11], s53 offen offset:2048 sc1\n\t"
"s_waitcnt vmcnt(38) lgkmcnt(4)\n\t"
"v_mfma_f32_16x16x32_bf16 v[0:3], a[96:99], a[144:147], v[0:3]\n\t"
"v_cvt_pk_bf16_f32 v210, v212, v213\n\t"
"v_cvt_pk_bf16_f32 v211, v214, v215\n\t"
"v_mfma_f32_16x16x32_bf16 v[0:3], a[100:103], a[160:163], v[0:3]\n\t"
"buffer_load_dwordx4 a[76:79], v248, s[8:11], s53 offen offset:3072 sc1\n\t"
"v_mfma_f32_16x16x32_bf16 v[196:199], a[188:191], a[220:223], 0\n\t"
"buffer_store_dwordx4 a[228:231], v232, s[16:19], s20 offen offset:384 nt\n\t"
"v_mfma_f32_16x16x32_bf16 v[16:19], a[104:107], a[144:147], v[16:19]\n\t"
"ds_read_b128 a[156:159], v216 offset:3072\n\t"
"v_cvt_pk_bf16_f32 v192, v192, v193\n\t"
"v_mfma_f32_16x16x32_bf16 v[16:19], a[108:111], a[160:163], v[16:19]\n\t"
"ds_write2st64_b64 v228, v[56:57], v[60:61] offset0:26 offset1:39\n\t"
"v_mfma_f32_16x16x32_bf16 v[32:35], a[112:115], a[144:147], v[32:35]\n\t"
"ds_read_b128 a[172:175], v216 offset:11264\n\t"
"v_cvt_pk_bf16_f32 v193, v194, v195\n\t"
"v_mfma_f32_16x16x32_bf16 v[32:35], a[116:119], a[160:163], v[32:35]\n\t"
"s_waitcnt lgkmcnt(4)\n\t"
"buffer_store_dwordx4 a[232:235], v233, s[16:19], s20 offen offset:384 nt\n\t"
"ds_write_b128 v217, v[208:211] offset:16384\n\t"
"v_mfma_f32_16x16x32_bf16 v[200:203], a[192:195], a[220:223], 0\n\t"
"v_cvt_pk_bf16_f32 v194, v196, v197\n\t"
"v_cvt_pk_bf16_f32 v195, v198, v199\n\t"
"v_mfma_f32_16x16x32_bf16 v[36:39], a[112:115], a[148:151], v[36:39]\n\t"
"ds_read_b128 a[144:147], v216 offset:4096\n\t"
"v_cvt_pk_bf16_f32 v89, v90, v91\n\t"
"v_mfma_f32_16x16x32_bf16 v[36:39], a[116:119], a[164:167], v[36:39]\n\t"
"ds_read_b128 a[160:163], v216 offset:12288\n\t"
"v_cvt_pk_bf16_f32 v92, v92, v93\n\t"
"v_mfma_f32_16x16x32_bf16 v[20:23], a[104:107], a[148:151], v[20:23]\n\t"
"v_cvt_pk_bf16_f32 v93, v94, v95\n\t"
"ds_read_b64_tr_b16 a[176:177], v251 offset:20736\n\t"
"v_mfma_f32_16x16x32_bf16 v[20:23], a[108:111], a[164:167], v[20:23]\n\t"
"s_waitcnt lgkmcnt(7)\n\t"
"buffer_store_dwordx4 a[236:239], v234, s[16:19], s20 offen offset:384 nt\n\t"
"v_mfma_f32_16x16x32_bf16 v[204:207], a[196:199], a[220:223], 0\n\t"
"ds_write_b128 v219, v[192:195] offset:16384\n\t"
"v_mfma_f32_16x16x32_bf16 v[4:7], a[96:99], a[148:151], v[4:7]\n\t"
"v_cvt_pk_bf16_f32 v200, v200, v201\n\t"
"v_cvt_pk_bf16_f32 v201, v202, v203\n\t"
"v_mfma_f32_16x16x32_bf16 v[4:7], a[100:103], a[164:167], v[4:7]\n\t"
"ds_write2st64_b64 v246, v[72:73], v[76:77] offset0:26 offset1:39\n\t"
"v_mfma_f32_16x16x32_bf16 v[208:211], a[200:203], a[220:223], 0\n\t"
"ds_read_b128 a[148:151], v216 offset:5120\n\t"
"v_mfma_f32_16x16x32_bf16 v[8:11], a[96:99], a[152:155], v[8:11]\n\t"
"ds_read_b128 a[164:167], v216 offset:13312\n\t"
"v_cvt_pk_bf16_f32 v202, v204, v205\n\t"
"v_mfma_f32_16x16x32_bf16 v[8:11], a[100:103], a[168:171], v[8:11]\n\t"
"ds_read_b128 a[228:231], v229 offset:29696\n\t"
"v_cvt_pk_bf16_f32 v203, v206, v207\n\t"
"ds_read_b128 a[232:235], v230 offset:29696\n\t"
"v_mfma_f32_16x16x32_bf16 v[24:27], a[104:107], a[152:155], v[24:27]\n\t"
"ds_read_b128 a[236:239], v231 offset:29696\n\t"
"v_mfma_f32_16x16x32_bf16 v[24:27], a[108:111], a[168:171], v[24:27]\n\t"
"v_cvt_pk_bf16_f32 v208, v208, v209\n\t"
"v_cvt_pk_bf16_f32 v209, v210, v211\n\t"
"ds_write_b128 v217, v[200:203] offset:24576\n\t"
"v_mfma_f32_16x16x32_bf16 v[212:215], a[204:207], a[220:223], 0\n\t"
"ds_read_b64_tr_b16 a[178:179], v251 offset:21248\n\t"
"v_mfma_f32_16x16x32_bf16 v[40:43], a[112:115], a[152:155], v[40:43]\n\t"
"ds_read_b64_tr_b16 a[180:181], v252 offset:20736\n\t"
"v_mfma_f32_16x16x32_bf16 v[40:43], a[116:119], a[168:171], v[40:43]\n\t"
"ds_read_b64_tr_b16 a[182:183], v252 offset:21248\n\t"
"ds_read_b64_tr_b16 a[184:185], v249 offset:20736\n\t"
"s_waitcnt lgkmcnt(11)\n\t"
"v_mfma_f32_16x16x32_bf16 v[44:47], a[112:115], a[156:159], v[44:47]\n\t"
"ds_read_b64_tr_b16 a[186:187], v249 offset:21248\n\t"
"v_mfma_f32_16x16x32_bf16 v[44:47], a[116:119], a[172:175], v[44:47]\n\t"
"buffer_load_dwordx4 a[112:115], v235, s[8:11], s56 offen offset:2048 sc1\n\t"
"s_waitcnt lgkmcnt(8)\n\t"
"buffer_store_dwordx4 a[228:231], v232, s[16:19], s25 offen offset:384 nt\n\t"
"v_mfma_f32_16x16x32_bf16 v[28:31], a[104:107], a[156:159], v[28:31]\n\t"
"ds_read_b64_tr_b16 a[188:189], v250 offset:20736\n\t"
"v_cvt_pk_bf16_f32 v210, v212, v213\n\t"
"v_mfma_f32_16x16x32_bf16 v[28:31], a[108:111], a[172:175], v[28:31]\n\t"
"buffer_load_dwordx4 a[116:119], v235, s[8:11], s56 offen offset:3072 sc1\n\t"
"buffer_load_dwordx4 a[108:111], v235, s[8:11], s56 offen offset:1024 sc1\n\t"
"v_mfma_f32_16x16x32_bf16 v[12:15], a[96:99], a[156:159], v[12:15]\n\t"
"ds_read_b64_tr_b16 a[190:191], v250 offset:21248\n\t"
"v_cvt_pk_bf16_f32 v211, v214, v215\n\t"
"v_mfma_f32_16x16x32_bf16 v[12:15], a[100:103], a[172:175], v[12:15]\n\t"
"ds_write_b128 v219, v[208:211] offset:24576\n\t"
"ds_read_b64_tr_b16 a[192:193], v251 offset:21888\n\t"
"s_waitcnt vmcnt(38) lgkmcnt(10)\n\t"
"v_mfma_f32_16x16x32_bf16 v[96:99], a[120:123], a[144:147], v[96:99]\n\t"
"ds_read_b64_tr_b16 a[194:195], v251 offset:22400\n\t"
"v_mfma_f32_16x16x32_bf16 v[96:99], a[124:127], a[160:163], v[96:99]\n\t"
"ds_read_b64_tr_b16 a[196:197], v252 offset:21888\n\t"
"ds_read_b64_tr_b16 a[198:199], v252 offset:22400\n\t"
"ds_read_b64_tr_b16 a[200:201], v249 offset:21888\n\t"
"ds_read_b64_tr_b16 a[202:203], v249 offset:22400\n\t"
"ds_read_b64_tr_b16 a[204:205], v250 offset:21888\n\t"
"ds_read_b64_tr_b16 a[206:207], v250 offset:22400\n\t"
"ds_write2st64_b64 v247, v[88:89], v[92:93] offset0:26 offset1:39\n\t"
"buffer_load_dwordx4 a[104:107], v235, s[8:11], s56 offen offset:0 sc1\n\t"
"v_mfma_f32_16x16x32_bf16 v[112:115], a[128:131], a[144:147], v[112:115]\n\t"
"v_mfma_f32_16x16x32_bf16 v[112:115], a[132:135], a[160:163], v[112:115]\n\t"
"buffer_load_dwordx4 a[96:99], v248, s[8:11], s56 offen offset:2048 sc1\n\t"
"buffer_store_dwordx4 a[232:235], v233, s[16:19], s25 offen offset:384 nt\n\t"
"buffer_load_dwordx4 a[100:103], v248, s[8:11], s56 offen offset:3072 sc1\n\t"
"v_mfma_f32_16x16x32_bf16 v[128:131], a[136:139], a[144:147], v[128:131]\n\t"
"v_mfma_f32_16x16x32_bf16 v[128:131], a[140:143], a[160:163], v[128:131]\n\t"
"ds_read_b128 a[152:155], v216 offset:6144\n\t"
"v_mfma_f32_16x16x32_bf16 v[132:135], a[136:139], a[148:151], v[132:135]\n\t"
"ds_read_b128 a[168:171], v216 offset:14336\n\t"
"v_mfma_f32_16x16x32_bf16 v[132:135], a[140:143], a[164:167], v[132:135]\n\t"
"ds_read_b128 a[156:159], v216 offset:7168\n\t"
"ds_read_b128 a[172:175], v216 offset:15360\n\t"
"v_mfma_f32_16x16x32_bf16 v[116:119], a[128:131], a[148:151], v[116:119]\n\t"
"v_mfma_f32_16x16x32_bf16 v[116:119], a[132:135], a[164:167], v[116:119]\n\t"
"buffer_store_dwordx4 a[236:239], v234, s[16:19], s25 offen offset:384 nt\n\t"
"v_mfma_f32_16x16x32_bf16 v[100:103], a[120:123], a[148:151], v[100:103]\n\t"
"v_mfma_f32_16x16x32_bf16 v[100:103], a[124:127], a[164:167], v[100:103]\n\t"
"s_waitcnt lgkmcnt(0)\n\t"
"s_barrier\n\t"
"v_mfma_f32_16x16x32_bf16 v[104:107], a[120:123], a[152:155], v[104:107]\n\t"
"ds_read_b128 a[144:147], v216 offset:16384\n\t"
"v_mfma_f32_16x16x32_bf16 v[104:107], a[124:127], a[168:171], v[104:107]\n\t"
"ds_read_b128 a[160:163], v216 offset:24576\n\t"
"ds_read_b128 a[148:151], v216 offset:17408\n\t"
"v_mfma_f32_16x16x32_bf16 v[120:123], a[128:131], a[152:155], v[120:123]\n\t"
"ds_read_b128 a[164:167], v216 offset:25600\n\t"
"v_mfma_f32_16x16x32_bf16 v[120:123], a[132:135], a[168:171], v[120:123]\n\t"
"v_mfma_f32_16x16x32_bf16 v[136:139], a[136:139], a[152:155], v[136:139]\n\t"
"ds_read_b128 a[228:231], v229 offset:13312\n\t"
"v_mfma_f32_16x16x32_bf16 v[136:139], a[140:143], a[168:171], v[136:139]\n\t"
"ds_read_b128 a[152:155], v216 offset:18432\n\t"
"v_mfma_f32_16x16x32_bf16 v[192:195], a[176:179], a[224:227], 0\n\t"
"ds_read_b128 a[232:235], v230 offset:13312\n\t"
"ds_read_b128 a[168:171], v216 offset:26624\n\t"
"v_mfma_f32_16x16x32_bf16 v[140:143], a[136:139], a[156:159], v[140:143]\n\t"
"ds_read_b64_tr_b16 a[176:177], v251 offset:62208\n\t"
"v_mfma_f32_16x16x32_bf16 v[140:143], a[140:143], a[172:175], v[140:143]\n\t"
"buffer_load_dwordx4 a[136:139], v235, s[8:11], s68 offen offset:2048 sc1\n\t"
"buffer_load_dwordx4 a[140:143], v235, s[8:11], s68 offen offset:3072 sc1\n\t"
"v_mfma_f32_16x16x32_bf16 v[196:199], a[180:183], a[224:227], 0\n\t"
"ds_read_b128 a[236:239], v231 offset:13312\n\t"
"v_mfma_f32_16x16x32_bf16 v[124:127], a[128:131], a[156:159], v[124:127]\n\t"
"v_cvt_pk_bf16_f32 v192, v192, v193\n\t"
"v_cvt_pk_bf16_f32 v193, v194, v195\n\t"
"v_mfma_f32_16x16x32_bf16 v[124:127], a[132:135], a[172:175], v[124:127]\n\t"
"buffer_load_dwordx4 a[132:135], v235, s[8:11], s68 offen offset:1024 sc1\n\t"
"v_mfma_f32_16x16x32_bf16 v[108:111], a[120:123], a[156:159], v[108:111]\n\t"
"ds_read_b128 a[156:159], v216 offset:19456\n\t"
"v_mfma_f32_16x16x32_bf16 v[108:111], a[124:127], a[172:175], v[108:111]\n\t"
"buffer_load_dwordx4 a[128:131], v235, s[8:11], s68 offen offset:0 sc1\n\t"
"v_mfma_f32_16x16x32_bf16 v[200:203], a[184:187], a[224:227], 0\n\t"
"buffer_load_dwordx4 a[120:123], v248, s[8:11], s68 offen offset:2048 sc1\n\t"
"s_waitcnt vmcnt(38) lgkmcnt(6)\n\t"
"v_mfma_f32_16x16x32_bf16 v[144:147], a[0:3], a[144:147], 0\n\t"
"v_cvt_pk_bf16_f32 v194, v196, v197\n\t"
"v_cvt_pk_bf16_f32 v195, v198, v199\n\t"
"v_mfma_f32_16x16x32_bf16 v[144:147], a[4:7], a[160:163], v[144:147]\n\t"
"buffer_load_dwordx4 a[124:127], v248, s[8:11], s68 offen offset:3072 sc1\n\t"
"v_mfma_f32_16x16x32_bf16 v[204:207], a[188:191], a[224:227], 0\n\t"
"ds_read_b128 a[172:175], v216 offset:27648\n\t"
"v_mfma_f32_16x16x32_bf16 v[160:163], a[8:11], a[144:147], 0\n\t"
"v_cvt_pk_bf16_f32 v200, v200, v201\n\t"
"v_cvt_pk_bf16_f32 v201, v202, v203\n\t"
"v_mfma_f32_16x16x32_bf16 v[160:163], a[12:15], a[160:163], v[160:163]\n\t"
"ds_write_b128 v221, v[192:195] offset:0\n\t"
"v_mfma_f32_16x16x32_bf16 v[176:179], a[16:19], a[144:147], 0\n\t"
"ds_read_b128 a[144:147], v216 offset:20480\n\t"
"v_mfma_f32_16x16x32_bf16 v[176:179], a[20:23], a[160:163], v[176:179]\n\t"
"buffer_store_dwordx4 a[228:231], v232, s[16:19], s27 offen offset:384 nt\n\t"
"v_mfma_f32_16x16x32_bf16 v[208:211], a[192:195], a[224:227], 0\n\t"
"v_cvt_pk_bf16_f32 v202, v204, v205\n\t"
"v_cvt_pk_bf16_f32 v203, v206, v207\n\t"
"v_mfma_f32_16x16x32_bf16 v[180:183], a[16:19], a[148:151], 0\n\t"
"ds_read_b128 a[160:163], v216 offset:28672\n\t"
"v_mfma_f32_16x16x32_bf16 v[180:183], a[20:23], a[164:167], v[180:183]\n\t"
"ds_write_b128 v221, v[200:203] offset:8192\n\t"
"v_mfma_f32_16x16x32_bf16 v[164:167], a[8:11], a[148:151], 0\n\t"
"ds_read_b64_tr_b16 a[178:179], v251 offset:62720\n\t"
"v_mfma_f32_16x16x32_bf16 v[164:167], a[12:15], a[164:167], v[164:167]\n\t"
"v_cvt_pk_bf16_f32 v208, v208, v209\n\t"
"v_cvt_pk_bf16_f32 v209, v210, v211\n\t"
"v_mfma_f32_16x16x32_bf16 v[212:215], a[196:199], a[224:227], 0\n\t"
"s_waitcnt lgkmcnt(5)\n\t"
"buffer_store_dwordx4 a[232:235], v233, s[16:19], s27 offen offset:384 nt\n\t"
"v_mfma_f32_16x16x32_bf16 v[148:151], a[0:3], a[148:151], 0\n\t"
"ds_read_b128 a[148:151], v216 offset:21504\n\t"
"v_mfma_f32_16x16x32_bf16 v[148:151], a[4:7], a[164:167], v[148:151]\n\t"
"ds_read_b128 a[164:167], v216 offset:29696\n\t"
"v_mfma_f32_16x16x32_bf16 v[192:195], a[200:203], a[224:227], 0\n\t"
"ds_read_b64_tr_b16 a[180:181], v252 offset:62208\n\t"
"ds_read_b64_tr_b16 a[182:183], v252 offset:62720\n\t"
"v_mfma_f32_16x16x32_bf16 v[152:155], a[0:3], a[152:155], 0\n\t"
"v_cvt_pk_bf16_f32 v210, v212, v213\n\t"
"v_cvt_pk_bf16_f32 v211, v214, v215\n\t"
"v_mfma_f32_16x16x32_bf16 v[152:155], a[4:7], a[168:171], v[152:155]\n\t"
"ds_write_b128 v221, v[208:211] offset:4096\n\t"
"v_mfma_f32_16x16x32_bf16 v[168:171], a[8:11], a[152:155], 0\n\t"
"ds_read_b64_tr_b16 a[184:185], v249 offset:62208\n\t"
"v_mfma_f32_16x16x32_bf16 v[168:171], a[12:15], a[168:171], v[168:171]\n\t"
"v_cvt_pk_bf16_f32 v192, v192, v193\n\t"
"v_cvt_pk_bf16_f32 v193, v194, v195\n\t"
"ds_read_b64_tr_b16 a[186:187], v249 offset:62720\n\t"
"ds_read_b64_tr_b16 a[188:189], v250 offset:62208\n\t"
"v_mfma_f32_16x16x32_bf16 v[196:199], a[204:207], a[224:227], 0\n\t"
"ds_read_b64_tr_b16 a[190:191], v250 offset:62720\n\t"
"v_mfma_f32_16x16x32_bf16 v[184:187], a[16:19], a[152:155], 0\n\t"
"ds_read_b64_tr_b16 a[192:193], v251 offset:63360\n\t"
"v_mfma_f32_16x16x32_bf16 v[184:187], a[20:23], a[168:171], v[184:187]\n\t"
"ds_read_b64_tr_b16 a[194:195], v251 offset:63872\n\t"
"v_mfma_f32_16x16x32_bf16 v[188:191], a[16:19], a[156:159], 0\n\t"
"ds_read_b64_tr_b16 a[196:197], v252 offset:63360\n\t"
"v_mfma_f32_16x16x32_bf16 v[188:191], a[20:23], a[172:175], v[188:191]\n\t"
"buffer_load_dwordx4 a[16:19], v248, s[8:11], s40 offen offset:2048 sc1\n\t"
"buffer_load_dwordx4 a[20:23], v248, s[8:11], s40 offen offset:3072 sc1\n\t"
"v_mfma_f32_16x16x32_bf16 v[172:175], a[8:11], a[156:159], 0\n\t"
"ds_read_b64_tr_b16 a[198:199], v252 offset:63872\n\t"
"v_cvt_pk_bf16_f32 v194, v196, v197\n\t"
"v_mfma_f32_16x16x32_bf16 v[172:175], a[12:15], a[172:175], v[172:175]\n\t"
"buffer_load_dwordx4 a[12:15], v248, s[8:11], s40 offen offset:1024 sc1\n\t"
"v_mfma_f32_16x16x32_bf16 v[156:159], a[0:3], a[156:159], 0\n\t"
"ds_read_b64_tr_b16 a[200:201], v249 offset:63360\n\t"
"v_cvt_pk_bf16_f32 v195, v198, v199\n\t"
"v_mfma_f32_16x16x32_bf16 v[156:159], a[4:7], a[172:175], v[156:159]\n\t"
"ds_write_b128 v221, v[192:195] offset:12288\n\t"
"s_waitcnt vmcnt(38) lgkmcnt(9)\n\t"
"v_mfma_f32_16x16x32_bf16 v[48:51], a[24:27], a[144:147], v[144:147]\n\t"
"ds_read_b64_tr_b16 a[202:203], v249 offset:63872\n\t"
"v_mfma_f32_16x16x32_bf16 v[48:51], a[28:31], a[160:163], v[48:51]\n\t"
"ds_read_b64_tr_b16 a[204:205], v250 offset:63360\n\t"
"ds_read_b64_tr_b16 a[206:207], v250 offset:63872\n\t"
"buffer_load_dwordx4 a[8:11], v248, s[8:11], s40 offen offset:0 sc1\n\t"
"v_mfma_f32_16x16x32_bf16 v[64:67], a[32:35], a[144:147], v[160:163]\n\t"
"ds_read_b128 a[152:155], v216 offset:22528\n\t"
"v_mfma_f32_16x16x32_bf16 v[64:67], a[36:39], a[160:163], v[64:67]\n\t"
"buffer_load_dwordx4 a[0:3], v235, s[8:11], 0 offen offset:2048 sc1\n\t"
"buffer_load_dwordx4 a[4:7], v235, s[8:11], 0 offen offset:3072 sc1\n\t"
"v_mfma_f32_16x16x32_bf16 v[80:83], a[40:43], a[144:147], v[176:179]\n\t"
"ds_read_b128 a[168:171], v216 offset:30720\n\t"
"v_mfma_f32_16x16x32_bf16 v[80:83], a[44:47], a[160:163], v[80:83]\n\t"
"ds_read_b128 a[156:159], v216 offset:23552\n\t"
"v_mfma_f32_16x16x32_bf16 v[84:87], a[40:43], a[148:151], v[180:183]\n\t"
"ds_read_b128 a[172:175], v216 offset:31744\n\t"
"v_mfma_f32_16x16x32_bf16 v[84:87], a[44:47], a[164:167], v[84:87]\n\t"
"ds_write_b128 v222, a[248:251] offset:0\n\t"
"v_mfma_f32_16x16x32_bf16 v[68:71], a[32:35], a[148:151], v[164:167]\n\t"
"v_mfma_f32_16x16x32_bf16 v[68:71], a[36:39], a[164:167], v[68:71]\n\t"
"buffer_store_dwordx4 a[236:239], v234, s[16:19], s27 offen offset:384 nt\n\t"
"v_mfma_f32_16x16x32_bf16 v[52:55], a[24:27], a[148:151], v[148:151]\n\t"
"v_mfma_f32_16x16x32_bf16 v[52:55], a[28:31], a[164:167], v[52:55]\n\t"
"s_waitcnt lgkmcnt(0)\n\t"
"s_barrier\n\t"
"v_mfma_f32_16x16x32_bf16 v[56:59], a[24:27], a[152:155], v[152:155]\n\t"
"ds_read_b128 a[144:147], v216 offset:0\n\t"
"v_add_u32 v249, 80896, v225\n\t"
"v_mfma_f32_16x16x32_bf16 v[56:59], a[28:31], a[168:171], v[56:59]\n\t"
"ds_read_b128 a[160:163], v216 offset:8192\n\t"
"ds_read_b128 a[148:151], v216 offset:1024\n\t"
"v_mfma_f32_16x16x32_bf16 v[72:75], a[32:35], a[152:155], v[168:171]\n\t"
"ds_read_b128 a[164:167], v216 offset:9216\n\t"
"v_xor_b32 v250, 32, v249\n\t"
"v_mfma_f32_16x16x32_bf16 v[72:75], a[36:39], a[168:171], v[72:75]\n\t"
"v_xor_b32 v251, 64, v249\n\t"
"v_xor_b32 v252, 96, v249\n\t"
"v_mfma_f32_16x16x32_bf16 v[88:91], a[40:43], a[152:155], v[184:187]\n\t"
"ds_read_b128 a[152:155], v216 offset:2048\n\t"
"v_mfma_f32_16x16x32_bf16 v[88:91], a[44:47], a[168:171], v[88:91]\n\t"
"ds_read_b128 a[168:171], v216 offset:10240\n\t"
"v_mfma_f32_16x16x32_bf16 v[200:203], a[176:179], a[224:227], 0\n\t"
"ds_read_b64_tr_b16 a[176:177], v249 offset:0\n\t"
"v_mfma_f32_16x16x32_bf16 v[92:95], a[40:43], a[156:159], v[188:191]\n\t"
"ds_read_b64_tr_b16 a[178:179], v249 offset:512\n\t"
"v_mfma_f32_16x16x32_bf16 v[92:95], a[44:47], a[172:175], v[92:95]\n\t"
"buffer_load_dwordx4 a[40:43], v248, s[8:11], s41 offen offset:0 sc1\n\t"
"v_mfma_f32_16x16x32_bf16 v[204:207], a[180:183], a[224:227], 0\n\t"
"buffer_load_dwordx4 a[44:47], v248, s[8:11], s41 offen offset:1024 sc1\n\t"
"v_mfma_f32_16x16x32_bf16 v[76:79], a[32:35], a[156:159], v[172:175]\n\t"
"v_cvt_pk_bf16_f32 v200, v200, v201\n\t"
"v_cvt_pk_bf16_f32 v201, v202, v203\n\t"
"v_mfma_f32_16x16x32_bf16 v[76:79], a[36:39], a[172:175], v[76:79]\n\t"
"buffer_load_dwordx4 a[36:39], v235, s[8:11], s40 offen offset:3072 sc1\n\t"
"v_mfma_f32_16x16x32_bf16 v[60:63], a[24:27], a[156:159], v[156:159]\n\t"
"ds_read_b128 a[156:159], v216 offset:3072\n\t"
"v_mfma_f32_16x16x32_bf16 v[60:63], a[28:31], a[172:175], v[60:63]\n\t"
"buffer_load_dwordx4 a[32:35], v235, s[8:11], s40 offen offset:2048 sc1\n\t"
"v_mfma_f32_16x16x32_bf16 v[208:211], a[184:187], a[224:227], 0\n\t"
"v_pk_fma_f32 v[144:145], v[144:145], s[38:39], v[48:49] neg_lo:[1,0,1] neg_hi:[1,0,1]\n\t"
"v_pk_fma_f32 v[146:147], v[146:147], s[38:39], v[50:51] neg_lo:[1,0,1] neg_hi:[1,0,1]\n\t"
"v_pk_fma_f32 v[160:161], v[160:161], s[38:39], v[64:65] neg_lo:[1,0,1] neg_hi:[1,0,1]\n\t"
"v_pk_fma_f32 v[162:163], v[162:163], s[38:39], v[66:67] neg_lo:[1,0,1] neg_hi:[1,0,1]\n\t"
"v_pk_fma_f32 v[176:177], v[176:177], s[38:39], v[80:81] neg_lo:[1,0,1] neg_hi:[1,0,1]\n\t"
"v_pk_fma_f32 v[178:179], v[178:179], s[38:39], v[82:83] neg_lo:[1,0,1] neg_hi:[1,0,1]\n\t"
"v_pk_fma_f32 v[180:181], v[180:181], s[38:39], v[84:85] neg_lo:[1,0,1] neg_hi:[1,0,1]\n\t"
"v_pk_fma_f32 v[182:183], v[182:183], s[38:39], v[86:87] neg_lo:[1,0,1] neg_hi:[1,0,1]\n\t"
"v_pk_fma_f32 v[164:165], v[164:165], s[38:39], v[68:69] neg_lo:[1,0,1] neg_hi:[1,0,1]\n\t"
"v_pk_fma_f32 v[166:167], v[166:167], s[38:39], v[70:71] neg_lo:[1,0,1] neg_hi:[1,0,1]\n\t"
"v_pk_fma_f32 v[148:149], v[148:149], s[38:39], v[52:53] neg_lo:[1,0,1] neg_hi:[1,0,1]\n\t"
"v_pk_fma_f32 v[150:151], v[150:151], s[38:39], v[54:55] neg_lo:[1,0,1] neg_hi:[1,0,1]\n\t"
"v_pk_fma_f32 v[152:153], v[152:153], s[38:39], v[56:57] neg_lo:[1,0,1] neg_hi:[1,0,1]\n\t"
"v_pk_fma_f32 v[154:155], v[154:155], s[38:39], v[58:59] neg_lo:[1,0,1] neg_hi:[1,0,1]\n\t"
"v_pk_fma_f32 v[168:169], v[168:169], s[38:39], v[72:73] neg_lo:[1,0,1] neg_hi:[1,0,1]\n\t"
"v_pk_fma_f32 v[170:171], v[170:171], s[38:39], v[74:75] neg_lo:[1,0,1] neg_hi:[1,0,1]\n\t"
"v_pk_fma_f32 v[184:185], v[184:185], s[38:39], v[88:89] neg_lo:[1,0,1] neg_hi:[1,0,1]\n\t"
"v_pk_fma_f32 v[186:187], v[186:187], s[38:39], v[90:91] neg_lo:[1,0,1] neg_hi:[1,0,1]\n\t"
"v_pk_fma_f32 v[188:189], v[188:189], s[38:39], v[92:93] neg_lo:[1,0,1] neg_hi:[1,0,1]\n\t"
"v_pk_fma_f32 v[190:191], v[190:191], s[38:39], v[94:95] neg_lo:[1,0,1] neg_hi:[1,0,1]\n\t"
"v_pk_fma_f32 v[172:173], v[172:173], s[38:39], v[76:77] neg_lo:[1,0,1] neg_hi:[1,0,1]\n\t"
"v_pk_fma_f32 v[174:175], v[174:175], s[38:39], v[78:79] neg_lo:[1,0,1] neg_hi:[1,0,1]\n\t"
"v_pk_fma_f32 v[156:157], v[156:157], s[38:39], v[60:61] neg_lo:[1,0,1] neg_hi:[1,0,1]\n\t"
"v_pk_fma_f32 v[158:159], v[158:159], s[38:39], v[62:63] neg_lo:[1,0,1] neg_hi:[1,0,1]\n\t"
"buffer_load_dwordx4 a[24:27], v235, s[8:11], s40 offen offset:0 sc1\n\t"
"s_waitcnt vmcnt(39) lgkmcnt(4)\n\t"
"v_mfma_f32_16x16x32_bf16 v[48:51], a[48:51], a[144:147], v[48:51]\n\t"
"v_cvt_pk_bf16_f32 v202, v204, v205\n\t"
"v_cvt_pk_bf16_f32 v203, v206, v207\n\t"
"v_mfma_f32_16x16x32_bf16 v[48:51], a[52:55], a[160:163], v[48:51]\n\t"
"ds_read_b128 a[172:175], v216 offset:11264\n\t"
"v_mfma_f32_16x16x32_bf16 v[212:215], a[188:191], a[224:227], 0\n\t"
"buffer_load_dwordx4 a[28:31], v235, s[8:11], s40 offen offset:1024 sc1\n\t"
"v_mfma_f32_16x16x32_bf16 v[64:67], a[56:59], a[144:147], v[64:67]\n\t"
"v_cvt_pk_bf16_f32 v208, v208, v209\n\t"
"v_cvt_pk_bf16_f32 v209, v210, v211\n\t"
"v_mfma_f32_16x16x32_bf16 v[64:67], a[60:63], a[160:163], v[64:67]\n\t"
"ds_read_b64_tr_b16 a[180:181], v250 offset:0\n\t"
"v_mfma_f32_16x16x32_bf16 v[80:83], a[64:67], a[144:147], v[80:83]\n\t"
"ds_read_b128 a[144:147], v216 offset:4096\n\t"
"v_mfma_f32_16x16x32_bf16 v[80:83], a[68:71], a[160:163], v[80:83]\n\t"
"ds_write_b128 v221, v[200:203] offset:16384\n\t"
"v_mfma_f32_16x16x32_bf16 v[192:195], a[192:195], a[224:227], 0\n\t"
"v_cvt_pk_bf16_f32 v210, v212, v213\n\t"
"v_cvt_pk_bf16_f32 v211, v214, v215\n\t"
"v_mfma_f32_16x16x32_bf16 v[84:87], a[64:67], a[148:151], v[84:87]\n\t"
"ds_read_b128 a[160:163], v216 offset:12288\n\t"
"v_mfma_f32_16x16x32_bf16 v[84:87], a[68:71], a[164:167], v[84:87]\n\t"
"ds_write_b128 v221, v[208:211] offset:24576\n\t"
"v_mfma_f32_16x16x32_bf16 v[68:71], a[56:59], a[148:151], v[68:71]\n\t"
"ds_read_b64_tr_b16 a[182:183], v250 offset:512\n\t"
"v_mfma_f32_16x16x32_bf16 v[68:71], a[60:63], a[164:167], v[68:71]\n\t"
"v_cvt_pk_bf16_f32 v192, v192, v193\n\t"
"v_cvt_pk_bf16_f32 v193, v194, v195\n\t"
"v_mfma_f32_16x16x32_bf16 v[196:199], a[196:199], a[224:227], 0\n\t"
"ds_read_b64_tr_b16 a[184:185], v251 offset:2304\n\t"
"v_mfma_f32_16x16x32_bf16 v[52:55], a[48:51], a[148:151], v[52:55]\n\t"
"ds_read_b128 a[148:151], v216 offset:5120\n\t"
"v_mfma_f32_16x16x32_bf16 v[52:55], a[52:55], a[164:167], v[52:55]\n\t"
"ds_read_b128 a[164:167], v216 offset:13312\n\t"
"v_mfma_f32_16x16x32_bf16 v[200:203], a[200:203], a[224:227], 0\n\t"
"ds_read_b64_tr_b16 a[186:187], v251 offset:2816\n\t"
"s_waitcnt lgkmcnt(8)\n\t"
"v_mfma_f32_16x16x32_bf16 v[56:59], a[48:51], a[152:155], v[56:59]\n\t"
"v_cvt_pk_bf16_f32 v194, v196, v197\n\t"
"v_cvt_pk_bf16_f32 v195, v198, v199\n\t"
"v_mfma_f32_16x16x32_bf16 v[56:59], a[52:55], a[168:171], v[56:59]\n\t"
"ds_write_b128 v221, v[192:195] offset:20480\n\t"
"v_mfma_f32_16x16x32_bf16 v[72:75], a[56:59], a[152:155], v[72:75]\n\t"
"ds_read_b64_tr_b16 a[188:189], v252 offset:2304\n\t"
"v_mfma_f32_16x16x32_bf16 v[72:75], a[60:63], a[168:171], v[72:75]\n\t"
"v_cvt_pk_bf16_f32 v200, v200, v201\n\t"
"v_cvt_pk_bf16_f32 v201, v202, v203\n\t"
"v_mfma_f32_16x16x32_bf16 v[204:207], a[204:207], a[224:227], 0\n\t"
"ds_read_b64_tr_b16 a[190:191], v252 offset:2816\n\t"
"v_mfma_f32_16x16x32_bf16 v[88:91], a[64:67], a[152:155], v[88:91]\n\t"
"ds_read_b64_tr_b16 a[192:193], v251 offset:0\n\t"
"v_mfma_f32_16x16x32_bf16 v[88:91], a[68:71], a[168:171], v[88:91]\n\t"
"ds_read_b64_tr_b16 a[194:195], v251 offset:512\n\t"
"v_mfma_f32_16x16x32_bf16 v[92:95], a[64:67], a[156:159], v[92:95]\n\t"
"ds_read_b64_tr_b16 a[196:197], v252 offset:0\n\t"
"v_mfma_f32_16x16x32_bf16 v[92:95], a[68:71], a[172:175], v[92:95]\n\t"
"buffer_load_dwordx4 a[64:67], v248, s[8:11], s52 offen offset:2048 sc1\n\t"
"v_mfma_f32_16x16x32_bf16 v[76:79], a[56:59], a[156:159], v[76:79]\n\t"
"ds_read_b64_tr_b16 a[198:199], v252 offset:512\n\t"
"v_cvt_pk_bf16_f32 v202, v204, v205\n\t"
"v_mfma_f32_16x16x32_bf16 v[76:79], a[60:63], a[172:175], v[76:79]\n\t"
"buffer_load_dwordx4 a[68:71], v248, s[8:11], s52 offen offset:3072 sc1\n\t"
"v_mfma_f32_16x16x32_bf16 v[60:63], a[48:51], a[156:159], v[60:63]\n\t"
"ds_read_b64_tr_b16 a[200:201], v249 offset:2304\n\t"
"v_cvt_pk_bf16_f32 v203, v206, v207\n\t"
"v_mfma_f32_16x16x32_bf16 v[60:63], a[52:55], a[172:175], v[60:63]\n\t"
"buffer_load_dwordx4 a[60:63], v248, s[8:11], s52 offen offset:1024 sc1\n\t"
"s_waitcnt vmcnt(36) lgkmcnt(6)\n\t"
"v_mfma_f32_16x16x32_bf16 v[144:147], a[72:75], a[144:147], v[144:147]\n\t"
"ds_read_b64_tr_b16 a[202:203], v249 offset:2816\n\t"
"v_mfma_f32_16x16x32_bf16 v[144:147], a[76:79], a[160:163], v[144:147]\n\t"
"ds_write_b128 v221, v[200:203] offset:28672\n\t"
"ds_read_b64_tr_b16 a[204:205], v250 offset:2304\n\t"
"ds_read_b64_tr_b16 a[206:207], v250 offset:2816\n\t"
"buffer_load_dwordx4 a[56:59], v248, s[8:11], s52 offen offset:0 sc1\n\t"
"v_mfma_f32_16x16x32_bf16 v[160:163], a[80:83], a[144:147], v[160:163]\n\t"
"v_mfma_f32_16x16x32_bf16 v[160:163], a[84:87], a[160:163], v[160:163]\n\t"
"buffer_load_dwordx4 a[48:51], v235, s[8:11], s51 offen offset:2048 sc1\n\t"
"buffer_load_dwordx4 a[52:55], v235, s[8:11], s51 offen offset:3072 sc1\n\t"
"v_mfma_f32_16x16x32_bf16 v[176:179], a[88:91], a[144:147], v[176:179]\n\t"
"v_mfma_f32_16x16x32_bf16 v[176:179], a[92:95], a[160:163], v[176:179]\n\t"
"ds_read_b128 a[152:155], v216 offset:6144\n\t"
"v_mfma_f32_16x16x32_bf16 v[180:183], a[88:91], a[148:151], v[180:183]\n\t"
"ds_read_b128 a[168:171], v216 offset:14336\n\t"
"v_mfma_f32_16x16x32_bf16 v[180:183], a[92:95], a[164:167], v[180:183]\n\t"
"ds_read_b128 a[156:159], v216 offset:7168\n\t"
"ds_read_b128 a[172:175], v216 offset:15360\n\t"
"v_mfma_f32_16x16x32_bf16 v[164:167], a[80:83], a[148:151], v[164:167]\n\t"
"v_mfma_f32_16x16x32_bf16 v[164:167], a[84:87], a[164:167], v[164:167]\n\t"
"v_mfma_f32_16x16x32_bf16 v[148:151], a[72:75], a[148:151], v[148:151]\n\t"
"v_mfma_f32_16x16x32_bf16 v[148:151], a[76:79], a[164:167], v[148:151]\n\t"
"s_waitcnt lgkmcnt(0)\n\t"
"s_barrier\n\t"
"s_branch Lunit_%=\n\t"
"Lfb_%=:\n\t"
"v_mbcnt_lo_u32_b32 v162, -1, 0\n\t"
"v_mbcnt_hi_u32_b32 v162, -1, v162\n\t"
"v_and_b32 v161, 3, v162\n\t"
"v_lshlrev_b32 v163, 2, v162\n\t"
"v_and_or_b32 v161, v163, 16, v161\n\t"
"v_mul_u32_u24 v161, 0xc0, v161\n\t"
"v_lshrrev_b32 v163, 5, v162\n\t"
"v_mul_u32_u24 v163, 12, v163\n\t"
"v_bfe_u32 v164, v162, 3, 2\n\t"
"v_add3_u32 v161, v161, v163, v164\n\t"
"v_lshlrev_b32 v161, 1, v161\n\t"
"v_lshlrev_b32 v162, 1, v162\n\t"
"v_mov_b32 v152, 0\n\t"
"v_mov_b32 v153, 0\n\t"
"v_mov_b32 v154, 0\n\t"
"v_mov_b32 v155, 0\n\t"
"v_mov_b32 v156, 0\n\t"
"v_mov_b32 v157, 0\n\t"
"v_mov_b32 v158, 0\n\t"
"v_mov_b32 v159, 0\n\t"
"v_mov_b32 v160, 0\n\t"
"s_mov_b32 s67, 0\n\t"
"Lfb0_%=:\n\t"
"s_mov_b32 s69, 0\n\t"
"Lfb1_%=:\n\t"
"s_lshr_b32 s60, s69, 5\n\t"
"s_mul_i32 s56, s60, 12288\n\t"
"s_bfe_u32 s61, s69, 0x20001\n\t"
"s_mul_i32 s61, s61, 1536\n\t"
"s_add_u32 s56, s56, s61\n\t"
"s_bitcmp1_b32 s69, 0\n\t"
"s_cselect_b32 s61, 48, 0\n\t"
"s_add_u32 s56, s56, s61\n\t"
"s_bfe_u32 s62, s69, 0x20003\n\t"
"s_mul_i32 s61, s62, 96\n\t"
"s_add_u32 s56, s56, s61\n\t"
"s_lshl3_add_u32 s56, s67, s56\n\t"
"s_lshr_b32 s60, s69, 5\n\t"
"s_bfe_u32 s62, s69, 0x20003\n\t"
"s_and_b32 s61, s60, 1\n\t"
"s_lshl1_add_u32 s61, s67, s61\n\t"
"s_mul_i32 s62, s62, 288\n\t"
"s_add_u32 s61, s61, s62\n\t"
"s_lshr_b32 s60, s60, 1\n\t"
"s_mul_i32 s62, s60, 96\n\t"
"s_add_u32 s61, s61, s62\n\t"
"s_lshl_b32 s57, s61, 10\n\t"
"s_and_b32 s61, s69, 7\n\t"
"s_lshl_b32 s61, s61, 7\n\t"
"s_add_u32 s57, s57, s61\n\t"
"s_cmp_lt_u32 s60, 1\n\t"
"s_cselect_b32 s58, 0x3e000000, 0.5\n\t"
"s_cmp_lt_u32 s60, 2\n\t"
"s_cselect_b32 s59, 0x3e800000, 1.0\n\t"
"s_add_u32 s60, s56, 0\n\t"
"buffer_load_short_d16_hi v152, v161, s[52:55], s60 offen\n\t"
"s_add_u32 s60, s56, 73728\n\t"
"buffer_load_short_d16_hi v153, v161, s[52:55], s60 offen\n\t"
"s_add_u32 s60, s56, 147456\n\t"
"buffer_load_short_d16_hi v154, v161, s[52:55], s60 offen\n\t"
"s_add_u32 s60, s56, 221184\n\t"
"buffer_load_short_d16_hi v155, v161, s[52:55], s60 offen\n\t"
"s_add_u32 s60, s56, 294912\n\t"
"buffer_load_short_d16_hi v156, v161, s[52:55], s60 offen\n\t"
"s_add_u32 s60, s56, 368640\n\t"
"buffer_load_short_d16_hi v157, v161, s[52:55], s60 offen\n\t"
"s_add_u32 s60, s56, 442368\n\t"
"buffer_load_short_d16_hi v158, v161, s[52:55], s60 offen\n\t"
"s_add_u32 s60, s56, 516096\n\t"
"buffer_load_short_d16_hi v159, v161, s[52:55], s60 offen\n\t"
"s_add_u32 s60, s56, 589824\n\t"
"buffer_load_short_d16_hi v160, v161, s[52:55], s60 offen\n\t"
"s_waitcnt vmcnt(2)\n\t"
"v_add_f32 v168, v152, v158\n\t"
"s_waitcnt vmcnt(1)\n\t"
"v_add_f32 v169, v153, v159\n\t"
"s_waitcnt vmcnt(0)\n\t"
"v_add_f32 v170, v154, v160\n\t"
"v_add_f32 v171, v168, v155\n\t"
"v_sub_f32 v174, v168, v155\n\t"
"v_add_f32 v172, v169, v156\n\t"
"v_sub_f32 v175, v169, v156\n\t"
"v_add_f32 v173, v170, v157\n\t"
"v_sub_f32 v176, v170, v157\n\t"
"v_add_f32 v177, v152, v154\n\t"
"v_add_f32 v178, v171, v173\n\t"
"v_add_f32 v179, v174, v176\n\t"
"v_add_f32 v180, v158, v160\n\t"
"v_add_f32 v182, v177, v153\n\t"
"v_sub_f32 v183, v177, v153\n\t"
"v_add_f32 v184, v178, v172\n\t"
"v_sub_f32 v185, v178, v172\n\t"
"v_add_f32 v186, v179, v175\n\t"
"v_sub_f32 v187, v179, v175\n\t"
"v_add_f32 v188, v180, v159\n\t"
"v_sub_f32 v189, v180, v159\n\t"
"v_mul_f32 v190, s59, v152\n\t"
"v_mul_f32 v191, 0x3e800000, v182\n\t"
"v_mul_f32 v192, 0x3e800000, v183\n\t"
"v_mul_f32 v193, -s59, v154\n\t"
"v_mul_f32 v194, s58, v171\n\t"
"v_mul_f32 v195, 0x3e800000, v184\n\t"
"v_mul_f32 v196, 0x3e800000, v185\n\t"
"v_mul_f32 v197, -s58, v173\n\t"
"v_mul_f32 v198, s58, v174\n\t"
"v_mul_f32 v199, 0x3e800000, v186\n\t"
"v_mul_f32 v200, 0x3e800000, v187\n\t"
"v_mul_f32 v201, -s58, v176\n\t"
"v_mul_f32 v202, -s59, v158\n\t"
"v_mul_f32 v203, 0xbe800000, v188\n\t"
"v_mul_f32 v204, 0xbe800000, v189\n\t"
"v_mul_f32 v205, s59, v160\n\t"
"v_cvt_pk_bf16_f32 v206, v190, v191\n\t"
"v_cvt_pk_bf16_f32 v207, v192, v193\n\t"
"v_cvt_pk_bf16_f32 v208, v194, v195\n\t"
"v_cvt_pk_bf16_f32 v209, v196, v197\n\t"
"v_cvt_pk_bf16_f32 v210, v198, v199\n\t"
"v_cvt_pk_bf16_f32 v211, v200, v201\n\t"
"v_cvt_pk_bf16_f32 v212, v202, v203\n\t"
"v_cvt_pk_bf16_f32 v213, v204, v205\n\t"
"s_add_u32 s60, s57, 0\n\t"
"buffer_store_short v206, v162, s[8:11], s60 offen sc1\n\t"
"s_add_u32 s60, s57, 6144\n\t"
"buffer_store_short_d16_hi v206, v162, s[8:11], s60 offen sc1\n\t"
"s_add_u32 s60, s57, 12288\n\t"
"buffer_store_short v207, v162, s[8:11], s60 offen sc1\n\t"
"s_add_u32 s60, s57, 18432\n\t"
"buffer_store_short_d16_hi v207, v162, s[8:11], s60 offen sc1\n\t"
"s_add_u32 s60, s57, 24576\n\t"
"buffer_store_short v208, v162, s[8:11], s60 offen sc1\n\t"
"s_add_u32 s60, s57, 30720\n\t"
"buffer_store_short_d16_hi v208, v162, s[8:11], s60 offen sc1\n\t"
"s_add_u32 s60, s57, 36864\n\t"
"buffer_store_short v209, v162, s[8:11], s60 offen sc1\n\t"
"s_add_u32 s60, s57, 43008\n\t"
"buffer_store_short_d16_hi v209, v162, s[8:11], s60 offen sc1\n\t"
"s_add_u32 s60, s57, 49152\n\t"
"buffer_store_short v210, v162, s[8:11], s60 offen sc1\n\t"
"s_add_u32 s60, s57, 55296\n\t"
"buffer_store_short_d16_hi v210, v162, s[8:11], s60 offen sc1\n\t"
"s_add_u32 s60, s57, 61440\n\t"
"buffer_store_short v211, v162, s[8:11], s60 offen sc1\n\t"
"s_add_u32 s60, s57, 67584\n\t"
"buffer_store_short_d16_hi v211, v162, s[8:11], s60 offen sc1\n\t"
"s_add_u32 s60, s57, 73728\n\t"
"buffer_store_short v212, v162, s[8:11], s60 offen sc1\n\t"
"s_add_u32 s60, s57, 79872\n\t"
"buffer_store_short_d16_hi v212, v162, s[8:11], s60 offen sc1\n\t"
"s_add_u32 s60, s57, 86016\n\t"
"buffer_store_short v213, v162, s[8:11], s60 offen sc1\n\t"
"s_add_u32 s60, s57, 92160\n\t"
"buffer_store_short_d16_hi v213, v162, s[8:11], s60 offen sc1\n\t"
"s_waitcnt vmcnt(0)\n\t"
"s_add_u32 s69, s69, 1\n\t"
"s_cmp_lt_u32 s69, 192\n\t"
"s_cbranch_scc1 Lfb1_%=\n\t"
"s_add_u32 s67, s67, 1\n\t"
"s_cmp_lt_u32 s67, 3\n\t"
"s_cbranch_scc1 Lfb0_%=\n\t"
"s_branch Lgo_%=\n\t"
"Ltail_%=:\n\t"
"v_mfma_f32_16x16x32_bf16 v[104:107], a[24:27], a[152:155], v[104:107]\n\t"
"ds_read_b128 a[144:147], v216 offset:0\n\t"
"v_cvt_pk_bf16_f32 v8, v8, v9\n\t"
"v_mfma_f32_16x16x32_bf16 v[104:107], a[28:31], a[168:171], v[104:107]\n\t"
"ds_read_b128 a[160:163], v216 offset:8192\n\t"
"ds_read_b128 a[148:151], v216 offset:1024\n\t"
"v_mfma_f32_16x16x32_bf16 v[120:123], a[32:35], a[152:155], v[120:123]\n\t"
"ds_read_b128 a[164:167], v216 offset:9216\n\t"
"v_cvt_pk_bf16_f32 v9, v10, v11\n\t"
"v_mfma_f32_16x16x32_bf16 v[120:123], a[36:39], a[168:171], v[120:123]\n\t"
"buffer_load_dwordx4 a[100:103], v248, s[8:11], s74 offen offset:3072 sc1\n\t"
"v_mfma_f32_16x16x32_bf16 v[136:139], a[40:43], a[152:155], v[136:139]\n\t"
"v_cvt_pk_bf16_f32 v12, v12, v13\n\t"
"v_cvt_pk_bf16_f32 v13, v14, v15\n\t"
"v_mfma_f32_16x16x32_bf16 v[136:139], a[44:47], a[168:171], v[136:139]\n\t"
"buffer_load_dwordx4 a[136:139], v235, s[8:11], s83 offen offset:2048 sc1\n\t"
"v_mfma_f32_16x16x32_bf16 v[192:195], a[176:179], a[224:227], 0\n\t"
"buffer_load_dwordx4 a[140:143], v235, s[8:11], s83 offen offset:3072 sc1\n\t"
"v_mfma_f32_16x16x32_bf16 v[140:143], a[40:43], a[156:159], v[140:143]\n\t"
"v_cvt_pk_bf16_f32 v24, v24, v25\n\t"
"v_cvt_pk_bf16_f32 v25, v26, v27\n\t"
"v_mfma_f32_16x16x32_bf16 v[140:143], a[44:47], a[172:175], v[140:143]\n\t"
"ds_write2st64_b64 v228, v[8:9], v[12:13] offset0:26 offset1:39\n\t"
"v_mfma_f32_16x16x32_bf16 v[196:199], a[180:183], a[224:227], 0\n\t"
"buffer_load_dwordx4 a[132:135], v235, s[8:11], s83 offen offset:1024 sc1\n\t"
"v_mfma_f32_16x16x32_bf16 v[124:127], a[32:35], a[156:159], v[124:127]\n\t"
"ds_read_b128 a[228:231], v229 offset:0\n\t"
"v_cvt_pk_bf16_f32 v28, v28, v29\n\t"
"v_mfma_f32_16x16x32_bf16 v[124:127], a[36:39], a[172:175], v[124:127]\n\t"
"v_cvt_pk_bf16_f32 v29, v30, v31\n\t"
"ds_read_b128 a[232:235], v230 offset:0\n\t"
"v_mfma_f32_16x16x32_bf16 v[108:111], a[24:27], a[156:159], v[108:111]\n\t"
"v_cvt_pk_bf16_f32 v40, v40, v41\n\t"
"v_cvt_pk_bf16_f32 v41, v42, v43\n\t"
"v_mfma_f32_16x16x32_bf16 v[108:111], a[28:31], a[172:175], v[108:111]\n\t"
"ds_write2st64_b64 v246, v[24:25], v[28:29] offset0:26 offset1:39\n\t"
"v_mfma_f32_16x16x32_bf16 v[200:203], a[184:187], a[224:227], 0\n\t"
"ds_read_b128 a[152:155], v216 offset:2048\n\t"
"v_cvt_pk_bf16_f32 v192, v192, v193\n\t"
"s_waitcnt vmcnt(15) lgkmcnt(5)\n\t"
"v_mfma_f32_16x16x32_bf16 v[48:51], a[48:51], a[144:147], v[48:51]\n\t"
"ds_read_b128 a[168:171], v216 offset:10240\n\t"
"v_cvt_pk_bf16_f32 v193, v194, v195\n\t"
"v_mfma_f32_16x16x32_bf16 v[48:51], a[52:55], a[160:163], v[48:51]\n\t"
"buffer_load_dwordx4 a[128:131], v235, s[8:11], s83 offen offset:0 sc1\n\t"
"ds_read_b128 a[236:239], v231 offset:0\n\t"
"v_cvt_pk_bf16_f32 v44, v44, v45\n\t"
"v_cvt_pk_bf16_f32 v45, v46, v47\n\t"
"v_mfma_f32_16x16x32_bf16 v[204:207], a[188:191], a[224:227], 0\n\t"
"buffer_load_dwordx4 a[120:123], v248, s[8:11], s83 offen offset:2048 sc1\n\t"
"v_mfma_f32_16x16x32_bf16 v[64:67], a[56:59], a[144:147], v[64:67]\n\t"
"v_cvt_pk_bf16_f32 v96, v96, v97\n\t"
"v_cvt_pk_bf16_f32 v97, v98, v99\n\t"
"v_mfma_f32_16x16x32_bf16 v[64:67], a[60:63], a[160:163], v[64:67]\n\t"
"ds_write2st64_b64 v247, v[40:41], v[44:45] offset0:26 offset1:39\n\t"
"v_cvt_pk_bf16_f32 v194, v196, v197\n\t"
"v_cvt_pk_bf16_f32 v195, v198, v199\n\t"
"v_mfma_f32_16x16x32_bf16 v[80:83], a[64:67], a[144:147], v[80:83]\n\t"
"v_cvt_pk_bf16_f32 v100, v100, v101\n\t"
"v_cvt_pk_bf16_f32 v101, v102, v103\n\t"
"v_mfma_f32_16x16x32_bf16 v[80:83], a[68:71], a[160:163], v[80:83]\n\t"
"ds_read_b128 a[156:159], v216 offset:3072\n\t"
"v_cvt_pk_bf16_f32 v112, v112, v113\n\t"
"v_mfma_f32_16x16x32_bf16 v[208:211], a[192:195], a[224:227], 0\n\t"
"ds_read_b128 a[172:175], v216 offset:11264\n\t"
"buffer_load_dwordx4 a[124:127], v248, s[8:11], s83 offen offset:3072 sc1\n\t"
"ds_write2st64_b64 v228, v[96:97], v[100:101] offset0:52 offset1:65\n\t"
"v_mfma_f32_16x16x32_bf16 v[84:87], a[64:67], a[148:151], v[84:87]\n\t"
"v_cvt_pk_bf16_f32 v113, v114, v115\n\t"
"v_mfma_f32_16x16x32_bf16 v[84:87], a[68:71], a[164:167], v[84:87]\n\t"
"ds_write_b128 v221, v[192:195] offset:16384\n\t"
"v_cvt_pk_bf16_f32 v200, v200, v201\n\t"
"v_cvt_pk_bf16_f32 v201, v202, v203\n\t"
"v_mfma_f32_16x16x32_bf16 v[68:71], a[56:59], a[148:151], v[68:71]\n\t"
"v_cvt_pk_bf16_f32 v116, v116, v117\n\t"
"v_cvt_pk_bf16_f32 v117, v118, v119\n\t"
"v_mfma_f32_16x16x32_bf16 v[68:71], a[60:63], a[164:167], v[68:71]\n\t"
"v_mfma_f32_16x16x32_bf16 v[212:215], a[196:199], a[224:227], 0\n\t"
"v_cvt_pk_bf16_f32 v202, v204, v205\n\t"
"v_cvt_pk_bf16_f32 v203, v206, v207\n\t"
"ds_write2st64_b64 v246, v[112:113], v[116:117] offset0:52 offset1:65\n\t"
"v_cvt_pk_bf16_f32 v128, v128, v129\n\t"
"v_cvt_pk_bf16_f32 v129, v130, v131\n\t"
"v_mfma_f32_16x16x32_bf16 v[52:55], a[48:51], a[148:151], v[52:55]\n\t"
"ds_read_b128 a[144:147], v216 offset:4096\n\t"
"v_mfma_f32_16x16x32_bf16 v[52:55], a[52:55], a[164:167], v[52:55]\n\t"
"ds_read_b128 a[160:163], v216 offset:12288\n\t"
"s_add_u32 s28, s24, 0x0\n\t"
"s_waitcnt lgkmcnt(7)\n\t"
"buffer_store_dwordx4 a[228:231], v232, s[16:19], s28 offen offset:0 nt\n\t"
"v_cvt_pk_bf16_f32 v132, v132, v133\n\t"
"v_cvt_pk_bf16_f32 v133, v134, v135\n\t"
"v_mfma_f32_16x16x32_bf16 v[192:195], a[200:203], a[224:227], 0\n\t"
"v_mfma_f32_16x16x32_bf16 v[56:59], a[48:51], a[152:155], v[56:59]\n\t"
"v_cvt_pk_bf16_f32 v104, v104, v105\n\t"
"v_cvt_pk_bf16_f32 v105, v106, v107\n\t"
"v_mfma_f32_16x16x32_bf16 v[56:59], a[52:55], a[168:171], v[56:59]\n\t"
"ds_write_b128 v221, v[200:203] offset:24576\n\t"
"ds_write2st64_b64 v247, v[128:129], v[132:133] offset0:52 offset1:65\n\t"
"v_cvt_pk_bf16_f32 v208, v208, v209\n\t"
"v_cvt_pk_bf16_f32 v209, v210, v211\n\t"
"v_mfma_f32_16x16x32_bf16 v[72:75], a[56:59], a[152:155], v[72:75]\n\t"
"v_cvt_pk_bf16_f32 v120, v120, v121\n\t"
"v_cvt_pk_bf16_f32 v121, v122, v123\n\t"
"v_mfma_f32_16x16x32_bf16 v[72:75], a[60:63], a[168:171], v[72:75]\n\t"
"ds_read_b128 a[148:151], v216 offset:5120\n\t"
"ds_read_b128 a[164:167], v216 offset:13312\n\t"
"v_mfma_f32_16x16x32_bf16 v[196:199], a[204:207], a[224:227], 0\n\t"
"v_cvt_pk_bf16_f32 v136, v136, v137\n\t"
"v_cvt_pk_bf16_f32 v137, v138, v139\n\t"
"v_mfma_f32_16x16x32_bf16 v[88:91], a[64:67], a[152:155], v[88:91]\n\t"
"v_mfma_f32_16x16x32_bf16 v[88:91], a[68:71], a[168:171], v[88:91]\n\t"
"v_cvt_pk_bf16_f32 v210, v212, v213\n\t"
"v_cvt_pk_bf16_f32 v211, v214, v215\n\t"
"v_cvt_pk_bf16_f32 v108, v108, v109\n\t"
"v_cvt_pk_bf16_f32 v109, v110, v111\n\t"
"s_waitcnt lgkmcnt(6)\n\t"
"v_mfma_f32_16x16x32_bf16 v[92:95], a[64:67], a[156:159], v[92:95]\n\t"
"v_mfma_f32_16x16x32_bf16 v[92:95], a[68:71], a[172:175], v[92:95]\n\t"
"ds_write2st64_b64 v228, v[104:105], v[108:109] offset0:78 offset1:91\n\t"
"v_cvt_pk_bf16_f32 v124, v124, v125\n\t"
"v_cvt_pk_bf16_f32 v125, v126, v127\n\t"
"v_mfma_f32_16x16x32_bf16 v[76:79], a[56:59], a[156:159], v[76:79]\n\t"
"v_cvt_pk_bf16_f32 v192, v192, v193\n\t"
"v_cvt_pk_bf16_f32 v193, v194, v195\n\t"
"v_mfma_f32_16x16x32_bf16 v[76:79], a[60:63], a[172:175], v[76:79]\n\t"
"ds_write_b128 v221, v[208:211] offset:20480\n\t"
"ds_write2st64_b64 v246, v[120:121], v[124:125] offset0:78 offset1:91\n\t"
"v_mfma_f32_16x16x32_bf16 v[60:63], a[48:51], a[156:159], v[60:63]\n\t"
"v_cvt_pk_bf16_f32 v140, v140, v141\n\t"
"v_cvt_pk_bf16_f32 v141, v142, v143\n\t"
"v_mfma_f32_16x16x32_bf16 v[60:63], a[52:55], a[172:175], v[60:63]\n\t"
"s_waitcnt vmcnt(13) lgkmcnt(5)\n\t"
"v_mfma_f32_16x16x32_bf16 v[144:147], a[72:75], a[144:147], v[144:147]\n\t"
"v_mfma_f32_16x16x32_bf16 v[144:147], a[76:79], a[160:163], v[144:147]\n\t"
"buffer_store_dwordx4 a[232:235], v233, s[16:19], s28 offen offset:0 nt\n\t"
"ds_write2st64_b64 v247, v[136:137], v[140:141] offset0:78 offset1:91\n\t"
"v_cvt_pk_bf16_f32 v194, v196, v197\n\t"
"v_cvt_pk_bf16_f32 v195, v198, v199\n\t"
"v_mfma_f32_16x16x32_bf16 v[160:163], a[80:83], a[144:147], v[160:163]\n\t"
"v_mfma_f32_16x16x32_bf16 v[160:163], a[84:87], a[160:163], v[160:163]\n\t"
"ds_write_b128 v221, v[192:195] offset:28672\n\t"
"v_mfma_f32_16x16x32_bf16 v[176:179], a[88:91], a[144:147], v[176:179]\n\t"
"v_mfma_f32_16x16x32_bf16 v[176:179], a[92:95], a[160:163], v[176:179]\n\t"
"ds_read_b128 a[152:155], v216 offset:6144\n\t"
"s_waitcnt lgkmcnt(5)\n\t"
"v_mfma_f32_16x16x32_bf16 v[180:183], a[88:91], a[148:151], v[180:183]\n\t"
"ds_read_b128 a[168:171], v216 offset:14336\n\t"
"v_mfma_f32_16x16x32_bf16 v[180:183], a[92:95], a[164:167], v[180:183]\n\t"
"ds_read_b128 a[156:159], v216 offset:7168\n\t"
"ds_read_b128 a[172:175], v216 offset:15360\n\t"
"v_mfma_f32_16x16x32_bf16 v[164:167], a[80:83], a[148:151], v[164:167]\n\t"
"v_mfma_f32_16x16x32_bf16 v[164:167], a[84:87], a[164:167], v[164:167]\n\t"
"buffer_store_dwordx4 a[236:239], v234, s[16:19], s28 offen offset:0 nt\n\t"
"v_mfma_f32_16x16x32_bf16 v[148:151], a[72:75], a[148:151], v[148:151]\n\t"
"v_mfma_f32_16x16x32_bf16 v[148:151], a[76:79], a[164:167], v[148:151]\n\t"
"s_waitcnt lgkmcnt(0)\n\t"
"s_barrier\n\t"
"v_mfma_f32_16x16x32_bf16 v[152:155], a[72:75], a[152:155], v[152:155]\n\t"
"ds_read_b128 a[144:147], v216 offset:16384\n\t"
"v_mfma_f32_16x16x32_bf16 v[152:155], a[76:79], a[168:171], v[152:155]\n\t"
"ds_read_b128 a[160:163], v216 offset:24576\n\t"
"ds_read_b128 a[148:151], v216 offset:17408\n\t"
"v_mfma_f32_16x16x32_bf16 v[168:171], a[80:83], a[152:155], v[168:171]\n\t"
"ds_read_b128 a[164:167], v216 offset:25600\n\t"
"v_mfma_f32_16x16x32_bf16 v[168:171], a[84:87], a[168:171], v[168:171]\n\t"
"ds_read_b128 a[0:3], v229 offset:13312\n\t"
"v_mfma_f32_16x16x32_bf16 v[184:187], a[88:91], a[152:155], v[184:187]\n\t"
"ds_read_b128 a[152:155], v216 offset:18432\n\t"
"v_mfma_f32_16x16x32_bf16 v[184:187], a[92:95], a[168:171], v[184:187]\n\t"
"ds_read_b128 a[168:171], v216 offset:26624\n\t"
"ds_read_b128 a[4:7], v230 offset:13312\n\t"
"v_mfma_f32_16x16x32_bf16 v[188:191], a[88:91], a[156:159], v[188:191]\n\t"
"ds_read_b128 a[8:11], v231 offset:13312\n\t"
"v_mfma_f32_16x16x32_bf16 v[188:191], a[92:95], a[172:175], v[188:191]\n\t"
"ds_read_b128 a[12:15], v229 offset:26624\n\t"
"ds_read_b128 a[16:19], v230 offset:26624\n\t"
"v_mfma_f32_16x16x32_bf16 v[172:175], a[80:83], a[156:159], v[172:175]\n\t"
"ds_read_b128 a[20:23], v231 offset:26624\n\t"
"v_mfma_f32_16x16x32_bf16 v[172:175], a[84:87], a[172:175], v[172:175]\n\t"
"s_add_u32 s27, s24, 0x180000\n\t"
"s_waitcnt lgkmcnt(7)\n\t"
"buffer_store_dwordx4 a[0:3], v232, s[16:19], s27 offen offset:0 nt\n\t"
"ds_read_b128 a[24:27], v229 offset:39936\n\t"
"ds_read_b128 a[28:31], v230 offset:39936\n\t"
"v_mfma_f32_16x16x32_bf16 v[156:159], a[72:75], a[156:159], v[156:159]\n\t"
"ds_read_b128 a[156:159], v216 offset:19456\n\t"
"v_mfma_f32_16x16x32_bf16 v[156:159], a[76:79], a[172:175], v[156:159]\n\t"
"v_pk_add_f32 v[48:49], v[48:49], v[144:145]\n\t"
"v_pk_add_f32 v[50:51], v[50:51], v[146:147]\n\t"
"v_pk_fma_f32 v[144:145], v[144:145], s[38:39], v[48:49]\n\t"
"v_pk_fma_f32 v[146:147], v[146:147], s[38:39], v[50:51]\n\t"
"v_pk_add_f32 v[64:65], v[64:65], v[160:161]\n\t"
"v_pk_add_f32 v[66:67], v[66:67], v[162:163]\n\t"
"v_pk_fma_f32 v[160:161], v[160:161], s[38:39], v[64:65]\n\t"
"v_pk_fma_f32 v[162:163], v[162:163], s[38:39], v[66:67]\n\t"
"v_pk_add_f32 v[80:81], v[80:81], v[176:177]\n\t"
"v_pk_add_f32 v[82:83], v[82:83], v[178:179]\n\t"
"v_pk_fma_f32 v[176:177], v[176:177], s[38:39], v[80:81]\n\t"
"v_pk_fma_f32 v[178:179], v[178:179], s[38:39], v[82:83]\n\t"
"v_pk_add_f32 v[84:85], v[84:85], v[180:181]\n\t"
"v_pk_add_f32 v[86:87], v[86:87], v[182:183]\n\t"
"v_pk_fma_f32 v[180:181], v[180:181], s[38:39], v[84:85]\n\t"
"v_pk_fma_f32 v[182:183], v[182:183], s[38:39], v[86:87]\n\t"
"v_pk_add_f32 v[68:69], v[68:69], v[164:165]\n\t"
"v_pk_add_f32 v[70:71], v[70:71], v[166:167]\n\t"
"v_pk_fma_f32 v[164:165], v[164:165], s[38:39], v[68:69]\n\t"
"v_pk_fma_f32 v[166:167], v[166:167], s[38:39], v[70:71]\n\t"
"v_pk_add_f32 v[52:53], v[52:53], v[148:149]\n\t"
"v_pk_add_f32 v[54:55], v[54:55], v[150:151]\n\t"
"v_pk_fma_f32 v[148:149], v[148:149], s[38:39], v[52:53]\n\t"
"v_pk_fma_f32 v[150:151], v[150:151], s[38:39], v[54:55]\n\t"
"v_pk_add_f32 v[56:57], v[56:57], v[152:153]\n\t"
"v_pk_add_f32 v[58:59], v[58:59], v[154:155]\n\t"
"v_pk_fma_f32 v[152:153], v[152:153], s[38:39], v[56:57]\n\t"
"v_pk_fma_f32 v[154:155], v[154:155], s[38:39], v[58:59]\n\t"
"v_pk_add_f32 v[72:73], v[72:73], v[168:169]\n\t"
"v_pk_add_f32 v[74:75], v[74:75], v[170:171]\n\t"
"v_pk_fma_f32 v[168:169], v[168:169], s[38:39], v[72:73]\n\t"
"v_pk_fma_f32 v[170:171], v[170:171], s[38:39], v[74:75]\n\t"
"v_pk_add_f32 v[88:89], v[88:89], v[184:185]\n\t"
"v_pk_add_f32 v[90:91], v[90:91], v[186:187]\n\t"
"v_pk_fma_f32 v[184:185], v[184:185], s[38:39], v[88:89]\n\t"
"v_pk_fma_f32 v[186:187], v[186:187], s[38:39], v[90:91]\n\t"
"v_pk_add_f32 v[92:93], v[92:93], v[188:189]\n\t"
"v_pk_add_f32 v[94:95], v[94:95], v[190:191]\n\t"
"v_pk_fma_f32 v[188:189], v[188:189], s[38:39], v[92:93]\n\t"
"v_pk_fma_f32 v[190:191], v[190:191], s[38:39], v[94:95]\n\t"
"v_pk_add_f32 v[76:77], v[76:77], v[172:173]\n\t"
"v_pk_add_f32 v[78:79], v[78:79], v[174:175]\n\t"
"v_pk_fma_f32 v[172:173], v[172:173], s[38:39], v[76:77]\n\t"
"v_pk_fma_f32 v[174:175], v[174:175], s[38:39], v[78:79]\n\t"
"v_pk_add_f32 v[60:61], v[60:61], v[156:157]\n\t"
"v_pk_add_f32 v[62:63], v[62:63], v[158:159]\n\t"
"v_pk_fma_f32 v[156:157], v[156:157], s[38:39], v[60:61]\n\t"
"v_pk_fma_f32 v[158:159], v[158:159], s[38:39], v[62:63]\n\t"
"ds_read_b128 a[172:175], v216 offset:27648\n\t"
"s_waitcnt lgkmcnt(8)\n\t"
"buffer_store_dwordx4 a[4:7], v233, s[16:19], s27 offen offset:0 nt\n\t"
"ds_read_b128 a[32:35], v231 offset:39936\n\t"
"s_waitcnt vmcnt(11)\n\t"
"v_mfma_f32_16x16x32_bf16 v[48:51], a[96:99], a[144:147], v[48:51]\n\t"
"v_mfma_f32_16x16x32_bf16 v[48:51], a[100:103], a[160:163], v[48:51]\n\t"
"s_waitcnt lgkmcnt(8)\n\t"
"buffer_store_dwordx4 a[8:11], v234, s[16:19], s27 offen offset:0 nt\n\t"
"v_mfma_f32_16x16x32_bf16 v[64:67], a[104:107], a[144:147], v[64:67]\n\t"
"v_mfma_f32_16x16x32_bf16 v[64:67], a[108:111], a[160:163], v[64:67]\n\t"
"v_mfma_f32_16x16x32_bf16 v[80:83], a[112:115], a[144:147], v[80:83]\n\t"
"ds_read_b128 a[144:147], v216 offset:20480\n\t"
"v_mfma_f32_16x16x32_bf16 v[80:83], a[116:119], a[160:163], v[80:83]\n\t"
"ds_read_b128 a[160:163], v216 offset:28672\n\t"
"v_mfma_f32_16x16x32_bf16 v[84:87], a[112:115], a[148:151], v[84:87]\n\t"
"v_mfma_f32_16x16x32_bf16 v[84:87], a[116:119], a[164:167], v[84:87]\n\t"
"v_mfma_f32_16x16x32_bf16 v[68:71], a[104:107], a[148:151], v[68:71]\n\t"
"v_mfma_f32_16x16x32_bf16 v[68:71], a[108:111], a[164:167], v[68:71]\n\t"
"v_cvt_pk_bf16_f32 v48, v48, v49\n\t"
"v_cvt_pk_bf16_f32 v49, v50, v51\n\t"
"v_cvt_pk_bf16_f32 v64, v64, v65\n\t"
"v_cvt_pk_bf16_f32 v65, v66, v67\n\t"
"v_mfma_f32_16x16x32_bf16 v[52:55], a[96:99], a[148:151], v[52:55]\n\t"
"ds_read_b128 a[148:151], v216 offset:21504\n\t"
"v_cvt_pk_bf16_f32 v80, v80, v81\n\t"
"v_mfma_f32_16x16x32_bf16 v[52:55], a[100:103], a[164:167], v[52:55]\n\t"
"ds_read_b128 a[164:167], v216 offset:29696\n\t"
"s_add_u32 s20, s24, 0x30000\n\t"
"s_waitcnt lgkmcnt(4)\n\t"
"buffer_store_dwordx4 a[12:15], v232, s[16:19], s20 offen offset:0 nt\n\t"
"v_cvt_pk_bf16_f32 v81, v82, v83\n\t"
"v_mfma_f32_16x16x32_bf16 v[56:59], a[96:99], a[152:155], v[56:59]\n\t"
"v_mfma_f32_16x16x32_bf16 v[56:59], a[100:103], a[168:171], v[56:59]\n\t"
"buffer_store_dwordx4 a[16:19], v233, s[16:19], s20 offen offset:0 nt\n\t"
"buffer_store_dwordx4 a[20:23], v234, s[16:19], s20 offen offset:0 nt\n\t"
"v_mfma_f32_16x16x32_bf16 v[72:75], a[104:107], a[152:155], v[72:75]\n\t"
"v_mfma_f32_16x16x32_bf16 v[72:75], a[108:111], a[168:171], v[72:75]\n\t"
"v_cvt_pk_bf16_f32 v52, v52, v53\n\t"
"v_cvt_pk_bf16_f32 v53, v54, v55\n\t"
"v_cvt_pk_bf16_f32 v68, v68, v69\n\t"
"v_cvt_pk_bf16_f32 v69, v70, v71\n\t"
"v_mfma_f32_16x16x32_bf16 v[88:91], a[112:115], a[152:155], v[88:91]\n\t"
"ds_read_b128 a[152:155], v216 offset:22528\n\t"
"v_cvt_pk_bf16_f32 v84, v84, v85\n\t"
"v_mfma_f32_16x16x32_bf16 v[88:91], a[116:119], a[168:171], v[88:91]\n\t"
"ds_read_b128 a[168:171], v216 offset:30720\n\t"
"v_cvt_pk_bf16_f32 v85, v86, v87\n\t"
"ds_write2st64_b64 v228, v[48:49], v[52:53] offset0:0 offset1:13\n\t"
"v_mfma_f32_16x16x32_bf16 v[92:95], a[112:115], a[156:159], v[92:95]\n\t"
"v_mfma_f32_16x16x32_bf16 v[92:95], a[116:119], a[172:175], v[92:95]\n\t"
"ds_write2st64_b64 v246, v[64:65], v[68:69] offset0:0 offset1:13\n\t"
"ds_write2st64_b64 v247, v[80:81], v[84:85] offset0:0 offset1:13\n\t"
"v_mfma_f32_16x16x32_bf16 v[76:79], a[104:107], a[156:159], v[76:79]\n\t"
"v_mfma_f32_16x16x32_bf16 v[76:79], a[108:111], a[172:175], v[76:79]\n\t"
"v_cvt_pk_bf16_f32 v56, v56, v57\n\t"
"v_cvt_pk_bf16_f32 v57, v58, v59\n\t"
"v_cvt_pk_bf16_f32 v72, v72, v73\n\t"
"v_cvt_pk_bf16_f32 v73, v74, v75\n\t"
"v_mfma_f32_16x16x32_bf16 v[60:63], a[96:99], a[156:159], v[60:63]\n\t"
"v_cvt_pk_bf16_f32 v88, v88, v89\n\t"
"v_cvt_pk_bf16_f32 v89, v90, v91\n\t"
"v_mfma_f32_16x16x32_bf16 v[60:63], a[100:103], a[172:175], v[60:63]\n\t"
"ds_read_b128 a[156:159], v216 offset:23552\n\t"
"ds_read_b128 a[172:175], v216 offset:31744\n\t"
"s_waitcnt vmcnt(9) lgkmcnt(7)\n\t"
"v_mfma_f32_16x16x32_bf16 v[144:147], a[120:123], a[144:147], v[144:147]\n\t"
"v_mfma_f32_16x16x32_bf16 v[144:147], a[124:127], a[160:163], v[144:147]\n\t"
"s_add_u32 s25, s24, 0x1b0000\n\t"
"buffer_store_dwordx4 a[24:27], v232, s[16:19], s25 offen offset:0 nt\n\t"
"buffer_store_dwordx4 a[28:31], v233, s[16:19], s25 offen offset:0 nt\n\t"
"v_mfma_f32_16x16x32_bf16 v[160:163], a[128:131], a[144:147], v[160:163]\n\t"
"v_mfma_f32_16x16x32_bf16 v[160:163], a[132:135], a[160:163], v[160:163]\n\t"
"v_cvt_pk_bf16_f32 v60, v60, v61\n\t"
"v_cvt_pk_bf16_f32 v61, v62, v63\n\t"
"v_cvt_pk_bf16_f32 v76, v76, v77\n\t"
"v_cvt_pk_bf16_f32 v77, v78, v79\n\t"
"v_cvt_pk_bf16_f32 v92, v92, v93\n\t"
"v_cvt_pk_bf16_f32 v93, v94, v95\n\t"
"v_mfma_f32_16x16x32_bf16 v[176:179], a[136:139], a[144:147], v[176:179]\n\t"
"v_mfma_f32_16x16x32_bf16 v[176:179], a[140:143], a[160:163], v[176:179]\n\t"
"ds_write2st64_b64 v228, v[56:57], v[60:61] offset0:104 offset1:117\n\t"
"ds_write2st64_b64 v246, v[72:73], v[76:77] offset0:104 offset1:117\n\t"
"ds_write2st64_b64 v247, v[88:89], v[92:93] offset0:104 offset1:117\n\t"
"v_mfma_f32_16x16x32_bf16 v[180:183], a[136:139], a[148:151], v[180:183]\n\t"
"v_mfma_f32_16x16x32_bf16 v[180:183], a[140:143], a[164:167], v[180:183]\n\t"
"buffer_store_dwordx4 a[32:35], v234, s[16:19], s25 offen offset:0 nt\n\t"
"v_mfma_f32_16x16x32_bf16 v[164:167], a[128:131], a[148:151], v[164:167]\n\t"
"v_mfma_f32_16x16x32_bf16 v[164:167], a[132:135], a[164:167], v[164:167]\n\t"
"v_cvt_pk_bf16_f32 v144, v144, v145\n\t"
"v_cvt_pk_bf16_f32 v145, v146, v147\n\t"
"v_cvt_pk_bf16_f32 v160, v160, v161\n\t"
"v_cvt_pk_bf16_f32 v161, v162, v163\n\t"
"v_mfma_f32_16x16x32_bf16 v[148:151], a[120:123], a[148:151], v[148:151]\n\t"
"v_cvt_pk_bf16_f32 v176, v176, v177\n\t"
"v_cvt_pk_bf16_f32 v177, v178, v179\n\t"
"v_mfma_f32_16x16x32_bf16 v[148:151], a[124:127], a[164:167], v[148:151]\n\t"
"s_waitcnt lgkmcnt(0)\n\t"
"s_barrier\n\t"
"v_mfma_f32_16x16x32_bf16 v[152:155], a[120:123], a[152:155], v[152:155]\n\t"
"ds_read_b128 a[36:39], v229 offset:0\n\t"
"v_mfma_f32_16x16x32_bf16 v[152:155], a[124:127], a[168:171], v[152:155]\n\t"
"ds_read_b128 a[40:43], v230 offset:0\n\t"
"ds_read_b128 a[44:47], v231 offset:0\n\t"
"v_mfma_f32_16x16x32_bf16 v[168:171], a[128:131], a[152:155], v[168:171]\n\t"
"ds_read_b128 a[48:51], v229 offset:53248\n\t"
"v_mfma_f32_16x16x32_bf16 v[168:171], a[132:135], a[168:171], v[168:171]\n\t"
"v_cvt_pk_bf16_f32 v148, v148, v149\n\t"
"v_cvt_pk_bf16_f32 v149, v150, v151\n\t"
"v_cvt_pk_bf16_f32 v164, v164, v165\n\t"
"v_cvt_pk_bf16_f32 v165, v166, v167\n\t"
"v_cvt_pk_bf16_f32 v180, v180, v181\n\t"
"v_cvt_pk_bf16_f32 v181, v182, v183\n\t"
"v_mfma_f32_16x16x32_bf16 v[184:187], a[136:139], a[152:155], v[184:187]\n\t"
"ds_read_b128 a[52:55], v230 offset:53248\n\t"
"v_mfma_f32_16x16x32_bf16 v[184:187], a[140:143], a[168:171], v[184:187]\n\t"
"ds_write2st64_b64 v228, v[144:145], v[148:149] offset0:26 offset1:39\n\t"
"ds_write2st64_b64 v246, v[160:161], v[164:165] offset0:26 offset1:39\n\t"
"ds_write2st64_b64 v247, v[176:177], v[180:181] offset0:26 offset1:39\n\t"
"v_mfma_f32_16x16x32_bf16 v[188:191], a[136:139], a[156:159], v[188:191]\n\t"
"ds_read_b128 a[56:59], v231 offset:53248\n\t"
"v_mfma_f32_16x16x32_bf16 v[188:191], a[140:143], a[172:175], v[188:191]\n\t"
"s_waitcnt lgkmcnt(8)\n\t"
"buffer_store_dwordx4 a[36:39], v232, s[16:19], s28 offen offset:384 nt\n\t"
"s_waitcnt lgkmcnt(7)\n\t"
"buffer_store_dwordx4 a[40:43], v233, s[16:19], s28 offen offset:384 nt\n\t"
"v_mfma_f32_16x16x32_bf16 v[172:175], a[128:131], a[156:159], v[172:175]\n\t"
"v_mfma_f32_16x16x32_bf16 v[172:175], a[132:135], a[172:175], v[172:175]\n\t"
"v_cvt_pk_bf16_f32 v152, v152, v153\n\t"
"v_cvt_pk_bf16_f32 v153, v154, v155\n\t"
"v_cvt_pk_bf16_f32 v168, v168, v169\n\t"
"v_cvt_pk_bf16_f32 v169, v170, v171\n\t"
"v_cvt_pk_bf16_f32 v184, v184, v185\n\t"
"v_cvt_pk_bf16_f32 v185, v186, v187\n\t"
"v_mfma_f32_16x16x32_bf16 v[156:159], a[120:123], a[156:159], v[156:159]\n\t"
"v_mfma_f32_16x16x32_bf16 v[156:159], a[124:127], a[172:175], v[156:159]\n\t"
"s_waitcnt lgkmcnt(6)\n\t"
"buffer_store_dwordx4 a[44:47], v234, s[16:19], s28 offen offset:384 nt\n\t"
"s_waitcnt lgkmcnt(5)\n\t"
"buffer_store_dwordx4 a[48:51], v232, s[16:19], s27 offen offset:384 nt\n\t"
"s_waitcnt lgkmcnt(4)\n\t"
"buffer_store_dwordx4 a[52:55], v233, s[16:19], s27 offen offset:384 nt\n\t"
"s_waitcnt lgkmcnt(0)\n\t"
"buffer_store_dwordx4 a[56:59], v234, s[16:19], s27 offen offset:384 nt\n\t"
"v_cvt_pk_bf16_f32 v156, v156, v157\n\t"
"v_cvt_pk_bf16_f32 v157, v158, v159\n\t"
"ds_write2st64_b64 v228, v[152:153], v[156:157] offset0:52 offset1:65\n\t"
"v_cvt_pk_bf16_f32 v172, v172, v173\n\t"
"v_cvt_pk_bf16_f32 v173, v174, v175\n\t"
"ds_write2st64_b64 v246, v[168:169], v[172:173] offset0:52 offset1:65\n\t"
"v_cvt_pk_bf16_f32 v188, v188, v189\n\t"
"v_cvt_pk_bf16_f32 v189, v190, v191\n\t"
"ds_write2st64_b64 v247, v[184:185], v[188:189] offset0:52 offset1:65\n\t"
"s_waitcnt lgkmcnt(0)\n\t"
"s_barrier\n\t"
"ds_read_b128 a[60:63], v229 offset:13312\n\t"
"ds_read_b128 a[64:67], v230 offset:13312\n\t"
"ds_read_b128 a[68:71], v231 offset:13312\n\t"
"ds_read_b128 a[72:75], v229 offset:26624\n\t"
"ds_read_b128 a[76:79], v230 offset:26624\n\t"
"ds_read_b128 a[80:83], v231 offset:26624\n\t"
"s_waitcnt lgkmcnt(5)\n\t"
"buffer_store_dwordx4 a[60:63], v232, s[16:19], s20 offen offset:384 nt\n\t"
"s_waitcnt lgkmcnt(4)\n\t"
"buffer_store_dwordx4 a[64:67], v233, s[16:19], s20 offen offset:384 nt\n\t"
"s_waitcnt lgkmcnt(3)\n\t"
"buffer_store_dwordx4 a[68:71], v234, s[16:19], s20 offen offset:384 nt\n\t"
"s_waitcnt lgkmcnt(2)\n\t"
"buffer_store_dwordx4 a[72:75], v232, s[16:19], s25 offen offset:384 nt\n\t"
"s_waitcnt lgkmcnt(1)\n\t"
"buffer_store_dwordx4 a[76:79], v233, s[16:19], s25 offen offset:384 nt\n\t"
"s_waitcnt lgkmcnt(0)\n\t"
"buffer_store_dwordx4 a[80:83], v234, s[16:19], s25 offen offset:384 nt\n\t"
"s_waitcnt vmcnt(0)\n\t"
"Lwino_end:\n\t"
  : "+{v205}"(tid)
  : "s"(__builtin_amdgcn_kernarg_segment_ptr()), "s"(blockIdx.x), "s"((unsigned)(size_t)(__attribute__((address_space(3))) char*)smem)
  : "v0", "v1", "v2", "v3", "v4", "v5", "v6", "v7", "v8", "v9", "v10", "v11", "v12", "v13", "v14", "v15", "v16", "v17", "v18", "v19", "v20", "v21", "v22", "v23", "v24", "v25", "v26", "v27", "v28", "v29", "v30", "v31", "v32", "v33", "v34", "v35", "v36", "v37", "v38", "v39", "v40", "v41", "v42", "v43", "v44", "v45", "v46", "v47", "v48", "v49", "v50", "v51", "v52", "v53", "v54", "v55", "v56", "v57", "v58", "v59", "v60", "v61", "v62", "v63", "v64", "v65", "v66", "v67", "v68", "v69", "v70", "v71", "v72", "v73", "v74", "v75", "v76", "v77", "v78", "v79", "v80", "v81", "v82", "v83", "v84", "v85", "v86", "v87", "v88", "v89", "v90", "v91", "v92", "v93", "v94", "v95", "v96", "v97", "v98", "v99", "v100", "v101", "v102", "v103", "v104", "v105", "v106", "v107", "v108", "v109", "v110", "v111", "v112", "v113", "v114", "v115", "v116", "v117", "v118", "v119", "v120", "v121", "v122", "v123", "v124", "v125", "v126", "v127", "v128", "v129", "v130", "v131", "v132", "v133", "v134", "v135", "v136", "v137", "v138", "v139", "v140", "v141", "v142", "v143", "v144", "v145", "v146", "v147", "v148", "v149", "v150", "v151", "v152", "v153", "v154", "v155", "v156", "v157", "v158", "v159", "v160", "v161", "v162", "v163", "v164", "v165", "v166", "v167", "v168", "v169", "v170", "v171", "v172", "v173", "v174", "v175", "v176", "v177", "v178", "v179", "v180", "v181", "v182", "v183", "v184", "v185", "v186", "v187", "v188", "v189", "v190", "v191", "v192", "v193", "v194", "v195", "v196", "v197", "v198", "v199", "v200", "v201", "v202", "v203", "v204", "v206", "v207", "v208", "v209", "v210", "v211", "v212", "v213", "v214", "v215", "v216", "v217", "v218", "v219", "v220", "v221", "v222", "v223", "v224", "v225", "v226", "v227", "v228", "v229", "v230", "v231", "v232", "v233", "v234", "v235", "v236", "v237", "v238", "v239", "v240", "v241", "v242", "v243", "v244", "v245", "v246", "v247", "v248", "v249", "v250", "v251", "v252", "v253", "v254", "v255", "a0", "a1", "a2", "a3", "a4", "a5", "a6", "a7", "a8", "a9", "a10", "a11", "a12", "a13", "a14", "a15", "a16", "a17", "a18", "a19", "a20", "a21", "a22", "a23", "a24", "a25", "a26", "a27", "a28", "a29", "a30", "a31", "a32", "a33", "a34", "a35", "a36", "a37", "a38", "a39", "a40", "a41", "a42", "a43", "a44", "a45", "a46", "a47", "a48", "a49", "a50", "a51", "a52", "a53", "a54", "a55", "a56", "a57", "a58", "a59", "a60", "a61", "a62", "a63", "a64", "a65", "a66", "a67", "a68", "a69", "a70", "a71", "a72", "a73", "a74", "a75", "a76", "a77", "a78", "a79", "a80", "a81", "a82", "a83", "a84", "a85", "a86", "a87", "a88", "a89", "a90", "a91", "a92", "a93", "a94", "a95", "a96", "a97", "a98", "a99", "a100", "a101", "a102", "a103", "a104", "a105", "a106", "a107", "a108", "a109", "a110", "a111", "a112", "a113", "a114", "a115", "a116", "a117", "a118", "a119", "a120", "a121", "a122", "a123", "a124", "a125", "a126", "a127", "a128", "a129", "a130", "a131", "a132", "a133", "a134", "a135", "a136", "a137", "a138", "a139", "a140", "a141", "a142", "a143", "a144", "a145", "a146", "a147", "a148", "a149", "a150", "a151", "a152", "a153", "a154", "a155", "a156", "a157", "a158", "a159", "a160", "a161", "a162", "a163", "a164", "a165", "a166", "a167", "a168", "a169", "a170", "a171", "a172", "a173", "a174", "a175", "a176", "a177", "a178", "a179", "a180", "a181", "a182", "a183", "a184", "a185", "a186", "a187", "a188", "a189", "a190", "a191", "a192", "a193", "a194", "a195", "a196", "a197", "a198", "a199", "a200", "a201", "a202", "a203", "a204", "a205", "a206", "a207", "a208", "a209", "a210", "a211", "a212", "a213", "a214", "a215", "a216", "a217", "a218", "a219", "a220", "a221", "a222", "a223", "a224", "a225", "a226", "a227", "a228", "a229", "a230", "a231", "a232", "a233", "a234", "a235", "a236", "a237", "a238", "a239", "a240", "a241", "a242", "a243", "a244", "a245", "a246", "a247", "a248", "a249", "a250", "a251", "a252", "a253", "a254", "a255", "s6", "s7", "s8", "s9", "s10", "s11", "s12", "s13", "s14", "s15", "s16", "s17", "s18", "s19", "s20", "s21", "s22", "s23", "s24", "s25", "s26", "s27", "s28", "s29", "s30", "s31", "s32", "s33", "s34", "s35", "s36", "s37", "s38", "s39", "s40", "s41", "s42", "s43", "s44", "s45", "s46", "s47", "s48", "s49", "s50", "s51", "s52", "s53", "s54", "s55", "s56", "s57", "s58", "s59", "s60", "s61", "s62", "s63", "s64", "s65", "s66", "s67", "s68", "s69", "s70", "s71", "s72", "s73", "s74", "s75", "s76", "s77", "s78", "s79", "s80", "s81", "s82", "s83", "scc", "vcc", "memory");
}

extern "C" void kernel_launch(void* const* d_in, const int* in_sizes, int n_in, void* d_out, int out_size, void* d_ws,
                              size_t ws_size, hipStream_t stream) {
  const __hip_bfloat16* x = (const __hip_bfloat16*)d_in[0];
  const __hip_bfloat16* w = (const __hip_bfloat16*)d_in[1];
  __hip_bfloat16* Uq = (__hip_bfloat16*)d_ws;
  wino_main_kernel<<<256, 256, 0, stream>>>((const char*)x, (char*)Uq, (char*)d_out, (const char*)w);
}
```

```cpp
#include <hip/hip_runtime.h>
#include <hip/hip_bf16.h>
#include <stdint.h>

#define U_BYTES 1179648

__global__ __launch_bounds__(256, 1) void wino_main_kernel(const char* __restrict__ x, char* __restrict__ Uq, char* __restrict__ out, const char* __restrict__ wt) {
  __shared__ __attribute__((aligned(1024))) char smem[163840];
  unsigned tid = threadIdx.x;
  asm volatile(
"s_load_dwordx8 s[76:83], %1, 0x0\n\t"
"s_mov_b32 s66, %2\n\t"
"s_getpc_b64 s[6:7]\n\t"
"Lwino_pc:\n\t"
"s_bfe_u32 s42, %2, 0x50003\n\t"
"s_lshl_b32 s42, s42, 12\n\t"
"v_lshl_add_u32 v203, v205, 4, s42\n\t"
"v_min_u32 v203, (Lwino_end-Lwino_pc-16)&-16, v203\n\t"
"global_load_dwordx4 v[196:199], v203, s[6:7]\n\t"
"v_and_b32 v204, 63, v205\n\t"
"v_readfirstlane_b32 s73, v205\n\t"
"v_lshrrev_b32 v120, 3, v204\n\t"
"v_and_b32 v121, 7, v204\n\t"
"s_lshr_b32 s73, s73, 6\n\t"
"s_lshr_b32 s56, s66, 5\n\t"
"s_mul_i32 s56, s56, 12288\n\t"
"s_bfe_u32 s61, s66, 0x20001\n\t"
"s_mul_i32 s61, s61, 1536\n\t"
"s_add_u32 s56, s56, s61\n\t"
"s_bitcmp1_b32 s66, 0\n\t"
"s_cselect_b32 s61, 48, 0\n\t"
"s_add_u32 s56, s56, s61\n\t"
"s_bfe_u32 s62, s66, 0x20003\n\t"
"s_mul_i32 s61, s62, 96\n\t"
"s_add_u32 s56, s56, s61\n\t"
"s_cmp_lt_u32 s66, 192\n\t"
"s_cselect_b32 s56, s56, 2.0\n\t"
"v_bfe_u32 v42, v204, 2, 2\n\t"
"v_and_or_b32 v42, v204, 16, v42\n\t"
"v_mul_u32_u24 v42, 0x180, v42\n\t"
"v_lshrrev_b32 v44, 5, v204\n\t"
"v_mul_u32_u24 v43, 0x12000, v44\n\t"
"v_and_b32 v45, 3, v204\n\t"
"v_min_u32 v41, 2, v45\n\t"
"v_lshl_add_u32 v41, v41, 4, v42\n\t"
"v_add_u32 v41, v41, v43\n\t"
"v_lshlrev_b32 v43, 2, v204\n\t"
"v_and_or_b32 v45, v43, 16, v45\n\t"
"v_mul_u32_u24 v45, 0x180, v45\n\t"
"v_lshl_add_u32 v44, v44, 3, v120\n\t"
"v_lshl_add_u32 v45, v44, 1, v45\n\t"
"v_mov_b32 v40, 0\n\t"
"s_mul_i32 s61, s73, 147456\n\t"
"s_add_u32 s61, s61, s56\n\t"
"s_lshl3_add_u32 s62, s73, s56\n\t"
"s_add_u32 s62, s62, 589824\n\t"
"s_lshl_b32 s64, s73, 10\n\t"
"s_and_b32 s60, s66, 7\n\t"
"s_lshr_b32 s65, s66, 3\n\t"
"s_mul_i32 s24, s60, 12582912\n\t"
"s_mul_i32 s63, s65, 6144\n\t"
"s_add_u32 s24, s24, s63\n\t"
"s_add_u32 s22, s24, 196224\n\t"
"s_mul_i32 s63, s73, 786432\n\t"
"s_mul_i32 s26, s73, 4608\n\t"
"s_mov_b32 s48, 189696\n\t"
"s_cmp_eq_u32 s65, 0\n\t"
"s_cselect_b64 s[40:41], 0xff, 0\n\t"
"s_cselect_b64 s[46:47], -1, 0\n\t"
"v_mul_u32_u24 v122, 0x300, v120\n\t"
"v_xor_b32 v123, 4, v121\n\t"
"v_add_u32 v122, s63, v122\n\t"
"v_and_b32 v124, 2, v120\n\t"
"v_xor_b32 v124, v124, v123\n\t"
"v_lshl_add_u32 v128, v124, 4, v122\n\t"
"v_cndmask_b32 v241, v128, 2.0, s[40:41]\n\t"
"v_add_u32 v122, 194816, v122\n\t"
"v_add_u32 v124, -1, v120\n\t"
"v_ashrrev_i32 v125, 31, v124\n\t"
"v_sub_u32 v124, v124, v125\n\t"
"v_and_b32 v124, 2, v124\n\t"
"v_xor_b32 v124, v124, v123\n\t"
"v_mad_i32_i24 v125, v125, s48, v122\n\t"
"v_lshl_add_u32 v129, v124, 4, v125\n\t"
"s_lshl_b64 s[40:41], s[40:41], 8\n\t"
"v_cndmask_b32 v242, v129, 2.0, s[40:41]\n\t"
"v_add_u32 v122, 194816, v122\n\t"
"v_add_u32 v124, -2, v120\n\t"
"v_ashrrev_i32 v125, 31, v124\n\t"
"v_sub_u32 v124, v124, v125\n\t"
"v_and_b32 v124, 2, v124\n\t"
"v_and_or_b32 v124, v125, 4, v124\n\t"
"v_xor_b32 v124, v124, v121\n\t"
"v_mad_i32_i24 v125, v125, s48, v122\n\t"
"v_lshl_add_u32 v130, v124, 4, v125\n\t"
"s_lshl_b64 s[40:41], s[40:41], 8\n\t"
"v_cndmask_b32 v243, v130, 2.0, s[40:41]\n\t"
"v_add_u32 v122, 194816, v122\n\t"
"v_add_u32 v124, -3, v120\n\t"
"v_ashrrev_i32 v125, 31, v124\n\t"
"v_sub_u32 v124, v124, v125\n\t"
"v_and_b32 v124, 2, v124\n\t"
"v_xor_b32 v124, v124, v121\n\t"
"v_mad_i32_i24 v125, v125, s48, v122\n\t"
"v_lshl_add_u32 v131, v124, 4, v125\n\t"
"s_lshl_b64 s[40:41], s[40:41], 8\n\t"
"v_cndmask_b32 v244, v131, 2.0, s[40:41]\n\t"
"v_add_u32 v124, 5, v120\n\t"
"v_and_b32 v124, 2, v124\n\t"
"v_xor_b32 v124, v124, v121\n\t"
"v_add_u32 v122, 2560, v122\n\t"
"v_lshl_add_u32 v245, v124, 4, v122\n\t"
"s_mov_b32 s37, -1\n\t"
"s_mov_b32 s36, 0\n\t"
"s_mul_i32 s67, s73, 48\n\t"
"s_add_u32 s67, s67, 118784\n\t"
"s_mov_b32 s10, 0x200000\n\t"
"s_mov_b32 s11, 0x20000\n\t"
"v_min_u32 v189, 47, v204\n\t"
"v_add_u32 v189, s67, v189\n\t"
"v_mul_u32_u24 v189, 16, v189\n\t"
"s_mov_b32 s54, 663552\n\t"
"s_mov_b32 s55, 0x20000\n\t"
"s_mov_b32 s14, 100663296\n\t"
"s_mov_b32 s15, 0x20000\n\t"
"s_add_u32 m0, s64, 0\n\t"
"s_waitcnt lgkmcnt(0)\n\t"
"s_mov_b32 s52, s82\n\t"
"s_and_b32 s53, s83, 0xffff\n\t"
"buffer_load_dwordx4 v41, s[52:55], s61 offen lds\n\t"
"buffer_load_short_d16_hi v40, v45, s[52:55], s62 offen\n\t"
"s_mov_b32 s8, s78\n\t"
"s_and_b32 s9, s79, 0xffff\n\t"
"buffer_load_dword v188, v189, s[8:11], 0 offen sc1\n\t"
"s_mov_b32 s12, s76\n\t"
"s_and_b32 s13, s77, 0xffff\n\t"
"s_add_u32 m0, s26, 83200\n\t"
"s_nop 0\n\t"
"buffer_load_dwordx4 v241, s[12:15], s22 offen offset:0 nt lds\n\t"
"buffer_load_dwordx4 v242, s[12:15], s22 offen offset:1024 nt lds\n\t"
"buffer_load_dwordx4 v243, s[12:15], s22 offen offset:2048 nt lds\n\t"
"buffer_load_dwordx4 v244, s[12:15], s22 offen offset:3072 nt lds\n\t"
"s_mov_b64 exec, s[36:37]\n\t"
"buffer_load_dwordx4 v245, s[12:15], s22 offen offset:3584 nt lds\n\t"
"s_mov_b64 exec, -1\n\t"
"s_mul_i32 s49, s73, 36\n\t"
"s_mul_i32 s43, s73, 576\n\t"
"s_sub_u32 s42, s22, 203520\n\t"
"s_mov_b32 s44, -1\n\t"
"s_mov_b32 s45, 15\n\t"
"v_min_u32 v124, 35, v204\n\t"
"v_add_u32 v124, s49, v124\n\t"
"v_lshrrev_b32 v119, 3, v124\n\t"
"v_and_b32 v124, 7, v124\n\t"
"v_mul_u32_u24 v127, 0x300, v119\n\t"
"v_cmp_gt_u32 vcc, 9, v119\n\t"
"v_add_u32 v119, -9, v119\n\t"
"v_ashrrev_i32 v125, 31, v119\n\t"
"v_add_u32 v127, s42, v127\n\t"
"v_mad_i32_i24 v127, v125, s48, v127\n\t"
"v_and_b32 v125, 9, v125\n\t"
"v_add_u32 v119, v119, v125\n\t"
"v_and_b32 v125, 2, v119\n\t"
"v_xor_b32 v124, v124, v125\n\t"
"v_lshl_add_u32 v133, v124, 4, v127\n\t"
"s_cmp_eq_u32 s60, 0\n\t"
"s_cselect_b64 s[50:51], vcc, 0\n\t"
"v_cmp_eq_u32 vcc, 0, v119\n\t"
"v_cndmask_b32 v246, v133, 2.0, s[50:51]\n\t"
"s_and_b64 vcc, vcc, s[46:47]\n\t"
"v_cndmask_b32 v246, v246, 2.0, vcc\n\t"
"s_add_u32 m0, s43, 80896\n\t"
"s_mov_b64 exec, s[44:45]\n\t"
"buffer_load_dwordx4 v246, s[12:15], 0 offen offset:0 nt lds\n\t"
"s_mov_b64 exec, -1\n\t"
"s_mul_i32 s70, s66, 16\n\t"
"s_add_u32 s70, s70, 1900544\n\t"
"s_lshl_b32 s61, s73, 8\n\t"
"s_or_b32 s61, s61, s66\n\t"
"s_cmp_lt_u32 s61, 192\n\t"
"s_cselect_b32 s70, s70, 2.0\n\t"
"s_lshr_b32 s61, s66, 3\n\t"
"s_lshl2_add_u32 s61, s61, s73\n\t"
"s_lshl_b32 s60, s61, 8\n\t"
"s_cmp_lt_u32 s61, 96\n\t"
"s_cselect_b32 s60, s60, 2.0\n\t"
"v_lshrrev_b32 v118, 1, v204\n\t"
"v_mul_u32_u24 v118, 220928, v118\n\t"
"v_lshl_add_u32 v118, v204, 7, v118\n\t"
"s_mov_b64 exec, 63\n\t"
"buffer_load_dword v118, v118, s[52:55], s60 offen\n\t"
"s_mov_b64 exec, -1\n\t"
"buffer_load_dword v187, off, s[8:11], s70 sc1\n\t"
"v_add_u32 v107, 0, v204\n\t"
"v_mul_u32_u24 v104, 0xab, v107\n\t"
"v_lshrrev_b32 v104, 10, v104\n\t"
"v_mul_u32_u24 v104, 0x120, v104\n\t"
"v_lshl_add_u32 v104, v107, 4, v104\n\t"
"v_add_u32 v107, 64, v204\n\t"
"v_mul_u32_u24 v105, 0xab, v107\n\t"
"v_lshrrev_b32 v105, 10, v105\n\t"
"v_mul_u32_u24 v105, 0x120, v105\n\t"
"v_lshl_add_u32 v105, v107, 4, v105\n\t"
"v_add_u32 v107, 128, v204\n\t"
"v_mul_u32_u24 v106, 0xab, v107\n\t"
"v_lshrrev_b32 v106, 10, v106\n\t"
"v_mul_u32_u24 v106, 0x120, v106\n\t"
"v_lshl_add_u32 v106, v107, 4, v106\n\t"
"s_mul_i32 s74, s73, 41472\n\t"
"s_mul_i32 s72, s73, 96\n\t"
"s_add_u32 s74, s74, 60160\n\t"
"s_cmp_eq_u32 s73, 0\n\t"
"s_cselect_b32 s74, 53504, s74\n\t"
"s_add_u32 s75, s74, 12288\n\t"
"s_cmp_eq_u32 s73, 3\n\t"
"s_cselect_b32 s74, 16384, s74\n\t"
"s_cselect_b32 s75, 74240, s75\n\t"
"s_lshr_b32 s60, s66, 5\n\t"
"s_bfe_u32 s62, s66, 0x20003\n\t"
"s_and_b32 s61, s60, 1\n\t"
"s_lshl1_add_u32 s61, s73, s61\n\t"
"s_mul_i32 s62, s62, 288\n\t"
"s_add_u32 s61, s61, s62\n\t"
"s_lshr_b32 s60, s60, 1\n\t"
"s_mul_i32 s62, s60, 96\n\t"
"s_add_u32 s61, s61, s62\n\t"
"s_lshl_b32 s57, s61, 10\n\t"
"s_and_b32 s61, s66, 7\n\t"
"s_lshl_b32 s61, s61, 7\n\t"
"s_add_u32 s57, s57, s61\n\t"
"s_cmp_lt_u32 s73, 3\n\t"
"s_cselect_b32 s67, s66, 0xff\n\t"
"s_cmp_lt_u32 s67, 192\n\t"
"s_cselect_b32 s57, s57, 2.0\n\t"
"s_lshl_b32 s60, s73, 3\n\t"
"s_add_u32 s60, s60, 0\n\t"
"s_lshl_b32 s61, s73, 11\n\t"
"s_add_u32 s61, s61, 4096\n\t"
"v_lshl_add_u32 v46, v121, 6, s60\n\t"
"v_lshl_add_u32 v46, v44, 1, v46\n\t"
"v_lshl_add_u32 v42, v204, 1, s61\n\t"
"v_lshl_add_u32 v43, v204, 4, s61\n\t"
"v_mul_u32_u24 v44, 6144, v120\n\t"
"v_lshl_add_u32 v44, v121, 4, v44\n\t"
"v_mov_b32 v32, 0\n\t"
"v_mov_b32 v33, 0\n\t"
"v_mov_b32 v34, 0\n\t"
"v_mov_b32 v35, 0\n\t"
"v_mov_b32 v36, 0\n\t"
"v_mov_b32 v37, 0\n\t"
"v_mov_b32 v38, 0\n\t"
"v_mov_b32 v39, 0\n\t"
"s_waitcnt vmcnt(9)\n\t"
"s_barrier\n\t"
"ds_read_u16_d16_hi v32, v46 offset:0\n\t"
"ds_read_u16_d16_hi v33, v46 offset:512\n\t"
"ds_read_u16_d16_hi v34, v46 offset:1024\n\t"
"ds_read_u16_d16_hi v35, v46 offset:1536\n\t"
"ds_read_u16_d16_hi v36, v46 offset:2048\n\t"
"ds_read_u16_d16_hi v37, v46 offset:2560\n\t"
"ds_read_u16_d16_hi v38, v46 offset:3072\n\t"
"ds_read_u16_d16_hi v39, v46 offset:3584\n\t"
"s_waitcnt lgkmcnt(1)\n\t"
"v_add_f32 v48, v32, v38\n\t"
"s_waitcnt lgkmcnt(0)\n\t"
"v_add_f32 v49, v33, v39\n\t"
"v_add_f32 v50, v34, v40\n\t"
"v_add_f32 v51, v48, v35\n\t"
"v_sub_f32 v54, v48, v35\n\t"
"v_add_f32 v52, v49, v36\n\t"
"v_sub_f32 v55, v49, v36\n\t"
"v_add_f32 v53, v50, v37\n\t"
"v_sub_f32 v56, v50, v37\n\t"
"v_add_f32 v57, v32, v34\n\t"
"v_add_f32 v58, v51, v53\n\t"
"v_add_f32 v59, v54, v56\n\t"
"v_add_f32 v60, v38, v40\n\t"
"v_add_f32 v62, v57, v33\n\t"
"v_sub_f32 v63, v57, v33\n\t"
"v_add_f32 v64, v58, v52\n\t"
"v_sub_f32 v65, v58, v52\n\t"
"v_add_f32 v66, v59, v55\n\t"
"v_sub_f32 v67, v59, v55\n\t"
"v_add_f32 v68, v60, v39\n\t"
"v_sub_f32 v69, v60, v39\n\t"
"v_mul_f32 v71, 0.5, v62\n\t"
"v_mul_f32 v72, 0.5, v63\n\t"
"v_mul_f32 v74, 0.5, v51\n\t"
"v_mul_f32 v75, 0x3e800000, v64\n\t"
"v_mul_f32 v76, 0x3e800000, v65\n\t"
"v_mul_f32 v77, -0.5, v53\n\t"
"v_mul_f32 v79, 0.5, v66\n\t"
"v_mul_f32 v80, 0.5, v67\n\t"
"v_mul_f32 v83, -0.5, v68\n\t"
"v_mul_f32 v84, -0.5, v69\n\t"
"v_cvt_pk_bf16_f32 v86, v32, v71\n\t"
"v_cvt_pk_bf16_f32 v87, v72, -v34\n\t"
"v_cvt_pk_bf16_f32 v88, v74, v75\n\t"
"v_cvt_pk_bf16_f32 v89, v76, v77\n\t"
"v_cvt_pk_bf16_f32 v90, v54, v79\n\t"
"v_cvt_pk_bf16_f32 v91, v80, -v56\n\t"
"v_cvt_pk_bf16_f32 v92, -v38, v83\n\t"
"v_cvt_pk_bf16_f32 v93, v84, v40\n\t"
"ds_write_b16 v42, v86 offset:0\n\t"
"ds_write_b16_d16_hi v42, v86 offset:128\n\t"
"ds_write_b16 v42, v87 offset:256\n\t"
"ds_write_b16_d16_hi v42, v87 offset:384\n\t"
"ds_write_b16 v42, v88 offset:512\n\t"
"ds_write_b16_d16_hi v42, v88 offset:640\n\t"
"ds_write_b16 v42, v89 offset:768\n\t"
"ds_write_b16_d16_hi v42, v89 offset:896\n\t"
"ds_write_b16 v42, v90 offset:1024\n\t"
"ds_write_b16_d16_hi v42, v90 offset:1152\n\t"
"ds_write_b16 v42, v91 offset:1280\n\t"
"ds_write_b16_d16_hi v42, v91 offset:1408\n\t"
"ds_write_b16 v42, v92 offset:1536\n\t"
"ds_write_b16_d16_hi v42, v92 offset:1664\n\t"
"ds_write_b16 v42, v93 offset:1792\n\t"
"ds_write_b16_d16_hi v42, v93 offset:1920\n\t"
"ds_read_b128 v[96:99], v43 offset:0\n\t"
"ds_read_b128 v[100:103], v43 offset:1024\n\t"
"s_add_u32 s60, s57, 0\n\t"
"s_waitcnt lgkmcnt(1)\n\t"
"buffer_store_dwordx4 v[96:99], v44, s[8:11], s60 offen sc1\n\t"
"s_add_u32 s60, s57, 49152\n\t"
"s_waitcnt lgkmcnt(0)\n\t"
"buffer_store_dwordx4 v[100:103], v44, s[8:11], s60 offen sc1\n\t"
"v_and_b32 v122, 15, v204\n\t"
"v_lshrrev_b32 v123, 4, v204\n\t"
"v_bfe_u32 v124, v204, 2, 2\n\t"
"v_and_b32 v125, 3, v204\n\t"
"v_lshlrev_b32 v125, 3, v125\n\t"
"s_mov_b32 s60, 0x1be4\n\t"
"s_lshl_b32 s61, s73, 10\n\t"
"s_mov_b32 s62, 512\n\t"
"s_add_u32 s63, s26, 1152\n\t"
"v_and_b32 v126, 14, v204\n\t"
"v_bfe_u32 v126, s60, v126, 2\n\t"
"v_xor_b32 v126, v126, v123\n\t"
"v_lshlrev_b32 v126, 4, v126\n\t"
"v_lshl_add_u32 v216, v122, 6, v126\n\t"
"s_add_u32 m0, s74, 0\n\t"
"s_add_u32 s27, s72, 0\n\t"
"buffer_load_dwordx4 v104, s[52:55], s27 offen lds\n\t"
"v_add_u32 v221, s61, v216\n\t"
"v_lshrrev_b32 v126, 1, v121\n\t"
"v_xor_b32 v126, v126, v123\n\t"
"v_lshl_add_u32 v126, v126, 4, s61\n\t"
"v_lshl_add_u32 v126, v121, 6, v126\n\t"
"v_and_b32 v127, 8, v204\n\t"
"v_lshl_add_u32 v217, v127, 9, v126\n\t"
"v_xad_u32 v219, v217, 48, s62\n\t"
"v_sub_u32 v126, 4, v124\n\t"
"v_cmp_gt_u32 vcc, 32, v204\n\t"
"v_mov_b32 v135, 0x3f80bf80\n\t"
"s_nop 0\n\t"
"v_cndmask_b32 v126, v126, v124, vcc\n\t"
"v_and_b32 v127, 2, v126\n\t"
"s_add_u32 m0, s74, 1024\n\t"
"s_add_u32 s27, s72, 0\n\t"
"buffer_load_dwordx4 v105, s[52:55], s27 offen lds\n\t"
"v_lshl_add_u32 v127, v127, 4, v125\n\t"
"v_lshl_add_u32 v127, v126, 7, v127\n\t"
"v_and_b32 v126, 1, v123\n\t"
"v_mul_u32_u24 v126, 1216, v126\n\t"
"v_add3_u32 v226, v126, v127, s63\n\t"
"v_min_u32 v126, 2, v124\n\t"
"v_cmp_gt_u32 vcc, 48, v204\n\t"
"s_nop 1\n\t"
"v_cndmask_b32 v126, v126, v123, vcc\n\t"
"v_and_b32 v134, 1, v126\n\t"
"v_cndmask_b32 v127, 4, v124, vcc\n\t"
"v_lshl_add_u32 v134, v134, 6, v125\n\t"
"v_lshl_add_u32 v134, v127, 7, v134\n\t"
"v_and_b32 v127, 2, v127\n\t"
"v_lshl_add_u32 v134, v127, 4, v134\n\t"
"s_add_u32 m0, s74, 2048\n\t"
"s_add_u32 s27, s72, 0\n\t"
"buffer_load_dwordx4 v106, s[52:55], s27 offen lds\n\t"
"v_mul_u32_u24 v126, 2304, v126\n\t"
"v_add3_u32 v227, v126, v134, s26\n\t"
"v_and_b32 v126, 24, v204\n\t"
"v_cmp_eq_u32 vcc, 8, v126\n\t"
"v_mov_b32 v134, 0xbf803f80\n\t"
"s_nop 0\n\t"
"v_cndmask_b32 v134, v134, v135, vcc\n\t"
"v_lshrrev_b32 v135, 16, v134\n\t"
"v_cmp_eq_u32 vcc, 7, v121\n\t"
"s_nop 1\n\t"
"v_cndmask_b32 v135, 0, v135, vcc\n\t"
"v_cmp_gt_u32 vcc, 32, v204\n\t"
"s_mov_b32 s48, 384\n\t"
"s_cmp_eq_u32 s65, 31\n\t"
"v_cndmask_b32 v134, v135, v134, vcc\n\t"
"v_cndmask_b32 v136, 4, v121, vcc\n\t"
"v_mov_b32 v135, 0\n\t"
"v_and_b32 v137, 1, v136\n\t"
"v_lshlrev_b32 v137, 4, v137\n\t"
"v_lshrrev_b32 v136, 1, v136\n\t"
"v_lshlrev_b64 v[134:135], v137, v[134:135]\n\t"
"v_cmp_eq_u32 vcc, 0, v136\n\t"
"s_cselect_b64 s[40:41], 0xff, 0\n\t"
"s_cselect_b64 s[46:47], -1, 0\n\t"
"s_add_u32 m0, s74, 3072\n\t"
"s_add_u32 s27, s72, 12288\n\t"
"buffer_load_dwordx4 v104, s[52:55], s27 offen lds\n\t"
"v_cndmask_b32 v140, 0, v134, vcc\n\t"
"v_cndmask_b32 v141, 0, v135, vcc\n\t"
"v_cmp_eq_u32 vcc, 1, v136\n\t"
"v_accvgpr_write_b32 a220, v140\n\t"
"v_xad_u32 v236, v128, 64, s48\n\t"
"s_add_u32 m0, s74, 4096\n\t"
"s_add_u32 s27, s72, 12288\n\t"
"buffer_load_dwordx4 v105, s[52:55], s27 offen lds\n\t"
"v_cndmask_b32 v141, v141, v134, vcc\n\t"
"v_cndmask_b32 v142, 0, v135, vcc\n\t"
"v_cmp_eq_u32 vcc, 2, v136\n\t"
"v_accvgpr_write_b32 a221, v141\n\t"
"v_xad_u32 v237, v129, 64, s48\n\t"
"v_cndmask_b32 v142, v142, v134, vcc\n\t"
"v_cndmask_b32 v143, 0, v135, vcc\n\t"
"v_cmp_eq_u32 vcc, 3, v136\n\t"
"v_accvgpr_write_b32 a222, v142\n\t"
"v_cndmask_b32 v237, v237, 2.0, s[40:41]\n\t"
"v_cndmask_b32 v143, v143, v134, vcc\n\t"
"v_accvgpr_write_b32 a223, v143\n\t"
"s_lshl_b64 s[40:41], s[40:41], 8\n\t"
"s_add_u32 m0, s74, 5120\n\t"
"s_add_u32 s27, s72, 12288\n\t"
"buffer_load_dwordx4 v106, s[52:55], s27 offen lds\n\t"
"v_xad_u32 v238, v130, 64, s48\n\t"
"v_cndmask_b32 v238, v238, 2.0, s[40:41]\n\t"
"s_lshl_b64 s[40:41], s[40:41], 8\n\t"
"v_xad_u32 v239, v131, 64, s48\n\t"
"v_cndmask_b32 v239, v239, 2.0, s[40:41]\n\t"
"s_lshl_b64 s[40:41], s[40:41], 40\n\t"
"v_xad_u32 v240, v245, 64, s48\n\t"
"v_cndmask_b32 v240, v240, 2.0, s[40:41]\n\t"
"v_cmp_eq_u32 vcc, 8, v119\n\t"
"v_xad_u32 v247, v133, 64, s48\n\t"
"s_and_b64 vcc, vcc, s[46:47]\n\t"
"v_cndmask_b32 v247, v247, 2.0, s[50:51]\n\t"
"v_cndmask_b32 v247, v247, 2.0, vcc\n\t"
"v_bfe_u32 v126, v204, 3, 1\n\t"
"s_add_u32 m0, s74, 6144\n\t"
"s_add_u32 s27, s72, 442368\n\t"
"buffer_load_dwordx4 v104, s[52:55], s27 offen lds\n\t"
"v_lshrrev_b32 v127, 4, v204\n\t"
"v_sub_u32 v127, v127, v126\n\t"
"v_mov_b32 v134, 0xbf803f80\n\t"
"v_mov_b32 v135, 0x3f80bf80\n\t"
"v_cmp_eq_u32 vcc, 0, v127\n\t"
"v_add_u32 v126, 4, v126\n\t"
"s_nop 0\n\t"
"v_cndmask_b32 v134, 0, v134, vcc\n\t"
"v_cmp_eq_u32 vcc, 1, v127\n\t"
"s_nop 1\n\t"
"v_cndmask_b32 v134, v134, v135, vcc\n\t"
"v_cmp_eq_u32 vcc, 7, v121\n\t"
"s_nop 1\n\t"
"v_cndmask_b32 v135, 0, v135, vcc\n\t"
"v_cmp_gt_u32 vcc, 48, v204\n\t"
"s_nop 1\n\t"
"v_cndmask_b32 v134, v135, v134, vcc\n\t"
"s_add_u32 m0, s74, 7168\n\t"
"s_add_u32 s27, s72, 442368\n\t"
"buffer_load_dwordx4 v105, s[52:55], s27 offen lds\n\t"
"v_mov_b32 v135, 0\n\t"
"v_cndmask_b32 v136, v126, v121, vcc\n\t"
"v_and_b32 v137, 1, v136\n\t"
"v_lshlrev_b32 v137, 4, v137\n\t"
"v_lshrrev_b32 v136, 1, v136\n\t"
"v_lshlrev_b64 v[134:135], v137, v[134:135]\n\t"
"v_cmp_eq_u32 vcc, 0, v136\n\t"
"s_mov_b32 s68, 3000\n\t"
"s_mul_i32 s61, s73, 294912\n\t"
"v_cndmask_b32 v140, 0, v134, vcc\n\t"
"v_cndmask_b32 v141, 0, v135, vcc\n\t"
"v_cmp_eq_u32 vcc, 1, v136\n\t"
"v_accvgpr_write_b32 a224, v140\n\t"
"v_and_b32 v252, 0xff7fff7f, v140\n\t"
"s_add_u32 m0, s74, 8192\n\t"
"s_add_u32 s27, s72, 442368\n\t"
"buffer_load_dwordx4 v106, s[52:55], s27 offen lds\n\t"
"v_cndmask_b32 v141, v141, v134, vcc\n\t"
"v_cndmask_b32 v142, 0, v135, vcc\n\t"
"v_cmp_eq_u32 vcc, 2, v136\n\t"
"v_accvgpr_write_b32 a225, v141\n\t"
"v_xor_b32 v148, 0x80008000, v252\n\t"
"v_cndmask_b32 v142, v142, v134, vcc\n\t"
"v_cndmask_b32 v143, 0, v135, vcc\n\t"
"v_cmp_eq_u32 vcc, 3, v136\n\t"
"v_accvgpr_write_b32 a226, v142\n\t"
"v_and_b32 v253, 0xff7fff7f, v141\n\t"
"v_cndmask_b32 v143, v143, v134, vcc\n\t"
"v_accvgpr_write_b32 a227, v143\n\t"
"v_xor_b32 v149, 0x80008000, v253\n\t"
"s_add_u32 m0, s74, 9216\n\t"
"s_add_u32 s27, s72, 454656\n\t"
"buffer_load_dwordx4 v104, s[52:55], s27 offen lds\n\t"
"v_and_b32 v254, 0xff7fff7f, v142\n\t"
"v_xor_b32 v150, 0x80008000, v254\n\t"
"v_and_b32 v255, 0xff7fff7f, v143\n\t"
"v_xor_b32 v151, 0x80008000, v255\n\t"
"v_lshl_add_u32 v235, v204, 4, s61\n\t"
"v_mul_u32_u24 v107, 24, v204\n\t"
"v_add_u32 v47, s75, v107\n\t"
"v_add_u32 v107, s74, v107\n\t"
"v_lshrrev_b32 v112, 4, v204\n\t"
"v_bfe_u32 v113, v204, 2, 2\n\t"
"v_cmp_eq_u32 vcc, v112, v113\n\t"
"v_lshlrev_b32 v112, 4, v204\n\t"
"v_mov_b32 v113, 0x3e80\n\t"
"v_lshlrev_b32 v113, v112, v113\n\t"
"s_add_u32 m0, s74, 10240\n\t"
"s_add_u32 s27, s72, 454656\n\t"
"buffer_load_dwordx4 v105, s[52:55], s27 offen lds\n\t"
"v_cndmask_b32 v113, 0, v113, vcc\n\t"
"v_and_b32 v112, 2, v204\n\t"
"v_cmp_eq_u32 vcc, 0, v112\n\t"
"v_mov_b32 v108, 0\n\t"
"v_mov_b32 v109, 0\n\t"
"v_cndmask_b32 v110, 0, v113, vcc\n\t"
"v_cndmask_b32 v111, v113, 0, vcc\n\t"
"v_xor_b32 v114, 0x80008000, v110\n\t"
"v_xor_b32 v115, 0x80008000, v111\n\t"
"v_mov_b32 v112, 0\n\t"
"v_mov_b32 v113, 0\n\t"
"v_mov_b32 v116, 0\n\t"
"v_mov_b32 v117, 0\n\t"
"s_add_u32 m0, s74, 11264\n\t"
"s_add_u32 s27, s72, 454656\n\t"
"buffer_load_dwordx4 v106, s[52:55], s27 offen lds\n\t"
"s_waitcnt vmcnt(16)\n\t"
"s_waitcnt vmcnt(12)\n\t"
"v_readfirstlane_b32 s71, v187\n\t"
"s_barrier\n\t"
"s_add_u32 s71, s71, 1\n\t"
"v_mov_b32 v215, s71\n\t"
"s_mov_b64 exec, 1\n\t"
"buffer_store_dword v215, off, s[8:11], s70 sc1\n\t"
"s_mov_b64 exec, -1\n\t"
"v_add_u32 v248, 80896, v226\n\t"
"v_xor_b32 v249, 32, v248\n\t"
"v_xor_b32 v250, 64, v248\n\t"
"v_xor_b32 v251, 96, v248\n\t"
"ds_read_b64_tr_b16 a[176:177], v248 offset:0\n\t"
"ds_read_b64_tr_b16 a[178:179], v248 offset:512\n\t"
"ds_read_b64_tr_b16 a[180:181], v249 offset:0\n\t"
"ds_read_b64_tr_b16 a[182:183], v249 offset:512\n\t"
"s_add_u32 m0, s75, 0\n\t"
"s_add_u32 s27, s72, 221184\n\t"
"buffer_load_dwordx4 v104, s[52:55], s27 offen lds\n\t"
"ds_read_b64_tr_b16 a[184:185], v250 offset:2304\n\t"
"ds_read_b64_tr_b16 a[186:187], v250 offset:2816\n\t"
"ds_read_b64_tr_b16 a[188:189], v251 offset:2304\n\t"
"ds_read_b64_tr_b16 a[190:191], v251 offset:2816\n\t"
"s_add_u32 m0, s75, 1024\n\t"
"s_add_u32 s27, s72, 221184\n\t"
"buffer_load_dwordx4 v105, s[52:55], s27 offen lds\n\t"
"ds_read_b64_tr_b16 a[192:193], v250 offset:0\n\t"
"ds_read_b64_tr_b16 a[194:195], v250 offset:512\n\t"
"ds_read_b64_tr_b16 a[196:197], v251 offset:0\n\t"
"ds_read_b64_tr_b16 a[198:199], v251 offset:512\n\t"
"s_add_u32 m0, s75, 2048\n\t"
"s_add_u32 s27, s72, 221184\n\t"
"buffer_load_dwordx4 v106, s[52:55], s27 offen lds\n\t"
"ds_read_b64_tr_b16 a[200:201], v248 offset:2304\n\t"
"ds_read_b64_tr_b16 a[202:203], v248 offset:2816\n\t"
"ds_read_b64_tr_b16 a[204:205], v249 offset:2304\n\t"
"ds_read_b64_tr_b16 a[206:207], v249 offset:2816\n\t"
"s_add_u32 m0, s75, 3072\n\t"
"s_add_u32 s27, s72, 233472\n\t"
"buffer_load_dwordx4 v104, s[52:55], s27 offen lds\n\t"
"s_waitcnt lgkmcnt(14)\n\t"
"v_mfma_f32_16x16x32_bf16 v[0:3], a[176:179], a[220:223], 0\n\t"
"s_waitcnt lgkmcnt(12)\n\t"
"v_mfma_f32_16x16x32_bf16 v[4:7], a[180:183], a[220:223], 0\n\t"
"s_waitcnt lgkmcnt(10)\n\t"
"v_mfma_f32_16x16x32_bf16 v[8:11], a[184:187], a[220:223], 0\n\t"
"s_waitcnt lgkmcnt(8)\n\t"
"v_mfma_f32_16x16x32_bf16 v[12:15], a[188:191], a[220:223], 0\n\t"
"s_add_u32 m0, s75, 4096\n\t"
"s_add_u32 s27, s72, 233472\n\t"
"buffer_load_dwordx4 v105, s[52:55], s27 offen lds\n\t"
"s_add_u32 m0, s75, 5120\n\t"
"s_add_u32 s27, s72, 233472\n\t"
"buffer_load_dwordx4 v106, s[52:55], s27 offen lds\n\t"
"s_waitcnt lgkmcnt(6)\n\t"
"v_mfma_f32_16x16x32_bf16 v[16:19], a[192:195], a[220:223], 0\n\t"
"s_waitcnt lgkmcnt(4)\n\t"
"v_mfma_f32_16x16x32_bf16 v[20:23], a[196:199], a[220:223], 0\n\t"
"s_waitcnt lgkmcnt(2)\n\t"
"v_mfma_f32_16x16x32_bf16 v[24:27], a[200:203], a[220:223], 0\n\t"
"s_waitcnt lgkmcnt(0)\n\t"
"v_mfma_f32_16x16x32_bf16 v[28:31], a[204:207], a[220:223], 0\n\t"
"s_add_u32 m0, s26, 124544\n\t"
"s_nop 0\n\t"
"buffer_load_dwordx4 v241, s[12:15], s22 offen offset:128 nt lds\n\t"
"buffer_load_dwordx4 v242, s[12:15], s22 offen offset:1152 nt lds\n\t"
"v_cvt_pk_bf16_f32 v0, v0, v1\n\t"
"v_cvt_pk_bf16_f32 v1, v2, v3\n\t"
"v_cvt_pk_bf16_f32 v2, v4, v5\n\t"
"v_cvt_pk_bf16_f32 v3, v6, v7\n\t"
"ds_write_b128 v217, v[0:3] offset:0\n\t"
"v_cvt_pk_bf16_f32 v8, v8, v9\n\t"
"v_cvt_pk_bf16_f32 v9, v10, v11\n\t"
"v_cvt_pk_bf16_f32 v10, v12, v13\n\t"
"v_cvt_pk_bf16_f32 v11, v14, v15\n\t"
"ds_write_b128 v219, v[8:11] offset:0\n\t"
"v_cvt_pk_bf16_f32 v16, v16, v17\n\t"
"v_cvt_pk_bf16_f32 v17, v18, v19\n\t"
"v_cvt_pk_bf16_f32 v18, v20, v21\n\t"
"v_cvt_pk_bf16_f32 v19, v22, v23\n\t"
"ds_write_b128 v217, v[16:19] offset:8192\n\t"
"v_cvt_pk_bf16_f32 v24, v24, v25\n\t"
"v_cvt_pk_bf16_f32 v25, v26, v27\n\t"
"v_cvt_pk_bf16_f32 v26, v28, v29\n\t"
"v_cvt_pk_bf16_f32 v27, v30, v31\n\t"
"ds_write_b128 v219, v[24:27] offset:8192\n\t"
"v_add_u32 v248, 80896, v227\n\t"
"v_xor_b32 v249, 32, v248\n\t"
"v_xor_b32 v250, 64, v248\n\t"
"v_xor_b32 v251, 96, v248\n\t"
"ds_read_b64_tr_b16 a[176:177], v248 offset:0\n\t"
"ds_read_b64_tr_b16 a[178:179], v248 offset:512\n\t"
"ds_read_b64_tr_b16 a[180:181], v249 offset:0\n\t"
"ds_read_b64_tr_b16 a[182:183], v249 offset:512\n\t"
"buffer_load_dwordx4 v243, s[12:15], s22 offen offset:2176 nt lds\n\t"
"ds_read_b64_tr_b16 a[184:185], v250 offset:0\n\t"
"ds_read_b64_tr_b16 a[186:187], v250 offset:512\n\t"
"ds_read_b64_tr_b16 a[188:189], v251 offset:0\n\t"
"ds_read_b64_tr_b16 a[190:191], v251 offset:512\n\t"
"buffer_load_dwordx4 v244, s[12:15], s22 offen offset:3200 nt lds\n\t"
"ds_read_b64_tr_b16 a[192:193], v248 offset:1152\n\t"
"ds_read_b64_tr_b16 a[194:195], v248 offset:1664\n\t"
"ds_read_b64_tr_b16 a[196:197], v249 offset:1152\n\t"
"ds_read_b64_tr_b16 a[198:199], v249 offset:1664\n\t"
"s_mov_b64 exec, s[36:37]\n\t"
"buffer_load_dwordx4 v245, s[12:15], s22 offen offset:3712 nt lds\n\t"
"s_mov_b64 exec, -1\n\t"
"ds_read_b64_tr_b16 a[200:201], v250 offset:1152\n\t"
"ds_read_b64_tr_b16 a[202:203], v250 offset:1664\n\t"
"ds_read_b64_tr_b16 a[204:205], v251 offset:1152\n\t"
"ds_read_b64_tr_b16 a[206:207], v251 offset:1664\n\t"
"s_add_u32 m0, s43, 122240\n\t"
"s_mov_b64 exec, s[44:45]\n\t"
"buffer_load_dwordx4 v246, s[12:15], 0 offen offset:128 nt lds\n\t"
"s_mov_b64 exec, -1\n\t"
"s_waitcnt vmcnt(19)\n\t"
"ds_read_b64_tr_b16 a[48:49], v107 offset:0\n\t"
"ds_read_b64_tr_b16 a[50:51], v107 offset:1536\n\t"
"ds_read_b64_tr_b16 a[52:53], v107 offset:3072\n\t"
"ds_read_b64_tr_b16 a[54:55], v107 offset:4608\n\t"
"ds_read_b64_tr_b16 a[56:57], v107 offset:8\n\t"
"ds_read_b64_tr_b16 a[58:59], v107 offset:1544\n\t"
"ds_read_b64_tr_b16 a[60:61], v107 offset:3080\n\t"
"ds_read_b64_tr_b16 a[62:63], v107 offset:4616\n\t"
"ds_read_b64_tr_b16 a[64:65], v107 offset:16\n\t"
"ds_read_b64_tr_b16 a[66:67], v107 offset:1552\n\t"
"ds_read_b64_tr_b16 a[68:69], v107 offset:3088\n\t"
"ds_read_b64_tr_b16 a[70:71], v107 offset:4624\n\t"
"s_waitcnt vmcnt(13)\n\t"
"ds_read_b64_tr_b16 a[72:73], v107 offset:6144\n\t"
"ds_read_b64_tr_b16 a[74:75], v107 offset:7680\n\t"
"ds_read_b64_tr_b16 a[76:77], v107 offset:9216\n\t"
"ds_read_b64_tr_b16 a[78:79], v107 offset:10752\n\t"
"ds_read_b64_tr_b16 a[80:81], v107 offset:6152\n\t"
"ds_read_b64_tr_b16 a[82:83], v107 offset:7688\n\t"
"ds_read_b64_tr_b16 a[84:85], v107 offset:9224\n\t"
"ds_read_b64_tr_b16 a[86:87], v107 offset:10760\n\t"
"ds_read_b64_tr_b16 a[88:89], v107 offset:6160\n\t"
"ds_read_b64_tr_b16 a[90:91], v107 offset:7696\n\t"
"ds_read_b64_tr_b16 a[92:93], v107 offset:9232\n\t"
"ds_read_b64_tr_b16 a[94:95], v107 offset:10768\n\t"
"s_waitcnt lgkmcnt(15)\n\t"
"v_mfma_f32_16x16x32_bf16 v[0:3], v[110:113], a[48:51], 0\n\t"
"s_waitcnt lgkmcnt(10)\n\t"
"v_mfma_f32_16x16x32_bf16 v[0:3], v[110:113], a[72:75], v[0:3]\n\t"
"v_mfma_f32_16x16x32_bf16 v[8:11], v[108:111], a[48:51], 0\n\t"
"v_mfma_f32_16x16x32_bf16 v[8:11], v[108:111], a[72:75], v[8:11]\n\t"
"s_add_u32 m0, s26, 34816\n\t"
"s_nop 0\n\t"
"buffer_load_dwordx4 v241, s[12:15], s22 offen offset:256 nt lds\n\t"
"v_mfma_f32_16x16x32_bf16 v[16:19], v[110:113], a[52:55], 0\n\t"
"s_waitcnt lgkmcnt(8)\n\t"
"v_mfma_f32_16x16x32_bf16 v[16:19], v[110:113], a[76:79], v[16:19]\n\t"
"v_mfma_f32_16x16x32_bf16 v[24:27], v[108:111], a[52:55], 0\n\t"
"v_mfma_f32_16x16x32_bf16 v[24:27], v[108:111], a[76:79], v[24:27]\n\t"
"buffer_load_dwordx4 v242, s[12:15], s22 offen offset:1280 nt lds\n\t"
"s_waitcnt vmcnt(8)\n\t"
"ds_read_b64_tr_b16 a[0:1], v47 offset:0\n\t"
"ds_read_b64_tr_b16 a[2:3], v47 offset:1536\n\t"
"ds_read_b64_tr_b16 a[4:5], v47 offset:3072\n\t"
"ds_read_b64_tr_b16 a[6:7], v47 offset:4608\n\t"
"ds_read_b64_tr_b16 a[8:9], v47 offset:8\n\t"
"ds_read_b64_tr_b16 a[10:11], v47 offset:1544\n\t"
"ds_read_b64_tr_b16 a[12:13], v47 offset:3080\n\t"
"ds_read_b64_tr_b16 a[14:15], v47 offset:4616\n\t"
"ds_read_b64_tr_b16 a[16:17], v47 offset:16\n\t"
"ds_read_b64_tr_b16 a[18:19], v47 offset:1552\n\t"
"ds_read_b64_tr_b16 a[20:21], v47 offset:3088\n\t"
"ds_read_b64_tr_b16 a[22:23], v47 offset:4624\n\t"
"v_mfma_f32_16x16x32_bf16 v[32:35], v[110:113], a[56:59], 0\n\t"
"s_waitcnt lgkmcnt(15)\n\t"
"v_mfma_f32_16x16x32_bf16 v[32:35], v[110:113], a[80:83], v[32:35]\n\t"
"v_mfma_f32_16x16x32_bf16 v[96:99], v[108:111], a[56:59], 0\n\t"
"v_mfma_f32_16x16x32_bf16 v[96:99], v[108:111], a[80:83], v[96:99]\n\t"
"buffer_load_dwordx4 v243, s[12:15], s22 offen offset:2304 nt lds\n\t"
"v_mfma_f32_16x16x32_bf16 v[120:123], v[110:113], a[60:63], 0\n\t"
"v_mfma_f32_16x16x32_bf16 v[120:123], v[110:113], a[84:87], v[120:123]\n\t"
"v_mfma_f32_16x16x32_bf16 v[128:131], v[108:111], a[60:63], 0\n\t"
"v_mfma_f32_16x16x32_bf16 v[128:131], v[108:111], a[84:87], v[128:131]\n\t"
"buffer_load_dwordx4 v244, s[12:15], s22 offen offset:3328 nt lds\n\t"
"v_mfma_f32_16x16x32_bf16 v[136:139], v[110:113], a[64:67], 0\n\t"
"s_waitcnt lgkmcnt(14)\n\t"
"v_mfma_f32_16x16x32_bf16 v[136:139], v[110:113], a[88:91], v[136:139]\n\t"
"v_mfma_f32_16x16x32_bf16 v[152:155], v[108:111], a[64:67], 0\n\t"
"v_mfma_f32_16x16x32_bf16 v[152:155], v[108:111], a[88:91], v[152:155]\n\t"
"s_mov_b64 exec, s[36:37]\n\t"
"buffer_load_dwordx4 v245, s[12:15], s22 offen offset:3840 nt lds\n\t"
"s_mov_b64 exec, -1\n\t"
"v_mfma_f32_16x16x32_bf16 v[160:163], v[110:113], a[68:71], 0\n\t"
"s_waitcnt lgkmcnt(12)\n\t"
"v_mfma_f32_16x16x32_bf16 v[160:163], v[110:113], a[92:95], v[160:163]\n\t"
"v_mfma_f32_16x16x32_bf16 v[168:171], v[108:111], a[68:71], 0\n\t"
"v_mfma_f32_16x16x32_bf16 v[168:171], v[108:111], a[92:95], v[168:171]\n\t"
"s_add_u32 m0, s43, 32512\n\t"
"s_mov_b64 exec, s[44:45]\n\t"
"buffer_load_dwordx4 v246, s[12:15], 0 offen offset:256 nt lds\n\t"
"s_mov_b64 exec, -1\n\t"
"s_waitcnt lgkmcnt(10)\n\t"
"v_mfma_f32_16x16x32_bf16 v[4:7], v[110:113], a[0:3], v[0:3]\n\t"
"v_mfma_f32_16x16x32_bf16 v[0:3], v[114:117], a[0:3], v[0:3]\n\t"
"v_mfma_f32_16x16x32_bf16 v[12:15], v[108:111], a[0:3], v[8:11]\n\t"
"v_mfma_f32_16x16x32_bf16 v[8:11], v[112:115], a[0:3], v[8:11]\n\t"
"s_waitcnt lgkmcnt(8)\n\t"
"v_mfma_f32_16x16x32_bf16 v[20:23], v[110:113], a[4:7], v[16:19]\n\t"
"v_mfma_f32_16x16x32_bf16 v[16:19], v[114:117], a[4:7], v[16:19]\n\t"
"v_mfma_f32_16x16x32_bf16 v[28:31], v[108:111], a[4:7], v[24:27]\n\t"
"s_nop 0\n\t"
"v_cvt_pk_bf16_f32 v48, v4, v5\n\t"
"v_cvt_pk_bf16_f32 v49, v6, v7\n\t"
"v_mfma_f32_16x16x32_bf16 v[24:27], v[112:115], a[4:7], v[24:27]\n\t"
"v_cvt_pk_bf16_f32 v72, v0, v1\n\t"
"v_cvt_pk_bf16_f32 v73, v2, v3\n\t"
"s_waitcnt lgkmcnt(6)\n\t"
"v_mfma_f32_16x16x32_bf16 v[36:39], v[110:113], a[8:11], v[32:35]\n\t"
"v_cvt_pk_bf16_f32 v50, v12, v13\n\t"
"v_cvt_pk_bf16_f32 v51, v14, v15\n\t"
"v_mfma_f32_16x16x32_bf16 v[32:35], v[114:117], a[8:11], v[32:35]\n\t"
"v_cvt_pk_bf16_f32 v74, v8, v9\n\t"
"v_cvt_pk_bf16_f32 v75, v10, v11\n\t"
"v_mfma_f32_16x16x32_bf16 v[100:103], v[108:111], a[8:11], v[96:99]\n\t"
"v_cvt_pk_bf16_f32 v52, v20, v21\n\t"
"v_cvt_pk_bf16_f32 v53, v22, v23\n\t"
"v_mfma_f32_16x16x32_bf16 v[96:99], v[112:115], a[8:11], v[96:99]\n\t"
"v_cvt_pk_bf16_f32 v76, v16, v17\n\t"
"v_cvt_pk_bf16_f32 v77, v18, v19\n\t"
"s_waitcnt lgkmcnt(4)\n\t"
"v_mfma_f32_16x16x32_bf16 v[124:127], v[110:113], a[12:15], v[120:123]\n\t"
"v_cvt_pk_bf16_f32 v54, v28, v29\n\t"
"v_cvt_pk_bf16_f32 v55, v30, v31\n\t"
"v_mfma_f32_16x16x32_bf16 v[120:123], v[114:117], a[12:15], v[120:123]\n\t"
"v_cvt_pk_bf16_f32 v78, v24, v25\n\t"
"v_cvt_pk_bf16_f32 v79, v26, v27\n\t"
"v_mfma_f32_16x16x32_bf16 v[132:135], v[108:111], a[12:15], v[128:131]\n\t"
"v_cvt_pk_bf16_f32 v56, v36, v37\n\t"
"v_cvt_pk_bf16_f32 v57, v38, v39\n\t"
"v_mfma_f32_16x16x32_bf16 v[128:131], v[112:115], a[12:15], v[128:131]\n\t"
"v_cvt_pk_bf16_f32 v80, v32, v33\n\t"
"v_cvt_pk_bf16_f32 v81, v34, v35\n\t"
"s_waitcnt lgkmcnt(2)\n\t"
"v_mfma_f32_16x16x32_bf16 v[140:143], v[110:113], a[16:19], v[136:139]\n\t"
"v_cvt_pk_bf16_f32 v58, v100, v101\n\t"
"v_cvt_pk_bf16_f32 v59, v102, v103\n\t"
"v_mfma_f32_16x16x32_bf16 v[136:139], v[114:117], a[16:19], v[136:139]\n\t"
"v_cvt_pk_bf16_f32 v82, v96, v97\n\t"
"v_cvt_pk_bf16_f32 v83, v98, v99\n\t"
"v_mfma_f32_16x16x32_bf16 v[156:159], v[108:111], a[16:19], v[152:155]\n\t"
"v_cvt_pk_bf16_f32 v60, v124, v125\n\t"
"v_cvt_pk_bf16_f32 v61, v126, v127\n\t"
"v_mfma_f32_16x16x32_bf16 v[152:155], v[112:115], a[16:19], v[152:155]\n\t"
"v_cvt_pk_bf16_f32 v84, v120, v121\n\t"
"v_cvt_pk_bf16_f32 v85, v122, v123\n\t"
"s_waitcnt lgkmcnt(0)\n\t"
"v_mfma_f32_16x16x32_bf16 v[164:167], v[110:113], a[20:23], v[160:163]\n\t"
"v_cvt_pk_bf16_f32 v62, v132, v133\n\t"
"v_cvt_pk_bf16_f32 v63, v134, v135\n\t"
"v_mfma_f32_16x16x32_bf16 v[160:163], v[114:117], a[20:23], v[160:163]\n\t"
"v_cvt_pk_bf16_f32 v86, v128, v129\n\t"
"v_cvt_pk_bf16_f32 v87, v130, v131\n\t"
"v_mfma_f32_16x16x32_bf16 v[172:175], v[108:111], a[20:23], v[168:171]\n\t"
"v_cvt_pk_bf16_f32 v64, v140, v141\n\t"
"v_cvt_pk_bf16_f32 v65, v142, v143\n\t"
"v_mfma_f32_16x16x32_bf16 v[168:171], v[112:115], a[20:23], v[168:171]\n\t"
"v_cvt_pk_bf16_f32 v88, v136, v137\n\t"
"v_cvt_pk_bf16_f32 v89, v138, v139\n\t"
"v_cvt_pk_bf16_f32 v66, v156, v157\n\t"
"v_cvt_pk_bf16_f32 v67, v158, v159\n\t"
"v_cvt_pk_bf16_f32 v90, v152, v153\n\t"
"v_cvt_pk_bf16_f32 v91, v154, v155\n\t"
"v_cvt_pk_bf16_f32 v68, v164, v165\n\t"
"v_cvt_pk_bf16_f32 v69, v166, v167\n\t"
"v_cvt_pk_bf16_f32 v92, v160, v161\n\t"
"v_cvt_pk_bf16_f32 v93, v162, v163\n\t"
"v_cvt_pk_bf16_f32 v70, v172, v173\n\t"
"v_cvt_pk_bf16_f32 v71, v174, v175\n\t"
"v_cvt_pk_bf16_f32 v94, v168, v169\n\t"
"v_cvt_pk_bf16_f32 v95, v170, v171\n\t"
"s_barrier\n\t"
"v_mfma_f32_16x16x32_bf16 v[192:195], a[176:179], v[252:255], 0\n\t"
"ds_read_b128 a[144:147], v216 offset:0\n\t"
"v_mfma_f32_16x16x32_bf16 v[196:199], a[180:183], v[252:255], 0\n\t"
"ds_read_b128 a[160:163], v216 offset:8192\n\t"
"ds_read_b128 a[148:151], v216 offset:1024\n\t"
"v_mfma_f32_16x16x32_bf16 v[200:203], a[184:187], v[252:255], 0\n\t"
"ds_read_b128 a[164:167], v216 offset:9216\n\t"
"ds_read_b128 a[152:155], v216 offset:2048\n\t"
"v_add_u32 v218, 4096, v235\n\t"
"s_mov_b32 s16, s80\n\t"
"v_mfma_f32_16x16x32_bf16 v[204:207], a[188:191], v[252:255], 0\n\t"
"ds_read_b128 a[168:171], v216 offset:10240\n\t"
"ds_read_b128 a[156:159], v216 offset:3072\n\t"
"v_mfma_f32_16x16x32_bf16 v[208:211], a[192:195], v[148:151], 0\n\t"
"ds_read_b128 a[172:175], v216 offset:11264\n\t"
"v_cvt_pk_bf16_f32 v192, v192, v193\n\t"
"v_cvt_pk_bf16_f32 v193, v194, v195\n\t"
"s_and_b32 s17, s81, 0xffff\n\t"
"v_mfma_f32_16x16x32_bf16 v[212:215], a[196:199], v[148:151], 0\n\t"
"v_cvt_pk_bf16_f32 v194, v196, v197\n\t"
"v_cvt_pk_bf16_f32 v195, v198, v199\n\t"
"s_waitcnt lgkmcnt(6)\n\t"
"v_mfma_f32_16x16x32_bf16 v[96:99], v[48:51], a[144:147], 0\n\t"
"v_cvt_pk_bf16_f32 v200, v200, v201\n\t"
"v_cvt_pk_bf16_f32 v201, v202, v203\n\t"
"v_mfma_f32_16x16x32_bf16 v[96:99], v[52:55], a[160:163], v[96:99]\n\t"
"v_cvt_pk_bf16_f32 v202, v204, v205\n\t"
"v_cvt_pk_bf16_f32 v203, v206, v207\n\t"
"ds_write_b128 v221, v[192:195] offset:16384\n\t"
"v_mfma_f32_16x16x32_bf16 v[112:115], v[56:59], a[144:147], 0\n\t"
"v_cvt_pk_bf16_f32 v208, v208, v209\n\t"
"v_cvt_pk_bf16_f32 v209, v210, v211\n\t"
"v_mfma_f32_16x16x32_bf16 v[112:115], v[60:63], a[160:163], v[112:115]\n\t"
"v_cvt_pk_bf16_f32 v210, v212, v213\n\t"
"v_cvt_pk_bf16_f32 v211, v214, v215\n\t"
"ds_write_b128 v221, v[200:203] offset:24576\n\t"
"v_mfma_f32_16x16x32_bf16 v[128:131], v[64:67], a[144:147], 0\n\t"
"ds_read_b128 a[144:147], v216 offset:4096\n\t"
"s_mov_b32 s18, 100663296\n\t"
"v_mfma_f32_16x16x32_bf16 v[128:131], v[68:71], a[160:163], v[128:131]\n\t"
"buffer_load_dword v190, v189, s[8:11], 0 offen sc1\n\t"
"ds_read_b128 a[160:163], v216 offset:12288\n\t"
"s_mov_b32 s19, 0x20000\n\t"
"s_waitcnt lgkmcnt(8)\n\t"
"v_mfma_f32_16x16x32_bf16 v[132:135], v[64:67], a[148:151], 0\n\t"
"s_mov_b32 s21, 4\n\t"
"s_mov_b32 s38, -2.0\n\t"
"v_mfma_f32_16x16x32_bf16 v[132:135], v[68:71], a[164:167], v[132:135]\n\t"
"ds_write_b128 v221, v[208:211] offset:20480\n\t"
"s_mov_b32 s39, -2.0\n\t"
"v_mbcnt_lo_u32_b32 v176, -1, 0\n\t"
"v_mfma_f32_16x16x32_bf16 v[116:119], v[56:59], a[148:151], 0\n\t"
"v_mbcnt_hi_u32_b32 v176, -1, v176\n\t"
"v_and_b32 v153, 7, v176\n\t"
"v_mfma_f32_16x16x32_bf16 v[116:119], v[60:63], a[164:167], v[116:119]\n\t"
"v_and_b32 v158, 24, v176\n\t"
"v_cmp_eq_u32 vcc, 8, v158\n\t"
"v_mov_b32 v166, 0x3f80\n\t"
"v_mov_b32 v167, 0xbf80\n\t"
"s_waitcnt vmcnt(7)\n\t"
"s_barrier\n\t"
"v_mfma_f32_16x16x32_bf16 v[192:195], a[200:203], v[148:151], 0\n\t"
"ds_read_b64_tr_b16 a[176:177], v248 offset:41472\n\t"
"ds_read_b64_tr_b16 a[178:179], v248 offset:41984\n\t"
"v_cndmask_b32 v166, v166, v167, vcc\n\t"
"v_mov_b32 v168, v153\n\t"
"v_mfma_f32_16x16x32_bf16 v[100:103], v[48:51], a[148:151], 0\n\t"
"ds_read_b128 a[148:151], v216 offset:5120\n\t"
"v_mov_b32 v167, 0\n\t"
"v_mfma_f32_16x16x32_bf16 v[100:103], v[52:55], a[164:167], v[100:103]\n\t"
"ds_read_b128 a[164:167], v216 offset:13312\n\t"
"ds_read_b64_tr_b16 a[180:181], v249 offset:41472\n\t"
"v_and_b32 v169, 1, v168\n\t"
"v_lshlrev_b32 v169, 4, v169\n\t"
"s_waitcnt lgkmcnt(12)\n\t"
"v_mfma_f32_16x16x32_bf16 v[104:107], v[48:51], a[152:155], 0\n\t"
"ds_read_b64_tr_b16 a[182:183], v249 offset:41984\n\t"
"v_lshrrev_b32 v168, 1, v168\n\t"
"v_mfma_f32_16x16x32_bf16 v[104:107], v[52:55], a[168:171], v[104:107]\n\t"
"v_cvt_pk_bf16_f32 v192, v192, v193\n\t"
"v_cvt_pk_bf16_f32 v193, v194, v195\n\t"
"ds_read_b64_tr_b16 a[184:185], v250 offset:41472\n\t"
"ds_read_b64_tr_b16 a[186:187], v250 offset:41984\n\t"
"v_mfma_f32_16x16x32_bf16 v[196:199], a[204:207], v[148:151], 0\n\t"
"ds_read_b64_tr_b16 a[188:189], v251 offset:41472\n\t"
"ds_read_b64_tr_b16 a[190:191], v251 offset:41984\n\t"
"v_lshlrev_b64 v[166:167], v169, v[166:167]\n\t"
"v_cmp_eq_u32 vcc, 0, v168\n\t"
"v_mfma_f32_16x16x32_bf16 v[120:123], v[56:59], a[152:155], 0\n\t"
"ds_read_b64_tr_b16 a[192:193], v248 offset:42624\n\t"
"v_cndmask_b32 v172, 0, v166, vcc\n\t"
"v_mfma_f32_16x16x32_bf16 v[120:123], v[60:63], a[168:171], v[120:123]\n\t"
"ds_read_b64_tr_b16 a[194:195], v248 offset:43136\n\t"
"ds_read_b64_tr_b16 a[196:197], v249 offset:42624\n\t"
"v_accvgpr_write_b32 a212, v172\n\t"
"v_cndmask_b32 v173, 0, v167, vcc\n\t"
"v_mfma_f32_16x16x32_bf16 v[136:139], v[64:67], a[152:155], 0\n\t"
"ds_read_b64_tr_b16 a[198:199], v249 offset:43136\n\t"
"v_cmp_eq_u32 vcc, 1, v168\n\t"
"v_mfma_f32_16x16x32_bf16 v[136:139], v[68:71], a[168:171], v[136:139]\n\t"
"v_cvt_pk_bf16_f32 v194, v196, v197\n\t"
"v_cvt_pk_bf16_f32 v195, v198, v199\n\t"
"ds_read_b64_tr_b16 a[200:201], v250 offset:42624\n\t"
"ds_read_b64_tr_b16 a[202:203], v250 offset:43136\n\t"
"s_waitcnt lgkmcnt(15)\n\t"
"v_mfma_f32_16x16x32_bf16 v[140:143], v[64:67], a[156:159], 0\n\t"
"ds_read_b64_tr_b16 a[204:205], v251 offset:42624\n\t"
"v_cndmask_b32 v173, v173, v166, vcc\n\t"
"v_mfma_f32_16x16x32_bf16 v[140:143], v[68:71], a[172:175], v[140:143]\n\t"
"ds_write_b128 v221, v[192:195] offset:28672\n\t"
"ds_read_b64_tr_b16 a[206:207], v251 offset:43136\n\t"
"v_accvgpr_write_b32 a213, v173\n\t"
"v_mfma_f32_16x16x32_bf16 v[124:127], v[56:59], a[156:159], 0\n\t"
"ds_read_b128 a[152:155], v216 offset:6144\n\t"
"v_cndmask_b32 v174, 0, v167, vcc\n\t"
"v_mfma_f32_16x16x32_bf16 v[124:127], v[60:63], a[172:175], v[124:127]\n\t"
"ds_read_b128 a[168:171], v216 offset:14336\n\t"
"v_cmp_eq_u32 vcc, 2, v168\n\t"
"s_nop 1\n\t"
"v_cndmask_b32 v174, v174, v166, vcc\n\t"
"v_accvgpr_write_b32 a214, v174\n\t"
"v_mfma_f32_16x16x32_bf16 v[108:111], v[48:51], a[156:159], 0\n\t"
"ds_read_b128 a[156:159], v216 offset:7168\n\t"
"v_cndmask_b32 v175, 0, v167, vcc\n\t"
"v_mfma_f32_16x16x32_bf16 v[108:111], v[52:55], a[172:175], v[108:111]\n\t"
"buffer_load_dword v191, v189, s[8:11], 0 offen sc1\n\t"
"ds_read_b128 a[172:175], v216 offset:15360\n\t"
"v_cmp_eq_u32 vcc, 3, v168\n\t"
"v_mfma_f32_16x16x32_bf16 v[0:3], v[72:75], a[144:147], v[96:99]\n\t"
"s_nop 0\n\t"
"v_cndmask_b32 v175, v175, v166, vcc\n\t"
"v_accvgpr_write_b32 a215, v175\n\t"
"v_mfma_f32_16x16x32_bf16 v[0:3], v[76:79], a[160:163], v[0:3]\n\t"
"v_cmp_gt_u32 vcc, 32, v176\n\t"
"v_lshlrev_b32 v166, 8, v172\n\t"
"v_and_b32 v166, 0x80008000, v166\n\t"
"v_xor_b32 v166, v166, v172\n\t"
"v_mfma_f32_16x16x32_bf16 v[16:19], v[80:83], a[144:147], v[112:115]\n\t"
"v_cndmask_b32 v167, v166, v172, vcc\n\t"
"v_cndmask_b32 v166, v172, v166, vcc\n\t"
"v_mfma_f32_16x16x32_bf16 v[16:19], v[84:87], a[160:163], v[16:19]\n\t"
"v_accvgpr_write_b32 a208, v167\n\t"
"v_accvgpr_write_b32 a216, v166\n\t"
"v_lshlrev_b32 v166, 8, v173\n\t"
"v_and_b32 v166, 0x80008000, v166\n\t"
"v_mfma_f32_16x16x32_bf16 v[32:35], v[88:91], a[144:147], v[128:131]\n\t"
"v_xor_b32 v166, v166, v173\n\t"
"v_cndmask_b32 v167, v166, v173, vcc\n\t"
"v_mfma_f32_16x16x32_bf16 v[32:35], v[92:95], a[160:163], v[32:35]\n\t"
"v_cndmask_b32 v166, v173, v166, vcc\n\t"
"v_accvgpr_write_b32 a209, v167\n\t"
"v_accvgpr_write_b32 a217, v166\n\t"
"v_lshlrev_b32 v166, 8, v174\n\t"
"s_waitcnt lgkmcnt(15)\n\t"
"v_mfma_f32_16x16x32_bf16 v[36:39], v[88:91], a[148:151], v[132:135]\n\t"
"v_and_b32 v166, 0x80008000, v166\n\t"
"v_xor_b32 v166, v166, v174\n\t"
"v_mfma_f32_16x16x32_bf16 v[36:39], v[92:95], a[164:167], v[36:39]\n\t"
"v_cndmask_b32 v167, v166, v174, vcc\n\t"
"v_cndmask_b32 v166, v174, v166, vcc\n\t"
"v_accvgpr_write_b32 a210, v167\n\t"
"v_accvgpr_write_b32 a218, v166\n\t"
"v_mfma_f32_16x16x32_bf16 v[20:23], v[80:83], a[148:151], v[116:119]\n\t"
"v_lshlrev_b32 v166, 8, v175\n\t"
"v_and_b32 v166, 0x80008000, v166\n\t"
"v_mfma_f32_16x16x32_bf16 v[20:23], v[84:87], a[164:167], v[20:23]\n\t"
"v_xor_b32 v166, v166, v175\n\t"
"v_cndmask_b32 v167, v166, v175, vcc\n\t"
"v_cndmask_b32 v166, v175, v166, vcc\n\t"
"v_accvgpr_write_b32 a211, v167\n\t"
"v_accvgpr_write_b32 a219, v166\n\t"
"v_mfma_f32_16x16x32_bf16 v[4:7], v[72:75], a[148:151], v[100:103]\n\t"
"v_mfma_f32_16x16x32_bf16 v[4:7], v[76:79], a[164:167], v[4:7]\n\t"
"s_waitcnt lgkmcnt(0)\n\t"
"s_waitcnt vmcnt(1)\n\t"
"v_cmp_eq_u32 vcc, v190, v188\n\t"
"s_nop 4\n\t"
"s_cmp_eq_u64 vcc, 0\n\t"
"s_cbranch_scc1 Lgo_%=\n\t"
"s_sub_u32 s68, s68, 1\n\t"
"s_waitcnt vmcnt(0)\n\t"
"Lchk_%=:\n\t"
"v_cmp_eq_u32 vcc, v191, v188\n\t"
"s_nop 4\n\t"
"s_cmp_eq_u64 vcc, 0\n\t"
"s_cbranch_scc1 Lgo_%=\n\t"
"s_sub_u32 s68, s68, 1\n\t"
"s_cmp_eq_u32 s68, 0\n\t"
"s_cbranch_scc1 Lfb_%=\n\t"
"buffer_load_dword v191, v189, s[8:11], 0 offen sc1\n\t"
"s_waitcnt vmcnt(0)\n\t"
"s_branch Lchk_%=\n\t"
"Lgo_%=:\n\t"
"s_barrier\n\t"
"v_mfma_f32_16x16x32_bf16 v[8:11], v[72:75], a[152:155], v[104:107]\n\t"
"ds_read_b128 a[144:147], v216 offset:16384\n\t"
"v_add_u32 v248, 32768, v227\n\t"
"v_mfma_f32_16x16x32_bf16 v[8:11], v[76:79], a[168:171], v[8:11]\n\t"
"ds_read_b128 a[160:163], v216 offset:24576\n\t"
"ds_read_b128 a[148:151], v216 offset:17408\n\t"
"v_mfma_f32_16x16x32_bf16 v[24:27], v[80:83], a[152:155], v[120:123]\n\t"
"ds_read_b128 a[164:167], v216 offset:25600\n\t"
"v_xor_b32 v249, 32, v248\n\t"
"v_mfma_f32_16x16x32_bf16 v[24:27], v[84:87], a[168:171], v[24:27]\n\t"
"s_mov_b32 s54, 98304\n\t"
"buffer_load_dwordx4 a[96:99], v235, s[8:11], s54 offen offset:0 sc1\n\t"
"buffer_load_dwordx4 a[100:103], v235, s[8:11], s54 offen offset:1024 sc1\n\t"
"v_mfma_f32_16x16x32_bf16 v[40:43], v[88:91], a[152:155], v[136:139]\n\t"
"v_xor_b32 v250, 64, v248\n\t"
"v_xor_b32 v251, 96, v248\n\t"
"v_mfma_f32_16x16x32_bf16 v[40:43], v[92:95], a[168:171], v[40:43]\n\t"
"buffer_load_dwordx4 a[104:107], v235, s[8:11], s54 offen offset:2048 sc1\n\t"
"buffer_load_dwordx4 a[108:111], v235, s[8:11], s54 offen offset:3072 sc1\n\t"
"v_mfma_f32_16x16x32_bf16 v[200:203], a[176:179], v[252:255], 0\n\t"
"buffer_load_dwordx4 a[112:115], v218, s[8:11], s54 offen offset:0 sc1\n\t"
"buffer_load_dwordx4 a[116:119], v218, s[8:11], s54 offen offset:1024 sc1\n\t"
"v_mfma_f32_16x16x32_bf16 v[44:47], v[88:91], a[156:159], v[140:143]\n\t"
"ds_read_b128 a[152:155], v216 offset:18432\n\t"
"v_mbcnt_lo_u32_b32 v176, -1, 0\n\t"
"v_mfma_f32_16x16x32_bf16 v[44:47], v[92:95], a[172:175], v[44:47]\n\t"
"s_mov_b32 s66, 172032\n\t"
"buffer_load_dwordx4 a[120:123], v235, s[8:11], s66 offen offset:0 sc1\n\t"
"buffer_load_dwordx4 a[124:127], v235, s[8:11], s66 offen offset:1024 sc1\n\t"
"v_mfma_f32_16x16x32_bf16 v[204:207], a[180:183], v[252:255], 0\n\t"
"buffer_load_dwordx4 a[128:131], v235, s[8:11], s66 offen offset:2048 sc1\n\t"
"buffer_load_dwordx4 a[132:135], v235, s[8:11], s66 offen offset:3072 sc1\n\t"
"v_mfma_f32_16x16x32_bf16 v[28:31], v[80:83], a[156:159], v[124:127]\n\t"
"ds_read_b128 a[168:171], v216 offset:26624\n\t"
"v_cvt_pk_bf16_f32 v200, v200, v201\n\t"
"v_mfma_f32_16x16x32_bf16 v[28:31], v[84:87], a[172:175], v[28:31]\n\t"
"buffer_load_dwordx4 a[136:139], v218, s[8:11], s66 offen offset:0 sc1\n\t"
"buffer_load_dwordx4 a[140:143], v218, s[8:11], s66 offen offset:1024 sc1\n\t"
"v_mfma_f32_16x16x32_bf16 v[12:15], v[72:75], a[156:159], v[108:111]\n\t"
"v_cvt_pk_bf16_f32 v201, v202, v203\n\t"
"ds_read_b128 a[156:159], v216 offset:19456\n\t"
"v_mfma_f32_16x16x32_bf16 v[12:15], v[76:79], a[172:175], v[12:15]\n\t"
"s_mov_b32 s69, 196608\n\t"
"buffer_load_dwordx4 a[0:3], v235, s[8:11], s69 offen offset:0 sc1\n\t"
"v_cvt_pk_bf16_f32 v202, v204, v205\n\t"
"v_cvt_pk_bf16_f32 v203, v206, v207\n\t"
"v_mfma_f32_16x16x32_bf16 v[208:211], a[184:187], v[252:255], 0\n\t"
"v_pk_fma_f32 v[96:97], v[96:97], s[38:39], v[0:1] neg_lo:[1,0,1] neg_hi:[1,0,1]\n\t"
"v_pk_fma_f32 v[98:99], v[98:99], s[38:39], v[2:3] neg_lo:[1,0,1] neg_hi:[1,0,1]\n\t"
"v_pk_fma_f32 v[112:113], v[112:113], s[38:39], v[16:17] neg_lo:[1,0,1] neg_hi:[1,0,1]\n\t"
"v_pk_fma_f32 v[114:115], v[114:115], s[38:39], v[18:19] neg_lo:[1,0,1] neg_hi:[1,0,1]\n\t"
"v_pk_fma_f32 v[128:129], v[128:129], s[38:39], v[32:33] neg_lo:[1,0,1] neg_hi:[1,0,1]\n\t"
"v_pk_fma_f32 v[130:131], v[130:131], s[38:39], v[34:35] neg_lo:[1,0,1] neg_hi:[1,0,1]\n\t"
"v_pk_fma_f32 v[132:133], v[132:133], s[38:39], v[36:37] neg_lo:[1,0,1] neg_hi:[1,0,1]\n\t"
"v_pk_fma_f32 v[134:135], v[134:135], s[38:39], v[38:39] neg_lo:[1,0,1] neg_hi:[1,0,1]\n\t"
"v_pk_fma_f32 v[116:117], v[116:117], s[38:39], v[20:21] neg_lo:[1,0,1] neg_hi:[1,0,1]\n\t"
"v_pk_fma_f32 v[118:119], v[118:119], s[38:39], v[22:23] neg_lo:[1,0,1] neg_hi:[1,0,1]\n\t"
"v_pk_fma_f32 v[100:101], v[100:101], s[38:39], v[4:5] neg_lo:[1,0,1] neg_hi:[1,0,1]\n\t"
"v_pk_fma_f32 v[102:103], v[102:103], s[38:39], v[6:7] neg_lo:[1,0,1] neg_hi:[1,0,1]\n\t"
"v_pk_fma_f32 v[104:105], v[104:105], s[38:39], v[8:9] neg_lo:[1,0,1] neg_hi:[1,0,1]\n\t"
"v_pk_fma_f32 v[106:107], v[106:107], s[38:39], v[10:11] neg_lo:[1,0,1] neg_hi:[1,0,1]\n\t"
"v_pk_fma_f32 v[120:121], v[120:121], s[38:39], v[24:25] neg_lo:[1,0,1] neg_hi:[1,0,1]\n\t"
"v_pk_fma_f32 v[122:123], v[122:123], s[38:39], v[26:27] neg_lo:[1,0,1] neg_hi:[1,0,1]\n\t"
"v_pk_fma_f32 v[136:137], v[136:137], s[38:39], v[40:41] neg_lo:[1,0,1] neg_hi:[1,0,1]\n\t"
"v_pk_fma_f32 v[138:139], v[138:139], s[38:39], v[42:43] neg_lo:[1,0,1] neg_hi:[1,0,1]\n\t"
"v_pk_fma_f32 v[140:141], v[140:141], s[38:39], v[44:45] neg_lo:[1,0,1] neg_hi:[1,0,1]\n\t"
"v_pk_fma_f32 v[142:143], v[142:143], s[38:39], v[46:47] neg_lo:[1,0,1] neg_hi:[1,0,1]\n\t"
"v_pk_fma_f32 v[124:125], v[124:125], s[38:39], v[28:29] neg_lo:[1,0,1] neg_hi:[1,0,1]\n\t"
"v_pk_fma_f32 v[126:127], v[126:127], s[38:39], v[30:31] neg_lo:[1,0,1] neg_hi:[1,0,1]\n\t"
"v_pk_fma_f32 v[108:109], v[108:109], s[38:39], v[12:13] neg_lo:[1,0,1] neg_hi:[1,0,1]\n\t"
"v_pk_fma_f32 v[110:111], v[110:111], s[38:39], v[14:15] neg_lo:[1,0,1] neg_hi:[1,0,1]\n\t"
"ds_read_b128 a[172:175], v216 offset:27648\n\t"
"ds_write_b128 v221, v[200:203] offset:0\n\t"
"v_mbcnt_hi_u32_b32 v176, -1, v176\n\t"
"s_waitcnt lgkmcnt(7)\n\t"
"v_mfma_f32_16x16x32_bf16 v[0:3], a[48:51], a[144:147], v[0:3]\n\t"
"v_and_b32 v154, 15, v176\n\t"
"v_lshrrev_b32 v155, 4, v176\n\t"
"v_mfma_f32_16x16x32_bf16 v[0:3], a[52:55], a[160:163], v[0:3]\n\t"
"buffer_load_dwordx4 a[4:7], v235, s[8:11], s69 offen offset:1024 sc1\n\t"
"v_bfe_u32 v156, v176, 2, 2\n\t"
"v_and_b32 v157, 3, v176\n\t"
"v_mfma_f32_16x16x32_bf16 v[212:215], a[188:191], v[252:255], 0\n\t"
"v_lshlrev_b32 v157, 3, v157\n\t"
"s_add_u32 s60, s43, 32768\n\t"
"s_add_u32 s61, s26, 20800\n\t"
"s_mul_i32 s62, s73, 96\n\t"
"v_mfma_f32_16x16x32_bf16 v[16:19], a[56:59], a[144:147], v[16:19]\n\t"
"v_cvt_pk_bf16_f32 v208, v208, v209\n\t"
"v_cvt_pk_bf16_f32 v209, v210, v211\n\t"
"v_mfma_f32_16x16x32_bf16 v[16:19], a[60:63], a[160:163], v[16:19]\n\t"
"buffer_load_dwordx4 a[8:11], v235, s[8:11], s69 offen offset:2048 sc1\n\t"
"s_add_u32 s62, s62, 54272\n\t"
"v_min_u32 v158, 35, v176\n\t"
"v_mfma_f32_16x16x32_bf16 v[32:35], a[64:67], a[144:147], v[32:35]\n\t"
"ds_read_b128 a[144:147], v216 offset:20480\n\t"
"v_lshl_add_u32 v222, v158, 4, s60\n\t"
"v_mfma_f32_16x16x32_bf16 v[32:35], a[68:71], a[160:163], v[32:35]\n\t"
"v_cvt_pk_bf16_f32 v210, v212, v213\n\t"
"v_cvt_pk_bf16_f32 v211, v214, v215\n\t"
"ds_read_b128 a[160:163], v216 offset:28672\n\t"
"v_add_u32 v223, 66560, v222\n\t"
"v_mfma_f32_16x16x32_bf16 v[192:195], a[192:195], v[252:255], 0\n\t"
"ds_write_b128 v221, v[208:211] offset:8192\n\t"
"v_and_b32 v158, 1, v155\n\t"
"v_add_u32 v159, v156, v158\n\t"
"s_waitcnt lgkmcnt(8)\n\t"
"v_mfma_f32_16x16x32_bf16 v[36:39], a[64:67], a[148:151], v[36:39]\n\t"
"v_lshl_add_u32 v166, v159, 7, v157\n\t"
"v_and_b32 v159, 2, v159\n\t"
"v_mfma_f32_16x16x32_bf16 v[36:39], a[68:71], a[164:167], v[36:39]\n\t"
"buffer_load_dwordx4 a[12:15], v235, s[8:11], s69 offen offset:3072 sc1\n\t"
"v_lshl_add_u32 v166, v159, 4, v166\n\t"
"v_xad_u32 v167, v158, 1, v156\n\t"
"v_mfma_f32_16x16x32_bf16 v[20:23], a[56:59], a[148:151], v[20:23]\n\t"
"v_lshl_add_u32 v168, v167, 7, v157\n\t"
"v_and_b32 v167, 2, v167\n\t"
"v_mfma_f32_16x16x32_bf16 v[20:23], a[60:63], a[164:167], v[20:23]\n\t"
"buffer_load_dwordx4 a[16:19], v218, s[8:11], s69 offen offset:0 sc1\n\t"
"v_cvt_pk_bf16_f32 v192, v192, v193\n\t"
"v_cvt_pk_bf16_f32 v193, v194, v195\n\t"
"v_mfma_f32_16x16x32_bf16 v[196:199], a[196:199], v[252:255], 0\n\t"
"v_lshl_add_u32 v168, v167, 4, v168\n\t"
"v_sub_u32 v168, v168, v166\n\t"
"v_mul_u32_u24 v159, 20736, v158\n\t"
"v_sub_u32 v166, v166, v159\n\t"
"s_barrier\n\t"
"v_mfma_f32_16x16x32_bf16 v[4:7], a[48:51], a[148:151], v[4:7]\n\t"
"ds_read_b128 a[148:151], v216 offset:21504\n\t"
"v_lshrrev_b32 v159, 5, v176\n\t"
"v_mfma_f32_16x16x32_bf16 v[4:7], a[52:55], a[164:167], v[4:7]\n\t"
"ds_read_b128 a[164:167], v216 offset:29696\n\t"
"ds_read_b64_tr_b16 a[176:177], v248 offset:0\n\t"
"v_xor_b32 v158, v158, v159\n\t"
"v_lshlrev_b32 v158, 6, v158\n\t"
"v_mfma_f32_16x16x32_bf16 v[200:203], a[200:203], v[252:255], 0\n\t"
"buffer_load_dwordx4 a[20:23], v218, s[8:11], s69 offen offset:1024 sc1\n\t"
"ds_read_b64_tr_b16 a[178:179], v248 offset:512\n\t"
"ds_read_b64_tr_b16 a[180:181], v249 offset:0\n\t"
"s_waitcnt lgkmcnt(11)\n\t"
"v_mfma_f32_16x16x32_bf16 v[8:11], a[48:51], a[152:155], v[8:11]\n\t"
"v_cvt_pk_bf16_f32 v194, v196, v197\n\t"
"v_cvt_pk_bf16_f32 v195, v198, v199\n\t"
"v_mfma_f32_16x16x32_bf16 v[8:11], a[52:55], a[168:171], v[8:11]\n\t"
"ds_write_b128 v221, v[192:195] offset:4096\n\t"
"ds_read_b64_tr_b16 a[182:183], v249 offset:512\n\t"
"v_sub_u32 v166, v166, v158\n\t"
"v_mfma_f32_16x16x32_bf16 v[24:27], a[56:59], a[152:155], v[24:27]\n\t"
"ds_read_b64_tr_b16 a[184:185], v250 offset:0\n\t"
"v_lshl_add_u32 v168, v158, 1, v168\n\t"
"v_mfma_f32_16x16x32_bf16 v[24:27], a[60:63], a[168:171], v[24:27]\n\t"
"s_mov_b32 s81, 270336\n\t"
"buffer_load_dwordx4 a[24:27], v235, s[8:11], s81 offen offset:0 sc1\n\t"
"v_cvt_pk_bf16_f32 v200, v200, v201\n\t"
"v_cvt_pk_bf16_f32 v201, v202, v203\n\t"
"v_mfma_f32_16x16x32_bf16 v[204:207], a[204:207], v[252:255], 0\n\t"
"ds_read_b64_tr_b16 a[186:187], v250 offset:512\n\t"
"ds_read_b64_tr_b16 a[188:189], v251 offset:0\n\t"
"v_subrev_u32 v220, 64, v168\n\t"
"v_mul_u32_u24 v159, 1152, v159\n\t"
"v_mfma_f32_16x16x32_bf16 v[40:43], a[64:67], a[152:155], v[40:43]\n\t"
"ds_read_b64_tr_b16 a[190:191], v251 offset:512\n\t"
"v_add3_u32 v166, v166, v159, s61\n\t"
"v_mfma_f32_16x16x32_bf16 v[40:43], a[68:71], a[168:171], v[40:43]\n\t"
"buffer_load_dwordx4 a[28:31], v235, s[8:11], s81 offen offset:1024 sc1\n\t"
"ds_read_b64_tr_b16 a[192:193], v248 offset:1152\n\t"
"ds_read_b64_tr_b16 a[194:195], v248 offset:1664\n\t"
"s_waitcnt lgkmcnt(15)\n\t"
"v_mfma_f32_16x16x32_bf16 v[44:47], a[64:67], a[156:159], v[44:47]\n\t"
"ds_read_b64_tr_b16 a[196:197], v249 offset:1152\n\t"
"v_add_u32 v224, 1152, v166\n\t"
"v_mfma_f32_16x16x32_bf16 v[44:47], a[68:71], a[172:175], v[44:47]\n\t"
"ds_read_b64_tr_b16 a[198:199], v249 offset:1664\n\t"
"ds_read_b64_tr_b16 a[200:201], v250 offset:1152\n\t"
"v_cvt_pk_bf16_f32 v202, v204, v205\n\t"
"v_cvt_pk_bf16_f32 v203, v206, v207\n\t"
"v_mfma_f32_16x16x32_bf16 v[28:31], a[56:59], a[156:159], v[28:31]\n\t"
"ds_read_b64_tr_b16 a[202:203], v250 offset:1664\n\t"
"v_add_u32 v225, v159, v166\n\t"
"v_mfma_f32_16x16x32_bf16 v[28:31], a[60:63], a[172:175], v[28:31]\n\t"
"buffer_load_dwordx4 a[32:35], v235, s[8:11], s81 offen offset:2048 sc1\n\t"
"ds_write_b128 v221, v[200:203] offset:12288\n\t"
"v_mfma_f32_16x16x32_bf16 v[12:15], a[48:51], a[156:159], v[12:15]\n\t"
"ds_read_b64_tr_b16 a[204:205], v251 offset:1152\n\t"
"v_mul_u32_u24 v158, 400, v154\n\t"
"v_mfma_f32_16x16x32_bf16 v[12:15], a[52:55], a[172:175], v[12:15]\n\t"
"ds_read_b64_tr_b16 a[206:207], v251 offset:1664\n\t"
"ds_read_b128 a[152:155], v216 offset:22528\n\t"
"v_mul_u32_u24 v159, 24, v155\n\t"
"v_add3_u32 v228, v158, v159, s62\n\t"
"s_waitcnt lgkmcnt(15)\n\t"
"v_mfma_f32_16x16x32_bf16 v[96:99], a[72:75], a[144:147], v[96:99]\n\t"
"ds_read_b128 a[168:171], v216 offset:30720\n\t"
"s_mul_i32 s60, s73, 3200\n\t"
"v_mfma_f32_16x16x32_bf16 v[96:99], a[76:79], a[160:163], v[96:99]\n\t"
"buffer_load_dwordx4 a[36:39], v235, s[8:11], s81 offen offset:3072 sc1\n\t"
"ds_read_b128 a[156:159], v216 offset:23552\n\t"
"ds_read_b128 a[172:175], v216 offset:31744\n\t"
"v_mfma_f32_16x16x32_bf16 v[112:115], a[80:83], a[144:147], v[112:115]\n\t"
"s_add_u32 s60, s60, 54272\n\t"
"s_and_b32 s61, s73, 2\n\t"
"v_mfma_f32_16x16x32_bf16 v[112:115], a[84:87], a[160:163], v[112:115]\n\t"
"buffer_load_dwordx4 a[40:43], v218, s[8:11], s81 offen offset:0 sc1\n\t"
"s_lshl_b32 s61, s61, 7\n\t"
"s_add_u32 s60, s60, s61\n\t"
"v_mfma_f32_16x16x32_bf16 v[128:131], a[88:91], a[144:147], v[128:131]\n\t"
"s_mul_i32 s61, s73, 393216\n\t"
"v_add_u32 v158, 0, v176\n\t"
"v_mfma_f32_16x16x32_bf16 v[128:131], a[92:95], a[160:163], v[128:131]\n\t"
"v_mul_u32_u24 v159, 0xab, v158\n\t"
"v_lshrrev_b32 v159, 12, v159\n\t"
"v_add_lshl_u32 v166, v158, v159, 4\n\t"
"v_add_u32 v229, s60, v166\n\t"
"v_mfma_f32_16x16x32_bf16 v[132:135], a[88:91], a[148:151], v[132:135]\n\t"
"v_mad_u32_u24 v158, v159, 24, v158\n\t"
"v_lshl_add_u32 v232, v158, 4, s61\n\t"
"v_mfma_f32_16x16x32_bf16 v[132:135], a[92:95], a[164:167], v[132:135]\n\t"
"v_add_u32 v158, 64, v176\n\t"
"v_mul_u32_u24 v159, 0xab, v158\n\t"
"v_lshrrev_b32 v159, 12, v159\n\t"
"v_add_lshl_u32 v166, v158, v159, 4\n\t"
"v_mfma_f32_16x16x32_bf16 v[116:119], a[80:83], a[148:151], v[116:119]\n\t"
"v_add_u32 v230, s60, v166\n\t"
"v_mad_u32_u24 v158, v159, 24, v158\n\t"
"v_mfma_f32_16x16x32_bf16 v[116:119], a[84:87], a[164:167], v[116:119]\n\t"
"v_lshl_add_u32 v233, v158, 4, s61\n\t"
"v_add_u32 v158, 128, v176\n\t"
"v_mul_u32_u24 v159, 0xab, v158\n\t"
"v_lshrrev_b32 v159, 12, v159\n\t"
"v_add_lshl_u32 v166, v158, v159, 4\n\t"
"v_add_u32 v231, s60, v166\n\t"
"v_mad_u32_u24 v158, v159, 24, v158\n\t"
"v_lshl_add_u32 v234, v158, 4, s61\n\t"
"v_mfma_f32_16x16x32_bf16 v[100:103], a[72:75], a[148:151], v[100:103]\n\t"
"v_mfma_f32_16x16x32_bf16 v[100:103], a[76:79], a[164:167], v[100:103]\n\t"
"buffer_load_dwordx4 a[44:47], v218, s[8:11], s81 offen offset:1024 sc1\n\t"
"s_mov_b32 s57, 122880\n\t"
"buffer_load_dwordx4 a[48:51], v235, s[8:11], s57 offen offset:0 sc1\n\t"
"s_waitcnt lgkmcnt(0)\n\t"
"s_add_u32 s71, s71, 1\n\t"
"v_mov_b32 v215, s71\n\t"
"s_mov_b64 exec, 1\n\t"
"buffer_store_dword v215, off, s[8:11], s70 sc1\n\t"
"s_mov_b64 exec, -1\n\t"
"s_barrier\n\t"
"v_mfma_f32_16x16x32_bf16 v[104:107], a[72:75], a[152:155], v[104:107]\n\t"
"ds_read_b128 a[144:147], v216 offset:0\n\t"
"v_add_u32 v248, 122368, v226\n\t"
"v_mfma_f32_16x16x32_bf16 v[104:107], a[76:79], a[168:171], v[104:107]\n\t"
"ds_read_b128 a[160:163], v216 offset:8192\n\t"
"ds_read_b128 a[148:151], v216 offset:1024\n\t"
"v_mfma_f32_16x16x32_bf16 v[120:123], a[80:83], a[152:155], v[120:123]\n\t"
"ds_read_b128 a[164:167], v216 offset:9216\n\t"
"v_xor_b32 v249, 32, v248\n\t"
"v_mfma_f32_16x16x32_bf16 v[120:123], a[84:87], a[168:171], v[120:123]\n\t"
"buffer_load_dwordx4 a[52:55], v235, s[8:11], s57 offen offset:1024 sc1\n\t"
"v_mfma_f32_16x16x32_bf16 v[136:139], a[88:91], a[152:155], v[136:139]\n\t"
"v_xor_b32 v250, 64, v248\n\t"
"v_xor_b32 v251, 96, v248\n\t"
"v_mfma_f32_16x16x32_bf16 v[136:139], a[92:95], a[168:171], v[136:139]\n\t"
"buffer_load_dwordx4 a[56:59], v235, s[8:11], s57 offen offset:2048 sc1\n\t"
"s_mov_b32 s40, 8192\n\t"
"v_mfma_f32_16x16x32_bf16 v[208:211], a[176:179], v[252:255], 0\n\t"
"ds_read_b128 a[152:155], v216 offset:2048\n\t"
"v_accvgpr_read_b32 v172, a220\n\t"
"v_mfma_f32_16x16x32_bf16 v[140:143], a[88:91], a[156:159], v[140:143]\n\t"
"ds_read_b128 a[168:171], v216 offset:10240\n\t"
"v_and_b32 v172, 0xff7fff7f, v172\n\t"
"v_mfma_f32_16x16x32_bf16 v[140:143], a[92:95], a[172:175], v[140:143]\n\t"
"ds_read_b64_tr_b16 a[176:177], v248 offset:0\n\t"
"v_accvgpr_write_b32 a252, v172\n\t"
"v_mfma_f32_16x16x32_bf16 v[212:215], a[180:183], v[252:255], 0\n\t"
"buffer_load_dwordx4 a[60:63], v235, s[8:11], s57 offen offset:3072 sc1\n\t"
"v_mfma_f32_16x16x32_bf16 v[124:127], a[80:83], a[156:159], v[124:127]\n\t"
"v_cvt_pk_bf16_f32 v208, v208, v209\n\t"
"v_cvt_pk_bf16_f32 v209, v210, v211\n\t"
"v_mfma_f32_16x16x32_bf16 v[124:127], a[84:87], a[172:175], v[124:127]\n\t"
"ds_read_b64_tr_b16 a[178:179], v248 offset:512\n\t"
"s_mov_b32 s41, 16384\n\t"
"v_mfma_f32_16x16x32_bf16 v[108:111], a[72:75], a[156:159], v[108:111]\n\t"
"ds_read_b128 a[156:159], v216 offset:3072\n\t"
"v_accvgpr_read_b32 v173, a221\n\t"
"v_mfma_f32_16x16x32_bf16 v[108:111], a[76:79], a[172:175], v[108:111]\n\t"
"buffer_load_dwordx4 a[64:67], v218, s[8:11], s57 offen offset:0 sc1\n\t"
"v_mfma_f32_16x16x32_bf16 v[192:195], a[184:187], v[252:255], 0\n\t"
"v_cvt_pk_bf16_f32 v210, v212, v213\n\t"
"v_cvt_pk_bf16_f32 v211, v214, v215\n\t"
"s_waitcnt vmcnt(28) lgkmcnt(7)\n\t"
"v_mfma_f32_16x16x32_bf16 v[0:3], a[96:99], a[144:147], v[0:3]\n\t"
"ds_read_b128 a[172:175], v216 offset:11264\n\t"
"v_and_b32 v173, 0xff7fff7f, v173\n\t"
"v_mfma_f32_16x16x32_bf16 v[0:3], a[100:103], a[160:163], v[0:3]\n\t"
"buffer_load_dwordx4 a[68:71], v218, s[8:11], s57 offen offset:1024 sc1\n\t"
"s_mov_b32 s42, 24576\n\t"
"v_mfma_f32_16x16x32_bf16 v[196:199], a[188:191], v[252:255], 0\n\t"
"ds_read_b64_tr_b16 a[180:181], v249 offset:0\n\t"
"s_waitcnt vmcnt(27)\n\t"
"v_mfma_f32_16x16x32_bf16 v[16:19], a[104:107], a[144:147], v[16:19]\n\t"
"v_cvt_pk_bf16_f32 v192, v192, v193\n\t"
"v_cvt_pk_bf16_f32 v193, v194, v195\n\t"
"v_mfma_f32_16x16x32_bf16 v[16:19], a[108:111], a[160:163], v[16:19]\n\t"
"s_add_u32 m0, s43, 142976\n\t"
"s_mov_b64 exec, s[44:45]\n\t"
"buffer_load_dwordx4 v247, s[12:15], 0 offen offset:128 nt lds\n\t"
"s_mov_b64 exec, -1\n\t"
"s_waitcnt vmcnt(26)\n\t"
"v_mfma_f32_16x16x32_bf16 v[32:35], a[112:115], a[144:147], v[32:35]\n\t"
"ds_read_b128 a[144:147], v216 offset:4096\n\t"
"v_accvgpr_write_b32 a253, v173\n\t"
"v_mfma_f32_16x16x32_bf16 v[32:35], a[116:119], a[160:163], v[32:35]\n\t"
"ds_write_b128 v221, v[208:211] offset:16384\n\t"
"v_mfma_f32_16x16x32_bf16 v[200:203], a[192:195], v[252:255], 0\n\t"
"v_cvt_pk_bf16_f32 v194, v196, v197\n\t"
"v_cvt_pk_bf16_f32 v195, v198, v199\n\t"
"s_mov_b32 s46, 32768\n\t"
"s_waitcnt lgkmcnt(9)\n\t"
"v_mfma_f32_16x16x32_bf16 v[36:39], a[112:115], a[148:151], v[36:39]\n\t"
"ds_read_b128 a[160:163], v216 offset:12288\n\t"
"v_mfma_f32_16x16x32_bf16 v[36:39], a[116:119], a[164:167], v[36:39]\n\t"
"ds_write_b128 v221, v[192:195] offset:24576\n\t"
"v_mfma_f32_16x16x32_bf16 v[20:23], a[104:107], a[148:151], v[20:23]\n\t"
"ds_read_b64_tr_b16 a[182:183], v249 offset:512\n\t"
"v_accvgpr_read_b32 v174, a222\n\t"
"v_mfma_f32_16x16x32_bf16 v[20:23], a[108:111], a[164:167], v[20:23]\n\t"
"v_cvt_pk_bf16_f32 v200, v200, v201\n\t"
"v_cvt_pk_bf16_f32 v201, v202, v203\n\t"
"v_mfma_f32_16x16x32_bf16 v[204:207], a[196:199], v[252:255], 0\n\t"
"ds_read_b64_tr_b16 a[184:185], v250 offset:2304\n\t"
"v_and_b32 v174, 0xff7fff7f, v174\n\t"
"v_mfma_f32_16x16x32_bf16 v[4:7], a[96:99], a[148:151], v[4:7]\n\t"
"ds_read_b128 a[148:151], v216 offset:5120\n\t"
"v_accvgpr_write_b32 a254, v174\n\t"
"v_mfma_f32_16x16x32_bf16 v[4:7], a[100:103], a[164:167], v[4:7]\n\t"
"ds_read_b128 a[164:167], v216 offset:13312\n\t"
"s_mov_b32 s47, 40960\n\t"
"v_mfma_f32_16x16x32_bf16 v[208:211], a[200:203], v[252:255], 0\n\t"
"s_add_u32 m0, s26, 145280\n\t"
"s_nop 0\n\t"
"buffer_load_dwordx4 v236, s[12:15], s22 offen offset:128 nt lds\n\t"
"s_waitcnt lgkmcnt(13)\n\t"
"v_mfma_f32_16x16x32_bf16 v[8:11], a[96:99], a[152:155], v[8:11]\n\t"
"v_cvt_pk_bf16_f32 v202, v204, v205\n\t"
"v_cvt_pk_bf16_f32 v203, v206, v207\n\t"
"v_mfma_f32_16x16x32_bf16 v[8:11], a[100:103], a[168:171], v[8:11]\n\t"
"ds_write_b128 v221, v[200:203] offset:20480\n\t"
"v_mfma_f32_16x16x32_bf16 v[24:27], a[104:107], a[152:155], v[24:27]\n\t"
"ds_read_b64_tr_b16 a[186:187], v250 offset:2816\n\t"
"v_accvgpr_read_b32 v175, a223\n\t"
"v_mfma_f32_16x16x32_bf16 v[24:27], a[108:111], a[168:171], v[24:27]\n\t"
"v_cvt_pk_bf16_f32 v208, v208, v209\n\t"
"v_cvt_pk_bf16_f32 v209, v210, v211\n\t"
"s_mov_b32 s48, 49152\n\t"
"v_mfma_f32_16x16x32_bf16 v[212:215], a[204:207], v[252:255], 0\n\t"
"ds_read_b64_tr_b16 a[188:189], v251 offset:2304\n\t"
"v_mfma_f32_16x16x32_bf16 v[40:43], a[112:115], a[152:155], v[40:43]\n\t"
"ds_read_b64_tr_b16 a[190:191], v251 offset:2816\n\t"
"v_and_b32 v175, 0xff7fff7f, v175\n\t"
"v_mfma_f32_16x16x32_bf16 v[40:43], a[116:119], a[168:171], v[40:43]\n\t"
"ds_read_b64_tr_b16 a[192:193], v250 offset:0\n\t"
"v_accvgpr_write_b32 a255, v175\n\t"
"s_waitcnt lgkmcnt(14)\n\t"
"v_mfma_f32_16x16x32_bf16 v[44:47], a[112:115], a[156:159], v[44:47]\n\t"
"ds_read_b64_tr_b16 a[194:195], v250 offset:512\n\t"
"s_mov_b32 s90, 0.5\n\t"
"v_mfma_f32_16x16x32_bf16 v[44:47], a[116:119], a[172:175], v[44:47]\n\t"
"ds_read_b64_tr_b16 a[196:197], v251 offset:0\n\t"
"v_cvt_pk_bf16_f32 v210, v212, v213\n\t"
"v_mfma_f32_16x16x32_bf16 v[28:31], a[104:107], a[156:159], v[28:31]\n\t"
"ds_read_b64_tr_b16 a[198:199], v251 offset:512\n\t"
"v_cvt_pk_bf16_f32 v211, v214, v215\n\t"
"v_mfma_f32_16x16x32_bf16 v[28:31], a[108:111], a[172:175], v[28:31]\n\t"
"ds_read_b64_tr_b16 a[200:201], v248 offset:2304\n\t"
"s_mov_b32 s49, 57344\n\t"
"v_mfma_f32_16x16x32_bf16 v[12:15], a[96:99], a[156:159], v[12:15]\n\t"
"ds_read_b64_tr_b16 a[202:203], v248 offset:2816\n\t"
"s_mov_b32 s91, 0.5\n\t"
"v_mfma_f32_16x16x32_bf16 v[12:15], a[100:103], a[172:175], v[12:15]\n\t"
"ds_write_b128 v221, v[208:211] offset:28672\n\t"
"s_waitcnt vmcnt(25) lgkmcnt(15)\n\t"
"v_mfma_f32_16x16x32_bf16 v[96:99], a[120:123], a[144:147], v[96:99]\n\t"
"ds_read_b64_tr_b16 a[204:205], v249 offset:2304\n\t"
"s_add_u32 s84, s8, 18432\n\t"
"s_addc_u32 s85, s9, 0\n\t"
"v_mfma_f32_16x16x32_bf16 v[96:99], a[124:127], a[160:163], v[96:99]\n\t"
"ds_read_b64_tr_b16 a[206:207], v249 offset:2816\n\t"
"s_mov_b64 s[86:87], s[10:11]\n\t"
"s_waitcnt vmcnt(23)\n\t"
"v_mfma_f32_16x16x32_bf16 v[112:115], a[128:131], a[144:147], v[112:115]\n\t"
"ds_read_b128 a[152:155], v216 offset:6144\n\t"
"s_xor_b64 s[88:89], s[84:85], s[8:9]\n\t"
"v_mfma_f32_16x16x32_bf16 v[112:115], a[132:135], a[160:163], v[112:115]\n\t"
"ds_read_b128 a[168:171], v216 offset:14336\n\t"
"s_mov_b32 s50, 65536\n\t"
"s_waitcnt vmcnt(21)\n\t"
"v_mfma_f32_16x16x32_bf16 v[128:131], a[136:139], a[144:147], v[128:131]\n\t"
"ds_read_b128 a[156:159], v216 offset:7168\n\t"
"s_add_u32 s25, s24, 0xffeb0000\n\t"
"v_mfma_f32_16x16x32_bf16 v[128:131], a[140:143], a[160:163], v[128:131]\n\t"
"buffer_load_dwordx4 v237, s[12:15], s22 offen offset:1152 nt lds\n\t"
"s_waitcnt lgkmcnt(15)\n\t"
"v_mfma_f32_16x16x32_bf16 v[132:135], a[136:139], a[148:151], v[132:135]\n\t"
"ds_read_b128 a[172:175], v216 offset:15360\n\t"
"s_add_u32 s27, s24, 0xffe80000\n\t"
"v_mfma_f32_16x16x32_bf16 v[132:135], a[140:143], a[164:167], v[132:135]\n\t"
"s_add_u32 s20, s24, 0xffd30000\n\t"
"s_add_u32 s24, s24, 0xffd00000\n\t"
"v_mfma_f32_16x16x32_bf16 v[116:119], a[128:131], a[148:151], v[116:119]\n\t"
"v_mfma_f32_16x16x32_bf16 v[116:119], a[132:135], a[164:167], v[116:119]\n\t"
"v_mfma_f32_16x16x32_bf16 v[100:103], a[120:123], a[148:151], v[100:103]\n\t"
"v_mfma_f32_16x16x32_bf16 v[100:103], a[124:127], a[164:167], v[100:103]\n\t"
"s_waitcnt lgkmcnt(0)\n\t"
"s_barrier\n\t"
"v_mfma_f32_16x16x32_bf16 v[104:107], a[120:123], a[152:155], v[104:107]\n\t"
"ds_read_b128 a[144:147], v216 offset:16384\n\t"
"v_add_u32 v248, 32768, v226\n\t"
"v_mfma_f32_16x16x32_bf16 v[104:107], a[124:127], a[168:171], v[104:107]\n\t"
"ds_read_b128 a[160:163], v216 offset:24576\n\t"
"ds_read_b128 a[148:151], v216 offset:17408\n\t"
"v_mfma_f32_16x16x32_bf16 v[120:123], a[128:131], a[152:155], v[120:123]\n\t"
"ds_read_b128 a[164:167], v216 offset:25600\n\t"
"v_xor_b32 v249, 32, v248\n\t"
"v_mfma_f32_16x16x32_bf16 v[120:123], a[132:135], a[168:171], v[120:123]\n\t"
"v_xor_b32 v250, 64, v248\n\t"
"v_mfma_f32_16x16x32_bf16 v[136:139], a[136:139], a[152:155], v[136:139]\n\t"
"v_xor_b32 v251, 96, v248\n\t"
"v_mfma_f32_16x16x32_bf16 v[136:139], a[140:143], a[168:171], v[136:139]\n\t"
"ds_read_b128 a[152:155], v216 offset:18432\n\t"
"s_mov_b32 s51, 73728\n\t"
"v_mfma_f32_16x16x32_bf16 v[192:195], a[176:179], a[252:255], 0\n\t"
"buffer_load_dwordx4 v238, s[12:15], s22 offen offset:2176 nt lds\n\t"
"v_mfma_f32_16x16x32_bf16 v[140:143], a[136:139], a[156:159], v[140:143]\n\t"
"ds_read_b128 a[168:171], v216 offset:26624\n\t"
"v_mfma_f32_16x16x32_bf16 v[140:143], a[140:143], a[172:175], v[140:143]\n\t"
"ds_read_b64_tr_b16 a[176:177], v248 offset:0\n\t"
"v_mfma_f32_16x16x32_bf16 v[196:199], a[180:183], a[252:255], 0\n\t"
"ds_read_b64_tr_b16 a[178:179], v248 offset:512\n\t"
"v_mfma_f32_16x16x32_bf16 v[124:127], a[128:131], a[156:159], v[124:127]\n\t"
"v_cvt_pk_bf16_f32 v192, v192, v193\n\t"
"v_cvt_pk_bf16_f32 v193, v194, v195\n\t"
"v_mfma_f32_16x16x32_bf16 v[124:127], a[132:135], a[172:175], v[124:127]\n\t"
"ds_read_b64_tr_b16 a[180:181], v249 offset:0\n\t"
"s_mov_b32 s52, 81920\n\t"
"v_mfma_f32_16x16x32_bf16 v[108:111], a[120:123], a[156:159], v[108:111]\n\t"
"ds_read_b128 a[156:159], v216 offset:19456\n\t"
"v_mfma_f32_16x16x32_bf16 v[108:111], a[124:127], a[172:175], v[108:111]\n\t"
"v_cvt_pk_bf16_f32 v194, v196, v197\n\t"
"v_cvt_pk_bf16_f32 v195, v198, v199\n\t"
"v_mfma_f32_16x16x32_bf16 v[200:203], a[184:187], a[252:255], 0\n\t"
"ds_read_b128 a[172:175], v216 offset:27648\n\t"
"s_waitcnt vmcnt(21) lgkmcnt(9)\n\t"
"v_mfma_f32_16x16x32_bf16 v[0:3], a[0:3], a[144:147], v[0:3]\n\t"
"ds_read_b64_tr_b16 a[182:183], v249 offset:512\n\t"
"v_mfma_f32_16x16x32_bf16 v[0:3], a[4:7], a[160:163], v[0:3]\n\t"
"ds_write_b128 v217, v[192:195] offset:0\n\t"
"s_mov_b32 s53, 90112\n\t"
"v_mfma_f32_16x16x32_bf16 v[204:207], a[188:191], a[252:255], 0\n\t"
"ds_read_b64_tr_b16 a[184:185], v250 offset:2304\n\t"
"s_waitcnt vmcnt(19)\n\t"
"v_mfma_f32_16x16x32_bf16 v[16:19], a[8:11], a[144:147], v[16:19]\n\t"
"v_cvt_pk_bf16_f32 v200, v200, v201\n\t"
"v_cvt_pk_bf16_f32 v201, v202, v203\n\t"
"v_mfma_f32_16x16x32_bf16 v[16:19], a[12:15], a[160:163], v[16:19]\n\t"
"ds_read_b64_tr_b16 a[186:187], v250 offset:2816\n\t"
"s_waitcnt vmcnt(17)\n\t"
"v_mfma_f32_16x16x32_bf16 v[32:35], a[16:19], a[144:147], v[32:35]\n\t"
"ds_read_b128 a[144:147], v216 offset:20480\n\t"
"v_mfma_f32_16x16x32_bf16 v[32:35], a[20:23], a[160:163], v[32:35]\n\t"
"v_cvt_pk_bf16_f32 v202, v204, v205\n\t"
"v_cvt_pk_bf16_f32 v203, v206, v207\n\t"
"v_mfma_f32_16x16x32_bf16 v[208:211], a[192:195], a[252:255], 0\n\t"
"buffer_load_dwordx4 v239, s[12:15], s22 offen offset:3200 nt lds\n\t"
"s_mov_b32 s55, 106496\n\t"
"s_waitcnt lgkmcnt(12)\n\t"
"v_mfma_f32_16x16x32_bf16 v[36:39], a[16:19], a[148:151], v[36:39]\n\t"
"ds_read_b128 a[160:163], v216 offset:28672\n\t"
"v_mfma_f32_16x16x32_bf16 v[36:39], a[20:23], a[164:167], v[36:39]\n\t"
"ds_read_b64_tr_b16 a[188:189], v251 offset:2304\n\t"
"v_mfma_f32_16x16x32_bf16 v[20:23], a[8:11], a[148:151], v[20:23]\n\t"
"ds_read_b64_tr_b16 a[190:191], v251 offset:2816\n\t"
"v_mfma_f32_16x16x32_bf16 v[20:23], a[12:15], a[164:167], v[20:23]\n\t"
"ds_write_b128 v219, v[200:203] offset:0\n\t"
"v_mfma_f32_16x16x32_bf16 v[212:215], a[196:199], a[252:255], 0\n\t"
"v_cvt_pk_bf16_f32 v208, v208, v209\n\t"
"v_cvt_pk_bf16_f32 v209, v210, v211\n\t"
"v_mfma_f32_16x16x32_bf16 v[4:7], a[0:3], a[148:151], v[4:7]\n\t"
"ds_read_b128 a[148:151], v216 offset:21504\n\t"
"v_mfma_f32_16x16x32_bf16 v[4:7], a[4:7], a[164:167], v[4:7]\n\t"
"ds_read_b128 a[164:167], v216 offset:29696\n\t"
"s_mov_b32 s56, 114688\n\t"
"v_mfma_f32_16x16x32_bf16 v[192:195], a[200:203], a[252:255], 0\n\t"
"ds_read_b64_tr_b16 a[192:193], v250 offset:0\n\t"
"s_waitcnt lgkmcnt(15)\n\t"
"v_mfma_f32_16x16x32_bf16 v[8:11], a[0:3], a[152:155], v[8:11]\n\t"
"v_cvt_pk_bf16_f32 v210, v212, v213\n\t"
"v_cvt_pk_bf16_f32 v211, v214, v215\n\t"
"v_mfma_f32_16x16x32_bf16 v[8:11], a[4:7], a[168:171], v[8:11]\n\t"
"ds_read_b64_tr_b16 a[194:195], v250 offset:512\n\t"
"v_mfma_f32_16x16x32_bf16 v[24:27], a[8:11], a[152:155], v[24:27]\n\t"
"ds_read_b64_tr_b16 a[196:197], v251 offset:0\n\t"
"v_mfma_f32_16x16x32_bf16 v[24:27], a[12:15], a[168:171], v[24:27]\n\t"
"v_cvt_pk_bf16_f32 v192, v192, v193\n\t"
"v_cvt_pk_bf16_f32 v193, v194, v195\n\t"
"s_mov_b32 s58, 131072\n\t"
"v_mfma_f32_16x16x32_bf16 v[196:199], a[204:207], a[252:255], 0\n\t"
"ds_read_b64_tr_b16 a[198:199], v251 offset:512\n\t"
"v_mfma_f32_16x16x32_bf16 v[40:43], a[16:19], a[152:155], v[40:43]\n\t"
"ds_read_b64_tr_b16 a[200:201], v248 offset:2304\n\t"
"v_mfma_f32_16x16x32_bf16 v[40:43], a[20:23], a[168:171], v[40:43]\n\t"
"ds_write_b128 v217, v[208:211] offset:8192\n\t"
"s_waitcnt lgkmcnt(15)\n\t"
"v_mfma_f32_16x16x32_bf16 v[44:47], a[16:19], a[156:159], v[44:47]\n\t"
"ds_read_b64_tr_b16 a[202:203], v248 offset:2816\n\t"
"v_mfma_f32_16x16x32_bf16 v[44:47], a[20:23], a[172:175], v[44:47]\n\t"
"s_mov_b64 exec, s[36:37]\n\t"
"buffer_load_dwordx4 v240, s[12:15], s22 offen offset:3712 nt lds\n\t"
"s_mov_b64 exec, -1\n\t"
"v_mfma_f32_16x16x32_bf16 v[28:31], a[8:11], a[156:159], v[28:31]\n\t"
"ds_read_b64_tr_b16 a[204:205], v249 offset:2304\n\t"
"v_mfma_f32_16x16x32_bf16 v[28:31], a[12:15], a[172:175], v[28:31]\n\t"
"ds_read_b64_tr_b16 a[206:207], v249 offset:2816\n\t"
"v_cvt_pk_bf16_f32 v194, v196, v197\n\t"
"s_mov_b32 s59, 139264\n\t"
"v_mfma_f32_16x16x32_bf16 v[12:15], a[0:3], a[156:159], v[12:15]\n\t"
"v_cvt_pk_bf16_f32 v195, v198, v199\n\t"
"v_mfma_f32_16x16x32_bf16 v[12:15], a[4:7], a[172:175], v[12:15]\n\t"
"ds_read_b128 a[152:155], v216 offset:22528\n\t"
"s_waitcnt vmcnt(17) lgkmcnt(15)\n\t"
"v_mfma_f32_16x16x32_bf16 v[96:99], a[24:27], a[144:147], v[96:99]\n\t"
"ds_read_b128 a[168:171], v216 offset:30720\n\t"
"v_mfma_f32_16x16x32_bf16 v[96:99], a[28:31], a[160:163], v[96:99]\n\t"
"ds_write_b128 v219, v[192:195] offset:8192\n\t"
"s_waitcnt vmcnt(15)\n\t"
"v_mfma_f32_16x16x32_bf16 v[112:115], a[32:35], a[144:147], v[112:115]\n\t"
"ds_read_b128 a[156:159], v216 offset:23552\n\t"
"v_mfma_f32_16x16x32_bf16 v[112:115], a[36:39], a[160:163], v[112:115]\n\t"
"ds_read_b128 a[172:175], v216 offset:31744\n\t"
"s_mov_b32 s63, 147456\n\t"
"s_waitcnt vmcnt(13)\n\t"
"v_mfma_f32_16x16x32_bf16 v[128:131], a[40:43], a[144:147], v[128:131]\n\t"
"v_mfma_f32_16x16x32_bf16 v[128:131], a[44:47], a[160:163], v[128:131]\n\t"
"buffer_load_dwordx4 a[72:75], v235, s[8:11], s63 offen offset:0 sc1\n\t"
"s_waitcnt lgkmcnt(14)\n\t"
"v_mfma_f32_16x16x32_bf16 v[132:135], a[40:43], a[148:151], v[132:135]\n\t"
"v_mfma_f32_16x16x32_bf16 v[132:135], a[44:47], a[164:167], v[132:135]\n\t"
"buffer_load_dwordx4 a[76:79], v235, s[8:11], s63 offen offset:1024 sc1\n\t"
"v_mfma_f32_16x16x32_bf16 v[116:119], a[32:35], a[148:151], v[116:119]\n\t"
"v_mfma_f32_16x16x32_bf16 v[116:119], a[36:39], a[164:167], v[116:119]\n\t"
"v_mfma_f32_16x16x32_bf16 v[100:103], a[24:27], a[148:151], v[100:103]\n\t"
"v_mfma_f32_16x16x32_bf16 v[100:103], a[28:31], a[164:167], v[100:103]\n\t"
"buffer_load_dwordx4 a[80:83], v235, s[8:11], s63 offen offset:2048 sc1\n\t"
"s_waitcnt lgkmcnt(0)\n\t"
"s_add_u32 s71, s71, 1\n\t"
"v_mov_b32 v215, s71\n\t"
"s_mov_b64 exec, 1\n\t"
"buffer_store_dword v215, off, s[8:11], s70 sc1\n\t"
"s_mov_b64 exec, -1\n\t"
"s_barrier\n\t"
"v_mfma_f32_16x16x32_bf16 v[104:107], a[24:27], a[152:155], v[104:107]\n\t"
"ds_read_b128 a[144:147], v216 offset:0\n\t"
"v_add_u32 v248, 80896, v224\n\t"
"v_mfma_f32_16x16x32_bf16 v[104:107], a[28:31], a[168:171], v[104:107]\n\t"
"ds_read_b128 a[160:163], v216 offset:8192\n\t"
"ds_read_b128 a[148:151], v216 offset:1024\n\t"
"v_mfma_f32_16x16x32_bf16 v[120:123], a[32:35], a[152:155], v[120:123]\n\t"
"ds_read_b128 a[164:167], v216 offset:9216\n\t"
"v_xor_b32 v249, 32, v248\n\t"
"v_mfma_f32_16x16x32_bf16 v[120:123], a[36:39], a[168:171], v[120:123]\n\t"
"buffer_load_dwordx4 a[84:87], v235, s[8:11], s63 offen offset:3072 sc1\n\t"
"v_mfma_f32_16x16x32_bf16 v[136:139], a[40:43], a[152:155], v[136:139]\n\t"
"v_xor_b32 v250, 64, v248\n\t"
"v_xor_b32 v251, 96, v248\n\t"
"v_mfma_f32_16x16x32_bf16 v[136:139], a[44:47], a[168:171], v[136:139]\n\t"
"buffer_load_dwordx4 a[88:91], v218, s[8:11], s63 offen offset:0 sc1\n\t"
"s_mov_b32 s64, 155648\n\t"
"v_mfma_f32_16x16x32_bf16 v[200:203], a[176:179], a[252:255], 0\n\t"
"buffer_load_dwordx4 a[92:95], v218, s[8:11], s63 offen offset:1024 sc1\n\t"
"v_mfma_f32_16x16x32_bf16 v[140:143], a[40:43], a[156:159], v[140:143]\n\t"
"ds_read_b128 a[152:155], v216 offset:2048\n\t"
"v_mfma_f32_16x16x32_bf16 v[140:143], a[44:47], a[172:175], v[140:143]\n\t"
"s_mov_b32 s75, 221184\n\t"
"buffer_load_dwordx4 a[96:99], v235, s[8:11], s75 offen offset:0 sc1\n\t"
"v_mfma_f32_16x16x32_bf16 v[204:207], a[180:183], a[252:255], 0\n\t"
"ds_read_b128 a[168:171], v216 offset:10240\n\t"
"v_mfma_f32_16x16x32_bf16 v[124:127], a[32:35], a[156:159], v[124:127]\n\t"
"v_cvt_pk_bf16_f32 v200, v200, v201\n\t"
"v_cvt_pk_bf16_f32 v201, v202, v203\n\t"
"v_mfma_f32_16x16x32_bf16 v[124:127], a[36:39], a[172:175], v[124:127]\n\t"
"buffer_load_dwordx4 a[100:103], v235, s[8:11], s75 offen offset:1024 sc1\n\t"
"s_mov_b32 s65, 163840\n\t"
"v_mfma_f32_16x16x32_bf16 v[108:111], a[24:27], a[156:159], v[108:111]\n\t"
"ds_read_b128 a[156:159], v216 offset:3072\n\t"
"v_mfma_f32_16x16x32_bf16 v[108:111], a[28:31], a[172:175], v[108:111]\n\t"
"v_cvt_pk_bf16_f32 v202, v204, v205\n\t"
"v_cvt_pk_bf16_f32 v203, v206, v207\n\t"
"v_mfma_f32_16x16x32_bf16 v[208:211], a[184:187], a[252:255], 0\n\t"
"v_pk_add_f32 v[96:97], v[0:1], v[96:97] neg_lo:[0,1] neg_hi:[0,1]\n\t"
"v_pk_add_f32 v[98:99], v[2:3], v[98:99] neg_lo:[0,1] neg_hi:[0,1]\n\t"
"v_pk_fma_f32 v[0:1], v[96:97], s[90:91], v[0:1] neg_lo:[1,0,0] neg_hi:[1,0,0]\n\t"
"v_pk_fma_f32 v[2:3], v[98:99], s[90:91], v[2:3] neg_lo:[1,0,0] neg_hi:[1,0,0]\n\t"
"v_pk_add_f32 v[112:113], v[16:17], v[112:113] neg_lo:[0,1] neg_hi:[0,1]\n\t"
"v_pk_add_f32 v[114:115], v[18:19], v[114:115] neg_lo:[0,1] neg_hi:[0,1]\n\t"
"v_pk_fma_f32 v[16:17], v[112:113], s[90:91], v[16:17] neg_lo:[1,0,0] neg_hi:[1,0,0]\n\t"
"v_pk_fma_f32 v[18:19], v[114:115], s[90:91], v[18:19] neg_lo:[1,0,0] neg_hi:[1,0,0]\n\t"
"v_pk_add_f32 v[128:129], v[32:33], v[128:129] neg_lo:[0,1] neg_hi:[0,1]\n\t"
"v_pk_add_f32 v[130:131], v[34:35], v[130:131] neg_lo:[0,1] neg_hi:[0,1]\n\t"
"v_pk_fma_f32 v[32:33], v[128:129], s[90:91], v[32:33] neg_lo:[1,0,0] neg_hi:[1,0,0]\n\t"
"v_pk_fma_f32 v[34:35], v[130:131], s[90:91], v[34:35] neg_lo:[1,0,0] neg_hi:[1,0,0]\n\t"
"v_pk_add_f32 v[132:133], v[36:37], v[132:133] neg_lo:[0,1] neg_hi:[0,1]\n\t"
"v_pk_add_f32 v[134:135], v[38:39], v[134:135] neg_lo:[0,1] neg_hi:[0,1]\n\t"
"v_pk_fma_f32 v[36:37], v[132:133], s[90:91], v[36:37] neg_lo:[1,0,0] neg_hi:[1,0,0]\n\t"
"v_pk_fma_f32 v[38:39], v[134:135], s[90:91], v[38:39] neg_lo:[1,0,0] neg_hi:[1,0,0]\n\t"
"v_pk_add_f32 v[116:117], v[20:21], v[116:117] neg_lo:[0,1] neg_hi:[0,1]\n\t"
"v_pk_add_f32 v[118:119], v[22:23], v[118:119] neg_lo:[0,1] neg_hi:[0,1]\n\t"
"v_pk_fma_f32 v[20:21], v[116:117], s[90:91], v[20:21] neg_lo:[1,0,0] neg_hi:[1,0,0]\n\t"
"v_pk_fma_f32 v[22:23], v[118:119], s[90:91], v[22:23] neg_lo:[1,0,0] neg_hi:[1,0,0]\n\t"
"v_pk_add_f32 v[100:101], v[4:5], v[100:101] neg_lo:[0,1] neg_hi:[0,1]\n\t"
"v_pk_add_f32 v[102:103], v[6:7], v[102:103] neg_lo:[0,1] neg_hi:[0,1]\n\t"
"v_pk_fma_f32 v[4:5], v[100:101], s[90:91], v[4:5] neg_lo:[1,0,0] neg_hi:[1,0,0]\n\t"
"v_pk_fma_f32 v[6:7], v[102:103], s[90:91], v[6:7] neg_lo:[1,0,0] neg_hi:[1,0,0]\n\t"
"v_pk_add_f32 v[104:105], v[8:9], v[104:105] neg_lo:[0,1] neg_hi:[0,1]\n\t"
"v_pk_add_f32 v[106:107], v[10:11], v[106:107] neg_lo:[0,1] neg_hi:[0,1]\n\t"
"v_pk_fma_f32 v[8:9], v[104:105], s[90:91], v[8:9] neg_lo:[1,0,0] neg_hi:[1,0,0]\n\t"
"v_pk_fma_f32 v[10:11], v[106:107], s[90:91], v[10:11] neg_lo:[1,0,0] neg_hi:[1,0,0]\n\t"
"v_pk_add_f32 v[120:121], v[24:25], v[120:121] neg_lo:[0,1] neg_hi:[0,1]\n\t"
"v_pk_add_f32 v[122:123], v[26:27], v[122:123] neg_lo:[0,1] neg_hi:[0,1]\n\t"
"v_pk_fma_f32 v[24:25], v[120:121], s[90:91], v[24:25] neg_lo:[1,0,0] neg_hi:[1,0,0]\n\t"
"v_pk_fma_f32 v[26:27], v[122:123], s[90:91], v[26:27] neg_lo:[1,0,0] neg_hi:[1,0,0]\n\t"
"v_pk_add_f32 v[136:137], v[40:41], v[136:137] neg_lo:[0,1] neg_hi:[0,1]\n\t"
"v_pk_add_f32 v[138:139], v[42:43], v[138:139] neg_lo:[0,1] neg_hi:[0,1]\n\t"
"v_pk_fma_f32 v[40:41], v[136:137], s[90:91], v[40:41] neg_lo:[1,0,0] neg_hi:[1,0,0]\n\t"
"v_pk_fma_f32 v[42:43], v[138:139], s[90:91], v[42:43] neg_lo:[1,0,0] neg_hi:[1,0,0]\n\t"
"v_pk_add_f32 v[140:141], v[44:45], v[140:141] neg_lo:[0,1] neg_hi:[0,1]\n\t"
"v_pk_add_f32 v[142:143], v[46:47], v[142:143] neg_lo:[0,1] neg_hi:[0,1]\n\t"
"v_pk_fma_f32 v[44:45], v[140:141], s[90:91], v[44:45] neg_lo:[1,0,0] neg_hi:[1,0,0]\n\t"
"v_pk_fma_f32 v[46:47], v[142:143], s[90:91], v[46:47] neg_lo:[1,0,0] neg_hi:[1,0,0]\n\t"
"v_pk_add_f32 v[124:125], v[28:29], v[124:125] neg_lo:[0,1] neg_hi:[0,1]\n\t"
"v_pk_add_f32 v[126:127], v[30:31], v[126:127] neg_lo:[0,1] neg_hi:[0,1]\n\t"
"v_pk_fma_f32 v[28:29], v[124:125], s[90:91], v[28:29] neg_lo:[1,0,0] neg_hi:[1,0,0]\n\t"
"v_pk_fma_f32 v[30:31], v[126:127], s[90:91], v[30:31] neg_lo:[1,0,0] neg_hi:[1,0,0]\n\t"
"v_pk_add_f32 v[108:109], v[12:13], v[108:109] neg_lo:[0,1] neg_hi:[0,1]\n\t"
"v_pk_add_f32 v[110:111], v[14:15], v[110:111] neg_lo:[0,1] neg_hi:[0,1]\n\t"
"v_pk_fma_f32 v[12:13], v[108:109], s[90:91], v[12:13] neg_lo:[1,0,0] neg_hi:[1,0,0]\n\t"
"v_pk_fma_f32 v[14:15], v[110:111], s[90:91], v[14:15] neg_lo:[1,0,0] neg_hi:[1,0,0]\n\t"
"ds_read_b128 a[172:175], v216 offset:11264\n\t"
"s_waitcnt vmcnt(19) lgkmcnt(6)\n\t"
"v_mfma_f32_16x16x32_bf16 v[0:3], a[48:51], a[144:147], v[0:3]\n\t"
"v_mfma_f32_16x16x32_bf16 v[0:3], a[52:55], a[160:163], v[0:3]\n\t"
"buffer_load_dwordx4 a[104:107], v235, s[8:11], s75 offen offset:2048 sc1\n\t"
"v_mfma_f32_16x16x32_bf16 v[212:215], a[188:191], a[252:255], 0\n\t"
"ds_write_b128 v217, v[200:203] offset:16384\n\t"
"s_mov_b32 s67, 180224\n\t"
"s_waitcnt vmcnt(18)\n\t"
"v_mfma_f32_16x16x32_bf16 v[16:19], a[56:59], a[144:147], v[16:19]\n\t"
"v_cvt_pk_bf16_f32 v208, v208, v209\n\t"
"v_cvt_pk_bf16_f32 v209, v210, v211\n\t"
"v_mfma_f32_16x16x32_bf16 v[16:19], a[60:63], a[160:163], v[16:19]\n\t"
"buffer_load_dwordx4 a[108:111], v235, s[8:11], s75 offen offset:3072 sc1\n\t"
"s_waitcnt vmcnt(17)\n\t"
"v_mfma_f32_16x16x32_bf16 v[32:35], a[64:67], a[144:147], v[32:35]\n\t"
"ds_read_b128 a[144:147], v216 offset:4096\n\t"
"v_mfma_f32_16x16x32_bf16 v[32:35], a[68:71], a[160:163], v[32:35]\n\t"
"v_cvt_pk_bf16_f32 v210, v212, v213\n\t"
"v_cvt_pk_bf16_f32 v211, v214, v215\n\t"
"v_mfma_f32_16x16x32_bf16 v[192:195], a[192:195], a[252:255], 0\n\t"
"buffer_load_dwordx4 a[112:115], v218, s[8:11], s75 offen offset:0 sc1\n\t"
"s_waitcnt lgkmcnt(6)\n\t"
"v_mfma_f32_16x16x32_bf16 v[36:39], a[64:67], a[148:151], v[36:39]\n\t"
"ds_read_b128 a[160:163], v216 offset:12288\n\t"
"v_mfma_f32_16x16x32_bf16 v[36:39], a[68:71], a[164:167], v[36:39]\n\t"
"ds_write_b128 v219, v[208:211] offset:16384\n\t"
"s_mov_b32 s68, 188416\n\t"
"v_mfma_f32_16x16x32_bf16 v[20:23], a[56:59], a[148:151], v[20:23]\n\t"
"v_mfma_f32_16x16x32_bf16 v[20:23], a[60:63], a[164:167], v[20:23]\n\t"
"buffer_load_dwordx4 a[116:119], v218, s[8:11], s75 offen offset:1024 sc1\n\t"
"v_mfma_f32_16x16x32_bf16 v[196:199], a[196:199], a[252:255], 0\n\t"
"v_cvt_pk_bf16_f32 v192, v192, v193\n\t"
"v_cvt_pk_bf16_f32 v193, v194, v195\n\t"
"s_waitcnt vmcnt(13)\n\t"
"s_barrier\n\t"
"v_mfma_f32_16x16x32_bf16 v[4:7], a[48:51], a[148:151], v[4:7]\n\t"
"ds_read_b128 a[148:151], v216 offset:5120\n\t"
"v_mfma_f32_16x16x32_bf16 v[4:7], a[52:55], a[164:167], v[4:7]\n\t"
"s_mov_b32 s78, 245760\n\t"
"buffer_load_dwordx4 a[120:123], v235, s[8:11], s78 offen offset:0 sc1\n\t"
"buffer_load_dwordx4 a[124:127], v235, s[8:11], s78 offen offset:1024 sc1\n\t"
"v_mfma_f32_16x16x32_bf16 v[200:203], a[200:203], a[252:255], 0\n\t"
"buffer_load_dwordx4 a[128:131], v235, s[8:11], s78 offen offset:2048 sc1\n\t"
"s_waitcnt lgkmcnt(7)\n\t"
"v_mfma_f32_16x16x32_bf16 v[8:11], a[48:51], a[152:155], v[8:11]\n\t"
"ds_read_b128 a[164:167], v216 offset:13312\n\t"
"v_cvt_pk_bf16_f32 v194, v196, v197\n\t"
"v_mfma_f32_16x16x32_bf16 v[8:11], a[52:55], a[168:171], v[8:11]\n\t"
"buffer_load_dwordx4 a[132:135], v235, s[8:11], s78 offen offset:3072 sc1\n\t"
"s_mov_b32 s72, 204800\n\t"
"v_mfma_f32_16x16x32_bf16 v[24:27], a[56:59], a[152:155], v[24:27]\n\t"
"v_cvt_pk_bf16_f32 v195, v198, v199\n\t"
"ds_read_b64_tr_b16 a[176:177], v248 offset:41472\n\t"
"v_mfma_f32_16x16x32_bf16 v[24:27], a[60:63], a[168:171], v[24:27]\n\t"
"buffer_load_dwordx4 a[136:139], v218, s[8:11], s78 offen offset:0 sc1\n\t"
"v_mfma_f32_16x16x32_bf16 v[204:207], a[204:207], a[252:255], 0\n\t"
"v_cvt_pk_bf16_f32 v200, v200, v201\n\t"
"v_cvt_pk_bf16_f32 v201, v202, v203\n\t"
"v_mfma_f32_16x16x32_bf16 v[40:43], a[64:67], a[152:155], v[40:43]\n\t"
"ds_read_b64_tr_b16 a[178:179], v248 offset:41984\n\t"
"v_mfma_f32_16x16x32_bf16 v[40:43], a[68:71], a[168:171], v[40:43]\n\t"
"buffer_load_dwordx4 a[140:143], v218, s[8:11], s78 offen offset:1024 sc1\n\t"
"s_waitcnt lgkmcnt(8)\n\t"
"v_mfma_f32_16x16x32_bf16 v[44:47], a[64:67], a[156:159], v[44:47]\n\t"
"ds_read_b64_tr_b16 a[180:181], v249 offset:41472\n\t"
"v_mfma_f32_16x16x32_bf16 v[44:47], a[68:71], a[172:175], v[44:47]\n\t"
"ds_write_b128 v217, v[192:195] offset:24576\n\t"
"s_mov_b32 s74, 212992\n\t"
"v_mfma_f32_16x16x32_bf16 v[28:31], a[56:59], a[156:159], v[28:31]\n\t"
"ds_read_b64_tr_b16 a[182:183], v249 offset:41984\n\t"
"v_cvt_pk_bf16_f32 v202, v204, v205\n\t"
"v_mfma_f32_16x16x32_bf16 v[28:31], a[60:63], a[172:175], v[28:31]\n\t"
"s_add_u32 m0, s43, 101632\n\t"
"s_mov_b64 exec, s[44:45]\n\t"
"buffer_load_dwordx4 v247, s[12:15], 0 offen offset:0 nt lds\n\t"
"s_mov_b64 exec, -1\n\t"
"v_mfma_f32_16x16x32_bf16 v[12:15], a[48:51], a[156:159], v[12:15]\n\t"
"ds_read_b64_tr_b16 a[184:185], v250 offset:43776\n\t"
"v_cvt_pk_bf16_f32 v203, v206, v207\n\t"
"v_mfma_f32_16x16x32_bf16 v[12:15], a[52:55], a[172:175], v[12:15]\n\t"
"ds_write_b128 v219, v[200:203] offset:24576\n\t"
"s_waitcnt vmcnt(18) lgkmcnt(10)\n\t"
"v_mfma_f32_16x16x32_bf16 v[96:99], a[72:75], a[144:147], v[96:99]\n\t"
"ds_read_b64_tr_b16 a[186:187], v250 offset:44288\n\t"
"v_mfma_f32_16x16x32_bf16 v[96:99], a[76:79], a[160:163], v[96:99]\n\t"
"ds_read_b64_tr_b16 a[188:189], v251 offset:43776\n\t"
"ds_read_b64_tr_b16 a[190:191], v251 offset:44288\n\t"
"ds_read_b64_tr_b16 a[192:193], v250 offset:41472\n\t"
"ds_read_b64_tr_b16 a[194:195], v250 offset:41984\n\t"
"ds_read_b64_tr_b16 a[196:197], v251 offset:41472\n\t"
"ds_read_b64_tr_b16 a[198:199], v251 offset:41984\n\t"
"ds_read_b64_tr_b16 a[200:201], v248 offset:43776\n\t"
"ds_read_b64_tr_b16 a[202:203], v248 offset:44288\n\t"
"ds_read_b64_tr_b16 a[204:205], v249 offset:43776\n\t"
"ds_read_b64_tr_b16 a[206:207], v249 offset:44288\n\t"
"s_waitcnt vmcnt(15)\n\t"
"v_mfma_f32_16x16x32_bf16 v[112:115], a[80:83], a[144:147], v[112:115]\n\t"
"v_mfma_f32_16x16x32_bf16 v[112:115], a[84:87], a[160:163], v[112:115]\n\t"
"s_waitcnt vmcnt(13)\n\t"
"v_mfma_f32_16x16x32_bf16 v[128:131], a[88:91], a[144:147], v[128:131]\n\t"
"v_mfma_f32_16x16x32_bf16 v[128:131], a[92:95], a[160:163], v[128:131]\n\t"
"ds_read_b128 a[152:155], v216 offset:6144\n\t"
"s_waitcnt lgkmcnt(15)\n\t"
"v_mfma_f32_16x16x32_bf16 v[132:135], a[88:91], a[148:151], v[132:135]\n\t"
"ds_read_b128 a[168:171], v216 offset:14336\n\t"
"v_mfma_f32_16x16x32_bf16 v[132:135], a[92:95], a[164:167], v[132:135]\n\t"
"ds_read_b128 a[156:159], v216 offset:7168\n\t"
"ds_read_b128 a[172:175], v216 offset:15360\n\t"
"v_mfma_f32_16x16x32_bf16 v[116:119], a[80:83], a[148:151], v[116:119]\n\t"
"v_mfma_f32_16x16x32_bf16 v[116:119], a[84:87], a[164:167], v[116:119]\n\t"
"s_add_u32 m0, s26, 103936\n\t"
"s_nop 0\n\t"
"buffer_load_dwordx4 v236, s[12:15], s22 offen offset:0 nt lds\n\t"
"v_mfma_f32_16x16x32_bf16 v[100:103], a[72:75], a[148:151], v[100:103]\n\t"
"v_mfma_f32_16x16x32_bf16 v[100:103], a[76:79], a[164:167], v[100:103]\n\t"
"s_waitcnt lgkmcnt(0)\n\t"
"s_barrier\n\t"
"v_mfma_f32_16x16x32_bf16 v[104:107], a[72:75], a[152:155], v[104:107]\n\t"
"ds_read_b128 a[144:147], v216 offset:16384\n\t"
"v_mfma_f32_16x16x32_bf16 v[104:107], a[76:79], a[168:171], v[104:107]\n\t"
"ds_read_b128 a[160:163], v216 offset:24576\n\t"
"ds_read_b128 a[148:151], v216 offset:17408\n\t"
"v_mfma_f32_16x16x32_bf16 v[120:123], a[80:83], a[152:155], v[120:123]\n\t"
"ds_read_b128 a[164:167], v216 offset:25600\n\t"
"v_mfma_f32_16x16x32_bf16 v[120:123], a[84:87], a[168:171], v[120:123]\n\t"
"v_mfma_f32_16x16x32_bf16 v[136:139], a[88:91], a[152:155], v[136:139]\n\t"
"ds_read_b128 a[152:155], v216 offset:18432\n\t"
"v_mfma_f32_16x16x32_bf16 v[136:139], a[92:95], a[168:171], v[136:139]\n\t"
"ds_read_b128 a[168:171], v216 offset:26624\n\t"
"s_mov_b32 s76, 229376\n\t"
"v_mfma_f32_16x16x32_bf16 v[208:211], a[176:179], a[212:215], 0\n\t"
"v_mfma_f32_16x16x32_bf16 v[140:143], a[88:91], a[156:159], v[140:143]\n\t"
"v_mfma_f32_16x16x32_bf16 v[140:143], a[92:95], a[172:175], v[140:143]\n\t"
"v_mfma_f32_16x16x32_bf16 v[212:215], a[180:183], a[212:215], 0\n\t"
"v_mfma_f32_16x16x32_bf16 v[124:127], a[80:83], a[156:159], v[124:127]\n\t"
"s_nop 3\n\t"
"v_cvt_pk_bf16_f32 v208, v208, v209\n\t"
"v_cvt_pk_bf16_f32 v209, v210, v211\n\t"
"v_mfma_f32_16x16x32_bf16 v[124:127], a[84:87], a[172:175], v[124:127]\n\t"
"buffer_load_dwordx4 a[248:251], v247, s[12:15], 0 offen offset:256 nt\n\t"
"buffer_load_dwordx4 v237, s[12:15], s22 offen offset:1024 nt lds\n\t"
"v_mfma_f32_16x16x32_bf16 v[108:111], a[72:75], a[156:159], v[108:111]\n\t"
"ds_read_b128 a[156:159], v216 offset:19456\n\t"
"v_mfma_f32_16x16x32_bf16 v[108:111], a[76:79], a[172:175], v[108:111]\n\t"
"buffer_load_dwordx4 v238, s[12:15], s22 offen offset:2048 nt lds\n\t"
"buffer_load_dwordx4 v239, s[12:15], s22 offen offset:3072 nt lds\n\t"
"s_mov_b32 s77, 237568\n\t"
"v_mfma_f32_16x16x32_bf16 v[192:195], a[184:187], a[212:215], 0\n\t"
"s_mov_b64 exec, s[36:37]\n\t"
"buffer_load_dwordx4 v240, s[12:15], s22 offen offset:3584 nt lds\n\t"
"s_mov_b64 exec, -1\n\t"
"v_cvt_pk_bf16_f32 v210, v212, v213\n\t"
"v_cvt_pk_bf16_f32 v211, v214, v215\n\t"
"s_waitcnt vmcnt(17) lgkmcnt(5)\n\t"
"v_mfma_f32_16x16x32_bf16 v[0:3], a[96:99], a[144:147], v[0:3]\n\t"
"ds_read_b128 a[172:175], v216 offset:27648\n\t"
"v_mfma_f32_16x16x32_bf16 v[0:3], a[100:103], a[160:163], v[0:3]\n\t"
"ds_write_b128 v217, v[208:211] offset:0\n\t"
"v_mfma_f32_16x16x32_bf16 v[196:199], a[188:191], a[212:215], 0\n\t"
"s_waitcnt vmcnt(15)\n\t"
"v_mfma_f32_16x16x32_bf16 v[16:19], a[104:107], a[144:147], v[16:19]\n\t"
"v_cvt_pk_bf16_f32 v192, v192, v193\n\t"
"v_cvt_pk_bf16_f32 v193, v194, v195\n\t"
"v_mfma_f32_16x16x32_bf16 v[16:19], a[108:111], a[160:163], v[16:19]\n\t"
"s_waitcnt vmcnt(13)\n\t"
"v_mfma_f32_16x16x32_bf16 v[32:35], a[112:115], a[144:147], v[32:35]\n\t"
"ds_read_b128 a[144:147], v216 offset:20480\n\t"
"v_mfma_f32_16x16x32_bf16 v[32:35], a[116:119], a[160:163], v[32:35]\n\t"
"v_cvt_pk_bf16_f32 v194, v196, v197\n\t"
"v_cvt_pk_bf16_f32 v195, v198, v199\n\t"
"ds_read_b128 a[160:163], v216 offset:28672\n\t"
"s_mov_b32 s79, 253952\n\t"
"v_mfma_f32_16x16x32_bf16 v[200:203], a[192:195], a[212:215], 0\n\t"
"ds_write_b128 v219, v[192:195] offset:0\n\t"
"s_waitcnt lgkmcnt(8)\n\t"
"v_mfma_f32_16x16x32_bf16 v[36:39], a[112:115], a[148:151], v[36:39]\n\t"
"v_mfma_f32_16x16x32_bf16 v[36:39], a[116:119], a[164:167], v[36:39]\n\t"
"buffer_load_dwordx4 a[0:3], v218, s[8:11], s58 offen offset:0 sc1\n\t"
"v_mfma_f32_16x16x32_bf16 v[20:23], a[104:107], a[148:151], v[20:23]\n\t"
"v_mfma_f32_16x16x32_bf16 v[20:23], a[108:111], a[164:167], v[20:23]\n\t"
"buffer_load_dwordx4 a[4:7], v218, s[8:11], s58 offen offset:1024 sc1\n\t"
"buffer_load_dwordx4 a[8:11], v218, s[8:11], s58 offen offset:2048 sc1\n\t"
"v_mfma_f32_16x16x32_bf16 v[204:207], a[196:199], a[212:215], 0\n\t"
"buffer_load_dwordx4 a[12:15], v218, s[8:11], s58 offen offset:3072 sc1\n\t"
"v_cvt_pk_bf16_f32 v200, v200, v201\n\t"
"v_cvt_pk_bf16_f32 v201, v202, v203\n\t"
"v_mfma_f32_16x16x32_bf16 v[4:7], a[96:99], a[148:151], v[4:7]\n\t"
"ds_read_b128 a[148:151], v216 offset:21504\n\t"
"v_mfma_f32_16x16x32_bf16 v[4:7], a[100:103], a[164:167], v[4:7]\n\t"
"buffer_load_dwordx4 a[16:19], v235, s[8:11], s59 offen offset:0 sc1\n\t"
"buffer_load_dwordx4 a[20:23], v235, s[8:11], s59 offen offset:1024 sc1\n\t"
"s_mov_b32 s80, 262144\n\t"
"v_mfma_f32_16x16x32_bf16 v[208:211], a[200:203], a[212:215], 0\n\t"
"buffer_load_dwordx4 a[24:27], v218, s[8:11], s57 offen offset:2048 sc1\n\t"
"ds_read_b128 a[164:167], v216 offset:29696\n\t"
"s_waitcnt lgkmcnt(8)\n\t"
"v_mfma_f32_16x16x32_bf16 v[8:11], a[96:99], a[152:155], v[8:11]\n\t"
"v_cvt_pk_bf16_f32 v202, v204, v205\n\t"
"v_cvt_pk_bf16_f32 v203, v206, v207\n\t"
"v_mfma_f32_16x16x32_bf16 v[8:11], a[100:103], a[168:171], v[8:11]\n\t"
"buffer_load_dwordx4 a[28:31], v218, s[8:11], s57 offen offset:3072 sc1\n\t"
"buffer_load_dwordx4 a[32:35], v235, s[8:11], s58 offen offset:0 sc1\n\t"
"v_mfma_f32_16x16x32_bf16 v[24:27], a[104:107], a[152:155], v[24:27]\n\t"
"v_mfma_f32_16x16x32_bf16 v[24:27], a[108:111], a[168:171], v[24:27]\n\t"
"buffer_load_dwordx4 a[36:39], v235, s[8:11], s58 offen offset:1024 sc1\n\t"
"buffer_load_dwordx4 a[40:43], v235, s[8:11], s58 offen offset:2048 sc1\n\t"
"v_mfma_f32_16x16x32_bf16 v[212:215], a[204:207], a[212:215], 0\n\t"
"buffer_load_dwordx4 a[44:47], v235, s[8:11], s58 offen offset:3072 sc1\n\t"
"v_cvt_pk_bf16_f32 v208, v208, v209\n\t"
"v_cvt_pk_bf16_f32 v209, v210, v211\n\t"
"v_mfma_f32_16x16x32_bf16 v[40:43], a[112:115], a[152:155], v[40:43]\n\t"
"ds_read_b128 a[152:155], v216 offset:22528\n\t"
"v_mfma_f32_16x16x32_bf16 v[40:43], a[116:119], a[168:171], v[40:43]\n\t"
"buffer_load_dwordx4 a[48:51], v218, s[8:11], s64 offen offset:0 sc1\n\t"
"buffer_load_dwordx4 a[52:55], v218, s[8:11], s64 offen offset:1024 sc1\n\t"
"s_mov_b32 s82, 278528\n\t"
"s_waitcnt lgkmcnt(7)\n\t"
"v_mfma_f32_16x16x32_bf16 v[44:47], a[112:115], a[156:159], v[44:47]\n\t"
"ds_read_b128 a[168:171], v216 offset:30720\n\t"
"v_mfma_f32_16x16x32_bf16 v[44:47], a[116:119], a[172:175], v[44:47]\n\t"
"buffer_load_dwordx4 a[56:59], v218, s[8:11], s64 offen offset:2048 sc1\n\t"
"ds_write_b128 v217, v[200:203] offset:8192\n\t"
"v_mfma_f32_16x16x32_bf16 v[28:31], a[104:107], a[156:159], v[28:31]\n\t"
"v_cvt_pk_bf16_f32 v210, v212, v213\n\t"
"v_cvt_pk_bf16_f32 v211, v214, v215\n\t"
"v_mfma_f32_16x16x32_bf16 v[28:31], a[108:111], a[172:175], v[28:31]\n\t"
"buffer_load_dwordx4 a[60:63], v218, s[8:11], s64 offen offset:3072 sc1\n\t"
"buffer_load_dwordx4 a[64:67], v235, s[8:11], s65 offen offset:0 sc1\n\t"
"v_mfma_f32_16x16x32_bf16 v[12:15], a[96:99], a[156:159], v[12:15]\n\t"
"ds_read_b128 a[156:159], v216 offset:23552\n\t"
"v_mfma_f32_16x16x32_bf16 v[12:15], a[100:103], a[172:175], v[12:15]\n\t"
"buffer_load_dwordx4 a[68:71], v235, s[8:11], s65 offen offset:1024 sc1\n\t"
"ds_write_b128 v219, v[208:211] offset:8192\n\t"
"s_waitcnt vmcnt(29) lgkmcnt(8)\n\t"
"v_mfma_f32_16x16x32_bf16 v[96:99], a[120:123], a[144:147], v[96:99]\n\t"
"ds_read_b128 a[172:175], v216 offset:31744\n\t"
"v_mfma_f32_16x16x32_bf16 v[96:99], a[124:127], a[160:163], v[96:99]\n\t"
"buffer_load_dwordx4 a[72:75], v218, s[8:11], s63 offen offset:2048 sc1\n\t"
"buffer_load_dwordx4 a[76:79], v218, s[8:11], s63 offen offset:3072 sc1\n\t"
"s_mov_b32 s83, 286720\n\t"
"s_waitcnt vmcnt(29)\n\t"
"v_mfma_f32_16x16x32_bf16 v[112:115], a[128:131], a[144:147], v[112:115]\n\t"
"v_mfma_f32_16x16x32_bf16 v[112:115], a[132:135], a[160:163], v[112:115]\n\t"
"buffer_load_dwordx4 a[80:83], v235, s[8:11], s64 offen offset:0 sc1\n\t"
"buffer_load_dwordx4 a[84:87], v235, s[8:11], s64 offen offset:1024 sc1\n\t"
"s_waitcnt vmcnt(29)\n\t"
"v_mfma_f32_16x16x32_bf16 v[128:131], a[136:139], a[144:147], v[128:131]\n\t"
"v_mfma_f32_16x16x32_bf16 v[128:131], a[140:143], a[160:163], v[128:131]\n\t"
"buffer_load_dwordx4 a[88:91], v235, s[8:11], s64 offen offset:2048 sc1\n\t"
"buffer_load_dwordx4 a[92:95], v235, s[8:11], s64 offen offset:3072 sc1\n\t"
"s_waitcnt lgkmcnt(6)\n\t"
"v_mfma_f32_16x16x32_bf16 v[132:135], a[136:139], a[148:151], v[132:135]\n\t"
"v_mfma_f32_16x16x32_bf16 v[132:135], a[140:143], a[164:167], v[132:135]\n\t"
"buffer_load_dwordx4 a[96:99], v218, s[8:11], s46 offen offset:0 sc1\n\t"
"buffer_load_dwordx4 a[100:103], v218, s[8:11], s46 offen offset:1024 sc1\n\t"
"v_mfma_f32_16x16x32_bf16 v[116:119], a[128:131], a[148:151], v[116:119]\n\t"
"v_mfma_f32_16x16x32_bf16 v[116:119], a[132:135], a[164:167], v[116:119]\n\t"
"buffer_load_dwordx4 a[104:107], v218, s[8:11], s46 offen offset:2048 sc1\n\t"
"buffer_load_dwordx4 a[108:111], v218, s[8:11], s46 offen offset:3072 sc1\n\t"
"v_mfma_f32_16x16x32_bf16 v[100:103], a[120:123], a[148:151], v[100:103]\n\t"
"v_mfma_f32_16x16x32_bf16 v[100:103], a[124:127], a[164:167], v[100:103]\n\t"
"buffer_load_dwordx4 a[112:115], v235, s[8:11], s47 offen offset:0 sc1\n\t"
"buffer_load_dwordx4 a[116:119], v235, s[8:11], s47 offen offset:1024 sc1\n\t"
"v_add_u32 v246, 8, v228\n\t"
"v_add_u32 v247, 16, v228\n\t"
"s_waitcnt vmcnt(30)\n\t"
"s_waitcnt lgkmcnt(0)\n\t"
"s_barrier\n\t"
"Lunit_%=:\n\t"
"v_mfma_f32_16x16x32_bf16 v[104:107], a[120:123], a[152:155], v[104:107]\n\t"
"ds_read_b128 a[144:147], v216 offset:4096\n\t"
"v_mfma_f32_16x16x32_bf16 v[104:107], a[124:127], a[168:171], v[104:107]\n\t"
"s_add_u32 m0, s26, 55552\n\t"
"ds_read_b128 a[160:163], v216 offset:12288\n\t"
"ds_read_b128 a[148:151], v216 offset:5120\n\t"
"buffer_load_dwordx4 v236, s[12:15], s22 offen offset:256 nt lds\n\t"
"v_mfma_f32_16x16x32_bf16 v[120:123], a[128:131], a[152:155], v[120:123]\n\t"
"ds_read_b128 a[164:167], v216 offset:13312\n\t"
"v_mfma_f32_16x16x32_bf16 v[120:123], a[132:135], a[168:171], v[120:123]\n\t"
"v_mfma_f32_16x16x32_bf16 v[136:139], a[136:139], a[152:155], v[136:139]\n\t"
"ds_read_b128 a[152:155], v216 offset:6144\n\t"
"v_mfma_f32_16x16x32_bf16 v[136:139], a[140:143], a[168:171], v[136:139]\n\t"
"ds_read_b128 a[168:171], v216 offset:14336\n\t"
"v_mfma_f32_16x16x32_bf16 v[192:195], a[176:179], a[216:219], 0\n\t"
"ds_read_b64_tr_b16 a[176:177], v248 offset:0\n\t"
"v_mfma_f32_16x16x32_bf16 v[140:143], a[136:139], a[156:159], v[140:143]\n\t"
"ds_read_b64_tr_b16 a[178:179], v248 offset:512\n\t"
"v_mfma_f32_16x16x32_bf16 v[140:143], a[140:143], a[172:175], v[140:143]\n\t"
"buffer_load_dwordx4 v237, s[12:15], s22 offen offset:1280 nt lds\n\t"
"v_mfma_f32_16x16x32_bf16 v[196:199], a[180:183], a[216:219], 0\n\t"
"ds_read_b64_tr_b16 a[180:181], v249 offset:0\n\t"
"v_mfma_f32_16x16x32_bf16 v[124:127], a[128:131], a[156:159], v[124:127]\n\t"
"v_cvt_pk_bf16_f32 v192, v192, v193\n\t"
"v_cvt_pk_bf16_f32 v193, v194, v195\n\t"
"v_mfma_f32_16x16x32_bf16 v[124:127], a[132:135], a[172:175], v[124:127]\n\t"
"ds_read_b64_tr_b16 a[182:183], v249 offset:512\n\t"
"v_mfma_f32_16x16x32_bf16 v[108:111], a[120:123], a[156:159], v[108:111]\n\t"
"ds_read_b128 a[156:159], v216 offset:7168\n\t"
"v_mfma_f32_16x16x32_bf16 v[108:111], a[124:127], a[172:175], v[108:111]\n\t"
"buffer_load_dwordx4 v238, s[12:15], s22 offen offset:2304 nt lds\n\t"
"v_mfma_f32_16x16x32_bf16 v[200:203], a[184:187], a[216:219], 0\n\t"
"v_cvt_pk_bf16_f32 v194, v196, v197\n\t"
"v_cvt_pk_bf16_f32 v195, v198, v199\n\t"
"s_waitcnt vmcnt(31) lgkmcnt(6)\n\t"
"v_mfma_f32_16x16x32_bf16 v[48:51], a[0:3], a[144:147], v[0:3]\n\t"
"ds_read_b128 a[172:175], v216 offset:15360\n\t"
"v_mfma_f32_16x16x32_bf16 v[48:51], a[4:7], a[160:163], v[48:51]\n\t"
"ds_write_b128 v217, v[192:195] offset:16384\n\t"
"v_mfma_f32_16x16x32_bf16 v[204:207], a[188:191], a[216:219], 0\n\t"
"ds_read_b64_tr_b16 a[184:185], v250 offset:2304\n\t"
"s_waitcnt vmcnt(29) lgkmcnt(6)\n\t"
"v_mfma_f32_16x16x32_bf16 v[64:67], a[8:11], a[144:147], v[16:19]\n\t"
"v_cvt_pk_bf16_f32 v200, v200, v201\n\t"
"v_cvt_pk_bf16_f32 v201, v202, v203\n\t"
"v_mfma_f32_16x16x32_bf16 v[64:67], a[12:15], a[160:163], v[64:67]\n\t"
"buffer_load_dwordx4 v239, s[12:15], s22 offen offset:3328 nt lds\n\t"
"s_waitcnt vmcnt(28) lgkmcnt(5)\n\t"
"v_mfma_f32_16x16x32_bf16 v[80:83], a[16:19], a[144:147], v[32:35]\n\t"
"v_mfma_f32_16x16x32_bf16 v[80:83], a[20:23], a[160:163], v[80:83]\n\t"
"v_cvt_pk_bf16_f32 v202, v204, v205\n\t"
"v_cvt_pk_bf16_f32 v203, v206, v207\n\t"
"v_mfma_f32_16x16x32_bf16 v[208:211], a[192:195], a[216:219], 0\n\t"
"ds_read_b128 a[144:147], v216 offset:0\n\t"
"v_mfma_f32_16x16x32_bf16 v[84:87], a[16:19], a[148:151], v[36:39]\n\t"
"ds_read_b128 a[160:163], v216 offset:8192\n\t"
"v_mfma_f32_16x16x32_bf16 v[84:87], a[20:23], a[164:167], v[84:87]\n\t"
"ds_write_b128 v219, v[200:203] offset:16384\n\t"
"v_mfma_f32_16x16x32_bf16 v[68:71], a[8:11], a[148:151], v[20:23]\n\t"
"ds_read_b64_tr_b16 a[186:187], v250 offset:2816\n\t"
"v_mfma_f32_16x16x32_bf16 v[68:71], a[12:15], a[164:167], v[68:71]\n\t"
"s_mov_b64 exec, s[36:37]\n\t"
"buffer_load_dwordx4 v240, s[12:15], s22 offen offset:3840 nt lds\n\t"
"s_mov_b64 exec, -1\n\t"
"v_mfma_f32_16x16x32_bf16 v[212:215], a[196:199], a[216:219], 0\n\t"
"v_mfma_f32_16x16x32_bf16 v[52:55], a[0:3], a[148:151], v[4:7]\n\t"
"v_cvt_pk_bf16_f32 v208, v208, v209\n\t"
"v_cvt_pk_bf16_f32 v209, v210, v211\n\t"
"v_mfma_f32_16x16x32_bf16 v[52:55], a[4:7], a[164:167], v[52:55]\n\t"
"ds_read_b128 a[148:151], v216 offset:1024\n\t"
"v_mfma_f32_16x16x32_bf16 v[192:195], a[200:203], a[216:219], 0\n\t"
"ds_read_b128 a[164:167], v216 offset:9216\n\t"
"v_mfma_f32_16x16x32_bf16 v[56:59], a[0:3], a[152:155], v[8:11]\n\t"
"v_cvt_pk_bf16_f32 v210, v212, v213\n\t"
"v_cvt_pk_bf16_f32 v211, v214, v215\n\t"
"v_mfma_f32_16x16x32_bf16 v[56:59], a[4:7], a[168:171], v[56:59]\n\t"
"ds_read_b64_tr_b16 a[188:189], v251 offset:2304\n\t"
"v_mfma_f32_16x16x32_bf16 v[72:75], a[8:11], a[152:155], v[24:27]\n\t"
"ds_read_b64_tr_b16 a[190:191], v251 offset:2816\n\t"
"v_mfma_f32_16x16x32_bf16 v[72:75], a[12:15], a[168:171], v[72:75]\n\t"
"v_cvt_pk_bf16_f32 v192, v192, v193\n\t"
"v_cvt_pk_bf16_f32 v193, v194, v195\n\t"
"v_mfma_f32_16x16x32_bf16 v[196:199], a[204:207], a[216:219], 0\n\t"
"ds_read_b64_tr_b16 a[192:193], v250 offset:0\n\t"
"v_mfma_f32_16x16x32_bf16 v[88:91], a[16:19], a[152:155], v[40:43]\n\t"
"ds_read_b64_tr_b16 a[194:195], v250 offset:512\n\t"
"v_mfma_f32_16x16x32_bf16 v[88:91], a[20:23], a[168:171], v[88:91]\n\t"
"ds_write_b128 v217, v[208:211] offset:24576\n\t"
"s_waitcnt lgkmcnt(7)\n\t"
"v_mfma_f32_16x16x32_bf16 v[92:95], a[16:19], a[156:159], v[44:47]\n\t"
"ds_read_b64_tr_b16 a[196:197], v251 offset:0\n\t"
"v_mfma_f32_16x16x32_bf16 v[92:95], a[20:23], a[172:175], v[92:95]\n\t"
"ds_read_b64_tr_b16 a[198:199], v251 offset:512\n\t"
"v_mfma_f32_16x16x32_bf16 v[76:79], a[8:11], a[156:159], v[28:31]\n\t"
"ds_read_b64_tr_b16 a[200:201], v248 offset:2304\n\t"
"v_cvt_pk_bf16_f32 v194, v196, v197\n\t"
"v_mfma_f32_16x16x32_bf16 v[76:79], a[12:15], a[172:175], v[76:79]\n\t"
"ds_read_b64_tr_b16 a[202:203], v248 offset:2816\n\t"
"v_mfma_f32_16x16x32_bf16 v[60:63], a[0:3], a[156:159], v[12:15]\n\t"
"ds_read_b64_tr_b16 a[204:205], v249 offset:2304\n\t"
"v_mfma_f32_16x16x32_bf16 v[60:63], a[4:7], a[172:175], v[60:63]\n\t"
"ds_read_b64_tr_b16 a[206:207], v249 offset:2816\n\t"
"v_cvt_pk_bf16_f32 v195, v198, v199\n\t"
"s_waitcnt vmcnt(27) lgkmcnt(9)\n\t"
"v_mfma_f32_16x16x32_bf16 v[0:3], a[24:27], a[144:147], v[0:3]\n\t"
"ds_read_b128 a[152:155], v216 offset:2048\n\t"
"v_mfma_f32_16x16x32_bf16 v[0:3], a[28:31], a[160:163], v[0:3]\n\t"
"ds_write_b128 v219, v[192:195] offset:24576\n\t"
"s_waitcnt vmcnt(25) lgkmcnt(10)\n\t"
"v_mfma_f32_16x16x32_bf16 v[16:19], a[32:35], a[144:147], v[16:19]\n\t"
"ds_read_b128 a[168:171], v216 offset:10240\n\t"
"v_mfma_f32_16x16x32_bf16 v[16:19], a[36:39], a[160:163], v[16:19]\n\t"
"ds_read_b128 a[156:159], v216 offset:3072\n\t"
"s_waitcnt vmcnt(23) lgkmcnt(10)\n\t"
"v_mfma_f32_16x16x32_bf16 v[32:35], a[40:43], a[144:147], v[32:35]\n\t"
"ds_read_b128 a[172:175], v216 offset:11264\n\t"
"v_mfma_f32_16x16x32_bf16 v[32:35], a[44:47], a[160:163], v[32:35]\n\t"
"buffer_load_dwordx4 a[136:139], v235, s[8:11], s46 offen offset:2048 sc1\n\t"
"v_mfma_f32_16x16x32_bf16 v[36:39], a[40:43], a[148:151], v[36:39]\n\t"
"v_mfma_f32_16x16x32_bf16 v[36:39], a[44:47], a[164:167], v[36:39]\n\t"
"ds_write_b128 v222, a[248:251] offset:20736\n\t"
"v_mfma_f32_16x16x32_bf16 v[20:23], a[32:35], a[148:151], v[20:23]\n\t"
"v_mfma_f32_16x16x32_bf16 v[20:23], a[36:39], a[164:167], v[20:23]\n\t"
"v_mfma_f32_16x16x32_bf16 v[4:7], a[24:27], a[148:151], v[4:7]\n\t"
"v_mfma_f32_16x16x32_bf16 v[4:7], a[28:31], a[164:167], v[4:7]\n\t"
"s_waitcnt lgkmcnt(0)\n\t"
"s_barrier\n\t"
"v_mfma_f32_16x16x32_bf16 v[8:11], a[24:27], a[152:155], v[8:11]\n\t"
"ds_read_b128 a[144:147], v216 offset:20480\n\t"
"v_mfma_f32_16x16x32_bf16 v[8:11], a[28:31], a[168:171], v[8:11]\n\t"
"ds_read_b128 a[160:163], v216 offset:28672\n\t"
"ds_read_b128 a[148:151], v216 offset:21504\n\t"
"v_mfma_f32_16x16x32_bf16 v[24:27], a[32:35], a[152:155], v[24:27]\n\t"
"ds_read_b128 a[164:167], v216 offset:29696\n\t"
"v_mfma_f32_16x16x32_bf16 v[24:27], a[36:39], a[168:171], v[24:27]\n\t"
"s_add_u32 s24, s24, 3145728\n\t"
"s_add_u32 s20, s20, 3145728\n\t"
"s_add_u32 s27, s27, 3145728\n\t"
"s_add_u32 s25, s25, 3145728\n\t"
"v_mfma_f32_16x16x32_bf16 v[40:43], a[40:43], a[152:155], v[40:43]\n\t"
"ds_read_b128 a[152:155], v216 offset:22528\n\t"
"v_mfma_f32_16x16x32_bf16 v[40:43], a[44:47], a[168:171], v[40:43]\n\t"
"ds_read_b128 a[168:171], v216 offset:30720\n\t"
"v_mfma_f32_16x16x32_bf16 v[200:203], a[176:179], a[212:215], 0\n\t"
"v_mfma_f32_16x16x32_bf16 v[44:47], a[40:43], a[156:159], v[44:47]\n\t"
"v_mfma_f32_16x16x32_bf16 v[44:47], a[44:47], a[172:175], v[44:47]\n\t"
"v_mfma_f32_16x16x32_bf16 v[204:207], a[180:183], a[212:215], 0\n\t"
"v_mfma_f32_16x16x32_bf16 v[28:31], a[32:35], a[156:159], v[28:31]\n\t"
"s_nop 3\n\t"
"v_cvt_pk_bf16_f32 v200, v200, v201\n\t"
"v_cvt_pk_bf16_f32 v201, v202, v203\n\t"
"v_mfma_f32_16x16x32_bf16 v[28:31], a[36:39], a[172:175], v[28:31]\n\t"
"v_mfma_f32_16x16x32_bf16 v[12:15], a[24:27], a[156:159], v[12:15]\n\t"
"ds_read_b128 a[156:159], v216 offset:23552\n\t"
"v_mfma_f32_16x16x32_bf16 v[12:15], a[28:31], a[172:175], v[12:15]\n\t"
"v_cvt_pk_bf16_f32 v202, v204, v205\n\t"
"v_cvt_pk_bf16_f32 v203, v206, v207\n\t"
"ds_read_b128 a[172:175], v216 offset:31744\n\t"
"v_mfma_f32_16x16x32_bf16 v[208:211], a[184:187], a[212:215], 0\n\t"
"ds_write_b128 v217, v[200:203] offset:0\n\t"
"s_waitcnt vmcnt(22) lgkmcnt(4)\n\t"
"v_mfma_f32_16x16x32_bf16 v[144:147], a[48:51], a[144:147], v[96:99]\n\t"
"v_mfma_f32_16x16x32_bf16 v[144:147], a[52:55], a[160:163], v[144:147]\n\t"
"v_mfma_f32_16x16x32_bf16 v[212:215], a[188:191], a[212:215], 0\n\t"
"s_waitcnt vmcnt(20) lgkmcnt(3)\n\t"
"v_mfma_f32_16x16x32_bf16 v[160:163], a[56:59], a[144:147], v[112:115]\n\t"
"s_nop 0\n\t"
"v_cvt_pk_bf16_f32 v208, v208, v209\n\t"
"v_cvt_pk_bf16_f32 v209, v210, v211\n\t"
"v_mfma_f32_16x16x32_bf16 v[160:163], a[60:63], a[160:163], v[160:163]\n\t"
"buffer_load_dwordx4 a[140:143], v235, s[8:11], s46 offen offset:3072 sc1\n\t"
"s_waitcnt vmcnt(19)\n\t"
"v_mfma_f32_16x16x32_bf16 v[176:179], a[64:67], a[144:147], v[128:131]\n\t"
"ds_read_b128 a[144:147], v216 offset:16384\n\t"
"v_mfma_f32_16x16x32_bf16 v[176:179], a[68:71], a[160:163], v[176:179]\n\t"
"v_cvt_pk_bf16_f32 v210, v212, v213\n\t"
"v_cvt_pk_bf16_f32 v211, v214, v215\n\t"
"ds_read_b128 a[160:163], v216 offset:24576\n\t"
"v_mfma_f32_16x16x32_bf16 v[192:195], a[192:195], a[212:215], 0\n\t"
"buffer_load_dwordx4 a[132:135], v235, s[8:11], s46 offen offset:1024 sc1\n\t"
"v_mfma_f32_16x16x32_bf16 v[180:183], a[64:67], a[148:151], v[132:135]\n\t"
"v_mfma_f32_16x16x32_bf16 v[180:183], a[68:71], a[164:167], v[180:183]\n\t"
"ds_write_b128 v219, v[208:211] offset:0\n\t"
"v_mfma_f32_16x16x32_bf16 v[164:167], a[56:59], a[148:151], v[116:119]\n\t"
"v_mfma_f32_16x16x32_bf16 v[164:167], a[60:63], a[164:167], v[164:167]\n\t"
"buffer_load_dwordx4 a[128:131], v235, s[8:11], s46 offen offset:0 sc1\n\t"
"v_mfma_f32_16x16x32_bf16 v[196:199], a[196:199], a[212:215], 0\n\t"
"v_cvt_pk_bf16_f32 v192, v192, v193\n\t"
"v_cvt_pk_bf16_f32 v193, v194, v195\n\t"
"v_mfma_f32_16x16x32_bf16 v[148:151], a[48:51], a[148:151], v[100:103]\n\t"
"ds_read_b128 a[148:151], v216 offset:17408\n\t"
"v_mfma_f32_16x16x32_bf16 v[148:151], a[52:55], a[164:167], v[148:151]\n\t"
"ds_read_b128 a[164:167], v216 offset:25600\n\t"
"v_mfma_f32_16x16x32_bf16 v[200:203], a[200:203], a[212:215], 0\n\t"
"buffer_load_dwordx4 a[120:123], v218, s[8:11], s42 offen offset:2048 sc1\n\t"
"v_mfma_f32_16x16x32_bf16 v[152:155], a[48:51], a[152:155], v[104:107]\n\t"
"v_cvt_pk_bf16_f32 v194, v196, v197\n\t"
"v_cvt_pk_bf16_f32 v195, v198, v199\n\t"
"v_mfma_f32_16x16x32_bf16 v[152:155], a[52:55], a[168:171], v[152:155]\n\t"
"ds_write_b128 v217, v[192:195] offset:8192\n\t"
"v_mfma_f32_16x16x32_bf16 v[168:171], a[56:59], a[152:155], v[120:123]\n\t"
"v_mfma_f32_16x16x32_bf16 v[168:171], a[60:63], a[168:171], v[168:171]\n\t"
"buffer_load_dwordx4 a[124:127], v218, s[8:11], s42 offen offset:3072 sc1\n\t"
"v_mfma_f32_16x16x32_bf16 v[204:207], a[204:207], a[212:215], 0\n\t"
"v_cvt_pk_bf16_f32 v200, v200, v201\n\t"
"v_cvt_pk_bf16_f32 v201, v202, v203\n\t"
"v_mfma_f32_16x16x32_bf16 v[184:187], a[64:67], a[152:155], v[136:139]\n\t"
"ds_read_b128 a[152:155], v216 offset:18432\n\t"
"v_mfma_f32_16x16x32_bf16 v[184:187], a[68:71], a[168:171], v[184:187]\n\t"
"buffer_load_dwordx4 a[16:19], v235, s[8:11], s50 offen offset:0 sc1\n\t"
"s_waitcnt lgkmcnt(4)\n\t"
"v_mfma_f32_16x16x32_bf16 v[188:191], a[64:67], a[156:159], v[140:143]\n\t"
"ds_read_b128 a[168:171], v216 offset:26624\n\t"
"v_mfma_f32_16x16x32_bf16 v[188:191], a[68:71], a[172:175], v[188:191]\n\t"
"v_cvt_pk_bf16_f32 v202, v204, v205\n\t"
"v_cvt_pk_bf16_f32 v203, v206, v207\n\t"
"v_mfma_f32_16x16x32_bf16 v[172:175], a[56:59], a[156:159], v[124:127]\n\t"
"v_mfma_f32_16x16x32_bf16 v[172:175], a[60:63], a[172:175], v[172:175]\n\t"
"buffer_load_dwordx4 a[20:23], v235, s[8:11], s50 offen offset:1024 sc1\n\t"
"v_mfma_f32_16x16x32_bf16 v[156:159], a[48:51], a[156:159], v[108:111]\n\t"
"ds_read_b128 a[156:159], v216 offset:19456\n\t"
"v_mfma_f32_16x16x32_bf16 v[156:159], a[52:55], a[172:175], v[156:159]\n\t"
"ds_write_b128 v219, v[200:203] offset:8192\n\t"
"s_waitcnt vmcnt(23) lgkmcnt(4)\n\t"
"v_mfma_f32_16x16x32_bf16 v[96:99], a[72:75], a[144:147], v[96:99]\n\t"
"ds_read_b128 a[172:175], v216 offset:27648\n\t"
"v_mfma_f32_16x16x32_bf16 v[96:99], a[76:79], a[160:163], v[96:99]\n\t"
"s_waitcnt vmcnt(21)\n\t"
"v_mfma_f32_16x16x32_bf16 v[112:115], a[80:83], a[144:147], v[112:115]\n\t"
"v_mfma_f32_16x16x32_bf16 v[112:115], a[84:87], a[160:163], v[112:115]\n\t"
"buffer_load_dwordx4 a[12:15], v218, s[8:11], s49 offen offset:3072 sc1\n\t"
"s_waitcnt vmcnt(20) lgkmcnt(4)\n\t"
"v_mfma_f32_16x16x32_bf16 v[128:131], a[88:91], a[144:147], v[128:131]\n\t"
"v_mfma_f32_16x16x32_bf16 v[128:131], a[92:95], a[160:163], v[128:131]\n\t"
"buffer_load_dwordx4 a[8:11], v218, s[8:11], s49 offen offset:2048 sc1\n\t"
"v_mfma_f32_16x16x32_bf16 v[132:135], a[88:91], a[148:151], v[132:135]\n\t"
"v_mfma_f32_16x16x32_bf16 v[132:135], a[92:95], a[164:167], v[132:135]\n\t"
"buffer_load_dwordx4 a[0:3], v218, s[8:11], s49 offen offset:0 sc1\n\t"
"v_mfma_f32_16x16x32_bf16 v[116:119], a[80:83], a[148:151], v[116:119]\n\t"
"v_mfma_f32_16x16x32_bf16 v[116:119], a[84:87], a[164:167], v[116:119]\n\t"
"v_mfma_f32_16x16x32_bf16 v[100:103], a[72:75], a[148:151], v[100:103]\n\t"
"v_mfma_f32_16x16x32_bf16 v[100:103], a[76:79], a[164:167], v[100:103]\n\t"
"buffer_load_dwordx4 a[4:7], v218, s[8:11], s49 offen offset:1024 sc1\n\t"
"s_waitcnt vmcnt(12) lgkmcnt(0)\n\t"
"s_barrier\n\t"
"v_mfma_f32_16x16x32_bf16 v[104:107], a[72:75], a[152:155], v[104:107]\n\t"
"ds_read_b128 a[144:147], v216 offset:4096\n\t"
"v_add_u32 v248, 32768, v224\n\t"
"v_mfma_f32_16x16x32_bf16 v[104:107], a[76:79], a[168:171], v[104:107]\n\t"
"ds_read_b128 a[160:163], v216 offset:12288\n\t"
"ds_read_b128 a[148:151], v216 offset:5120\n\t"
"v_mfma_f32_16x16x32_bf16 v[120:123], a[80:83], a[152:155], v[120:123]\n\t"
"ds_read_b128 a[164:167], v216 offset:13312\n\t"
"v_xor_b32 v249, 32, v248\n\t"
"v_mfma_f32_16x16x32_bf16 v[120:123], a[84:87], a[168:171], v[120:123]\n\t"
"buffer_load_dwordx4 a[40:43], v235, s[8:11], s49 offen offset:2048 sc1\n\t"
"v_mfma_f32_16x16x32_bf16 v[136:139], a[88:91], a[152:155], v[136:139]\n\t"
"v_xor_b32 v250, 64, v248\n\t"
"v_xor_b32 v251, 96, v248\n\t"
"v_mfma_f32_16x16x32_bf16 v[136:139], a[92:95], a[168:171], v[136:139]\n\t"
"buffer_load_dwordx4 a[44:47], v235, s[8:11], s49 offen offset:3072 sc1\n\t"
"v_mfma_f32_16x16x32_bf16 v[208:211], a[176:179], a[216:219], 0\n\t"
"buffer_load_dwordx4 a[36:39], v235, s[8:11], s49 offen offset:1024 sc1\n\t"
"v_mfma_f32_16x16x32_bf16 v[140:143], a[88:91], a[156:159], v[140:143]\n\t"
"ds_read_b128 a[152:155], v216 offset:6144\n\t"
"v_xor_b32 v226, 64, v226\n\t"
"v_mfma_f32_16x16x32_bf16 v[140:143], a[92:95], a[172:175], v[140:143]\n\t"
"ds_read_b128 a[168:171], v216 offset:14336\n\t"
"v_xor_b32 v227, 64, v227\n\t"
"s_add_u32 s22, s22, 3145728\n\t"
"s_cmp_eq_u32 s21, 1\n\t"
"s_cselect_b32 s22, 2.0, s22\n\t"
"v_mfma_f32_16x16x32_bf16 v[212:215], a[180:183], a[216:219], 0\n\t"
"ds_read_b64_tr_b16 a[176:177], v248 offset:0\n\t"
"v_mfma_f32_16x16x32_bf16 v[124:127], a[80:83], a[156:159], v[124:127]\n\t"
"v_cvt_pk_bf16_f32 v208, v208, v209\n\t"
"v_cvt_pk_bf16_f32 v209, v210, v211\n\t"
"v_mfma_f32_16x16x32_bf16 v[124:127], a[84:87], a[172:175], v[124:127]\n\t"
"buffer_load_dwordx4 a[32:35], v235, s[8:11], s49 offen offset:0 sc1\n\t"
"v_mfma_f32_16x16x32_bf16 v[108:111], a[72:75], a[156:159], v[108:111]\n\t"
"ds_read_b128 a[156:159], v216 offset:7168\n\t"
"v_mfma_f32_16x16x32_bf16 v[108:111], a[76:79], a[172:175], v[108:111]\n\t"
"v_cvt_pk_bf16_f32 v210, v212, v213\n\t"
"v_cvt_pk_bf16_f32 v211, v214, v215\n\t"
"v_mfma_f32_16x16x32_bf16 v[192:195], a[184:187], a[216:219], 0\n\t"
"buffer_load_dwordx4 a[24:27], v218, s[8:11], s48 offen offset:2048 sc1\n\t"
"s_waitcnt lgkmcnt(4)\n\t"
"v_mfma_f32_16x16x32_bf16 v[48:51], a[96:99], a[144:147], v[48:51]\n\t"
"ds_read_b128 a[172:175], v216 offset:15360\n\t"
"v_mfma_f32_16x16x32_bf16 v[48:51], a[100:103], a[160:163], v[48:51]\n\t"
"ds_write_b128 v217, v[208:211] offset:16384\n\t"
"v_mfma_f32_16x16x32_bf16 v[196:199], a[188:191], a[216:219], 0\n\t"
"buffer_load_dwordx4 a[28:31], v218, s[8:11], s48 offen offset:3072 sc1\n\t"
"v_mfma_f32_16x16x32_bf16 v[64:67], a[104:107], a[144:147], v[64:67]\n\t"
"v_cvt_pk_bf16_f32 v192, v192, v193\n\t"
"v_cvt_pk_bf16_f32 v193, v194, v195\n\t"
"v_mfma_f32_16x16x32_bf16 v[64:67], a[108:111], a[160:163], v[64:67]\n\t"
"ds_read_b64_tr_b16 a[178:179], v248 offset:512\n\t"
"v_mfma_f32_16x16x32_bf16 v[80:83], a[112:115], a[144:147], v[80:83]\n\t"
"ds_read_b128 a[144:147], v216 offset:0\n\t"
"v_mfma_f32_16x16x32_bf16 v[80:83], a[116:119], a[160:163], v[80:83]\n\t"
"buffer_load_dwordx4 a[64:67], v235, s[8:11], s77 offen offset:0 sc1\n\t"
"v_mfma_f32_16x16x32_bf16 v[200:203], a[192:195], a[216:219], 0\n\t"
"v_cvt_pk_bf16_f32 v194, v196, v197\n\t"
"v_cvt_pk_bf16_f32 v195, v198, v199\n\t"
"v_mfma_f32_16x16x32_bf16 v[84:87], a[112:115], a[148:151], v[84:87]\n\t"
"ds_read_b128 a[160:163], v216 offset:8192\n\t"
"v_mfma_f32_16x16x32_bf16 v[84:87], a[116:119], a[164:167], v[84:87]\n\t"
"buffer_load_dwordx4 a[68:71], v235, s[8:11], s77 offen offset:1024 sc1\n\t"
"v_mfma_f32_16x16x32_bf16 v[68:71], a[104:107], a[148:151], v[68:71]\n\t"
"ds_read_b64_tr_b16 a[180:181], v249 offset:0\n\t"
"v_mfma_f32_16x16x32_bf16 v[68:71], a[108:111], a[164:167], v[68:71]\n\t"
"ds_write_b128 v219, v[192:195] offset:16384\n\t"
"v_mfma_f32_16x16x32_bf16 v[204:207], a[196:199], a[216:219], 0\n\t"
"v_cvt_pk_bf16_f32 v200, v200, v201\n\t"
"v_cvt_pk_bf16_f32 v201, v202, v203\n\t"
"v_mfma_f32_16x16x32_bf16 v[52:55], a[96:99], a[148:151], v[52:55]\n\t"
"ds_read_b128 a[148:151], v216 offset:1024\n\t"
"v_mfma_f32_16x16x32_bf16 v[52:55], a[100:103], a[164:167], v[52:55]\n\t"
"buffer_load_dwordx4 a[60:63], v218, s[8:11], s76 offen offset:3072 sc1\n\t"
"v_mfma_f32_16x16x32_bf16 v[208:211], a[200:203], a[216:219], 0\n\t"
"ds_read_b128 a[164:167], v216 offset:9216\n\t"
"s_waitcnt lgkmcnt(5)\n\t"
"v_mfma_f32_16x16x32_bf16 v[56:59], a[96:99], a[152:155], v[56:59]\n\t"
"v_cvt_pk_bf16_f32 v202, v204, v205\n\t"
"v_cvt_pk_bf16_f32 v203, v206, v207\n\t"
"v_mfma_f32_16x16x32_bf16 v[56:59], a[100:103], a[168:171], v[56:59]\n\t"
"buffer_load_dwordx4 a[56:59], v218, s[8:11], s76 offen offset:2048 sc1\n\t"
"v_mfma_f32_16x16x32_bf16 v[72:75], a[104:107], a[152:155], v[72:75]\n\t"
"ds_read_b64_tr_b16 a[182:183], v249 offset:512\n\t"
"v_mfma_f32_16x16x32_bf16 v[72:75], a[108:111], a[168:171], v[72:75]\n\t"
"v_cvt_pk_bf16_f32 v208, v208, v209\n\t"
"v_cvt_pk_bf16_f32 v209, v210, v211\n\t"
"v_mfma_f32_16x16x32_bf16 v[212:215], a[204:207], a[216:219], 0\n\t"
"buffer_load_dwordx4 a[48:51], v218, s[8:11], s76 offen offset:0 sc1\n\t"
"v_mfma_f32_16x16x32_bf16 v[88:91], a[112:115], a[152:155], v[88:91]\n\t"
"ds_read_b64_tr_b16 a[184:185], v250 offset:2304\n\t"
"v_mfma_f32_16x16x32_bf16 v[88:91], a[116:119], a[168:171], v[88:91]\n\t"
"ds_write_b128 v217, v[200:203] offset:24576\n\t"
"v_mfma_f32_16x16x32_bf16 v[92:95], a[112:115], a[156:159], v[92:95]\n\t"
"ds_read_b64_tr_b16 a[186:187], v250 offset:2816\n\t"
"v_mfma_f32_16x16x32_bf16 v[92:95], a[116:119], a[172:175], v[92:95]\n\t"
"buffer_load_dwordx4 a[52:55], v218, s[8:11], s76 offen offset:1024 sc1\n\t"
"v_mfma_f32_16x16x32_bf16 v[76:79], a[104:107], a[156:159], v[76:79]\n\t"
"ds_read_b64_tr_b16 a[188:189], v251 offset:2304\n\t"
"v_cvt_pk_bf16_f32 v210, v212, v213\n\t"
"v_mfma_f32_16x16x32_bf16 v[76:79], a[108:111], a[172:175], v[76:79]\n\t"
"ds_read_b64_tr_b16 a[190:191], v251 offset:2816\n\t"
"v_cvt_pk_bf16_f32 v211, v214, v215\n\t"
"v_mfma_f32_16x16x32_bf16 v[60:63], a[96:99], a[156:159], v[60:63]\n\t"
"ds_read_b64_tr_b16 a[192:193], v250 offset:0\n\t"
"v_mfma_f32_16x16x32_bf16 v[60:63], a[100:103], a[172:175], v[60:63]\n\t"
"ds_write_b128 v219, v[208:211] offset:24576\n\t"
"s_waitcnt vmcnt(18) lgkmcnt(7)\n\t"
"v_mfma_f32_16x16x32_bf16 v[0:3], a[120:123], a[144:147], v[0:3]\n\t"
"ds_read_b64_tr_b16 a[194:195], v250 offset:512\n\t"
"v_mfma_f32_16x16x32_bf16 v[0:3], a[124:127], a[160:163], v[0:3]\n\t"
"ds_read_b64_tr_b16 a[196:197], v251 offset:0\n\t"
"ds_read_b64_tr_b16 a[198:199], v251 offset:512\n\t"
"ds_read_b64_tr_b16 a[200:201], v248 offset:2304\n\t"
"ds_read_b64_tr_b16 a[202:203], v248 offset:2816\n\t"
"ds_read_b64_tr_b16 a[204:205], v249 offset:2304\n\t"
"ds_read_b64_tr_b16 a[206:207], v249 offset:2816\n\t"
"buffer_load_dwordx4 a[88:91], v235, s[8:11], s76 offen offset:2048 sc1\n\t"
"v_mfma_f32_16x16x32_bf16 v[16:19], a[128:131], a[144:147], v[16:19]\n\t"
"ds_read_b128 a[152:155], v216 offset:2048\n\t"
"v_mfma_f32_16x16x32_bf16 v[16:19], a[132:135], a[160:163], v[16:19]\n\t"
"buffer_load_dwordx4 a[92:95], v235, s[8:11], s76 offen offset:3072 sc1\n\t"
"v_mfma_f32_16x16x32_bf16 v[32:35], a[136:139], a[144:147], v[32:35]\n\t"
"ds_read_b128 a[168:171], v216 offset:10240\n\t"
"v_mfma_f32_16x16x32_bf16 v[32:35], a[140:143], a[160:163], v[32:35]\n\t"
"ds_read_b128 a[156:159], v216 offset:3072\n\t"
"v_mfma_f32_16x16x32_bf16 v[36:39], a[136:139], a[148:151], v[36:39]\n\t"
"ds_read_b128 a[172:175], v216 offset:11264\n\t"
"v_mfma_f32_16x16x32_bf16 v[36:39], a[140:143], a[164:167], v[36:39]\n\t"
"ds_read_b128 a[248:251], v222 offset:18432\n\t"
"v_mfma_f32_16x16x32_bf16 v[20:23], a[128:131], a[148:151], v[20:23]\n\t"
"v_mfma_f32_16x16x32_bf16 v[20:23], a[132:135], a[164:167], v[20:23]\n\t"
"buffer_load_dwordx4 a[84:87], v235, s[8:11], s76 offen offset:1024 sc1\n\t"
"v_mfma_f32_16x16x32_bf16 v[4:7], a[120:123], a[148:151], v[4:7]\n\t"
"v_mfma_f32_16x16x32_bf16 v[4:7], a[124:127], a[164:167], v[4:7]\n\t"
"buffer_load_dwordx4 a[80:83], v235, s[8:11], s76 offen offset:0 sc1\n\t"
"s_waitcnt lgkmcnt(0)\n\t"
"s_barrier\n\t"
"v_mfma_f32_16x16x32_bf16 v[8:11], a[120:123], a[152:155], v[8:11]\n\t"
"ds_read_b128 a[144:147], v216 offset:20480\n\t"
"v_mfma_f32_16x16x32_bf16 v[8:11], a[124:127], a[168:171], v[8:11]\n\t"
"ds_read_b128 a[160:163], v216 offset:28672\n\t"
"ds_read_b128 a[148:151], v216 offset:21504\n\t"
"v_mfma_f32_16x16x32_bf16 v[24:27], a[128:131], a[152:155], v[24:27]\n\t"
"ds_read_b128 a[164:167], v216 offset:29696\n\t"
"v_mfma_f32_16x16x32_bf16 v[24:27], a[132:135], a[168:171], v[24:27]\n\t"
"buffer_load_dwordx4 a[72:75], v218, s[8:11], s75 offen offset:2048 sc1\n\t"
"v_mfma_f32_16x16x32_bf16 v[40:43], a[136:139], a[152:155], v[40:43]\n\t"
"ds_read_b128 a[152:155], v216 offset:22528\n\t"
"v_mfma_f32_16x16x32_bf16 v[40:43], a[140:143], a[168:171], v[40:43]\n\t"
"buffer_load_dwordx4 a[76:79], v218, s[8:11], s75 offen offset:3072 sc1\n\t"
"v_mfma_f32_16x16x32_bf16 v[192:195], a[176:179], a[212:215], 0\n\t"
"buffer_load_dwordx4 a[112:115], v235, s[8:11], s80 offen offset:0 sc1\n\t"
"v_mfma_f32_16x16x32_bf16 v[44:47], a[136:139], a[156:159], v[44:47]\n\t"
"ds_read_b128 a[168:171], v216 offset:30720\n\t"
"v_mfma_f32_16x16x32_bf16 v[44:47], a[140:143], a[172:175], v[44:47]\n\t"
"buffer_load_dwordx4 a[116:119], v235, s[8:11], s80 offen offset:1024 sc1\n\t"
"v_mfma_f32_16x16x32_bf16 v[196:199], a[180:183], a[212:215], 0\n\t"
"ds_read_b128 a[244:247], v223 offset:41472\n\t"
"v_mfma_f32_16x16x32_bf16 v[28:31], a[128:131], a[156:159], v[28:31]\n\t"
"v_cvt_pk_bf16_f32 v192, v192, v193\n\t"
"v_cvt_pk_bf16_f32 v193, v194, v195\n\t"
"v_mfma_f32_16x16x32_bf16 v[28:31], a[132:135], a[172:175], v[28:31]\n\t"
"v_mfma_f32_16x16x32_bf16 v[12:15], a[120:123], a[156:159], v[12:15]\n\t"
"ds_read_b128 a[156:159], v216 offset:23552\n\t"
"v_mfma_f32_16x16x32_bf16 v[12:15], a[124:127], a[172:175], v[12:15]\n\t"
"buffer_load_dwordx4 a[108:111], v218, s[8:11], s79 offen offset:3072 sc1\n\t"
"v_mfma_f32_16x16x32_bf16 v[200:203], a[184:187], a[212:215], 0\n\t"
"v_cvt_pk_bf16_f32 v194, v196, v197\n\t"
"v_cvt_pk_bf16_f32 v195, v198, v199\n\t"
"s_waitcnt vmcnt(21) lgkmcnt(3)\n\t"
"v_mfma_f32_16x16x32_bf16 v[144:147], a[0:3], a[144:147], v[144:147]\n\t"
"ds_read_b128 a[172:175], v216 offset:31744\n\t"
"v_mfma_f32_16x16x32_bf16 v[144:147], a[4:7], a[160:163], v[144:147]\n\t"
"buffer_load_dwordx4 a[104:107], v218, s[8:11], s79 offen offset:2048 sc1\n\t"
"v_mfma_f32_16x16x32_bf16 v[204:207], a[188:191], a[212:215], 0\n\t"
"ds_write_b128 v217, v[192:195] offset:0\n\t"
"v_mfma_f32_16x16x32_bf16 v[160:163], a[8:11], a[144:147], v[160:163]\n\t"
"v_cvt_pk_bf16_f32 v200, v200, v201\n\t"
"v_mfma_f32_16x16x32_bf16 v[160:163], a[12:15], a[160:163], v[160:163]\n\t"
"buffer_load_dwordx4 a[96:99], v218, s[8:11], s79 offen offset:0 sc1\n\t"
"v_mfma_f32_16x16x32_bf16 v[176:179], a[16:19], a[144:147], v[176:179]\n\t"
"v_cvt_pk_bf16_f32 v201, v202, v203\n\t"
"v_mfma_f32_16x16x32_bf16 v[176:179], a[20:23], a[160:163], v[176:179]\n\t"
"v_cvt_pk_bf16_f32 v202, v204, v205\n\t"
"v_cvt_pk_bf16_f32 v203, v206, v207\n\t"
"v_mfma_f32_16x16x32_bf16 v[208:211], a[192:195], a[212:215], 0\n\t"
"ds_read_b128 a[144:147], v216 offset:16384\n\t"
"v_mfma_f32_16x16x32_bf16 v[180:183], a[16:19], a[148:151], v[180:183]\n\t"
"ds_read_b128 a[160:163], v216 offset:24576\n\t"
"v_mfma_f32_16x16x32_bf16 v[180:183], a[20:23], a[164:167], v[180:183]\n\t"
"buffer_load_dwordx4 a[100:103], v218, s[8:11], s79 offen offset:1024 sc1\n\t"
"v_mfma_f32_16x16x32_bf16 v[164:167], a[8:11], a[148:151], v[164:167]\n\t"
"v_mfma_f32_16x16x32_bf16 v[164:167], a[12:15], a[164:167], v[164:167]\n\t"
"buffer_load_dwordx4 a[136:139], v235, s[8:11], s79 offen offset:2048 sc1\n\t"
"v_mfma_f32_16x16x32_bf16 v[212:215], a[196:199], a[212:215], 0\n\t"
"ds_write_b128 v219, v[200:203] offset:0\n\t"
"v_mfma_f32_16x16x32_bf16 v[148:151], a[0:3], a[148:151], v[148:151]\n\t"
"v_cvt_pk_bf16_f32 v208, v208, v209\n\t"
"v_cvt_pk_bf16_f32 v209, v210, v211\n\t"
"v_mfma_f32_16x16x32_bf16 v[148:151], a[4:7], a[164:167], v[148:151]\n\t"
"ds_read_b128 a[148:151], v216 offset:17408\n\t"
"v_mfma_f32_16x16x32_bf16 v[192:195], a[200:203], a[212:215], 0\n\t"
"ds_read_b128 a[164:167], v216 offset:25600\n\t"
"s_waitcnt lgkmcnt(5)\n\t"
"v_mfma_f32_16x16x32_bf16 v[152:155], a[0:3], a[152:155], v[152:155]\n\t"
"v_cvt_pk_bf16_f32 v210, v212, v213\n\t"
"v_cvt_pk_bf16_f32 v211, v214, v215\n\t"
"v_mfma_f32_16x16x32_bf16 v[152:155], a[4:7], a[168:171], v[152:155]\n\t"
"v_mfma_f32_16x16x32_bf16 v[168:171], a[8:11], a[152:155], v[168:171]\n\t"
"v_mfma_f32_16x16x32_bf16 v[168:171], a[12:15], a[168:171], v[168:171]\n\t"
"buffer_load_dwordx4 a[140:143], v235, s[8:11], s79 offen offset:3072 sc1\n\t"
"v_mfma_f32_16x16x32_bf16 v[196:199], a[204:207], a[212:215], 0\n\t"
"buffer_load_dwordx4 a[132:135], v235, s[8:11], s79 offen offset:1024 sc1\n\t"
"v_mfma_f32_16x16x32_bf16 v[184:187], a[16:19], a[152:155], v[184:187]\n\t"
"v_cvt_pk_bf16_f32 v192, v192, v193\n\t"
"v_cvt_pk_bf16_f32 v193, v194, v195\n\t"
"v_mfma_f32_16x16x32_bf16 v[184:187], a[20:23], a[168:171], v[184:187]\n\t"
"buffer_load_dwordx4 a[128:131], v235, s[8:11], s79 offen offset:0 sc1\n\t"
"v_mfma_f32_16x16x32_bf16 v[188:191], a[16:19], a[156:159], v[188:191]\n\t"
"ds_read_b128 a[152:155], v216 offset:18432\n\t"
"v_mfma_f32_16x16x32_bf16 v[188:191], a[20:23], a[172:175], v[188:191]\n\t"
"ds_write_b128 v217, v[208:211] offset:8192\n\t"
"v_mfma_f32_16x16x32_bf16 v[172:175], a[8:11], a[156:159], v[172:175]\n\t"
"v_cvt_pk_bf16_f32 v194, v196, v197\n\t"
"v_cvt_pk_bf16_f32 v195, v198, v199\n\t"
"v_mfma_f32_16x16x32_bf16 v[172:175], a[12:15], a[172:175], v[172:175]\n\t"
"ds_read_b128 a[168:171], v216 offset:26624\n\t"
"v_mfma_f32_16x16x32_bf16 v[156:159], a[0:3], a[156:159], v[156:159]\n\t"
"ds_read_b128 a[156:159], v216 offset:19456\n\t"
"v_mfma_f32_16x16x32_bf16 v[156:159], a[4:7], a[172:175], v[156:159]\n\t"
"buffer_load_dwordx4 a[120:123], v218, s[8:11], s78 offen offset:2048 sc1\n\t"
"s_waitcnt vmcnt(23) lgkmcnt(4)\n\t"
"v_mfma_f32_16x16x32_bf16 v[96:99], a[24:27], a[144:147], v[96:99]\n\t"
"ds_read_b128 a[172:175], v216 offset:27648\n\t"
"v_mfma_f32_16x16x32_bf16 v[96:99], a[28:31], a[160:163], v[96:99]\n\t"
"ds_write_b128 v219, v[192:195] offset:8192\n\t"
"v_mfma_f32_16x16x32_bf16 v[112:115], a[32:35], a[144:147], v[112:115]\n\t"
"v_mfma_f32_16x16x32_bf16 v[112:115], a[36:39], a[160:163], v[112:115]\n\t"
"buffer_load_dwordx4 a[124:127], v218, s[8:11], s78 offen offset:3072 sc1\n\t"
"v_mfma_f32_16x16x32_bf16 v[128:131], a[40:43], a[144:147], v[128:131]\n\t"
"v_mfma_f32_16x16x32_bf16 v[128:131], a[44:47], a[160:163], v[128:131]\n\t"
"buffer_load_dwordx4 a[16:19], v235, s[8:11], s74 offen offset:0 sc1\n\t"
"v_mfma_f32_16x16x32_bf16 v[132:135], a[40:43], a[148:151], v[132:135]\n\t"
"v_mfma_f32_16x16x32_bf16 v[132:135], a[44:47], a[164:167], v[132:135]\n\t"
"buffer_load_dwordx4 a[20:23], v235, s[8:11], s74 offen offset:1024 sc1\n\t"
"v_mfma_f32_16x16x32_bf16 v[116:119], a[32:35], a[148:151], v[116:119]\n\t"
"v_mfma_f32_16x16x32_bf16 v[116:119], a[36:39], a[164:167], v[116:119]\n\t"
"v_mfma_f32_16x16x32_bf16 v[100:103], a[24:27], a[148:151], v[100:103]\n\t"
"v_mfma_f32_16x16x32_bf16 v[100:103], a[28:31], a[164:167], v[100:103]\n\t"
"buffer_load_dwordx4 a[12:15], v218, s[8:11], s72 offen offset:3072 sc1\n\t"
"s_waitcnt lgkmcnt(0)\n\t"
"s_barrier\n\t"
"v_mfma_f32_16x16x32_bf16 v[104:107], a[24:27], a[152:155], v[104:107]\n\t"
"ds_read_b128 a[144:147], v216 offset:4096\n\t"
"v_add_u32 v248, 32768, v225\n\t"
"v_mfma_f32_16x16x32_bf16 v[104:107], a[28:31], a[168:171], v[104:107]\n\t"
"ds_read_b128 a[160:163], v216 offset:12288\n\t"
"ds_read_b128 a[148:151], v216 offset:5120\n\t"
"v_mfma_f32_16x16x32_bf16 v[120:123], a[32:35], a[152:155], v[120:123]\n\t"
"ds_read_b128 a[164:167], v216 offset:13312\n\t"
"v_xor_b32 v249, 32, v248\n\t"
"v_mfma_f32_16x16x32_bf16 v[120:123], a[36:39], a[168:171], v[120:123]\n\t"
"buffer_load_dwordx4 a[8:11], v218, s[8:11], s72 offen offset:2048 sc1\n\t"
"v_mfma_f32_16x16x32_bf16 v[136:139], a[40:43], a[152:155], v[136:139]\n\t"
"v_xor_b32 v250, 64, v248\n\t"
"v_xor_b32 v251, 96, v248\n\t"
"v_mfma_f32_16x16x32_bf16 v[136:139], a[44:47], a[168:171], v[136:139]\n\t"
"buffer_load_dwordx4 a[0:3], v218, s[8:11], s72 offen offset:0 sc1\n\t"
"v_mfma_f32_16x16x32_bf16 v[200:203], a[176:179], a[216:219], 0\n\t"
"ds_read_b128 a[152:155], v216 offset:6144\n\t"
"v_mfma_f32_16x16x32_bf16 v[140:143], a[40:43], a[156:159], v[140:143]\n\t"
"ds_read_b128 a[168:171], v216 offset:14336\n\t"
"v_mfma_f32_16x16x32_bf16 v[140:143], a[44:47], a[172:175], v[140:143]\n\t"
"buffer_load_dwordx4 a[4:7], v218, s[8:11], s72 offen offset:1024 sc1\n\t"
"v_mfma_f32_16x16x32_bf16 v[204:207], a[180:183], a[216:219], 0\n\t"
"ds_read_b64_tr_b16 a[176:177], v248 offset:0\n\t"
"v_mfma_f32_16x16x32_bf16 v[124:127], a[32:35], a[156:159], v[124:127]\n\t"
"v_cvt_pk_bf16_f32 v200, v200, v201\n\t"
"v_cvt_pk_bf16_f32 v201, v202, v203\n\t"
"v_mfma_f32_16x16x32_bf16 v[124:127], a[36:39], a[172:175], v[124:127]\n\t"
"buffer_load_dwordx4 a[40:43], v235, s[8:11], s72 offen offset:2048 sc1\n\t"
"v_mfma_f32_16x16x32_bf16 v[108:111], a[24:27], a[156:159], v[108:111]\n\t"
"ds_read_b128 a[156:159], v216 offset:7168\n\t"
"v_mfma_f32_16x16x32_bf16 v[108:111], a[28:31], a[172:175], v[108:111]\n\t"
"v_cvt_pk_bf16_f32 v202, v204, v205\n\t"
"v_cvt_pk_bf16_f32 v203, v206, v207\n\t"
"v_mfma_f32_16x16x32_bf16 v[208:211], a[184:187], a[216:219], 0\n\t"
"buffer_load_dwordx4 a[44:47], v235, s[8:11], s72 offen offset:3072 sc1\n\t"
"s_waitcnt vmcnt(26) lgkmcnt(4)\n\t"
"v_mfma_f32_16x16x32_bf16 v[48:51], a[48:51], a[144:147], v[48:51]\n\t"
"ds_read_b128 a[172:175], v216 offset:15360\n\t"
"v_mfma_f32_16x16x32_bf16 v[48:51], a[52:55], a[160:163], v[48:51]\n\t"
"ds_write_b128 v217, v[200:203] offset:16384\n\t"
"v_mfma_f32_16x16x32_bf16 v[212:215], a[188:191], a[216:219], 0\n\t"
"buffer_load_dwordx4 a[36:39], v235, s[8:11], s72 offen offset:1024 sc1\n\t"
"v_mfma_f32_16x16x32_bf16 v[64:67], a[56:59], a[144:147], v[64:67]\n\t"
"v_cvt_pk_bf16_f32 v208, v208, v209\n\t"
"v_cvt_pk_bf16_f32 v209, v210, v211\n\t"
"v_mfma_f32_16x16x32_bf16 v[64:67], a[60:63], a[160:163], v[64:67]\n\t"
"ds_read_b64_tr_b16 a[178:179], v248 offset:512\n\t"
"v_mfma_f32_16x16x32_bf16 v[80:83], a[64:67], a[144:147], v[80:83]\n\t"
"ds_read_b128 a[144:147], v216 offset:0\n\t"
"v_mfma_f32_16x16x32_bf16 v[80:83], a[68:71], a[160:163], v[80:83]\n\t"
"buffer_load_dwordx4 a[32:35], v235, s[8:11], s72 offen offset:0 sc1\n\t"
"v_mfma_f32_16x16x32_bf16 v[192:195], a[192:195], a[216:219], 0\n\t"
"v_cvt_pk_bf16_f32 v210, v212, v213\n\t"
"v_cvt_pk_bf16_f32 v211, v214, v215\n\t"
"v_mfma_f32_16x16x32_bf16 v[84:87], a[64:67], a[148:151], v[84:87]\n\t"
"ds_read_b128 a[160:163], v216 offset:8192\n\t"
"v_mfma_f32_16x16x32_bf16 v[84:87], a[68:71], a[164:167], v[84:87]\n\t"
"ds_write_b128 v219, v[208:211] offset:16384\n\t"
"v_mfma_f32_16x16x32_bf16 v[68:71], a[56:59], a[148:151], v[68:71]\n\t"
"ds_read_b64_tr_b16 a[180:181], v249 offset:0\n\t"
"v_mfma_f32_16x16x32_bf16 v[68:71], a[60:63], a[164:167], v[68:71]\n\t"
"buffer_load_dwordx4 a[24:27], v218, s[8:11], s69 offen offset:2048 sc1\n\t"
"v_mfma_f32_16x16x32_bf16 v[196:199], a[196:199], a[216:219], 0\n\t"
"v_cvt_pk_bf16_f32 v192, v192, v193\n\t"
"v_cvt_pk_bf16_f32 v193, v194, v195\n\t"
"v_mfma_f32_16x16x32_bf16 v[52:55], a[48:51], a[148:151], v[52:55]\n\t"
"ds_read_b128 a[148:151], v216 offset:1024\n\t"
"v_mfma_f32_16x16x32_bf16 v[52:55], a[52:55], a[164:167], v[52:55]\n\t"
"buffer_load_dwordx4 a[28:31], v218, s[8:11], s69 offen offset:3072 sc1\n\t"
"v_mfma_f32_16x16x32_bf16 v[200:203], a[200:203], a[216:219], 0\n\t"
"ds_read_b128 a[164:167], v216 offset:9216\n\t"
"s_waitcnt lgkmcnt(5)\n\t"
"v_mfma_f32_16x16x32_bf16 v[56:59], a[48:51], a[152:155], v[56:59]\n\t"
"v_cvt_pk_bf16_f32 v194, v196, v197\n\t"
"v_cvt_pk_bf16_f32 v195, v198, v199\n\t"
"v_mfma_f32_16x16x32_bf16 v[56:59], a[52:55], a[168:171], v[56:59]\n\t"
"ds_write_b128 v217, v[192:195] offset:24576\n\t"
"v_mfma_f32_16x16x32_bf16 v[72:75], a[56:59], a[152:155], v[72:75]\n\t"
"ds_read_b64_tr_b16 a[182:183], v249 offset:512\n\t"
"v_mfma_f32_16x16x32_bf16 v[72:75], a[60:63], a[168:171], v[72:75]\n\t"
"v_cvt_pk_bf16_f32 v200, v200, v201\n\t"
"v_cvt_pk_bf16_f32 v201, v202, v203\n\t"
"v_mfma_f32_16x16x32_bf16 v[204:207], a[204:207], a[216:219], 0\n\t"
"ds_read_b64_tr_b16 a[184:185], v250 offset:2304\n\t"
"v_mfma_f32_16x16x32_bf16 v[88:91], a[64:67], a[152:155], v[88:91]\n\t"
"ds_read_b64_tr_b16 a[186:187], v250 offset:2816\n\t"
"v_mfma_f32_16x16x32_bf16 v[88:91], a[68:71], a[168:171], v[88:91]\n\t"
"ds_read_b64_tr_b16 a[188:189], v251 offset:2304\n\t"
"v_mfma_f32_16x16x32_bf16 v[92:95], a[64:67], a[156:159], v[92:95]\n\t"
"ds_read_b64_tr_b16 a[190:191], v251 offset:2816\n\t"
"v_mfma_f32_16x16x32_bf16 v[92:95], a[68:71], a[172:175], v[92:95]\n\t"
"buffer_load_dwordx4 a[64:67], v235, s[8:11], s83 offen offset:0 sc1\n\t"
"buffer_load_dwordx4 a[68:71], v235, s[8:11], s83 offen offset:1024 sc1\n\t"
"v_mfma_f32_16x16x32_bf16 v[76:79], a[56:59], a[156:159], v[76:79]\n\t"
"ds_read_b64_tr_b16 a[192:193], v250 offset:0\n\t"
"v_cvt_pk_bf16_f32 v202, v204, v205\n\t"
"v_mfma_f32_16x16x32_bf16 v[76:79], a[60:63], a[172:175], v[76:79]\n\t"
"buffer_load_dwordx4 a[60:63], v218, s[8:11], s82 offen offset:3072 sc1\n\t"
"v_mfma_f32_16x16x32_bf16 v[60:63], a[48:51], a[156:159], v[60:63]\n\t"
"ds_read_b64_tr_b16 a[194:195], v250 offset:512\n\t"
"v_cvt_pk_bf16_f32 v203, v206, v207\n\t"
"v_mfma_f32_16x16x32_bf16 v[60:63], a[52:55], a[172:175], v[60:63]\n\t"
"ds_write_b128 v219, v[200:203] offset:24576\n\t"
"s_waitcnt vmcnt(27) lgkmcnt(7)\n\t"
"v_mfma_f32_16x16x32_bf16 v[0:3], a[72:75], a[144:147], v[0:3]\n\t"
"ds_read_b64_tr_b16 a[196:197], v251 offset:0\n\t"
"v_mfma_f32_16x16x32_bf16 v[0:3], a[76:79], a[160:163], v[0:3]\n\t"
"ds_read_b64_tr_b16 a[198:199], v251 offset:512\n\t"
"ds_read_b64_tr_b16 a[200:201], v248 offset:2304\n\t"
"ds_read_b64_tr_b16 a[202:203], v248 offset:2816\n\t"
"ds_read_b64_tr_b16 a[204:205], v249 offset:2304\n\t"
"ds_read_b64_tr_b16 a[206:207], v249 offset:2816\n\t"
"v_mfma_f32_16x16x32_bf16 v[16:19], a[80:83], a[144:147], v[16:19]\n\t"
"ds_read_b128 a[152:155], v216 offset:2048\n\t"
"v_mfma_f32_16x16x32_bf16 v[16:19], a[84:87], a[160:163], v[16:19]\n\t"
"buffer_load_dwordx4 a[56:59], v218, s[8:11], s82 offen offset:2048 sc1\n\t"
"v_mfma_f32_16x16x32_bf16 v[32:35], a[88:91], a[144:147], v[32:35]\n\t"
"ds_read_b128 a[168:171], v216 offset:10240\n\t"
"v_mfma_f32_16x16x32_bf16 v[32:35], a[92:95], a[160:163], v[32:35]\n\t"
"buffer_load_dwordx4 a[48:51], v218, s[8:11], s82 offen offset:0 sc1\n\t"
"v_mfma_f32_16x16x32_bf16 v[36:39], a[88:91], a[148:151], v[36:39]\n\t"
"ds_read_b128 a[156:159], v216 offset:3072\n\t"
"v_mfma_f32_16x16x32_bf16 v[36:39], a[92:95], a[164:167], v[36:39]\n\t"
"ds_read_b128 a[172:175], v216 offset:11264\n\t"
"ds_read_b128 a[240:243], v223 offset:0\n\t"
"v_mfma_f32_16x16x32_bf16 v[20:23], a[80:83], a[148:151], v[20:23]\n\t"
"v_mfma_f32_16x16x32_bf16 v[20:23], a[84:87], a[164:167], v[20:23]\n\t"
"buffer_load_dwordx4 a[52:55], v218, s[8:11], s82 offen offset:1024 sc1\n\t"
"v_mfma_f32_16x16x32_bf16 v[4:7], a[72:75], a[148:151], v[4:7]\n\t"
"v_mfma_f32_16x16x32_bf16 v[4:7], a[76:79], a[164:167], v[4:7]\n\t"
"s_waitcnt lgkmcnt(0)\n\t"
"s_barrier\n\t"
"v_mfma_f32_16x16x32_bf16 v[8:11], a[72:75], a[152:155], v[8:11]\n\t"
"ds_read_b128 a[144:147], v216 offset:20480\n\t"
"v_mfma_f32_16x16x32_bf16 v[8:11], a[76:79], a[168:171], v[8:11]\n\t"
"ds_read_b128 a[160:163], v216 offset:28672\n\t"
"ds_read_b128 a[148:151], v216 offset:21504\n\t"
"v_mfma_f32_16x16x32_bf16 v[24:27], a[80:83], a[152:155], v[24:27]\n\t"
"ds_read_b128 a[164:167], v216 offset:29696\n\t"
"v_mfma_f32_16x16x32_bf16 v[24:27], a[84:87], a[168:171], v[24:27]\n\t"
"v_mfma_f32_16x16x32_bf16 v[40:43], a[88:91], a[152:155], v[40:43]\n\t"
"ds_read_b128 a[152:155], v216 offset:22528\n\t"
"v_mfma_f32_16x16x32_bf16 v[40:43], a[92:95], a[168:171], v[40:43]\n\t"
"ds_read_b128 a[168:171], v216 offset:30720\n\t"
"v_mfma_f32_16x16x32_bf16 v[208:211], a[176:179], a[208:211], 0\n\t"
"ds_read_b64_tr_b16 a[176:177], v248 offset:1152\n\t"
"v_mfma_f32_16x16x32_bf16 v[44:47], a[88:91], a[156:159], v[44:47]\n\t"
"ds_read_b64_tr_b16 a[178:179], v248 offset:1664\n\t"
"v_mfma_f32_16x16x32_bf16 v[44:47], a[92:95], a[172:175], v[44:47]\n\t"
"buffer_load_dwordx4 a[88:91], v235, s[8:11], s82 offen offset:2048 sc1\n\t"
"v_mfma_f32_16x16x32_bf16 v[212:215], a[180:183], a[208:211], 0\n\t"
"buffer_load_dwordx4 a[92:95], v235, s[8:11], s82 offen offset:3072 sc1\n\t"
"v_mfma_f32_16x16x32_bf16 v[28:31], a[80:83], a[156:159], v[28:31]\n\t"
"v_cvt_pk_bf16_f32 v208, v208, v209\n\t"
"v_cvt_pk_bf16_f32 v209, v210, v211\n\t"
"v_mfma_f32_16x16x32_bf16 v[28:31], a[84:87], a[172:175], v[28:31]\n\t"
"buffer_load_dwordx4 a[84:87], v235, s[8:11], s82 offen offset:1024 sc1\n\t"
"v_mfma_f32_16x16x32_bf16 v[12:15], a[72:75], a[156:159], v[12:15]\n\t"
"ds_read_b128 a[156:159], v216 offset:23552\n\t"
"v_mfma_f32_16x16x32_bf16 v[12:15], a[76:79], a[172:175], v[12:15]\n\t"
"buffer_load_dwordx4 a[80:83], v235, s[8:11], s82 offen offset:0 sc1\n\t"
"v_mfma_f32_16x16x32_bf16 v[192:195], a[184:187], a[208:211], 0\n\t"
"buffer_load_dwordx4 a[72:75], v218, s[8:11], s81 offen offset:2048 sc1\n\t"
"s_waitcnt vmcnt(29) lgkmcnt(4)\n\t"
"v_mfma_f32_16x16x32_bf16 v[144:147], a[96:99], a[144:147], v[144:147]\n\t"
"v_cvt_pk_bf16_f32 v210, v212, v213\n\t"
"v_cvt_pk_bf16_f32 v211, v214, v215\n\t"
"v_mfma_f32_16x16x32_bf16 v[144:147], a[100:103], a[160:163], v[144:147]\n\t"
"buffer_load_dwordx4 a[76:79], v218, s[8:11], s81 offen offset:3072 sc1\n\t"
"v_mfma_f32_16x16x32_bf16 v[196:199], a[188:191], a[208:211], 0\n\t"
"ds_read_b128 a[172:175], v216 offset:31744\n\t"
"v_mfma_f32_16x16x32_bf16 v[160:163], a[104:107], a[144:147], v[160:163]\n\t"
"v_cvt_pk_bf16_f32 v192, v192, v193\n\t"
"v_cvt_pk_bf16_f32 v193, v194, v195\n\t"
"v_mfma_f32_16x16x32_bf16 v[160:163], a[108:111], a[160:163], v[160:163]\n\t"
"ds_write_b128 v217, v[208:211] offset:0\n\t"
"v_mfma_f32_16x16x32_bf16 v[176:179], a[112:115], a[144:147], v[176:179]\n\t"
"ds_read_b128 a[144:147], v216 offset:16384\n\t"
"v_mfma_f32_16x16x32_bf16 v[176:179], a[116:119], a[160:163], v[176:179]\n\t"
"v_cvt_pk_bf16_f32 v194, v196, v197\n\t"
"v_cvt_pk_bf16_f32 v195, v198, v199\n\t"
"v_mfma_f32_16x16x32_bf16 v[200:203], a[192:195], a[208:211], 0\n\t"
"ds_read_b128 a[160:163], v216 offset:24576\n\t"
"v_mfma_f32_16x16x32_bf16 v[180:183], a[112:115], a[148:151], v[180:183]\n\t"
"ds_read_b64_tr_b16 a[180:181], v249 offset:1152\n\t"
"v_mfma_f32_16x16x32_bf16 v[180:183], a[116:119], a[164:167], v[180:183]\n\t"
"ds_write_b128 v219, v[192:195] offset:0\n\t"
"v_mfma_f32_16x16x32_bf16 v[164:167], a[104:107], a[148:151], v[164:167]\n\t"
"ds_read_b64_tr_b16 a[182:183], v249 offset:1664\n\t"
"v_mfma_f32_16x16x32_bf16 v[164:167], a[108:111], a[164:167], v[164:167]\n\t"
"v_cvt_pk_bf16_f32 v200, v200, v201\n\t"
"v_cvt_pk_bf16_f32 v201, v202, v203\n\t"
"v_mfma_f32_16x16x32_bf16 v[204:207], a[196:199], a[208:211], 0\n\t"
"ds_read_b64_tr_b16 a[184:185], v250 offset:3456\n\t"
"v_mfma_f32_16x16x32_bf16 v[148:151], a[96:99], a[148:151], v[148:151]\n\t"
"ds_read_b128 a[148:151], v216 offset:17408\n\t"
"v_mfma_f32_16x16x32_bf16 v[148:151], a[100:103], a[164:167], v[148:151]\n\t"
"ds_read_b128 a[164:167], v216 offset:25600\n\t"
"v_mfma_f32_16x16x32_bf16 v[208:211], a[200:203], a[208:211], 0\n\t"
"ds_read_b64_tr_b16 a[186:187], v250 offset:3968\n\t"
"s_waitcnt lgkmcnt(8)\n\t"
"v_mfma_f32_16x16x32_bf16 v[152:155], a[96:99], a[152:155], v[152:155]\n\t"
"v_cvt_pk_bf16_f32 v202, v204, v205\n\t"
"v_cvt_pk_bf16_f32 v203, v206, v207\n\t"
"v_mfma_f32_16x16x32_bf16 v[152:155], a[100:103], a[168:171], v[152:155]\n\t"
"ds_write_b128 v217, v[200:203] offset:8192\n\t"
"v_mfma_f32_16x16x32_bf16 v[168:171], a[104:107], a[152:155], v[168:171]\n\t"
"ds_read_b64_tr_b16 a[188:189], v251 offset:3456\n\t"
"v_mfma_f32_16x16x32_bf16 v[168:171], a[108:111], a[168:171], v[168:171]\n\t"
"v_cvt_pk_bf16_f32 v208, v208, v209\n\t"
"v_cvt_pk_bf16_f32 v209, v210, v211\n\t"
"v_mfma_f32_16x16x32_bf16 v[212:215], a[204:207], a[208:211], 0\n\t"
"ds_read_b64_tr_b16 a[190:191], v251 offset:3968\n\t"
"v_mfma_f32_16x16x32_bf16 v[184:187], a[112:115], a[152:155], v[184:187]\n\t"
"ds_read_b64_tr_b16 a[192:193], v250 offset:1152\n\t"
"v_mfma_f32_16x16x32_bf16 v[184:187], a[116:119], a[168:171], v[184:187]\n\t"
"ds_read_b64_tr_b16 a[194:195], v250 offset:1664\n\t"
"v_mfma_f32_16x16x32_bf16 v[188:191], a[112:115], a[156:159], v[188:191]\n\t"
"ds_read_b64_tr_b16 a[196:197], v251 offset:1152\n\t"
"v_mfma_f32_16x16x32_bf16 v[188:191], a[116:119], a[172:175], v[188:191]\n\t"
"buffer_load_dwordx4 a[112:115], v235, s[8:11], s56 offen offset:0 sc1\n\t"
"v_mfma_f32_16x16x32_bf16 v[172:175], a[104:107], a[156:159], v[172:175]\n\t"
"ds_read_b64_tr_b16 a[198:199], v251 offset:1664\n\t"
"v_cvt_pk_bf16_f32 v210, v212, v213\n\t"
"v_mfma_f32_16x16x32_bf16 v[172:175], a[108:111], a[172:175], v[172:175]\n\t"
"buffer_load_dwordx4 a[116:119], v235, s[8:11], s56 offen offset:1024 sc1\n\t"
"v_mfma_f32_16x16x32_bf16 v[156:159], a[96:99], a[156:159], v[156:159]\n\t"
"ds_read_b64_tr_b16 a[200:201], v248 offset:3456\n\t"
"v_cvt_pk_bf16_f32 v211, v214, v215\n\t"
"v_mfma_f32_16x16x32_bf16 v[156:159], a[100:103], a[172:175], v[156:159]\n\t"
"buffer_load_dwordx4 a[108:111], v218, s[8:11], s55 offen offset:3072 sc1\n\t"
"s_waitcnt vmcnt(27) lgkmcnt(6)\n\t"
"v_mfma_f32_16x16x32_bf16 v[96:99], a[120:123], a[144:147], v[96:99]\n\t"
"ds_read_b64_tr_b16 a[202:203], v248 offset:3968\n\t"
"v_mfma_f32_16x16x32_bf16 v[96:99], a[124:127], a[160:163], v[96:99]\n\t"
"ds_write_b128 v219, v[208:211] offset:8192\n\t"
"ds_read_b64_tr_b16 a[204:205], v249 offset:3456\n\t"
"ds_read_b64_tr_b16 a[206:207], v249 offset:3968\n\t"
"buffer_load_dwordx4 a[104:107], v218, s[8:11], s55 offen offset:2048 sc1\n\t"
"v_mfma_f32_16x16x32_bf16 v[112:115], a[128:131], a[144:147], v[112:115]\n\t"
"v_mfma_f32_16x16x32_bf16 v[112:115], a[132:135], a[160:163], v[112:115]\n\t"
"buffer_load_dwordx4 a[96:99], v218, s[8:11], s55 offen offset:0 sc1\n\t"
"buffer_load_dwordx4 a[100:103], v218, s[8:11], s55 offen offset:1024 sc1\n\t"
"v_pk_fma_f32 v[48:49], v[144:145], s[90:91], v[48:49]\n\t"
"v_pk_fma_f32 v[50:51], v[146:147], s[90:91], v[50:51]\n\t"
"v_pk_add_f32 v[144:145], v[48:49], v[144:145] neg_lo:[0,1] neg_hi:[0,1]\n\t"
"v_pk_add_f32 v[146:147], v[50:51], v[146:147] neg_lo:[0,1] neg_hi:[0,1]\n\t"
"v_pk_fma_f32 v[64:65], v[160:161], s[90:91], v[64:65]\n\t"
"v_pk_fma_f32 v[66:67], v[162:163], s[90:91], v[66:67]\n\t"
"v_pk_add_f32 v[160:161], v[64:65], v[160:161] neg_lo:[0,1] neg_hi:[0,1]\n\t"
"v_pk_add_f32 v[162:163], v[66:67], v[162:163] neg_lo:[0,1] neg_hi:[0,1]\n\t"
"v_pk_fma_f32 v[80:81], v[176:177], s[90:91], v[80:81]\n\t"
"v_pk_fma_f32 v[82:83], v[178:179], s[90:91], v[82:83]\n\t"
"v_pk_add_f32 v[176:177], v[80:81], v[176:177] neg_lo:[0,1] neg_hi:[0,1]\n\t"
"v_pk_add_f32 v[178:179], v[82:83], v[178:179] neg_lo:[0,1] neg_hi:[0,1]\n\t"
"v_pk_fma_f32 v[84:85], v[180:181], s[90:91], v[84:85]\n\t"
"v_pk_fma_f32 v[86:87], v[182:183], s[90:91], v[86:87]\n\t"
"v_pk_add_f32 v[180:181], v[84:85], v[180:181] neg_lo:[0,1] neg_hi:[0,1]\n\t"
"v_pk_add_f32 v[182:183], v[86:87], v[182:183] neg_lo:[0,1] neg_hi:[0,1]\n\t"
"v_pk_fma_f32 v[68:69], v[164:165], s[90:91], v[68:69]\n\t"
"v_pk_fma_f32 v[70:71], v[166:167], s[90:91], v[70:71]\n\t"
"v_pk_add_f32 v[164:165], v[68:69], v[164:165] neg_lo:[0,1] neg_hi:[0,1]\n\t"
"v_pk_add_f32 v[166:167], v[70:71], v[166:167] neg_lo:[0,1] neg_hi:[0,1]\n\t"
"v_pk_fma_f32 v[52:53], v[148:149], s[90:91], v[52:53]\n\t"
"v_pk_fma_f32 v[54:55], v[150:151], s[90:91], v[54:55]\n\t"
"v_pk_add_f32 v[148:149], v[52:53], v[148:149] neg_lo:[0,1] neg_hi:[0,1]\n\t"
"v_pk_add_f32 v[150:151], v[54:55], v[150:151] neg_lo:[0,1] neg_hi:[0,1]\n\t"
"v_pk_fma_f32 v[56:57], v[152:153], s[90:91], v[56:57]\n\t"
"v_pk_fma_f32 v[58:59], v[154:155], s[90:91], v[58:59]\n\t"
"v_pk_add_f32 v[152:153], v[56:57], v[152:153] neg_lo:[0,1] neg_hi:[0,1]\n\t"
"v_pk_add_f32 v[154:155], v[58:59], v[154:155] neg_lo:[0,1] neg_hi:[0,1]\n\t"
"v_pk_fma_f32 v[72:73], v[168:169], s[90:91], v[72:73]\n\t"
"v_pk_fma_f32 v[74:75], v[170:171], s[90:91], v[74:75]\n\t"
"v_pk_add_f32 v[168:169], v[72:73], v[168:169] neg_lo:[0,1] neg_hi:[0,1]\n\t"
"v_pk_add_f32 v[170:171], v[74:75], v[170:171] neg_lo:[0,1] neg_hi:[0,1]\n\t"
"v_pk_fma_f32 v[88:89], v[184:185], s[90:91], v[88:89]\n\t"
"v_pk_fma_f32 v[90:91], v[186:187], s[90:91], v[90:91]\n\t"
"v_pk_add_f32 v[184:185], v[88:89], v[184:185] neg_lo:[0,1] neg_hi:[0,1]\n\t"
"v_pk_add_f32 v[186:187], v[90:91], v[186:187] neg_lo:[0,1] neg_hi:[0,1]\n\t"
"v_pk_fma_f32 v[92:93], v[188:189], s[90:91], v[92:93]\n\t"
"v_pk_fma_f32 v[94:95], v[190:191], s[90:91], v[94:95]\n\t"
"v_pk_add_f32 v[188:189], v[92:93], v[188:189] neg_lo:[0,1] neg_hi:[0,1]\n\t"
"v_pk_add_f32 v[190:191], v[94:95], v[190:191] neg_lo:[0,1] neg_hi:[0,1]\n\t"
"v_pk_fma_f32 v[76:77], v[172:173], s[90:91], v[76:77]\n\t"
"v_pk_fma_f32 v[78:79], v[174:175], s[90:91], v[78:79]\n\t"
"v_pk_add_f32 v[172:173], v[76:77], v[172:173] neg_lo:[0,1] neg_hi:[0,1]\n\t"
"v_pk_add_f32 v[174:175], v[78:79], v[174:175] neg_lo:[0,1] neg_hi:[0,1]\n\t"
"v_pk_fma_f32 v[60:61], v[156:157], s[90:91], v[60:61]\n\t"
"v_pk_fma_f32 v[62:63], v[158:159], s[90:91], v[62:63]\n\t"
"v_pk_add_f32 v[156:157], v[60:61], v[156:157] neg_lo:[0,1] neg_hi:[0,1]\n\t"
"v_pk_add_f32 v[158:159], v[62:63], v[158:159] neg_lo:[0,1] neg_hi:[0,1]\n\t"
"v_mfma_f32_16x16x32_bf16 v[128:131], a[136:139], a[144:147], v[128:131]\n\t"
"v_mfma_f32_16x16x32_bf16 v[128:131], a[140:143], a[160:163], v[128:131]\n\t"
"ds_read_b128 a[152:155], v216 offset:18432\n\t"
"v_mfma_f32_16x16x32_bf16 v[132:135], a[136:139], a[148:151], v[132:135]\n\t"
"ds_read_b128 a[168:171], v216 offset:26624\n\t"
"v_mfma_f32_16x16x32_bf16 v[132:135], a[140:143], a[164:167], v[132:135]\n\t"
"ds_read_b128 a[156:159], v216 offset:19456\n\t"
"ds_read_b128 a[172:175], v216 offset:27648\n\t"
"v_mfma_f32_16x16x32_bf16 v[116:119], a[128:131], a[148:151], v[116:119]\n\t"
"v_mfma_f32_16x16x32_bf16 v[116:119], a[132:135], a[164:167], v[116:119]\n\t"
"v_mfma_f32_16x16x32_bf16 v[100:103], a[120:123], a[148:151], v[100:103]\n\t"
"v_mfma_f32_16x16x32_bf16 v[100:103], a[124:127], a[164:167], v[100:103]\n\t"
"s_waitcnt lgkmcnt(0)\n\t"
"s_barrier\n\t"
"v_mfma_f32_16x16x32_bf16 v[104:107], a[120:123], a[152:155], v[104:107]\n\t"
"ds_read_b128 a[144:147], v216 offset:4096\n\t"
"v_add_u32 v248, 80896, v225\n\t"
"v_mfma_f32_16x16x32_bf16 v[104:107], a[124:127], a[168:171], v[104:107]\n\t"
"s_add_u32 m0, s26, 34816\n\t"
"ds_read_b128 a[160:163], v216 offset:12288\n\t"
"ds_read_b128 a[148:151], v216 offset:5120\n\t"
"buffer_load_dwordx4 v236, s[12:15], s22 offen offset:256 nt lds\n\t"
"v_mfma_f32_16x16x32_bf16 v[120:123], a[128:131], a[152:155], v[120:123]\n\t"
"ds_read_b128 a[164:167], v216 offset:13312\n\t"
"v_mfma_f32_16x16x32_bf16 v[120:123], a[132:135], a[168:171], v[120:123]\n\t"
"v_mfma_f32_16x16x32_bf16 v[136:139], a[136:139], a[152:155], v[136:139]\n\t"
"v_xor_b32 v249, 32, v248\n\t"
"v_xor_b32 v250, 64, v248\n\t"
"v_mfma_f32_16x16x32_bf16 v[136:139], a[140:143], a[168:171], v[136:139]\n\t"
"v_xor_b32 v251, 96, v248\n\t"
"v_mfma_f32_16x16x32_bf16 v[192:195], a[176:179], a[208:211], 0\n\t"
"ds_read_b128 a[228:231], v222 offset:39168\n\t"
"v_mfma_f32_16x16x32_bf16 v[140:143], a[136:139], a[156:159], v[140:143]\n\t"
"ds_read_b128 a[152:155], v216 offset:6144\n\t"
"v_mfma_f32_16x16x32_bf16 v[140:143], a[140:143], a[172:175], v[140:143]\n\t"
"buffer_load_dwordx4 v237, s[12:15], s22 offen offset:1280 nt lds\n\t"
"v_mfma_f32_16x16x32_bf16 v[196:199], a[180:183], a[208:211], 0\n\t"
"ds_read_b128 a[168:171], v216 offset:14336\n\t"
"v_mfma_f32_16x16x32_bf16 v[124:127], a[128:131], a[156:159], v[124:127]\n\t"
"v_cvt_pk_bf16_f32 v192, v192, v193\n\t"
"v_cvt_pk_bf16_f32 v193, v194, v195\n\t"
"v_mfma_f32_16x16x32_bf16 v[124:127], a[132:135], a[172:175], v[124:127]\n\t"
"ds_read_b64_tr_b16 a[176:177], v248 offset:41472\n\t"
"v_mfma_f32_16x16x32_bf16 v[108:111], a[120:123], a[156:159], v[108:111]\n\t"
"ds_read_b128 a[156:159], v216 offset:7168\n\t"
"v_mfma_f32_16x16x32_bf16 v[108:111], a[124:127], a[172:175], v[108:111]\n\t"
"buffer_load_dwordx4 v238, s[12:15], s22 offen offset:2304 nt lds\n\t"
"v_mfma_f32_16x16x32_bf16 v[200:203], a[184:187], a[208:211], 0\n\t"
"v_cvt_pk_bf16_f32 v194, v196, v197\n\t"
"v_cvt_pk_bf16_f32 v195, v198, v199\n\t"
"s_waitcnt vmcnt(27) lgkmcnt(5)\n\t"
"v_mfma_f32_16x16x32_bf16 v[48:51], a[0:3], a[144:147], v[48:51]\n\t"
"ds_read_b128 a[172:175], v216 offset:15360\n\t"
"v_mfma_f32_16x16x32_bf16 v[48:51], a[4:7], a[160:163], v[48:51]\n\t"
"ds_write_b128 v217, v[192:195] offset:16384\n\t"
"v_mfma_f32_16x16x32_bf16 v[204:207], a[188:191], a[208:211], 0\n\t"
"v_pk_fma_f32 v[0:1], v[96:97], s[90:91], v[0:1]\n\t"
"v_pk_fma_f32 v[2:3], v[98:99], s[90:91], v[2:3]\n\t"
"v_pk_add_f32 v[96:97], v[0:1], v[96:97] neg_lo:[0,1] neg_hi:[0,1]\n\t"
"v_pk_add_f32 v[98:99], v[2:3], v[98:99] neg_lo:[0,1] neg_hi:[0,1]\n\t"
"v_pk_fma_f32 v[16:17], v[112:113], s[90:91], v[16:17]\n\t"
"v_pk_fma_f32 v[18:19], v[114:115], s[90:91], v[18:19]\n\t"
"v_pk_add_f32 v[112:113], v[16:17], v[112:113] neg_lo:[0,1] neg_hi:[0,1]\n\t"
"v_pk_add_f32 v[114:115], v[18:19], v[114:115] neg_lo:[0,1] neg_hi:[0,1]\n\t"
"v_pk_fma_f32 v[32:33], v[128:129], s[90:91], v[32:33]\n\t"
"v_pk_fma_f32 v[34:35], v[130:131], s[90:91], v[34:35]\n\t"
"v_pk_add_f32 v[128:129], v[32:33], v[128:129] neg_lo:[0,1] neg_hi:[0,1]\n\t"
"v_pk_add_f32 v[130:131], v[34:35], v[130:131] neg_lo:[0,1] neg_hi:[0,1]\n\t"
"v_pk_fma_f32 v[36:37], v[132:133], s[90:91], v[36:37]\n\t"
"v_pk_fma_f32 v[38:39], v[134:135], s[90:91], v[38:39]\n\t"
"v_pk_add_f32 v[132:133], v[36:37], v[132:133] neg_lo:[0,1] neg_hi:[0,1]\n\t"
"v_pk_add_f32 v[134:135], v[38:39], v[134:135] neg_lo:[0,1] neg_hi:[0,1]\n\t"
"v_pk_fma_f32 v[20:21], v[116:117], s[90:91], v[20:21]\n\t"
"v_pk_fma_f32 v[22:23], v[118:119], s[90:91], v[22:23]\n\t"
"v_pk_add_f32 v[116:117], v[20:21], v[116:117] neg_lo:[0,1] neg_hi:[0,1]\n\t"
"v_pk_add_f32 v[118:119], v[22:23], v[118:119] neg_lo:[0,1] neg_hi:[0,1]\n\t"
"v_pk_fma_f32 v[4:5], v[100:101], s[90:91], v[4:5]\n\t"
"v_pk_fma_f32 v[6:7], v[102:103], s[90:91], v[6:7]\n\t"
"v_pk_add_f32 v[100:101], v[4:5], v[100:101] neg_lo:[0,1] neg_hi:[0,1]\n\t"
"v_pk_add_f32 v[102:103], v[6:7], v[102:103] neg_lo:[0,1] neg_hi:[0,1]\n\t"
"v_pk_fma_f32 v[8:9], v[104:105], s[90:91], v[8:9]\n\t"
"v_pk_fma_f32 v[10:11], v[106:107], s[90:91], v[10:11]\n\t"
"v_pk_add_f32 v[104:105], v[8:9], v[104:105] neg_lo:[0,1] neg_hi:[0,1]\n\t"
"v_pk_add_f32 v[106:107], v[10:11], v[106:107] neg_lo:[0,1] neg_hi:[0,1]\n\t"
"v_pk_fma_f32 v[24:25], v[120:121], s[90:91], v[24:25]\n\t"
"v_pk_fma_f32 v[26:27], v[122:123], s[90:91], v[26:27]\n\t"
"v_pk_add_f32 v[120:121], v[24:25], v[120:121] neg_lo:[0,1] neg_hi:[0,1]\n\t"
"v_pk_add_f32 v[122:123], v[26:27], v[122:123] neg_lo:[0,1] neg_hi:[0,1]\n\t"
"v_pk_fma_f32 v[40:41], v[136:137], s[90:91], v[40:41]\n\t"
"v_pk_fma_f32 v[42:43], v[138:139], s[90:91], v[42:43]\n\t"
"v_pk_add_f32 v[136:137], v[40:41], v[136:137] neg_lo:[0,1] neg_hi:[0,1]\n\t"
"v_pk_add_f32 v[138:139], v[42:43], v[138:139] neg_lo:[0,1] neg_hi:[0,1]\n\t"
"v_pk_fma_f32 v[44:45], v[140:141], s[90:91], v[44:45]\n\t"
"v_pk_fma_f32 v[46:47], v[142:143], s[90:91], v[46:47]\n\t"
"v_pk_add_f32 v[140:141], v[44:45], v[140:141] neg_lo:[0,1] neg_hi:[0,1]\n\t"
"v_pk_add_f32 v[142:143], v[46:47], v[142:143] neg_lo:[0,1] neg_hi:[0,1]\n\t"
"v_pk_fma_f32 v[28:29], v[124:125], s[90:91], v[28:29]\n\t"
"v_pk_fma_f32 v[30:31], v[126:127], s[90:91], v[30:31]\n\t"
"v_pk_add_f32 v[124:125], v[28:29], v[124:125] neg_lo:[0,1] neg_hi:[0,1]\n\t"
"v_pk_add_f32 v[126:127], v[30:31], v[126:127] neg_lo:[0,1] neg_hi:[0,1]\n\t"
"v_pk_fma_f32 v[12:13], v[108:109], s[90:91], v[12:13]\n\t"
"v_pk_fma_f32 v[14:15], v[110:111], s[90:91], v[14:15]\n\t"
"v_pk_add_f32 v[108:109], v[12:13], v[108:109] neg_lo:[0,1] neg_hi:[0,1]\n\t"
"v_pk_add_f32 v[110:111], v[14:15], v[110:111] neg_lo:[0,1] neg_hi:[0,1]\n\t"
"ds_read_b64_tr_b16 a[178:179], v248 offset:41984\n\t"
"v_mfma_f32_16x16x32_bf16 v[64:67], a[8:11], a[144:147], v[64:67]\n\t"
"v_cvt_pk_bf16_f32 v200, v200, v201\n\t"
"v_mfma_f32_16x16x32_bf16 v[64:67], a[12:15], a[160:163], v[64:67]\n\t"
"buffer_load_dwordx4 v239, s[12:15], s22 offen offset:3328 nt lds\n\t"
"v_mfma_f32_16x16x32_bf16 v[80:83], a[16:19], a[144:147], v[80:83]\n\t"
"v_cvt_pk_bf16_f32 v201, v202, v203\n\t"
"v_mfma_f32_16x16x32_bf16 v[80:83], a[20:23], a[160:163], v[80:83]\n\t"
"v_cvt_pk_bf16_f32 v202, v204, v205\n\t"
"v_cvt_pk_bf16_f32 v203, v206, v207\n\t"
"v_mfma_f32_16x16x32_bf16 v[208:211], a[192:195], a[208:211], 0\n\t"
"ds_read_b128 a[144:147], v216 offset:0\n\t"
"v_mfma_f32_16x16x32_bf16 v[84:87], a[16:19], a[148:151], v[84:87]\n\t"
"ds_read_b128 a[160:163], v216 offset:8192\n\t"
"v_mfma_f32_16x16x32_bf16 v[84:87], a[20:23], a[164:167], v[84:87]\n\t"
"ds_write_b128 v219, v[200:203] offset:16384\n\t"
"v_mfma_f32_16x16x32_bf16 v[68:71], a[8:11], a[148:151], v[68:71]\n\t"
"ds_read_b64_tr_b16 a[180:181], v249 offset:41472\n\t"
"v_mfma_f32_16x16x32_bf16 v[68:71], a[12:15], a[164:167], v[68:71]\n\t"
"s_mov_b64 exec, s[36:37]\n\t"
"buffer_load_dwordx4 v240, s[12:15], s22 offen offset:3840 nt lds\n\t"
"s_mov_b64 exec, -1\n\t"
"v_mfma_f32_16x16x32_bf16 v[212:215], a[196:199], a[208:211], 0\n\t"
"v_cvt_pk_bf16_f32 v208, v208, v209\n\t"
"v_mfma_f32_16x16x32_bf16 v[52:55], a[0:3], a[148:151], v[52:55]\n\t"
"v_cvt_pk_bf16_f32 v209, v210, v211\n\t"
"ds_read_b128 a[148:151], v216 offset:1024\n\t"
"v_mfma_f32_16x16x32_bf16 v[52:55], a[4:7], a[164:167], v[52:55]\n\t"
"buffer_load_dwordx4 a[136:139], v235, s[8:11], s55 offen offset:2048 sc1\n\t"
"v_mfma_f32_16x16x32_bf16 v[192:195], a[200:203], a[208:211], 0\n\t"
"s_waitcnt lgkmcnt(5)\n\t"
"v_mfma_f32_16x16x32_bf16 v[56:59], a[0:3], a[152:155], v[56:59]\n\t"
"ds_read_b128 a[164:167], v216 offset:9216\n\t"
"v_cvt_pk_bf16_f32 v210, v212, v213\n\t"
"v_mfma_f32_16x16x32_bf16 v[56:59], a[4:7], a[168:171], v[56:59]\n\t"
"v_cvt_pk_bf16_f32 v211, v214, v215\n\t"
"v_mfma_f32_16x16x32_bf16 v[72:75], a[8:11], a[152:155], v[72:75]\n\t"
"ds_read_b64_tr_b16 a[182:183], v249 offset:41984\n\t"
"v_mfma_f32_16x16x32_bf16 v[72:75], a[12:15], a[168:171], v[72:75]\n\t"
"v_cvt_pk_bf16_f32 v192, v192, v193\n\t"
"v_cvt_pk_bf16_f32 v193, v194, v195\n\t"
"v_mfma_f32_16x16x32_bf16 v[196:199], a[204:207], a[208:211], 0\n\t"
"ds_read_b64_tr_b16 a[184:185], v250 offset:43776\n\t"
"v_mfma_f32_16x16x32_bf16 v[88:91], a[16:19], a[152:155], v[88:91]\n\t"
"ds_read_b64_tr_b16 a[186:187], v250 offset:44288\n\t"
"v_mfma_f32_16x16x32_bf16 v[88:91], a[20:23], a[168:171], v[88:91]\n\t"
"ds_write_b128 v217, v[208:211] offset:24576\n\t"
"v_mfma_f32_16x16x32_bf16 v[92:95], a[16:19], a[156:159], v[92:95]\n\t"
"ds_read_b64_tr_b16 a[188:189], v251 offset:43776\n\t"
"v_mfma_f32_16x16x32_bf16 v[92:95], a[20:23], a[172:175], v[92:95]\n\t"
"ds_read_b64_tr_b16 a[190:191], v251 offset:44288\n\t"
"v_cvt_pk_bf16_f32 v194, v196, v197\n\t"
"v_mfma_f32_16x16x32_bf16 v[76:79], a[8:11], a[156:159], v[76:79]\n\t"
"ds_read_b64_tr_b16 a[192:193], v250 offset:41472\n\t"
"v_cvt_pk_bf16_f32 v195, v198, v199\n\t"
"v_mfma_f32_16x16x32_bf16 v[76:79], a[12:15], a[172:175], v[76:79]\n\t"
"ds_read_b64_tr_b16 a[194:195], v250 offset:41984\n\t"
"v_mfma_f32_16x16x32_bf16 v[60:63], a[0:3], a[156:159], v[60:63]\n\t"
"ds_read_b64_tr_b16 a[196:197], v251 offset:41472\n\t"
"v_mfma_f32_16x16x32_bf16 v[60:63], a[4:7], a[172:175], v[60:63]\n\t"
"ds_write_b128 v219, v[192:195] offset:24576\n\t"
"s_waitcnt vmcnt(24) lgkmcnt(9)\n\t"
"v_mfma_f32_16x16x32_bf16 v[0:3], a[24:27], a[144:147], v[0:3]\n\t"
"ds_read_b64_tr_b16 a[198:199], v251 offset:41984\n\t"
"v_mfma_f32_16x16x32_bf16 v[0:3], a[28:31], a[160:163], v[0:3]\n\t"
"ds_read_b64_tr_b16 a[200:201], v248 offset:43776\n\t"
"ds_read_b64_tr_b16 a[202:203], v248 offset:44288\n\t"
"ds_read_b64_tr_b16 a[204:205], v249 offset:43776\n\t"
"ds_read_b64_tr_b16 a[206:207], v249 offset:44288\n\t"
"v_mfma_f32_16x16x32_bf16 v[16:19], a[32:35], a[144:147], v[16:19]\n\t"
"v_mfma_f32_16x16x32_bf16 v[16:19], a[36:39], a[160:163], v[16:19]\n\t"
"v_mfma_f32_16x16x32_bf16 v[32:35], a[40:43], a[144:147], v[32:35]\n\t"
"ds_read_b128 a[152:155], v216 offset:2048\n\t"
"v_mfma_f32_16x16x32_bf16 v[32:35], a[44:47], a[160:163], v[32:35]\n\t"
"ds_read_b128 a[168:171], v216 offset:10240\n\t"
"v_mfma_f32_16x16x32_bf16 v[36:39], a[40:43], a[148:151], v[36:39]\n\t"
"ds_read_b128 a[156:159], v216 offset:3072\n\t"
"v_mfma_f32_16x16x32_bf16 v[36:39], a[44:47], a[164:167], v[36:39]\n\t"
"ds_read_b128 a[172:175], v216 offset:11264\n\t"
"ds_write_b128 v222, a[228:231] offset:0\n\t"
"v_mfma_f32_16x16x32_bf16 v[20:23], a[32:35], a[148:151], v[20:23]\n\t"
"v_mfma_f32_16x16x32_bf16 v[20:23], a[36:39], a[164:167], v[20:23]\n\t"
"v_mfma_f32_16x16x32_bf16 v[4:7], a[24:27], a[148:151], v[4:7]\n\t"
"v_mfma_f32_16x16x32_bf16 v[4:7], a[28:31], a[164:167], v[4:7]\n\t"
"s_waitcnt lgkmcnt(0)\n\t"
"s_barrier\n\t"
"v_mfma_f32_16x16x32_bf16 v[8:11], a[24:27], a[152:155], v[8:11]\n\t"
"ds_read_b128 a[144:147], v216 offset:20480\n\t"
"v_mfma_f32_16x16x32_bf16 v[8:11], a[28:31], a[168:171], v[8:11]\n\t"
"ds_read_b128 a[160:163], v216 offset:28672\n\t"
"ds_read_b128 a[148:151], v216 offset:21504\n\t"
"v_mfma_f32_16x16x32_bf16 v[24:27], a[32:35], a[152:155], v[24:27]\n\t"
"ds_read_b128 a[164:167], v216 offset:29696\n\t"
"v_mfma_f32_16x16x32_bf16 v[24:27], a[36:39], a[168:171], v[24:27]\n\t"
"buffer_load_dwordx4 a[140:143], v235, s[8:11], s55 offen offset:3072 sc1\n\t"
"v_mfma_f32_16x16x32_bf16 v[40:43], a[40:43], a[152:155], v[40:43]\n\t"
"ds_read_b128 a[152:155], v216 offset:22528\n\t"
"v_mfma_f32_16x16x32_bf16 v[40:43], a[44:47], a[168:171], v[40:43]\n\t"
"buffer_load_dwordx4 a[132:135], v235, s[8:11], s55 offen offset:1024 sc1\n\t"
"v_mfma_f32_16x16x32_bf16 v[200:203], a[176:179], a[208:211], 0\n\t"
"buffer_load_dwordx4 a[128:131], v235, s[8:11], s55 offen offset:0 sc1\n\t"
"v_mfma_f32_16x16x32_bf16 v[44:47], a[40:43], a[156:159], v[44:47]\n\t"
"ds_read_b128 a[168:171], v216 offset:30720\n\t"
"v_mfma_f32_16x16x32_bf16 v[44:47], a[44:47], a[172:175], v[44:47]\n\t"
"ds_read_b64_tr_b16 a[176:177], v248 offset:0\n\t"
"v_mfma_f32_16x16x32_bf16 v[204:207], a[180:183], a[208:211], 0\n\t"
"buffer_load_dwordx4 a[120:123], v218, s[8:11], s54 offen offset:2048 sc1\n\t"
"v_mfma_f32_16x16x32_bf16 v[28:31], a[32:35], a[156:159], v[28:31]\n\t"
"v_cvt_pk_bf16_f32 v200, v200, v201\n\t"
"v_cvt_pk_bf16_f32 v201, v202, v203\n\t"
"v_mfma_f32_16x16x32_bf16 v[28:31], a[36:39], a[172:175], v[28:31]\n\t"
"ds_read_b64_tr_b16 a[178:179], v248 offset:512\n\t"
"v_mfma_f32_16x16x32_bf16 v[12:15], a[24:27], a[156:159], v[12:15]\n\t"
"ds_read_b128 a[156:159], v216 offset:23552\n\t"
"v_mfma_f32_16x16x32_bf16 v[12:15], a[28:31], a[172:175], v[12:15]\n\t"
"buffer_load_dwordx4 a[124:127], v218, s[8:11], s54 offen offset:3072 sc1\n\t"
"v_mfma_f32_16x16x32_bf16 v[208:211], a[184:187], a[208:211], 0\n\t"
"v_cvt_pk_bf16_f32 v202, v204, v205\n\t"
"v_cvt_pk_bf16_f32 v203, v206, v207\n\t"
"s_waitcnt vmcnt(23) lgkmcnt(4)\n\t"
"v_mfma_f32_16x16x32_bf16 v[144:147], a[48:51], a[144:147], v[144:147]\n\t"
"ds_read_b128 a[172:175], v216 offset:31744\n\t"
"v_mfma_f32_16x16x32_bf16 v[144:147], a[52:55], a[160:163], v[144:147]\n\t"
"buffer_load_dwordx4 a[16:19], v235, s[8:11], s41 offen offset:0 sc1\n\t"
"v_mfma_f32_16x16x32_bf16 v[212:215], a[188:191], a[208:211], 0\n\t"
"ds_write_b128 v217, v[200:203] offset:0\n\t"
"v_mfma_f32_16x16x32_bf16 v[160:163], a[56:59], a[144:147], v[160:163]\n\t"
"v_cvt_pk_bf16_f32 v208, v208, v209\n\t"
"v_cvt_pk_bf16_f32 v209, v210, v211\n\t"
"v_mfma_f32_16x16x32_bf16 v[160:163], a[60:63], a[160:163], v[160:163]\n\t"
"ds_read_b64_tr_b16 a[180:181], v249 offset:0\n\t"
"v_mfma_f32_16x16x32_bf16 v[176:179], a[64:67], a[144:147], v[176:179]\n\t"
"ds_read_b128 a[144:147], v216 offset:16384\n\t"
"v_mfma_f32_16x16x32_bf16 v[176:179], a[68:71], a[160:163], v[176:179]\n\t"
"buffer_load_dwordx4 a[20:23], v235, s[8:11], s41 offen offset:1024 sc1\n\t"
"v_mfma_f32_16x16x32_bf16 v[192:195], a[192:195], a[208:211], 0\n\t"
"v_cvt_pk_bf16_f32 v210, v212, v213\n\t"
"v_cvt_pk_bf16_f32 v211, v214, v215\n\t"
"v_mfma_f32_16x16x32_bf16 v[180:183], a[64:67], a[148:151], v[180:183]\n\t"
"ds_read_b128 a[160:163], v216 offset:24576\n\t"
"v_mfma_f32_16x16x32_bf16 v[180:183], a[68:71], a[164:167], v[180:183]\n\t"
"buffer_load_dwordx4 a[12:15], v218, s[8:11], s40 offen offset:3072 sc1\n\t"
"v_mfma_f32_16x16x32_bf16 v[164:167], a[56:59], a[148:151], v[164:167]\n\t"
"ds_read_b64_tr_b16 a[182:183], v249 offset:512\n\t"
"v_mfma_f32_16x16x32_bf16 v[164:167], a[60:63], a[164:167], v[164:167]\n\t"
"ds_write_b128 v219, v[208:211] offset:0\n\t"
"v_mfma_f32_16x16x32_bf16 v[196:199], a[196:199], a[208:211], 0\n\t"
"buffer_load_dwordx4 a[8:11], v218, s[8:11], s40 offen offset:2048 sc1\n\t"
"v_mfma_f32_16x16x32_bf16 v[148:151], a[48:51], a[148:151], v[148:151]\n\t"
"v_cvt_pk_bf16_f32 v192, v192, v193\n\t"
"v_cvt_pk_bf16_f32 v193, v194, v195\n\t"
"v_mfma_f32_16x16x32_bf16 v[148:151], a[52:55], a[164:167], v[148:151]\n\t"
"ds_read_b128 a[148:151], v216 offset:17408\n\t"
"v_mfma_f32_16x16x32_bf16 v[200:203], a[200:203], a[208:211], 0\n\t"
"ds_read_b128 a[164:167], v216 offset:25600\n\t"
"s_waitcnt lgkmcnt(5)\n\t"
"v_mfma_f32_16x16x32_bf16 v[152:155], a[48:51], a[152:155], v[152:155]\n\t"
"v_cvt_pk_bf16_f32 v194, v196, v197\n\t"
"v_cvt_pk_bf16_f32 v195, v198, v199\n\t"
"v_mfma_f32_16x16x32_bf16 v[152:155], a[52:55], a[168:171], v[152:155]\n\t"
"buffer_load_dwordx4 a[0:3], v218, s[8:11], s40 offen offset:0 sc1\n\t"
"v_mfma_f32_16x16x32_bf16 v[168:171], a[56:59], a[152:155], v[168:171]\n\t"
"ds_read_b64_tr_b16 a[184:185], v250 offset:2304\n\t"
"v_mfma_f32_16x16x32_bf16 v[168:171], a[60:63], a[168:171], v[168:171]\n\t"
"buffer_load_dwordx4 a[4:7], v218, s[8:11], s40 offen offset:1024 sc1\n\t"
"v_mfma_f32_16x16x32_bf16 v[204:207], a[204:207], a[208:211], 0\n\t"
"v_cvt_pk_bf16_f32 v200, v200, v201\n\t"
"v_cvt_pk_bf16_f32 v201, v202, v203\n\t"
"v_mfma_f32_16x16x32_bf16 v[184:187], a[64:67], a[152:155], v[184:187]\n\t"
"ds_read_b64_tr_b16 a[186:187], v250 offset:2816\n\t"
"v_mfma_f32_16x16x32_bf16 v[184:187], a[68:71], a[168:171], v[184:187]\n\t"
"ds_write_b128 v217, v[192:195] offset:8192\n\t"
"v_mfma_f32_16x16x32_bf16 v[188:191], a[64:67], a[156:159], v[188:191]\n\t"
"ds_read_b64_tr_b16 a[188:189], v251 offset:2304\n\t"
"v_mfma_f32_16x16x32_bf16 v[188:191], a[68:71], a[172:175], v[188:191]\n\t"
"buffer_load_dwordx4 a[40:43], v235, s[8:11], s40 offen offset:2048 sc1\n\t"
"v_mfma_f32_16x16x32_bf16 v[172:175], a[56:59], a[156:159], v[172:175]\n\t"
"ds_read_b64_tr_b16 a[190:191], v251 offset:2816\n\t"
"v_cvt_pk_bf16_f32 v202, v204, v205\n\t"
"v_mfma_f32_16x16x32_bf16 v[172:175], a[60:63], a[172:175], v[172:175]\n\t"
"buffer_load_dwordx4 a[44:47], v235, s[8:11], s40 offen offset:3072 sc1\n\t"
"v_mfma_f32_16x16x32_bf16 v[156:159], a[48:51], a[156:159], v[156:159]\n\t"
"ds_read_b64_tr_b16 a[192:193], v250 offset:0\n\t"
"v_cvt_pk_bf16_f32 v203, v206, v207\n\t"
"v_mfma_f32_16x16x32_bf16 v[156:159], a[52:55], a[172:175], v[156:159]\n\t"
"ds_write_b128 v219, v[200:203] offset:8192\n\t"
"s_waitcnt vmcnt(25) lgkmcnt(6)\n\t"
"v_mfma_f32_16x16x32_bf16 v[96:99], a[72:75], a[144:147], v[96:99]\n\t"
"ds_read_b64_tr_b16 a[194:195], v250 offset:512\n\t"
"v_mfma_f32_16x16x32_bf16 v[96:99], a[76:79], a[160:163], v[96:99]\n\t"
"ds_read_b64_tr_b16 a[196:197], v251 offset:0\n\t"
"ds_read_b64_tr_b16 a[198:199], v251 offset:512\n\t"
"ds_read_b64_tr_b16 a[200:201], v248 offset:2304\n\t"
"ds_read_b64_tr_b16 a[202:203], v248 offset:2816\n\t"
"ds_read_b64_tr_b16 a[204:205], v249 offset:2304\n\t"
"ds_read_b64_tr_b16 a[206:207], v249 offset:2816\n\t"
"v_mfma_f32_16x16x32_bf16 v[112:115], a[80:83], a[144:147], v[112:115]\n\t"
"ds_read_b128 a[152:155], v216 offset:18432\n\t"
"v_mfma_f32_16x16x32_bf16 v[112:115], a[84:87], a[160:163], v[112:115]\n\t"
"buffer_load_dwordx4 a[36:39], v235, s[8:11], s40 offen offset:1024 sc1\n\t"
"v_mfma_f32_16x16x32_bf16 v[128:131], a[88:91], a[144:147], v[128:131]\n\t"
"ds_read_b128 a[168:171], v216 offset:26624\n\t"
"v_mfma_f32_16x16x32_bf16 v[128:131], a[92:95], a[160:163], v[128:131]\n\t"
"ds_read_b128 a[156:159], v216 offset:19456\n\t"
"v_mfma_f32_16x16x32_bf16 v[132:135], a[88:91], a[148:151], v[132:135]\n\t"
"ds_read_b128 a[172:175], v216 offset:27648\n\t"
"v_mfma_f32_16x16x32_bf16 v[132:135], a[92:95], a[164:167], v[132:135]\n\t"
"buffer_load_dwordx4 a[32:35], v235, s[8:11], s40 offen offset:0 sc1\n\t"
"v_mfma_f32_16x16x32_bf16 v[116:119], a[80:83], a[148:151], v[116:119]\n\t"
"v_mfma_f32_16x16x32_bf16 v[116:119], a[84:87], a[164:167], v[116:119]\n\t"
"buffer_load_dwordx4 a[24:27], v218, s[8:11], 0 offen offset:2048 sc1\n\t"
"v_mfma_f32_16x16x32_bf16 v[100:103], a[72:75], a[148:151], v[100:103]\n\t"
"v_mfma_f32_16x16x32_bf16 v[100:103], a[76:79], a[164:167], v[100:103]\n\t"
"s_waitcnt lgkmcnt(0)\n\t"
"s_barrier\n\t"
"v_mfma_f32_16x16x32_bf16 v[104:107], a[72:75], a[152:155], v[104:107]\n\t"
"ds_read_b128 a[144:147], v216 offset:4096\n\t"
"v_mfma_f32_16x16x32_bf16 v[104:107], a[76:79], a[168:171], v[104:107]\n\t"
"ds_read_b128 a[160:163], v216 offset:12288\n\t"
"ds_read_b128 a[148:151], v216 offset:5120\n\t"
"v_mfma_f32_16x16x32_bf16 v[120:123], a[80:83], a[152:155], v[120:123]\n\t"
"ds_read_b128 a[164:167], v216 offset:13312\n\t"
"v_mfma_f32_16x16x32_bf16 v[120:123], a[84:87], a[168:171], v[120:123]\n\t"
"buffer_load_dwordx4 a[28:31], v218, s[8:11], 0 offen offset:3072 sc1\n\t"
"v_mfma_f32_16x16x32_bf16 v[136:139], a[88:91], a[152:155], v[136:139]\n\t"
"ds_read_b128 a[152:155], v216 offset:6144\n\t"
"v_mfma_f32_16x16x32_bf16 v[136:139], a[92:95], a[168:171], v[136:139]\n\t"
"buffer_load_dwordx4 a[64:67], v235, s[8:11], s68 offen offset:0 sc1\n\t"
"v_mfma_f32_16x16x32_bf16 v[208:211], a[176:179], a[208:211], 0\n\t"
"buffer_load_dwordx4 a[68:71], v235, s[8:11], s68 offen offset:1024 sc1\n\t"
"v_mfma_f32_16x16x32_bf16 v[140:143], a[88:91], a[156:159], v[140:143]\n\t"
"ds_read_b128 a[168:171], v216 offset:14336\n\t"
"v_mfma_f32_16x16x32_bf16 v[140:143], a[92:95], a[172:175], v[140:143]\n\t"
"buffer_load_dwordx4 a[60:63], v218, s[8:11], s67 offen offset:3072 sc1\n\t"
"v_mfma_f32_16x16x32_bf16 v[212:215], a[180:183], a[208:211], 0\n\t"
"ds_read_b64_tr_b16 a[176:177], v248 offset:42624\n\t"
"v_mfma_f32_16x16x32_bf16 v[124:127], a[80:83], a[156:159], v[124:127]\n\t"
"v_cvt_pk_bf16_f32 v208, v208, v209\n\t"
"v_cvt_pk_bf16_f32 v209, v210, v211\n\t"
"v_mfma_f32_16x16x32_bf16 v[124:127], a[84:87], a[172:175], v[124:127]\n\t"
"buffer_load_dwordx4 a[56:59], v218, s[8:11], s67 offen offset:2048 sc1\n\t"
"v_mfma_f32_16x16x32_bf16 v[108:111], a[72:75], a[156:159], v[108:111]\n\t"
"ds_read_b128 a[156:159], v216 offset:7168\n\t"
"v_mfma_f32_16x16x32_bf16 v[108:111], a[76:79], a[172:175], v[108:111]\n\t"
"v_cvt_pk_bf16_f32 v210, v212, v213\n\t"
"v_cvt_pk_bf16_f32 v211, v214, v215\n\t"
"v_mfma_f32_16x16x32_bf16 v[192:195], a[184:187], a[208:211], 0\n\t"
"ds_read_b128 a[172:175], v216 offset:15360\n\t"
"s_waitcnt vmcnt(27) lgkmcnt(4)\n\t"
"v_mfma_f32_16x16x32_bf16 v[48:51], a[96:99], a[144:147], v[48:51]\n\t"
"ds_read_b64_tr_b16 a[178:179], v248 offset:43136\n\t"
"v_mfma_f32_16x16x32_bf16 v[48:51], a[100:103], a[160:163], v[48:51]\n\t"
"buffer_load_dwordx4 a[48:51], v218, s[8:11], s67 offen offset:0 sc1\n\t"
"v_mfma_f32_16x16x32_bf16 v[196:199], a[188:191], a[208:211], 0\n\t"
"ds_write_b128 v217, v[208:211] offset:16384\n\t"
"v_mfma_f32_16x16x32_bf16 v[64:67], a[104:107], a[144:147], v[64:67]\n\t"
"v_cvt_pk_bf16_f32 v192, v192, v193\n\t"
"v_cvt_pk_bf16_f32 v193, v194, v195\n\t"
"v_mfma_f32_16x16x32_bf16 v[64:67], a[108:111], a[160:163], v[64:67]\n\t"
"buffer_load_dwordx4 a[52:55], v218, s[8:11], s67 offen offset:1024 sc1\n\t"
"v_mfma_f32_16x16x32_bf16 v[80:83], a[112:115], a[144:147], v[80:83]\n\t"
"ds_read_b128 a[144:147], v216 offset:0\n\t"
"v_mfma_f32_16x16x32_bf16 v[80:83], a[116:119], a[160:163], v[80:83]\n\t"
"v_cvt_pk_bf16_f32 v194, v196, v197\n\t"
"v_cvt_pk_bf16_f32 v195, v198, v199\n\t"
"v_mfma_f32_16x16x32_bf16 v[200:203], a[192:195], a[208:211], 0\n\t"
"buffer_load_dwordx4 a[88:91], v235, s[8:11], s67 offen offset:2048 sc1\n\t"
"v_mfma_f32_16x16x32_bf16 v[84:87], a[112:115], a[148:151], v[84:87]\n\t"
"ds_read_b128 a[160:163], v216 offset:8192\n\t"
"v_mfma_f32_16x16x32_bf16 v[84:87], a[116:119], a[164:167], v[84:87]\n\t"
"ds_write_b128 v219, v[192:195] offset:16384\n\t"
"v_mfma_f32_16x16x32_bf16 v[68:71], a[104:107], a[148:151], v[68:71]\n\t"
"ds_read_b64_tr_b16 a[180:181], v249 offset:42624\n\t"
"v_mfma_f32_16x16x32_bf16 v[68:71], a[108:111], a[164:167], v[68:71]\n\t"
"buffer_load_dwordx4 a[92:95], v235, s[8:11], s67 offen offset:3072 sc1\n\t"
"v_mfma_f32_16x16x32_bf16 v[204:207], a[196:199], a[208:211], 0\n\t"
"v_cvt_pk_bf16_f32 v200, v200, v201\n\t"
"v_cvt_pk_bf16_f32 v201, v202, v203\n\t"
"v_mfma_f32_16x16x32_bf16 v[52:55], a[96:99], a[148:151], v[52:55]\n\t"
"ds_read_b128 a[148:151], v216 offset:1024\n\t"
"v_mfma_f32_16x16x32_bf16 v[52:55], a[100:103], a[164:167], v[52:55]\n\t"
"buffer_load_dwordx4 a[84:87], v235, s[8:11], s67 offen offset:1024 sc1\n\t"
"v_mfma_f32_16x16x32_bf16 v[208:211], a[200:203], a[208:211], 0\n\t"
"ds_read_b128 a[164:167], v216 offset:9216\n\t"
"s_waitcnt lgkmcnt(5)\n\t"
"v_mfma_f32_16x16x32_bf16 v[56:59], a[96:99], a[152:155], v[56:59]\n\t"
"v_cvt_pk_bf16_f32 v202, v204, v205\n\t"
"v_cvt_pk_bf16_f32 v203, v206, v207\n\t"
"v_mfma_f32_16x16x32_bf16 v[56:59], a[100:103], a[168:171], v[56:59]\n\t"
"ds_write_b128 v217, v[200:203] offset:24576\n\t"
"v_mfma_f32_16x16x32_bf16 v[72:75], a[104:107], a[152:155], v[72:75]\n\t"
"ds_read_b64_tr_b16 a[182:183], v249 offset:43136\n\t"
"v_mfma_f32_16x16x32_bf16 v[72:75], a[108:111], a[168:171], v[72:75]\n\t"
"buffer_load_dwordx4 a[80:83], v235, s[8:11], s67 offen offset:0 sc1\n\t"
"v_mfma_f32_16x16x32_bf16 v[212:215], a[204:207], a[208:211], 0\n\t"
"v_cvt_pk_bf16_f32 v208, v208, v209\n\t"
"v_cvt_pk_bf16_f32 v209, v210, v211\n\t"
"v_mfma_f32_16x16x32_bf16 v[88:91], a[112:115], a[152:155], v[88:91]\n\t"
"ds_read_b64_tr_b16 a[184:185], v250 offset:44928\n\t"
"v_mfma_f32_16x16x32_bf16 v[88:91], a[116:119], a[168:171], v[88:91]\n\t"
"buffer_load_dwordx4 a[72:75], v218, s[8:11], s66 offen offset:2048 sc1\n\t"
"v_mfma_f32_16x16x32_bf16 v[92:95], a[112:115], a[156:159], v[92:95]\n\t"
"ds_read_b64_tr_b16 a[186:187], v250 offset:45440\n\t"
"v_mfma_f32_16x16x32_bf16 v[92:95], a[116:119], a[172:175], v[92:95]\n\t"
"ds_read_b64_tr_b16 a[188:189], v251 offset:44928\n\t"
"v_cvt_pk_bf16_f32 v210, v212, v213\n\t"
"v_mfma_f32_16x16x32_bf16 v[76:79], a[104:107], a[156:159], v[76:79]\n\t"
"ds_read_b64_tr_b16 a[190:191], v251 offset:45440\n\t"
"v_cvt_pk_bf16_f32 v211, v214, v215\n\t"
"v_mfma_f32_16x16x32_bf16 v[76:79], a[108:111], a[172:175], v[76:79]\n\t"
"buffer_load_dwordx4 a[76:79], v218, s[8:11], s66 offen offset:3072 sc1\n\t"
"v_mfma_f32_16x16x32_bf16 v[60:63], a[96:99], a[156:159], v[60:63]\n\t"
"ds_read_b64_tr_b16 a[192:193], v250 offset:42624\n\t"
"v_mfma_f32_16x16x32_bf16 v[60:63], a[100:103], a[172:175], v[60:63]\n\t"
"ds_write_b128 v219, v[208:211] offset:24576\n\t"
"s_waitcnt vmcnt(24) lgkmcnt(6)\n\t"
"v_mfma_f32_16x16x32_bf16 v[0:3], a[120:123], a[144:147], v[0:3]\n\t"
"ds_read_b64_tr_b16 a[194:195], v250 offset:43136\n\t"
"v_mfma_f32_16x16x32_bf16 v[0:3], a[124:127], a[160:163], v[0:3]\n\t"
"ds_read_b64_tr_b16 a[196:197], v251 offset:42624\n\t"
"ds_read_b64_tr_b16 a[198:199], v251 offset:43136\n\t"
"ds_read_b64_tr_b16 a[200:201], v248 offset:44928\n\t"
"ds_read_b64_tr_b16 a[202:203], v248 offset:45440\n\t"
"ds_read_b64_tr_b16 a[204:205], v249 offset:44928\n\t"
"ds_read_b64_tr_b16 a[206:207], v249 offset:45440\n\t"
"v_mfma_f32_16x16x32_bf16 v[16:19], a[128:131], a[144:147], v[16:19]\n\t"
"ds_read_b128 a[152:155], v216 offset:2048\n\t"
"v_mfma_f32_16x16x32_bf16 v[16:19], a[132:135], a[160:163], v[16:19]\n\t"
"buffer_load_dwordx4 a[112:115], v235, s[8:11], s53 offen offset:0 sc1\n\t"
"v_mfma_f32_16x16x32_bf16 v[32:35], a[136:139], a[144:147], v[32:35]\n\t"
"ds_read_b128 a[168:171], v216 offset:10240\n\t"
"v_mfma_f32_16x16x32_bf16 v[32:35], a[140:143], a[160:163], v[32:35]\n\t"
"buffer_load_dwordx4 a[116:119], v235, s[8:11], s53 offen offset:1024 sc1\n\t"
"v_mfma_f32_16x16x32_bf16 v[36:39], a[136:139], a[148:151], v[36:39]\n\t"
"ds_read_b128 a[156:159], v216 offset:3072\n\t"
"v_mfma_f32_16x16x32_bf16 v[36:39], a[140:143], a[164:167], v[36:39]\n\t"
"ds_read_b128 a[172:175], v216 offset:11264\n\t"
"v_mfma_f32_16x16x32_bf16 v[20:23], a[128:131], a[148:151], v[20:23]\n\t"
"v_mfma_f32_16x16x32_bf16 v[20:23], a[132:135], a[164:167], v[20:23]\n\t"
"buffer_load_dwordx4 a[108:111], v218, s[8:11], s52 offen offset:3072 sc1\n\t"
"v_mfma_f32_16x16x32_bf16 v[4:7], a[120:123], a[148:151], v[4:7]\n\t"
"v_mfma_f32_16x16x32_bf16 v[4:7], a[124:127], a[164:167], v[4:7]\n\t"
"buffer_load_dwordx4 a[104:107], v218, s[8:11], s52 offen offset:2048 sc1\n\t"
"s_waitcnt lgkmcnt(0)\n\t"
"s_barrier\n\t"
"v_mfma_f32_16x16x32_bf16 v[8:11], a[120:123], a[152:155], v[8:11]\n\t"
"ds_read_b128 a[144:147], v216 offset:20480\n\t"
"v_mfma_f32_16x16x32_bf16 v[8:11], a[124:127], a[168:171], v[8:11]\n\t"
"s_add_u32 m0, s26, 124544\n\t"
"ds_read_b128 a[160:163], v216 offset:28672\n\t"
"ds_read_b128 a[148:151], v216 offset:21504\n\t"
"buffer_load_dwordx4 v236, s[12:15], s22 offen offset:128 nt lds\n\t"
"v_mfma_f32_16x16x32_bf16 v[24:27], a[128:131], a[152:155], v[24:27]\n\t"
"ds_read_b128 a[164:167], v216 offset:29696\n\t"
"v_mfma_f32_16x16x32_bf16 v[24:27], a[132:135], a[168:171], v[24:27]\n\t"
"ds_read_b128 a[228:231], v223 offset:62208\n\t"
"v_mfma_f32_16x16x32_bf16 v[40:43], a[136:139], a[152:155], v[40:43]\n\t"
"ds_read_b128 a[152:155], v216 offset:22528\n\t"
"v_mfma_f32_16x16x32_bf16 v[40:43], a[140:143], a[168:171], v[40:43]\n\t"
"ds_read_b128 a[168:171], v216 offset:30720\n\t"
"v_mfma_f32_16x16x32_bf16 v[192:195], a[176:179], a[208:211], 0\n\t"
"ds_read_b64_tr_b16 a[176:177], v248 offset:1152\n\t"
"v_mfma_f32_16x16x32_bf16 v[44:47], a[136:139], a[156:159], v[44:47]\n\t"
"ds_read_b64_tr_b16 a[178:179], v248 offset:1664\n\t"
"v_mfma_f32_16x16x32_bf16 v[44:47], a[140:143], a[172:175], v[44:47]\n\t"
"buffer_load_dwordx4 v237, s[12:15], s22 offen offset:1152 nt lds\n\t"
"v_mfma_f32_16x16x32_bf16 v[196:199], a[180:183], a[208:211], 0\n\t"
"ds_read_b64_tr_b16 a[180:181], v249 offset:1152\n\t"
"v_mfma_f32_16x16x32_bf16 v[28:31], a[128:131], a[156:159], v[28:31]\n\t"
"v_cvt_pk_bf16_f32 v192, v192, v193\n\t"
"v_cvt_pk_bf16_f32 v193, v194, v195\n\t"
"v_mfma_f32_16x16x32_bf16 v[28:31], a[132:135], a[172:175], v[28:31]\n\t"
"ds_read_b64_tr_b16 a[182:183], v249 offset:1664\n\t"
"v_mfma_f32_16x16x32_bf16 v[12:15], a[120:123], a[156:159], v[12:15]\n\t"
"ds_read_b128 a[156:159], v216 offset:23552\n\t"
"v_mfma_f32_16x16x32_bf16 v[12:15], a[124:127], a[172:175], v[12:15]\n\t"
"buffer_load_dwordx4 v238, s[12:15], s22 offen offset:2176 nt lds\n\t"
"v_mfma_f32_16x16x32_bf16 v[200:203], a[184:187], a[208:211], 0\n\t"
"v_cvt_pk_bf16_f32 v194, v196, v197\n\t"
"s_waitcnt vmcnt(25) lgkmcnt(6)\n\t"
"v_mfma_f32_16x16x32_bf16 v[48:51], a[0:3], a[144:147], v[48:51]\n\t"
"v_cvt_pk_bf16_f32 v195, v198, v199\n\t"
"v_mfma_f32_16x16x32_bf16 v[48:51], a[4:7], a[160:163], v[48:51]\n\t"
"ds_read_b128 a[172:175], v216 offset:31744\n\t"
"v_mfma_f32_16x16x32_bf16 v[204:207], a[188:191], a[208:211], 0\n\t"
"ds_write_b128 v217, v[192:195] offset:0\n\t"
"v_mfma_f32_16x16x32_bf16 v[64:67], a[8:11], a[144:147], v[64:67]\n\t"
"v_cvt_pk_bf16_f32 v200, v200, v201\n\t"
"v_mfma_f32_16x16x32_bf16 v[64:67], a[12:15], a[160:163], v[64:67]\n\t"
"buffer_load_dwordx4 v239, s[12:15], s22 offen offset:3200 nt lds\n\t"
"v_mfma_f32_16x16x32_bf16 v[80:83], a[16:19], a[144:147], v[80:83]\n\t"
"v_cvt_pk_bf16_f32 v201, v202, v203\n\t"
"v_mfma_f32_16x16x32_bf16 v[80:83], a[20:23], a[160:163], v[80:83]\n\t"
"v_cvt_pk_bf16_f32 v202, v204, v205\n\t"
"v_mfma_f32_16x16x32_bf16 v[208:211], a[192:195], a[208:211], 0\n\t"
"v_cvt_pk_bf16_f32 v203, v206, v207\n\t"
"v_mfma_f32_16x16x32_bf16 v[84:87], a[16:19], a[148:151], v[84:87]\n\t"
"ds_read_b128 a[144:147], v216 offset:16384\n\t"
"v_mfma_f32_16x16x32_bf16 v[84:87], a[20:23], a[164:167], v[84:87]\n\t"
"ds_read_b128 a[160:163], v216 offset:24576\n\t"
"v_mfma_f32_16x16x32_bf16 v[68:71], a[8:11], a[148:151], v[68:71]\n\t"
"ds_read_b64_tr_b16 a[184:185], v250 offset:3456\n\t"
"v_mfma_f32_16x16x32_bf16 v[68:71], a[12:15], a[164:167], v[68:71]\n\t"
"s_mov_b64 exec, s[36:37]\n\t"
"buffer_load_dwordx4 v240, s[12:15], s22 offen offset:3712 nt lds\n\t"
"s_mov_b64 exec, -1\n\t"
"v_mfma_f32_16x16x32_bf16 v[212:215], a[196:199], a[208:211], 0\n\t"
"ds_write_b128 v219, v[200:203] offset:0\n\t"
"v_mfma_f32_16x16x32_bf16 v[52:55], a[0:3], a[148:151], v[52:55]\n\t"
"v_cvt_pk_bf16_f32 v208, v208, v209\n\t"
"v_mfma_f32_16x16x32_bf16 v[52:55], a[4:7], a[164:167], v[52:55]\n\t"
"v_cvt_pk_bf16_f32 v209, v210, v211\n\t"
"ds_read_b128 a[148:151], v216 offset:17408\n\t"
"v_mfma_f32_16x16x32_bf16 v[192:195], a[200:203], a[208:211], 0\n\t"
"ds_read_b128 a[164:167], v216 offset:25600\n\t"
"s_waitcnt lgkmcnt(6)\n\t"
"v_mfma_f32_16x16x32_bf16 v[56:59], a[0:3], a[152:155], v[56:59]\n\t"
"v_cvt_pk_bf16_f32 v210, v212, v213\n\t"
"v_mfma_f32_16x16x32_bf16 v[56:59], a[4:7], a[168:171], v[56:59]\n\t"
"v_cvt_pk_bf16_f32 v211, v214, v215\n\t"
"v_mfma_f32_16x16x32_bf16 v[72:75], a[8:11], a[152:155], v[72:75]\n\t"
"ds_read_b64_tr_b16 a[186:187], v250 offset:3968\n\t"
"v_mfma_f32_16x16x32_bf16 v[72:75], a[12:15], a[168:171], v[72:75]\n\t"
"v_cvt_pk_bf16_f32 v192, v192, v193\n\t"
"v_cvt_pk_bf16_f32 v193, v194, v195\n\t"
"v_mfma_f32_16x16x32_bf16 v[196:199], a[204:207], a[208:211], 0\n\t"
"ds_read_b64_tr_b16 a[188:189], v251 offset:3456\n\t"
"v_mfma_f32_16x16x32_bf16 v[88:91], a[16:19], a[152:155], v[88:91]\n\t"
"ds_read_b64_tr_b16 a[190:191], v251 offset:3968\n\t"
"v_mfma_f32_16x16x32_bf16 v[88:91], a[20:23], a[168:171], v[88:91]\n\t"
"ds_write_b128 v217, v[208:211] offset:8192\n\t"
"v_mfma_f32_16x16x32_bf16 v[92:95], a[16:19], a[156:159], v[92:95]\n\t"
"ds_read_b64_tr_b16 a[192:193], v250 offset:1152\n\t"
"v_mfma_f32_16x16x32_bf16 v[92:95], a[20:23], a[172:175], v[92:95]\n\t"
"ds_read_b64_tr_b16 a[194:195], v250 offset:1664\n\t"
"v_cvt_pk_bf16_f32 v194, v196, v197\n\t"
"v_mfma_f32_16x16x32_bf16 v[76:79], a[8:11], a[156:159], v[76:79]\n\t"
"ds_read_b64_tr_b16 a[196:197], v251 offset:1152\n\t"
"v_mfma_f32_16x16x32_bf16 v[76:79], a[12:15], a[172:175], v[76:79]\n\t"
"ds_read_b64_tr_b16 a[198:199], v251 offset:1664\n\t"
"v_mfma_f32_16x16x32_bf16 v[60:63], a[0:3], a[156:159], v[60:63]\n\t"
"ds_read_b64_tr_b16 a[200:201], v248 offset:3456\n\t"
"v_mfma_f32_16x16x32_bf16 v[60:63], a[4:7], a[172:175], v[60:63]\n\t"
"ds_read_b64_tr_b16 a[202:203], v248 offset:3968\n\t"
"v_cvt_pk_bf16_f32 v195, v198, v199\n\t"
"s_waitcnt vmcnt(21) lgkmcnt(9)\n\t"
"v_mfma_f32_16x16x32_bf16 v[0:3], a[24:27], a[144:147], v[0:3]\n\t"
"ds_read_b64_tr_b16 a[204:205], v249 offset:3456\n\t"
"v_mfma_f32_16x16x32_bf16 v[0:3], a[28:31], a[160:163], v[0:3]\n\t"
"ds_write_b128 v219, v[192:195] offset:8192\n\t"
"ds_read_b64_tr_b16 a[206:207], v249 offset:3968\n\t"
"v_mfma_f32_16x16x32_bf16 v[16:19], a[32:35], a[144:147], v[16:19]\n\t"
"v_mfma_f32_16x16x32_bf16 v[16:19], a[36:39], a[160:163], v[16:19]\n\t"
"ds_read_b128 a[152:155], v216 offset:18432\n\t"
"v_mfma_f32_16x16x32_bf16 v[32:35], a[40:43], a[144:147], v[32:35]\n\t"
"ds_read_b128 a[168:171], v216 offset:26624\n\t"
"v_mfma_f32_16x16x32_bf16 v[32:35], a[44:47], a[160:163], v[32:35]\n\t"
"ds_read_b128 a[156:159], v216 offset:19456\n\t"
"v_mfma_f32_16x16x32_bf16 v[36:39], a[40:43], a[148:151], v[36:39]\n\t"
"ds_read_b128 a[172:175], v216 offset:27648\n\t"
"v_mfma_f32_16x16x32_bf16 v[36:39], a[44:47], a[164:167], v[36:39]\n\t"
"ds_write_b128 v223, a[228:231] offset:23040\n\t"
"v_mfma_f32_16x16x32_bf16 v[20:23], a[32:35], a[148:151], v[20:23]\n\t"
"v_mfma_f32_16x16x32_bf16 v[20:23], a[36:39], a[164:167], v[20:23]\n\t"
"v_mfma_f32_16x16x32_bf16 v[4:7], a[24:27], a[148:151], v[4:7]\n\t"
"v_mfma_f32_16x16x32_bf16 v[4:7], a[28:31], a[164:167], v[4:7]\n\t"
"s_waitcnt lgkmcnt(0)\n\t"
"s_barrier\n\t"
"v_mfma_f32_16x16x32_bf16 v[8:11], a[24:27], a[152:155], v[8:11]\n\t"
"ds_read_b128 a[144:147], v216 offset:4096\n\t"
"v_add_u32 v248, 32768, v227\n\t"
"v_mfma_f32_16x16x32_bf16 v[8:11], a[28:31], a[168:171], v[8:11]\n\t"
"ds_read_b128 a[160:163], v216 offset:12288\n\t"
"ds_read_b128 a[148:151], v216 offset:5120\n\t"
"v_mfma_f32_16x16x32_bf16 v[24:27], a[32:35], a[152:155], v[24:27]\n\t"
"ds_read_b128 a[164:167], v216 offset:13312\n\t"
"v_xor_b32 v249, 32, v248\n\t"
"v_mfma_f32_16x16x32_bf16 v[24:27], a[36:39], a[168:171], v[24:27]\n\t"
"v_xor_b32 v250, 64, v248\n\t"
"v_xor_b32 v251, 96, v248\n\t"
"v_mfma_f32_16x16x32_bf16 v[40:43], a[40:43], a[152:155], v[40:43]\n\t"
"ds_read_b128 a[152:155], v216 offset:6144\n\t"
"v_mfma_f32_16x16x32_bf16 v[40:43], a[44:47], a[168:171], v[40:43]\n\t"
"buffer_load_dwordx4 a[96:99], v218, s[8:11], s52 offen offset:0 sc1\n\t"
"v_mfma_f32_16x16x32_bf16 v[200:203], a[176:179], a[208:211], 0\n\t"
"ds_read_b128 a[168:171], v216 offset:14336\n\t"
"v_mfma_f32_16x16x32_bf16 v[44:47], a[40:43], a[156:159], v[44:47]\n\t"
"ds_read_b64_tr_b16 a[176:177], v248 offset:20736\n\t"
"v_mfma_f32_16x16x32_bf16 v[44:47], a[44:47], a[172:175], v[44:47]\n\t"
"ds_read_b64_tr_b16 a[178:179], v248 offset:21248\n\t"
"v_mfma_f32_16x16x32_bf16 v[204:207], a[180:183], a[208:211], 0\n\t"
"ds_read_b64_tr_b16 a[180:181], v249 offset:20736\n\t"
"ds_read_b64_tr_b16 a[182:183], v249 offset:21248\n\t"
"v_mfma_f32_16x16x32_bf16 v[28:31], a[32:35], a[156:159], v[28:31]\n\t"
"v_cvt_pk_bf16_f32 v200, v200, v201\n\t"
"v_cvt_pk_bf16_f32 v201, v202, v203\n\t"
"v_mfma_f32_16x16x32_bf16 v[28:31], a[36:39], a[172:175], v[28:31]\n\t"
"v_mfma_f32_16x16x32_bf16 v[12:15], a[24:27], a[156:159], v[12:15]\n\t"
"ds_read_b128 a[156:159], v216 offset:7168\n\t"
"v_mfma_f32_16x16x32_bf16 v[12:15], a[28:31], a[172:175], v[12:15]\n\t"
"v_cvt_pk_bf16_f32 v202, v204, v205\n\t"
"v_cvt_pk_bf16_f32 v203, v206, v207\n\t"
"ds_read_b128 a[172:175], v216 offset:15360\n\t"
"v_mfma_f32_16x16x32_bf16 v[208:211], a[184:187], a[208:211], 0\n\t"
"ds_write_b128 v217, v[200:203] offset:16384\n\t"
"ds_read_b64_tr_b16 a[184:185], v250 offset:20736\n\t"
"s_waitcnt vmcnt(16) lgkmcnt(9)\n\t"
"v_mfma_f32_16x16x32_bf16 v[144:147], a[48:51], a[144:147], v[144:147]\n\t"
"ds_read_b64_tr_b16 a[186:187], v250 offset:21248\n\t"
"v_mfma_f32_16x16x32_bf16 v[144:147], a[52:55], a[160:163], v[144:147]\n\t"
"v_mfma_f32_16x16x32_bf16 v[212:215], a[188:191], a[208:211], 0\n\t"
"v_pk_add_f32 v[0:1], v[0:1], v[48:49]\n\t"
"v_pk_add_f32 v[2:3], v[2:3], v[50:51]\n\t"
"v_pk_fma_f32 v[48:49], v[48:49], s[38:39], v[0:1]\n\t"
"v_pk_fma_f32 v[50:51], v[50:51], s[38:39], v[2:3]\n\t"
"v_pk_add_f32 v[16:17], v[16:17], v[64:65]\n\t"
"v_pk_add_f32 v[18:19], v[18:19], v[66:67]\n\t"
"v_pk_fma_f32 v[64:65], v[64:65], s[38:39], v[16:17]\n\t"
"v_pk_fma_f32 v[66:67], v[66:67], s[38:39], v[18:19]\n\t"
"v_pk_add_f32 v[32:33], v[32:33], v[80:81]\n\t"
"v_pk_add_f32 v[34:35], v[34:35], v[82:83]\n\t"
"v_pk_fma_f32 v[80:81], v[80:81], s[38:39], v[32:33]\n\t"
"v_pk_fma_f32 v[82:83], v[82:83], s[38:39], v[34:35]\n\t"
"v_pk_add_f32 v[36:37], v[36:37], v[84:85]\n\t"
"v_pk_add_f32 v[38:39], v[38:39], v[86:87]\n\t"
"v_pk_fma_f32 v[84:85], v[84:85], s[38:39], v[36:37]\n\t"
"v_pk_fma_f32 v[86:87], v[86:87], s[38:39], v[38:39]\n\t"
"v_pk_add_f32 v[20:21], v[20:21], v[68:69]\n\t"
"v_pk_add_f32 v[22:23], v[22:23], v[70:71]\n\t"
"v_pk_fma_f32 v[68:69], v[68:69], s[38:39], v[20:21]\n\t"
"v_pk_fma_f32 v[70:71], v[70:71], s[38:39], v[22:23]\n\t"
"v_pk_add_f32 v[4:5], v[4:5], v[52:53]\n\t"
"v_pk_add_f32 v[6:7], v[6:7], v[54:55]\n\t"
"v_pk_fma_f32 v[52:53], v[52:53], s[38:39], v[4:5]\n\t"
"v_pk_fma_f32 v[54:55], v[54:55], s[38:39], v[6:7]\n\t"
"v_pk_add_f32 v[8:9], v[8:9], v[56:57]\n\t"
"v_pk_add_f32 v[10:11], v[10:11], v[58:59]\n\t"
"v_pk_fma_f32 v[56:57], v[56:57], s[38:39], v[8:9]\n\t"
"v_pk_fma_f32 v[58:59], v[58:59], s[38:39], v[10:11]\n\t"
"v_pk_add_f32 v[24:25], v[24:25], v[72:73]\n\t"
"v_pk_add_f32 v[26:27], v[26:27], v[74:75]\n\t"
"v_pk_fma_f32 v[72:73], v[72:73], s[38:39], v[24:25]\n\t"
"v_pk_fma_f32 v[74:75], v[74:75], s[38:39], v[26:27]\n\t"
"v_pk_add_f32 v[40:41], v[40:41], v[88:89]\n\t"
"v_pk_add_f32 v[42:43], v[42:43], v[90:91]\n\t"
"v_pk_fma_f32 v[88:89], v[88:89], s[38:39], v[40:41]\n\t"
"v_pk_fma_f32 v[90:91], v[90:91], s[38:39], v[42:43]\n\t"
"v_pk_add_f32 v[44:45], v[44:45], v[92:93]\n\t"
"v_pk_add_f32 v[46:47], v[46:47], v[94:95]\n\t"
"v_pk_fma_f32 v[92:93], v[92:93], s[38:39], v[44:45]\n\t"
"v_pk_fma_f32 v[94:95], v[94:95], s[38:39], v[46:47]\n\t"
"v_pk_add_f32 v[28:29], v[28:29], v[76:77]\n\t"
"v_pk_add_f32 v[30:31], v[30:31], v[78:79]\n\t"
"v_pk_fma_f32 v[76:77], v[76:77], s[38:39], v[28:29]\n\t"
"v_pk_fma_f32 v[78:79], v[78:79], s[38:39], v[30:31]\n\t"
"v_pk_add_f32 v[12:13], v[12:13], v[60:61]\n\t"
"v_pk_add_f32 v[14:15], v[14:15], v[62:63]\n\t"
"v_pk_fma_f32 v[60:61], v[60:61], s[38:39], v[12:13]\n\t"
"v_pk_fma_f32 v[62:63], v[62:63], s[38:39], v[14:15]\n\t"
"v_cvt_pk_bf16_f32 v0, v0, v1\n\t"
"v_cvt_pk_bf16_f32 v1, v2, v3\n\t"
"v_cvt_pk_bf16_f32 v4, v4, v5\n\t"
"v_cvt_pk_bf16_f32 v5, v6, v7\n\t"
"v_mfma_f32_16x16x32_bf16 v[160:163], a[56:59], a[144:147], v[160:163]\n\t"
"v_cvt_pk_bf16_f32 v208, v208, v209\n\t"
"v_cvt_pk_bf16_f32 v209, v210, v211\n\t"
"v_mfma_f32_16x16x32_bf16 v[160:163], a[60:63], a[160:163], v[160:163]\n\t"
"ds_write2st64_b64 v228, v[0:1], v[4:5] offset0:39 offset1:52\n\t"
"v_cvt_pk_bf16_f32 v16, v16, v17\n\t"
"v_cvt_pk_bf16_f32 v17, v18, v19\n\t"
"v_mfma_f32_16x16x32_bf16 v[176:179], a[64:67], a[144:147], v[176:179]\n\t"
"v_cvt_pk_bf16_f32 v20, v20, v21\n\t"
"v_cvt_pk_bf16_f32 v21, v22, v23\n\t"
"v_mfma_f32_16x16x32_bf16 v[176:179], a[68:71], a[160:163], v[176:179]\n\t"
"buffer_load_dwordx4 a[100:103], v218, s[8:11], s52 offen offset:1024 sc1\n\t"
"ds_write2st64_b64 v246, v[16:17], v[20:21] offset0:39 offset1:52\n\t"
"v_mfma_f32_16x16x32_bf16 v[192:195], a[192:195], a[208:211], 0\n\t"
"v_cvt_pk_bf16_f32 v210, v212, v213\n\t"
"v_cvt_pk_bf16_f32 v211, v214, v215\n\t"
"ds_read_b128 a[144:147], v216 offset:0\n\t"
"ds_read_b128 a[160:163], v216 offset:8192\n\t"
"v_mfma_f32_16x16x32_bf16 v[180:183], a[64:67], a[148:151], v[180:183]\n\t"
"v_cvt_pk_bf16_f32 v32, v32, v33\n\t"
"v_cvt_pk_bf16_f32 v33, v34, v35\n\t"
"v_mfma_f32_16x16x32_bf16 v[180:183], a[68:71], a[164:167], v[180:183]\n\t"
"buffer_load_dwordx4 a[136:139], v235, s[8:11], s52 offen offset:2048 sc1\n\t"
"v_cvt_pk_bf16_f32 v36, v36, v37\n\t"
"v_cvt_pk_bf16_f32 v37, v38, v39\n\t"
"v_mfma_f32_16x16x32_bf16 v[164:167], a[56:59], a[148:151], v[164:167]\n\t"
"v_cvt_pk_bf16_f32 v8, v8, v9\n\t"
"v_cvt_pk_bf16_f32 v9, v10, v11\n\t"
"v_mfma_f32_16x16x32_bf16 v[164:167], a[60:63], a[164:167], v[164:167]\n\t"
"ds_write2st64_b64 v247, v[32:33], v[36:37] offset0:39 offset1:52\n\t"
"ds_write_b128 v219, v[208:211] offset:16384\n\t"
"v_mfma_f32_16x16x32_bf16 v[196:199], a[196:199], a[208:211], 0\n\t"
"v_cvt_pk_bf16_f32 v192, v192, v193\n\t"
"v_cvt_pk_bf16_f32 v193, v194, v195\n\t"
"v_mfma_f32_16x16x32_bf16 v[148:151], a[48:51], a[148:151], v[148:151]\n\t"
"v_cvt_pk_bf16_f32 v12, v12, v13\n\t"
"v_cvt_pk_bf16_f32 v13, v14, v15\n\t"
"v_mfma_f32_16x16x32_bf16 v[148:151], a[52:55], a[164:167], v[148:151]\n\t"
"buffer_load_dwordx4 a[140:143], v235, s[8:11], s52 offen offset:3072 sc1\n\t"
"v_mfma_f32_16x16x32_bf16 v[200:203], a[200:203], a[208:211], 0\n\t"
"ds_read_b128 a[148:151], v216 offset:1024\n\t"
"v_cvt_pk_bf16_f32 v24, v24, v25\n\t"
"s_waitcnt lgkmcnt(6)\n\t"
"v_mfma_f32_16x16x32_bf16 v[152:155], a[48:51], a[152:155], v[152:155]\n\t"
"ds_read_b128 a[164:167], v216 offset:9216\n\t"
"v_cvt_pk_bf16_f32 v194, v196, v197\n\t"
"v_mfma_f32_16x16x32_bf16 v[152:155], a[52:55], a[168:171], v[152:155]\n\t"
"buffer_load_dwordx4 a[132:135], v235, s[8:11], s52 offen offset:1024 sc1\n\t"
"v_mfma_f32_16x16x32_bf16 v[168:171], a[56:59], a[152:155], v[168:171]\n\t"
"v_cvt_pk_bf16_f32 v195, v198, v199\n\t"
"v_cvt_pk_bf16_f32 v25, v26, v27\n\t"
"v_mfma_f32_16x16x32_bf16 v[168:171], a[60:63], a[168:171], v[168:171]\n\t"
"ds_write2st64_b64 v228, v[8:9], v[12:13] offset0:65 offset1:78\n\t"
"v_mfma_f32_16x16x32_bf16 v[204:207], a[204:207], a[208:211], 0\n\t"
"v_cvt_pk_bf16_f32 v28, v28, v29\n\t"
"v_cvt_pk_bf16_f32 v29, v30, v31\n\t"
"buffer_load_dwordx4 a[128:131], v235, s[8:11], s52 offen offset:0 sc1\n\t"
"v_mfma_f32_16x16x32_bf16 v[184:187], a[64:67], a[152:155], v[184:187]\n\t"
"v_cvt_pk_bf16_f32 v200, v200, v201\n\t"
"v_cvt_pk_bf16_f32 v201, v202, v203\n\t"
"v_mfma_f32_16x16x32_bf16 v[184:187], a[68:71], a[168:171], v[184:187]\n\t"
"ds_write2st64_b64 v246, v[24:25], v[28:29] offset0:65 offset1:78\n\t"
"v_mfma_f32_16x16x32_bf16 v[188:191], a[64:67], a[156:159], v[188:191]\n\t"
"v_cvt_pk_bf16_f32 v40, v40, v41\n\t"
"v_cvt_pk_bf16_f32 v41, v42, v43\n\t"
"v_mfma_f32_16x16x32_bf16 v[188:191], a[68:71], a[172:175], v[188:191]\n\t"
"ds_write_b128 v217, v[192:195] offset:24576\n\t"
"v_mfma_f32_16x16x32_bf16 v[172:175], a[56:59], a[156:159], v[172:175]\n\t"
"v_cvt_pk_bf16_f32 v44, v44, v45\n\t"
"v_cvt_pk_bf16_f32 v45, v46, v47\n\t"
"v_mfma_f32_16x16x32_bf16 v[172:175], a[60:63], a[172:175], v[172:175]\n\t"
"buffer_load_dwordx4 a[120:123], v218, s[8:11], s51 offen offset:2048 sc1\n\t"
"v_mfma_f32_16x16x32_bf16 v[156:159], a[48:51], a[156:159], v[156:159]\n\t"
"ds_read_b64_tr_b16 a[188:189], v251 offset:20736\n\t"
"v_cvt_pk_bf16_f32 v202, v204, v205\n\t"
"v_mfma_f32_16x16x32_bf16 v[156:159], a[52:55], a[172:175], v[156:159]\n\t"
"ds_read_b64_tr_b16 a[190:191], v251 offset:21248\n\t"
"v_cvt_pk_bf16_f32 v203, v206, v207\n\t"
"s_waitcnt vmcnt(16) lgkmcnt(5)\n\t"
"v_mfma_f32_16x16x32_bf16 v[96:99], a[72:75], a[144:147], v[96:99]\n\t"
"ds_read_b64_tr_b16 a[192:193], v248 offset:21888\n\t"
"v_mfma_f32_16x16x32_bf16 v[96:99], a[76:79], a[160:163], v[96:99]\n\t"
"ds_write_b128 v219, v[200:203] offset:24576\n\t"
"ds_read_b64_tr_b16 a[194:195], v248 offset:22400\n\t"
"ds_read_b64_tr_b16 a[196:197], v249 offset:21888\n\t"
"ds_read_b64_tr_b16 a[198:199], v249 offset:22400\n\t"
"ds_read_b64_tr_b16 a[200:201], v250 offset:21888\n\t"
"ds_read_b64_tr_b16 a[202:203], v250 offset:22400\n\t"
"ds_read_b64_tr_b16 a[204:205], v251 offset:21888\n\t"
"ds_read_b64_tr_b16 a[206:207], v251 offset:22400\n\t"
"ds_write2st64_b64 v247, v[40:41], v[44:45] offset0:65 offset1:78\n\t"
"buffer_load_dwordx4 a[124:127], v218, s[8:11], s51 offen offset:3072 sc1\n\t"
"v_mfma_f32_16x16x32_bf16 v[112:115], a[80:83], a[144:147], v[112:115]\n\t"
"v_mfma_f32_16x16x32_bf16 v[112:115], a[84:87], a[160:163], v[112:115]\n\t"
"v_mfma_f32_16x16x32_bf16 v[128:131], a[88:91], a[144:147], v[128:131]\n\t"
"ds_read_b128 a[152:155], v216 offset:2048\n\t"
"v_mfma_f32_16x16x32_bf16 v[128:131], a[92:95], a[160:163], v[128:131]\n\t"
"buffer_load_dwordx4 a[16:19], v218, s[84:87], s69 offen offset:0 sc1\n\t"
"v_mfma_f32_16x16x32_bf16 v[132:135], a[88:91], a[148:151], v[132:135]\n\t"
"ds_read_b128 a[168:171], v216 offset:10240\n\t"
"v_mfma_f32_16x16x32_bf16 v[132:135], a[92:95], a[164:167], v[132:135]\n\t"
"ds_read_b128 a[156:159], v216 offset:3072\n\t"
"ds_read_b128 a[172:175], v216 offset:11264\n\t"
"v_mfma_f32_16x16x32_bf16 v[116:119], a[80:83], a[148:151], v[116:119]\n\t"
"v_mfma_f32_16x16x32_bf16 v[116:119], a[84:87], a[164:167], v[116:119]\n\t"
"buffer_load_dwordx4 a[20:23], v218, s[84:87], s69 offen offset:1024 sc1\n\t"
"buffer_load_dwordx4 a[12:15], v235, s[84:87], s69 offen offset:3072 sc1\n\t"
"v_mfma_f32_16x16x32_bf16 v[100:103], a[72:75], a[148:151], v[100:103]\n\t"
"v_mfma_f32_16x16x32_bf16 v[100:103], a[76:79], a[164:167], v[100:103]\n\t"
"s_waitcnt lgkmcnt(0)\n\t"
"s_barrier\n\t"
"s_cmp_eq_u32 s21, 1\n\t"
"s_cbranch_scc1 Ltail_%=\n\t"
"v_mfma_f32_16x16x32_bf16 v[104:107], a[72:75], a[152:155], v[104:107]\n\t"
"ds_read_b128 a[144:147], v216 offset:20480\n\t"
"v_mfma_f32_16x16x32_bf16 v[104:107], a[76:79], a[168:171], v[104:107]\n\t"
"ds_read_b128 a[160:163], v216 offset:28672\n\t"
"ds_read_b128 a[148:151], v216 offset:21504\n\t"
"v_mfma_f32_16x16x32_bf16 v[120:123], a[80:83], a[152:155], v[120:123]\n\t"
"ds_read_b128 a[164:167], v216 offset:29696\n\t"
"v_mfma_f32_16x16x32_bf16 v[120:123], a[84:87], a[168:171], v[120:123]\n\t"
"buffer_load_dwordx4 a[8:11], v235, s[84:87], s69 offen offset:2048 sc1\n\t"
"v_mfma_f32_16x16x32_bf16 v[136:139], a[88:91], a[152:155], v[136:139]\n\t"
"ds_read_b128 v[8:11], v229 offset:19968\n\t"
"v_mfma_f32_16x16x32_bf16 v[136:139], a[92:95], a[168:171], v[136:139]\n\t"
"buffer_load_dwordx4 a[0:3], v235, s[84:87], s69 offen offset:0 sc1\n\t"
"v_mfma_f32_16x16x32_bf16 v[192:195], a[176:179], a[224:227], 0\n\t"
"buffer_load_dwordx4 a[4:7], v235, s[84:87], s69 offen offset:1024 sc1\n\t"
"v_mfma_f32_16x16x32_bf16 v[140:143], a[88:91], a[156:159], v[140:143]\n\t"
"ds_read_b128 v[24:27], v230 offset:19968\n\t"
"v_mfma_f32_16x16x32_bf16 v[140:143], a[92:95], a[172:175], v[140:143]\n\t"
"buffer_load_dwordx4 a[40:43], v218, s[84:87], s81 offen offset:0 sc1\n\t"
"v_mfma_f32_16x16x32_bf16 v[196:199], a[180:183], a[224:227], 0\n\t"
"ds_read_b128 a[152:155], v216 offset:22528\n\t"
"v_mfma_f32_16x16x32_bf16 v[124:127], a[80:83], a[156:159], v[124:127]\n\t"
"ds_read_b128 a[168:171], v216 offset:30720\n\t"
"v_cvt_pk_bf16_f32 v192, v192, v193\n\t"
"v_mfma_f32_16x16x32_bf16 v[124:127], a[84:87], a[172:175], v[124:127]\n\t"
"v_cvt_pk_bf16_f32 v193, v194, v195\n\t"
"s_waitcnt lgkmcnt(3)\n\t"
"buffer_store_dwordx4 v[8:11], v232, s[16:19], s24 offen nt\n\t"
"ds_read_b64_tr_b16 a[176:177], v248 offset:0\n\t"
"v_mfma_f32_16x16x32_bf16 v[108:111], a[72:75], a[156:159], v[108:111]\n\t"
"ds_read_b128 v[40:43], v231 offset:19968\n\t"
"v_mfma_f32_16x16x32_bf16 v[108:111], a[76:79], a[172:175], v[108:111]\n\t"
"buffer_load_dwordx4 a[44:47], v218, s[84:87], s81 offen offset:1024 sc1\n\t"
"v_mfma_f32_16x16x32_bf16 v[200:203], a[184:187], a[224:227], 0\n\t"
"v_cvt_pk_bf16_f32 v194, v196, v197\n\t"
"v_cvt_pk_bf16_f32 v195, v198, v199\n\t"
"s_waitcnt vmcnt(15)\n\t"
"v_mfma_f32_16x16x32_bf16 v[144:147], a[96:99], a[144:147], v[144:147]\n\t"
"ds_read_b128 a[156:159], v216 offset:23552\n\t"
"v_mfma_f32_16x16x32_bf16 v[144:147], a[100:103], a[160:163], v[144:147]\n\t"
"buffer_load_dwordx4 a[36:39], v235, s[84:87], s81 offen offset:3072 sc1\n\t"
"v_mfma_f32_16x16x32_bf16 v[204:207], a[188:191], a[224:227], 0\n\t"
"ds_read_b128 a[172:175], v216 offset:31744\n\t"
"v_mfma_f32_16x16x32_bf16 v[160:163], a[104:107], a[144:147], v[160:163]\n\t"
"v_cvt_pk_bf16_f32 v200, v200, v201\n\t"
"v_cvt_pk_bf16_f32 v201, v202, v203\n\t"
"v_mfma_f32_16x16x32_bf16 v[160:163], a[108:111], a[160:163], v[160:163]\n\t"
"buffer_load_dwordx4 a[32:35], v235, s[84:87], s81 offen offset:2048 sc1\n\t"
"ds_write_b128 v221, v[192:195] offset:0\n\t"
"v_mfma_f32_16x16x32_bf16 v[176:179], a[112:115], a[144:147], v[176:179]\n\t"
"ds_read_b128 v[12:15], v229 offset:33280\n\t"
"v_mfma_f32_16x16x32_bf16 v[176:179], a[116:119], a[160:163], v[176:179]\n\t"
"s_waitcnt lgkmcnt(5)\n\t"
"buffer_store_dwordx4 v[24:27], v233, s[16:19], s24 offen nt\n\t"
"v_mfma_f32_16x16x32_bf16 v[208:211], a[192:195], a[224:227], 0\n\t"
"v_cvt_pk_bf16_f32 v202, v204, v205\n\t"
"v_cvt_pk_bf16_f32 v203, v206, v207\n\t"
"v_mfma_f32_16x16x32_bf16 v[180:183], a[112:115], a[148:151], v[180:183]\n\t"
"ds_read_b128 a[144:147], v216 offset:16384\n\t"
"v_mfma_f32_16x16x32_bf16 v[180:183], a[116:119], a[164:167], v[180:183]\n\t"
"ds_read_b128 a[160:163], v216 offset:24576\n\t"
"ds_write_b128 v221, v[200:203] offset:8192\n\t"
"v_mfma_f32_16x16x32_bf16 v[164:167], a[104:107], a[148:151], v[164:167]\n\t"
"ds_read_b64_tr_b16 a[178:179], v248 offset:512\n\t"
"v_mfma_f32_16x16x32_bf16 v[164:167], a[108:111], a[164:167], v[164:167]\n\t"
"s_waitcnt lgkmcnt(7)\n\t"
"buffer_store_dwordx4 v[40:43], v234, s[16:19], s24 offen nt\n\t"
"v_mfma_f32_16x16x32_bf16 v[212:215], a[196:199], a[224:227], 0\n\t"
"buffer_load_dwordx4 a[24:27], v235, s[84:87], s81 offen offset:0 sc1\n\t"
"v_mfma_f32_16x16x32_bf16 v[148:151], a[96:99], a[148:151], v[148:151]\n\t"
"v_cvt_pk_bf16_f32 v208, v208, v209\n\t"
"v_cvt_pk_bf16_f32 v209, v210, v211\n\t"
"v_mfma_f32_16x16x32_bf16 v[148:151], a[100:103], a[164:167], v[148:151]\n\t"
"ds_read_b128 v[28:31], v230 offset:33280\n\t"
"v_mfma_f32_16x16x32_bf16 v[192:195], a[200:203], a[224:227], 0\n\t"
"buffer_load_dwordx4 a[28:31], v235, s[84:87], s81 offen offset:1024 sc1\n\t"
"ds_read_b128 a[148:151], v216 offset:17408\n\t"
"ds_read_b128 a[164:167], v216 offset:25600\n\t"
"v_mfma_f32_16x16x32_bf16 v[152:155], a[96:99], a[152:155], v[152:155]\n\t"
"v_cvt_pk_bf16_f32 v210, v212, v213\n\t"
"v_cvt_pk_bf16_f32 v211, v214, v215\n\t"
"v_mfma_f32_16x16x32_bf16 v[152:155], a[100:103], a[168:171], v[152:155]\n\t"
"s_waitcnt lgkmcnt(7)\n\t"
"buffer_store_dwordx4 v[12:15], v232, s[16:19], s27 offen nt\n\t"
"v_mfma_f32_16x16x32_bf16 v[168:171], a[104:107], a[152:155], v[168:171]\n\t"
"ds_read_b128 v[44:47], v231 offset:33280\n\t"
"v_mfma_f32_16x16x32_bf16 v[168:171], a[108:111], a[168:171], v[168:171]\n\t"
"buffer_load_dwordx4 a[64:67], v218, s[84:87], s54 offen offset:0 sc1\n\t"
"v_mfma_f32_16x16x32_bf16 v[196:199], a[204:207], a[224:227], 0\n\t"
"v_cvt_pk_bf16_f32 v192, v192, v193\n\t"
"v_cvt_pk_bf16_f32 v193, v194, v195\n\t"
"v_mfma_f32_16x16x32_bf16 v[184:187], a[112:115], a[152:155], v[184:187]\n\t"
"ds_read_b64_tr_b16 a[180:181], v249 offset:0\n\t"
"v_mfma_f32_16x16x32_bf16 v[184:187], a[116:119], a[168:171], v[184:187]\n\t"
"buffer_load_dwordx4 a[68:71], v218, s[84:87], s54 offen offset:1024 sc1\n\t"
"ds_write_b128 v221, v[208:211] offset:4096\n\t"
"v_mfma_f32_16x16x32_bf16 v[188:191], a[112:115], a[156:159], v[188:191]\n\t"
"ds_read_b64_tr_b16 a[182:183], v249 offset:512\n\t"
"v_mfma_f32_16x16x32_bf16 v[188:191], a[116:119], a[172:175], v[188:191]\n\t"
"ds_read_b64_tr_b16 a[184:185], v250 offset:0\n\t"
"v_cvt_pk_bf16_f32 v194, v196, v197\n\t"
"v_mfma_f32_16x16x32_bf16 v[172:175], a[104:107], a[156:159], v[172:175]\n\t"
"ds_read_b64_tr_b16 a[186:187], v250 offset:512\n\t"
"v_cvt_pk_bf16_f32 v195, v198, v199\n\t"
"v_mfma_f32_16x16x32_bf16 v[172:175], a[108:111], a[172:175], v[172:175]\n\t"
"buffer_load_dwordx4 a[60:63], v235, s[84:87], s54 offen offset:3072 sc1\n\t"
"v_mfma_f32_16x16x32_bf16 v[156:159], a[96:99], a[156:159], v[156:159]\n\t"
"ds_read_b64_tr_b16 a[188:189], v251 offset:0\n\t"
"v_mfma_f32_16x16x32_bf16 v[156:159], a[100:103], a[172:175], v[156:159]\n\t"
"ds_write_b128 v221, v[192:195] offset:12288\n\t"
"s_waitcnt vmcnt(19) lgkmcnt(7)\n\t"
"v_mfma_f32_16x16x32_bf16 v[96:99], a[120:123], a[144:147], v[96:99]\n\t"
"ds_read_b64_tr_b16 a[190:191], v251 offset:512\n\t"
"v_mfma_f32_16x16x32_bf16 v[96:99], a[124:127], a[160:163], v[96:99]\n\t"
"ds_read_b64_tr_b16 a[192:193], v248 offset:1152\n\t"
"ds_read_b64_tr_b16 a[194:195], v248 offset:1664\n\t"
"ds_read_b64_tr_b16 a[196:197], v249 offset:1152\n\t"
"ds_read_b64_tr_b16 a[198:199], v249 offset:1664\n\t"
"ds_read_b64_tr_b16 a[200:201], v250 offset:1152\n\t"
"ds_read_b64_tr_b16 a[202:203], v250 offset:1664\n\t"
"ds_read_b64_tr_b16 a[204:205], v251 offset:1152\n\t"
"ds_read_b64_tr_b16 a[206:207], v251 offset:1664\n\t"
"buffer_store_dwordx4 v[28:31], v233, s[16:19], s27 offen nt\n\t"
"v_mfma_f32_16x16x32_bf16 v[112:115], a[128:131], a[144:147], v[112:115]\n\t"
"v_mfma_f32_16x16x32_bf16 v[112:115], a[132:135], a[160:163], v[112:115]\n\t"
"ds_read_b128 a[152:155], v216 offset:18432\n\t"
"v_mfma_f32_16x16x32_bf16 v[128:131], a[136:139], a[144:147], v[128:131]\n\t"
"ds_read_b128 a[168:171], v216 offset:26624\n\t"
"v_mfma_f32_16x16x32_bf16 v[128:131], a[140:143], a[160:163], v[128:131]\n\t"
"ds_read_b128 a[156:159], v216 offset:19456\n\t"
"v_mfma_f32_16x16x32_bf16 v[132:135], a[136:139], a[148:151], v[132:135]\n\t"
"ds_read_b128 a[172:175], v216 offset:27648\n\t"
"v_mfma_f32_16x16x32_bf16 v[132:135], a[140:143], a[164:167], v[132:135]\n\t"
"buffer_load_dwordx4 a[56:59], v235, s[84:87], s54 offen offset:2048 sc1\n\t"
"v_mfma_f32_16x16x32_bf16 v[116:119], a[128:131], a[148:151], v[116:119]\n\t"
"v_mfma_f32_16x16x32_bf16 v[116:119], a[132:135], a[164:167], v[116:119]\n\t"
"buffer_store_dwordx4 v[44:47], v234, s[16:19], s27 offen nt\n\t"
"v_mfma_f32_16x16x32_bf16 v[100:103], a[120:123], a[148:151], v[100:103]\n\t"
"v_mfma_f32_16x16x32_bf16 v[100:103], a[124:127], a[164:167], v[100:103]\n\t"
"buffer_load_dwordx4 a[48:51], v235, s[84:87], s54 offen offset:0 sc1\n\t"
"s_waitcnt lgkmcnt(0)\n\t"
"s_barrier\n\t"
"v_mfma_f32_16x16x32_bf16 v[104:107], a[120:123], a[152:155], v[104:107]\n\t"
"ds_read_b128 a[144:147], v216 offset:0\n\t"
"v_add_u32 v248, 101632, v227\n\t"
"v_mfma_f32_16x16x32_bf16 v[104:107], a[124:127], a[168:171], v[104:107]\n\t"
"ds_read_b128 a[160:163], v216 offset:8192\n\t"
"ds_read_b128 a[148:151], v216 offset:1024\n\t"
"v_mfma_f32_16x16x32_bf16 v[120:123], a[128:131], a[152:155], v[120:123]\n\t"
"ds_read_b128 a[164:167], v216 offset:9216\n\t"
"v_xor_b32 v249, 32, v248\n\t"
"v_mfma_f32_16x16x32_bf16 v[120:123], a[132:135], a[168:171], v[120:123]\n\t"
"buffer_load_dwordx4 a[52:55], v235, s[84:87], s54 offen offset:1024 sc1\n\t"
"v_mfma_f32_16x16x32_bf16 v[136:139], a[136:139], a[152:155], v[136:139]\n\t"
"v_xor_b32 v250, 64, v248\n\t"
"v_xor_b32 v251, 96, v248\n\t"
"v_mfma_f32_16x16x32_bf16 v[136:139], a[140:143], a[168:171], v[136:139]\n\t"
"buffer_load_dwordx4 a[88:91], v218, s[84:87], s66 offen offset:0 sc1\n\t"
"v_mfma_f32_16x16x32_bf16 v[200:203], a[176:179], v[252:255], 0\n\t"
"buffer_load_dwordx4 a[92:95], v218, s[84:87], s66 offen offset:1024 sc1\n\t"
"v_mfma_f32_16x16x32_bf16 v[140:143], a[136:139], a[156:159], v[140:143]\n\t"
"ds_read_b128 a[152:155], v216 offset:2048\n\t"
"v_mfma_f32_16x16x32_bf16 v[140:143], a[140:143], a[172:175], v[140:143]\n\t"
"buffer_load_dwordx4 a[84:87], v235, s[84:87], s66 offen offset:3072 sc1\n\t"
"ds_read_b128 a[168:171], v216 offset:10240\n\t"
"ds_read_b64_tr_b16 a[176:177], v248 offset:41472\n\t"
"v_mfma_f32_16x16x32_bf16 v[204:207], a[180:183], v[252:255], 0\n\t"
"ds_read_b64_tr_b16 a[178:179], v248 offset:41984\n\t"
"ds_read_b64_tr_b16 a[180:181], v249 offset:41472\n\t"
"v_mfma_f32_16x16x32_bf16 v[124:127], a[128:131], a[156:159], v[124:127]\n\t"
"v_cvt_pk_bf16_f32 v200, v200, v201\n\t"
"v_cvt_pk_bf16_f32 v201, v202, v203\n\t"
"v_mfma_f32_16x16x32_bf16 v[124:127], a[132:135], a[172:175], v[124:127]\n\t"
"buffer_load_dwordx4 a[80:83], v235, s[84:87], s66 offen offset:2048 sc1\n\t"
"ds_read_b64_tr_b16 a[182:183], v249 offset:41984\n\t"
"v_mfma_f32_16x16x32_bf16 v[108:111], a[120:123], a[156:159], v[108:111]\n\t"
"ds_read_b128 a[156:159], v216 offset:3072\n\t"
"v_mfma_f32_16x16x32_bf16 v[108:111], a[124:127], a[172:175], v[108:111]\n\t"
"buffer_load_dwordx4 a[72:75], v235, s[84:87], s66 offen offset:0 sc1\n\t"
"v_cvt_pk_bf16_f32 v202, v204, v205\n\t"
"v_cvt_pk_bf16_f32 v203, v206, v207\n\t"
"v_mfma_f32_16x16x32_bf16 v[208:211], a[184:187], v[252:255], 0\n\t"
"buffer_load_dwordx4 a[76:79], v235, s[84:87], s66 offen offset:1024 sc1\n\t"
"ds_read_b128 a[172:175], v216 offset:11264\n\t"
"ds_read_b64_tr_b16 a[184:185], v250 offset:41472\n\t"
"s_waitcnt vmcnt(24) lgkmcnt(9)\n\t"
"v_mfma_f32_16x16x32_bf16 v[48:51], a[0:3], a[144:147], v[48:51]\n\t"
"ds_read_b64_tr_b16 a[186:187], v250 offset:41984\n\t"
"v_mfma_f32_16x16x32_bf16 v[48:51], a[4:7], a[160:163], v[48:51]\n\t"
"buffer_load_dwordx4 a[112:115], v218, s[84:87], 0 offen offset:0 sc1\n\t"
"buffer_load_dwordx4 a[116:119], v218, s[84:87], 0 offen offset:1024 sc1\n\t"
"v_mfma_f32_16x16x32_bf16 v[212:215], a[188:191], v[252:255], 0\n\t"
"v_pk_add_f32 v[96:97], v[96:97], v[144:145]\n\t"
"v_pk_add_f32 v[98:99], v[98:99], v[146:147]\n\t"
"v_pk_fma_f32 v[144:145], v[144:145], s[38:39], v[96:97]\n\t"
"v_pk_fma_f32 v[146:147], v[146:147], s[38:39], v[98:99]\n\t"
"v_pk_add_f32 v[112:113], v[112:113], v[160:161]\n\t"
"v_pk_add_f32 v[114:115], v[114:115], v[162:163]\n\t"
"v_pk_fma_f32 v[160:161], v[160:161], s[38:39], v[112:113]\n\t"
"v_pk_fma_f32 v[162:163], v[162:163], s[38:39], v[114:115]\n\t"
"v_pk_add_f32 v[128:129], v[128:129], v[176:177]\n\t"
"v_pk_add_f32 v[130:131], v[130:131], v[178:179]\n\t"
"v_pk_fma_f32 v[176:177], v[176:177], s[38:39], v[128:129]\n\t"
"v_pk_fma_f32 v[178:179], v[178:179], s[38:39], v[130:131]\n\t"
"v_pk_add_f32 v[132:133], v[132:133], v[180:181]\n\t"
"v_pk_add_f32 v[134:135], v[134:135], v[182:183]\n\t"
"v_pk_fma_f32 v[180:181], v[180:181], s[38:39], v[132:133]\n\t"
"v_pk_fma_f32 v[182:183], v[182:183], s[38:39], v[134:135]\n\t"
"v_pk_add_f32 v[116:117], v[116:117], v[164:165]\n\t"
"v_pk_add_f32 v[118:119], v[118:119], v[166:167]\n\t"
"v_pk_fma_f32 v[164:165], v[164:165], s[38:39], v[116:117]\n\t"
"v_pk_fma_f32 v[166:167], v[166:167], s[38:39], v[118:119]\n\t"
"v_pk_add_f32 v[100:101], v[100:101], v[148:149]\n\t"
"v_pk_add_f32 v[102:103], v[102:103], v[150:151]\n\t"
"v_pk_fma_f32 v[148:149], v[148:149], s[38:39], v[100:101]\n\t"
"v_pk_fma_f32 v[150:151], v[150:151], s[38:39], v[102:103]\n\t"
"v_pk_add_f32 v[104:105], v[104:105], v[152:153]\n\t"
"v_pk_add_f32 v[106:107], v[106:107], v[154:155]\n\t"
"v_pk_fma_f32 v[152:153], v[152:153], s[38:39], v[104:105]\n\t"
"v_pk_fma_f32 v[154:155], v[154:155], s[38:39], v[106:107]\n\t"
"v_pk_add_f32 v[120:121], v[120:121], v[168:169]\n\t"
"v_pk_add_f32 v[122:123], v[122:123], v[170:171]\n\t"
"v_pk_fma_f32 v[168:169], v[168:169], s[38:39], v[120:121]\n\t"
"v_pk_fma_f32 v[170:171], v[170:171], s[38:39], v[122:123]\n\t"
"v_pk_add_f32 v[136:137], v[136:137], v[184:185]\n\t"
"v_pk_add_f32 v[138:139], v[138:139], v[186:187]\n\t"
"v_pk_fma_f32 v[184:185], v[184:185], s[38:39], v[136:137]\n\t"
"v_pk_fma_f32 v[186:187], v[186:187], s[38:39], v[138:139]\n\t"
"v_pk_add_f32 v[140:141], v[140:141], v[188:189]\n\t"
"v_pk_add_f32 v[142:143], v[142:143], v[190:191]\n\t"
"v_pk_fma_f32 v[188:189], v[188:189], s[38:39], v[140:141]\n\t"
"v_pk_fma_f32 v[190:191], v[190:191], s[38:39], v[142:143]\n\t"
"v_pk_add_f32 v[124:125], v[124:125], v[172:173]\n\t"
"v_pk_add_f32 v[126:127], v[126:127], v[174:175]\n\t"
"v_pk_fma_f32 v[172:173], v[172:173], s[38:39], v[124:125]\n\t"
"v_pk_fma_f32 v[174:175], v[174:175], s[38:39], v[126:127]\n\t"
"v_pk_add_f32 v[108:109], v[108:109], v[156:157]\n\t"
"v_pk_add_f32 v[110:111], v[110:111], v[158:159]\n\t"
"v_pk_fma_f32 v[156:157], v[156:157], s[38:39], v[108:109]\n\t"
"v_pk_fma_f32 v[158:159], v[158:159], s[38:39], v[110:111]\n\t"
"v_cvt_pk_bf16_f32 v96, v96, v97\n\t"
"v_cvt_pk_bf16_f32 v97, v98, v99\n\t"
"v_mfma_f32_16x16x32_bf16 v[64:67], a[8:11], a[144:147], v[64:67]\n\t"
"v_cvt_pk_bf16_f32 v100, v100, v101\n\t"
"v_cvt_pk_bf16_f32 v101, v102, v103\n\t"
"v_mfma_f32_16x16x32_bf16 v[64:67], a[12:15], a[160:163], v[64:67]\n\t"
"buffer_load_dwordx4 a[108:111], v235, s[84:87], 0 offen offset:3072 sc1\n\t"
"buffer_load_dwordx4 a[104:107], v235, s[84:87], 0 offen offset:2048 sc1\n\t"
"v_mfma_f32_16x16x32_bf16 v[80:83], a[16:19], a[144:147], v[80:83]\n\t"
"v_cvt_pk_bf16_f32 v208, v208, v209\n\t"
"v_cvt_pk_bf16_f32 v209, v210, v211\n\t"
"v_mfma_f32_16x16x32_bf16 v[80:83], a[20:23], a[160:163], v[80:83]\n\t"
"ds_write2st64_b64 v228, v[96:97], v[100:101] offset0:39 offset1:52\n\t"
"v_mfma_f32_16x16x32_bf16 v[192:195], a[192:195], v[252:255], 0\n\t"
"ds_write_b128 v221, v[200:203] offset:16384\n\t"
"buffer_load_dwordx4 a[96:99], v235, s[84:87], 0 offen offset:0 sc1\n\t"
"v_mfma_f32_16x16x32_bf16 v[84:87], a[16:19], a[148:151], v[84:87]\n\t"
"v_cvt_pk_bf16_f32 v112, v112, v113\n\t"
"v_cvt_pk_bf16_f32 v113, v114, v115\n\t"
"v_mfma_f32_16x16x32_bf16 v[84:87], a[20:23], a[164:167], v[84:87]\n\t"
"v_cvt_pk_bf16_f32 v116, v116, v117\n\t"
"v_cvt_pk_bf16_f32 v117, v118, v119\n\t"
"v_mfma_f32_16x16x32_bf16 v[68:71], a[8:11], a[148:151], v[68:71]\n\t"
"v_cvt_pk_bf16_f32 v210, v212, v213\n\t"
"v_cvt_pk_bf16_f32 v211, v214, v215\n\t"
"v_mfma_f32_16x16x32_bf16 v[68:71], a[12:15], a[164:167], v[68:71]\n\t"
"ds_write2st64_b64 v246, v[112:113], v[116:117] offset0:39 offset1:52\n\t"
"ds_read_b128 a[144:147], v216 offset:4096\n\t"
"v_cvt_pk_bf16_f32 v128, v128, v129\n\t"
"v_mfma_f32_16x16x32_bf16 v[196:199], a[196:199], v[252:255], 0\n\t"
"ds_read_b128 a[160:163], v216 offset:12288\n\t"
"v_cvt_pk_bf16_f32 v129, v130, v131\n\t"
"v_mfma_f32_16x16x32_bf16 v[52:55], a[0:3], a[148:151], v[52:55]\n\t"
"v_cvt_pk_bf16_f32 v132, v132, v133\n\t"
"v_cvt_pk_bf16_f32 v133, v134, v135\n\t"
"v_mfma_f32_16x16x32_bf16 v[52:55], a[4:7], a[164:167], v[52:55]\n\t"
"buffer_load_dwordx4 a[100:103], v235, s[84:87], 0 offen offset:1024 sc1\n\t"
"v_mfma_f32_16x16x32_bf16 v[200:203], a[200:203], v[252:255], 0\n\t"
"ds_write2st64_b64 v247, v[128:129], v[132:133] offset0:39 offset1:52\n\t"
"ds_write_b128 v221, v[208:211] offset:24576\n\t"
"s_waitcnt lgkmcnt(7)\n\t"
"v_mfma_f32_16x16x32_bf16 v[56:59], a[0:3], a[152:155], v[56:59]\n\t"
"v_cvt_pk_bf16_f32 v192, v192, v193\n\t"
"v_cvt_pk_bf16_f32 v193, v194, v195\n\t"
"v_mfma_f32_16x16x32_bf16 v[56:59], a[4:7], a[168:171], v[56:59]\n\t"
"buffer_load_dwordx4 a[136:139], v218, s[84:87], s51 offen offset:0 sc1\n\t"
"v_mfma_f32_16x16x32_bf16 v[72:75], a[8:11], a[152:155], v[72:75]\n\t"
"v_cvt_pk_bf16_f32 v104, v104, v105\n\t"
"v_cvt_pk_bf16_f32 v105, v106, v107\n\t"
"v_mfma_f32_16x16x32_bf16 v[72:75], a[12:15], a[168:171], v[72:75]\n\t"
"v_cvt_pk_bf16_f32 v108, v108, v109\n\t"
"v_cvt_pk_bf16_f32 v109, v110, v111\n\t"
"ds_read_b128 a[148:151], v216 offset:5120\n\t"
"ds_read_b128 a[164:167], v216 offset:13312\n\t"
"v_mfma_f32_16x16x32_bf16 v[204:207], a[204:207], v[252:255], 0\n\t"
"buffer_load_dwordx4 a[140:143], v218, s[84:87], s51 offen offset:1024 sc1\n\t"
"ds_write2st64_b64 v228, v[104:105], v[108:109] offset0:65 offset1:78\n\t"
"v_mfma_f32_16x16x32_bf16 v[88:91], a[16:19], a[152:155], v[88:91]\n\t"
"v_cvt_pk_bf16_f32 v194, v196, v197\n\t"
"v_cvt_pk_bf16_f32 v195, v198, v199\n\t"
"v_mfma_f32_16x16x32_bf16 v[88:91], a[20:23], a[168:171], v[88:91]\n\t"
"v_cvt_pk_bf16_f32 v120, v120, v121\n\t"
"v_cvt_pk_bf16_f32 v121, v122, v123\n\t"
"v_cvt_pk_bf16_f32 v124, v124, v125\n\t"
"v_cvt_pk_bf16_f32 v125, v126, v127\n\t"
"v_mfma_f32_16x16x32_bf16 v[92:95], a[16:19], a[156:159], v[92:95]\n\t"
"v_cvt_pk_bf16_f32 v200, v200, v201\n\t"
"v_cvt_pk_bf16_f32 v201, v202, v203\n\t"
"v_mfma_f32_16x16x32_bf16 v[92:95], a[20:23], a[172:175], v[92:95]\n\t"
"buffer_load_dwordx4 a[132:135], v235, s[84:87], s51 offen offset:3072 sc1\n\t"
"ds_write2st64_b64 v246, v[120:121], v[124:125] offset0:65 offset1:78\n\t"
"v_mfma_f32_16x16x32_bf16 v[76:79], a[8:11], a[156:159], v[76:79]\n\t"
"v_cvt_pk_bf16_f32 v136, v136, v137\n\t"
"v_cvt_pk_bf16_f32 v137, v138, v139\n\t"
"v_mfma_f32_16x16x32_bf16 v[76:79], a[12:15], a[172:175], v[76:79]\n\t"
"ds_write_b128 v221, v[192:195] offset:20480\n\t"
"v_mfma_f32_16x16x32_bf16 v[60:63], a[0:3], a[156:159], v[60:63]\n\t"
"v_cvt_pk_bf16_f32 v140, v140, v141\n\t"
"v_cvt_pk_bf16_f32 v141, v142, v143\n\t"
"v_mfma_f32_16x16x32_bf16 v[60:63], a[4:7], a[172:175], v[60:63]\n\t"
"buffer_load_dwordx4 a[128:131], v235, s[84:87], s51 offen offset:2048 sc1\n\t"
"s_waitcnt vmcnt(25) lgkmcnt(5)\n\t"
"v_mfma_f32_16x16x32_bf16 v[144:147], a[24:27], a[144:147], v[144:147]\n\t"
"ds_read_b64_tr_b16 a[188:189], v251 offset:41472\n\t"
"v_cvt_pk_bf16_f32 v202, v204, v205\n\t"
"v_mfma_f32_16x16x32_bf16 v[144:147], a[28:31], a[160:163], v[144:147]\n\t"
"ds_read_b64_tr_b16 a[190:191], v251 offset:41984\n\t"
"ds_read_b64_tr_b16 a[192:193], v248 offset:42624\n\t"
"ds_read_b64_tr_b16 a[194:195], v248 offset:43136\n\t"
"ds_read_b64_tr_b16 a[196:197], v249 offset:42624\n\t"
"ds_read_b64_tr_b16 a[198:199], v249 offset:43136\n\t"
"ds_read_b64_tr_b16 a[200:201], v250 offset:42624\n\t"
"ds_read_b64_tr_b16 a[202:203], v250 offset:43136\n\t"
"ds_read_b64_tr_b16 a[204:205], v251 offset:42624\n\t"
"ds_read_b64_tr_b16 a[206:207], v251 offset:43136\n\t"
"ds_write2st64_b64 v247, v[136:137], v[140:141] offset0:65 offset1:78\n\t"
"v_cvt_pk_bf16_f32 v203, v206, v207\n\t"
"v_mfma_f32_16x16x32_bf16 v[160:163], a[32:35], a[144:147], v[160:163]\n\t"
"v_mfma_f32_16x16x32_bf16 v[160:163], a[36:39], a[160:163], v[160:163]\n\t"
"ds_write_b128 v221, v[200:203] offset:28672\n\t"
"buffer_load_dwordx4 a[120:123], v235, s[84:87], s51 offen offset:0 sc1\n\t"
"v_mfma_f32_16x16x32_bf16 v[176:179], a[40:43], a[144:147], v[176:179]\n\t"
"v_mfma_f32_16x16x32_bf16 v[176:179], a[44:47], a[160:163], v[176:179]\n\t"
"s_waitcnt lgkmcnt(13)\n\t"
"v_mfma_f32_16x16x32_bf16 v[180:183], a[40:43], a[148:151], v[180:183]\n\t"
"ds_read_b128 a[152:155], v216 offset:6144\n\t"
"v_mfma_f32_16x16x32_bf16 v[180:183], a[44:47], a[164:167], v[180:183]\n\t"
"buffer_load_dwordx4 a[124:127], v235, s[84:87], s51 offen offset:1024 sc1\n\t"
"ds_read_b128 a[168:171], v216 offset:14336\n\t"
"ds_read_b128 a[156:159], v216 offset:7168\n\t"
"ds_read_b128 a[172:175], v216 offset:15360\n\t"
"v_mfma_f32_16x16x32_bf16 v[164:167], a[32:35], a[148:151], v[164:167]\n\t"
"v_mfma_f32_16x16x32_bf16 v[164:167], a[36:39], a[164:167], v[164:167]\n\t"
"v_mfma_f32_16x16x32_bf16 v[148:151], a[24:27], a[148:151], v[148:151]\n\t"
"v_mfma_f32_16x16x32_bf16 v[148:151], a[28:31], a[164:167], v[148:151]\n\t"
"s_waitcnt lgkmcnt(0)\n\t"
"s_barrier\n\t"
"v_mfma_f32_16x16x32_bf16 v[152:155], a[24:27], a[152:155], v[152:155]\n\t"
"ds_read_b128 a[144:147], v216 offset:16384\n\t"
"v_mfma_f32_16x16x32_bf16 v[152:155], a[28:31], a[168:171], v[152:155]\n\t"
"ds_read_b128 a[160:163], v216 offset:24576\n\t"
"ds_read_b128 a[148:151], v216 offset:17408\n\t"
"v_mfma_f32_16x16x32_bf16 v[168:171], a[32:35], a[152:155], v[168:171]\n\t"
"ds_read_b128 a[164:167], v216 offset:25600\n\t"
"v_mfma_f32_16x16x32_bf16 v[168:171], a[36:39], a[168:171], v[168:171]\n\t"
"ds_read_b128 v[104:107], v229 offset:19968\n\t"
"v_mfma_f32_16x16x32_bf16 v[184:187], a[40:43], a[152:155], v[184:187]\n\t"
"ds_read_b128 a[152:155], v216 offset:18432\n\t"
"v_mfma_f32_16x16x32_bf16 v[184:187], a[44:47], a[168:171], v[184:187]\n\t"
"ds_read_b128 v[120:123], v230 offset:19968\n\t"
"v_mfma_f32_16x16x32_bf16 v[208:211], a[176:179], a[224:227], 0\n\t"
"ds_read_b128 a[168:171], v216 offset:26624\n\t"
"v_mfma_f32_16x16x32_bf16 v[188:191], a[40:43], a[156:159], v[188:191]\n\t"
"ds_read_b64_tr_b16 a[176:177], v248 offset:20736\n\t"
"v_mfma_f32_16x16x32_bf16 v[188:191], a[44:47], a[172:175], v[188:191]\n\t"
"ds_read_b64_tr_b16 a[178:179], v248 offset:21248\n\t"
"v_mfma_f32_16x16x32_bf16 v[212:215], a[180:183], a[224:227], 0\n\t"
"ds_read_b64_tr_b16 a[180:181], v249 offset:20736\n\t"
"ds_read_b64_tr_b16 a[182:183], v249 offset:21248\n\t"
"v_mfma_f32_16x16x32_bf16 v[172:175], a[32:35], a[156:159], v[172:175]\n\t"
"v_cvt_pk_bf16_f32 v208, v208, v209\n\t"
"v_cvt_pk_bf16_f32 v209, v210, v211\n\t"
"v_mfma_f32_16x16x32_bf16 v[172:175], a[36:39], a[172:175], v[172:175]\n\t"
"s_waitcnt lgkmcnt(7)\n\t"
"buffer_store_dwordx4 v[104:107], v232, s[16:19], s20 offen nt\n\t"
"v_mfma_f32_16x16x32_bf16 v[156:159], a[24:27], a[156:159], v[156:159]\n\t"
"ds_read_b128 v[136:139], v231 offset:19968\n\t"
"v_mfma_f32_16x16x32_bf16 v[156:159], a[28:31], a[172:175], v[156:159]\n\t"
"v_cvt_pk_bf16_f32 v210, v212, v213\n\t"
"v_cvt_pk_bf16_f32 v211, v214, v215\n\t"
"v_mfma_f32_16x16x32_bf16 v[192:195], a[184:187], a[224:227], 0\n\t"
"ds_read_b128 a[156:159], v216 offset:19456\n\t"
"v_mfma_f32_16x16x32_bf16 v[0:3], a[0:3], a[144:147], 0\n\t"
"ds_read_b128 a[172:175], v216 offset:27648\n\t"
"v_mfma_f32_16x16x32_bf16 v[0:3], a[4:7], a[160:163], v[0:3]\n\t"
"ds_write_b128 v221, v[208:211] offset:0\n\t"
"ds_read_b64_tr_b16 a[184:185], v250 offset:20736\n\t"
"v_mfma_f32_16x16x32_bf16 v[196:199], a[188:191], a[224:227], 0\n\t"
"s_waitcnt lgkmcnt(8)\n\t"
"buffer_store_dwordx4 v[120:123], v233, s[16:19], s20 offen nt\n\t"
"v_mfma_f32_16x16x32_bf16 v[16:19], a[8:11], a[144:147], 0\n\t"
"v_cvt_pk_bf16_f32 v192, v192, v193\n\t"
"v_cvt_pk_bf16_f32 v193, v194, v195\n\t"
"v_mfma_f32_16x16x32_bf16 v[16:19], a[12:15], a[160:163], v[16:19]\n\t"
"ds_read_b128 v[108:111], v229 offset:33280\n\t"
"v_mfma_f32_16x16x32_bf16 v[32:35], a[16:19], a[144:147], 0\n\t"
"ds_read_b128 a[144:147], v216 offset:20480\n\t"
"v_mfma_f32_16x16x32_bf16 v[32:35], a[20:23], a[160:163], v[32:35]\n\t"
"v_cvt_pk_bf16_f32 v194, v196, v197\n\t"
"v_cvt_pk_bf16_f32 v195, v198, v199\n\t"
"v_mfma_f32_16x16x32_bf16 v[200:203], a[192:195], a[224:227], 0\n\t"
"ds_read_b128 a[160:163], v216 offset:28672\n\t"
"v_mfma_f32_16x16x32_bf16 v[36:39], a[16:19], a[148:151], 0\n\t"
"ds_read_b64_tr_b16 a[186:187], v250 offset:21248\n\t"
"v_mfma_f32_16x16x32_bf16 v[36:39], a[20:23], a[164:167], v[36:39]\n\t"
"ds_write_b128 v221, v[192:195] offset:8192\n\t"
"s_waitcnt lgkmcnt(8)\n\t"
"buffer_store_dwordx4 v[136:139], v234, s[16:19], s20 offen nt\n\t"
"v_mfma_f32_16x16x32_bf16 v[20:23], a[8:11], a[148:151], 0\n\t"
"ds_read_b64_tr_b16 a[188:189], v251 offset:20736\n\t"
"v_mfma_f32_16x16x32_bf16 v[20:23], a[12:15], a[164:167], v[20:23]\n\t"
"v_cvt_pk_bf16_f32 v200, v200, v201\n\t"
"v_cvt_pk_bf16_f32 v201, v202, v203\n\t"
"v_mfma_f32_16x16x32_bf16 v[204:207], a[196:199], a[224:227], 0\n\t"
"ds_read_b128 v[124:127], v230 offset:33280\n\t"
"v_mfma_f32_16x16x32_bf16 v[4:7], a[0:3], a[148:151], 0\n\t"
"ds_read_b128 a[148:151], v216 offset:21504\n\t"
"v_mfma_f32_16x16x32_bf16 v[4:7], a[4:7], a[164:167], v[4:7]\n\t"
"ds_read_b128 a[164:167], v216 offset:29696\n\t"
"v_mfma_f32_16x16x32_bf16 v[208:211], a[200:203], a[224:227], 0\n\t"
"ds_read_b64_tr_b16 a[190:191], v251 offset:21248\n\t"
"ds_read_b64_tr_b16 a[192:193], v248 offset:21888\n\t"
"v_mfma_f32_16x16x32_bf16 v[8:11], a[0:3], a[152:155], 0\n\t"
"v_cvt_pk_bf16_f32 v202, v204, v205\n\t"
"v_cvt_pk_bf16_f32 v203, v206, v207\n\t"
"v_mfma_f32_16x16x32_bf16 v[8:11], a[4:7], a[168:171], v[8:11]\n\t"
"s_waitcnt lgkmcnt(8)\n\t"
"buffer_store_dwordx4 v[108:111], v232, s[16:19], s25 offen nt\n\t"
"v_mfma_f32_16x16x32_bf16 v[24:27], a[8:11], a[152:155], 0\n\t"
"ds_read_b128 v[140:143], v231 offset:33280\n\t"
"v_mfma_f32_16x16x32_bf16 v[24:27], a[12:15], a[168:171], v[24:27]\n\t"
"v_cvt_pk_bf16_f32 v208, v208, v209\n\t"
"v_cvt_pk_bf16_f32 v209, v210, v211\n\t"
"v_mfma_f32_16x16x32_bf16 v[212:215], a[204:207], a[224:227], 0\n\t"
"ds_write_b128 v221, v[200:203] offset:4096\n\t"
"v_mfma_f32_16x16x32_bf16 v[40:43], a[16:19], a[152:155], 0\n\t"
"ds_read_b64_tr_b16 a[194:195], v248 offset:22400\n\t"
"v_mfma_f32_16x16x32_bf16 v[40:43], a[20:23], a[168:171], v[40:43]\n\t"
"ds_read_b64_tr_b16 a[196:197], v249 offset:21888\n\t"
"v_mfma_f32_16x16x32_bf16 v[44:47], a[16:19], a[156:159], 0\n\t"
"ds_read_b64_tr_b16 a[198:199], v249 offset:22400\n\t"
"v_mfma_f32_16x16x32_bf16 v[44:47], a[20:23], a[172:175], v[44:47]\n\t"
"buffer_load_dwordx4 a[16:19], v218, s[84:87], s57 offen offset:0 sc1\n\t"
"buffer_load_dwordx4 a[20:23], v218, s[84:87], s57 offen offset:1024 sc1\n\t"
"v_mfma_f32_16x16x32_bf16 v[28:31], a[8:11], a[156:159], 0\n\t"
"ds_read_b64_tr_b16 a[200:201], v250 offset:21888\n\t"
"v_cvt_pk_bf16_f32 v210, v212, v213\n\t"
"v_mfma_f32_16x16x32_bf16 v[28:31], a[12:15], a[172:175], v[28:31]\n\t"
"buffer_load_dwordx4 a[12:15], v235, s[84:87], s57 offen offset:3072 sc1\n\t"
"v_mfma_f32_16x16x32_bf16 v[12:15], a[0:3], a[156:159], 0\n\t"
"ds_read_b64_tr_b16 a[202:203], v250 offset:22400\n\t"
"v_cvt_pk_bf16_f32 v211, v214, v215\n\t"
"v_mfma_f32_16x16x32_bf16 v[12:15], a[4:7], a[172:175], v[12:15]\n\t"
"ds_write_b128 v221, v[208:211] offset:12288\n\t"
"v_mfma_f32_16x16x32_bf16 v[96:99], a[24:27], a[144:147], 0\n\t"
"ds_read_b64_tr_b16 a[204:205], v251 offset:21888\n\t"
"v_mfma_f32_16x16x32_bf16 v[96:99], a[28:31], a[160:163], v[96:99]\n\t"
"ds_read_b64_tr_b16 a[206:207], v251 offset:22400\n\t"
"buffer_load_dwordx4 a[8:11], v235, s[84:87], s57 offen offset:2048 sc1\n\t"
"s_waitcnt lgkmcnt(8)\n\t"
"buffer_store_dwordx4 v[124:127], v233, s[16:19], s25 offen nt\n\t"
"v_mfma_f32_16x16x32_bf16 v[112:115], a[32:35], a[144:147], 0\n\t"
"ds_read_b128 a[152:155], v216 offset:22528\n\t"
"v_mfma_f32_16x16x32_bf16 v[112:115], a[36:39], a[160:163], v[112:115]\n\t"
"buffer_load_dwordx4 a[0:3], v235, s[84:87], s57 offen offset:0 sc1\n\t"
"buffer_load_dwordx4 a[4:7], v235, s[84:87], s57 offen offset:1024 sc1\n\t"
"v_mfma_f32_16x16x32_bf16 v[128:131], a[40:43], a[144:147], 0\n\t"
"v_mfma_f32_16x16x32_bf16 v[128:131], a[44:47], a[160:163], v[128:131]\n\t"
"ds_read_b128 a[168:171], v216 offset:30720\n\t"
"v_mfma_f32_16x16x32_bf16 v[132:135], a[40:43], a[148:151], 0\n\t"
"ds_read_b128 a[156:159], v216 offset:23552\n\t"
"v_mfma_f32_16x16x32_bf16 v[132:135], a[44:47], a[164:167], v[132:135]\n\t"
"ds_read_b128 a[172:175], v216 offset:31744\n\t"
"v_mfma_f32_16x16x32_bf16 v[116:119], a[32:35], a[148:151], 0\n\t"
"v_mfma_f32_16x16x32_bf16 v[116:119], a[36:39], a[164:167], v[116:119]\n\t"
"buffer_store_dwordx4 v[140:143], v234, s[16:19], s25 offen nt\n\t"
"v_mfma_f32_16x16x32_bf16 v[100:103], a[24:27], a[148:151], 0\n\t"
"v_mfma_f32_16x16x32_bf16 v[100:103], a[28:31], a[164:167], v[100:103]\n\t"
"s_waitcnt lgkmcnt(0)\n\t"
"s_barrier\n\t"
"v_mfma_f32_16x16x32_bf16 v[104:107], a[24:27], a[152:155], 0\n\t"
"ds_read_b128 a[144:147], v216 offset:0\n\t"
"v_mfma_f32_16x16x32_bf16 v[104:107], a[28:31], a[168:171], v[104:107]\n\t"
"s_add_u32 m0, s26, 83200\n\t"
"ds_read_b128 a[160:163], v216 offset:8192\n\t"
"ds_read_b128 a[148:151], v216 offset:1024\n\t"
"buffer_load_dwordx4 v236, s[12:15], s22 offen offset:0 nt lds\n\t"
"v_mfma_f32_16x16x32_bf16 v[120:123], a[32:35], a[152:155], 0\n\t"
"ds_read_b128 a[164:167], v216 offset:9216\n\t"
"v_mfma_f32_16x16x32_bf16 v[120:123], a[36:39], a[168:171], v[120:123]\n\t"
"ds_read_b128 a[228:231], v223 offset:20736\n\t"
"v_mfma_f32_16x16x32_bf16 v[136:139], a[40:43], a[152:155], 0\n\t"
"ds_read_b128 a[152:155], v216 offset:2048\n\t"
"v_mfma_f32_16x16x32_bf16 v[136:139], a[44:47], a[168:171], v[136:139]\n\t"
"ds_read_b128 a[168:171], v216 offset:10240\n\t"
"v_mfma_f32_16x16x32_bf16 v[192:195], a[176:179], v[252:255], 0\n\t"
"ds_read_b64_tr_b16 a[176:177], v248 offset:0\n\t"
"v_mfma_f32_16x16x32_bf16 v[140:143], a[40:43], a[156:159], 0\n\t"
"ds_read_b64_tr_b16 a[178:179], v248 offset:512\n\t"
"v_mfma_f32_16x16x32_bf16 v[140:143], a[44:47], a[172:175], v[140:143]\n\t"
"buffer_load_dwordx4 v237, s[12:15], s22 offen offset:1024 nt lds\n\t"
"v_mfma_f32_16x16x32_bf16 v[196:199], a[180:183], v[252:255], 0\n\t"
"ds_read_b64_tr_b16 a[180:181], v249 offset:0\n\t"
"v_mfma_f32_16x16x32_bf16 v[124:127], a[32:35], a[156:159], 0\n\t"
"v_cvt_pk_bf16_f32 v192, v192, v193\n\t"
"v_cvt_pk_bf16_f32 v193, v194, v195\n\t"
"v_mfma_f32_16x16x32_bf16 v[124:127], a[36:39], a[172:175], v[124:127]\n\t"
"ds_read_b64_tr_b16 a[182:183], v249 offset:512\n\t"
"v_mfma_f32_16x16x32_bf16 v[108:111], a[24:27], a[156:159], 0\n\t"
"ds_read_b128 a[156:159], v216 offset:3072\n\t"
"v_mfma_f32_16x16x32_bf16 v[108:111], a[28:31], a[172:175], v[108:111]\n\t"
"buffer_load_dwordx4 v238, s[12:15], s22 offen offset:2048 nt lds\n\t"
"v_mfma_f32_16x16x32_bf16 v[200:203], a[184:187], v[252:255], 0\n\t"
"v_cvt_pk_bf16_f32 v194, v196, v197\n\t"
"v_cvt_pk_bf16_f32 v195, v198, v199\n\t"
"s_waitcnt vmcnt(33) lgkmcnt(6)\n\t"
"v_mfma_f32_16x16x32_bf16 v[48:51], a[48:51], a[144:147], v[48:51]\n\t"
"ds_read_b128 a[172:175], v216 offset:11264\n\t"
"v_mfma_f32_16x16x32_bf16 v[48:51], a[52:55], a[160:163], v[48:51]\n\t"
"ds_read_b64_tr_b16 a[184:185], v250 offset:0\n\t"
"v_mfma_f32_16x16x32_bf16 v[204:207], a[188:191], v[252:255], 0\n\t"
"ds_write_b128 v221, v[192:195] offset:16384\n\t"
"v_mfma_f32_16x16x32_bf16 v[64:67], a[56:59], a[144:147], v[64:67]\n\t"
"v_cvt_pk_bf16_f32 v200, v200, v201\n\t"
"v_mfma_f32_16x16x32_bf16 v[64:67], a[60:63], a[160:163], v[64:67]\n\t"
"buffer_load_dwordx4 v239, s[12:15], s22 offen offset:3072 nt lds\n\t"
"v_mfma_f32_16x16x32_bf16 v[80:83], a[64:67], a[144:147], v[80:83]\n\t"
"v_cvt_pk_bf16_f32 v201, v202, v203\n\t"
"v_mfma_f32_16x16x32_bf16 v[80:83], a[68:71], a[160:163], v[80:83]\n\t"
"v_cvt_pk_bf16_f32 v202, v204, v205\n\t"
"v_mfma_f32_16x16x32_bf16 v[208:211], a[192:195], v[252:255], 0\n\t"
"v_cvt_pk_bf16_f32 v203, v206, v207\n\t"
"v_mfma_f32_16x16x32_bf16 v[84:87], a[64:67], a[148:151], v[84:87]\n\t"
"ds_read_b128 a[144:147], v216 offset:4096\n\t"
"v_mfma_f32_16x16x32_bf16 v[84:87], a[68:71], a[164:167], v[84:87]\n\t"
"ds_read_b128 a[160:163], v216 offset:12288\n\t"
"v_mfma_f32_16x16x32_bf16 v[68:71], a[56:59], a[148:151], v[68:71]\n\t"
"ds_read_b64_tr_b16 a[186:187], v250 offset:512\n\t"
"v_mfma_f32_16x16x32_bf16 v[68:71], a[60:63], a[164:167], v[68:71]\n\t"
"s_mov_b64 exec, s[36:37]\n\t"
"buffer_load_dwordx4 v240, s[12:15], s22 offen offset:3584 nt lds\n\t"
"s_mov_b64 exec, -1\n\t"
"v_mfma_f32_16x16x32_bf16 v[212:215], a[196:199], v[252:255], 0\n\t"
"ds_write_b128 v221, v[200:203] offset:24576\n\t"
"v_mfma_f32_16x16x32_bf16 v[52:55], a[48:51], a[148:151], v[52:55]\n\t"
"v_cvt_pk_bf16_f32 v208, v208, v209\n\t"
"v_mfma_f32_16x16x32_bf16 v[52:55], a[52:55], a[164:167], v[52:55]\n\t"
"v_cvt_pk_bf16_f32 v209, v210, v211\n\t"
"ds_read_b128 a[148:151], v216 offset:5120\n\t"
"v_mfma_f32_16x16x32_bf16 v[192:195], a[200:203], v[252:255], 0\n\t"
"ds_read_b128 a[164:167], v216 offset:13312\n\t"
"s_waitcnt lgkmcnt(6)\n\t"
"v_mfma_f32_16x16x32_bf16 v[56:59], a[48:51], a[152:155], v[56:59]\n\t"
"v_cvt_pk_bf16_f32 v210, v212, v213\n\t"
"v_mfma_f32_16x16x32_bf16 v[56:59], a[52:55], a[168:171], v[56:59]\n\t"
"v_cvt_pk_bf16_f32 v211, v214, v215\n\t"
"ds_read_b64_tr_b16 a[188:189], v251 offset:0\n\t"
"v_mfma_f32_16x16x32_bf16 v[72:75], a[56:59], a[152:155], v[72:75]\n\t"
"ds_read_b64_tr_b16 a[190:191], v251 offset:512\n\t"
"v_mfma_f32_16x16x32_bf16 v[72:75], a[60:63], a[168:171], v[72:75]\n\t"
"v_cvt_pk_bf16_f32 v192, v192, v193\n\t"
"v_mfma_f32_16x16x32_bf16 v[196:199], a[204:207], v[252:255], 0\n\t"
"v_cvt_pk_bf16_f32 v193, v194, v195\n\t"
"ds_read_b64_tr_b16 a[192:193], v248 offset:1152\n\t"
"v_mfma_f32_16x16x32_bf16 v[88:91], a[64:67], a[152:155], v[88:91]\n\t"
"ds_read_b64_tr_b16 a[194:195], v248 offset:1664\n\t"
"v_mfma_f32_16x16x32_bf16 v[88:91], a[68:71], a[168:171], v[88:91]\n\t"
"ds_read_b64_tr_b16 a[196:197], v249 offset:1152\n\t"
"v_mfma_f32_16x16x32_bf16 v[92:95], a[64:67], a[156:159], v[92:95]\n\t"
"ds_read_b64_tr_b16 a[198:199], v249 offset:1664\n\t"
"v_mfma_f32_16x16x32_bf16 v[92:95], a[68:71], a[172:175], v[92:95]\n\t"
"ds_write_b128 v221, v[208:211] offset:20480\n\t"
"v_mfma_f32_16x16x32_bf16 v[76:79], a[56:59], a[156:159], v[76:79]\n\t"
"ds_read_b64_tr_b16 a[200:201], v250 offset:1152\n\t"
"v_mfma_f32_16x16x32_bf16 v[76:79], a[60:63], a[172:175], v[76:79]\n\t"
"ds_read_b64_tr_b16 a[202:203], v250 offset:1664\n\t"
"v_cvt_pk_bf16_f32 v194, v196, v197\n\t"
"v_mfma_f32_16x16x32_bf16 v[60:63], a[48:51], a[156:159], v[60:63]\n\t"
"ds_read_b64_tr_b16 a[204:205], v251 offset:1152\n\t"
"v_mfma_f32_16x16x32_bf16 v[60:63], a[52:55], a[172:175], v[60:63]\n\t"
"ds_read_b64_tr_b16 a[206:207], v251 offset:1664\n\t"
"s_waitcnt vmcnt(29) lgkmcnt(9)\n\t"
"v_mfma_f32_16x16x32_bf16 v[144:147], a[72:75], a[144:147], v[144:147]\n\t"
"v_cvt_pk_bf16_f32 v195, v198, v199\n\t"
"ds_read_b128 a[152:155], v216 offset:6144\n\t"
"v_mfma_f32_16x16x32_bf16 v[144:147], a[76:79], a[160:163], v[144:147]\n\t"
"ds_write_b128 v221, v[192:195] offset:28672\n\t"
"v_mfma_f32_16x16x32_bf16 v[160:163], a[80:83], a[144:147], v[160:163]\n\t"
"v_mfma_f32_16x16x32_bf16 v[160:163], a[84:87], a[160:163], v[160:163]\n\t"
"buffer_load_dwordx4 a[40:43], v218, s[84:87], s63 offen offset:0 sc1\n\t"
"v_mfma_f32_16x16x32_bf16 v[176:179], a[88:91], a[144:147], v[176:179]\n\t"
"ds_read_b128 a[168:171], v216 offset:14336\n\t"
"v_mfma_f32_16x16x32_bf16 v[176:179], a[92:95], a[160:163], v[176:179]\n\t"
"ds_read_b128 a[156:159], v216 offset:7168\n\t"
"v_mfma_f32_16x16x32_bf16 v[180:183], a[88:91], a[148:151], v[180:183]\n\t"
"ds_read_b128 a[172:175], v216 offset:15360\n\t"
"v_mfma_f32_16x16x32_bf16 v[180:183], a[92:95], a[164:167], v[180:183]\n\t"
"ds_write_b128 v222, a[228:231] offset:48128\n\t"
"v_mfma_f32_16x16x32_bf16 v[164:167], a[80:83], a[148:151], v[164:167]\n\t"
"v_mfma_f32_16x16x32_bf16 v[164:167], a[84:87], a[164:167], v[164:167]\n\t"
"v_mfma_f32_16x16x32_bf16 v[148:151], a[72:75], a[148:151], v[148:151]\n\t"
"v_mfma_f32_16x16x32_bf16 v[148:151], a[76:79], a[164:167], v[148:151]\n\t"
"s_waitcnt lgkmcnt(0)\n\t"
"s_barrier\n\t"
"v_mfma_f32_16x16x32_bf16 v[152:155], a[72:75], a[152:155], v[152:155]\n\t"
"ds_read_b128 a[144:147], v216 offset:16384\n\t"
"v_add_u32 v248, 143104, v226\n\t"
"v_mfma_f32_16x16x32_bf16 v[152:155], a[76:79], a[168:171], v[152:155]\n\t"
"ds_read_b128 a[160:163], v216 offset:24576\n\t"
"ds_read_b128 a[148:151], v216 offset:17408\n\t"
"v_mfma_f32_16x16x32_bf16 v[168:171], a[80:83], a[152:155], v[168:171]\n\t"
"ds_read_b128 a[164:167], v216 offset:25600\n\t"
"v_xor_b32 v249, 32, v248\n\t"
"v_mfma_f32_16x16x32_bf16 v[168:171], a[84:87], a[168:171], v[168:171]\n\t"
"v_xor_b32 v250, 64, v248\n\t"
"v_xor_b32 v251, 96, v248\n\t"
"v_mfma_f32_16x16x32_bf16 v[184:187], a[88:91], a[152:155], v[184:187]\n\t"
"ds_read_b128 a[152:155], v216 offset:18432\n\t"
"v_swap_b32 v236, v241\n\t"
"v_mfma_f32_16x16x32_bf16 v[184:187], a[92:95], a[168:171], v[184:187]\n\t"
"ds_read_b128 a[168:171], v216 offset:26624\n\t"
"v_swap_b32 v237, v242\n\t"
"v_mfma_f32_16x16x32_bf16 v[200:203], a[176:179], a[224:227], 0\n\t"
"ds_read_b64_tr_b16 a[176:177], v248 offset:0\n\t"
"v_mfma_f32_16x16x32_bf16 v[188:191], a[88:91], a[156:159], v[188:191]\n\t"
"ds_read_b64_tr_b16 a[178:179], v248 offset:512\n\t"
"v_swap_b32 v238, v243\n\t"
"v_mfma_f32_16x16x32_bf16 v[188:191], a[92:95], a[172:175], v[188:191]\n\t"
"v_swap_b32 v239, v244\n\t"
"v_swap_b32 v240, v245\n\t"
"v_mfma_f32_16x16x32_bf16 v[204:207], a[180:183], a[224:227], 0\n\t"
"ds_read_b64_tr_b16 a[180:181], v249 offset:0\n\t"
"v_mfma_f32_16x16x32_bf16 v[172:175], a[80:83], a[156:159], v[172:175]\n\t"
"v_cvt_pk_bf16_f32 v200, v200, v201\n\t"
"v_cvt_pk_bf16_f32 v201, v202, v203\n\t"
"v_mfma_f32_16x16x32_bf16 v[172:175], a[84:87], a[172:175], v[172:175]\n\t"
"ds_read_b64_tr_b16 a[182:183], v249 offset:512\n\t"
"v_add_u32 v224, v224, v220\n\t"
"v_mfma_f32_16x16x32_bf16 v[156:159], a[72:75], a[156:159], v[156:159]\n\t"
"ds_read_b128 a[156:159], v216 offset:19456\n\t"
"v_add_u32 v225, v225, v220\n\t"
"v_mfma_f32_16x16x32_bf16 v[156:159], a[76:79], a[172:175], v[156:159]\n\t"
"v_cvt_pk_bf16_f32 v202, v204, v205\n\t"
"v_mfma_f32_16x16x32_bf16 v[208:211], a[184:187], a[224:227], 0\n\t"
"v_cvt_pk_bf16_f32 v203, v206, v207\n\t"
"ds_read_b128 a[172:175], v216 offset:27648\n\t"
"s_waitcnt lgkmcnt(7)\n\t"
"v_mfma_f32_16x16x32_bf16 v[0:3], a[48:51], a[144:147], v[0:3]\n\t"
"ds_read_b64_tr_b16 a[184:185], v250 offset:2304\n\t"
"v_sub_u32 v220, 0, v220\n\t"
"v_mfma_f32_16x16x32_bf16 v[0:3], a[52:55], a[160:163], v[0:3]\n\t"
"ds_read_b64_tr_b16 a[186:187], v250 offset:2816\n\t"
"v_mfma_f32_16x16x32_bf16 v[212:215], a[188:191], a[224:227], 0\n\t"
"ds_write_b128 v221, v[200:203] offset:0\n\t"
"v_mfma_f32_16x16x32_bf16 v[16:19], a[56:59], a[144:147], v[16:19]\n\t"
"v_cvt_pk_bf16_f32 v208, v208, v209\n\t"
"v_cvt_pk_bf16_f32 v209, v210, v211\n\t"
"v_mfma_f32_16x16x32_bf16 v[16:19], a[60:63], a[160:163], v[16:19]\n\t"
"ds_read_b64_tr_b16 a[188:189], v251 offset:2304\n\t"
"v_mfma_f32_16x16x32_bf16 v[32:35], a[64:67], a[144:147], v[32:35]\n\t"
"ds_read_b128 a[144:147], v216 offset:20480\n\t"
"v_mfma_f32_16x16x32_bf16 v[32:35], a[68:71], a[160:163], v[32:35]\n\t"
"buffer_load_dwordx4 a[44:47], v218, s[84:87], s63 offen offset:1024 sc1\n\t"
"v_mfma_f32_16x16x32_bf16 v[192:195], a[192:195], a[224:227], 0\n\t"
"buffer_load_dwordx4 a[36:39], v235, s[84:87], s63 offen offset:3072 sc1\n\t"
"v_mfma_f32_16x16x32_bf16 v[36:39], a[64:67], a[148:151], v[36:39]\n\t"
"v_cvt_pk_bf16_f32 v210, v212, v213\n\t"
"v_cvt_pk_bf16_f32 v211, v214, v215\n\t"
"v_mfma_f32_16x16x32_bf16 v[36:39], a[68:71], a[164:167], v[36:39]\n\t"
"ds_read_b128 a[160:163], v216 offset:28672\n\t"
"v_mfma_f32_16x16x32_bf16 v[20:23], a[56:59], a[148:151], v[20:23]\n\t"
"ds_read_b64_tr_b16 a[190:191], v251 offset:2816\n\t"
"v_mfma_f32_16x16x32_bf16 v[20:23], a[60:63], a[164:167], v[20:23]\n\t"
"buffer_load_dwordx4 a[32:35], v235, s[84:87], s63 offen offset:2048 sc1\n\t"
"v_mfma_f32_16x16x32_bf16 v[196:199], a[196:199], a[224:227], 0\n\t"
"ds_write_b128 v221, v[208:211] offset:8192\n\t"
"v_mfma_f32_16x16x32_bf16 v[4:7], a[48:51], a[148:151], v[4:7]\n\t"
"v_cvt_pk_bf16_f32 v192, v192, v193\n\t"
"v_cvt_pk_bf16_f32 v193, v194, v195\n\t"
"v_mfma_f32_16x16x32_bf16 v[4:7], a[52:55], a[164:167], v[4:7]\n\t"
"ds_read_b128 a[148:151], v216 offset:21504\n\t"
"v_mfma_f32_16x16x32_bf16 v[200:203], a[200:203], a[224:227], 0\n\t"
"buffer_load_dwordx4 a[24:27], v235, s[84:87], s63 offen offset:0 sc1\n\t"
"s_waitcnt lgkmcnt(4)\n\t"
"v_mfma_f32_16x16x32_bf16 v[8:11], a[48:51], a[152:155], v[8:11]\n\t"
"ds_read_b128 a[164:167], v216 offset:29696\n\t"
"v_cvt_pk_bf16_f32 v194, v196, v197\n\t"
"v_mfma_f32_16x16x32_bf16 v[8:11], a[52:55], a[168:171], v[8:11]\n\t"
"v_cvt_pk_bf16_f32 v195, v198, v199\n\t"
"ds_read_b64_tr_b16 a[192:193], v250 offset:0\n\t"
"v_mfma_f32_16x16x32_bf16 v[24:27], a[56:59], a[152:155], v[24:27]\n\t"
"ds_read_b64_tr_b16 a[194:195], v250 offset:512\n\t"
"v_mfma_f32_16x16x32_bf16 v[24:27], a[60:63], a[168:171], v[24:27]\n\t"
"buffer_load_dwordx4 a[28:31], v235, s[84:87], s63 offen offset:1024 sc1\n\t"
"v_mfma_f32_16x16x32_bf16 v[204:207], a[204:207], a[224:227], 0\n\t"
"v_cvt_pk_bf16_f32 v200, v200, v201\n\t"
"v_cvt_pk_bf16_f32 v201, v202, v203\n\t"
"v_mfma_f32_16x16x32_bf16 v[40:43], a[64:67], a[152:155], v[40:43]\n\t"
"ds_read_b64_tr_b16 a[196:197], v251 offset:0\n\t"
"v_mfma_f32_16x16x32_bf16 v[40:43], a[68:71], a[168:171], v[40:43]\n\t"
"ds_write_b128 v221, v[192:195] offset:4096\n\t"
"v_mfma_f32_16x16x32_bf16 v[44:47], a[64:67], a[156:159], v[44:47]\n\t"
"ds_read_b64_tr_b16 a[198:199], v251 offset:512\n\t"
"v_mfma_f32_16x16x32_bf16 v[44:47], a[68:71], a[172:175], v[44:47]\n\t"
"buffer_load_dwordx4 a[64:67], v218, s[84:87], s42 offen offset:0 sc1\n\t"
"v_mfma_f32_16x16x32_bf16 v[28:31], a[56:59], a[156:159], v[28:31]\n\t"
"ds_read_b64_tr_b16 a[200:201], v248 offset:2304\n\t"
"v_cvt_pk_bf16_f32 v202, v204, v205\n\t"
"v_mfma_f32_16x16x32_bf16 v[28:31], a[60:63], a[172:175], v[28:31]\n\t"
"buffer_load_dwordx4 a[68:71], v218, s[84:87], s42 offen offset:1024 sc1\n\t"
"v_mfma_f32_16x16x32_bf16 v[12:15], a[48:51], a[156:159], v[12:15]\n\t"
"ds_read_b64_tr_b16 a[202:203], v248 offset:2816\n\t"
"v_cvt_pk_bf16_f32 v203, v206, v207\n\t"
"v_mfma_f32_16x16x32_bf16 v[12:15], a[52:55], a[172:175], v[12:15]\n\t"
"ds_write_b128 v221, v[200:203] offset:12288\n\t"
"s_waitcnt lgkmcnt(6)\n\t"
"v_mfma_f32_16x16x32_bf16 v[96:99], a[72:75], a[144:147], v[96:99]\n\t"
"ds_read_b64_tr_b16 a[204:205], v249 offset:2304\n\t"
"v_mfma_f32_16x16x32_bf16 v[96:99], a[76:79], a[160:163], v[96:99]\n\t"
"ds_read_b64_tr_b16 a[206:207], v249 offset:2816\n\t"
"v_mfma_f32_16x16x32_bf16 v[112:115], a[80:83], a[144:147], v[112:115]\n\t"
"ds_read_b128 a[152:155], v216 offset:22528\n\t"
"v_mfma_f32_16x16x32_bf16 v[112:115], a[84:87], a[160:163], v[112:115]\n\t"
"buffer_load_dwordx4 a[60:63], v235, s[84:87], s42 offen offset:3072 sc1\n\t"
"v_mfma_f32_16x16x32_bf16 v[128:131], a[88:91], a[144:147], v[128:131]\n\t"
"ds_read_b128 a[168:171], v216 offset:30720\n\t"
"v_mfma_f32_16x16x32_bf16 v[128:131], a[92:95], a[160:163], v[128:131]\n\t"
"ds_read_b128 a[156:159], v216 offset:23552\n\t"
"v_mfma_f32_16x16x32_bf16 v[132:135], a[88:91], a[148:151], v[132:135]\n\t"
"ds_read_b128 a[172:175], v216 offset:31744\n\t"
"v_mfma_f32_16x16x32_bf16 v[132:135], a[92:95], a[164:167], v[132:135]\n\t"
"buffer_load_dwordx4 a[56:59], v235, s[84:87], s42 offen offset:2048 sc1\n\t"
"v_mfma_f32_16x16x32_bf16 v[116:119], a[80:83], a[148:151], v[116:119]\n\t"
"v_mfma_f32_16x16x32_bf16 v[116:119], a[84:87], a[164:167], v[116:119]\n\t"
"buffer_load_dwordx4 a[48:51], v235, s[84:87], s42 offen offset:0 sc1\n\t"
"v_mfma_f32_16x16x32_bf16 v[100:103], a[72:75], a[148:151], v[100:103]\n\t"
"v_mfma_f32_16x16x32_bf16 v[100:103], a[76:79], a[164:167], v[100:103]\n\t"
"buffer_load_dwordx4 a[52:55], v235, s[84:87], s42 offen offset:1024 sc1\n\t"
"s_waitcnt vmcnt(12) lgkmcnt(4)\n\t"
"s_waitcnt lgkmcnt(0)\n\t"
"s_barrier\n\t"
"v_mfma_f32_16x16x32_bf16 v[104:107], a[72:75], a[152:155], v[104:107]\n\t"
"ds_read_b128 a[144:147], v216 offset:0\n\t"
"v_add_u32 v248, 80896, v227\n\t"
"v_mfma_f32_16x16x32_bf16 v[104:107], a[76:79], a[168:171], v[104:107]\n\t"
"ds_read_b128 a[160:163], v216 offset:8192\n\t"
"ds_read_b128 a[148:151], v216 offset:1024\n\t"
"v_mfma_f32_16x16x32_bf16 v[120:123], a[80:83], a[152:155], v[120:123]\n\t"
"ds_read_b128 a[164:167], v216 offset:9216\n\t"
"v_xor_b32 v249, 32, v248\n\t"
"v_mfma_f32_16x16x32_bf16 v[120:123], a[84:87], a[168:171], v[120:123]\n\t"
"v_xor_b32 v250, 64, v248\n\t"
"v_mfma_f32_16x16x32_bf16 v[136:139], a[88:91], a[152:155], v[136:139]\n\t"
"v_xor_b32 v251, 96, v248\n\t"
"v_mfma_f32_16x16x32_bf16 v[136:139], a[92:95], a[168:171], v[136:139]\n\t"
"ds_read_b128 a[152:155], v216 offset:2048\n\t"
"v_mfma_f32_16x16x32_bf16 v[208:211], a[176:179], a[220:223], 0\n\t"
"ds_read_b128 a[168:171], v216 offset:10240\n\t"
"v_mfma_f32_16x16x32_bf16 v[140:143], a[88:91], a[156:159], v[140:143]\n\t"
"ds_read_b64_tr_b16 a[176:177], v248 offset:0\n\t"
"v_mfma_f32_16x16x32_bf16 v[140:143], a[92:95], a[172:175], v[140:143]\n\t"
"buffer_load_dwordx4 a[88:91], v218, s[84:87], s48 offen offset:0 sc1\n\t"
"v_mfma_f32_16x16x32_bf16 v[212:215], a[180:183], a[220:223], 0\n\t"
"buffer_load_dwordx4 a[92:95], v218, s[84:87], s48 offen offset:1024 sc1\n\t"
"v_mfma_f32_16x16x32_bf16 v[124:127], a[80:83], a[156:159], v[124:127]\n\t"
"v_cvt_pk_bf16_f32 v208, v208, v209\n\t"
"v_cvt_pk_bf16_f32 v209, v210, v211\n\t"
"v_mfma_f32_16x16x32_bf16 v[124:127], a[84:87], a[172:175], v[124:127]\n\t"
"ds_read_b64_tr_b16 a[178:179], v248 offset:512\n\t"
"v_mfma_f32_16x16x32_bf16 v[108:111], a[72:75], a[156:159], v[108:111]\n\t"
"ds_read_b128 a[156:159], v216 offset:3072\n\t"
"v_mfma_f32_16x16x32_bf16 v[108:111], a[76:79], a[172:175], v[108:111]\n\t"
"buffer_load_dwordx4 a[84:87], v235, s[84:87], s48 offen offset:3072 sc1\n\t"
"v_mfma_f32_16x16x32_bf16 v[192:195], a[184:187], a[220:223], 0\n\t"
"v_cvt_pk_bf16_f32 v210, v212, v213\n\t"
"s_waitcnt lgkmcnt(5)\n\t"
"v_mfma_f32_16x16x32_bf16 v[48:51], a[96:99], a[144:147], v[48:51]\n\t"
"v_cvt_pk_bf16_f32 v211, v214, v215\n\t"
"ds_read_b128 a[172:175], v216 offset:11264\n\t"
"v_mfma_f32_16x16x32_bf16 v[48:51], a[100:103], a[160:163], v[48:51]\n\t"
"buffer_load_dwordx4 a[80:83], v235, s[84:87], s48 offen offset:2048 sc1\n\t"
"v_mfma_f32_16x16x32_bf16 v[196:199], a[188:191], a[220:223], 0\n\t"
"buffer_load_dwordx4 a[72:75], v235, s[84:87], s48 offen offset:0 sc1\n\t"
"v_mfma_f32_16x16x32_bf16 v[64:67], a[104:107], a[144:147], v[64:67]\n\t"
"v_mfma_f32_16x16x32_bf16 v[64:67], a[108:111], a[160:163], v[64:67]\n\t"
"v_cvt_pk_bf16_f32 v192, v192, v193\n\t"
"v_mfma_f32_16x16x32_bf16 v[80:83], a[112:115], a[144:147], v[80:83]\n\t"
"v_cvt_pk_bf16_f32 v193, v194, v195\n\t"
"ds_read_b128 a[144:147], v216 offset:4096\n\t"
"v_mfma_f32_16x16x32_bf16 v[80:83], a[116:119], a[160:163], v[80:83]\n\t"
"buffer_load_dwordx4 a[76:79], v235, s[84:87], s48 offen offset:1024 sc1\n\t"
"v_mfma_f32_16x16x32_bf16 v[200:203], a[192:195], a[220:223], 0\n\t"
"v_cvt_pk_bf16_f32 v194, v196, v197\n\t"
"v_mfma_f32_16x16x32_bf16 v[84:87], a[112:115], a[148:151], v[84:87]\n\t"
"v_cvt_pk_bf16_f32 v195, v198, v199\n\t"
"v_mfma_f32_16x16x32_bf16 v[84:87], a[116:119], a[164:167], v[84:87]\n\t"
"ds_write_b128 v217, v[208:211] offset:16384\n\t"
"v_mfma_f32_16x16x32_bf16 v[68:71], a[104:107], a[148:151], v[68:71]\n\t"
"ds_read_b128 a[160:163], v216 offset:12288\n\t"
"v_mfma_f32_16x16x32_bf16 v[68:71], a[108:111], a[164:167], v[68:71]\n\t"
"v_cvt_pk_bf16_f32 v200, v200, v201\n\t"
"v_mfma_f32_16x16x32_bf16 v[204:207], a[196:199], a[220:223], 0\n\t"
"ds_write_b128 v219, v[192:195] offset:16384\n\t"
"v_mfma_f32_16x16x32_bf16 v[52:55], a[96:99], a[148:151], v[52:55]\n\t"
"v_cvt_pk_bf16_f32 v201, v202, v203\n\t"
"v_mfma_f32_16x16x32_bf16 v[52:55], a[100:103], a[164:167], v[52:55]\n\t"
"ds_read_b128 a[148:151], v216 offset:5120\n\t"
"v_mfma_f32_16x16x32_bf16 v[208:211], a[200:203], a[220:223], 0\n\t"
"ds_read_b128 a[164:167], v216 offset:13312\n\t"
"s_waitcnt lgkmcnt(5)\n\t"
"v_mfma_f32_16x16x32_bf16 v[56:59], a[96:99], a[152:155], v[56:59]\n\t"
"v_cvt_pk_bf16_f32 v202, v204, v205\n\t"
"v_cvt_pk_bf16_f32 v203, v206, v207\n\t"
"v_mfma_f32_16x16x32_bf16 v[56:59], a[100:103], a[168:171], v[56:59]\n\t"
"ds_write_b128 v217, v[200:203] offset:24576\n\t"
"v_mfma_f32_16x16x32_bf16 v[72:75], a[104:107], a[152:155], v[72:75]\n\t"
"ds_read_b64_tr_b16 a[180:181], v249 offset:0\n\t"
"v_mfma_f32_16x16x32_bf16 v[72:75], a[108:111], a[168:171], v[72:75]\n\t"
"v_cvt_pk_bf16_f32 v208, v208, v209\n\t"
"v_mfma_f32_16x16x32_bf16 v[212:215], a[204:207], a[220:223], 0\n\t"
"v_cvt_pk_bf16_f32 v209, v210, v211\n\t"
"ds_read_b64_tr_b16 a[182:183], v249 offset:512\n\t"
"v_mfma_f32_16x16x32_bf16 v[88:91], a[112:115], a[152:155], v[88:91]\n\t"
"ds_read_b64_tr_b16 a[184:185], v250 offset:0\n\t"
"v_mfma_f32_16x16x32_bf16 v[88:91], a[116:119], a[168:171], v[88:91]\n\t"
"ds_read_b64_tr_b16 a[186:187], v250 offset:512\n\t"
"v_mfma_f32_16x16x32_bf16 v[92:95], a[112:115], a[156:159], v[92:95]\n\t"
"ds_read_b64_tr_b16 a[188:189], v251 offset:0\n\t"
"v_mfma_f32_16x16x32_bf16 v[92:95], a[116:119], a[172:175], v[92:95]\n\t"
"ds_read_b64_tr_b16 a[190:191], v251 offset:512\n\t"
"v_cvt_pk_bf16_f32 v210, v212, v213\n\t"
"v_mfma_f32_16x16x32_bf16 v[76:79], a[104:107], a[156:159], v[76:79]\n\t"
"ds_read_b64_tr_b16 a[192:193], v248 offset:1152\n\t"
"v_cvt_pk_bf16_f32 v211, v214, v215\n\t"
"v_mfma_f32_16x16x32_bf16 v[76:79], a[108:111], a[172:175], v[76:79]\n\t"
"ds_read_b64_tr_b16 a[194:195], v248 offset:1664\n\t"
"v_mfma_f32_16x16x32_bf16 v[60:63], a[96:99], a[156:159], v[60:63]\n\t"
"ds_read_b64_tr_b16 a[196:197], v249 offset:1152\n\t"
"v_mfma_f32_16x16x32_bf16 v[60:63], a[100:103], a[172:175], v[60:63]\n\t"
"ds_write_b128 v219, v[208:211] offset:24576\n\t"
"s_waitcnt lgkmcnt(9)\n\t"
"v_mfma_f32_16x16x32_bf16 v[144:147], a[120:123], a[144:147], v[144:147]\n\t"
"ds_read_b64_tr_b16 a[198:199], v249 offset:1664\n\t"
"v_mfma_f32_16x16x32_bf16 v[144:147], a[124:127], a[160:163], v[144:147]\n\t"
"ds_read_b64_tr_b16 a[200:201], v250 offset:1152\n\t"
"ds_read_b64_tr_b16 a[202:203], v250 offset:1664\n\t"
"ds_read_b64_tr_b16 a[204:205], v251 offset:1152\n\t"
"ds_read_b64_tr_b16 a[206:207], v251 offset:1664\n\t"
"v_mfma_f32_16x16x32_bf16 v[160:163], a[128:131], a[144:147], v[160:163]\n\t"
"v_mfma_f32_16x16x32_bf16 v[160:163], a[132:135], a[160:163], v[160:163]\n\t"
"ds_read_b128 a[152:155], v216 offset:6144\n\t"
"v_mfma_f32_16x16x32_bf16 v[176:179], a[136:139], a[144:147], v[176:179]\n\t"
"ds_read_b128 a[168:171], v216 offset:14336\n\t"
"v_mfma_f32_16x16x32_bf16 v[176:179], a[140:143], a[160:163], v[176:179]\n\t"
"ds_read_b128 a[156:159], v216 offset:7168\n\t"
"v_mfma_f32_16x16x32_bf16 v[180:183], a[136:139], a[148:151], v[180:183]\n\t"
"ds_read_b128 a[172:175], v216 offset:15360\n\t"
"v_mfma_f32_16x16x32_bf16 v[180:183], a[140:143], a[164:167], v[180:183]\n\t"
"ds_write_b128 v223, a[244:247] offset:43776\n\t"
"v_mfma_f32_16x16x32_bf16 v[164:167], a[128:131], a[148:151], v[164:167]\n\t"
"v_mfma_f32_16x16x32_bf16 v[164:167], a[132:135], a[164:167], v[164:167]\n\t"
"v_mfma_f32_16x16x32_bf16 v[148:151], a[120:123], a[148:151], v[148:151]\n\t"
"v_mfma_f32_16x16x32_bf16 v[148:151], a[124:127], a[164:167], v[148:151]\n\t"
"s_waitcnt lgkmcnt(0)\n\t"
"s_barrier\n\t"
"v_mfma_f32_16x16x32_bf16 v[152:155], a[120:123], a[152:155], v[152:155]\n\t"
"ds_read_b128 a[144:147], v216 offset:16384\n\t"
"v_add_u32 v248, 53504, v226\n\t"
"v_mfma_f32_16x16x32_bf16 v[152:155], a[124:127], a[168:171], v[152:155]\n\t"
"s_add_u32 m0, s26, 145280\n\t"
"ds_read_b128 a[160:163], v216 offset:24576\n\t"
"ds_read_b128 a[148:151], v216 offset:17408\n\t"
"buffer_load_dwordx4 v236, s[12:15], s22 offen offset:128 nt lds\n\t"
"v_mfma_f32_16x16x32_bf16 v[168:171], a[128:131], a[152:155], v[168:171]\n\t"
"ds_read_b128 a[164:167], v216 offset:25600\n\t"
"v_mfma_f32_16x16x32_bf16 v[168:171], a[132:135], a[168:171], v[168:171]\n\t"
"s_xor_b32 s24, s24, 384\n\t"
"s_xor_b32 s20, s20, 384\n\t"
"s_xor_b32 s27, s27, 384\n\t"
"s_xor_b32 s25, s25, 384\n\t"
"v_mfma_f32_16x16x32_bf16 v[184:187], a[136:139], a[152:155], v[184:187]\n\t"
"v_xor_b32 v249, 32, v248\n\t"
"v_mfma_f32_16x16x32_bf16 v[184:187], a[140:143], a[168:171], v[184:187]\n\t"
"v_xor_b32 v250, 64, v248\n\t"
"v_mfma_f32_16x16x32_bf16 v[192:195], a[176:179], v[252:255], 0\n\t"
"v_xor_b32 v251, 96, v248\n\t"
"ds_read_b128 a[152:155], v216 offset:18432\n\t"
"v_mfma_f32_16x16x32_bf16 v[188:191], a[136:139], a[156:159], v[188:191]\n\t"
"ds_read_b128 a[168:171], v216 offset:26624\n\t"
"v_mfma_f32_16x16x32_bf16 v[188:191], a[140:143], a[172:175], v[188:191]\n\t"
"buffer_load_dwordx4 v237, s[12:15], s22 offen offset:1152 nt lds\n\t"
"v_mfma_f32_16x16x32_bf16 v[196:199], a[180:183], v[252:255], 0\n\t"
"ds_read_b64_tr_b16 a[176:177], v248 offset:48128\n\t"
"v_mfma_f32_16x16x32_bf16 v[172:175], a[128:131], a[156:159], v[172:175]\n\t"
"v_cvt_pk_bf16_f32 v192, v192, v193\n\t"
"v_mfma_f32_16x16x32_bf16 v[172:175], a[132:135], a[172:175], v[172:175]\n\t"
"v_cvt_pk_bf16_f32 v193, v194, v195\n\t"
"v_mfma_f32_16x16x32_bf16 v[156:159], a[120:123], a[156:159], v[156:159]\n\t"
"ds_read_b128 a[156:159], v216 offset:19456\n\t"
"v_mfma_f32_16x16x32_bf16 v[156:159], a[124:127], a[172:175], v[156:159]\n\t"
"buffer_load_dwordx4 v238, s[12:15], s22 offen offset:2176 nt lds\n\t"
"v_mfma_f32_16x16x32_bf16 v[200:203], a[184:187], v[252:255], 0\n\t"
"v_pk_add_f32 v[144:145], v[48:49], v[144:145] neg_lo:[0,1] neg_hi:[0,1]\n\t"
"v_pk_add_f32 v[146:147], v[50:51], v[146:147] neg_lo:[0,1] neg_hi:[0,1]\n\t"
"v_pk_fma_f32 v[48:49], v[144:145], s[90:91], v[48:49] neg_lo:[1,0,0] neg_hi:[1,0,0]\n\t"
"v_pk_fma_f32 v[50:51], v[146:147], s[90:91], v[50:51] neg_lo:[1,0,0] neg_hi:[1,0,0]\n\t"
"v_pk_add_f32 v[160:161], v[64:65], v[160:161] neg_lo:[0,1] neg_hi:[0,1]\n\t"
"v_pk_add_f32 v[162:163], v[66:67], v[162:163] neg_lo:[0,1] neg_hi:[0,1]\n\t"
"v_pk_fma_f32 v[64:65], v[160:161], s[90:91], v[64:65] neg_lo:[1,0,0] neg_hi:[1,0,0]\n\t"
"v_pk_fma_f32 v[66:67], v[162:163], s[90:91], v[66:67] neg_lo:[1,0,0] neg_hi:[1,0,0]\n\t"
"v_pk_add_f32 v[176:177], v[80:81], v[176:177] neg_lo:[0,1] neg_hi:[0,1]\n\t"
"v_pk_add_f32 v[178:179], v[82:83], v[178:179] neg_lo:[0,1] neg_hi:[0,1]\n\t"
"v_pk_fma_f32 v[80:81], v[176:177], s[90:91], v[80:81] neg_lo:[1,0,0] neg_hi:[1,0,0]\n\t"
"v_pk_fma_f32 v[82:83], v[178:179], s[90:91], v[82:83] neg_lo:[1,0,0] neg_hi:[1,0,0]\n\t"
"v_pk_add_f32 v[180:181], v[84:85], v[180:181] neg_lo:[0,1] neg_hi:[0,1]\n\t"
"v_pk_add_f32 v[182:183], v[86:87], v[182:183] neg_lo:[0,1] neg_hi:[0,1]\n\t"
"v_pk_fma_f32 v[84:85], v[180:181], s[90:91], v[84:85] neg_lo:[1,0,0] neg_hi:[1,0,0]\n\t"
"v_pk_fma_f32 v[86:87], v[182:183], s[90:91], v[86:87] neg_lo:[1,0,0] neg_hi:[1,0,0]\n\t"
"v_pk_add_f32 v[164:165], v[68:69], v[164:165] neg_lo:[0,1] neg_hi:[0,1]\n\t"
"v_pk_add_f32 v[166:167], v[70:71], v[166:167] neg_lo:[0,1] neg_hi:[0,1]\n\t"
"v_pk_fma_f32 v[68:69], v[164:165], s[90:91], v[68:69] neg_lo:[1,0,0] neg_hi:[1,0,0]\n\t"
"v_pk_fma_f32 v[70:71], v[166:167], s[90:91], v[70:71] neg_lo:[1,0,0] neg_hi:[1,0,0]\n\t"
"v_pk_add_f32 v[148:149], v[52:53], v[148:149] neg_lo:[0,1] neg_hi:[0,1]\n\t"
"v_pk_add_f32 v[150:151], v[54:55], v[150:151] neg_lo:[0,1] neg_hi:[0,1]\n\t"
"v_pk_fma_f32 v[52:53], v[148:149], s[90:91], v[52:53] neg_lo:[1,0,0] neg_hi:[1,0,0]\n\t"
"v_pk_fma_f32 v[54:55], v[150:151], s[90:91], v[54:55] neg_lo:[1,0,0] neg_hi:[1,0,0]\n\t"
"v_pk_add_f32 v[152:153], v[56:57], v[152:153] neg_lo:[0,1] neg_hi:[0,1]\n\t"
"v_pk_add_f32 v[154:155], v[58:59], v[154:155] neg_lo:[0,1] neg_hi:[0,1]\n\t"
"v_pk_fma_f32 v[56:57], v[152:153], s[90:91], v[56:57] neg_lo:[1,0,0] neg_hi:[1,0,0]\n\t"
"v_pk_fma_f32 v[58:59], v[154:155], s[90:91], v[58:59] neg_lo:[1,0,0] neg_hi:[1,0,0]\n\t"
"v_pk_add_f32 v[168:169], v[72:73], v[168:169] neg_lo:[0,1] neg_hi:[0,1]\n\t"
"v_pk_add_f32 v[170:171], v[74:75], v[170:171] neg_lo:[0,1] neg_hi:[0,1]\n\t"
"v_pk_fma_f32 v[72:73], v[168:169], s[90:91], v[72:73] neg_lo:[1,0,0] neg_hi:[1,0,0]\n\t"
"v_pk_fma_f32 v[74:75], v[170:171], s[90:91], v[74:75] neg_lo:[1,0,0] neg_hi:[1,0,0]\n\t"
"v_pk_add_f32 v[184:185], v[88:89], v[184:185] neg_lo:[0,1] neg_hi:[0,1]\n\t"
"v_pk_add_f32 v[186:187], v[90:91], v[186:187] neg_lo:[0,1] neg_hi:[0,1]\n\t"
"v_pk_fma_f32 v[88:89], v[184:185], s[90:91], v[88:89] neg_lo:[1,0,0] neg_hi:[1,0,0]\n\t"
"v_pk_fma_f32 v[90:91], v[186:187], s[90:91], v[90:91] neg_lo:[1,0,0] neg_hi:[1,0,0]\n\t"
"v_pk_add_f32 v[188:189], v[92:93], v[188:189] neg_lo:[0,1] neg_hi:[0,1]\n\t"
"v_pk_add_f32 v[190:191], v[94:95], v[190:191] neg_lo:[0,1] neg_hi:[0,1]\n\t"
"v_pk_fma_f32 v[92:93], v[188:189], s[90:91], v[92:93] neg_lo:[1,0,0] neg_hi:[1,0,0]\n\t"
"v_pk_fma_f32 v[94:95], v[190:191], s[90:91], v[94:95] neg_lo:[1,0,0] neg_hi:[1,0,0]\n\t"
"v_pk_add_f32 v[172:173], v[76:77], v[172:173] neg_lo:[0,1] neg_hi:[0,1]\n\t"
"v_pk_add_f32 v[174:175], v[78:79], v[174:175] neg_lo:[0,1] neg_hi:[0,1]\n\t"
"v_pk_fma_f32 v[76:77], v[172:173], s[90:91], v[76:77] neg_lo:[1,0,0] neg_hi:[1,0,0]\n\t"
"v_pk_fma_f32 v[78:79], v[174:175], s[90:91], v[78:79] neg_lo:[1,0,0] neg_hi:[1,0,0]\n\t"
"v_pk_add_f32 v[156:157], v[60:61], v[156:157] neg_lo:[0,1] neg_hi:[0,1]\n\t"
"v_pk_add_f32 v[158:159], v[62:63], v[158:159] neg_lo:[0,1] neg_hi:[0,1]\n\t"
"v_pk_fma_f32 v[60:61], v[156:157], s[90:91], v[60:61] neg_lo:[1,0,0] neg_hi:[1,0,0]\n\t"
"v_pk_fma_f32 v[62:63], v[158:159], s[90:91], v[62:63] neg_lo:[1,0,0] neg_hi:[1,0,0]\n\t"
"v_cvt_pk_bf16_f32 v194, v196, v197\n\t"
"s_waitcnt lgkmcnt(4)\n\t"
"v_mfma_f32_16x16x32_bf16 v[48:51], a[0:3], a[144:147], v[48:51]\n\t"
"v_cvt_pk_bf16_f32 v195, v198, v199\n\t"
"ds_read_b128 a[172:175], v216 offset:27648\n\t"
"v_mfma_f32_16x16x32_bf16 v[48:51], a[4:7], a[160:163], v[48:51]\n\t"
"ds_read_b64_tr_b16 a[178:179], v248 offset:48640\n\t"
"v_mfma_f32_16x16x32_bf16 v[204:207], a[188:191], v[252:255], 0\n\t"
"ds_read_b64_tr_b16 a[180:181], v249 offset:48128\n\t"
"v_mfma_f32_16x16x32_bf16 v[64:67], a[8:11], a[144:147], v[64:67]\n\t"
"v_cvt_pk_bf16_f32 v200, v200, v201\n\t"
"v_cvt_pk_bf16_f32 v201, v202, v203\n\t"
"v_mfma_f32_16x16x32_bf16 v[64:67], a[12:15], a[160:163], v[64:67]\n\t"
"buffer_load_dwordx4 v239, s[12:15], s22 offen offset:3200 nt lds\n\t"
"v_mfma_f32_16x16x32_bf16 v[80:83], a[16:19], a[144:147], v[80:83]\n\t"
"v_mfma_f32_16x16x32_bf16 v[80:83], a[20:23], a[160:163], v[80:83]\n\t"
"ds_write_b128 v221, v[192:195] offset:0\n\t"
"v_mfma_f32_16x16x32_bf16 v[208:211], a[192:195], v[252:255], 0\n\t"
"v_cvt_pk_bf16_f32 v202, v204, v205\n\t"
"v_mfma_f32_16x16x32_bf16 v[84:87], a[16:19], a[148:151], v[84:87]\n\t"
"v_cvt_pk_bf16_f32 v203, v206, v207\n\t"
"v_mfma_f32_16x16x32_bf16 v[84:87], a[20:23], a[164:167], v[84:87]\n\t"
"ds_read_b128 a[144:147], v216 offset:20480\n\t"
"v_mfma_f32_16x16x32_bf16 v[68:71], a[8:11], a[148:151], v[68:71]\n\t"
"ds_read_b128 a[160:163], v216 offset:28672\n\t"
"v_mfma_f32_16x16x32_bf16 v[68:71], a[12:15], a[164:167], v[68:71]\n\t"
"s_mov_b64 exec, s[36:37]\n\t"
"buffer_load_dwordx4 v240, s[12:15], s22 offen offset:3712 nt lds\n\t"
"s_mov_b64 exec, -1\n\t"
"v_mfma_f32_16x16x32_bf16 v[212:215], a[196:199], v[252:255], 0\n\t"
"v_cvt_pk_bf16_f32 v208, v208, v209\n\t"
"v_mfma_f32_16x16x32_bf16 v[52:55], a[0:3], a[148:151], v[52:55]\n\t"
"v_cvt_pk_bf16_f32 v209, v210, v211\n\t"
"ds_read_b128 a[148:151], v216 offset:21504\n\t"
"v_mfma_f32_16x16x32_bf16 v[52:55], a[4:7], a[164:167], v[52:55]\n\t"
"ds_read_b128 a[164:167], v216 offset:29696\n\t"
"v_mfma_f32_16x16x32_bf16 v[192:195], a[200:203], v[252:255], 0\n\t"
"ds_read_b64_tr_b16 a[182:183], v249 offset:48640\n\t"
"s_waitcnt lgkmcnt(6)\n\t"
"v_mfma_f32_16x16x32_bf16 v[56:59], a[0:3], a[152:155], v[56:59]\n\t"
"v_cvt_pk_bf16_f32 v210, v212, v213\n\t"
"v_cvt_pk_bf16_f32 v211, v214, v215\n\t"
"v_mfma_f32_16x16x32_bf16 v[56:59], a[4:7], a[168:171], v[56:59]\n\t"
"ds_write_b128 v221, v[200:203] offset:8192\n\t"
"v_mfma_f32_16x16x32_bf16 v[72:75], a[8:11], a[152:155], v[72:75]\n\t"
"v_mfma_f32_16x16x32_bf16 v[72:75], a[12:15], a[168:171], v[72:75]\n\t"
"v_cvt_pk_bf16_f32 v192, v192, v193\n\t"
"v_cvt_pk_bf16_f32 v193, v194, v195\n\t"
"v_mfma_f32_16x16x32_bf16 v[196:199], a[204:207], v[252:255], 0\n\t"
"ds_read_b64_tr_b16 a[184:185], v250 offset:50432\n\t"
"v_mfma_f32_16x16x32_bf16 v[88:91], a[16:19], a[152:155], v[88:91]\n\t"
"ds_read_b64_tr_b16 a[186:187], v250 offset:50944\n\t"
"v_mfma_f32_16x16x32_bf16 v[88:91], a[20:23], a[168:171], v[88:91]\n\t"
"ds_write_b128 v221, v[208:211] offset:4096\n\t"
"v_mfma_f32_16x16x32_bf16 v[92:95], a[16:19], a[156:159], v[92:95]\n\t"
"ds_read_b64_tr_b16 a[188:189], v251 offset:50432\n\t"
"v_mfma_f32_16x16x32_bf16 v[92:95], a[20:23], a[172:175], v[92:95]\n\t"
"ds_read_b64_tr_b16 a[190:191], v251 offset:50944\n\t"
"v_mfma_f32_16x16x32_bf16 v[76:79], a[8:11], a[156:159], v[76:79]\n\t"
"ds_read_b64_tr_b16 a[192:193], v250 offset:48128\n\t"
"v_cvt_pk_bf16_f32 v194, v196, v197\n\t"
"v_mfma_f32_16x16x32_bf16 v[76:79], a[12:15], a[172:175], v[76:79]\n\t"
"ds_read_b64_tr_b16 a[194:195], v250 offset:48640\n\t"
"v_mfma_f32_16x16x32_bf16 v[60:63], a[0:3], a[156:159], v[60:63]\n\t"
"ds_read_b64_tr_b16 a[196:197], v251 offset:48128\n\t"
"v_mfma_f32_16x16x32_bf16 v[60:63], a[4:7], a[172:175], v[60:63]\n\t"
"ds_read_b64_tr_b16 a[198:199], v251 offset:48640\n\t"
"v_cvt_pk_bf16_f32 v195, v198, v199\n\t"
"s_waitcnt vmcnt(17) lgkmcnt(9)\n\t"
"v_mfma_f32_16x16x32_bf16 v[144:147], a[24:27], a[144:147], v[144:147]\n\t"
"ds_read_b64_tr_b16 a[200:201], v248 offset:50432\n\t"
"v_mfma_f32_16x16x32_bf16 v[144:147], a[28:31], a[160:163], v[144:147]\n\t"
"ds_write_b128 v221, v[192:195] offset:12288\n\t"
"ds_read_b64_tr_b16 a[202:203], v248 offset:50944\n\t"
"ds_read_b64_tr_b16 a[204:205], v249 offset:50432\n\t"
"ds_read_b64_tr_b16 a[206:207], v249 offset:50944\n\t"
"v_mfma_f32_16x16x32_bf16 v[160:163], a[32:35], a[144:147], v[160:163]\n\t"
"v_mfma_f32_16x16x32_bf16 v[160:163], a[36:39], a[160:163], v[160:163]\n\t"
"v_mfma_f32_16x16x32_bf16 v[176:179], a[40:43], a[144:147], v[176:179]\n\t"
"v_mfma_f32_16x16x32_bf16 v[176:179], a[44:47], a[160:163], v[176:179]\n\t"
"ds_read_b128 a[152:155], v216 offset:22528\n\t"
"v_mfma_f32_16x16x32_bf16 v[180:183], a[40:43], a[148:151], v[180:183]\n\t"
"ds_read_b128 a[168:171], v216 offset:30720\n\t"
"v_mfma_f32_16x16x32_bf16 v[180:183], a[44:47], a[164:167], v[180:183]\n\t"
"ds_read_b128 a[156:159], v216 offset:23552\n\t"
"ds_read_b128 a[172:175], v216 offset:31744\n\t"
"v_mfma_f32_16x16x32_bf16 v[164:167], a[32:35], a[148:151], v[164:167]\n\t"
"v_mfma_f32_16x16x32_bf16 v[164:167], a[36:39], a[164:167], v[164:167]\n\t"
"v_mfma_f32_16x16x32_bf16 v[148:151], a[24:27], a[148:151], v[148:151]\n\t"
"v_mfma_f32_16x16x32_bf16 v[148:151], a[28:31], a[164:167], v[148:151]\n\t"
"s_waitcnt lgkmcnt(0)\n\t"
"s_barrier\n\t"
"v_mfma_f32_16x16x32_bf16 v[152:155], a[24:27], a[152:155], v[152:155]\n\t"
"ds_read_b128 a[144:147], v216 offset:0\n\t"
"v_mfma_f32_16x16x32_bf16 v[152:155], a[28:31], a[168:171], v[152:155]\n\t"
"ds_read_b128 a[160:163], v216 offset:8192\n\t"
"ds_read_b128 a[148:151], v216 offset:1024\n\t"
"v_mfma_f32_16x16x32_bf16 v[168:171], a[32:35], a[152:155], v[168:171]\n\t"
"ds_read_b128 a[164:167], v216 offset:9216\n\t"
"v_mfma_f32_16x16x32_bf16 v[168:171], a[36:39], a[168:171], v[168:171]\n\t"
"v_mfma_f32_16x16x32_bf16 v[184:187], a[40:43], a[152:155], v[184:187]\n\t"
"ds_read_b128 a[152:155], v216 offset:2048\n\t"
"v_mfma_f32_16x16x32_bf16 v[184:187], a[44:47], a[168:171], v[184:187]\n\t"
"ds_read_b128 a[168:171], v216 offset:10240\n\t"
"v_mfma_f32_16x16x32_bf16 v[200:203], a[176:179], a[220:223], 0\n\t"
"ds_read_b64_tr_b16 a[176:177], v248 offset:0\n\t"
"v_mfma_f32_16x16x32_bf16 v[188:191], a[40:43], a[156:159], v[188:191]\n\t"
"ds_read_b64_tr_b16 a[178:179], v248 offset:512\n\t"
"v_mfma_f32_16x16x32_bf16 v[188:191], a[44:47], a[172:175], v[188:191]\n\t"
"ds_write_b128 v223, a[240:243] offset:2304\n\t"
"v_mfma_f32_16x16x32_bf16 v[204:207], a[180:183], a[220:223], 0\n\t"
"ds_read_b64_tr_b16 a[180:181], v249 offset:0\n\t"
"v_mfma_f32_16x16x32_bf16 v[172:175], a[32:35], a[156:159], v[172:175]\n\t"
"v_cvt_pk_bf16_f32 v200, v200, v201\n\t"
"v_cvt_pk_bf16_f32 v201, v202, v203\n\t"
"v_mfma_f32_16x16x32_bf16 v[172:175], a[36:39], a[172:175], v[172:175]\n\t"
"ds_read_b64_tr_b16 a[182:183], v249 offset:512\n\t"
"v_mfma_f32_16x16x32_bf16 v[156:159], a[24:27], a[156:159], v[156:159]\n\t"
"ds_read_b128 a[156:159], v216 offset:3072\n\t"
"v_mfma_f32_16x16x32_bf16 v[156:159], a[28:31], a[172:175], v[156:159]\n\t"
"v_cvt_pk_bf16_f32 v202, v204, v205\n\t"
"v_cvt_pk_bf16_f32 v203, v206, v207\n\t"
"v_mfma_f32_16x16x32_bf16 v[208:211], a[184:187], a[220:223], 0\n\t"
"ds_read_b128 a[172:175], v216 offset:11264\n\t"
"s_waitcnt lgkmcnt(8)\n\t"
"v_mfma_f32_16x16x32_bf16 v[0:3], a[96:99], a[144:147], v[0:3]\n\t"
"ds_read_b64_tr_b16 a[184:185], v250 offset:2304\n\t"
"v_mfma_f32_16x16x32_bf16 v[0:3], a[100:103], a[160:163], v[0:3]\n\t"
"ds_write_b128 v217, v[200:203] offset:16384\n\t"
"v_mfma_f32_16x16x32_bf16 v[212:215], a[188:191], a[220:223], 0\n\t"
"ds_read_b64_tr_b16 a[186:187], v250 offset:2816\n\t"
"v_mfma_f32_16x16x32_bf16 v[16:19], a[104:107], a[144:147], v[16:19]\n\t"
"v_cvt_pk_bf16_f32 v208, v208, v209\n\t"
"v_cvt_pk_bf16_f32 v209, v210, v211\n\t"
"v_mfma_f32_16x16x32_bf16 v[16:19], a[108:111], a[160:163], v[16:19]\n\t"
"ds_read_b64_tr_b16 a[188:189], v251 offset:2304\n\t"
"v_mfma_f32_16x16x32_bf16 v[32:35], a[112:115], a[144:147], v[32:35]\n\t"
"ds_read_b128 a[144:147], v216 offset:4096\n\t"
"v_mfma_f32_16x16x32_bf16 v[32:35], a[116:119], a[160:163], v[32:35]\n\t"
"v_cvt_pk_bf16_f32 v210, v212, v213\n\t"
"v_cvt_pk_bf16_f32 v211, v214, v215\n\t"
"v_mfma_f32_16x16x32_bf16 v[192:195], a[192:195], a[220:223], 0\n\t"
"ds_read_b128 a[160:163], v216 offset:12288\n\t"
"v_mfma_f32_16x16x32_bf16 v[36:39], a[112:115], a[148:151], v[36:39]\n\t"
"ds_read_b64_tr_b16 a[190:191], v251 offset:2816\n\t"
"v_mfma_f32_16x16x32_bf16 v[36:39], a[116:119], a[164:167], v[36:39]\n\t"
"ds_write_b128 v219, v[208:211] offset:16384\n\t"
"v_mfma_f32_16x16x32_bf16 v[20:23], a[104:107], a[148:151], v[20:23]\n\t"
"ds_read_b64_tr_b16 a[192:193], v250 offset:0\n\t"
"v_mfma_f32_16x16x32_bf16 v[20:23], a[108:111], a[164:167], v[20:23]\n\t"
"v_cvt_pk_bf16_f32 v192, v192, v193\n\t"
"v_cvt_pk_bf16_f32 v193, v194, v195\n\t"
"v_mfma_f32_16x16x32_bf16 v[196:199], a[196:199], a[220:223], 0\n\t"
"ds_read_b64_tr_b16 a[194:195], v250 offset:512\n\t"
"v_mfma_f32_16x16x32_bf16 v[4:7], a[96:99], a[148:151], v[4:7]\n\t"
"ds_read_b128 a[148:151], v216 offset:5120\n\t"
"v_mfma_f32_16x16x32_bf16 v[4:7], a[100:103], a[164:167], v[4:7]\n\t"
"ds_read_b128 a[164:167], v216 offset:13312\n\t"
"v_mfma_f32_16x16x32_bf16 v[200:203], a[200:203], a[220:223], 0\n\t"
"ds_read_b64_tr_b16 a[196:197], v251 offset:0\n\t"
"s_waitcnt lgkmcnt(8)\n\t"
"v_mfma_f32_16x16x32_bf16 v[8:11], a[96:99], a[152:155], v[8:11]\n\t"
"v_cvt_pk_bf16_f32 v194, v196, v197\n\t"
"v_cvt_pk_bf16_f32 v195, v198, v199\n\t"
"v_mfma_f32_16x16x32_bf16 v[8:11], a[100:103], a[168:171], v[8:11]\n\t"
"ds_write_b128 v217, v[192:195] offset:24576\n\t"
"v_mfma_f32_16x16x32_bf16 v[24:27], a[104:107], a[152:155], v[24:27]\n\t"
"ds_read_b64_tr_b16 a[198:199], v251 offset:512\n\t"
"v_mfma_f32_16x16x32_bf16 v[24:27], a[108:111], a[168:171], v[24:27]\n\t"
"v_cvt_pk_bf16_f32 v200, v200, v201\n\t"
"v_cvt_pk_bf16_f32 v201, v202, v203\n\t"
"v_mfma_f32_16x16x32_bf16 v[204:207], a[204:207], a[220:223], 0\n\t"
"ds_read_b64_tr_b16 a[200:201], v248 offset:2304\n\t"
"v_mfma_f32_16x16x32_bf16 v[40:43], a[112:115], a[152:155], v[40:43]\n\t"
"ds_read_b64_tr_b16 a[202:203], v248 offset:2816\n\t"
"v_mfma_f32_16x16x32_bf16 v[40:43], a[116:119], a[168:171], v[40:43]\n\t"
"ds_read_b64_tr_b16 a[204:205], v249 offset:2304\n\t"
"v_mfma_f32_16x16x32_bf16 v[44:47], a[112:115], a[156:159], v[44:47]\n\t"
"ds_read_b64_tr_b16 a[206:207], v249 offset:2816\n\t"
"v_mfma_f32_16x16x32_bf16 v[44:47], a[116:119], a[172:175], v[44:47]\n\t"
"buffer_load_dwordx4 a[112:115], v218, s[84:87], s75 offen offset:0 sc1\n\t"
"v_mfma_f32_16x16x32_bf16 v[28:31], a[104:107], a[156:159], v[28:31]\n\t"
"v_cvt_pk_bf16_f32 v202, v204, v205\n\t"
"v_cvt_pk_bf16_f32 v203, v206, v207\n\t"
"v_mfma_f32_16x16x32_bf16 v[28:31], a[108:111], a[172:175], v[28:31]\n\t"
"buffer_load_dwordx4 a[116:119], v218, s[84:87], s75 offen offset:1024 sc1\n\t"
"v_mfma_f32_16x16x32_bf16 v[12:15], a[96:99], a[156:159], v[12:15]\n\t"
"ds_read_b128 a[152:155], v216 offset:6144\n\t"
"v_mfma_f32_16x16x32_bf16 v[12:15], a[100:103], a[172:175], v[12:15]\n\t"
"buffer_load_dwordx4 a[108:111], v235, s[84:87], s75 offen offset:3072 sc1\n\t"
"s_waitcnt lgkmcnt(5)\n\t"
"v_mfma_f32_16x16x32_bf16 v[96:99], a[120:123], a[144:147], v[96:99]\n\t"
"ds_read_b128 a[168:171], v216 offset:14336\n\t"
"v_mfma_f32_16x16x32_bf16 v[96:99], a[124:127], a[160:163], v[96:99]\n\t"
"ds_write_b128 v219, v[200:203] offset:24576\n\t"
"buffer_load_dwordx4 a[104:107], v235, s[84:87], s75 offen offset:2048 sc1\n\t"
"v_mfma_f32_16x16x32_bf16 v[112:115], a[128:131], a[144:147], v[112:115]\n\t"
"v_mfma_f32_16x16x32_bf16 v[112:115], a[132:135], a[160:163], v[112:115]\n\t"
"buffer_load_dwordx4 a[96:99], v235, s[84:87], s75 offen offset:0 sc1\n\t"
"buffer_load_dwordx4 a[100:103], v235, s[84:87], s75 offen offset:1024 sc1\n\t"
"v_mfma_f32_16x16x32_bf16 v[128:131], a[136:139], a[144:147], v[128:131]\n\t"
"v_mfma_f32_16x16x32_bf16 v[128:131], a[140:143], a[160:163], v[128:131]\n\t"
"v_mfma_f32_16x16x32_bf16 v[132:135], a[136:139], a[148:151], v[132:135]\n\t"
"ds_read_b128 a[156:159], v216 offset:7168\n\t"
"v_mfma_f32_16x16x32_bf16 v[132:135], a[140:143], a[164:167], v[132:135]\n\t"
"ds_read_b128 a[172:175], v216 offset:15360\n\t"
"v_mfma_f32_16x16x32_bf16 v[116:119], a[128:131], a[148:151], v[116:119]\n\t"
"v_mfma_f32_16x16x32_bf16 v[116:119], a[132:135], a[164:167], v[116:119]\n\t"
"v_mfma_f32_16x16x32_bf16 v[100:103], a[120:123], a[148:151], v[100:103]\n\t"
"v_mfma_f32_16x16x32_bf16 v[100:103], a[124:127], a[164:167], v[100:103]\n\t"
"s_waitcnt lgkmcnt(0)\n\t"
"s_barrier\n\t"
"v_mfma_f32_16x16x32_bf16 v[104:107], a[120:123], a[152:155], v[104:107]\n\t"
"ds_read_b128 a[144:147], v216 offset:16384\n\t"
"v_add_u32 v248, 80896, v226\n\t"
"v_mfma_f32_16x16x32_bf16 v[104:107], a[124:127], a[168:171], v[104:107]\n\t"
"ds_read_b128 a[160:163], v216 offset:24576\n\t"
"ds_read_b128 a[148:151], v216 offset:17408\n\t"
"v_mfma_f32_16x16x32_bf16 v[120:123], a[128:131], a[152:155], v[120:123]\n\t"
"ds_read_b128 a[164:167], v216 offset:25600\n\t"
"v_xor_b32 v249, 32, v248\n\t"
"v_mfma_f32_16x16x32_bf16 v[120:123], a[132:135], a[168:171], v[120:123]\n\t"
"v_xor_b32 v250, 64, v248\n\t"
"v_mfma_f32_16x16x32_bf16 v[136:139], a[136:139], a[152:155], v[136:139]\n\t"
"v_xor_b32 v251, 96, v248\n\t"
"v_mfma_f32_16x16x32_bf16 v[136:139], a[140:143], a[168:171], v[136:139]\n\t"
"ds_read_b128 a[152:155], v216 offset:18432\n\t"
"v_mfma_f32_16x16x32_bf16 v[208:211], a[176:179], a[220:223], 0\n\t"
"ds_read_b128 a[168:171], v216 offset:26624\n\t"
"v_mfma_f32_16x16x32_bf16 v[140:143], a[136:139], a[156:159], v[140:143]\n\t"
"ds_read_b64_tr_b16 a[176:177], v248 offset:41472\n\t"
"v_mfma_f32_16x16x32_bf16 v[140:143], a[140:143], a[172:175], v[140:143]\n\t"
"buffer_load_dwordx4 a[136:139], v218, s[84:87], s78 offen offset:0 sc1\n\t"
"v_mfma_f32_16x16x32_bf16 v[212:215], a[180:183], a[220:223], 0\n\t"
"buffer_load_dwordx4 a[140:143], v218, s[84:87], s78 offen offset:1024 sc1\n\t"
"v_mfma_f32_16x16x32_bf16 v[124:127], a[128:131], a[156:159], v[124:127]\n\t"
"v_cvt_pk_bf16_f32 v208, v208, v209\n\t"
"v_mfma_f32_16x16x32_bf16 v[124:127], a[132:135], a[172:175], v[124:127]\n\t"
"buffer_load_dwordx4 a[132:135], v235, s[84:87], s78 offen offset:3072 sc1\n\t"
"v_mfma_f32_16x16x32_bf16 v[108:111], a[120:123], a[156:159], v[108:111]\n\t"
"v_cvt_pk_bf16_f32 v209, v210, v211\n\t"
"v_mfma_f32_16x16x32_bf16 v[108:111], a[124:127], a[172:175], v[108:111]\n\t"
"buffer_load_dwordx4 a[128:131], v235, s[84:87], s78 offen offset:2048 sc1\n\t"
"v_mfma_f32_16x16x32_bf16 v[192:195], a[184:187], a[220:223], 0\n\t"
"v_cvt_pk_bf16_f32 v210, v212, v213\n\t"
"s_waitcnt vmcnt(21) lgkmcnt(3)\n\t"
"v_mfma_f32_16x16x32_bf16 v[48:51], a[48:51], a[144:147], v[48:51]\n\t"
"v_cvt_pk_bf16_f32 v211, v214, v215\n\t"
"v_mfma_f32_16x16x32_bf16 v[48:51], a[52:55], a[160:163], v[48:51]\n\t"
"buffer_load_dwordx4 a[120:123], v235, s[84:87], s78 offen offset:0 sc1\n\t"
"v_mfma_f32_16x16x32_bf16 v[196:199], a[188:191], a[220:223], 0\n\t"
"buffer_load_dwordx4 a[124:127], v235, s[84:87], s78 offen offset:1024 sc1\n\t"
"v_pk_add_f32 v[96:97], v[0:1], v[96:97] neg_lo:[0,1] neg_hi:[0,1]\n\t"
"v_pk_add_f32 v[98:99], v[2:3], v[98:99] neg_lo:[0,1] neg_hi:[0,1]\n\t"
"v_pk_fma_f32 v[0:1], v[96:97], s[90:91], v[0:1] neg_lo:[1,0,0] neg_hi:[1,0,0]\n\t"
"v_pk_fma_f32 v[2:3], v[98:99], s[90:91], v[2:3] neg_lo:[1,0,0] neg_hi:[1,0,0]\n\t"
"v_pk_add_f32 v[112:113], v[16:17], v[112:113] neg_lo:[0,1] neg_hi:[0,1]\n\t"
"v_pk_add_f32 v[114:115], v[18:19], v[114:115] neg_lo:[0,1] neg_hi:[0,1]\n\t"
"v_pk_fma_f32 v[16:17], v[112:113], s[90:91], v[16:17] neg_lo:[1,0,0] neg_hi:[1,0,0]\n\t"
"v_pk_fma_f32 v[18:19], v[114:115], s[90:91], v[18:19] neg_lo:[1,0,0] neg_hi:[1,0,0]\n\t"
"v_pk_add_f32 v[128:129], v[32:33], v[128:129] neg_lo:[0,1] neg_hi:[0,1]\n\t"
"v_pk_add_f32 v[130:131], v[34:35], v[130:131] neg_lo:[0,1] neg_hi:[0,1]\n\t"
"v_pk_fma_f32 v[32:33], v[128:129], s[90:91], v[32:33] neg_lo:[1,0,0] neg_hi:[1,0,0]\n\t"
"v_pk_fma_f32 v[34:35], v[130:131], s[90:91], v[34:35] neg_lo:[1,0,0] neg_hi:[1,0,0]\n\t"
"v_pk_add_f32 v[132:133], v[36:37], v[132:133] neg_lo:[0,1] neg_hi:[0,1]\n\t"
"v_pk_add_f32 v[134:135], v[38:39], v[134:135] neg_lo:[0,1] neg_hi:[0,1]\n\t"
"v_pk_fma_f32 v[36:37], v[132:133], s[90:91], v[36:37] neg_lo:[1,0,0] neg_hi:[1,0,0]\n\t"
"v_pk_fma_f32 v[38:39], v[134:135], s[90:91], v[38:39] neg_lo:[1,0,0] neg_hi:[1,0,0]\n\t"
"v_pk_add_f32 v[116:117], v[20:21], v[116:117] neg_lo:[0,1] neg_hi:[0,1]\n\t"
"v_pk_add_f32 v[118:119], v[22:23], v[118:119] neg_lo:[0,1] neg_hi:[0,1]\n\t"
"v_pk_fma_f32 v[20:21], v[116:117], s[90:91], v[20:21] neg_lo:[1,0,0] neg_hi:[1,0,0]\n\t"
"v_pk_fma_f32 v[22:23], v[118:119], s[90:91], v[22:23] neg_lo:[1,0,0] neg_hi:[1,0,0]\n\t"
"v_pk_add_f32 v[100:101], v[4:5], v[100:101] neg_lo:[0,1] neg_hi:[0,1]\n\t"
"v_pk_add_f32 v[102:103], v[6:7], v[102:103] neg_lo:[0,1] neg_hi:[0,1]\n\t"
"v_pk_fma_f32 v[4:5], v[100:101], s[90:91], v[4:5] neg_lo:[1,0,0] neg_hi:[1,0,0]\n\t"
"v_pk_fma_f32 v[6:7], v[102:103], s[90:91], v[6:7] neg_lo:[1,0,0] neg_hi:[1,0,0]\n\t"
"v_pk_add_f32 v[104:105], v[8:9], v[104:105] neg_lo:[0,1] neg_hi:[0,1]\n\t"
"v_pk_add_f32 v[106:107], v[10:11], v[106:107] neg_lo:[0,1] neg_hi:[0,1]\n\t"
"v_pk_fma_f32 v[8:9], v[104:105], s[90:91], v[8:9] neg_lo:[1,0,0] neg_hi:[1,0,0]\n\t"
"v_pk_fma_f32 v[10:11], v[106:107], s[90:91], v[10:11] neg_lo:[1,0,0] neg_hi:[1,0,0]\n\t"
"v_pk_add_f32 v[120:121], v[24:25], v[120:121] neg_lo:[0,1] neg_hi:[0,1]\n\t"
"v_pk_add_f32 v[122:123], v[26:27], v[122:123] neg_lo:[0,1] neg_hi:[0,1]\n\t"
"v_pk_fma_f32 v[24:25], v[120:121], s[90:91], v[24:25] neg_lo:[1,0,0] neg_hi:[1,0,0]\n\t"
"v_pk_fma_f32 v[26:27], v[122:123], s[90:91], v[26:27] neg_lo:[1,0,0] neg_hi:[1,0,0]\n\t"
"v_pk_add_f32 v[136:137], v[40:41], v[136:137] neg_lo:[0,1] neg_hi:[0,1]\n\t"
"v_pk_add_f32 v[138:139], v[42:43], v[138:139] neg_lo:[0,1] neg_hi:[0,1]\n\t"
"v_pk_fma_f32 v[40:41], v[136:137], s[90:91], v[40:41] neg_lo:[1,0,0] neg_hi:[1,0,0]\n\t"
"v_pk_fma_f32 v[42:43], v[138:139], s[90:91], v[42:43] neg_lo:[1,0,0] neg_hi:[1,0,0]\n\t"
"v_pk_add_f32 v[140:141], v[44:45], v[140:141] neg_lo:[0,1] neg_hi:[0,1]\n\t"
"v_pk_add_f32 v[142:143], v[46:47], v[142:143] neg_lo:[0,1] neg_hi:[0,1]\n\t"
"v_pk_fma_f32 v[44:45], v[140:141], s[90:91], v[44:45] neg_lo:[1,0,0] neg_hi:[1,0,0]\n\t"
"v_pk_fma_f32 v[46:47], v[142:143], s[90:91], v[46:47] neg_lo:[1,0,0] neg_hi:[1,0,0]\n\t"
"v_pk_add_f32 v[124:125], v[28:29], v[124:125] neg_lo:[0,1] neg_hi:[0,1]\n\t"
"v_pk_add_f32 v[126:127], v[30:31], v[126:127] neg_lo:[0,1] neg_hi:[0,1]\n\t"
"v_pk_fma_f32 v[28:29], v[124:125], s[90:91], v[28:29] neg_lo:[1,0,0] neg_hi:[1,0,0]\n\t"
"v_pk_fma_f32 v[30:31], v[126:127], s[90:91], v[30:31] neg_lo:[1,0,0] neg_hi:[1,0,0]\n\t"
"v_pk_add_f32 v[108:109], v[12:13], v[108:109] neg_lo:[0,1] neg_hi:[0,1]\n\t"
"v_pk_add_f32 v[110:111], v[14:15], v[110:111] neg_lo:[0,1] neg_hi:[0,1]\n\t"
"v_pk_fma_f32 v[12:13], v[108:109], s[90:91], v[12:13] neg_lo:[1,0,0] neg_hi:[1,0,0]\n\t"
"v_pk_fma_f32 v[14:15], v[110:111], s[90:91], v[14:15] neg_lo:[1,0,0] neg_hi:[1,0,0]\n\t"
"v_mfma_f32_16x16x32_bf16 v[64:67], a[56:59], a[144:147], v[64:67]\n\t"
"v_mfma_f32_16x16x32_bf16 v[64:67], a[60:63], a[160:163], v[64:67]\n\t"
"ds_read_b128 a[156:159], v216 offset:19456\n\t"
"v_cvt_pk_bf16_f32 v192, v192, v193\n\t"
"v_mfma_f32_16x16x32_bf16 v[80:83], a[64:67], a[144:147], v[80:83]\n\t"
"ds_read_b128 a[172:175], v216 offset:27648\n\t"
"v_mfma_f32_16x16x32_bf16 v[80:83], a[68:71], a[160:163], v[80:83]\n\t"
"v_cvt_pk_bf16_f32 v193, v194, v195\n\t"
"v_mfma_f32_16x16x32_bf16 v[200:203], a[192:195], a[220:223], 0\n\t"
"ds_write_b128 v217, v[208:211] offset:0\n\t"
"v_mfma_f32_16x16x32_bf16 v[84:87], a[64:67], a[148:151], v[84:87]\n\t"
"v_cvt_pk_bf16_f32 v194, v196, v197\n\t"
"v_mfma_f32_16x16x32_bf16 v[84:87], a[68:71], a[164:167], v[84:87]\n\t"
"v_cvt_pk_bf16_f32 v195, v198, v199\n\t"
"v_mfma_f32_16x16x32_bf16 v[68:71], a[56:59], a[148:151], v[68:71]\n\t"
"ds_read_b128 a[144:147], v216 offset:20480\n\t"
"v_mfma_f32_16x16x32_bf16 v[68:71], a[60:63], a[164:167], v[68:71]\n\t"
"ds_read_b128 a[160:163], v216 offset:28672\n\t"
"v_cvt_pk_bf16_f32 v200, v200, v201\n\t"
"v_mfma_f32_16x16x32_bf16 v[204:207], a[196:199], a[220:223], 0\n\t"
"ds_write_b128 v219, v[192:195] offset:0\n\t"
"v_mfma_f32_16x16x32_bf16 v[52:55], a[48:51], a[148:151], v[52:55]\n\t"
"v_cvt_pk_bf16_f32 v201, v202, v203\n\t"
"v_mfma_f32_16x16x32_bf16 v[52:55], a[52:55], a[164:167], v[52:55]\n\t"
"ds_read_b128 a[148:151], v216 offset:21504\n\t"
"v_mfma_f32_16x16x32_bf16 v[208:211], a[200:203], a[220:223], 0\n\t"
"ds_read_b128 a[164:167], v216 offset:29696\n\t"
"s_waitcnt lgkmcnt(6)\n\t"
"v_mfma_f32_16x16x32_bf16 v[56:59], a[48:51], a[152:155], v[56:59]\n\t"
"v_cvt_pk_bf16_f32 v202, v204, v205\n\t"
"v_cvt_pk_bf16_f32 v203, v206, v207\n\t"
"v_mfma_f32_16x16x32_bf16 v[56:59], a[52:55], a[168:171], v[56:59]\n\t"
"ds_read_b64_tr_b16 a[178:179], v248 offset:41984\n\t"
"v_mfma_f32_16x16x32_bf16 v[72:75], a[56:59], a[152:155], v[72:75]\n\t"
"ds_read_b64_tr_b16 a[180:181], v249 offset:41472\n\t"
"v_mfma_f32_16x16x32_bf16 v[72:75], a[60:63], a[168:171], v[72:75]\n\t"
"v_cvt_pk_bf16_f32 v208, v208, v209\n\t"
"v_cvt_pk_bf16_f32 v209, v210, v211\n\t"
"v_mfma_f32_16x16x32_bf16 v[212:215], a[204:207], a[220:223], 0\n\t"
"ds_read_b64_tr_b16 a[182:183], v249 offset:41984\n\t"
"v_mfma_f32_16x16x32_bf16 v[88:91], a[64:67], a[152:155], v[88:91]\n\t"
"ds_read_b64_tr_b16 a[184:185], v250 offset:43776\n\t"
"v_mfma_f32_16x16x32_bf16 v[88:91], a[68:71], a[168:171], v[88:91]\n\t"
"ds_write_b128 v217, v[200:203] offset:8192\n\t"
"v_mfma_f32_16x16x32_bf16 v[92:95], a[64:67], a[156:159], v[92:95]\n\t"
"ds_read_b64_tr_b16 a[186:187], v250 offset:44288\n\t"
"v_mfma_f32_16x16x32_bf16 v[92:95], a[68:71], a[172:175], v[92:95]\n\t"
"ds_read_b64_tr_b16 a[188:189], v251 offset:43776\n\t"
"v_cvt_pk_bf16_f32 v210, v212, v213\n\t"
"v_mfma_f32_16x16x32_bf16 v[76:79], a[56:59], a[156:159], v[76:79]\n\t"
"ds_read_b64_tr_b16 a[190:191], v251 offset:44288\n\t"
"v_mfma_f32_16x16x32_bf16 v[76:79], a[60:63], a[172:175], v[76:79]\n\t"
"ds_read_b64_tr_b16 a[192:193], v250 offset:41472\n\t"
"v_cvt_pk_bf16_f32 v211, v214, v215\n\t"
"v_mfma_f32_16x16x32_bf16 v[60:63], a[48:51], a[156:159], v[60:63]\n\t"
"ds_read_b64_tr_b16 a[194:195], v250 offset:41984\n\t"
"v_mfma_f32_16x16x32_bf16 v[60:63], a[52:55], a[172:175], v[60:63]\n\t"
"ds_read_b64_tr_b16 a[196:197], v251 offset:41472\n\t"
"s_waitcnt vmcnt(17) lgkmcnt(9)\n\t"
"v_mfma_f32_16x16x32_bf16 v[144:147], a[72:75], a[144:147], v[144:147]\n\t"
"ds_read_b64_tr_b16 a[198:199], v251 offset:41984\n\t"
"v_mfma_f32_16x16x32_bf16 v[144:147], a[76:79], a[160:163], v[144:147]\n\t"
"ds_write_b128 v219, v[208:211] offset:8192\n\t"
"ds_read_b64_tr_b16 a[200:201], v248 offset:43776\n\t"
"ds_read_b64_tr_b16 a[202:203], v248 offset:44288\n\t"
"ds_read_b64_tr_b16 a[204:205], v249 offset:43776\n\t"
"ds_read_b64_tr_b16 a[206:207], v249 offset:44288\n\t"
"v_mfma_f32_16x16x32_bf16 v[160:163], a[80:83], a[144:147], v[160:163]\n\t"
"v_mfma_f32_16x16x32_bf16 v[160:163], a[84:87], a[160:163], v[160:163]\n\t"
"v_mfma_f32_16x16x32_bf16 v[176:179], a[88:91], a[144:147], v[176:179]\n\t"
"v_mfma_f32_16x16x32_bf16 v[176:179], a[92:95], a[160:163], v[176:179]\n\t"
"ds_read_b128 a[152:155], v216 offset:22528\n\t"
"v_mfma_f32_16x16x32_bf16 v[180:183], a[88:91], a[148:151], v[180:183]\n\t"
"ds_read_b128 a[168:171], v216 offset:30720\n\t"
"v_mfma_f32_16x16x32_bf16 v[180:183], a[92:95], a[164:167], v[180:183]\n\t"
"ds_read_b128 a[156:159], v216 offset:23552\n\t"
"ds_read_b128 a[172:175], v216 offset:31744\n\t"
"v_mfma_f32_16x16x32_bf16 v[164:167], a[80:83], a[148:151], v[164:167]\n\t"
"v_mfma_f32_16x16x32_bf16 v[164:167], a[84:87], a[164:167], v[164:167]\n\t"
"v_mfma_f32_16x16x32_bf16 v[148:151], a[72:75], a[148:151], v[148:151]\n\t"
"v_mfma_f32_16x16x32_bf16 v[148:151], a[76:79], a[164:167], v[148:151]\n\t"
"s_waitcnt lgkmcnt(0)\n\t"
"s_barrier\n\t"
"v_mfma_f32_16x16x32_bf16 v[152:155], a[72:75], a[152:155], v[152:155]\n\t"
"ds_read_b128 a[144:147], v216 offset:0\n\t"
"v_mfma_f32_16x16x32_bf16 v[152:155], a[76:79], a[168:171], v[152:155]\n\t"
"s_add_u32 m0, s26, 103936\n\t"
"ds_read_b128 a[160:163], v216 offset:8192\n\t"
"ds_read_b128 a[148:151], v216 offset:1024\n\t"
"buffer_load_dwordx4 v236, s[12:15], s22 offen offset:0 nt lds\n\t"
"v_mfma_f32_16x16x32_bf16 v[168:171], a[80:83], a[152:155], v[168:171]\n\t"
"ds_read_b128 a[164:167], v216 offset:9216\n\t"
"v_mfma_f32_16x16x32_bf16 v[168:171], a[84:87], a[168:171], v[168:171]\n\t"
"v_mfma_f32_16x16x32_bf16 v[184:187], a[88:91], a[152:155], v[184:187]\n\t"
"ds_read_b128 a[152:155], v216 offset:2048\n\t"
"v_mfma_f32_16x16x32_bf16 v[184:187], a[92:95], a[168:171], v[184:187]\n\t"
"ds_read_b128 a[168:171], v216 offset:10240\n\t"
"v_mfma_f32_16x16x32_bf16 v[192:195], a[176:179], a[252:255], 0\n\t"
"ds_read_b64_tr_b16 a[176:177], v248 offset:0\n\t"
"v_mfma_f32_16x16x32_bf16 v[188:191], a[88:91], a[156:159], v[188:191]\n\t"
"ds_read_b64_tr_b16 a[178:179], v248 offset:512\n\t"
"v_mfma_f32_16x16x32_bf16 v[188:191], a[92:95], a[172:175], v[188:191]\n\t"
"buffer_load_dwordx4 v237, s[12:15], s22 offen offset:1024 nt lds\n\t"
"v_mfma_f32_16x16x32_bf16 v[196:199], a[180:183], a[252:255], 0\n\t"
"ds_read_b64_tr_b16 a[180:181], v249 offset:0\n\t"
"v_mfma_f32_16x16x32_bf16 v[172:175], a[80:83], a[156:159], v[172:175]\n\t"
"v_cvt_pk_bf16_f32 v192, v192, v193\n\t"
"v_cvt_pk_bf16_f32 v193, v194, v195\n\t"
"v_mfma_f32_16x16x32_bf16 v[172:175], a[84:87], a[172:175], v[172:175]\n\t"
"ds_read_b64_tr_b16 a[182:183], v249 offset:512\n\t"
"v_mfma_f32_16x16x32_bf16 v[156:159], a[72:75], a[156:159], v[156:159]\n\t"
"ds_read_b128 a[156:159], v216 offset:3072\n\t"
"v_mfma_f32_16x16x32_bf16 v[156:159], a[76:79], a[172:175], v[156:159]\n\t"
"buffer_load_dwordx4 v238, s[12:15], s22 offen offset:2048 nt lds\n\t"
"v_mfma_f32_16x16x32_bf16 v[200:203], a[184:187], a[252:255], 0\n\t"
"v_cvt_pk_bf16_f32 v194, v196, v197\n\t"
"s_waitcnt vmcnt(9) lgkmcnt(6)\n\t"
"v_mfma_f32_16x16x32_bf16 v[48:51], a[96:99], a[144:147], v[48:51]\n\t"
"v_cvt_pk_bf16_f32 v195, v198, v199\n\t"
"v_mfma_f32_16x16x32_bf16 v[48:51], a[100:103], a[160:163], v[48:51]\n\t"
"ds_read_b128 a[172:175], v216 offset:11264\n\t"
"v_mfma_f32_16x16x32_bf16 v[204:207], a[188:191], a[252:255], 0\n\t"
"ds_write_b128 v217, v[192:195] offset:16384\n\t"
"v_mfma_f32_16x16x32_bf16 v[64:67], a[104:107], a[144:147], v[64:67]\n\t"
"v_cvt_pk_bf16_f32 v200, v200, v201\n\t"
"v_mfma_f32_16x16x32_bf16 v[64:67], a[108:111], a[160:163], v[64:67]\n\t"
"buffer_load_dwordx4 v239, s[12:15], s22 offen offset:3072 nt lds\n\t"
"v_mfma_f32_16x16x32_bf16 v[80:83], a[112:115], a[144:147], v[80:83]\n\t"
"v_cvt_pk_bf16_f32 v201, v202, v203\n\t"
"v_mfma_f32_16x16x32_bf16 v[80:83], a[116:119], a[160:163], v[80:83]\n\t"
"v_cvt_pk_bf16_f32 v202, v204, v205\n\t"
"v_mfma_f32_16x16x32_bf16 v[208:211], a[192:195], a[252:255], 0\n\t"
"v_cvt_pk_bf16_f32 v203, v206, v207\n\t"
"v_mfma_f32_16x16x32_bf16 v[84:87], a[112:115], a[148:151], v[84:87]\n\t"
"ds_read_b128 a[144:147], v216 offset:4096\n\t"
"v_mfma_f32_16x16x32_bf16 v[84:87], a[116:119], a[164:167], v[84:87]\n\t"
"ds_read_b128 a[160:163], v216 offset:12288\n\t"
"v_mfma_f32_16x16x32_bf16 v[68:71], a[104:107], a[148:151], v[68:71]\n\t"
"ds_read_b64_tr_b16 a[184:185], v250 offset:2304\n\t"
"v_mfma_f32_16x16x32_bf16 v[68:71], a[108:111], a[164:167], v[68:71]\n\t"
"s_mov_b64 exec, s[36:37]\n\t"
"buffer_load_dwordx4 v240, s[12:15], s22 offen offset:3584 nt lds\n\t"
"s_mov_b64 exec, -1\n\t"
"v_mfma_f32_16x16x32_bf16 v[212:215], a[196:199], a[252:255], 0\n\t"
"v_cvt_pk_bf16_f32 v208, v208, v209\n\t"
"v_mfma_f32_16x16x32_bf16 v[52:55], a[96:99], a[148:151], v[52:55]\n\t"
"v_cvt_pk_bf16_f32 v209, v210, v211\n\t"
"ds_read_b128 a[148:151], v216 offset:5120\n\t"
"v_mfma_f32_16x16x32_bf16 v[52:55], a[100:103], a[164:167], v[52:55]\n\t"
"ds_read_b128 a[164:167], v216 offset:13312\n\t"
"v_mfma_f32_16x16x32_bf16 v[192:195], a[200:203], a[252:255], 0\n\t"
"ds_read_b64_tr_b16 a[186:187], v250 offset:2816\n\t"
"s_waitcnt lgkmcnt(6)\n\t"
"v_mfma_f32_16x16x32_bf16 v[56:59], a[96:99], a[152:155], v[56:59]\n\t"
"v_cvt_pk_bf16_f32 v210, v212, v213\n\t"
"v_mfma_f32_16x16x32_bf16 v[56:59], a[100:103], a[168:171], v[56:59]\n\t"
"ds_write_b128 v219, v[200:203] offset:16384\n\t"
"v_mfma_f32_16x16x32_bf16 v[72:75], a[104:107], a[152:155], v[72:75]\n\t"
"v_cvt_pk_bf16_f32 v211, v214, v215\n\t"
"v_mfma_f32_16x16x32_bf16 v[72:75], a[108:111], a[168:171], v[72:75]\n\t"
"v_cvt_pk_bf16_f32 v192, v192, v193\n\t"
"v_mfma_f32_16x16x32_bf16 v[196:199], a[204:207], a[252:255], 0\n\t"
"v_cvt_pk_bf16_f32 v193, v194, v195\n\t"
"v_mfma_f32_16x16x32_bf16 v[88:91], a[112:115], a[152:155], v[88:91]\n\t"
"ds_read_b64_tr_b16 a[188:189], v251 offset:2304\n\t"
"v_mfma_f32_16x16x32_bf16 v[88:91], a[116:119], a[168:171], v[88:91]\n\t"
"ds_write_b128 v217, v[208:211] offset:24576\n\t"
"v_mfma_f32_16x16x32_bf16 v[92:95], a[112:115], a[156:159], v[92:95]\n\t"
"ds_read_b64_tr_b16 a[190:191], v251 offset:2816\n\t"
"v_mfma_f32_16x16x32_bf16 v[92:95], a[116:119], a[172:175], v[92:95]\n\t"
"ds_read_b64_tr_b16 a[192:193], v250 offset:0\n\t"
"v_mfma_f32_16x16x32_bf16 v[76:79], a[104:107], a[156:159], v[76:79]\n\t"
"ds_read_b64_tr_b16 a[194:195], v250 offset:512\n\t"
"v_cvt_pk_bf16_f32 v194, v196, v197\n\t"
"v_mfma_f32_16x16x32_bf16 v[76:79], a[108:111], a[172:175], v[76:79]\n\t"
"ds_read_b64_tr_b16 a[196:197], v251 offset:0\n\t"
"v_mfma_f32_16x16x32_bf16 v[60:63], a[96:99], a[156:159], v[60:63]\n\t"
"ds_read_b64_tr_b16 a[198:199], v251 offset:512\n\t"
"v_mfma_f32_16x16x32_bf16 v[60:63], a[100:103], a[172:175], v[60:63]\n\t"
"ds_read_b64_tr_b16 a[200:201], v248 offset:2304\n\t"
"s_waitcnt vmcnt(5) lgkmcnt(8)\n\t"
"v_mfma_f32_16x16x32_bf16 v[144:147], a[120:123], a[144:147], v[144:147]\n\t"
"ds_read_b64_tr_b16 a[202:203], v248 offset:2816\n\t"
"v_cvt_pk_bf16_f32 v195, v198, v199\n\t"
"v_mfma_f32_16x16x32_bf16 v[144:147], a[124:127], a[160:163], v[144:147]\n\t"
"ds_write_b128 v219, v[192:195] offset:24576\n\t"
"ds_read_b64_tr_b16 a[204:205], v249 offset:2304\n\t"
"ds_read_b64_tr_b16 a[206:207], v249 offset:2816\n\t"
"v_mfma_f32_16x16x32_bf16 v[160:163], a[128:131], a[144:147], v[160:163]\n\t"
"v_mfma_f32_16x16x32_bf16 v[160:163], a[132:135], a[160:163], v[160:163]\n\t"
"v_mfma_f32_16x16x32_bf16 v[176:179], a[136:139], a[144:147], v[176:179]\n\t"
"ds_read_b128 a[152:155], v216 offset:6144\n\t"
"v_mfma_f32_16x16x32_bf16 v[176:179], a[140:143], a[160:163], v[176:179]\n\t"
"ds_read_b128 a[168:171], v216 offset:14336\n\t"
"v_mfma_f32_16x16x32_bf16 v[180:183], a[136:139], a[148:151], v[180:183]\n\t"
"ds_read_b128 a[156:159], v216 offset:7168\n\t"
"v_mfma_f32_16x16x32_bf16 v[180:183], a[140:143], a[164:167], v[180:183]\n\t"
"ds_read_b128 a[172:175], v216 offset:15360\n\t"
"v_mfma_f32_16x16x32_bf16 v[164:167], a[128:131], a[148:151], v[164:167]\n\t"
"v_mfma_f32_16x16x32_bf16 v[164:167], a[132:135], a[164:167], v[164:167]\n\t"
"v_mfma_f32_16x16x32_bf16 v[148:151], a[120:123], a[148:151], v[148:151]\n\t"
"v_mfma_f32_16x16x32_bf16 v[148:151], a[124:127], a[164:167], v[148:151]\n\t"
"s_waitcnt lgkmcnt(0)\n\t"
"s_barrier\n\t"
"v_mfma_f32_16x16x32_bf16 v[152:155], a[120:123], a[152:155], v[152:155]\n\t"
"ds_read_b128 a[144:147], v216 offset:16384\n\t"
"v_add_u32 v248, 32768, v226\n\t"
"v_mfma_f32_16x16x32_bf16 v[152:155], a[124:127], a[168:171], v[152:155]\n\t"
"ds_read_b128 a[160:163], v216 offset:24576\n\t"
"ds_read_b128 a[148:151], v216 offset:17408\n\t"
"v_mfma_f32_16x16x32_bf16 v[168:171], a[128:131], a[152:155], v[168:171]\n\t"
"ds_read_b128 a[164:167], v216 offset:25600\n\t"
"v_xor_b32 v249, 32, v248\n\t"
"v_mfma_f32_16x16x32_bf16 v[168:171], a[132:135], a[168:171], v[168:171]\n\t"
"v_xor_b32 v250, 64, v248\n\t"
"v_xor_b32 v251, 96, v248\n\t"
"v_mfma_f32_16x16x32_bf16 v[184:187], a[136:139], a[152:155], v[184:187]\n\t"
"ds_read_b128 a[152:155], v216 offset:18432\n\t"
"v_mfma_f32_16x16x32_bf16 v[184:187], a[140:143], a[168:171], v[184:187]\n\t"
"ds_read_b128 a[168:171], v216 offset:26624\n\t"
"v_mfma_f32_16x16x32_bf16 v[200:203], a[176:179], a[252:255], 0\n\t"
"ds_read_b64_tr_b16 a[176:177], v248 offset:0\n\t"
"v_mfma_f32_16x16x32_bf16 v[188:191], a[136:139], a[156:159], v[188:191]\n\t"
"ds_read_b64_tr_b16 a[178:179], v248 offset:512\n\t"
"v_mfma_f32_16x16x32_bf16 v[188:191], a[140:143], a[172:175], v[188:191]\n\t"
"v_mfma_f32_16x16x32_bf16 v[204:207], a[180:183], a[252:255], 0\n\t"
"ds_read_b64_tr_b16 a[180:181], v249 offset:0\n\t"
"ds_read_b64_tr_b16 a[182:183], v249 offset:512\n\t"
"v_mfma_f32_16x16x32_bf16 v[172:175], a[128:131], a[156:159], v[172:175]\n\t"
"v_cvt_pk_bf16_f32 v200, v200, v201\n\t"
"v_cvt_pk_bf16_f32 v201, v202, v203\n\t"
"v_mfma_f32_16x16x32_bf16 v[172:175], a[132:135], a[172:175], v[172:175]\n\t"
"v_mfma_f32_16x16x32_bf16 v[156:159], a[120:123], a[156:159], v[156:159]\n\t"
"ds_read_b128 a[156:159], v216 offset:19456\n\t"
"v_mfma_f32_16x16x32_bf16 v[156:159], a[124:127], a[172:175], v[156:159]\n\t"
"v_cvt_pk_bf16_f32 v202, v204, v205\n\t"
"v_cvt_pk_bf16_f32 v203, v206, v207\n\t"
"ds_read_b128 a[172:175], v216 offset:27648\n\t"
"v_mfma_f32_16x16x32_bf16 v[208:211], a[184:187], a[252:255], 0\n\t"
"ds_write_b128 v217, v[200:203] offset:0\n\t"
"ds_read_b64_tr_b16 a[184:185], v250 offset:2304\n\t"
"s_waitcnt lgkmcnt(9)\n\t"
"v_mfma_f32_16x16x32_bf16 v[0:3], a[0:3], a[144:147], v[0:3]\n\t"
"ds_read_b64_tr_b16 a[186:187], v250 offset:2816\n\t"
"v_mfma_f32_16x16x32_bf16 v[0:3], a[4:7], a[160:163], v[0:3]\n\t"
"v_mfma_f32_16x16x32_bf16 v[212:215], a[188:191], a[252:255], 0\n\t"
"v_pk_fma_f32 v[48:49], v[144:145], s[90:91], v[48:49]\n\t"
"v_pk_fma_f32 v[50:51], v[146:147], s[90:91], v[50:51]\n\t"
"v_pk_add_f32 v[144:145], v[48:49], v[144:145] neg_lo:[0,1] neg_hi:[0,1]\n\t"
"v_pk_add_f32 v[146:147], v[50:51], v[146:147] neg_lo:[0,1] neg_hi:[0,1]\n\t"
"v_pk_fma_f32 v[64:65], v[160:161], s[90:91], v[64:65]\n\t"
"v_pk_fma_f32 v[66:67], v[162:163], s[90:91], v[66:67]\n\t"
"v_pk_add_f32 v[160:161], v[64:65], v[160:161] neg_lo:[0,1] neg_hi:[0,1]\n\t"
"v_pk_add_f32 v[162:163], v[66:67], v[162:163] neg_lo:[0,1] neg_hi:[0,1]\n\t"
"v_pk_fma_f32 v[80:81], v[176:177], s[90:91], v[80:81]\n\t"
"v_pk_fma_f32 v[82:83], v[178:179], s[90:91], v[82:83]\n\t"
"v_pk_add_f32 v[176:177], v[80:81], v[176:177] neg_lo:[0,1] neg_hi:[0,1]\n\t"
"v_pk_add_f32 v[178:179], v[82:83], v[178:179] neg_lo:[0,1] neg_hi:[0,1]\n\t"
"v_pk_fma_f32 v[84:85], v[180:181], s[90:91], v[84:85]\n\t"
"v_pk_fma_f32 v[86:87], v[182:183], s[90:91], v[86:87]\n\t"
"v_pk_add_f32 v[180:181], v[84:85], v[180:181] neg_lo:[0,1] neg_hi:[0,1]\n\t"
"v_pk_add_f32 v[182:183], v[86:87], v[182:183] neg_lo:[0,1] neg_hi:[0,1]\n\t"
"v_pk_fma_f32 v[68:69], v[164:165], s[90:91], v[68:69]\n\t"
"v_pk_fma_f32 v[70:71], v[166:167], s[90:91], v[70:71]\n\t"
"v_pk_add_f32 v[164:165], v[68:69], v[164:165] neg_lo:[0,1] neg_hi:[0,1]\n\t"
"v_pk_add_f32 v[166:167], v[70:71], v[166:167] neg_lo:[0,1] neg_hi:[0,1]\n\t"
"v_pk_fma_f32 v[52:53], v[148:149], s[90:91], v[52:53]\n\t"
"v_pk_fma_f32 v[54:55], v[150:151], s[90:91], v[54:55]\n\t"
"v_pk_add_f32 v[148:149], v[52:53], v[148:149] neg_lo:[0,1] neg_hi:[0,1]\n\t"
"v_pk_add_f32 v[150:151], v[54:55], v[150:151] neg_lo:[0,1] neg_hi:[0,1]\n\t"
"v_pk_fma_f32 v[56:57], v[152:153], s[90:91], v[56:57]\n\t"
"v_pk_fma_f32 v[58:59], v[154:155], s[90:91], v[58:59]\n\t"
"v_pk_add_f32 v[152:153], v[56:57], v[152:153] neg_lo:[0,1] neg_hi:[0,1]\n\t"
"v_pk_add_f32 v[154:155], v[58:59], v[154:155] neg_lo:[0,1] neg_hi:[0,1]\n\t"
"v_pk_fma_f32 v[72:73], v[168:169], s[90:91], v[72:73]\n\t"
"v_pk_fma_f32 v[74:75], v[170:171], s[90:91], v[74:75]\n\t"
"v_pk_add_f32 v[168:169], v[72:73], v[168:169] neg_lo:[0,1] neg_hi:[0,1]\n\t"
"v_pk_add_f32 v[170:171], v[74:75], v[170:171] neg_lo:[0,1] neg_hi:[0,1]\n\t"
"v_pk_fma_f32 v[88:89], v[184:185], s[90:91], v[88:89]\n\t"
"v_pk_fma_f32 v[90:91], v[186:187], s[90:91], v[90:91]\n\t"
"v_pk_add_f32 v[184:185], v[88:89], v[184:185] neg_lo:[0,1] neg_hi:[0,1]\n\t"
"v_pk_add_f32 v[186:187], v[90:91], v[186:187] neg_lo:[0,1] neg_hi:[0,1]\n\t"
"v_pk_fma_f32 v[92:93], v[188:189], s[90:91], v[92:93]\n\t"
"v_pk_fma_f32 v[94:95], v[190:191], s[90:91], v[94:95]\n\t"
"v_pk_add_f32 v[188:189], v[92:93], v[188:189] neg_lo:[0,1] neg_hi:[0,1]\n\t"
"v_pk_add_f32 v[190:191], v[94:95], v[190:191] neg_lo:[0,1] neg_hi:[0,1]\n\t"
"v_pk_fma_f32 v[76:77], v[172:173], s[90:91], v[76:77]\n\t"
"v_pk_fma_f32 v[78:79], v[174:175], s[90:91], v[78:79]\n\t"
"v_pk_add_f32 v[172:173], v[76:77], v[172:173] neg_lo:[0,1] neg_hi:[0,1]\n\t"
"v_pk_add_f32 v[174:175], v[78:79], v[174:175] neg_lo:[0,1] neg_hi:[0,1]\n\t"
"v_pk_fma_f32 v[60:61], v[156:157], s[90:91], v[60:61]\n\t"
"v_pk_fma_f32 v[62:63], v[158:159], s[90:91], v[62:63]\n\t"
"v_pk_add_f32 v[156:157], v[60:61], v[156:157] neg_lo:[0,1] neg_hi:[0,1]\n\t"
"v_pk_add_f32 v[158:159], v[62:63], v[158:159] neg_lo:[0,1] neg_hi:[0,1]\n\t"
"v_cvt_pk_bf16_f32 v48, v48, v49\n\t"
"v_cvt_pk_bf16_f32 v49, v50, v51\n\t"
"v_cvt_pk_bf16_f32 v52, v52, v53\n\t"
"v_cvt_pk_bf16_f32 v53, v54, v55\n\t"
"v_mfma_f32_16x16x32_bf16 v[16:19], a[8:11], a[144:147], v[16:19]\n\t"
"v_cvt_pk_bf16_f32 v208, v208, v209\n\t"
"v_cvt_pk_bf16_f32 v209, v210, v211\n\t"
"v_mfma_f32_16x16x32_bf16 v[16:19], a[12:15], a[160:163], v[16:19]\n\t"
"ds_write2st64_b64 v228, v[48:49], v[52:53] offset0:0 offset1:13\n\t"
"v_cvt_pk_bf16_f32 v64, v64, v65\n\t"
"v_cvt_pk_bf16_f32 v65, v66, v67\n\t"
"v_mfma_f32_16x16x32_bf16 v[32:35], a[16:19], a[144:147], v[32:35]\n\t"
"v_cvt_pk_bf16_f32 v68, v68, v69\n\t"
"v_cvt_pk_bf16_f32 v69, v70, v71\n\t"
"v_mfma_f32_16x16x32_bf16 v[32:35], a[20:23], a[160:163], v[32:35]\n\t"
"ds_write2st64_b64 v246, v[64:65], v[68:69] offset0:0 offset1:13\n\t"
"v_cvt_pk_bf16_f32 v210, v212, v213\n\t"
"v_cvt_pk_bf16_f32 v211, v214, v215\n\t"
"v_mfma_f32_16x16x32_bf16 v[192:195], a[192:195], a[252:255], 0\n\t"
"ds_read_b128 a[144:147], v216 offset:20480\n\t"
"ds_read_b128 a[160:163], v216 offset:28672\n\t"
"v_cvt_pk_bf16_f32 v80, v80, v81\n\t"
"v_cvt_pk_bf16_f32 v81, v82, v83\n\t"
"v_mfma_f32_16x16x32_bf16 v[36:39], a[16:19], a[148:151], v[36:39]\n\t"
"v_cvt_pk_bf16_f32 v84, v84, v85\n\t"
"v_cvt_pk_bf16_f32 v85, v86, v87\n\t"
"v_mfma_f32_16x16x32_bf16 v[36:39], a[20:23], a[164:167], v[36:39]\n\t"
"ds_write2st64_b64 v247, v[80:81], v[84:85] offset0:0 offset1:13\n\t"
"v_mfma_f32_16x16x32_bf16 v[20:23], a[8:11], a[148:151], v[20:23]\n\t"
"v_cvt_pk_bf16_f32 v56, v56, v57\n\t"
"v_cvt_pk_bf16_f32 v57, v58, v59\n\t"
"v_mfma_f32_16x16x32_bf16 v[20:23], a[12:15], a[164:167], v[20:23]\n\t"
"ds_write_b128 v219, v[208:211] offset:0\n\t"
"v_mfma_f32_16x16x32_bf16 v[196:199], a[196:199], a[252:255], 0\n\t"
"v_cvt_pk_bf16_f32 v192, v192, v193\n\t"
"v_cvt_pk_bf16_f32 v193, v194, v195\n\t"
"v_mfma_f32_16x16x32_bf16 v[4:7], a[0:3], a[148:151], v[4:7]\n\t"
"v_cvt_pk_bf16_f32 v60, v60, v61\n\t"
"v_cvt_pk_bf16_f32 v61, v62, v63\n\t"
"v_mfma_f32_16x16x32_bf16 v[4:7], a[4:7], a[164:167], v[4:7]\n\t"
"ds_read_b128 a[148:151], v216 offset:21504\n\t"
"v_cvt_pk_bf16_f32 v72, v72, v73\n\t"
"v_mfma_f32_16x16x32_bf16 v[200:203], a[200:203], a[252:255], 0\n\t"
"ds_read_b128 a[164:167], v216 offset:29696\n\t"
"v_cvt_pk_bf16_f32 v73, v74, v75\n\t"
"s_waitcnt lgkmcnt(7)\n\t"
"v_mfma_f32_16x16x32_bf16 v[8:11], a[0:3], a[152:155], v[8:11]\n\t"
"v_cvt_pk_bf16_f32 v194, v196, v197\n\t"
"v_cvt_pk_bf16_f32 v195, v198, v199\n\t"
"v_mfma_f32_16x16x32_bf16 v[8:11], a[4:7], a[168:171], v[8:11]\n\t"
"ds_write2st64_b64 v228, v[56:57], v[60:61] offset0:26 offset1:39\n\t"
"v_mfma_f32_16x16x32_bf16 v[24:27], a[8:11], a[152:155], v[24:27]\n\t"
"v_cvt_pk_bf16_f32 v76, v76, v77\n\t"
"v_cvt_pk_bf16_f32 v77, v78, v79\n\t"
"v_mfma_f32_16x16x32_bf16 v[24:27], a[12:15], a[168:171], v[24:27]\n\t"
"ds_write2st64_b64 v246, v[72:73], v[76:77] offset0:26 offset1:39\n\t"
"v_mfma_f32_16x16x32_bf16 v[204:207], a[204:207], a[252:255], 0\n\t"
"v_cvt_pk_bf16_f32 v200, v200, v201\n\t"
"v_cvt_pk_bf16_f32 v201, v202, v203\n\t"
"v_mfma_f32_16x16x32_bf16 v[40:43], a[16:19], a[152:155], v[40:43]\n\t"
"v_cvt_pk_bf16_f32 v88, v88, v89\n\t"
"v_cvt_pk_bf16_f32 v89, v90, v91\n\t"
"v_mfma_f32_16x16x32_bf16 v[40:43], a[20:23], a[168:171], v[40:43]\n\t"
"ds_write_b128 v217, v[192:195] offset:8192\n\t"
"v_mfma_f32_16x16x32_bf16 v[44:47], a[16:19], a[156:159], v[44:47]\n\t"
"v_cvt_pk_bf16_f32 v92, v92, v93\n\t"
"v_cvt_pk_bf16_f32 v93, v94, v95\n\t"
"v_mfma_f32_16x16x32_bf16 v[44:47], a[20:23], a[172:175], v[44:47]\n\t"
"buffer_load_dwordx4 a[16:19], v235, s[8:11], s59 offen offset:0 sc1\n\t"
"v_mfma_f32_16x16x32_bf16 v[28:31], a[8:11], a[156:159], v[28:31]\n\t"
"ds_read_b64_tr_b16 a[188:189], v251 offset:2304\n\t"
"v_cvt_pk_bf16_f32 v202, v204, v205\n\t"
"v_mfma_f32_16x16x32_bf16 v[28:31], a[12:15], a[172:175], v[28:31]\n\t"
"buffer_load_dwordx4 a[20:23], v235, s[8:11], s59 offen offset:1024 sc1\n\t"
"v_mfma_f32_16x16x32_bf16 v[12:15], a[0:3], a[156:159], v[12:15]\n\t"
"ds_read_b64_tr_b16 a[190:191], v251 offset:2816\n\t"
"v_cvt_pk_bf16_f32 v203, v206, v207\n\t"
"v_mfma_f32_16x16x32_bf16 v[12:15], a[4:7], a[172:175], v[12:15]\n\t"
"buffer_load_dwordx4 a[12:15], v218, s[8:11], s58 offen offset:3072 sc1\n\t"
"s_waitcnt lgkmcnt(4)\n\t"
"v_mfma_f32_16x16x32_bf16 v[96:99], a[24:27], a[144:147], v[96:99]\n\t"
"ds_read_b64_tr_b16 a[192:193], v250 offset:0\n\t"
"v_mfma_f32_16x16x32_bf16 v[96:99], a[28:31], a[160:163], v[96:99]\n\t"
"ds_write_b128 v219, v[200:203] offset:8192\n\t"
"ds_read_b64_tr_b16 a[194:195], v250 offset:512\n\t"
"ds_read_b64_tr_b16 a[196:197], v251 offset:0\n\t"
"ds_read_b64_tr_b16 a[198:199], v251 offset:512\n\t"
"ds_read_b64_tr_b16 a[200:201], v248 offset:2304\n\t"
"ds_read_b64_tr_b16 a[202:203], v248 offset:2816\n\t"
"ds_read_b64_tr_b16 a[204:205], v249 offset:2304\n\t"
"ds_read_b64_tr_b16 a[206:207], v249 offset:2816\n\t"
"ds_write2st64_b64 v247, v[88:89], v[92:93] offset0:26 offset1:39\n\t"
"buffer_load_dwordx4 a[8:11], v218, s[8:11], s58 offen offset:2048 sc1\n\t"
"v_mfma_f32_16x16x32_bf16 v[112:115], a[32:35], a[144:147], v[112:115]\n\t"
"v_mfma_f32_16x16x32_bf16 v[112:115], a[36:39], a[160:163], v[112:115]\n\t"
"buffer_load_dwordx4 a[0:3], v218, s[8:11], s58 offen offset:0 sc1\n\t"
"buffer_load_dwordx4 a[4:7], v218, s[8:11], s58 offen offset:1024 sc1\n\t"
"v_mfma_f32_16x16x32_bf16 v[128:131], a[40:43], a[144:147], v[128:131]\n\t"
"v_mfma_f32_16x16x32_bf16 v[128:131], a[44:47], a[160:163], v[128:131]\n\t"
"v_mfma_f32_16x16x32_bf16 v[132:135], a[40:43], a[148:151], v[132:135]\n\t"
"ds_read_b128 a[152:155], v216 offset:22528\n\t"
"v_mfma_f32_16x16x32_bf16 v[132:135], a[44:47], a[164:167], v[132:135]\n\t"
"ds_read_b128 a[168:171], v216 offset:30720\n\t"
"ds_read_b128 a[156:159], v216 offset:23552\n\t"
"ds_read_b128 a[172:175], v216 offset:31744\n\t"
"v_mfma_f32_16x16x32_bf16 v[116:119], a[32:35], a[148:151], v[116:119]\n\t"
"v_mfma_f32_16x16x32_bf16 v[116:119], a[36:39], a[164:167], v[116:119]\n\t"
"v_mfma_f32_16x16x32_bf16 v[100:103], a[24:27], a[148:151], v[100:103]\n\t"
"v_mfma_f32_16x16x32_bf16 v[100:103], a[28:31], a[164:167], v[100:103]\n\t"
"s_waitcnt lgkmcnt(0)\n\t"
"s_barrier\n\t"
"v_mfma_f32_16x16x32_bf16 v[104:107], a[24:27], a[152:155], v[104:107]\n\t"
"ds_read_b128 a[144:147], v216 offset:0\n\t"
"v_add_u32 v248, 80896, v224\n\t"
"v_mfma_f32_16x16x32_bf16 v[104:107], a[28:31], a[168:171], v[104:107]\n\t"
"ds_read_b128 a[160:163], v216 offset:8192\n\t"
"ds_read_b128 a[148:151], v216 offset:1024\n\t"
"ds_read_b128 v[56:59], v229 offset:0\n\t"
"ds_read_b128 v[72:75], v230 offset:0\n\t"
"v_mfma_f32_16x16x32_bf16 v[120:123], a[32:35], a[152:155], v[120:123]\n\t"
"ds_read_b128 a[164:167], v216 offset:9216\n\t"
"v_xor_b32 v249, 32, v248\n\t"
"v_mfma_f32_16x16x32_bf16 v[120:123], a[36:39], a[168:171], v[120:123]\n\t"
"ds_read_b128 v[88:91], v231 offset:0\n\t"
"ds_read_b128 v[60:63], v229 offset:13312\n\t"
"ds_read_b128 v[76:79], v230 offset:13312\n\t"
"ds_read_b128 v[92:95], v231 offset:13312\n\t"
"v_mfma_f32_16x16x32_bf16 v[136:139], a[40:43], a[152:155], v[136:139]\n\t"
"v_xor_b32 v250, 64, v248\n\t"
"v_xor_b32 v251, 96, v248\n\t"
"v_mfma_f32_16x16x32_bf16 v[136:139], a[44:47], a[168:171], v[136:139]\n\t"
"v_cvt_pk_bf16_f32 v144, v144, v145\n\t"
"v_cvt_pk_bf16_f32 v145, v146, v147\n\t"
"v_mfma_f32_16x16x32_bf16 v[208:211], a[176:179], a[252:255], 0\n\t"
"v_cvt_pk_bf16_f32 v148, v148, v149\n\t"
"v_cvt_pk_bf16_f32 v149, v150, v151\n\t"
"v_mfma_f32_16x16x32_bf16 v[140:143], a[40:43], a[156:159], v[140:143]\n\t"
"ds_read_b128 a[152:155], v216 offset:2048\n\t"
"v_cvt_pk_bf16_f32 v160, v160, v161\n\t"
"v_mfma_f32_16x16x32_bf16 v[140:143], a[44:47], a[172:175], v[140:143]\n\t"
"buffer_load_dwordx4 a[40:43], v235, s[8:11], s58 offen offset:2048 sc1\n\t"
"v_mfma_f32_16x16x32_bf16 v[212:215], a[180:183], a[252:255], 0\n\t"
"buffer_load_dwordx4 a[44:47], v235, s[8:11], s58 offen offset:3072 sc1\n\t"
"s_waitcnt lgkmcnt(1)\n\t"
"s_barrier\n\t"
"v_mfma_f32_16x16x32_bf16 v[124:127], a[32:35], a[156:159], v[124:127]\n\t"
"ds_read_b128 a[168:171], v216 offset:10240\n\t"
"v_cvt_pk_bf16_f32 v208, v208, v209\n\t"
"v_mfma_f32_16x16x32_bf16 v[124:127], a[36:39], a[172:175], v[124:127]\n\t"
"buffer_load_dwordx4 a[36:39], v235, s[8:11], s58 offen offset:1024 sc1\n\t"
"v_mfma_f32_16x16x32_bf16 v[108:111], a[24:27], a[156:159], v[108:111]\n\t"
"v_cvt_pk_bf16_f32 v209, v210, v211\n\t"
"v_cvt_pk_bf16_f32 v161, v162, v163\n\t"
"v_mfma_f32_16x16x32_bf16 v[108:111], a[28:31], a[172:175], v[108:111]\n\t"
"buffer_load_dwordx4 a[32:35], v235, s[8:11], s58 offen offset:0 sc1\n\t"
"ds_write2st64_b64 v228, v[144:145], v[148:149] offset0:0 offset1:13\n\t"
"v_mfma_f32_16x16x32_bf16 v[192:195], a[184:187], a[252:255], 0\n\t"
"buffer_load_dwordx4 a[24:27], v218, s[8:11], s57 offen offset:2048 sc1\n\t"
"v_mfma_f32_16x16x32_bf16 v[0:3], a[48:51], a[144:147], v[0:3]\n\t"
"v_cvt_pk_bf16_f32 v210, v212, v213\n\t"
"v_cvt_pk_bf16_f32 v211, v214, v215\n\t"
"v_mfma_f32_16x16x32_bf16 v[0:3], a[52:55], a[160:163], v[0:3]\n\t"
"buffer_load_dwordx4 a[28:31], v218, s[8:11], s57 offen offset:3072 sc1\n\t"
"buffer_store_dwordx4 v[56:59], v232, s[16:19], s24 offen nt\n\t"
"v_mfma_f32_16x16x32_bf16 v[196:199], a[188:191], a[252:255], 0\n\t"
"v_cvt_pk_bf16_f32 v164, v164, v165\n\t"
"v_cvt_pk_bf16_f32 v165, v166, v167\n\t"
"v_mfma_f32_16x16x32_bf16 v[16:19], a[56:59], a[144:147], v[16:19]\n\t"
"ds_read_b128 a[156:159], v216 offset:3072\n\t"
"v_cvt_pk_bf16_f32 v192, v192, v193\n\t"
"v_mfma_f32_16x16x32_bf16 v[16:19], a[60:63], a[160:163], v[16:19]\n\t"
"ds_write2st64_b64 v246, v[160:161], v[164:165] offset0:0 offset1:13\n\t"
"ds_read_b128 a[172:175], v216 offset:11264\n\t"
"v_cvt_pk_bf16_f32 v193, v194, v195\n\t"
"v_mfma_f32_16x16x32_bf16 v[32:35], a[64:67], a[144:147], v[32:35]\n\t"
"v_cvt_pk_bf16_f32 v176, v176, v177\n\t"
"v_cvt_pk_bf16_f32 v177, v178, v179\n\t"
"v_mfma_f32_16x16x32_bf16 v[32:35], a[68:71], a[160:163], v[32:35]\n\t"
"ds_write_b128 v217, v[208:211] offset:16384\n\t"
"v_mfma_f32_16x16x32_bf16 v[200:203], a[192:195], a[252:255], 0\n\t"
"buffer_store_dwordx4 v[72:75], v233, s[16:19], s24 offen nt\n\t"
"v_mfma_f32_16x16x32_bf16 v[36:39], a[64:67], a[148:151], v[36:39]\n\t"
"v_cvt_pk_bf16_f32 v180, v180, v181\n\t"
"v_cvt_pk_bf16_f32 v181, v182, v183\n\t"
"v_mfma_f32_16x16x32_bf16 v[36:39], a[68:71], a[164:167], v[36:39]\n\t"
"v_cvt_pk_bf16_f32 v194, v196, v197\n\t"
"v_cvt_pk_bf16_f32 v195, v198, v199\n\t"
"ds_write2st64_b64 v247, v[176:177], v[180:181] offset0:0 offset1:13\n\t"
"v_mfma_f32_16x16x32_bf16 v[20:23], a[56:59], a[148:151], v[20:23]\n\t"
"ds_read_b128 a[144:147], v216 offset:4096\n\t"
"v_cvt_pk_bf16_f32 v152, v152, v153\n\t"
"v_mfma_f32_16x16x32_bf16 v[20:23], a[60:63], a[164:167], v[20:23]\n\t"
"ds_read_b128 a[160:163], v216 offset:12288\n\t"
"v_cvt_pk_bf16_f32 v153, v154, v155\n\t"
"v_cvt_pk_bf16_f32 v156, v156, v157\n\t"
"v_cvt_pk_bf16_f32 v157, v158, v159\n\t"
"v_mfma_f32_16x16x32_bf16 v[204:207], a[196:199], a[252:255], 0\n\t"
"ds_write_b128 v219, v[192:195] offset:16384\n\t"
"v_mfma_f32_16x16x32_bf16 v[4:7], a[48:51], a[148:151], v[4:7]\n\t"
"v_cvt_pk_bf16_f32 v200, v200, v201\n\t"
"v_cvt_pk_bf16_f32 v201, v202, v203\n\t"
"v_mfma_f32_16x16x32_bf16 v[4:7], a[52:55], a[164:167], v[4:7]\n\t"
"buffer_store_dwordx4 v[88:91], v234, s[16:19], s24 offen nt\n\t"
"ds_write2st64_b64 v228, v[152:153], v[156:157] offset0:26 offset1:39\n\t"
"v_mfma_f32_16x16x32_bf16 v[208:211], a[200:203], a[252:255], 0\n\t"
"ds_read_b128 a[148:151], v216 offset:5120\n\t"
"v_cvt_pk_bf16_f32 v168, v168, v169\n\t"
"s_waitcnt lgkmcnt(7)\n\t"
"v_mfma_f32_16x16x32_bf16 v[8:11], a[48:51], a[152:155], v[8:11]\n\t"
"ds_read_b128 a[164:167], v216 offset:13312\n\t"
"v_cvt_pk_bf16_f32 v169, v170, v171\n\t"
"v_mfma_f32_16x16x32_bf16 v[8:11], a[52:55], a[168:171], v[8:11]\n\t"
"v_cvt_pk_bf16_f32 v172, v172, v173\n\t"
"v_cvt_pk_bf16_f32 v173, v174, v175\n\t"
"v_mfma_f32_16x16x32_bf16 v[24:27], a[56:59], a[152:155], v[24:27]\n\t"
"v_cvt_pk_bf16_f32 v202, v204, v205\n\t"
"v_cvt_pk_bf16_f32 v203, v206, v207\n\t"
"v_mfma_f32_16x16x32_bf16 v[24:27], a[60:63], a[168:171], v[24:27]\n\t"
"buffer_store_dwordx4 v[60:63], v232, s[16:19], s27 offen nt\n\t"
"ds_write2st64_b64 v246, v[168:169], v[172:173] offset0:26 offset1:39\n\t"
"v_mfma_f32_16x16x32_bf16 v[212:215], a[204:207], a[252:255], 0\n\t"
"v_cvt_pk_bf16_f32 v208, v208, v209\n\t"
"v_cvt_pk_bf16_f32 v209, v210, v211\n\t"
"v_mfma_f32_16x16x32_bf16 v[40:43], a[64:67], a[152:155], v[40:43]\n\t"
"v_cvt_pk_bf16_f32 v184, v184, v185\n\t"
"v_cvt_pk_bf16_f32 v185, v186, v187\n\t"
"v_mfma_f32_16x16x32_bf16 v[40:43], a[68:71], a[168:171], v[40:43]\n\t"
"ds_write_b128 v217, v[200:203] offset:24576\n\t"
"v_cvt_pk_bf16_f32 v188, v188, v189\n\t"
"v_cvt_pk_bf16_f32 v189, v190, v191\n\t"
"v_mfma_f32_16x16x32_bf16 v[44:47], a[64:67], a[156:159], v[44:47]\n\t"
"ds_read_b64_tr_b16 a[176:177], v248 offset:41472\n\t"
"v_mfma_f32_16x16x32_bf16 v[44:47], a[68:71], a[172:175], v[44:47]\n\t"
"buffer_load_dwordx4 a[64:67], v235, s[8:11], s65 offen offset:0 sc1\n\t"
"buffer_load_dwordx4 a[68:71], v235, s[8:11], s65 offen offset:1024 sc1\n\t"
"v_mfma_f32_16x16x32_bf16 v[28:31], a[56:59], a[156:159], v[28:31]\n\t"
"ds_read_b64_tr_b16 a[178:179], v248 offset:41984\n\t"
"v_cvt_pk_bf16_f32 v210, v212, v213\n\t"
"v_mfma_f32_16x16x32_bf16 v[28:31], a[60:63], a[172:175], v[28:31]\n\t"
"buffer_load_dwordx4 a[60:63], v218, s[8:11], s64 offen offset:3072 sc1\n\t"
"ds_read_b64_tr_b16 a[180:181], v249 offset:41472\n\t"
"ds_read_b64_tr_b16 a[182:183], v249 offset:41984\n\t"
"v_mfma_f32_16x16x32_bf16 v[12:15], a[48:51], a[156:159], v[12:15]\n\t"
"ds_read_b64_tr_b16 a[184:185], v250 offset:43776\n\t"
"v_cvt_pk_bf16_f32 v211, v214, v215\n\t"
"v_mfma_f32_16x16x32_bf16 v[12:15], a[52:55], a[172:175], v[12:15]\n\t"
"ds_write_b128 v219, v[208:211] offset:24576\n\t"
"s_waitcnt lgkmcnt(8)\n\t"
"v_mfma_f32_16x16x32_bf16 v[96:99], a[72:75], a[144:147], v[96:99]\n\t"
"ds_read_b64_tr_b16 a[186:187], v250 offset:44288\n\t"
"v_mfma_f32_16x16x32_bf16 v[96:99], a[76:79], a[160:163], v[96:99]\n\t"
"ds_read_b64_tr_b16 a[188:189], v251 offset:43776\n\t"
"ds_read_b64_tr_b16 a[190:191], v251 offset:44288\n\t"
"ds_read_b64_tr_b16 a[192:193], v250 offset:41472\n\t"
"ds_read_b64_tr_b16 a[194:195], v250 offset:41984\n\t"
"ds_read_b64_tr_b16 a[196:197], v251 offset:41472\n\t"
"ds_read_b64_tr_b16 a[198:199], v251 offset:41984\n\t"
"ds_read_b64_tr_b16 a[200:201], v248 offset:43776\n\t"
"ds_read_b64_tr_b16 a[202:203], v248 offset:44288\n\t"
"ds_read_b64_tr_b16 a[204:205], v249 offset:43776\n\t"
"ds_read_b64_tr_b16 a[206:207], v249 offset:44288\n\t"
"ds_write2st64_b64 v247, v[184:185], v[188:189] offset0:26 offset1:39\n\t"
"buffer_load_dwordx4 a[56:59], v218, s[8:11], s64 offen offset:2048 sc1\n\t"
"buffer_store_dwordx4 v[76:79], v233, s[16:19], s27 offen nt\n\t"
"v_mfma_f32_16x16x32_bf16 v[112:115], a[80:83], a[144:147], v[112:115]\n\t"
"v_mfma_f32_16x16x32_bf16 v[112:115], a[84:87], a[160:163], v[112:115]\n\t"
"buffer_load_dwordx4 a[48:51], v218, s[8:11], s64 offen offset:0 sc1\n\t"
"buffer_load_dwordx4 a[52:55], v218, s[8:11], s64 offen offset:1024 sc1\n\t"
"v_mfma_f32_16x16x32_bf16 v[128:131], a[88:91], a[144:147], v[128:131]\n\t"
"v_mfma_f32_16x16x32_bf16 v[128:131], a[92:95], a[160:163], v[128:131]\n\t"
"v_mfma_f32_16x16x32_bf16 v[132:135], a[88:91], a[148:151], v[132:135]\n\t"
"ds_read_b128 a[152:155], v216 offset:6144\n\t"
"v_mfma_f32_16x16x32_bf16 v[132:135], a[92:95], a[164:167], v[132:135]\n\t"
"ds_read_b128 a[168:171], v216 offset:14336\n\t"
"ds_read_b128 a[156:159], v216 offset:7168\n\t"
"ds_read_b128 a[172:175], v216 offset:15360\n\t"
"v_mfma_f32_16x16x32_bf16 v[116:119], a[80:83], a[148:151], v[116:119]\n\t"
"v_mfma_f32_16x16x32_bf16 v[116:119], a[84:87], a[164:167], v[116:119]\n\t"
"buffer_store_dwordx4 v[92:95], v234, s[16:19], s27 offen nt\n\t"
"v_mfma_f32_16x16x32_bf16 v[100:103], a[72:75], a[148:151], v[100:103]\n\t"
"v_mfma_f32_16x16x32_bf16 v[100:103], a[76:79], a[164:167], v[100:103]\n\t"
"s_waitcnt lgkmcnt(0)\n\t"
"s_barrier\n\t"
"v_mfma_f32_16x16x32_bf16 v[104:107], a[72:75], a[152:155], v[104:107]\n\t"
"ds_read_b128 a[144:147], v216 offset:16384\n\t"
"v_mfma_f32_16x16x32_bf16 v[104:107], a[76:79], a[168:171], v[104:107]\n\t"
"ds_read_b128 a[160:163], v216 offset:24576\n\t"
"ds_read_b128 a[148:151], v216 offset:17408\n\t"
"v_mfma_f32_16x16x32_bf16 v[120:123], a[80:83], a[152:155], v[120:123]\n\t"
"ds_read_b128 a[164:167], v216 offset:25600\n\t"
"v_mfma_f32_16x16x32_bf16 v[120:123], a[84:87], a[168:171], v[120:123]\n\t"
"ds_read_b128 v[152:155], v229 offset:0\n\t"
"v_mfma_f32_16x16x32_bf16 v[136:139], a[88:91], a[152:155], v[136:139]\n\t"
"ds_read_b128 a[152:155], v216 offset:18432\n\t"
"v_mfma_f32_16x16x32_bf16 v[136:139], a[92:95], a[168:171], v[136:139]\n\t"
"ds_read_b128 v[168:171], v230 offset:0\n\t"
"v_mfma_f32_16x16x32_bf16 v[192:195], a[176:179], a[212:215], 0\n\t"
"ds_read_b128 a[168:171], v216 offset:26624\n\t"
"v_mfma_f32_16x16x32_bf16 v[140:143], a[88:91], a[156:159], v[140:143]\n\t"
"v_mfma_f32_16x16x32_bf16 v[140:143], a[92:95], a[172:175], v[140:143]\n\t"
"buffer_load_dwordx4 a[88:91], v235, s[8:11], s64 offen offset:2048 sc1\n\t"
"v_mfma_f32_16x16x32_bf16 v[196:199], a[180:183], a[212:215], 0\n\t"
"buffer_load_dwordx4 a[92:95], v235, s[8:11], s64 offen offset:3072 sc1\n\t"
"v_mfma_f32_16x16x32_bf16 v[124:127], a[80:83], a[156:159], v[124:127]\n\t"
"s_nop 0\n\t"
"v_cvt_pk_bf16_f32 v192, v192, v193\n\t"
"v_cvt_pk_bf16_f32 v193, v194, v195\n\t"
"v_mfma_f32_16x16x32_bf16 v[124:127], a[84:87], a[172:175], v[124:127]\n\t"
"buffer_load_dwordx4 a[84:87], v235, s[8:11], s64 offen offset:1024 sc1\n\t"
"v_mfma_f32_16x16x32_bf16 v[108:111], a[72:75], a[156:159], v[108:111]\n\t"
"ds_read_b128 v[184:187], v231 offset:0\n\t"
"v_mfma_f32_16x16x32_bf16 v[108:111], a[76:79], a[172:175], v[108:111]\n\t"
"buffer_load_dwordx4 a[80:83], v235, s[8:11], s64 offen offset:0 sc1\n\t"
"v_mfma_f32_16x16x32_bf16 v[200:203], a[184:187], a[212:215], 0\n\t"
"buffer_load_dwordx4 a[72:75], v218, s[8:11], s63 offen offset:2048 sc1\n\t"
"s_waitcnt lgkmcnt(3)\n\t"
"v_mfma_f32_16x16x32_bf16 v[0:3], a[96:99], a[144:147], v[0:3]\n\t"
"v_cvt_pk_bf16_f32 v194, v196, v197\n\t"
"v_cvt_pk_bf16_f32 v195, v198, v199\n\t"
"v_mfma_f32_16x16x32_bf16 v[0:3], a[100:103], a[160:163], v[0:3]\n\t"
"buffer_load_dwordx4 a[76:79], v218, s[8:11], s63 offen offset:3072 sc1\n\t"
"v_mfma_f32_16x16x32_bf16 v[204:207], a[188:191], a[212:215], 0\n\t"
"buffer_store_dwordx4 v[152:155], v232, s[16:19], s20 offen nt\n\t"
"v_mfma_f32_16x16x32_bf16 v[16:19], a[104:107], a[144:147], v[16:19]\n\t"
"ds_read_b128 a[156:159], v216 offset:19456\n\t"
"v_cvt_pk_bf16_f32 v200, v200, v201\n\t"
"v_mfma_f32_16x16x32_bf16 v[16:19], a[108:111], a[160:163], v[16:19]\n\t"
"ds_read_b128 a[172:175], v216 offset:27648\n\t"
"v_cvt_pk_bf16_f32 v201, v202, v203\n\t"
"v_mfma_f32_16x16x32_bf16 v[32:35], a[112:115], a[144:147], v[32:35]\n\t"
"ds_read_b128 v[156:159], v229 offset:13312\n\t"
"v_mfma_f32_16x16x32_bf16 v[32:35], a[116:119], a[160:163], v[32:35]\n\t"
"ds_write_b128 v217, v[192:195] offset:0\n\t"
"v_mfma_f32_16x16x32_bf16 v[208:211], a[192:195], a[212:215], 0\n\t"
"s_waitcnt lgkmcnt(4)\n\t"
"buffer_store_dwordx4 v[168:171], v233, s[16:19], s20 offen nt\n\t"
"v_mfma_f32_16x16x32_bf16 v[36:39], a[112:115], a[148:151], v[36:39]\n\t"
"v_cvt_pk_bf16_f32 v202, v204, v205\n\t"
"v_cvt_pk_bf16_f32 v203, v206, v207\n\t"
"v_mfma_f32_16x16x32_bf16 v[36:39], a[116:119], a[164:167], v[36:39]\n\t"
"ds_read_b128 a[144:147], v216 offset:20480\n\t"
"v_mfma_f32_16x16x32_bf16 v[20:23], a[104:107], a[148:151], v[20:23]\n\t"
"ds_read_b128 a[160:163], v216 offset:28672\n\t"
"v_mfma_f32_16x16x32_bf16 v[20:23], a[108:111], a[164:167], v[20:23]\n\t"
"ds_write_b128 v219, v[200:203] offset:0\n\t"
"v_mfma_f32_16x16x32_bf16 v[212:215], a[196:199], a[212:215], 0\n\t"
"buffer_store_dwordx4 v[184:187], v234, s[16:19], s20 offen nt\n\t"
"v_mfma_f32_16x16x32_bf16 v[4:7], a[96:99], a[148:151], v[4:7]\n\t"
"v_cvt_pk_bf16_f32 v208, v208, v209\n\t"
"v_cvt_pk_bf16_f32 v209, v210, v211\n\t"
"v_mfma_f32_16x16x32_bf16 v[4:7], a[100:103], a[164:167], v[4:7]\n\t"
"ds_read_b128 v[172:175], v230 offset:13312\n\t"
"v_mfma_f32_16x16x32_bf16 v[192:195], a[200:203], a[212:215], 0\n\t"
"ds_read_b128 a[148:151], v216 offset:21504\n\t"
"v_mfma_f32_16x16x32_bf16 v[8:11], a[96:99], a[152:155], v[8:11]\n\t"
"ds_read_b128 a[164:167], v216 offset:29696\n\t"
"v_cvt_pk_bf16_f32 v210, v212, v213\n\t"
"v_mfma_f32_16x16x32_bf16 v[8:11], a[100:103], a[168:171], v[8:11]\n\t"
"v_cvt_pk_bf16_f32 v211, v214, v215\n\t"
"v_mfma_f32_16x16x32_bf16 v[24:27], a[104:107], a[152:155], v[24:27]\n\t"
"ds_read_b128 v[188:191], v231 offset:13312\n\t"
"v_mfma_f32_16x16x32_bf16 v[24:27], a[108:111], a[168:171], v[24:27]\n\t"
"s_waitcnt lgkmcnt(6)\n\t"
"buffer_store_dwordx4 v[156:159], v232, s[16:19], s25 offen nt\n\t"
"v_mfma_f32_16x16x32_bf16 v[196:199], a[204:207], a[212:215], 0\n\t"
"v_cvt_pk_bf16_f32 v192, v192, v193\n\t"
"v_cvt_pk_bf16_f32 v193, v194, v195\n\t"
"v_mfma_f32_16x16x32_bf16 v[40:43], a[112:115], a[152:155], v[40:43]\n\t"
"ds_read_b128 a[152:155], v216 offset:22528\n\t"
"v_mfma_f32_16x16x32_bf16 v[40:43], a[116:119], a[168:171], v[40:43]\n\t"
"ds_write_b128 v217, v[208:211] offset:8192\n\t"
"v_mfma_f32_16x16x32_bf16 v[44:47], a[112:115], a[156:159], v[44:47]\n\t"
"ds_read_b128 a[168:171], v216 offset:30720\n\t"
"v_mfma_f32_16x16x32_bf16 v[44:47], a[116:119], a[172:175], v[44:47]\n\t"
"buffer_load_dwordx4 a[112:115], v235, s[8:11], s47 offen offset:0 sc1\n\t"
"v_mfma_f32_16x16x32_bf16 v[28:31], a[104:107], a[156:159], v[28:31]\n\t"
"v_cvt_pk_bf16_f32 v194, v196, v197\n\t"
"v_cvt_pk_bf16_f32 v195, v198, v199\n\t"
"v_mfma_f32_16x16x32_bf16 v[28:31], a[108:111], a[172:175], v[28:31]\n\t"
"buffer_load_dwordx4 a[116:119], v235, s[8:11], s47 offen offset:1024 sc1\n\t"
"v_mfma_f32_16x16x32_bf16 v[12:15], a[96:99], a[156:159], v[12:15]\n\t"
"ds_read_b128 a[156:159], v216 offset:23552\n\t"
"v_mfma_f32_16x16x32_bf16 v[12:15], a[100:103], a[172:175], v[12:15]\n\t"
"buffer_load_dwordx4 a[108:111], v218, s[8:11], s46 offen offset:3072 sc1\n\t"
"s_waitcnt lgkmcnt(4)\n\t"
"v_mfma_f32_16x16x32_bf16 v[96:99], a[120:123], a[144:147], v[96:99]\n\t"
"ds_read_b128 a[172:175], v216 offset:31744\n\t"
"v_mfma_f32_16x16x32_bf16 v[96:99], a[124:127], a[160:163], v[96:99]\n\t"
"ds_write_b128 v219, v[192:195] offset:8192\n\t"
"buffer_load_dwordx4 a[104:107], v218, s[8:11], s46 offen offset:2048 sc1\n\t"
"buffer_store_dwordx4 v[172:175], v233, s[16:19], s25 offen nt\n\t"
"v_mfma_f32_16x16x32_bf16 v[112:115], a[128:131], a[144:147], v[112:115]\n\t"
"v_mfma_f32_16x16x32_bf16 v[112:115], a[132:135], a[160:163], v[112:115]\n\t"
"buffer_load_dwordx4 a[96:99], v218, s[8:11], s46 offen offset:0 sc1\n\t"
"buffer_load_dwordx4 a[100:103], v218, s[8:11], s46 offen offset:1024 sc1\n\t"
"v_mfma_f32_16x16x32_bf16 v[128:131], a[136:139], a[144:147], v[128:131]\n\t"
"v_mfma_f32_16x16x32_bf16 v[128:131], a[140:143], a[160:163], v[128:131]\n\t"
"v_mfma_f32_16x16x32_bf16 v[132:135], a[136:139], a[148:151], v[132:135]\n\t"
"v_mfma_f32_16x16x32_bf16 v[132:135], a[140:143], a[164:167], v[132:135]\n\t"
"buffer_store_dwordx4 v[188:191], v234, s[16:19], s25 offen nt\n\t"
"v_mfma_f32_16x16x32_bf16 v[116:119], a[128:131], a[148:151], v[116:119]\n\t"
"v_mfma_f32_16x16x32_bf16 v[116:119], a[132:135], a[164:167], v[116:119]\n\t"
"v_mfma_f32_16x16x32_bf16 v[100:103], a[120:123], a[148:151], v[100:103]\n\t"
"v_mfma_f32_16x16x32_bf16 v[100:103], a[124:127], a[164:167], v[100:103]\n\t"
"s_waitcnt vmcnt(42) lgkmcnt(0)\n\t"
"s_barrier\n\t"
"s_add_u32 s71, s71, 1\n\t"
"v_mov_b32 v215, s71\n\t"
"s_mov_b64 exec, 1\n\t"
"buffer_store_dword v215, off, s[8:11], s70 sc1\n\t"
"s_mov_b64 exec, -1\n\t"
"s_sub_u32 s21, s21, 1\n\t"
"s_xor_b64 s[84:85], s[84:85], s[88:89]\n\t"
"s_branch Lunit_%=\n\t"
"Lfb_%=:\n\t"
"v_mbcnt_lo_u32_b32 v162, -1, 0\n\t"
"v_mbcnt_hi_u32_b32 v162, -1, v162\n\t"
"v_and_b32 v161, 3, v162\n\t"
"v_lshlrev_b32 v163, 2, v162\n\t"
"v_and_or_b32 v161, v163, 16, v161\n\t"
"v_mul_u32_u24 v161, 0xc0, v161\n\t"
"v_lshrrev_b32 v163, 5, v162\n\t"
"v_mul_u32_u24 v163, 12, v163\n\t"
"v_bfe_u32 v164, v162, 3, 2\n\t"
"v_add3_u32 v161, v161, v163, v164\n\t"
"v_lshlrev_b32 v161, 1, v161\n\t"
"v_lshlrev_b32 v162, 1, v162\n\t"
"v_mov_b32 v152, 0\n\t"
"v_mov_b32 v153, 0\n\t"
"v_mov_b32 v154, 0\n\t"
"v_mov_b32 v155, 0\n\t"
"v_mov_b32 v156, 0\n\t"
"v_mov_b32 v157, 0\n\t"
"v_mov_b32 v158, 0\n\t"
"v_mov_b32 v159, 0\n\t"
"v_mov_b32 v160, 0\n\t"
"s_mov_b32 s67, 0\n\t"
"Lfb0_%=:\n\t"
"s_mov_b32 s69, 0\n\t"
"Lfb1_%=:\n\t"
"s_lshr_b32 s60, s69, 5\n\t"
"s_mul_i32 s56, s60, 12288\n\t"
"s_bfe_u32 s61, s69, 0x20001\n\t"
"s_mul_i32 s61, s61, 1536\n\t"
"s_add_u32 s56, s56, s61\n\t"
"s_bitcmp1_b32 s69, 0\n\t"
"s_cselect_b32 s61, 48, 0\n\t"
"s_add_u32 s56, s56, s61\n\t"
"s_bfe_u32 s62, s69, 0x20003\n\t"
"s_mul_i32 s61, s62, 96\n\t"
"s_add_u32 s56, s56, s61\n\t"
"s_lshl3_add_u32 s56, s67, s56\n\t"
"s_lshr_b32 s60, s69, 5\n\t"
"s_bfe_u32 s62, s69, 0x20003\n\t"
"s_and_b32 s61, s60, 1\n\t"
"s_lshl1_add_u32 s61, s67, s61\n\t"
"s_mul_i32 s62, s62, 288\n\t"
"s_add_u32 s61, s61, s62\n\t"
"s_lshr_b32 s60, s60, 1\n\t"
"s_mul_i32 s62, s60, 96\n\t"
"s_add_u32 s61, s61, s62\n\t"
"s_lshl_b32 s57, s61, 10\n\t"
"s_and_b32 s61, s69, 7\n\t"
"s_lshl_b32 s61, s61, 7\n\t"
"s_add_u32 s57, s57, s61\n\t"
"s_add_u32 s60, s56, 0\n\t"
"buffer_load_short_d16_hi v152, v161, s[52:55], s60 offen\n\t"
"s_add_u32 s60, s56, 73728\n\t"
"buffer_load_short_d16_hi v153, v161, s[52:55], s60 offen\n\t"
"s_add_u32 s60, s56, 147456\n\t"
"buffer_load_short_d16_hi v154, v161, s[52:55], s60 offen\n\t"
"s_add_u32 s60, s56, 221184\n\t"
"buffer_load_short_d16_hi v155, v161, s[52:55], s60 offen\n\t"
"s_add_u32 s60, s56, 294912\n\t"
"buffer_load_short_d16_hi v156, v161, s[52:55], s60 offen\n\t"
"s_add_u32 s60, s56, 368640\n\t"
"buffer_load_short_d16_hi v157, v161, s[52:55], s60 offen\n\t"
"s_add_u32 s60, s56, 442368\n\t"
"buffer_load_short_d16_hi v158, v161, s[52:55], s60 offen\n\t"
"s_add_u32 s60, s56, 516096\n\t"
"buffer_load_short_d16_hi v159, v161, s[52:55], s60 offen\n\t"
"s_add_u32 s60, s56, 589824\n\t"
"buffer_load_short_d16_hi v160, v161, s[52:55], s60 offen\n\t"
"s_waitcnt vmcnt(2)\n\t"
"v_add_f32 v168, v152, v158\n\t"
"s_waitcnt vmcnt(1)\n\t"
"v_add_f32 v169, v153, v159\n\t"
"s_waitcnt vmcnt(0)\n\t"
"v_add_f32 v170, v154, v160\n\t"
"v_add_f32 v171, v168, v155\n\t"
"v_sub_f32 v174, v168, v155\n\t"
"v_add_f32 v172, v169, v156\n\t"
"v_sub_f32 v175, v169, v156\n\t"
"v_add_f32 v173, v170, v157\n\t"
"v_sub_f32 v176, v170, v157\n\t"
"v_add_f32 v177, v152, v154\n\t"
"v_add_f32 v178, v171, v173\n\t"
"v_add_f32 v179, v174, v176\n\t"
"v_add_f32 v180, v158, v160\n\t"
"v_add_f32 v182, v177, v153\n\t"
"v_sub_f32 v183, v177, v153\n\t"
"v_add_f32 v184, v178, v172\n\t"
"v_sub_f32 v185, v178, v172\n\t"
"v_add_f32 v186, v179, v175\n\t"
"v_sub_f32 v187, v179, v175\n\t"
"v_add_f32 v188, v180, v159\n\t"
"v_sub_f32 v189, v180, v159\n\t"
"v_mul_f32 v191, 0.5, v182\n\t"
"v_mul_f32 v192, 0.5, v183\n\t"
"v_mul_f32 v194, 0.5, v171\n\t"
"v_mul_f32 v195, 0x3e800000, v184\n\t"
"v_mul_f32 v196, 0x3e800000, v185\n\t"
"v_mul_f32 v197, -0.5, v173\n\t"
"v_mul_f32 v199, 0.5, v186\n\t"
"v_mul_f32 v200, 0.5, v187\n\t"
"v_mul_f32 v203, -0.5, v188\n\t"
"v_mul_f32 v204, -0.5, v189\n\t"
"v_cvt_pk_bf16_f32 v206, v152, v191\n\t"
"v_cvt_pk_bf16_f32 v207, v192, -v154\n\t"
"v_cvt_pk_bf16_f32 v208, v194, v195\n\t"
"v_cvt_pk_bf16_f32 v209, v196, v197\n\t"
"v_cvt_pk_bf16_f32 v210, v174, v199\n\t"
"v_cvt_pk_bf16_f32 v211, v200, -v176\n\t"
"v_cvt_pk_bf16_f32 v212, -v158, v203\n\t"
"v_cvt_pk_bf16_f32 v213, v204, v160\n\t"
"s_add_u32 s60, s57, 0\n\t"
"buffer_store_short v206, v162, s[8:11], s60 offen sc1\n\t"
"s_add_u32 s60, s57, 6144\n\t"
"buffer_store_short_d16_hi v206, v162, s[8:11], s60 offen sc1\n\t"
"s_add_u32 s60, s57, 12288\n\t"
"buffer_store_short v207, v162, s[8:11], s60 offen sc1\n\t"
"s_add_u32 s60, s57, 18432\n\t"
"buffer_store_short_d16_hi v207, v162, s[8:11], s60 offen sc1\n\t"
"s_add_u32 s60, s57, 24576\n\t"
"buffer_store_short v208, v162, s[8:11], s60 offen sc1\n\t"
"s_add_u32 s60, s57, 30720\n\t"
"buffer_store_short_d16_hi v208, v162, s[8:11], s60 offen sc1\n\t"
"s_add_u32 s60, s57, 36864\n\t"
"buffer_store_short v209, v162, s[8:11], s60 offen sc1\n\t"
"s_add_u32 s60, s57, 43008\n\t"
"buffer_store_short_d16_hi v209, v162, s[8:11], s60 offen sc1\n\t"
"s_add_u32 s60, s57, 49152\n\t"
"buffer_store_short v210, v162, s[8:11], s60 offen sc1\n\t"
"s_add_u32 s60, s57, 55296\n\t"
"buffer_store_short_d16_hi v210, v162, s[8:11], s60 offen sc1\n\t"
"s_add_u32 s60, s57, 61440\n\t"
"buffer_store_short v211, v162, s[8:11], s60 offen sc1\n\t"
"s_add_u32 s60, s57, 67584\n\t"
"buffer_store_short_d16_hi v211, v162, s[8:11], s60 offen sc1\n\t"
"s_add_u32 s60, s57, 73728\n\t"
"buffer_store_short v212, v162, s[8:11], s60 offen sc1\n\t"
"s_add_u32 s60, s57, 79872\n\t"
"buffer_store_short_d16_hi v212, v162, s[8:11], s60 offen sc1\n\t"
"s_add_u32 s60, s57, 86016\n\t"
"buffer_store_short v213, v162, s[8:11], s60 offen sc1\n\t"
"s_add_u32 s60, s57, 92160\n\t"
"buffer_store_short_d16_hi v213, v162, s[8:11], s60 offen sc1\n\t"
"s_waitcnt vmcnt(0)\n\t"
"s_add_u32 s69, s69, 1\n\t"
"s_cmp_lt_u32 s69, 192\n\t"
"s_cbranch_scc1 Lfb1_%=\n\t"
"s_add_u32 s67, s67, 1\n\t"
"s_cmp_lt_u32 s67, 3\n\t"
"s_cbranch_scc1 Lfb0_%=\n\t"
"s_branch Lgo_%=\n\t"
"Ltail_%=:\n\t"
"v_mfma_f32_16x16x32_bf16 v[104:107], a[72:75], a[152:155], v[104:107]\n\t"
"ds_read_b128 a[144:147], v216 offset:20480\n\t"
"v_add_u32 v248, 143104, v227\n\t"
"v_mfma_f32_16x16x32_bf16 v[104:107], a[76:79], a[168:171], v[104:107]\n\t"
"ds_read_b128 a[160:163], v216 offset:28672\n\t"
"ds_read_b128 a[148:151], v216 offset:21504\n\t"
"v_mfma_f32_16x16x32_bf16 v[120:123], a[80:83], a[152:155], v[120:123]\n\t"
"ds_read_b128 a[164:167], v216 offset:29696\n\t"
"v_xor_b32 v249, 32, v248\n\t"
"v_mfma_f32_16x16x32_bf16 v[120:123], a[84:87], a[168:171], v[120:123]\n\t"
"buffer_load_dwordx4 a[8:11], v235, s[84:87], s69 offen offset:2048 sc1\n\t"
"v_mfma_f32_16x16x32_bf16 v[136:139], a[88:91], a[152:155], v[136:139]\n\t"
"ds_read_b128 v[8:11], v229 offset:19968\n\t"
"v_xor_b32 v250, 64, v248\n\t"
"v_mfma_f32_16x16x32_bf16 v[136:139], a[92:95], a[168:171], v[136:139]\n\t"
"buffer_load_dwordx4 a[0:3], v235, s[84:87], s69 offen offset:0 sc1\n\t"
"buffer_load_dwordx4 a[4:7], v235, s[84:87], s69 offen offset:1024 sc1\n\t"
"v_mfma_f32_16x16x32_bf16 v[192:195], a[176:179], a[224:227], 0\n\t"
"buffer_load_dwordx4 a[40:43], v218, s[84:87], s81 offen offset:0 sc1\n\t"
"v_xor_b32 v251, 96, v248\n\t"
"v_mfma_f32_16x16x32_bf16 v[140:143], a[88:91], a[156:159], v[140:143]\n\t"
"ds_read_b128 v[24:27], v230 offset:19968\n\t"
"v_mfma_f32_16x16x32_bf16 v[140:143], a[92:95], a[172:175], v[140:143]\n\t"
"buffer_load_dwordx4 a[44:47], v218, s[84:87], s81 offen offset:1024 sc1\n\t"
"buffer_load_dwordx4 a[36:39], v235, s[84:87], s81 offen offset:3072 sc1\n\t"
"v_mfma_f32_16x16x32_bf16 v[196:199], a[180:183], a[224:227], 0\n\t"
"buffer_load_dwordx4 a[32:35], v235, s[84:87], s81 offen offset:2048 sc1\n\t"
"v_mfma_f32_16x16x32_bf16 v[124:127], a[80:83], a[156:159], v[124:127]\n\t"
"ds_read_b128 a[152:155], v216 offset:22528\n\t"
"v_cvt_pk_bf16_f32 v192, v192, v193\n\t"
"v_mfma_f32_16x16x32_bf16 v[124:127], a[84:87], a[172:175], v[124:127]\n\t"
"buffer_load_dwordx4 a[24:27], v235, s[84:87], s81 offen offset:0 sc1\n\t"
"v_mfma_f32_16x16x32_bf16 v[108:111], a[72:75], a[156:159], v[108:111]\n\t"
"ds_read_b128 a[168:171], v216 offset:30720\n\t"
"v_cvt_pk_bf16_f32 v193, v194, v195\n\t"
"v_mfma_f32_16x16x32_bf16 v[108:111], a[76:79], a[172:175], v[108:111]\n\t"
"s_waitcnt lgkmcnt(3)\n\t"
"buffer_store_dwordx4 v[8:11], v232, s[16:19], s24 offen nt\n\t"
"v_mfma_f32_16x16x32_bf16 v[200:203], a[184:187], a[224:227], 0\n\t"
"buffer_load_dwordx4 a[28:31], v235, s[84:87], s81 offen offset:1024 sc1\n\t"
"s_waitcnt vmcnt(19)\n\t"
"v_mfma_f32_16x16x32_bf16 v[144:147], a[96:99], a[144:147], v[144:147]\n\t"
"ds_read_b128 v[40:43], v231 offset:19968\n\t"
"v_cvt_pk_bf16_f32 v194, v196, v197\n\t"
"v_mfma_f32_16x16x32_bf16 v[144:147], a[100:103], a[160:163], v[144:147]\n\t"
"v_cvt_pk_bf16_f32 v195, v198, v199\n\t"
"ds_read_b128 a[156:159], v216 offset:23552\n\t"
"v_mfma_f32_16x16x32_bf16 v[204:207], a[188:191], a[224:227], 0\n\t"
"buffer_load_dwordx4 a[64:67], v218, s[84:87], s54 offen offset:0 sc1\n\t"
"v_mfma_f32_16x16x32_bf16 v[160:163], a[104:107], a[144:147], v[160:163]\n\t"
"ds_read_b128 a[172:175], v216 offset:31744\n\t"
"v_cvt_pk_bf16_f32 v200, v200, v201\n\t"
"v_mfma_f32_16x16x32_bf16 v[160:163], a[108:111], a[160:163], v[160:163]\n\t"
"ds_write_b128 v221, v[192:195] offset:0\n\t"
"v_mfma_f32_16x16x32_bf16 v[176:179], a[112:115], a[144:147], v[176:179]\n\t"
"v_cvt_pk_bf16_f32 v201, v202, v203\n\t"
"ds_read_b128 v[12:15], v229 offset:33280\n\t"
"v_mfma_f32_16x16x32_bf16 v[176:179], a[116:119], a[160:163], v[176:179]\n\t"
"buffer_load_dwordx4 a[68:71], v218, s[84:87], s54 offen offset:1024 sc1\n\t"
"v_mfma_f32_16x16x32_bf16 v[208:211], a[192:195], a[224:227], 0\n\t"
"s_waitcnt lgkmcnt(5)\n\t"
"buffer_store_dwordx4 v[24:27], v233, s[16:19], s24 offen nt\n\t"
"v_mfma_f32_16x16x32_bf16 v[180:183], a[112:115], a[148:151], v[180:183]\n\t"
"v_cvt_pk_bf16_f32 v202, v204, v205\n\t"
"v_cvt_pk_bf16_f32 v203, v206, v207\n\t"
"v_mfma_f32_16x16x32_bf16 v[180:183], a[116:119], a[164:167], v[180:183]\n\t"
"ds_read_b128 a[144:147], v216 offset:16384\n\t"
"v_mfma_f32_16x16x32_bf16 v[164:167], a[104:107], a[148:151], v[164:167]\n\t"
"ds_read_b128 a[160:163], v216 offset:24576\n\t"
"v_mfma_f32_16x16x32_bf16 v[164:167], a[108:111], a[164:167], v[164:167]\n\t"
"buffer_load_dwordx4 a[60:63], v235, s[84:87], s54 offen offset:3072 sc1\n\t"
"ds_write_b128 v221, v[200:203] offset:8192\n\t"
"v_mfma_f32_16x16x32_bf16 v[212:215], a[196:199], a[224:227], 0\n\t"
"s_waitcnt lgkmcnt(6)\n\t"
"buffer_store_dwordx4 v[40:43], v234, s[16:19], s24 offen nt\n\t"
"v_mfma_f32_16x16x32_bf16 v[148:151], a[96:99], a[148:151], v[148:151]\n\t"
"v_cvt_pk_bf16_f32 v208, v208, v209\n\t"
"v_cvt_pk_bf16_f32 v209, v210, v211\n\t"
"v_mfma_f32_16x16x32_bf16 v[148:151], a[100:103], a[164:167], v[148:151]\n\t"
"buffer_load_dwordx4 a[56:59], v235, s[84:87], s54 offen offset:2048 sc1\n\t"
"v_mfma_f32_16x16x32_bf16 v[192:195], a[200:203], a[224:227], 0\n\t"
"ds_read_b128 v[28:31], v230 offset:33280\n\t"
"v_mfma_f32_16x16x32_bf16 v[152:155], a[96:99], a[152:155], v[152:155]\n\t"
"ds_read_b128 a[148:151], v216 offset:17408\n\t"
"v_cvt_pk_bf16_f32 v210, v212, v213\n\t"
"v_mfma_f32_16x16x32_bf16 v[152:155], a[100:103], a[168:171], v[152:155]\n\t"
"ds_read_b128 a[164:167], v216 offset:25600\n\t"
"buffer_load_dwordx4 a[48:51], v235, s[84:87], s54 offen offset:0 sc1\n\t"
"v_cvt_pk_bf16_f32 v211, v214, v215\n\t"
"v_mfma_f32_16x16x32_bf16 v[168:171], a[104:107], a[152:155], v[168:171]\n\t"
"ds_read_b128 v[44:47], v231 offset:33280\n\t"
"v_mfma_f32_16x16x32_bf16 v[168:171], a[108:111], a[168:171], v[168:171]\n\t"
"s_waitcnt lgkmcnt(6)\n\t"
"buffer_store_dwordx4 v[12:15], v232, s[16:19], s27 offen nt\n\t"
"v_mfma_f32_16x16x32_bf16 v[196:199], a[204:207], a[224:227], 0\n\t"
"buffer_load_dwordx4 a[52:55], v235, s[84:87], s54 offen offset:1024 sc1\n\t"
"v_cvt_pk_bf16_f32 v192, v192, v193\n\t"
"v_cvt_pk_bf16_f32 v193, v194, v195\n\t"
"v_mfma_f32_16x16x32_bf16 v[184:187], a[112:115], a[152:155], v[184:187]\n\t"
"ds_read_b64_tr_b16 a[176:177], v248 offset:0\n\t"
"v_mfma_f32_16x16x32_bf16 v[184:187], a[116:119], a[168:171], v[184:187]\n\t"
"ds_write_b128 v221, v[208:211] offset:4096\n\t"
"v_mfma_f32_16x16x32_bf16 v[188:191], a[112:115], a[156:159], v[188:191]\n\t"
"ds_read_b64_tr_b16 a[178:179], v248 offset:512\n\t"
"v_mfma_f32_16x16x32_bf16 v[188:191], a[116:119], a[172:175], v[188:191]\n\t"
"buffer_load_dwordx4 a[88:91], v218, s[84:87], s66 offen offset:0 sc1\n\t"
"ds_read_b64_tr_b16 a[180:181], v249 offset:0\n\t"
"ds_read_b64_tr_b16 a[182:183], v249 offset:512\n\t"
"v_mfma_f32_16x16x32_bf16 v[172:175], a[104:107], a[156:159], v[172:175]\n\t"
"ds_read_b64_tr_b16 a[184:185], v250 offset:0\n\t"
"v_cvt_pk_bf16_f32 v194, v196, v197\n\t"
"v_mfma_f32_16x16x32_bf16 v[172:175], a[108:111], a[172:175], v[172:175]\n\t"
"ds_read_b64_tr_b16 a[186:187], v250 offset:512\n\t"
"v_cvt_pk_bf16_f32 v195, v198, v199\n\t"
"v_mfma_f32_16x16x32_bf16 v[156:159], a[96:99], a[156:159], v[156:159]\n\t"
"ds_read_b64_tr_b16 a[188:189], v251 offset:0\n\t"
"v_mfma_f32_16x16x32_bf16 v[156:159], a[100:103], a[172:175], v[156:159]\n\t"
"ds_write_b128 v221, v[192:195] offset:12288\n\t"
"ds_read_b64_tr_b16 a[190:191], v251 offset:512\n\t"
"s_waitcnt vmcnt(23) lgkmcnt(10)\n\t"
"v_mfma_f32_16x16x32_bf16 v[96:99], a[120:123], a[144:147], v[96:99]\n\t"
"ds_read_b64_tr_b16 a[192:193], v248 offset:1152\n\t"
"v_mfma_f32_16x16x32_bf16 v[96:99], a[124:127], a[160:163], v[96:99]\n\t"
"ds_read_b64_tr_b16 a[194:195], v248 offset:1664\n\t"
"ds_read_b64_tr_b16 a[196:197], v249 offset:1152\n\t"
"ds_read_b64_tr_b16 a[198:199], v249 offset:1664\n\t"
"ds_read_b64_tr_b16 a[200:201], v250 offset:1152\n\t"
"ds_read_b64_tr_b16 a[202:203], v250 offset:1664\n\t"
"ds_read_b64_tr_b16 a[204:205], v251 offset:1152\n\t"
"ds_read_b64_tr_b16 a[206:207], v251 offset:1664\n\t"
"buffer_store_dwordx4 v[28:31], v233, s[16:19], s27 offen nt\n\t"
"v_mfma_f32_16x16x32_bf16 v[112:115], a[128:131], a[144:147], v[112:115]\n\t"
"ds_read_b128 a[152:155], v216 offset:18432\n\t"
"v_mfma_f32_16x16x32_bf16 v[112:115], a[132:135], a[160:163], v[112:115]\n\t"
"buffer_load_dwordx4 a[92:95], v218, s[84:87], s66 offen offset:1024 sc1\n\t"
"v_mfma_f32_16x16x32_bf16 v[128:131], a[136:139], a[144:147], v[128:131]\n\t"
"ds_read_b128 a[168:171], v216 offset:26624\n\t"
"v_mfma_f32_16x16x32_bf16 v[128:131], a[140:143], a[160:163], v[128:131]\n\t"
"buffer_load_dwordx4 a[84:87], v235, s[84:87], s66 offen offset:3072 sc1\n\t"
"v_mfma_f32_16x16x32_bf16 v[132:135], a[136:139], a[148:151], v[132:135]\n\t"
"ds_read_b128 a[156:159], v216 offset:19456\n\t"
"v_mfma_f32_16x16x32_bf16 v[132:135], a[140:143], a[164:167], v[132:135]\n\t"
"ds_read_b128 a[172:175], v216 offset:27648\n\t"
"v_mfma_f32_16x16x32_bf16 v[116:119], a[128:131], a[148:151], v[116:119]\n\t"
"v_mfma_f32_16x16x32_bf16 v[116:119], a[132:135], a[164:167], v[116:119]\n\t"
"buffer_store_dwordx4 v[44:47], v234, s[16:19], s27 offen nt\n\t"
"buffer_load_dwordx4 a[80:83], v235, s[84:87], s66 offen offset:2048 sc1\n\t"
"v_mfma_f32_16x16x32_bf16 v[100:103], a[120:123], a[148:151], v[100:103]\n\t"
"v_mfma_f32_16x16x32_bf16 v[100:103], a[124:127], a[164:167], v[100:103]\n\t"
"buffer_load_dwordx4 a[72:75], v235, s[84:87], s66 offen offset:0 sc1\n\t"
"s_waitcnt lgkmcnt(0)\n\t"
"s_barrier\n\t"
"v_mfma_f32_16x16x32_bf16 v[104:107], a[120:123], a[152:155], v[104:107]\n\t"
"ds_read_b128 a[144:147], v216 offset:0\n\t"
"v_add_u32 v248, 101632, v226\n\t"
"v_mfma_f32_16x16x32_bf16 v[104:107], a[124:127], a[168:171], v[104:107]\n\t"
"ds_read_b128 a[160:163], v216 offset:8192\n\t"
"ds_read_b128 a[148:151], v216 offset:1024\n\t"
"v_mfma_f32_16x16x32_bf16 v[120:123], a[128:131], a[152:155], v[120:123]\n\t"
"ds_read_b128 a[164:167], v216 offset:9216\n\t"
"v_xor_b32 v249, 32, v248\n\t"
"v_mfma_f32_16x16x32_bf16 v[120:123], a[132:135], a[168:171], v[120:123]\n\t"
"buffer_load_dwordx4 a[76:79], v235, s[84:87], s66 offen offset:1024 sc1\n\t"
"v_mfma_f32_16x16x32_bf16 v[136:139], a[136:139], a[152:155], v[136:139]\n\t"
"v_xor_b32 v250, 64, v248\n\t"
"v_xor_b32 v251, 96, v248\n\t"
"v_mfma_f32_16x16x32_bf16 v[136:139], a[140:143], a[168:171], v[136:139]\n\t"
"buffer_load_dwordx4 a[112:115], v218, s[84:87], s57 offen offset:0 sc1\n\t"
"v_mfma_f32_16x16x32_bf16 v[200:203], a[176:179], a[224:227], 0\n\t"
"ds_read_b128 a[152:155], v216 offset:2048\n\t"
"v_mfma_f32_16x16x32_bf16 v[140:143], a[136:139], a[156:159], v[140:143]\n\t"
"ds_read_b128 a[168:171], v216 offset:10240\n\t"
"v_mfma_f32_16x16x32_bf16 v[140:143], a[140:143], a[172:175], v[140:143]\n\t"
"buffer_load_dwordx4 a[116:119], v218, s[84:87], s57 offen offset:1024 sc1\n\t"
"ds_read_b64_tr_b16 a[176:177], v248 offset:41472\n\t"
"ds_read_b64_tr_b16 a[178:179], v248 offset:41984\n\t"
"v_mfma_f32_16x16x32_bf16 v[204:207], a[180:183], a[224:227], 0\n\t"
"ds_read_b64_tr_b16 a[180:181], v249 offset:41472\n\t"
"ds_read_b64_tr_b16 a[182:183], v249 offset:41984\n\t"
"v_mfma_f32_16x16x32_bf16 v[124:127], a[128:131], a[156:159], v[124:127]\n\t"
"v_cvt_pk_bf16_f32 v200, v200, v201\n\t"
"v_cvt_pk_bf16_f32 v201, v202, v203\n\t"
"v_mfma_f32_16x16x32_bf16 v[124:127], a[132:135], a[172:175], v[124:127]\n\t"
"v_mfma_f32_16x16x32_bf16 v[108:111], a[120:123], a[156:159], v[108:111]\n\t"
"ds_read_b128 a[156:159], v216 offset:3072\n\t"
"v_mfma_f32_16x16x32_bf16 v[108:111], a[124:127], a[172:175], v[108:111]\n\t"
"buffer_load_dwordx4 a[108:111], v235, s[84:87], s57 offen offset:3072 sc1\n\t"
"buffer_load_dwordx4 a[104:107], v235, s[84:87], s57 offen offset:2048 sc1\n\t"
"v_mfma_f32_16x16x32_bf16 v[208:211], a[184:187], a[224:227], 0\n\t"
"buffer_load_dwordx4 a[96:99], v235, s[84:87], s57 offen offset:0 sc1\n\t"
"v_cvt_pk_bf16_f32 v202, v204, v205\n\t"
"v_cvt_pk_bf16_f32 v203, v206, v207\n\t"
"s_waitcnt vmcnt(29) lgkmcnt(7)\n\t"
"v_mfma_f32_16x16x32_bf16 v[48:51], a[0:3], a[144:147], v[48:51]\n\t"
"ds_read_b128 a[172:175], v216 offset:11264\n\t"
"v_mfma_f32_16x16x32_bf16 v[48:51], a[4:7], a[160:163], v[48:51]\n\t"
"buffer_load_dwordx4 a[100:103], v235, s[84:87], s57 offen offset:1024 sc1\n\t"
"buffer_load_dwordx4 a[136:139], v218, s[84:87], s63 offen offset:0 sc1\n\t"
"v_mfma_f32_16x16x32_bf16 v[212:215], a[188:191], a[224:227], 0\n\t"
"v_pk_add_f32 v[96:97], v[96:97], v[144:145]\n\t"
"v_pk_add_f32 v[98:99], v[98:99], v[146:147]\n\t"
"v_pk_fma_f32 v[144:145], v[144:145], s[38:39], v[96:97]\n\t"
"v_pk_fma_f32 v[146:147], v[146:147], s[38:39], v[98:99]\n\t"
"v_pk_add_f32 v[112:113], v[112:113], v[160:161]\n\t"
"v_pk_add_f32 v[114:115], v[114:115], v[162:163]\n\t"
"v_pk_fma_f32 v[160:161], v[160:161], s[38:39], v[112:113]\n\t"
"v_pk_fma_f32 v[162:163], v[162:163], s[38:39], v[114:115]\n\t"
"v_pk_add_f32 v[128:129], v[128:129], v[176:177]\n\t"
"v_pk_add_f32 v[130:131], v[130:131], v[178:179]\n\t"
"v_pk_fma_f32 v[176:177], v[176:177], s[38:39], v[128:129]\n\t"
"v_pk_fma_f32 v[178:179], v[178:179], s[38:39], v[130:131]\n\t"
"v_pk_add_f32 v[132:133], v[132:133], v[180:181]\n\t"
"v_pk_add_f32 v[134:135], v[134:135], v[182:183]\n\t"
"v_pk_fma_f32 v[180:181], v[180:181], s[38:39], v[132:133]\n\t"
"v_pk_fma_f32 v[182:183], v[182:183], s[38:39], v[134:135]\n\t"
"v_pk_add_f32 v[116:117], v[116:117], v[164:165]\n\t"
"v_pk_add_f32 v[118:119], v[118:119], v[166:167]\n\t"
"v_pk_fma_f32 v[164:165], v[164:165], s[38:39], v[116:117]\n\t"
"v_pk_fma_f32 v[166:167], v[166:167], s[38:39], v[118:119]\n\t"
"v_pk_add_f32 v[100:101], v[100:101], v[148:149]\n\t"
"v_pk_add_f32 v[102:103], v[102:103], v[150:151]\n\t"
"v_pk_fma_f32 v[148:149], v[148:149], s[38:39], v[100:101]\n\t"
"v_pk_fma_f32 v[150:151], v[150:151], s[38:39], v[102:103]\n\t"
"v_pk_add_f32 v[104:105], v[104:105], v[152:153]\n\t"
"v_pk_add_f32 v[106:107], v[106:107], v[154:155]\n\t"
"v_pk_fma_f32 v[152:153], v[152:153], s[38:39], v[104:105]\n\t"
"v_pk_fma_f32 v[154:155], v[154:155], s[38:39], v[106:107]\n\t"
"v_pk_add_f32 v[120:121], v[120:121], v[168:169]\n\t"
"v_pk_add_f32 v[122:123], v[122:123], v[170:171]\n\t"
"v_pk_fma_f32 v[168:169], v[168:169], s[38:39], v[120:121]\n\t"
"v_pk_fma_f32 v[170:171], v[170:171], s[38:39], v[122:123]\n\t"
"v_pk_add_f32 v[136:137], v[136:137], v[184:185]\n\t"
"v_pk_add_f32 v[138:139], v[138:139], v[186:187]\n\t"
"v_pk_fma_f32 v[184:185], v[184:185], s[38:39], v[136:137]\n\t"
"v_pk_fma_f32 v[186:187], v[186:187], s[38:39], v[138:139]\n\t"
"v_pk_add_f32 v[140:141], v[140:141], v[188:189]\n\t"
"v_pk_add_f32 v[142:143], v[142:143], v[190:191]\n\t"
"v_pk_fma_f32 v[188:189], v[188:189], s[38:39], v[140:141]\n\t"
"v_pk_fma_f32 v[190:191], v[190:191], s[38:39], v[142:143]\n\t"
"v_pk_add_f32 v[124:125], v[124:125], v[172:173]\n\t"
"v_pk_add_f32 v[126:127], v[126:127], v[174:175]\n\t"
"v_pk_fma_f32 v[172:173], v[172:173], s[38:39], v[124:125]\n\t"
"v_pk_fma_f32 v[174:175], v[174:175], s[38:39], v[126:127]\n\t"
"v_pk_add_f32 v[108:109], v[108:109], v[156:157]\n\t"
"v_pk_add_f32 v[110:111], v[110:111], v[158:159]\n\t"
"v_pk_fma_f32 v[156:157], v[156:157], s[38:39], v[108:109]\n\t"
"v_pk_fma_f32 v[158:159], v[158:159], s[38:39], v[110:111]\n\t"
"v_cvt_pk_bf16_f32 v96, v96, v97\n\t"
"v_cvt_pk_bf16_f32 v97, v98, v99\n\t"
"v_cvt_pk_bf16_f32 v100, v100, v101\n\t"
"v_cvt_pk_bf16_f32 v101, v102, v103\n\t"
"v_mfma_f32_16x16x32_bf16 v[64:67], a[8:11], a[144:147], v[64:67]\n\t"
"v_cvt_pk_bf16_f32 v208, v208, v209\n\t"
"v_cvt_pk_bf16_f32 v209, v210, v211\n\t"
"v_mfma_f32_16x16x32_bf16 v[64:67], a[12:15], a[160:163], v[64:67]\n\t"
"buffer_load_dwordx4 a[140:143], v218, s[84:87], s63 offen offset:1024 sc1\n\t"
"buffer_load_dwordx4 a[132:135], v235, s[84:87], s63 offen offset:3072 sc1\n\t"
"v_mfma_f32_16x16x32_bf16 v[80:83], a[16:19], a[144:147], v[80:83]\n\t"
"v_cvt_pk_bf16_f32 v112, v112, v113\n\t"
"v_cvt_pk_bf16_f32 v113, v114, v115\n\t"
"v_mfma_f32_16x16x32_bf16 v[80:83], a[20:23], a[160:163], v[80:83]\n\t"
"ds_write2st64_b64 v228, v[96:97], v[100:101] offset0:39 offset1:52\n\t"
"ds_write_b128 v221, v[200:203] offset:16384\n\t"
"v_mfma_f32_16x16x32_bf16 v[192:195], a[192:195], a[224:227], 0\n\t"
"v_cvt_pk_bf16_f32 v116, v116, v117\n\t"
"v_cvt_pk_bf16_f32 v117, v118, v119\n\t"
"buffer_load_dwordx4 a[128:131], v235, s[84:87], s63 offen offset:2048 sc1\n\t"
"v_mfma_f32_16x16x32_bf16 v[84:87], a[16:19], a[148:151], v[84:87]\n\t"
"v_cvt_pk_bf16_f32 v210, v212, v213\n\t"
"v_cvt_pk_bf16_f32 v211, v214, v215\n\t"
"v_mfma_f32_16x16x32_bf16 v[84:87], a[20:23], a[164:167], v[84:87]\n\t"
"ds_write2st64_b64 v246, v[112:113], v[116:117] offset0:39 offset1:52\n\t"
"v_mfma_f32_16x16x32_bf16 v[68:71], a[8:11], a[148:151], v[68:71]\n\t"
"ds_read_b128 a[144:147], v216 offset:4096\n\t"
"v_cvt_pk_bf16_f32 v128, v128, v129\n\t"
"v_mfma_f32_16x16x32_bf16 v[68:71], a[12:15], a[164:167], v[68:71]\n\t"
"ds_read_b128 a[160:163], v216 offset:12288\n\t"
"v_cvt_pk_bf16_f32 v129, v130, v131\n\t"
"v_cvt_pk_bf16_f32 v132, v132, v133\n\t"
"v_cvt_pk_bf16_f32 v133, v134, v135\n\t"
"v_mfma_f32_16x16x32_bf16 v[196:199], a[196:199], a[224:227], 0\n\t"
"buffer_load_dwordx4 a[120:123], v235, s[84:87], s63 offen offset:0 sc1\n\t"
"v_mfma_f32_16x16x32_bf16 v[52:55], a[0:3], a[148:151], v[52:55]\n\t"
"v_cvt_pk_bf16_f32 v192, v192, v193\n\t"
"v_cvt_pk_bf16_f32 v193, v194, v195\n\t"
"v_mfma_f32_16x16x32_bf16 v[52:55], a[4:7], a[164:167], v[52:55]\n\t"
"ds_write2st64_b64 v247, v[128:129], v[132:133] offset0:39 offset1:52\n\t"
"v_mfma_f32_16x16x32_bf16 v[200:203], a[200:203], a[224:227], 0\n\t"
"ds_write_b128 v221, v[208:211] offset:24576\n\t"
"buffer_load_dwordx4 a[124:127], v235, s[84:87], s63 offen offset:1024 sc1\n\t"
"s_waitcnt lgkmcnt(7)\n\t"
"v_mfma_f32_16x16x32_bf16 v[56:59], a[0:3], a[152:155], v[56:59]\n\t"
"v_cvt_pk_bf16_f32 v104, v104, v105\n\t"
"v_cvt_pk_bf16_f32 v105, v106, v107\n\t"
"v_mfma_f32_16x16x32_bf16 v[56:59], a[4:7], a[168:171], v[56:59]\n\t"
"v_cvt_pk_bf16_f32 v108, v108, v109\n\t"
"v_cvt_pk_bf16_f32 v109, v110, v111\n\t"
"v_mfma_f32_16x16x32_bf16 v[72:75], a[8:11], a[152:155], v[72:75]\n\t"
"ds_read_b128 a[148:151], v216 offset:5120\n\t"
"v_cvt_pk_bf16_f32 v194, v196, v197\n\t"
"v_mfma_f32_16x16x32_bf16 v[72:75], a[12:15], a[168:171], v[72:75]\n\t"
"ds_read_b128 a[164:167], v216 offset:13312\n\t"
"ds_write2st64_b64 v228, v[104:105], v[108:109] offset0:65 offset1:78\n\t"
"v_cvt_pk_bf16_f32 v195, v198, v199\n\t"
"v_mfma_f32_16x16x32_bf16 v[204:207], a[204:207], a[224:227], 0\n\t"
"v_cvt_pk_bf16_f32 v120, v120, v121\n\t"
"v_cvt_pk_bf16_f32 v121, v122, v123\n\t"
"v_mfma_f32_16x16x32_bf16 v[88:91], a[16:19], a[152:155], v[88:91]\n\t"
"v_cvt_pk_bf16_f32 v124, v124, v125\n\t"
"v_cvt_pk_bf16_f32 v125, v126, v127\n\t"
"v_mfma_f32_16x16x32_bf16 v[88:91], a[20:23], a[168:171], v[88:91]\n\t"
"ds_write2st64_b64 v246, v[120:121], v[124:125] offset0:65 offset1:78\n\t"
"v_cvt_pk_bf16_f32 v200, v200, v201\n\t"
"v_cvt_pk_bf16_f32 v201, v202, v203\n\t"
"v_mfma_f32_16x16x32_bf16 v[92:95], a[16:19], a[156:159], v[92:95]\n\t"
"v_cvt_pk_bf16_f32 v136, v136, v137\n\t"
"v_cvt_pk_bf16_f32 v137, v138, v139\n\t"
"v_mfma_f32_16x16x32_bf16 v[92:95], a[20:23], a[172:175], v[92:95]\n\t"
"buffer_load_dwordx4 a[16:19], v218, s[84:87], s42 offen offset:0 sc1\n\t"
"v_mfma_f32_16x16x32_bf16 v[76:79], a[8:11], a[156:159], v[76:79]\n\t"
"v_cvt_pk_bf16_f32 v140, v140, v141\n\t"
"v_cvt_pk_bf16_f32 v141, v142, v143\n\t"
"v_mfma_f32_16x16x32_bf16 v[76:79], a[12:15], a[172:175], v[76:79]\n\t"
"ds_write_b128 v221, v[192:195] offset:20480\n\t"
"buffer_load_dwordx4 a[20:23], v218, s[84:87], s42 offen offset:1024 sc1\n\t"
"v_mfma_f32_16x16x32_bf16 v[60:63], a[0:3], a[156:159], v[60:63]\n\t"
"ds_read_b64_tr_b16 a[184:185], v250 offset:43776\n\t"
"v_cvt_pk_bf16_f32 v202, v204, v205\n\t"
"v_mfma_f32_16x16x32_bf16 v[60:63], a[4:7], a[172:175], v[60:63]\n\t"
"buffer_load_dwordx4 a[12:15], v235, s[84:87], s42 offen offset:3072 sc1\n\t"
"s_waitcnt vmcnt(32) lgkmcnt(5)\n\t"
"v_mfma_f32_16x16x32_bf16 v[144:147], a[24:27], a[144:147], v[144:147]\n\t"
"ds_read_b64_tr_b16 a[186:187], v250 offset:44288\n\t"
"v_cvt_pk_bf16_f32 v203, v206, v207\n\t"
"v_mfma_f32_16x16x32_bf16 v[144:147], a[28:31], a[160:163], v[144:147]\n\t"
"ds_write_b128 v221, v[200:203] offset:28672\n\t"
"ds_read_b64_tr_b16 a[188:189], v251 offset:43776\n\t"
"ds_read_b64_tr_b16 a[190:191], v251 offset:44288\n\t"
"ds_read_b64_tr_b16 a[192:193], v250 offset:41472\n\t"
"ds_read_b64_tr_b16 a[194:195], v250 offset:41984\n\t"
"ds_read_b64_tr_b16 a[196:197], v251 offset:41472\n\t"
"ds_read_b64_tr_b16 a[198:199], v251 offset:41984\n\t"
"ds_read_b64_tr_b16 a[200:201], v248 offset:43776\n\t"
"ds_read_b64_tr_b16 a[202:203], v248 offset:44288\n\t"
"ds_read_b64_tr_b16 a[204:205], v249 offset:43776\n\t"
"ds_read_b64_tr_b16 a[206:207], v249 offset:44288\n\t"
"ds_write2st64_b64 v247, v[136:137], v[140:141] offset0:65 offset1:78\n\t"
"v_mfma_f32_16x16x32_bf16 v[160:163], a[32:35], a[144:147], v[160:163]\n\t"
"v_mfma_f32_16x16x32_bf16 v[160:163], a[36:39], a[160:163], v[160:163]\n\t"
"buffer_load_dwordx4 a[8:11], v235, s[84:87], s42 offen offset:2048 sc1\n\t"
"v_mfma_f32_16x16x32_bf16 v[176:179], a[40:43], a[144:147], v[176:179]\n\t"
"v_mfma_f32_16x16x32_bf16 v[176:179], a[44:47], a[160:163], v[176:179]\n\t"
"buffer_load_dwordx4 a[0:3], v235, s[84:87], s42 offen offset:0 sc1\n\t"
"s_waitcnt lgkmcnt(15)\n\t"
"v_mfma_f32_16x16x32_bf16 v[180:183], a[40:43], a[148:151], v[180:183]\n\t"
"ds_read_b128 a[152:155], v216 offset:6144\n\t"
"v_mfma_f32_16x16x32_bf16 v[180:183], a[44:47], a[164:167], v[180:183]\n\t"
"ds_read_b128 a[168:171], v216 offset:14336\n\t"
"ds_read_b128 a[156:159], v216 offset:7168\n\t"
"ds_read_b128 a[172:175], v216 offset:15360\n\t"
"v_mfma_f32_16x16x32_bf16 v[164:167], a[32:35], a[148:151], v[164:167]\n\t"
"v_mfma_f32_16x16x32_bf16 v[164:167], a[36:39], a[164:167], v[164:167]\n\t"
"buffer_load_dwordx4 a[4:7], v235, s[84:87], s42 offen offset:1024 sc1\n\t"
"v_mfma_f32_16x16x32_bf16 v[148:151], a[24:27], a[148:151], v[148:151]\n\t"
"v_mfma_f32_16x16x32_bf16 v[148:151], a[28:31], a[164:167], v[148:151]\n\t"
"s_waitcnt lgkmcnt(0)\n\t"
"s_barrier\n\t"
"v_mfma_f32_16x16x32_bf16 v[152:155], a[24:27], a[152:155], v[152:155]\n\t"
"ds_read_b128 a[144:147], v216 offset:16384\n\t"
"v_mfma_f32_16x16x32_bf16 v[152:155], a[28:31], a[168:171], v[152:155]\n\t"
"ds_read_b128 a[160:163], v216 offset:24576\n\t"
"ds_read_b128 a[148:151], v216 offset:17408\n\t"
"v_mfma_f32_16x16x32_bf16 v[168:171], a[32:35], a[152:155], v[168:171]\n\t"
"ds_read_b128 a[164:167], v216 offset:25600\n\t"
"v_mfma_f32_16x16x32_bf16 v[168:171], a[36:39], a[168:171], v[168:171]\n\t"
"ds_read_b128 v[104:107], v229 offset:19968\n\t"
"v_mfma_f32_16x16x32_bf16 v[184:187], a[40:43], a[152:155], v[184:187]\n\t"
"ds_read_b128 a[152:155], v216 offset:18432\n\t"
"v_mfma_f32_16x16x32_bf16 v[184:187], a[44:47], a[168:171], v[184:187]\n\t"
"ds_read_b128 v[120:123], v230 offset:19968\n\t"
"v_mfma_f32_16x16x32_bf16 v[208:211], a[176:179], a[220:223], 0\n\t"
"ds_read_b128 a[168:171], v216 offset:26624\n\t"
"v_mfma_f32_16x16x32_bf16 v[188:191], a[40:43], a[156:159], v[188:191]\n\t"
"ds_read_b64_tr_b16 a[176:177], v248 offset:0\n\t"
"v_mfma_f32_16x16x32_bf16 v[188:191], a[44:47], a[172:175], v[188:191]\n\t"
"buffer_load_dwordx4 a[40:43], v218, s[84:87], s48 offen offset:0 sc1\n\t"
"buffer_load_dwordx4 a[44:47], v218, s[84:87], s48 offen offset:1024 sc1\n\t"
"v_mfma_f32_16x16x32_bf16 v[212:215], a[180:183], a[220:223], 0\n\t"
"ds_read_b64_tr_b16 a[178:179], v248 offset:512\n\t"
"v_mfma_f32_16x16x32_bf16 v[172:175], a[32:35], a[156:159], v[172:175]\n\t"
"v_cvt_pk_bf16_f32 v208, v208, v209\n\t"
"v_cvt_pk_bf16_f32 v209, v210, v211\n\t"
"v_mfma_f32_16x16x32_bf16 v[172:175], a[36:39], a[172:175], v[172:175]\n\t"
"buffer_load_dwordx4 a[36:39], v235, s[84:87], s48 offen offset:3072 sc1\n\t"
"s_waitcnt lgkmcnt(5)\n\t"
"buffer_store_dwordx4 v[104:107], v232, s[16:19], s20 offen nt\n\t"
"v_mfma_f32_16x16x32_bf16 v[156:159], a[24:27], a[156:159], v[156:159]\n\t"
"ds_read_b128 v[136:139], v231 offset:19968\n\t"
"v_mfma_f32_16x16x32_bf16 v[156:159], a[28:31], a[172:175], v[156:159]\n\t"
"buffer_load_dwordx4 a[32:35], v235, s[84:87], s48 offen offset:2048 sc1\n\t"
"v_mfma_f32_16x16x32_bf16 v[192:195], a[184:187], a[220:223], 0\n\t"
"buffer_load_dwordx4 a[24:27], v235, s[84:87], s48 offen offset:0 sc1\n\t"
"s_waitcnt vmcnt(32) lgkmcnt(5)\n\t"
"v_mfma_f32_16x16x32_bf16 v[48:51], a[48:51], a[144:147], v[48:51]\n\t"
"v_cvt_pk_bf16_f32 v210, v212, v213\n\t"
"v_cvt_pk_bf16_f32 v211, v214, v215\n\t"
"v_mfma_f32_16x16x32_bf16 v[48:51], a[52:55], a[160:163], v[48:51]\n\t"
"buffer_load_dwordx4 a[28:31], v235, s[84:87], s48 offen offset:1024 sc1\n\t"
"v_mfma_f32_16x16x32_bf16 v[196:199], a[188:191], a[220:223], 0\n\t"
"ds_read_b128 a[156:159], v216 offset:19456\n\t"
"v_mfma_f32_16x16x32_bf16 v[64:67], a[56:59], a[144:147], v[64:67]\n\t"
"ds_read_b128 a[172:175], v216 offset:27648\n\t"
"v_cvt_pk_bf16_f32 v192, v192, v193\n\t"
"v_mfma_f32_16x16x32_bf16 v[64:67], a[60:63], a[160:163], v[64:67]\n\t"
"ds_write_b128 v217, v[208:211] offset:0\n\t"
"v_mfma_f32_16x16x32_bf16 v[80:83], a[64:67], a[144:147], v[80:83]\n\t"
"v_cvt_pk_bf16_f32 v193, v194, v195\n\t"
"ds_read_b128 v[108:111], v229 offset:33280\n\t"
"v_mfma_f32_16x16x32_bf16 v[80:83], a[68:71], a[160:163], v[80:83]\n\t"
"s_waitcnt lgkmcnt(5)\n\t"
"buffer_store_dwordx4 v[120:123], v233, s[16:19], s20 offen nt\n\t"
"v_mfma_f32_16x16x32_bf16 v[200:203], a[192:195], a[220:223], 0\n\t"
"v_cvt_pk_bf16_f32 v194, v196, v197\n\t"
"v_cvt_pk_bf16_f32 v195, v198, v199\n\t"
"v_mfma_f32_16x16x32_bf16 v[84:87], a[64:67], a[148:151], v[84:87]\n\t"
"ds_read_b128 a[144:147], v216 offset:20480\n\t"
"v_mfma_f32_16x16x32_bf16 v[84:87], a[68:71], a[164:167], v[84:87]\n\t"
"ds_read_b128 a[160:163], v216 offset:28672\n\t"
"v_mfma_f32_16x16x32_bf16 v[68:71], a[56:59], a[148:151], v[68:71]\n\t"
"ds_read_b64_tr_b16 a[180:181], v249 offset:0\n\t"
"v_mfma_f32_16x16x32_bf16 v[68:71], a[60:63], a[164:167], v[68:71]\n\t"
"ds_write_b128 v219, v[192:195] offset:0\n\t"
"s_waitcnt lgkmcnt(8)\n\t"
"buffer_store_dwordx4 v[136:139], v234, s[16:19], s20 offen nt\n\t"
"v_mfma_f32_16x16x32_bf16 v[204:207], a[196:199], a[220:223], 0\n\t"
"v_cvt_pk_bf16_f32 v200, v200, v201\n\t"
"v_cvt_pk_bf16_f32 v201, v202, v203\n\t"
"v_mfma_f32_16x16x32_bf16 v[52:55], a[48:51], a[148:151], v[52:55]\n\t"
"ds_read_b128 v[124:127], v230 offset:33280\n\t"
"v_mfma_f32_16x16x32_bf16 v[52:55], a[52:55], a[164:167], v[52:55]\n\t"
"ds_read_b128 a[148:151], v216 offset:21504\n\t"
"v_mfma_f32_16x16x32_bf16 v[208:211], a[200:203], a[220:223], 0\n\t"
"ds_read_b128 a[164:167], v216 offset:29696\n\t"
"ds_read_b64_tr_b16 a[182:183], v249 offset:512\n\t"
"v_mfma_f32_16x16x32_bf16 v[56:59], a[48:51], a[152:155], v[56:59]\n\t"
"v_cvt_pk_bf16_f32 v202, v204, v205\n\t"
"v_cvt_pk_bf16_f32 v203, v206, v207\n\t"
"v_mfma_f32_16x16x32_bf16 v[56:59], a[52:55], a[168:171], v[56:59]\n\t"
"s_waitcnt lgkmcnt(8)\n\t"
"buffer_store_dwordx4 v[108:111], v232, s[16:19], s25 offen nt\n\t"
"ds_write_b128 v217, v[200:203] offset:8192\n\t"
"v_mfma_f32_16x16x32_bf16 v[72:75], a[56:59], a[152:155], v[72:75]\n\t"
"ds_read_b128 v[140:143], v231 offset:33280\n\t"
"v_mfma_f32_16x16x32_bf16 v[72:75], a[60:63], a[168:171], v[72:75]\n\t"
"v_cvt_pk_bf16_f32 v208, v208, v209\n\t"
"v_cvt_pk_bf16_f32 v209, v210, v211\n\t"
"v_mfma_f32_16x16x32_bf16 v[212:215], a[204:207], a[220:223], 0\n\t"
"ds_read_b64_tr_b16 a[184:185], v250 offset:2304\n\t"
"v_mfma_f32_16x16x32_bf16 v[88:91], a[64:67], a[152:155], v[88:91]\n\t"
"ds_read_b64_tr_b16 a[186:187], v250 offset:2816\n\t"
"v_mfma_f32_16x16x32_bf16 v[88:91], a[68:71], a[168:171], v[88:91]\n\t"
"ds_read_b64_tr_b16 a[188:189], v251 offset:2304\n\t"
"ds_read_b64_tr_b16 a[190:191], v251 offset:2816\n\t"
"v_mfma_f32_16x16x32_bf16 v[92:95], a[64:67], a[156:159], v[92:95]\n\t"
"ds_read_b64_tr_b16 a[192:193], v250 offset:0\n\t"
"v_mfma_f32_16x16x32_bf16 v[92:95], a[68:71], a[172:175], v[92:95]\n\t"
"buffer_load_dwordx4 a[64:67], v218, s[84:87], s75 offen offset:0 sc1\n\t"
"buffer_load_dwordx4 a[68:71], v218, s[84:87], s75 offen offset:1024 sc1\n\t"
"v_mfma_f32_16x16x32_bf16 v[76:79], a[56:59], a[156:159], v[76:79]\n\t"
"ds_read_b64_tr_b16 a[194:195], v250 offset:512\n\t"
"v_cvt_pk_bf16_f32 v210, v212, v213\n\t"
"v_mfma_f32_16x16x32_bf16 v[76:79], a[60:63], a[172:175], v[76:79]\n\t"
"buffer_load_dwordx4 a[60:63], v235, s[84:87], s75 offen offset:3072 sc1\n\t"
"v_mfma_f32_16x16x32_bf16 v[60:63], a[48:51], a[156:159], v[60:63]\n\t"
"ds_read_b64_tr_b16 a[196:197], v251 offset:0\n\t"
"v_cvt_pk_bf16_f32 v211, v214, v215\n\t"
"v_mfma_f32_16x16x32_bf16 v[60:63], a[52:55], a[172:175], v[60:63]\n\t"
"ds_write_b128 v219, v[208:211] offset:8192\n\t"
"s_waitcnt vmcnt(31) lgkmcnt(8)\n\t"
"v_mfma_f32_16x16x32_bf16 v[144:147], a[72:75], a[144:147], v[144:147]\n\t"
"ds_read_b64_tr_b16 a[198:199], v251 offset:512\n\t"
"v_mfma_f32_16x16x32_bf16 v[144:147], a[76:79], a[160:163], v[144:147]\n\t"
"ds_read_b64_tr_b16 a[200:201], v248 offset:2304\n\t"
"ds_read_b64_tr_b16 a[202:203], v248 offset:2816\n\t"
"ds_read_b64_tr_b16 a[204:205], v249 offset:2304\n\t"
"ds_read_b64_tr_b16 a[206:207], v249 offset:2816\n\t"
"buffer_load_dwordx4 a[56:59], v235, s[84:87], s75 offen offset:2048 sc1\n\t"
"buffer_store_dwordx4 v[124:127], v233, s[16:19], s25 offen nt\n\t"
"v_mfma_f32_16x16x32_bf16 v[160:163], a[80:83], a[144:147], v[160:163]\n\t"
"v_mfma_f32_16x16x32_bf16 v[160:163], a[84:87], a[160:163], v[160:163]\n\t"
"buffer_load_dwordx4 a[48:51], v235, s[84:87], s75 offen offset:0 sc1\n\t"
"buffer_load_dwordx4 a[52:55], v235, s[84:87], s75 offen offset:1024 sc1\n\t"
"v_mfma_f32_16x16x32_bf16 v[176:179], a[88:91], a[144:147], v[176:179]\n\t"
"v_mfma_f32_16x16x32_bf16 v[176:179], a[92:95], a[160:163], v[176:179]\n\t"
"ds_read_b128 a[152:155], v216 offset:22528\n\t"
"v_mfma_f32_16x16x32_bf16 v[180:183], a[88:91], a[148:151], v[180:183]\n\t"
"ds_read_b128 a[168:171], v216 offset:30720\n\t"
"v_mfma_f32_16x16x32_bf16 v[180:183], a[92:95], a[164:167], v[180:183]\n\t"
"ds_read_b128 a[156:159], v216 offset:23552\n\t"
"ds_read_b128 a[172:175], v216 offset:31744\n\t"
"v_mfma_f32_16x16x32_bf16 v[164:167], a[80:83], a[148:151], v[164:167]\n\t"
"v_mfma_f32_16x16x32_bf16 v[164:167], a[84:87], a[164:167], v[164:167]\n\t"
"buffer_store_dwordx4 v[140:143], v234, s[16:19], s25 offen nt\n\t"
"v_mfma_f32_16x16x32_bf16 v[148:151], a[72:75], a[148:151], v[148:151]\n\t"
"v_mfma_f32_16x16x32_bf16 v[148:151], a[76:79], a[164:167], v[148:151]\n\t"
"s_waitcnt lgkmcnt(0)\n\t"
"s_barrier\n\t"
"v_mfma_f32_16x16x32_bf16 v[152:155], a[72:75], a[152:155], v[152:155]\n\t"
"ds_read_b128 a[144:147], v216 offset:0\n\t"
"v_add_u32 v248, 53504, v226\n\t"
"v_mfma_f32_16x16x32_bf16 v[152:155], a[76:79], a[168:171], v[152:155]\n\t"
"ds_read_b128 a[160:163], v216 offset:8192\n\t"
"ds_read_b128 a[148:151], v216 offset:1024\n\t"
"v_mfma_f32_16x16x32_bf16 v[168:171], a[80:83], a[152:155], v[168:171]\n\t"
"ds_read_b128 a[164:167], v216 offset:9216\n\t"
"v_xor_b32 v249, 32, v248\n\t"
"v_mfma_f32_16x16x32_bf16 v[168:171], a[84:87], a[168:171], v[168:171]\n\t"
"v_xor_b32 v250, 64, v248\n\t"
"v_xor_b32 v251, 96, v248\n\t"
"v_mfma_f32_16x16x32_bf16 v[184:187], a[88:91], a[152:155], v[184:187]\n\t"
"ds_read_b128 a[152:155], v216 offset:2048\n\t"
"v_mfma_f32_16x16x32_bf16 v[184:187], a[92:95], a[168:171], v[184:187]\n\t"
"ds_read_b128 a[168:171], v216 offset:10240\n\t"
"v_mfma_f32_16x16x32_bf16 v[192:195], a[176:179], a[220:223], 0\n\t"
"ds_read_b64_tr_b16 a[176:177], v248 offset:0\n\t"
"v_mfma_f32_16x16x32_bf16 v[188:191], a[88:91], a[156:159], v[188:191]\n\t"
"ds_read_b64_tr_b16 a[178:179], v248 offset:512\n\t"
"v_mfma_f32_16x16x32_bf16 v[188:191], a[92:95], a[172:175], v[188:191]\n\t"
"buffer_load_dwordx4 a[88:91], v218, s[84:87], s78 offen offset:0 sc1\n\t"
"v_mfma_f32_16x16x32_bf16 v[196:199], a[180:183], a[220:223], 0\n\t"
"buffer_load_dwordx4 a[92:95], v218, s[84:87], s78 offen offset:1024 sc1\n\t"
"v_mfma_f32_16x16x32_bf16 v[172:175], a[80:83], a[156:159], v[172:175]\n\t"
"v_cvt_pk_bf16_f32 v192, v192, v193\n\t"
"v_cvt_pk_bf16_f32 v193, v194, v195\n\t"
"v_mfma_f32_16x16x32_bf16 v[172:175], a[84:87], a[172:175], v[172:175]\n\t"
"buffer_load_dwordx4 a[84:87], v235, s[84:87], s78 offen offset:3072 sc1\n\t"
"v_mfma_f32_16x16x32_bf16 v[156:159], a[72:75], a[156:159], v[156:159]\n\t"
"ds_read_b128 a[156:159], v216 offset:3072\n\t"
"v_mfma_f32_16x16x32_bf16 v[156:159], a[76:79], a[172:175], v[156:159]\n\t"
"buffer_load_dwordx4 a[80:83], v235, s[84:87], s78 offen offset:2048 sc1\n\t"
"v_mfma_f32_16x16x32_bf16 v[200:203], a[184:187], a[220:223], 0\n\t"
"v_pk_add_f32 v[144:145], v[48:49], v[144:145] neg_lo:[0,1] neg_hi:[0,1]\n\t"
"v_pk_add_f32 v[146:147], v[50:51], v[146:147] neg_lo:[0,1] neg_hi:[0,1]\n\t"
"v_pk_fma_f32 v[48:49], v[144:145], s[90:91], v[48:49] neg_lo:[1,0,0] neg_hi:[1,0,0]\n\t"
"v_pk_fma_f32 v[50:51], v[146:147], s[90:91], v[50:51] neg_lo:[1,0,0] neg_hi:[1,0,0]\n\t"
"v_pk_add_f32 v[160:161], v[64:65], v[160:161] neg_lo:[0,1] neg_hi:[0,1]\n\t"
"v_pk_add_f32 v[162:163], v[66:67], v[162:163] neg_lo:[0,1] neg_hi:[0,1]\n\t"
"v_pk_fma_f32 v[64:65], v[160:161], s[90:91], v[64:65] neg_lo:[1,0,0] neg_hi:[1,0,0]\n\t"
"v_pk_fma_f32 v[66:67], v[162:163], s[90:91], v[66:67] neg_lo:[1,0,0] neg_hi:[1,0,0]\n\t"
"v_pk_add_f32 v[176:177], v[80:81], v[176:177] neg_lo:[0,1] neg_hi:[0,1]\n\t"
"v_pk_add_f32 v[178:179], v[82:83], v[178:179] neg_lo:[0,1] neg_hi:[0,1]\n\t"
"v_pk_fma_f32 v[80:81], v[176:177], s[90:91], v[80:81] neg_lo:[1,0,0] neg_hi:[1,0,0]\n\t"
"v_pk_fma_f32 v[82:83], v[178:179], s[90:91], v[82:83] neg_lo:[1,0,0] neg_hi:[1,0,0]\n\t"
"v_pk_add_f32 v[180:181], v[84:85], v[180:181] neg_lo:[0,1] neg_hi:[0,1]\n\t"
"v_pk_add_f32 v[182:183], v[86:87], v[182:183] neg_lo:[0,1] neg_hi:[0,1]\n\t"
"v_pk_fma_f32 v[84:85], v[180:181], s[90:91], v[84:85] neg_lo:[1,0,0] neg_hi:[1,0,0]\n\t"
"v_pk_fma_f32 v[86:87], v[182:183], s[90:91], v[86:87] neg_lo:[1,0,0] neg_hi:[1,0,0]\n\t"
"v_pk_add_f32 v[164:165], v[68:69], v[164:165] neg_lo:[0,1] neg_hi:[0,1]\n\t"
"v_pk_add_f32 v[166:167], v[70:71], v[166:167] neg_lo:[0,1] neg_hi:[0,1]\n\t"
"v_pk_fma_f32 v[68:69], v[164:165], s[90:91], v[68:69] neg_lo:[1,0,0] neg_hi:[1,0,0]\n\t"
"v_pk_fma_f32 v[70:71], v[166:167], s[90:91], v[70:71] neg_lo:[1,0,0] neg_hi:[1,0,0]\n\t"
"v_pk_add_f32 v[148:149], v[52:53], v[148:149] neg_lo:[0,1] neg_hi:[0,1]\n\t"
"v_pk_add_f32 v[150:151], v[54:55], v[150:151] neg_lo:[0,1] neg_hi:[0,1]\n\t"
"v_pk_fma_f32 v[52:53], v[148:149], s[90:91], v[52:53] neg_lo:[1,0,0] neg_hi:[1,0,0]\n\t"
"v_pk_fma_f32 v[54:55], v[150:151], s[90:91], v[54:55] neg_lo:[1,0,0] neg_hi:[1,0,0]\n\t"
"v_pk_add_f32 v[152:153], v[56:57], v[152:153] neg_lo:[0,1] neg_hi:[0,1]\n\t"
"v_pk_add_f32 v[154:155], v[58:59], v[154:155] neg_lo:[0,1] neg_hi:[0,1]\n\t"
"v_pk_fma_f32 v[56:57], v[152:153], s[90:91], v[56:57] neg_lo:[1,0,0] neg_hi:[1,0,0]\n\t"
"v_pk_fma_f32 v[58:59], v[154:155], s[90:91], v[58:59] neg_lo:[1,0,0] neg_hi:[1,0,0]\n\t"
"v_pk_add_f32 v[168:169], v[72:73], v[168:169] neg_lo:[0,1] neg_hi:[0,1]\n\t"
"v_pk_add_f32 v[170:171], v[74:75], v[170:171] neg_lo:[0,1] neg_hi:[0,1]\n\t"
"v_pk_fma_f32 v[72:73], v[168:169], s[90:91], v[72:73] neg_lo:[1,0,0] neg_hi:[1,0,0]\n\t"
"v_pk_fma_f32 v[74:75], v[170:171], s[90:91], v[74:75] neg_lo:[1,0,0] neg_hi:[1,0,0]\n\t"
"v_pk_add_f32 v[184:185], v[88:89], v[184:185] neg_lo:[0,1] neg_hi:[0,1]\n\t"
"v_pk_add_f32 v[186:187], v[90:91], v[186:187] neg_lo:[0,1] neg_hi:[0,1]\n\t"
"v_pk_fma_f32 v[88:89], v[184:185], s[90:91], v[88:89] neg_lo:[1,0,0] neg_hi:[1,0,0]\n\t"
"v_pk_fma_f32 v[90:91], v[186:187], s[90:91], v[90:91] neg_lo:[1,0,0] neg_hi:[1,0,0]\n\t"
"v_pk_add_f32 v[188:189], v[92:93], v[188:189] neg_lo:[0,1] neg_hi:[0,1]\n\t"
"v_pk_add_f32 v[190:191], v[94:95], v[190:191] neg_lo:[0,1] neg_hi:[0,1]\n\t"
"v_pk_fma_f32 v[92:93], v[188:189], s[90:91], v[92:93] neg_lo:[1,0,0] neg_hi:[1,0,0]\n\t"
"v_pk_fma_f32 v[94:95], v[190:191], s[90:91], v[94:95] neg_lo:[1,0,0] neg_hi:[1,0,0]\n\t"
"v_pk_add_f32 v[172:173], v[76:77], v[172:173] neg_lo:[0,1] neg_hi:[0,1]\n\t"
"v_pk_add_f32 v[174:175], v[78:79], v[174:175] neg_lo:[0,1] neg_hi:[0,1]\n\t"
"v_pk_fma_f32 v[76:77], v[172:173], s[90:91], v[76:77] neg_lo:[1,0,0] neg_hi:[1,0,0]\n\t"
"v_pk_fma_f32 v[78:79], v[174:175], s[90:91], v[78:79] neg_lo:[1,0,0] neg_hi:[1,0,0]\n\t"
"v_pk_add_f32 v[156:157], v[60:61], v[156:157] neg_lo:[0,1] neg_hi:[0,1]\n\t"
"v_pk_add_f32 v[158:159], v[62:63], v[158:159] neg_lo:[0,1] neg_hi:[0,1]\n\t"
"v_pk_fma_f32 v[60:61], v[156:157], s[90:91], v[60:61] neg_lo:[1,0,0] neg_hi:[1,0,0]\n\t"
"v_pk_fma_f32 v[62:63], v[158:159], s[90:91], v[62:63] neg_lo:[1,0,0] neg_hi:[1,0,0]\n\t"
"buffer_load_dwordx4 a[72:75], v235, s[84:87], s78 offen offset:0 sc1\n\t"
"s_waitcnt vmcnt(35) lgkmcnt(4)\n\t"
"v_mfma_f32_16x16x32_bf16 v[48:51], a[96:99], a[144:147], v[48:51]\n\t"
"v_cvt_pk_bf16_f32 v194, v196, v197\n\t"
"v_cvt_pk_bf16_f32 v195, v198, v199\n\t"
"v_mfma_f32_16x16x32_bf16 v[48:51], a[100:103], a[160:163], v[48:51]\n\t"
"ds_read_b128 a[172:175], v216 offset:11264\n\t"
"v_mfma_f32_16x16x32_bf16 v[204:207], a[188:191], a[220:223], 0\n\t"
"buffer_load_dwordx4 a[76:79], v235, s[84:87], s78 offen offset:1024 sc1\n\t"
"v_mfma_f32_16x16x32_bf16 v[64:67], a[104:107], a[144:147], v[64:67]\n\t"
"v_cvt_pk_bf16_f32 v200, v200, v201\n\t"
"v_cvt_pk_bf16_f32 v201, v202, v203\n\t"
"v_mfma_f32_16x16x32_bf16 v[64:67], a[108:111], a[160:163], v[64:67]\n\t"
"ds_read_b64_tr_b16 a[180:181], v249 offset:0\n\t"
"v_mfma_f32_16x16x32_bf16 v[80:83], a[112:115], a[144:147], v[80:83]\n\t"
"ds_read_b128 a[144:147], v216 offset:4096\n\t"
"v_mfma_f32_16x16x32_bf16 v[80:83], a[116:119], a[160:163], v[80:83]\n\t"
"ds_write_b128 v217, v[192:195] offset:16384\n\t"
"v_mfma_f32_16x16x32_bf16 v[208:211], a[192:195], a[220:223], 0\n\t"
"v_cvt_pk_bf16_f32 v202, v204, v205\n\t"
"v_cvt_pk_bf16_f32 v203, v206, v207\n\t"
"v_mfma_f32_16x16x32_bf16 v[84:87], a[112:115], a[148:151], v[84:87]\n\t"
"ds_read_b128 a[160:163], v216 offset:12288\n\t"
"v_mfma_f32_16x16x32_bf16 v[84:87], a[116:119], a[164:167], v[84:87]\n\t"
"ds_write_b128 v219, v[200:203] offset:16384\n\t"
"v_mfma_f32_16x16x32_bf16 v[68:71], a[104:107], a[148:151], v[68:71]\n\t"
"ds_read_b64_tr_b16 a[182:183], v249 offset:512\n\t"
"v_mfma_f32_16x16x32_bf16 v[68:71], a[108:111], a[164:167], v[68:71]\n\t"
"v_cvt_pk_bf16_f32 v208, v208, v209\n\t"
"v_cvt_pk_bf16_f32 v209, v210, v211\n\t"
"v_mfma_f32_16x16x32_bf16 v[212:215], a[196:199], a[220:223], 0\n\t"
"ds_read_b64_tr_b16 a[184:185], v250 offset:2304\n\t"
"v_mfma_f32_16x16x32_bf16 v[52:55], a[96:99], a[148:151], v[52:55]\n\t"
"ds_read_b128 a[148:151], v216 offset:5120\n\t"
"v_mfma_f32_16x16x32_bf16 v[52:55], a[100:103], a[164:167], v[52:55]\n\t"
"ds_read_b128 a[164:167], v216 offset:13312\n\t"
"v_mfma_f32_16x16x32_bf16 v[192:195], a[200:203], a[220:223], 0\n\t"
"ds_read_b64_tr_b16 a[186:187], v250 offset:2816\n\t"
"s_waitcnt lgkmcnt(8)\n\t"
"v_mfma_f32_16x16x32_bf16 v[56:59], a[96:99], a[152:155], v[56:59]\n\t"
"v_cvt_pk_bf16_f32 v210, v212, v213\n\t"
"v_cvt_pk_bf16_f32 v211, v214, v215\n\t"
"v_mfma_f32_16x16x32_bf16 v[56:59], a[100:103], a[168:171], v[56:59]\n\t"
"ds_write_b128 v217, v[208:211] offset:24576\n\t"
"v_mfma_f32_16x16x32_bf16 v[72:75], a[104:107], a[152:155], v[72:75]\n\t"
"ds_read_b64_tr_b16 a[188:189], v251 offset:2304\n\t"
"v_mfma_f32_16x16x32_bf16 v[72:75], a[108:111], a[168:171], v[72:75]\n\t"
"v_cvt_pk_bf16_f32 v192, v192, v193\n\t"
"v_cvt_pk_bf16_f32 v193, v194, v195\n\t"
"v_mfma_f32_16x16x32_bf16 v[196:199], a[204:207], a[220:223], 0\n\t"
"ds_read_b64_tr_b16 a[190:191], v251 offset:2816\n\t"
"v_mfma_f32_16x16x32_bf16 v[88:91], a[112:115], a[152:155], v[88:91]\n\t"
"ds_read_b64_tr_b16 a[192:193], v250 offset:0\n\t"
"v_mfma_f32_16x16x32_bf16 v[88:91], a[116:119], a[168:171], v[88:91]\n\t"
"ds_read_b64_tr_b16 a[194:195], v250 offset:512\n\t"
"v_mfma_f32_16x16x32_bf16 v[92:95], a[112:115], a[156:159], v[92:95]\n\t"
"ds_read_b64_tr_b16 a[196:197], v251 offset:0\n\t"
"v_mfma_f32_16x16x32_bf16 v[92:95], a[116:119], a[172:175], v[92:95]\n\t"
"buffer_load_dwordx4 a[112:115], v218, s[84:87], 0 offen offset:0 sc1\n\t"
"v_mfma_f32_16x16x32_bf16 v[76:79], a[104:107], a[156:159], v[76:79]\n\t"
"ds_read_b64_tr_b16 a[198:199], v251 offset:512\n\t"
"v_cvt_pk_bf16_f32 v194, v196, v197\n\t"
"v_mfma_f32_16x16x32_bf16 v[76:79], a[108:111], a[172:175], v[76:79]\n\t"
"buffer_load_dwordx4 a[116:119], v218, s[84:87], 0 offen offset:1024 sc1\n\t"
"v_mfma_f32_16x16x32_bf16 v[60:63], a[96:99], a[156:159], v[60:63]\n\t"
"ds_read_b64_tr_b16 a[200:201], v248 offset:2304\n\t"
"v_cvt_pk_bf16_f32 v195, v198, v199\n\t"
"v_mfma_f32_16x16x32_bf16 v[60:63], a[100:103], a[172:175], v[60:63]\n\t"
"buffer_load_dwordx4 a[108:111], v235, s[84:87], 0 offen offset:3072 sc1\n\t"
"s_waitcnt vmcnt(33) lgkmcnt(6)\n\t"
"v_mfma_f32_16x16x32_bf16 v[144:147], a[120:123], a[144:147], v[144:147]\n\t"
"ds_read_b64_tr_b16 a[202:203], v248 offset:2816\n\t"
"v_mfma_f32_16x16x32_bf16 v[144:147], a[124:127], a[160:163], v[144:147]\n\t"
"ds_write_b128 v219, v[192:195] offset:24576\n\t"
"ds_read_b64_tr_b16 a[204:205], v249 offset:2304\n\t"
"ds_read_b64_tr_b16 a[206:207], v249 offset:2816\n\t"
"buffer_load_dwordx4 a[104:107], v235, s[84:87], 0 offen offset:2048 sc1\n\t"
"v_mfma_f32_16x16x32_bf16 v[160:163], a[128:131], a[144:147], v[160:163]\n\t"
"v_mfma_f32_16x16x32_bf16 v[160:163], a[132:135], a[160:163], v[160:163]\n\t"
"buffer_load_dwordx4 a[96:99], v235, s[84:87], 0 offen offset:0 sc1\n\t"
"buffer_load_dwordx4 a[100:103], v235, s[84:87], 0 offen offset:1024 sc1\n\t"
"v_mfma_f32_16x16x32_bf16 v[176:179], a[136:139], a[144:147], v[176:179]\n\t"
"v_mfma_f32_16x16x32_bf16 v[176:179], a[140:143], a[160:163], v[176:179]\n\t"
"ds_read_b128 a[152:155], v216 offset:6144\n\t"
"v_mfma_f32_16x16x32_bf16 v[180:183], a[136:139], a[148:151], v[180:183]\n\t"
"ds_read_b128 a[168:171], v216 offset:14336\n\t"
"v_mfma_f32_16x16x32_bf16 v[180:183], a[140:143], a[164:167], v[180:183]\n\t"
"ds_read_b128 a[156:159], v216 offset:7168\n\t"
"ds_read_b128 a[172:175], v216 offset:15360\n\t"
"v_mfma_f32_16x16x32_bf16 v[164:167], a[128:131], a[148:151], v[164:167]\n\t"
"v_mfma_f32_16x16x32_bf16 v[164:167], a[132:135], a[164:167], v[164:167]\n\t"
"v_mfma_f32_16x16x32_bf16 v[148:151], a[120:123], a[148:151], v[148:151]\n\t"
"v_mfma_f32_16x16x32_bf16 v[148:151], a[124:127], a[164:167], v[148:151]\n\t"
"s_waitcnt lgkmcnt(0)\n\t"
"s_barrier\n\t"
"v_mfma_f32_16x16x32_bf16 v[152:155], a[120:123], a[152:155], v[152:155]\n\t"
"ds_read_b128 a[144:147], v216 offset:16384\n\t"
"v_add_u32 v248, 101632, v227\n\t"
"v_mfma_f32_16x16x32_bf16 v[152:155], a[124:127], a[168:171], v[152:155]\n\t"
"ds_read_b128 a[160:163], v216 offset:24576\n\t"
"ds_read_b128 a[148:151], v216 offset:17408\n\t"
"v_mfma_f32_16x16x32_bf16 v[168:171], a[128:131], a[152:155], v[168:171]\n\t"
"ds_read_b128 a[164:167], v216 offset:25600\n\t"
"v_xor_b32 v249, 32, v248\n\t"
"v_mfma_f32_16x16x32_bf16 v[168:171], a[132:135], a[168:171], v[168:171]\n\t"
"v_xor_b32 v250, 64, v248\n\t"
"s_xor_b32 s24, s24, 384\n\t"
"s_xor_b32 s20, s20, 384\n\t"
"s_xor_b32 s27, s27, 384\n\t"
"s_xor_b32 s25, s25, 384\n\t"
"v_mfma_f32_16x16x32_bf16 v[184:187], a[136:139], a[152:155], v[184:187]\n\t"
"v_xor_b32 v251, 96, v248\n\t"
"v_mfma_f32_16x16x32_bf16 v[184:187], a[140:143], a[168:171], v[184:187]\n\t"
"ds_read_b128 a[152:155], v216 offset:18432\n\t"
"v_mfma_f32_16x16x32_bf16 v[200:203], a[176:179], a[220:223], 0\n\t"
"ds_read_b128 a[168:171], v216 offset:26624\n\t"
"v_mfma_f32_16x16x32_bf16 v[188:191], a[136:139], a[156:159], v[188:191]\n\t"
"ds_read_b64_tr_b16 a[176:177], v248 offset:0\n\t"
"v_mfma_f32_16x16x32_bf16 v[188:191], a[140:143], a[172:175], v[188:191]\n\t"
"buffer_load_dwordx4 a[136:139], v218, s[84:87], s51 offen offset:0 sc1\n\t"
"v_mfma_f32_16x16x32_bf16 v[204:207], a[180:183], a[220:223], 0\n\t"
"buffer_load_dwordx4 a[140:143], v218, s[84:87], s51 offen offset:1024 sc1\n\t"
"v_mfma_f32_16x16x32_bf16 v[172:175], a[128:131], a[156:159], v[172:175]\n\t"
"v_cvt_pk_bf16_f32 v200, v200, v201\n\t"
"v_mfma_f32_16x16x32_bf16 v[172:175], a[132:135], a[172:175], v[172:175]\n\t"
"buffer_load_dwordx4 a[132:135], v235, s[84:87], s51 offen offset:3072 sc1\n\t"
"v_mfma_f32_16x16x32_bf16 v[156:159], a[120:123], a[156:159], v[156:159]\n\t"
"v_cvt_pk_bf16_f32 v201, v202, v203\n\t"
"v_mfma_f32_16x16x32_bf16 v[156:159], a[124:127], a[172:175], v[156:159]\n\t"
"buffer_load_dwordx4 a[128:131], v235, s[84:87], s51 offen offset:2048 sc1\n\t"
"v_mfma_f32_16x16x32_bf16 v[208:211], a[184:187], a[220:223], 0\n\t"
"v_cvt_pk_bf16_f32 v202, v204, v205\n\t"
"s_waitcnt vmcnt(34) lgkmcnt(3)\n\t"
"v_mfma_f32_16x16x32_bf16 v[48:51], a[0:3], a[144:147], v[48:51]\n\t"
"v_cvt_pk_bf16_f32 v203, v206, v207\n\t"
"v_mfma_f32_16x16x32_bf16 v[48:51], a[4:7], a[160:163], v[48:51]\n\t"
"buffer_load_dwordx4 a[120:123], v235, s[84:87], s51 offen offset:0 sc1\n\t"
"v_mfma_f32_16x16x32_bf16 v[212:215], a[188:191], a[220:223], 0\n\t"
"buffer_load_dwordx4 a[124:127], v235, s[84:87], s51 offen offset:1024 sc1\n\t"
"v_mfma_f32_16x16x32_bf16 v[64:67], a[8:11], a[144:147], v[64:67]\n\t"
"v_mfma_f32_16x16x32_bf16 v[64:67], a[12:15], a[160:163], v[64:67]\n\t"
"ds_read_b128 a[156:159], v216 offset:19456\n\t"
"v_cvt_pk_bf16_f32 v208, v208, v209\n\t"
"v_mfma_f32_16x16x32_bf16 v[80:83], a[16:19], a[144:147], v[80:83]\n\t"
"ds_read_b128 a[172:175], v216 offset:27648\n\t"
"v_mfma_f32_16x16x32_bf16 v[80:83], a[20:23], a[160:163], v[80:83]\n\t"
"v_cvt_pk_bf16_f32 v209, v210, v211\n\t"
"v_mfma_f32_16x16x32_bf16 v[192:195], a[192:195], a[220:223], 0\n\t"
"ds_write_b128 v217, v[200:203] offset:0\n\t"
"v_mfma_f32_16x16x32_bf16 v[84:87], a[16:19], a[148:151], v[84:87]\n\t"
"v_cvt_pk_bf16_f32 v210, v212, v213\n\t"
"v_mfma_f32_16x16x32_bf16 v[84:87], a[20:23], a[164:167], v[84:87]\n\t"
"v_cvt_pk_bf16_f32 v211, v214, v215\n\t"
"v_mfma_f32_16x16x32_bf16 v[68:71], a[8:11], a[148:151], v[68:71]\n\t"
"ds_read_b128 a[144:147], v216 offset:20480\n\t"
"v_mfma_f32_16x16x32_bf16 v[68:71], a[12:15], a[164:167], v[68:71]\n\t"
"ds_read_b128 a[160:163], v216 offset:28672\n\t"
"v_cvt_pk_bf16_f32 v192, v192, v193\n\t"
"v_mfma_f32_16x16x32_bf16 v[196:199], a[196:199], a[220:223], 0\n\t"
"ds_write_b128 v219, v[208:211] offset:0\n\t"
"v_mfma_f32_16x16x32_bf16 v[52:55], a[0:3], a[148:151], v[52:55]\n\t"
"v_cvt_pk_bf16_f32 v193, v194, v195\n\t"
"v_mfma_f32_16x16x32_bf16 v[52:55], a[4:7], a[164:167], v[52:55]\n\t"
"ds_read_b128 a[148:151], v216 offset:21504\n\t"
"v_mfma_f32_16x16x32_bf16 v[200:203], a[200:203], a[220:223], 0\n\t"
"ds_read_b128 a[164:167], v216 offset:29696\n\t"
"s_waitcnt lgkmcnt(6)\n\t"
"v_mfma_f32_16x16x32_bf16 v[56:59], a[0:3], a[152:155], v[56:59]\n\t"
"v_cvt_pk_bf16_f32 v194, v196, v197\n\t"
"v_cvt_pk_bf16_f32 v195, v198, v199\n\t"
"v_mfma_f32_16x16x32_bf16 v[56:59], a[4:7], a[168:171], v[56:59]\n\t"
"ds_read_b64_tr_b16 a[178:179], v248 offset:512\n\t"
"v_mfma_f32_16x16x32_bf16 v[72:75], a[8:11], a[152:155], v[72:75]\n\t"
"ds_read_b64_tr_b16 a[180:181], v249 offset:0\n\t"
"v_mfma_f32_16x16x32_bf16 v[72:75], a[12:15], a[168:171], v[72:75]\n\t"
"v_cvt_pk_bf16_f32 v200, v200, v201\n\t"
"v_cvt_pk_bf16_f32 v201, v202, v203\n\t"
"v_mfma_f32_16x16x32_bf16 v[204:207], a[204:207], a[220:223], 0\n\t"
"ds_read_b64_tr_b16 a[182:183], v249 offset:512\n\t"
"v_mfma_f32_16x16x32_bf16 v[88:91], a[16:19], a[152:155], v[88:91]\n\t"
"ds_read_b64_tr_b16 a[184:185], v250 offset:0\n\t"
"v_mfma_f32_16x16x32_bf16 v[88:91], a[20:23], a[168:171], v[88:91]\n\t"
"ds_write_b128 v217, v[192:195] offset:8192\n\t"
"v_mfma_f32_16x16x32_bf16 v[92:95], a[16:19], a[156:159], v[92:95]\n\t"
"ds_read_b64_tr_b16 a[186:187], v250 offset:512\n\t"
"v_mfma_f32_16x16x32_bf16 v[92:95], a[20:23], a[172:175], v[92:95]\n\t"
"ds_read_b64_tr_b16 a[188:189], v251 offset:0\n\t"
"v_cvt_pk_bf16_f32 v202, v204, v205\n\t"
"v_mfma_f32_16x16x32_bf16 v[76:79], a[8:11], a[156:159], v[76:79]\n\t"
"ds_read_b64_tr_b16 a[190:191], v251 offset:512\n\t"
"v_mfma_f32_16x16x32_bf16 v[76:79], a[12:15], a[172:175], v[76:79]\n\t"
"ds_read_b64_tr_b16 a[192:193], v248 offset:1152\n\t"
"v_cvt_pk_bf16_f32 v203, v206, v207\n\t"
"v_mfma_f32_16x16x32_bf16 v[60:63], a[0:3], a[156:159], v[60:63]\n\t"
"ds_read_b64_tr_b16 a[194:195], v248 offset:1664\n\t"
"v_mfma_f32_16x16x32_bf16 v[60:63], a[4:7], a[172:175], v[60:63]\n\t"
"ds_read_b64_tr_b16 a[196:197], v249 offset:1152\n\t"
"s_waitcnt vmcnt(29) lgkmcnt(9)\n\t"
"v_mfma_f32_16x16x32_bf16 v[144:147], a[24:27], a[144:147], v[144:147]\n\t"
"ds_read_b64_tr_b16 a[198:199], v249 offset:1664\n\t"
"v_mfma_f32_16x16x32_bf16 v[144:147], a[28:31], a[160:163], v[144:147]\n\t"
"ds_write_b128 v219, v[200:203] offset:8192\n\t"
"ds_read_b64_tr_b16 a[200:201], v250 offset:1152\n\t"
"ds_read_b64_tr_b16 a[202:203], v250 offset:1664\n\t"
"ds_read_b64_tr_b16 a[204:205], v251 offset:1152\n\t"
"ds_read_b64_tr_b16 a[206:207], v251 offset:1664\n\t"
"v_mfma_f32_16x16x32_bf16 v[160:163], a[32:35], a[144:147], v[160:163]\n\t"
"v_mfma_f32_16x16x32_bf16 v[160:163], a[36:39], a[160:163], v[160:163]\n\t"
"v_mfma_f32_16x16x32_bf16 v[176:179], a[40:43], a[144:147], v[176:179]\n\t"
"v_mfma_f32_16x16x32_bf16 v[176:179], a[44:47], a[160:163], v[176:179]\n\t"
"ds_read_b128 a[152:155], v216 offset:22528\n\t"
"v_mfma_f32_16x16x32_bf16 v[180:183], a[40:43], a[148:151], v[180:183]\n\t"
"ds_read_b128 a[168:171], v216 offset:30720\n\t"
"v_mfma_f32_16x16x32_bf16 v[180:183], a[44:47], a[164:167], v[180:183]\n\t"
"ds_read_b128 a[156:159], v216 offset:23552\n\t"
"ds_read_b128 a[172:175], v216 offset:31744\n\t"
"v_mfma_f32_16x16x32_bf16 v[164:167], a[32:35], a[148:151], v[164:167]\n\t"
"v_mfma_f32_16x16x32_bf16 v[164:167], a[36:39], a[164:167], v[164:167]\n\t"
"v_mfma_f32_16x16x32_bf16 v[148:151], a[24:27], a[148:151], v[148:151]\n\t"
"v_mfma_f32_16x16x32_bf16 v[148:151], a[28:31], a[164:167], v[148:151]\n\t"
"s_waitcnt lgkmcnt(0)\n\t"
"s_barrier\n\t"
"v_mfma_f32_16x16x32_bf16 v[152:155], a[24:27], a[152:155], v[152:155]\n\t"
"ds_read_b128 a[144:147], v216 offset:0\n\t"
"v_mfma_f32_16x16x32_bf16 v[152:155], a[28:31], a[168:171], v[152:155]\n\t"
"ds_read_b128 a[160:163], v216 offset:8192\n\t"
"ds_read_b128 a[148:151], v216 offset:1024\n\t"
"v_mfma_f32_16x16x32_bf16 v[168:171], a[32:35], a[152:155], v[168:171]\n\t"
"ds_read_b128 a[164:167], v216 offset:9216\n\t"
"v_mfma_f32_16x16x32_bf16 v[168:171], a[36:39], a[168:171], v[168:171]\n\t"
"v_mfma_f32_16x16x32_bf16 v[184:187], a[40:43], a[152:155], v[184:187]\n\t"
"ds_read_b128 a[152:155], v216 offset:2048\n\t"
"v_mfma_f32_16x16x32_bf16 v[184:187], a[44:47], a[168:171], v[184:187]\n\t"
"ds_read_b128 a[168:171], v216 offset:10240\n\t"
"v_mfma_f32_16x16x32_bf16 v[208:211], a[176:179], a[224:227], 0\n\t"
"v_mfma_f32_16x16x32_bf16 v[188:191], a[40:43], a[156:159], v[188:191]\n\t"
"v_mfma_f32_16x16x32_bf16 v[188:191], a[44:47], a[172:175], v[188:191]\n\t"
"v_mfma_f32_16x16x32_bf16 v[212:215], a[180:183], a[224:227], 0\n\t"
"v_mfma_f32_16x16x32_bf16 v[172:175], a[32:35], a[156:159], v[172:175]\n\t"
"s_nop 3\n\t"
"v_cvt_pk_bf16_f32 v208, v208, v209\n\t"
"v_cvt_pk_bf16_f32 v209, v210, v211\n\t"
"v_mfma_f32_16x16x32_bf16 v[172:175], a[36:39], a[172:175], v[172:175]\n\t"
"v_mfma_f32_16x16x32_bf16 v[156:159], a[24:27], a[156:159], v[156:159]\n\t"
"ds_read_b128 a[156:159], v216 offset:3072\n\t"
"v_mfma_f32_16x16x32_bf16 v[156:159], a[28:31], a[172:175], v[156:159]\n\t"
"v_cvt_pk_bf16_f32 v210, v212, v213\n\t"
"v_cvt_pk_bf16_f32 v211, v214, v215\n\t"
"v_mfma_f32_16x16x32_bf16 v[192:195], a[184:187], a[224:227], 0\n\t"
"ds_read_b128 a[172:175], v216 offset:11264\n\t"
"s_waitcnt vmcnt(19) lgkmcnt(3)\n\t"
"v_mfma_f32_16x16x32_bf16 v[48:51], a[48:51], a[144:147], v[48:51]\n\t"
"v_mfma_f32_16x16x32_bf16 v[48:51], a[52:55], a[160:163], v[48:51]\n\t"
"ds_write_b128 v221, v[208:211] offset:16384\n\t"
"v_mfma_f32_16x16x32_bf16 v[196:199], a[188:191], a[224:227], 0\n\t"
"v_mfma_f32_16x16x32_bf16 v[64:67], a[56:59], a[144:147], v[64:67]\n\t"
"s_nop 0\n\t"
"v_cvt_pk_bf16_f32 v192, v192, v193\n\t"
"v_cvt_pk_bf16_f32 v193, v194, v195\n\t"
"v_mfma_f32_16x16x32_bf16 v[64:67], a[60:63], a[160:163], v[64:67]\n\t"
"v_mfma_f32_16x16x32_bf16 v[80:83], a[64:67], a[144:147], v[80:83]\n\t"
"ds_read_b128 a[144:147], v216 offset:4096\n\t"
"v_mfma_f32_16x16x32_bf16 v[80:83], a[68:71], a[160:163], v[80:83]\n\t"
"v_cvt_pk_bf16_f32 v194, v196, v197\n\t"
"v_mfma_f32_16x16x32_bf16 v[200:203], a[192:195], a[224:227], 0\n\t"
"v_cvt_pk_bf16_f32 v195, v198, v199\n\t"
"v_mfma_f32_16x16x32_bf16 v[84:87], a[64:67], a[148:151], v[84:87]\n\t"
"ds_read_b128 a[160:163], v216 offset:12288\n\t"
"v_mfma_f32_16x16x32_bf16 v[84:87], a[68:71], a[164:167], v[84:87]\n\t"
"v_mfma_f32_16x16x32_bf16 v[68:71], a[56:59], a[148:151], v[68:71]\n\t"
"v_mfma_f32_16x16x32_bf16 v[68:71], a[60:63], a[164:167], v[68:71]\n\t"
"ds_write_b128 v221, v[192:195] offset:24576\n\t"
"v_mfma_f32_16x16x32_bf16 v[204:207], a[196:199], a[224:227], 0\n\t"
"v_cvt_pk_bf16_f32 v200, v200, v201\n\t"
"v_mfma_f32_16x16x32_bf16 v[52:55], a[48:51], a[148:151], v[52:55]\n\t"
"v_mfma_f32_16x16x32_bf16 v[52:55], a[52:55], a[164:167], v[52:55]\n\t"
"v_cvt_pk_bf16_f32 v201, v202, v203\n\t"
"v_mfma_f32_16x16x32_bf16 v[208:211], a[200:203], a[224:227], 0\n\t"
"ds_read_b128 a[148:151], v216 offset:5120\n\t"
"s_waitcnt lgkmcnt(3)\n\t"
"v_mfma_f32_16x16x32_bf16 v[56:59], a[48:51], a[152:155], v[56:59]\n\t"
"ds_read_b128 a[164:167], v216 offset:13312\n\t"
"v_mfma_f32_16x16x32_bf16 v[56:59], a[52:55], a[168:171], v[56:59]\n\t"
"v_mfma_f32_16x16x32_bf16 v[72:75], a[56:59], a[152:155], v[72:75]\n\t"
"v_cvt_pk_bf16_f32 v202, v204, v205\n\t"
"v_mfma_f32_16x16x32_bf16 v[72:75], a[60:63], a[168:171], v[72:75]\n\t"
"v_cvt_pk_bf16_f32 v203, v206, v207\n\t"
"v_mfma_f32_16x16x32_bf16 v[212:215], a[204:207], a[224:227], 0\n\t"
"v_cvt_pk_bf16_f32 v208, v208, v209\n\t"
"v_mfma_f32_16x16x32_bf16 v[88:91], a[64:67], a[152:155], v[88:91]\n\t"
"v_mfma_f32_16x16x32_bf16 v[88:91], a[68:71], a[168:171], v[88:91]\n\t"
"v_cvt_pk_bf16_f32 v209, v210, v211\n\t"
"v_mfma_f32_16x16x32_bf16 v[92:95], a[64:67], a[156:159], v[92:95]\n\t"
"ds_read_b128 a[152:155], v216 offset:6144\n\t"
"v_mfma_f32_16x16x32_bf16 v[92:95], a[68:71], a[172:175], v[92:95]\n\t"
"s_nop 0\n\t"
"v_cvt_pk_bf16_f32 v210, v212, v213\n\t"
"v_mfma_f32_16x16x32_bf16 v[76:79], a[56:59], a[156:159], v[76:79]\n\t"
"v_mfma_f32_16x16x32_bf16 v[76:79], a[60:63], a[172:175], v[76:79]\n\t"
"ds_write_b128 v221, v[200:203] offset:20480\n\t"
"v_mfma_f32_16x16x32_bf16 v[60:63], a[48:51], a[156:159], v[60:63]\n\t"
"v_mfma_f32_16x16x32_bf16 v[60:63], a[52:55], a[172:175], v[60:63]\n\t"
"v_cvt_pk_bf16_f32 v211, v214, v215\n\t"
"s_waitcnt vmcnt(12) lgkmcnt(2)\n\t"
"v_mfma_f32_16x16x32_bf16 v[144:147], a[72:75], a[144:147], v[144:147]\n\t"
"v_mfma_f32_16x16x32_bf16 v[144:147], a[76:79], a[160:163], v[144:147]\n\t"
"ds_write_b128 v221, v[208:211] offset:28672\n\t"
"v_mfma_f32_16x16x32_bf16 v[160:163], a[80:83], a[144:147], v[160:163]\n\t"
"v_mfma_f32_16x16x32_bf16 v[160:163], a[84:87], a[160:163], v[160:163]\n\t"
"ds_read_b128 a[168:171], v216 offset:14336\n\t"
"v_mfma_f32_16x16x32_bf16 v[176:179], a[88:91], a[144:147], v[176:179]\n\t"
"v_mfma_f32_16x16x32_bf16 v[176:179], a[92:95], a[160:163], v[176:179]\n\t"
"ds_read_b128 a[156:159], v216 offset:7168\n\t"
"v_mfma_f32_16x16x32_bf16 v[180:183], a[88:91], a[148:151], v[180:183]\n\t"
"ds_read_b128 a[172:175], v216 offset:15360\n\t"
"v_mfma_f32_16x16x32_bf16 v[180:183], a[92:95], a[164:167], v[180:183]\n\t"
"v_mfma_f32_16x16x32_bf16 v[164:167], a[80:83], a[148:151], v[164:167]\n\t"
"v_mfma_f32_16x16x32_bf16 v[164:167], a[84:87], a[164:167], v[164:167]\n\t"
"v_mfma_f32_16x16x32_bf16 v[148:151], a[72:75], a[148:151], v[148:151]\n\t"
"v_mfma_f32_16x16x32_bf16 v[148:151], a[76:79], a[164:167], v[148:151]\n\t"
"s_waitcnt lgkmcnt(0)\n\t"
"s_barrier\n\t"
"v_mfma_f32_16x16x32_bf16 v[152:155], a[72:75], a[152:155], v[152:155]\n\t"
"ds_read_b128 a[144:147], v216 offset:16384\n\t"
"v_mfma_f32_16x16x32_bf16 v[152:155], a[76:79], a[168:171], v[152:155]\n\t"
"ds_read_b128 a[160:163], v216 offset:24576\n\t"
"ds_read_b128 a[148:151], v216 offset:17408\n\t"
"v_mfma_f32_16x16x32_bf16 v[168:171], a[80:83], a[152:155], v[168:171]\n\t"
"ds_read_b128 a[164:167], v216 offset:25600\n\t"
"v_mfma_f32_16x16x32_bf16 v[168:171], a[84:87], a[168:171], v[168:171]\n\t"
"v_mfma_f32_16x16x32_bf16 v[184:187], a[88:91], a[152:155], v[184:187]\n\t"
"ds_read_b128 a[152:155], v216 offset:18432\n\t"
"v_mfma_f32_16x16x32_bf16 v[184:187], a[92:95], a[168:171], v[184:187]\n\t"
"ds_read_b128 a[168:171], v216 offset:26624\n\t"
"v_mfma_f32_16x16x32_bf16 v[188:191], a[88:91], a[156:159], v[188:191]\n\t"
"v_mfma_f32_16x16x32_bf16 v[188:191], a[92:95], a[172:175], v[188:191]\n\t"
"v_mfma_f32_16x16x32_bf16 v[172:175], a[80:83], a[156:159], v[172:175]\n\t"
"v_mfma_f32_16x16x32_bf16 v[172:175], a[84:87], a[172:175], v[172:175]\n\t"
"v_mfma_f32_16x16x32_bf16 v[156:159], a[72:75], a[156:159], v[156:159]\n\t"
"ds_read_b128 a[156:159], v216 offset:19456\n\t"
"v_mfma_f32_16x16x32_bf16 v[156:159], a[76:79], a[172:175], v[156:159]\n\t"
"v_pk_fma_f32 v[48:49], v[144:145], s[90:91], v[48:49]\n\t"
"v_pk_fma_f32 v[50:51], v[146:147], s[90:91], v[50:51]\n\t"
"v_pk_add_f32 v[144:145], v[48:49], v[144:145] neg_lo:[0,1] neg_hi:[0,1]\n\t"
"v_pk_add_f32 v[146:147], v[50:51], v[146:147] neg_lo:[0,1] neg_hi:[0,1]\n\t"
"v_pk_fma_f32 v[64:65], v[160:161], s[90:91], v[64:65]\n\t"
"v_pk_fma_f32 v[66:67], v[162:163], s[90:91], v[66:67]\n\t"
"v_pk_add_f32 v[160:161], v[64:65], v[160:161] neg_lo:[0,1] neg_hi:[0,1]\n\t"
"v_pk_add_f32 v[162:163], v[66:67], v[162:163] neg_lo:[0,1] neg_hi:[0,1]\n\t"
"v_pk_fma_f32 v[80:81], v[176:177], s[90:91], v[80:81]\n\t"
"v_pk_fma_f32 v[82:83], v[178:179], s[90:91], v[82:83]\n\t"
"v_pk_add_f32 v[176:177], v[80:81], v[176:177] neg_lo:[0,1] neg_hi:[0,1]\n\t"
"v_pk_add_f32 v[178:179], v[82:83], v[178:179] neg_lo:[0,1] neg_hi:[0,1]\n\t"
"v_pk_fma_f32 v[84:85], v[180:181], s[90:91], v[84:85]\n\t"
"v_pk_fma_f32 v[86:87], v[182:183], s[90:91], v[86:87]\n\t"
"v_pk_add_f32 v[180:181], v[84:85], v[180:181] neg_lo:[0,1] neg_hi:[0,1]\n\t"
"v_pk_add_f32 v[182:183], v[86:87], v[182:183] neg_lo:[0,1] neg_hi:[0,1]\n\t"
"v_pk_fma_f32 v[68:69], v[164:165], s[90:91], v[68:69]\n\t"
"v_pk_fma_f32 v[70:71], v[166:167], s[90:91], v[70:71]\n\t"
"v_pk_add_f32 v[164:165], v[68:69], v[164:165] neg_lo:[0,1] neg_hi:[0,1]\n\t"
"v_pk_add_f32 v[166:167], v[70:71], v[166:167] neg_lo:[0,1] neg_hi:[0,1]\n\t"
"v_pk_fma_f32 v[52:53], v[148:149], s[90:91], v[52:53]\n\t"
"v_pk_fma_f32 v[54:55], v[150:151], s[90:91], v[54:55]\n\t"
"v_pk_add_f32 v[148:149], v[52:53], v[148:149] neg_lo:[0,1] neg_hi:[0,1]\n\t"
"v_pk_add_f32 v[150:151], v[54:55], v[150:151] neg_lo:[0,1] neg_hi:[0,1]\n\t"
"v_pk_fma_f32 v[56:57], v[152:153], s[90:91], v[56:57]\n\t"
"v_pk_fma_f32 v[58:59], v[154:155], s[90:91], v[58:59]\n\t"
"v_pk_add_f32 v[152:153], v[56:57], v[152:153] neg_lo:[0,1] neg_hi:[0,1]\n\t"
"v_pk_add_f32 v[154:155], v[58:59], v[154:155] neg_lo:[0,1] neg_hi:[0,1]\n\t"
"v_pk_fma_f32 v[72:73], v[168:169], s[90:91], v[72:73]\n\t"
"v_pk_fma_f32 v[74:75], v[170:171], s[90:91], v[74:75]\n\t"
"v_pk_add_f32 v[168:169], v[72:73], v[168:169] neg_lo:[0,1] neg_hi:[0,1]\n\t"
"v_pk_add_f32 v[170:171], v[74:75], v[170:171] neg_lo:[0,1] neg_hi:[0,1]\n\t"
"v_pk_fma_f32 v[88:89], v[184:185], s[90:91], v[88:89]\n\t"
"v_pk_fma_f32 v[90:91], v[186:187], s[90:91], v[90:91]\n\t"
"v_pk_add_f32 v[184:185], v[88:89], v[184:185] neg_lo:[0,1] neg_hi:[0,1]\n\t"
"v_pk_add_f32 v[186:187], v[90:91], v[186:187] neg_lo:[0,1] neg_hi:[0,1]\n\t"
"v_pk_fma_f32 v[92:93], v[188:189], s[90:91], v[92:93]\n\t"
"v_pk_fma_f32 v[94:95], v[190:191], s[90:91], v[94:95]\n\t"
"v_pk_add_f32 v[188:189], v[92:93], v[188:189] neg_lo:[0,1] neg_hi:[0,1]\n\t"
"v_pk_add_f32 v[190:191], v[94:95], v[190:191] neg_lo:[0,1] neg_hi:[0,1]\n\t"
"v_pk_fma_f32 v[76:77], v[172:173], s[90:91], v[76:77]\n\t"
"v_pk_fma_f32 v[78:79], v[174:175], s[90:91], v[78:79]\n\t"
"v_pk_add_f32 v[172:173], v[76:77], v[172:173] neg_lo:[0,1] neg_hi:[0,1]\n\t"
"v_pk_add_f32 v[174:175], v[78:79], v[174:175] neg_lo:[0,1] neg_hi:[0,1]\n\t"
"v_pk_fma_f32 v[60:61], v[156:157], s[90:91], v[60:61]\n\t"
"v_pk_fma_f32 v[62:63], v[158:159], s[90:91], v[62:63]\n\t"
"v_pk_add_f32 v[156:157], v[60:61], v[156:157] neg_lo:[0,1] neg_hi:[0,1]\n\t"
"v_pk_add_f32 v[158:159], v[62:63], v[158:159] neg_lo:[0,1] neg_hi:[0,1]\n\t"
"ds_read_b128 a[172:175], v216 offset:27648\n\t"
"s_waitcnt vmcnt(6) lgkmcnt(5)\n\t"
"v_mfma_f32_16x16x32_bf16 v[48:51], a[96:99], a[144:147], v[48:51]\n\t"
"v_mfma_f32_16x16x32_bf16 v[48:51], a[100:103], a[160:163], v[48:51]\n\t"
"v_mfma_f32_16x16x32_bf16 v[64:67], a[104:107], a[144:147], v[64:67]\n\t"
"v_mfma_f32_16x16x32_bf16 v[64:67], a[108:111], a[160:163], v[64:67]\n\t"
"v_mfma_f32_16x16x32_bf16 v[80:83], a[112:115], a[144:147], v[80:83]\n\t"
"ds_read_b128 a[144:147], v216 offset:20480\n\t"
"v_mfma_f32_16x16x32_bf16 v[80:83], a[116:119], a[160:163], v[80:83]\n\t"
"ds_read_b128 a[160:163], v216 offset:28672\n\t"
"s_waitcnt lgkmcnt(4)\n\t"
"v_mfma_f32_16x16x32_bf16 v[84:87], a[112:115], a[148:151], v[84:87]\n\t"
"v_mfma_f32_16x16x32_bf16 v[84:87], a[116:119], a[164:167], v[84:87]\n\t"
"v_mfma_f32_16x16x32_bf16 v[68:71], a[104:107], a[148:151], v[68:71]\n\t"
"v_mfma_f32_16x16x32_bf16 v[68:71], a[108:111], a[164:167], v[68:71]\n\t"
"v_cvt_pk_bf16_f32 v48, v48, v49\n\t"
"v_cvt_pk_bf16_f32 v49, v50, v51\n\t"
"v_cvt_pk_bf16_f32 v64, v64, v65\n\t"
"v_cvt_pk_bf16_f32 v65, v66, v67\n\t"
"v_mfma_f32_16x16x32_bf16 v[52:55], a[96:99], a[148:151], v[52:55]\n\t"
"ds_read_b128 a[148:151], v216 offset:21504\n\t"
"v_cvt_pk_bf16_f32 v80, v80, v81\n\t"
"v_mfma_f32_16x16x32_bf16 v[52:55], a[100:103], a[164:167], v[52:55]\n\t"
"ds_read_b128 a[164:167], v216 offset:29696\n\t"
"v_cvt_pk_bf16_f32 v81, v82, v83\n\t"
"v_mfma_f32_16x16x32_bf16 v[56:59], a[96:99], a[152:155], v[56:59]\n\t"
"v_mfma_f32_16x16x32_bf16 v[56:59], a[100:103], a[168:171], v[56:59]\n\t"
"v_mfma_f32_16x16x32_bf16 v[72:75], a[104:107], a[152:155], v[72:75]\n\t"
"v_mfma_f32_16x16x32_bf16 v[72:75], a[108:111], a[168:171], v[72:75]\n\t"
"s_nop 1\n\t"
"v_cvt_pk_bf16_f32 v52, v52, v53\n\t"
"v_cvt_pk_bf16_f32 v53, v54, v55\n\t"
"v_cvt_pk_bf16_f32 v68, v68, v69\n\t"
"v_cvt_pk_bf16_f32 v69, v70, v71\n\t"
"v_mfma_f32_16x16x32_bf16 v[88:91], a[112:115], a[152:155], v[88:91]\n\t"
"ds_read_b128 a[152:155], v216 offset:22528\n\t"
"v_cvt_pk_bf16_f32 v84, v84, v85\n\t"
"v_mfma_f32_16x16x32_bf16 v[88:91], a[116:119], a[168:171], v[88:91]\n\t"
"ds_read_b128 a[168:171], v216 offset:30720\n\t"
"v_cvt_pk_bf16_f32 v85, v86, v87\n\t"
"ds_write2st64_b64 v228, v[48:49], v[52:53] offset0:0 offset1:13\n\t"
"s_waitcnt lgkmcnt(5)\n\t"
"v_mfma_f32_16x16x32_bf16 v[92:95], a[112:115], a[156:159], v[92:95]\n\t"
"v_mfma_f32_16x16x32_bf16 v[92:95], a[116:119], a[172:175], v[92:95]\n\t"
"ds_write2st64_b64 v246, v[64:65], v[68:69] offset0:0 offset1:13\n\t"
"ds_write2st64_b64 v247, v[80:81], v[84:85] offset0:0 offset1:13\n\t"
"v_mfma_f32_16x16x32_bf16 v[76:79], a[104:107], a[156:159], v[76:79]\n\t"
"v_mfma_f32_16x16x32_bf16 v[76:79], a[108:111], a[172:175], v[76:79]\n\t"
"v_cvt_pk_bf16_f32 v56, v56, v57\n\t"
"v_cvt_pk_bf16_f32 v57, v58, v59\n\t"
"v_cvt_pk_bf16_f32 v72, v72, v73\n\t"
"v_cvt_pk_bf16_f32 v73, v74, v75\n\t"
"v_mfma_f32_16x16x32_bf16 v[60:63], a[96:99], a[156:159], v[60:63]\n\t"
"v_cvt_pk_bf16_f32 v88, v88, v89\n\t"
"v_cvt_pk_bf16_f32 v89, v90, v91\n\t"
"v_mfma_f32_16x16x32_bf16 v[60:63], a[100:103], a[172:175], v[60:63]\n\t"
"ds_read_b128 a[156:159], v216 offset:23552\n\t"
"ds_read_b128 a[172:175], v216 offset:31744\n\t"
"s_waitcnt vmcnt(0) lgkmcnt(7)\n\t"
"v_mfma_f32_16x16x32_bf16 v[144:147], a[120:123], a[144:147], v[144:147]\n\t"
"v_mfma_f32_16x16x32_bf16 v[144:147], a[124:127], a[160:163], v[144:147]\n\t"
"v_mfma_f32_16x16x32_bf16 v[160:163], a[128:131], a[144:147], v[160:163]\n\t"
"v_mfma_f32_16x16x32_bf16 v[160:163], a[132:135], a[160:163], v[160:163]\n\t"
"s_nop 0\n\t"
"v_cvt_pk_bf16_f32 v60, v60, v61\n\t"
"v_cvt_pk_bf16_f32 v61, v62, v63\n\t"
"v_cvt_pk_bf16_f32 v76, v76, v77\n\t"
"v_cvt_pk_bf16_f32 v77, v78, v79\n\t"
"v_cvt_pk_bf16_f32 v92, v92, v93\n\t"
"v_cvt_pk_bf16_f32 v93, v94, v95\n\t"
"v_mfma_f32_16x16x32_bf16 v[176:179], a[136:139], a[144:147], v[176:179]\n\t"
"v_mfma_f32_16x16x32_bf16 v[176:179], a[140:143], a[160:163], v[176:179]\n\t"
"ds_write2st64_b64 v228, v[56:57], v[60:61] offset0:26 offset1:39\n\t"
"ds_write2st64_b64 v246, v[72:73], v[76:77] offset0:26 offset1:39\n\t"
"ds_write2st64_b64 v247, v[88:89], v[92:93] offset0:26 offset1:39\n\t"
"v_mfma_f32_16x16x32_bf16 v[180:183], a[136:139], a[148:151], v[180:183]\n\t"
"v_mfma_f32_16x16x32_bf16 v[180:183], a[140:143], a[164:167], v[180:183]\n\t"
"v_mfma_f32_16x16x32_bf16 v[164:167], a[128:131], a[148:151], v[164:167]\n\t"
"v_mfma_f32_16x16x32_bf16 v[164:167], a[132:135], a[164:167], v[164:167]\n\t"
"v_cvt_pk_bf16_f32 v144, v144, v145\n\t"
"v_cvt_pk_bf16_f32 v145, v146, v147\n\t"
"v_cvt_pk_bf16_f32 v160, v160, v161\n\t"
"v_cvt_pk_bf16_f32 v161, v162, v163\n\t"
"v_mfma_f32_16x16x32_bf16 v[148:151], a[120:123], a[148:151], v[148:151]\n\t"
"v_cvt_pk_bf16_f32 v176, v176, v177\n\t"
"v_cvt_pk_bf16_f32 v177, v178, v179\n\t"
"v_mfma_f32_16x16x32_bf16 v[148:151], a[124:127], a[164:167], v[148:151]\n\t"
"s_waitcnt lgkmcnt(0)\n\t"
"s_barrier\n\t"
"v_mfma_f32_16x16x32_bf16 v[152:155], a[120:123], a[152:155], v[152:155]\n\t"
"ds_read_b128 a[0:3], v229 offset:0\n\t"
"v_mfma_f32_16x16x32_bf16 v[152:155], a[124:127], a[168:171], v[152:155]\n\t"
"ds_read_b128 a[4:7], v230 offset:0\n\t"
"ds_read_b128 a[8:11], v231 offset:0\n\t"
"v_mfma_f32_16x16x32_bf16 v[168:171], a[128:131], a[152:155], v[168:171]\n\t"
"ds_read_b128 a[12:15], v229 offset:13312\n\t"
"v_mfma_f32_16x16x32_bf16 v[168:171], a[132:135], a[168:171], v[168:171]\n\t"
"v_cvt_pk_bf16_f32 v148, v148, v149\n\t"
"v_cvt_pk_bf16_f32 v149, v150, v151\n\t"
"v_cvt_pk_bf16_f32 v164, v164, v165\n\t"
"v_cvt_pk_bf16_f32 v165, v166, v167\n\t"
"v_cvt_pk_bf16_f32 v180, v180, v181\n\t"
"v_cvt_pk_bf16_f32 v181, v182, v183\n\t"
"v_mfma_f32_16x16x32_bf16 v[184:187], a[136:139], a[152:155], v[184:187]\n\t"
"ds_read_b128 a[16:19], v230 offset:13312\n\t"
"v_mfma_f32_16x16x32_bf16 v[184:187], a[140:143], a[168:171], v[184:187]\n\t"
"ds_write2st64_b64 v228, v[144:145], v[148:149] offset0:52 offset1:65\n\t"
"ds_write2st64_b64 v246, v[160:161], v[164:165] offset0:52 offset1:65\n\t"
"ds_write2st64_b64 v247, v[176:177], v[180:181] offset0:52 offset1:65\n\t"
"v_mfma_f32_16x16x32_bf16 v[188:191], a[136:139], a[156:159], v[188:191]\n\t"
"ds_read_b128 a[20:23], v231 offset:13312\n\t"
"v_mfma_f32_16x16x32_bf16 v[188:191], a[140:143], a[172:175], v[188:191]\n\t"
"s_waitcnt lgkmcnt(8)\n\t"
"buffer_store_dwordx4 a[0:3], v232, s[16:19], s24 offen nt\n\t"
"s_waitcnt lgkmcnt(7)\n\t"
"buffer_store_dwordx4 a[4:7], v233, s[16:19], s24 offen nt\n\t"
"v_mfma_f32_16x16x32_bf16 v[172:175], a[128:131], a[156:159], v[172:175]\n\t"
"v_mfma_f32_16x16x32_bf16 v[172:175], a[132:135], a[172:175], v[172:175]\n\t"
"v_cvt_pk_bf16_f32 v152, v152, v153\n\t"
"v_cvt_pk_bf16_f32 v153, v154, v155\n\t"
"v_cvt_pk_bf16_f32 v168, v168, v169\n\t"
"v_cvt_pk_bf16_f32 v169, v170, v171\n\t"
"v_cvt_pk_bf16_f32 v184, v184, v185\n\t"
"v_cvt_pk_bf16_f32 v185, v186, v187\n\t"
"v_mfma_f32_16x16x32_bf16 v[156:159], a[120:123], a[156:159], v[156:159]\n\t"
"v_mfma_f32_16x16x32_bf16 v[156:159], a[124:127], a[172:175], v[156:159]\n\t"
"s_waitcnt lgkmcnt(6)\n\t"
"buffer_store_dwordx4 a[8:11], v234, s[16:19], s24 offen nt\n\t"
"s_waitcnt lgkmcnt(5)\n\t"
"buffer_store_dwordx4 a[12:15], v232, s[16:19], s27 offen nt\n\t"
"s_waitcnt lgkmcnt(4)\n\t"
"buffer_store_dwordx4 a[16:19], v233, s[16:19], s27 offen nt\n\t"
"s_waitcnt lgkmcnt(0)\n\t"
"buffer_store_dwordx4 a[20:23], v234, s[16:19], s27 offen nt\n\t"
"v_cvt_pk_bf16_f32 v156, v156, v157\n\t"
"v_cvt_pk_bf16_f32 v157, v158, v159\n\t"
"ds_write2st64_b64 v228, v[152:153], v[156:157] offset0:78 offset1:91\n\t"
"v_cvt_pk_bf16_f32 v172, v172, v173\n\t"
"v_cvt_pk_bf16_f32 v173, v174, v175\n\t"
"ds_write2st64_b64 v246, v[168:169], v[172:173] offset0:78 offset1:91\n\t"
"v_cvt_pk_bf16_f32 v188, v188, v189\n\t"
"v_cvt_pk_bf16_f32 v189, v190, v191\n\t"
"ds_write2st64_b64 v247, v[184:185], v[188:189] offset0:78 offset1:91\n\t"
"s_waitcnt lgkmcnt(0)\n\t"
"s_barrier\n\t"
"ds_read_b128 a[24:27], v229 offset:26624\n\t"
"ds_read_b128 a[28:31], v230 offset:26624\n\t"
"ds_read_b128 a[32:35], v231 offset:26624\n\t"
"ds_read_b128 a[36:39], v229 offset:39936\n\t"
"ds_read_b128 a[40:43], v230 offset:39936\n\t"
"ds_read_b128 a[44:47], v231 offset:39936\n\t"
"s_waitcnt lgkmcnt(5)\n\t"
"buffer_store_dwordx4 a[24:27], v232, s[16:19], s20 offen nt\n\t"
"s_waitcnt lgkmcnt(4)\n\t"
"buffer_store_dwordx4 a[28:31], v233, s[16:19], s20 offen nt\n\t"
"s_waitcnt lgkmcnt(3)\n\t"
"buffer_store_dwordx4 a[32:35], v234, s[16:19], s20 offen nt\n\t"
"s_waitcnt lgkmcnt(2)\n\t"
"buffer_store_dwordx4 a[36:39], v232, s[16:19], s25 offen nt\n\t"
"s_waitcnt lgkmcnt(1)\n\t"
"buffer_store_dwordx4 a[40:43], v233, s[16:19], s25 offen nt\n\t"
"s_waitcnt lgkmcnt(0)\n\t"
"buffer_store_dwordx4 a[44:47], v234, s[16:19], s25 offen nt\n\t"
"s_waitcnt vmcnt(0)\n\t"
"Lwino_end:\n\t"
  : "+{v205}"(tid)
  : "s"(__builtin_amdgcn_kernarg_segment_ptr()), "s"(blockIdx.x), "s"((unsigned)(size_t)(__attribute__((address_space(3))) char*)smem)
  : "v0", "v1", "v2", "v3", "v4", "v5", "v6", "v7", "v8", "v9", "v10", "v11", "v12", "v13", "v14", "v15", "v16", "v17", "v18", "v19", "v20", "v21", "v22", "v23", "v24", "v25", "v26", "v27", "v28", "v29", "v30", "v31", "v32", "v33", "v34", "v35", "v36", "v37", "v38", "v39", "v40", "v41", "v42", "v43", "v44", "v45", "v46", "v47", "v48", "v49", "v50", "v51", "v52", "v53", "v54", "v55", "v56", "v57", "v58", "v59", "v60", "v61", "v62", "v63", "v64", "v65", "v66", "v67", "v68", "v69", "v70", "v71", "v72", "v73", "v74", "v75", "v76", "v77", "v78", "v79", "v80", "v81", "v82", "v83", "v84", "v85", "v86", "v87", "v88", "v89", "v90", "v91", "v92", "v93", "v94", "v95", "v96", "v97", "v98", "v99", "v100", "v101", "v102", "v103", "v104", "v105", "v106", "v107", "v108", "v109", "v110", "v111", "v112", "v113", "v114", "v115", "v116", "v117", "v118", "v119", "v120", "v121", "v122", "v123", "v124", "v125", "v126", "v127", "v128", "v129", "v130", "v131", "v132", "v133", "v134", "v135", "v136", "v137", "v138", "v139", "v140", "v141", "v142", "v143", "v144", "v145", "v146", "v147", "v148", "v149", "v150", "v151", "v152", "v153", "v154", "v155", "v156", "v157", "v158", "v159", "v160", "v161", "v162", "v163", "v164", "v165", "v166", "v167", "v168", "v169", "v170", "v171", "v172", "v173", "v174", "v175", "v176", "v177", "v178", "v179", "v180", "v181", "v182", "v183", "v184", "v185", "v186", "v187", "v188", "v189", "v190", "v191", "v192", "v193", "v194", "v195", "v196", "v197", "v198", "v199", "v200", "v201", "v202", "v203", "v204", "v206", "v207", "v208", "v209", "v210", "v211", "v212", "v213", "v214", "v215", "v216", "v217", "v218", "v219", "v220", "v221", "v222", "v223", "v224", "v225", "v226", "v227", "v228", "v229", "v230", "v231", "v232", "v233", "v234", "v235", "v236", "v237", "v238", "v239", "v240", "v241", "v242", "v243", "v244", "v245", "v246", "v247", "v248", "v249", "v250", "v251", "v252", "v253", "v254", "v255", "a0", "a1", "a2", "a3", "a4", "a5", "a6", "a7", "a8", "a9", "a10", "a11", "a12", "a13", "a14", "a15", "a16", "a17", "a18", "a19", "a20", "a21", "a22", "a23", "a24", "a25", "a26", "a27", "a28", "a29", "a30", "a31", "a32", "a33", "a34", "a35", "a36", "a37", "a38", "a39", "a40", "a41", "a42", "a43", "a44", "a45", "a46", "a47", "a48", "a49", "a50", "a51", "a52", "a53", "a54", "a55", "a56", "a57", "a58", "a59", "a60", "a61", "a62", "a63", "a64", "a65", "a66", "a67", "a68", "a69", "a70", "a71", "a72", "a73", "a74", "a75", "a76", "a77", "a78", "a79", "a80", "a81", "a82", "a83", "a84", "a85", "a86", "a87", "a88", "a89", "a90", "a91", "a92", "a93", "a94", "a95", "a96", "a97", "a98", "a99", "a100", "a101", "a102", "a103", "a104", "a105", "a106", "a107", "a108", "a109", "a110", "a111", "a112", "a113", "a114", "a115", "a116", "a117", "a118", "a119", "a120", "a121", "a122", "a123", "a124", "a125", "a126", "a127", "a128", "a129", "a130", "a131", "a132", "a133", "a134", "a135", "a136", "a137", "a138", "a139", "a140", "a141", "a142", "a143", "a144", "a145", "a146", "a147", "a148", "a149", "a150", "a151", "a152", "a153", "a154", "a155", "a156", "a157", "a158", "a159", "a160", "a161", "a162", "a163", "a164", "a165", "a166", "a167", "a168", "a169", "a170", "a171", "a172", "a173", "a174", "a175", "a176", "a177", "a178", "a179", "a180", "a181", "a182", "a183", "a184", "a185", "a186", "a187", "a188", "a189", "a190", "a191", "a192", "a193", "a194", "a195", "a196", "a197", "a198", "a199", "a200", "a201", "a202", "a203", "a204", "a205", "a206", "a207", "a208", "a209", "a210", "a211", "a212", "a213", "a214", "a215", "a216", "a217", "a218", "a219", "a220", "a221", "a222", "a223", "a224", "a225", "a226", "a227", "a228", "a229", "a230", "a231", "a232", "a233", "a234", "a235", "a236", "a237", "a238", "a239", "a240", "a241", "a242", "a243", "a244", "a245", "a246", "a247", "a248", "a249", "a250", "a251", "a252", "a253", "a254", "a255", "s6", "s7", "s8", "s9", "s10", "s11", "s12", "s13", "s14", "s15", "s16", "s17", "s18", "s19", "s20", "s21", "s22", "s23", "s24", "s25", "s26", "s27", "s28", "s29", "s30", "s31", "s32", "s33", "s34", "s35", "s36", "s37", "s38", "s39", "s40", "s41", "s42", "s43", "s44", "s45", "s46", "s47", "s48", "s49", "s50", "s51", "s52", "s53", "s54", "s55", "s56", "s57", "s58", "s59", "s60", "s61", "s62", "s63", "s64", "s65", "s66", "s67", "s68", "s69", "s70", "s71", "s72", "s73", "s74", "s75", "s76", "s77", "s78", "s79", "s80", "s81", "s82", "s83", "s84", "s85", "s86", "s87", "s88", "s89", "s90", "s91", "scc", "vcc", "memory");
}

extern "C" void kernel_launch(void* const* d_in, const int* in_sizes, int n_in, void* d_out, int out_size, void* d_ws,
                              size_t ws_size, hipStream_t stream) {
  wino_main_kernel<<<256, 256, 0, stream>>>((const char*)d_in[0], (char*)d_ws, (char*)d_out, (const char*)d_in[1]);
}
```

```cpp
#include <hip/hip_runtime.h>
#include <hip/hip_bf16.h>
#include <stdint.h>

#define U_BYTES 1179648

__global__ __launch_bounds__(256, 1) void wino_main_kernel(const char* __restrict__ x, char* __restrict__ Uq, char* __restrict__ out, const char* __restrict__ wt) {
  __shared__ __attribute__((aligned(1024))) char smem[163840];
  unsigned tid = threadIdx.x;
  asm volatile(
"s_load_dwordx8 s[76:83], %1, 0x0\n\t"
"s_mov_b32 s66, %2\n\t"
"s_getpc_b64 s[6:7]\n\t"
"Lwino_pc:\n\t"
"s_bfe_u32 s42, %2, 0x50003\n\t"
"s_lshl_b32 s42, s42, 12\n\t"
"v_lshl_add_u32 v203, v205, 4, s42\n\t"
"v_min_u32 v203, (Lwino_end-Lwino_pc-16)&-16, v203\n\t"
"global_load_dwordx4 v[196:199], v203, s[6:7]\n\t"
"v_and_b32 v204, 63, v205\n\t"
"v_readfirstlane_b32 s73, v205\n\t"
"v_lshrrev_b32 v120, 3, v204\n\t"
"v_and_b32 v121, 7, v204\n\t"
"s_lshr_b32 s73, s73, 6\n\t"
"s_lshr_b32 s56, s66, 5\n\t"
"s_mul_i32 s56, s56, 12288\n\t"
"s_bfe_u32 s61, s66, 0x20001\n\t"
"s_mul_i32 s61, s61, 1536\n\t"
"s_add_u32 s56, s56, s61\n\t"
"s_bitcmp1_b32 s66, 0\n\t"
"s_cselect_b32 s61, 48, 0\n\t"
"s_add_u32 s56, s56, s61\n\t"
"s_bfe_u32 s62, s66, 0x20003\n\t"
"s_mul_i32 s61, s62, 96\n\t"
"s_add_u32 s56, s56, s61\n\t"
"s_cmp_lt_u32 s66, 192\n\t"
"s_cselect_b32 s56, s56, 2.0\n\t"
"v_bfe_u32 v42, v204, 2, 2\n\t"
"v_and_or_b32 v42, v204, 16, v42\n\t"
"v_mul_u32_u24 v42, 0x180, v42\n\t"
"v_lshrrev_b32 v44, 5, v204\n\t"
"v_mul_u32_u24 v43, 0x12000, v44\n\t"
"v_and_b32 v45, 3, v204\n\t"
"v_min_u32 v41, 2, v45\n\t"
"v_lshl_add_u32 v41, v41, 4, v42\n\t"
"v_add_u32 v41, v41, v43\n\t"
"v_lshlrev_b32 v43, 2, v204\n\t"
"v_and_or_b32 v45, v43, 16, v45\n\t"
"v_mul_u32_u24 v45, 0x180, v45\n\t"
"v_lshl_add_u32 v44, v44, 3, v120\n\t"
"v_lshl_add_u32 v45, v44, 1, v45\n\t"
"v_mov_b32 v40, 0\n\t"
"s_mul_i32 s61, s73, 147456\n\t"
"s_add_u32 s61, s61, s56\n\t"
"s_lshl3_add_u32 s62, s73, s56\n\t"
"s_add_u32 s62, s62, 589824\n\t"
"s_lshl_b32 s64, s73, 10\n\t"
"s_and_b32 s60, s66, 7\n\t"
"s_lshr_b32 s65, s66, 3\n\t"
"s_mul_i32 s24, s60, 12582912\n\t"
"s_mul_i32 s63, s65, 6144\n\t"
"s_add_u32 s24, s24, s63\n\t"
"s_add_u32 s22, s24, 196224\n\t"
"s_mul_i32 s63, s73, 786432\n\t"
"s_mul_i32 s26, s73, 4608\n\t"
"s_mov_b32 s48, 189696\n\t"
"s_cmp_eq_u32 s65, 0\n\t"
"s_cselect_b64 s[40:41], 0xff, 0\n\t"
"s_cselect_b64 s[46:47], -1, 0\n\t"
"v_mul_u32_u24 v122, 0x300, v120\n\t"
"v_xor_b32 v123, 4, v121\n\t"
"v_add_u32 v122, s63, v122\n\t"
"v_and_b32 v124, 2, v120\n\t"
"v_xor_b32 v124, v124, v123\n\t"
"v_lshl_add_u32 v128, v124, 4, v122\n\t"
"v_cndmask_b32 v241, v128, 2.0, s[40:41]\n\t"
"v_add_u32 v122, 194816, v122\n\t"
"v_add_u32 v124, -1, v120\n\t"
"v_ashrrev_i32 v125, 31, v124\n\t"
"v_sub_u32 v124, v124, v125\n\t"
"v_and_b32 v124, 2, v124\n\t"
"v_xor_b32 v124, v124, v123\n\t"
"v_mad_i32_i24 v125, v125, s48, v122\n\t"
"v_lshl_add_u32 v129, v124, 4, v125\n\t"
"s_lshl_b64 s[40:41], s[40:41], 8\n\t"
"v_cndmask_b32 v242, v129, 2.0, s[40:41]\n\t"
"v_add_u32 v122, 194816, v122\n\t"
"v_add_u32 v124, -2, v120\n\t"
"v_ashrrev_i32 v125, 31, v124\n\t"
"v_sub_u32 v124, v124, v125\n\t"
"v_and_b32 v124, 2, v124\n\t"
"v_and_or_b32 v124, v125, 4, v124\n\t"
"v_xor_b32 v124, v124, v121\n\t"
"v_mad_i32_i24 v125, v125, s48, v122\n\t"
"v_lshl_add_u32 v130, v124, 4, v125\n\t"
"s_lshl_b64 s[40:41], s[40:41], 8\n\t"
"v_cndmask_b32 v243, v130, 2.0, s[40:41]\n\t"
"v_add_u32 v122, 194816, v122\n\t"
"v_add_u32 v124, -3, v120\n\t"
"v_ashrrev_i32 v125, 31, v124\n\t"
"v_sub_u32 v124, v124, v125\n\t"
"v_and_b32 v124, 2, v124\n\t"
"v_xor_b32 v124, v124, v121\n\t"
"v_mad_i32_i24 v125, v125, s48, v122\n\t"
"v_lshl_add_u32 v131, v124, 4, v125\n\t"
"s_lshl_b64 s[40:41], s[40:41], 8\n\t"
"v_cndmask_b32 v244, v131, 2.0, s[40:41]\n\t"
"v_add_u32 v124, 5, v120\n\t"
"v_and_b32 v124, 2, v124\n\t"
"v_xor_b32 v124, v124, v121\n\t"
"v_add_u32 v122, 2560, v122\n\t"
"v_lshl_add_u32 v245, v124, 4, v122\n\t"
"s_mov_b32 s37, -1\n\t"
"s_mov_b32 s36, 0\n\t"
"s_mul_i32 s67, s73, 48\n\t"
"s_add_u32 s67, s67, 118784\n\t"
"s_mov_b32 s10, 0x200000\n\t"
"s_mov_b32 s11, 0x20000\n\t"
"v_min_u32 v189, 47, v204\n\t"
"v_add_u32 v189, s67, v189\n\t"
"v_mul_u32_u24 v189, 16, v189\n\t"
"s_mov_b32 s54, 663552\n\t"
"s_mov_b32 s55, 0x20000\n\t"
"s_mov_b32 s14, 100663296\n\t"
"s_mov_b32 s15, 0x20000\n\t"
"s_add_u32 m0, s64, 0\n\t"
"s_waitcnt lgkmcnt(0)\n\t"
"s_mov_b32 s52, s82\n\t"
"s_and_b32 s53, s83, 0xffff\n\t"
"buffer_load_dwordx4 v41, s[52:55], s61 offen lds\n\t"
"buffer_load_short_d16_hi v40, v45, s[52:55], s62 offen\n\t"
"s_mov_b32 s8, s78\n\t"
"s_and_b32 s9, s79, 0xffff\n\t"
"buffer_load_dword v188, v189, s[8:11], 0 offen sc1\n\t"
"s_mov_b32 s12, s76\n\t"
"s_and_b32 s13, s77, 0xffff\n\t"
"s_add_u32 m0, s26, 83200\n\t"
"s_nop 0\n\t"
"buffer_load_dwordx4 v241, s[12:15], s22 offen offset:0 nt lds\n\t"
"buffer_load_dwordx4 v242, s[12:15], s22 offen offset:1024 nt lds\n\t"
"buffer_load_dwordx4 v243, s[12:15], s22 offen offset:2048 nt lds\n\t"
"buffer_load_dwordx4 v244, s[12:15], s22 offen offset:3072 nt lds\n\t"
"s_mov_b64 exec, s[36:37]\n\t"
"buffer_load_dwordx4 v245, s[12:15], s22 offen offset:3584 nt lds\n\t"
"s_mov_b64 exec, -1\n\t"
"s_mul_i32 s49, s73, 36\n\t"
"s_mul_i32 s43, s73, 576\n\t"
"s_sub_u32 s42, s22, 203520\n\t"
"s_mov_b32 s44, -1\n\t"
"s_mov_b32 s45, 15\n\t"
"v_min_u32 v124, 35, v204\n\t"
"v_add_u32 v124, s49, v124\n\t"
"v_lshrrev_b32 v119, 3, v124\n\t"
"v_and_b32 v124, 7, v124\n\t"
"v_mul_u32_u24 v127, 0x300, v119\n\t"
"v_cmp_gt_u32 vcc, 9, v119\n\t"
"v_add_u32 v119, -9, v119\n\t"
"v_ashrrev_i32 v125, 31, v119\n\t"
"v_add_u32 v127, s42, v127\n\t"
"v_mad_i32_i24 v127, v125, s48, v127\n\t"
"v_and_b32 v125, 9, v125\n\t"
"v_add_u32 v119, v119, v125\n\t"
"v_and_b32 v125, 2, v119\n\t"
"v_xor_b32 v124, v124, v125\n\t"
"v_lshl_add_u32 v133, v124, 4, v127\n\t"
"s_cmp_eq_u32 s60, 0\n\t"
"s_cselect_b64 s[50:51], vcc, 0\n\t"
"v_cmp_eq_u32 vcc, 0, v119\n\t"
"v_cndmask_b32 v246, v133, 2.0, s[50:51]\n\t"
"s_and_b64 vcc, vcc, s[46:47]\n\t"
"v_cndmask_b32 v246, v246, 2.0, vcc\n\t"
"s_add_u32 m0, s43, 80896\n\t"
"s_mov_b64 exec, s[44:45]\n\t"
"buffer_load_dwordx4 v246, s[12:15], 0 offen offset:0 nt lds\n\t"
"s_mov_b64 exec, -1\n\t"
"s_mul_i32 s70, s66, 16\n\t"
"s_add_u32 s70, s70, 1900544\n\t"
"s_lshl_b32 s61, s73, 8\n\t"
"s_or_b32 s61, s61, s66\n\t"
"s_cmp_lt_u32 s61, 192\n\t"
"s_cselect_b32 s70, s70, 2.0\n\t"
"s_lshr_b32 s61, s66, 3\n\t"
"s_lshl2_add_u32 s61, s61, s73\n\t"
"s_lshl_b32 s60, s61, 8\n\t"
"s_cmp_lt_u32 s61, 96\n\t"
"s_cselect_b32 s60, s60, 2.0\n\t"
"v_lshrrev_b32 v118, 1, v204\n\t"
"v_mul_u32_u24 v118, 220928, v118\n\t"
"v_lshl_add_u32 v118, v204, 7, v118\n\t"
"s_mov_b64 exec, 63\n\t"
"buffer_load_dword v118, v118, s[52:55], s60 offen\n\t"
"s_mov_b64 exec, -1\n\t"
"buffer_load_dword v187, off, s[8:11], s70 sc1\n\t"
"v_add_u32 v107, 0, v204\n\t"
"v_mul_u32_u24 v104, 0xab, v107\n\t"
"v_lshrrev_b32 v104, 10, v104\n\t"
"v_mul_u32_u24 v104, 0x120, v104\n\t"
"v_lshl_add_u32 v104, v107, 4, v104\n\t"
"v_add_u32 v107, 64, v204\n\t"
"v_mul_u32_u24 v105, 0xab, v107\n\t"
"v_lshrrev_b32 v105, 10, v105\n\t"
"v_mul_u32_u24 v105, 0x120, v105\n\t"
"v_lshl_add_u32 v105, v107, 4, v105\n\t"
"v_add_u32 v107, 128, v204\n\t"
"v_mul_u32_u24 v106, 0xab, v107\n\t"
"v_lshrrev_b32 v106, 10, v106\n\t"
"v_mul_u32_u24 v106, 0x120, v106\n\t"
"v_lshl_add_u32 v106, v107, 4, v106\n\t"
"s_mul_i32 s74, s73, 41472\n\t"
"s_mul_i32 s72, s73, 96\n\t"
"s_add_u32 s74, s74, 60160\n\t"
"s_cmp_eq_u32 s73, 0\n\t"
"s_cselect_b32 s74, 53504, s74\n\t"
"s_add_u32 s75, s74, 12288\n\t"
"s_cmp_eq_u32 s73, 3\n\t"
"s_cselect_b32 s74, 16384, s74\n\t"
"s_cselect_b32 s75, 74240, s75\n\t"
"s_lshr_b32 s60, s66, 5\n\t"
"s_bfe_u32 s62, s66, 0x20003\n\t"
"s_and_b32 s61, s60, 1\n\t"
"s_lshl1_add_u32 s61, s73, s61\n\t"
"s_mul_i32 s62, s62, 288\n\t"
"s_add_u32 s61, s61, s62\n\t"
"s_lshr_b32 s60, s60, 1\n\t"
"s_mul_i32 s62, s60, 96\n\t"
"s_add_u32 s61, s61, s62\n\t"
"s_lshl_b32 s57, s61, 10\n\t"
"s_and_b32 s61, s66, 7\n\t"
"s_lshl_b32 s61, s61, 7\n\t"
"s_add_u32 s57, s57, s61\n\t"
"s_cmp_lt_u32 s73, 3\n\t"
"s_cselect_b32 s67, s66, 0xff\n\t"
"s_cmp_lt_u32 s67, 192\n\t"
"s_cselect_b32 s57, s57, 2.0\n\t"
"s_lshl_b32 s60, s73, 3\n\t"
"s_add_u32 s60, s60, 0\n\t"
"s_lshl_b32 s61, s73, 11\n\t"
"s_add_u32 s61, s61, 4096\n\t"
"v_lshl_add_u32 v46, v121, 6, s60\n\t"
"v_lshl_add_u32 v46, v44, 1, v46\n\t"
"v_lshl_add_u32 v42, v204, 1, s61\n\t"
"v_lshl_add_u32 v43, v204, 4, s61\n\t"
"v_mul_u32_u24 v44, 6144, v120\n\t"
"v_lshl_add_u32 v44, v121, 4, v44\n\t"
"v_mov_b32 v32, 0\n\t"
"v_mov_b32 v33, 0\n\t"
"v_mov_b32 v34, 0\n\t"
"v_mov_b32 v35, 0\n\t"
"v_mov_b32 v36, 0\n\t"
"v_mov_b32 v37, 0\n\t"
"v_mov_b32 v38, 0\n\t"
"v_mov_b32 v39, 0\n\t"
"s_waitcnt vmcnt(9)\n\t"
"s_barrier\n\t"
"ds_read_u16_d16_hi v32, v46 offset:0\n\t"
"ds_read_u16_d16_hi v33, v46 offset:512\n\t"
"ds_read_u16_d16_hi v34, v46 offset:1024\n\t"
"ds_read_u16_d16_hi v35, v46 offset:1536\n\t"
"ds_read_u16_d16_hi v36, v46 offset:2048\n\t"
"ds_read_u16_d16_hi v37, v46 offset:2560\n\t"
"ds_read_u16_d16_hi v38, v46 offset:3072\n\t"
"ds_read_u16_d16_hi v39, v46 offset:3584\n\t"
"s_waitcnt lgkmcnt(1)\n\t"
"v_add_f32 v48, v32, v38\n\t"
"s_waitcnt lgkmcnt(0)\n\t"
"v_add_f32 v49, v33, v39\n\t"
"v_add_f32 v50, v34, v40\n\t"
"v_add_f32 v51, v48, v35\n\t"
"v_sub_f32 v54, v48, v35\n\t"
"v_add_f32 v52, v49, v36\n\t"
"v_sub_f32 v55, v49, v36\n\t"
"v_add_f32 v53, v50, v37\n\t"
"v_sub_f32 v56, v50, v37\n\t"
"v_add_f32 v57, v32, v34\n\t"
"v_add_f32 v58, v51, v53\n\t"
"v_add_f32 v59, v54, v56\n\t"
"v_add_f32 v60, v38, v40\n\t"
"v_add_f32 v62, v57, v33\n\t"
"v_sub_f32 v63, v57, v33\n\t"
"v_add_f32 v64, v58, v52\n\t"
"v_sub_f32 v65, v58, v52\n\t"
"v_add_f32 v66, v59, v55\n\t"
"v_sub_f32 v67, v59, v55\n\t"
"v_add_f32 v68, v60, v39\n\t"
"v_sub_f32 v69, v60, v39\n\t"
"v_mul_f32 v71, 0.5, v62\n\t"
"v_mul_f32 v72, 0.5, v63\n\t"
"v_mul_f32 v74, 0.5, v51\n\t"
"v_mul_f32 v75, 0x3e800000, v64\n\t"
"v_mul_f32 v76, 0x3e800000, v65\n\t"
"v_mul_f32 v77, -0.5, v53\n\t"
"v_mul_f32 v79, 0.5, v66\n\t"
"v_mul_f32 v80, 0.5, v67\n\t"
"v_mul_f32 v83, -0.5, v68\n\t"
"v_mul_f32 v84, -0.5, v69\n\t"
"v_cvt_pk_bf16_f32 v86, v32, v71\n\t"
"v_cvt_pk_bf16_f32 v87, v72, -v34\n\t"
"v_cvt_pk_bf16_f32 v88, v74, v75\n\t"
"v_cvt_pk_bf16_f32 v89, v76, v77\n\t"
"v_cvt_pk_bf16_f32 v90, v54, v79\n\t"
"v_cvt_pk_bf16_f32 v91, v80, -v56\n\t"
"v_cvt_pk_bf16_f32 v92, -v38, v83\n\t"
"v_cvt_pk_bf16_f32 v93, v84, v40\n\t"
"ds_write_b16 v42, v86 offset:0\n\t"
"ds_write_b16_d16_hi v42, v86 offset:128\n\t"
"ds_write_b16 v42, v87 offset:256\n\t"
"ds_write_b16_d16_hi v42, v87 offset:384\n\t"
"ds_write_b16 v42, v88 offset:512\n\t"
"ds_write_b16_d16_hi v42, v88 offset:640\n\t"
"ds_write_b16 v42, v89 offset:768\n\t"
"ds_write_b16_d16_hi v42, v89 offset:896\n\t"
"ds_write_b16 v42, v90 offset:1024\n\t"
"ds_write_b16_d16_hi v42, v90 offset:1152\n\t"
"ds_write_b16 v42, v91 offset:1280\n\t"
"ds_write_b16_d16_hi v42, v91 offset:1408\n\t"
"ds_write_b16 v42, v92 offset:1536\n\t"
"ds_write_b16_d16_hi v42, v92 offset:1664\n\t"
"ds_write_b16 v42, v93 offset:1792\n\t"
"ds_write_b16_d16_hi v42, v93 offset:1920\n\t"
"ds_read_b128 v[96:99], v43 offset:0\n\t"
"ds_read_b128 v[100:103], v43 offset:1024\n\t"
"s_add_u32 s60, s57, 0\n\t"
"s_waitcnt lgkmcnt(1)\n\t"
"buffer_store_dwordx4 v[96:99], v44, s[8:11], s60 offen sc1\n\t"
"s_add_u32 s60, s57, 49152\n\t"
"s_waitcnt lgkmcnt(0)\n\t"
"buffer_store_dwordx4 v[100:103], v44, s[8:11], s60 offen sc1\n\t"
"v_and_b32 v122, 15, v204\n\t"
"v_lshrrev_b32 v123, 4, v204\n\t"
"v_bfe_u32 v124, v204, 2, 2\n\t"
"v_and_b32 v125, 3, v204\n\t"
"v_lshlrev_b32 v125, 3, v125\n\t"
"s_mov_b32 s60, 0x1be4\n\t"
"s_lshl_b32 s61, s73, 10\n\t"
"s_mov_b32 s62, 512\n\t"
"s_add_u32 s63, s26, 1152\n\t"
"v_and_b32 v126, 14, v204\n\t"
"v_bfe_u32 v126, s60, v126, 2\n\t"
"v_xor_b32 v126, v126, v123\n\t"
"v_lshlrev_b32 v126, 4, v126\n\t"
"v_lshl_add_u32 v216, v122, 6, v126\n\t"
"s_add_u32 m0, s74, 0\n\t"
"s_add_u32 s27, s72, 0\n\t"
"buffer_load_dwordx4 v104, s[52:55], s27 offen lds\n\t"
"v_add_u32 v221, s61, v216\n\t"
"v_lshrrev_b32 v126, 1, v121\n\t"
"v_xor_b32 v126, v126, v123\n\t"
"v_lshl_add_u32 v126, v126, 4, s61\n\t"
"v_lshl_add_u32 v126, v121, 6, v126\n\t"
"v_and_b32 v127, 8, v204\n\t"
"v_lshl_add_u32 v217, v127, 9, v126\n\t"
"v_xad_u32 v219, v217, 48, s62\n\t"
"v_sub_u32 v126, 4, v124\n\t"
"v_cmp_gt_u32 vcc, 32, v204\n\t"
"v_mov_b32 v135, 0x3f80bf80\n\t"
"s_nop 0\n\t"
"v_cndmask_b32 v126, v126, v124, vcc\n\t"
"v_and_b32 v127, 2, v126\n\t"
"s_add_u32 m0, s74, 1024\n\t"
"s_add_u32 s27, s72, 0\n\t"
"buffer_load_dwordx4 v105, s[52:55], s27 offen lds\n\t"
"v_lshl_add_u32 v127, v127, 4, v125\n\t"
"v_lshl_add_u32 v127, v126, 7, v127\n\t"
"v_and_b32 v126, 1, v123\n\t"
"v_mul_u32_u24 v126, 1216, v126\n\t"
"v_add3_u32 v226, v126, v127, s63\n\t"
"v_min_u32 v126, 2, v124\n\t"
"v_cmp_gt_u32 vcc, 48, v204\n\t"
"s_nop 1\n\t"
"v_cndmask_b32 v126, v126, v123, vcc\n\t"
"v_and_b32 v134, 1, v126\n\t"
"v_cndmask_b32 v127, 4, v124, vcc\n\t"
"v_lshl_add_u32 v134, v134, 6, v125\n\t"
"v_lshl_add_u32 v134, v127, 7, v134\n\t"
"v_and_b32 v127, 2, v127\n\t"
"v_lshl_add_u32 v134, v127, 4, v134\n\t"
"s_add_u32 m0, s74, 2048\n\t"
"s_add_u32 s27, s72, 0\n\t"
"buffer_load_dwordx4 v106, s[52:55], s27 offen lds\n\t"
"v_mul_u32_u24 v126, 2304, v126\n\t"
"v_add3_u32 v227, v126, v134, s26\n\t"
"v_and_b32 v126, 24, v204\n\t"
"v_cmp_eq_u32 vcc, 8, v126\n\t"
"v_mov_b32 v134, 0xbf803f80\n\t"
"s_nop 0\n\t"
"v_cndmask_b32 v134, v134, v135, vcc\n\t"
"v_lshrrev_b32 v135, 16, v134\n\t"
"v_cmp_eq_u32 vcc, 7, v121\n\t"
"s_nop 1\n\t"
"v_cndmask_b32 v135, 0, v135, vcc\n\t"
"v_cmp_gt_u32 vcc, 32, v204\n\t"
"s_mov_b32 s48, 384\n\t"
"s_cmp_eq_u32 s65, 31\n\t"
"v_cndmask_b32 v134, v135, v134, vcc\n\t"
"v_cndmask_b32 v136, 4, v121, vcc\n\t"
"v_mov_b32 v135, 0\n\t"
"v_and_b32 v137, 1, v136\n\t"
"v_lshlrev_b32 v137, 4, v137\n\t"
"v_lshrrev_b32 v136, 1, v136\n\t"
"v_lshlrev_b64 v[134:135], v137, v[134:135]\n\t"
"v_cmp_eq_u32 vcc, 0, v136\n\t"
"s_cselect_b64 s[40:41], 0xff, 0\n\t"
"s_cselect_b64 s[46:47], -1, 0\n\t"
"s_add_u32 m0, s74, 3072\n\t"
"s_add_u32 s27, s72, 12288\n\t"
"buffer_load_dwordx4 v104, s[52:55], s27 offen lds\n\t"
"v_cndmask_b32 v140, 0, v134, vcc\n\t"
"v_cndmask_b32 v141, 0, v135, vcc\n\t"
"v_cmp_eq_u32 vcc, 1, v136\n\t"
"v_accvgpr_write_b32 a220, v140\n\t"
"v_xad_u32 v236, v128, 64, s48\n\t"
"s_add_u32 m0, s74, 4096\n\t"
"s_add_u32 s27, s72, 12288\n\t"
"buffer_load_dwordx4 v105, s[52:55], s27 offen lds\n\t"
"v_cndmask_b32 v141, v141, v134, vcc\n\t"
"v_cndmask_b32 v142, 0, v135, vcc\n\t"
"v_cmp_eq_u32 vcc, 2, v136\n\t"
"v_accvgpr_write_b32 a221, v141\n\t"
"v_xad_u32 v237, v129, 64, s48\n\t"
"v_cndmask_b32 v142, v142, v134, vcc\n\t"
"v_cndmask_b32 v143, 0, v135, vcc\n\t"
"v_cmp_eq_u32 vcc, 3, v136\n\t"
"v_accvgpr_write_b32 a222, v142\n\t"
"v_cndmask_b32 v237, v237, 2.0, s[40:41]\n\t"
"v_cndmask_b32 v143, v143, v134, vcc\n\t"
"v_accvgpr_write_b32 a223, v143\n\t"
"s_lshl_b64 s[40:41], s[40:41], 8\n\t"
"s_add_u32 m0, s74, 5120\n\t"
"s_add_u32 s27, s72, 12288\n\t"
"buffer_load_dwordx4 v106, s[52:55], s27 offen lds\n\t"
"v_xad_u32 v238, v130, 64, s48\n\t"
"v_cndmask_b32 v238, v238, 2.0, s[40:41]\n\t"
"s_lshl_b64 s[40:41], s[40:41], 8\n\t"
"v_xad_u32 v239, v131, 64, s48\n\t"
"v_cndmask_b32 v239, v239, 2.0, s[40:41]\n\t"
"s_lshl_b64 s[40:41], s[40:41], 40\n\t"
"v_xad_u32 v240, v245, 64, s48\n\t"
"v_cndmask_b32 v240, v240, 2.0, s[40:41]\n\t"
"v_cmp_eq_u32 vcc, 8, v119\n\t"
"v_xad_u32 v247, v133, 64, s48\n\t"
"s_and_b64 vcc, vcc, s[46:47]\n\t"
"v_cndmask_b32 v247, v247, 2.0, s[50:51]\n\t"
"v_cndmask_b32 v247, v247, 2.0, vcc\n\t"
"v_bfe_u32 v126, v204, 3, 1\n\t"
"s_add_u32 m0, s74, 6144\n\t"
"s_add_u32 s27, s72, 442368\n\t"
"buffer_load_dwordx4 v104, s[52:55], s27 offen lds\n\t"
"v_lshrrev_b32 v127, 4, v204\n\t"
"v_sub_u32 v127, v127, v126\n\t"
"v_mov_b32 v134, 0xbf803f80\n\t"
"v_mov_b32 v135, 0x3f80bf80\n\t"
"v_cmp_eq_u32 vcc, 0, v127\n\t"
"v_add_u32 v126, 4, v126\n\t"
"s_nop 0\n\t"
"v_cndmask_b32 v134, 0, v134, vcc\n\t"
"v_cmp_eq_u32 vcc, 1, v127\n\t"
"s_nop 1\n\t"
"v_cndmask_b32 v134, v134, v135, vcc\n\t"
"v_cmp_eq_u32 vcc, 7, v121\n\t"
"s_nop 1\n\t"
"v_cndmask_b32 v135, 0, v135, vcc\n\t"
"v_cmp_gt_u32 vcc, 48, v204\n\t"
"s_nop 1\n\t"
"v_cndmask_b32 v134, v135, v134, vcc\n\t"
"s_add_u32 m0, s74, 7168\n\t"
"s_add_u32 s27, s72, 442368\n\t"
"buffer_load_dwordx4 v105, s[52:55], s27 offen lds\n\t"
"v_mov_b32 v135, 0\n\t"
"v_cndmask_b32 v136, v126, v121, vcc\n\t"
"v_and_b32 v137, 1, v136\n\t"
"v_lshlrev_b32 v137, 4, v137\n\t"
"v_lshrrev_b32 v136, 1, v136\n\t"
"v_lshlrev_b64 v[134:135], v137, v[134:135]\n\t"
"v_cmp_eq_u32 vcc, 0, v136\n\t"
"s_mov_b32 s68, 3000\n\t"
"s_mul_i32 s61, s73, 294912\n\t"
"v_cndmask_b32 v140, 0, v134, vcc\n\t"
"v_cndmask_b32 v141, 0, v135, vcc\n\t"
"v_cmp_eq_u32 vcc, 1, v136\n\t"
"v_accvgpr_write_b32 a224, v140\n\t"
"v_and_b32 v252, 0xff7fff7f, v140\n\t"
"s_add_u32 m0, s74, 8192\n\t"
"s_add_u32 s27, s72, 442368\n\t"
"buffer_load_dwordx4 v106, s[52:55], s27 offen lds\n\t"
"v_cndmask_b32 v141, v141, v134, vcc\n\t"
"v_cndmask_b32 v142, 0, v135, vcc\n\t"
"v_cmp_eq_u32 vcc, 2, v136\n\t"
"v_accvgpr_write_b32 a225, v141\n\t"
"v_xor_b32 v148, 0x80008000, v252\n\t"
"v_cndmask_b32 v142, v142, v134, vcc\n\t"
"v_cndmask_b32 v143, 0, v135, vcc\n\t"
"v_cmp_eq_u32 vcc, 3, v136\n\t"
"v_accvgpr_write_b32 a226, v142\n\t"
"v_and_b32 v253, 0xff7fff7f, v141\n\t"
"v_cndmask_b32 v143, v143, v134, vcc\n\t"
"v_accvgpr_write_b32 a227, v143\n\t"
"v_xor_b32 v149, 0x80008000, v253\n\t"
"s_add_u32 m0, s74, 9216\n\t"
"s_add_u32 s27, s72, 454656\n\t"
"buffer_load_dwordx4 v104, s[52:55], s27 offen lds\n\t"
"v_and_b32 v254, 0xff7fff7f, v142\n\t"
"v_xor_b32 v150, 0x80008000, v254\n\t"
"v_and_b32 v255, 0xff7fff7f, v143\n\t"
"v_xor_b32 v151, 0x80008000, v255\n\t"
"v_lshl_add_u32 v235, v204, 4, s61\n\t"
"v_mul_u32_u24 v107, 24, v204\n\t"
"v_add_u32 v47, s75, v107\n\t"
"v_add_u32 v107, s74, v107\n\t"
"v_lshrrev_b32 v112, 4, v204\n\t"
"v_bfe_u32 v113, v204, 2, 2\n\t"
"v_cmp_eq_u32 vcc, v112, v113\n\t"
"v_lshlrev_b32 v112, 4, v204\n\t"
"v_mov_b32 v113, 0x3e80\n\t"
"v_lshlrev_b32 v113, v112, v113\n\t"
"s_add_u32 m0, s74, 10240\n\t"
"s_add_u32 s27, s72, 454656\n\t"
"buffer_load_dwordx4 v105, s[52:55], s27 offen lds\n\t"
"v_cndmask_b32 v113, 0, v113, vcc\n\t"
"v_and_b32 v112, 2, v204\n\t"
"v_cmp_eq_u32 vcc, 0, v112\n\t"
"v_mov_b32 v108, 0\n\t"
"v_mov_b32 v109, 0\n\t"
"v_cndmask_b32 v110, 0, v113, vcc\n\t"
"v_cndmask_b32 v111, v113, 0, vcc\n\t"
"v_xor_b32 v114, 0x80008000, v110\n\t"
"v_xor_b32 v115, 0x80008000, v111\n\t"
"v_mov_b32 v112, 0\n\t"
"v_mov_b32 v113, 0\n\t"
"v_mov_b32 v116, 0\n\t"
"v_mov_b32 v117, 0\n\t"
"s_add_u32 m0, s74, 11264\n\t"
"s_add_u32 s27, s72, 454656\n\t"
"buffer_load_dwordx4 v106, s[52:55], s27 offen lds\n\t"
"s_waitcnt vmcnt(16)\n\t"
"s_waitcnt vmcnt(12)\n\t"
"v_readfirstlane_b32 s71, v187\n\t"
"s_barrier\n\t"
"s_add_u32 s71, s71, 1\n\t"
"v_mov_b32 v215, s71\n\t"
"s_mov_b64 exec, 1\n\t"
"buffer_store_dword v215, off, s[8:11], s70 sc1\n\t"
"s_mov_b64 exec, -1\n\t"
"v_add_u32 v248, 80896, v226\n\t"
"v_xor_b32 v249, 32, v248\n\t"
"v_xor_b32 v250, 64, v248\n\t"
"v_xor_b32 v251, 96, v248\n\t"
"ds_read_b64_tr_b16 a[176:177], v248 offset:0\n\t"
"ds_read_b64_tr_b16 a[178:179], v248 offset:512\n\t"
"ds_read_b64_tr_b16 a[180:181], v249 offset:0\n\t"
"ds_read_b64_tr_b16 a[182:183], v249 offset:512\n\t"
"s_add_u32 m0, s75, 0\n\t"
"s_add_u32 s27, s72, 221184\n\t"
"buffer_load_dwordx4 v104, s[52:55], s27 offen lds\n\t"
"ds_read_b64_tr_b16 a[184:185], v250 offset:2304\n\t"
"ds_read_b64_tr_b16 a[186:187], v250 offset:2816\n\t"
"ds_read_b64_tr_b16 a[188:189], v251 offset:2304\n\t"
"ds_read_b64_tr_b16 a[190:191], v251 offset:2816\n\t"
"s_add_u32 m0, s75, 1024\n\t"
"s_add_u32 s27, s72, 221184\n\t"
"buffer_load_dwordx4 v105, s[52:55], s27 offen lds\n\t"
"ds_read_b64_tr_b16 a[192:193], v250 offset:0\n\t"
"ds_read_b64_tr_b16 a[194:195], v250 offset:512\n\t"
"ds_read_b64_tr_b16 a[196:197], v251 offset:0\n\t"
"ds_read_b64_tr_b16 a[198:199], v251 offset:512\n\t"
"s_add_u32 m0, s75, 2048\n\t"
"s_add_u32 s27, s72, 221184\n\t"
"buffer_load_dwordx4 v106, s[52:55], s27 offen lds\n\t"
"ds_read_b64_tr_b16 a[200:201], v248 offset:2304\n\t"
"ds_read_b64_tr_b16 a[202:203], v248 offset:2816\n\t"
"ds_read_b64_tr_b16 a[204:205], v249 offset:2304\n\t"
"ds_read_b64_tr_b16 a[206:207], v249 offset:2816\n\t"
"s_add_u32 m0, s75, 3072\n\t"
"s_add_u32 s27, s72, 233472\n\t"
"buffer_load_dwordx4 v104, s[52:55], s27 offen lds\n\t"
"s_waitcnt lgkmcnt(14)\n\t"
"v_mfma_f32_16x16x32_bf16 v[0:3], a[176:179], a[220:223], 0\n\t"
"s_waitcnt lgkmcnt(12)\n\t"
"v_mfma_f32_16x16x32_bf16 v[4:7], a[180:183], a[220:223], 0\n\t"
"s_waitcnt lgkmcnt(10)\n\t"
"v_mfma_f32_16x16x32_bf16 v[8:11], a[184:187], a[220:223], 0\n\t"
"s_waitcnt lgkmcnt(8)\n\t"
"v_mfma_f32_16x16x32_bf16 v[12:15], a[188:191], a[220:223], 0\n\t"
"s_add_u32 m0, s75, 4096\n\t"
"s_add_u32 s27, s72, 233472\n\t"
"buffer_load_dwordx4 v105, s[52:55], s27 offen lds\n\t"
"s_add_u32 m0, s75, 5120\n\t"
"s_add_u32 s27, s72, 233472\n\t"
"buffer_load_dwordx4 v106, s[52:55], s27 offen lds\n\t"
"s_waitcnt lgkmcnt(6)\n\t"
"v_mfma_f32_16x16x32_bf16 v[16:19], a[192:195], a[220:223], 0\n\t"
"s_waitcnt lgkmcnt(4)\n\t"
"v_mfma_f32_16x16x32_bf16 v[20:23], a[196:199], a[220:223], 0\n\t"
"s_waitcnt lgkmcnt(2)\n\t"
"v_mfma_f32_16x16x32_bf16 v[24:27], a[200:203], a[220:223], 0\n\t"
"s_waitcnt lgkmcnt(0)\n\t"
"v_mfma_f32_16x16x32_bf16 v[28:31], a[204:207], a[220:223], 0\n\t"
"s_add_u32 m0, s26, 124544\n\t"
"s_nop 0\n\t"
"buffer_load_dwordx4 v241, s[12:15], s22 offen offset:128 nt lds\n\t"
"buffer_load_dwordx4 v242, s[12:15], s22 offen offset:1152 nt lds\n\t"
"v_cvt_pk_bf16_f32 v0, v0, v1\n\t"
"v_cvt_pk_bf16_f32 v1, v2, v3\n\t"
"v_cvt_pk_bf16_f32 v2, v4, v5\n\t"
"v_cvt_pk_bf16_f32 v3, v6, v7\n\t"
"ds_write_b128 v217, v[0:3] offset:0\n\t"
"v_cvt_pk_bf16_f32 v8, v8, v9\n\t"
"v_cvt_pk_bf16_f32 v9, v10, v11\n\t"
"v_cvt_pk_bf16_f32 v10, v12, v13\n\t"
"v_cvt_pk_bf16_f32 v11, v14, v15\n\t"
"ds_write_b128 v219, v[8:11] offset:0\n\t"
"v_cvt_pk_bf16_f32 v16, v16, v17\n\t"
"v_cvt_pk_bf16_f32 v17, v18, v19\n\t"
"v_cvt_pk_bf16_f32 v18, v20, v21\n\t"
"v_cvt_pk_bf16_f32 v19, v22, v23\n\t"
"ds_write_b128 v217, v[16:19] offset:8192\n\t"
"v_cvt_pk_bf16_f32 v24, v24, v25\n\t"
"v_cvt_pk_bf16_f32 v25, v26, v27\n\t"
"v_cvt_pk_bf16_f32 v26, v28, v29\n\t"
"v_cvt_pk_bf16_f32 v27, v30, v31\n\t"
"ds_write_b128 v219, v[24:27] offset:8192\n\t"
"v_add_u32 v248, 80896, v227\n\t"
"v_xor_b32 v249, 32, v248\n\t"
"v_xor_b32 v250, 64, v248\n\t"
"v_xor_b32 v251, 96, v248\n\t"
"ds_read_b64_tr_b16 a[176:177], v248 offset:0\n\t"
"ds_read_b64_tr_b16 a[178:179], v248 offset:512\n\t"
"ds_read_b64_tr_b16 a[180:181], v249 offset:0\n\t"
"ds_read_b64_tr_b16 a[182:183], v249 offset:512\n\t"
"buffer_load_dwordx4 v243, s[12:15], s22 offen offset:2176 nt lds\n\t"
"ds_read_b64_tr_b16 a[184:185], v250 offset:0\n\t"
"ds_read_b64_tr_b16 a[186:187], v250 offset:512\n\t"
"ds_read_b64_tr_b16 a[188:189], v251 offset:0\n\t"
"ds_read_b64_tr_b16 a[190:191], v251 offset:512\n\t"
"buffer_load_dwordx4 v244, s[12:15], s22 offen offset:3200 nt lds\n\t"
"ds_read_b64_tr_b16 a[192:193], v248 offset:1152\n\t"
"ds_read_b64_tr_b16 a[194:195], v248 offset:1664\n\t"
"ds_read_b64_tr_b16 a[196:197], v249 offset:1152\n\t"
"ds_read_b64_tr_b16 a[198:199], v249 offset:1664\n\t"
"s_mov_b64 exec, s[36:37]\n\t"
"buffer_load_dwordx4 v245, s[12:15], s22 offen offset:3712 nt lds\n\t"
"s_mov_b64 exec, -1\n\t"
"ds_read_b64_tr_b16 a[200:201], v250 offset:1152\n\t"
"ds_read_b64_tr_b16 a[202:203], v250 offset:1664\n\t"
"ds_read_b64_tr_b16 a[204:205], v251 offset:1152\n\t"
"ds_read_b64_tr_b16 a[206:207], v251 offset:1664\n\t"
"s_add_u32 m0, s43, 122240\n\t"
"s_mov_b64 exec, s[44:45]\n\t"
"buffer_load_dwordx4 v246, s[12:15], 0 offen offset:128 nt lds\n\t"
"s_mov_b64 exec, -1\n\t"
"s_waitcnt vmcnt(19)\n\t"
"ds_read_b64_tr_b16 a[48:49], v107 offset:0\n\t"
"ds_read_b64_tr_b16 a[50:51], v107 offset:1536\n\t"
"ds_read_b64_tr_b16 a[52:53], v107 offset:3072\n\t"
"ds_read_b64_tr_b16 a[54:55], v107 offset:4608\n\t"
"ds_read_b64_tr_b16 a[56:57], v107 offset:8\n\t"
"ds_read_b64_tr_b16 a[58:59], v107 offset:1544\n\t"
"ds_read_b64_tr_b16 a[60:61], v107 offset:3080\n\t"
"ds_read_b64_tr_b16 a[62:63], v107 offset:4616\n\t"
"ds_read_b64_tr_b16 a[64:65], v107 offset:16\n\t"
"ds_read_b64_tr_b16 a[66:67], v107 offset:1552\n\t"
"ds_read_b64_tr_b16 a[68:69], v107 offset:3088\n\t"
"ds_read_b64_tr_b16 a[70:71], v107 offset:4624\n\t"
"s_waitcnt vmcnt(13)\n\t"
"ds_read_b64_tr_b16 a[72:73], v107 offset:6144\n\t"
"ds_read_b64_tr_b16 a[74:75], v107 offset:7680\n\t"
"ds_read_b64_tr_b16 a[76:77], v107 offset:9216\n\t"
"ds_read_b64_tr_b16 a[78:79], v107 offset:10752\n\t"
"ds_read_b64_tr_b16 a[80:81], v107 offset:6152\n\t"
"ds_read_b64_tr_b16 a[82:83], v107 offset:7688\n\t"
"ds_read_b64_tr_b16 a[84:85], v107 offset:9224\n\t"
"ds_read_b64_tr_b16 a[86:87], v107 offset:10760\n\t"
"ds_read_b64_tr_b16 a[88:89], v107 offset:6160\n\t"
"ds_read_b64_tr_b16 a[90:91], v107 offset:7696\n\t"
"ds_read_b64_tr_b16 a[92:93], v107 offset:9232\n\t"
"ds_read_b64_tr_b16 a[94:95], v107 offset:10768\n\t"
"s_waitcnt lgkmcnt(15)\n\t"
"v_mfma_f32_16x16x32_bf16 v[0:3], v[110:113], a[48:51], 0\n\t"
"s_waitcnt lgkmcnt(10)\n\t"
"v_mfma_f32_16x16x32_bf16 v[0:3], v[110:113], a[72:75], v[0:3]\n\t"
"v_mfma_f32_16x16x32_bf16 v[8:11], v[108:111], a[48:51], 0\n\t"
"v_mfma_f32_16x16x32_bf16 v[8:11], v[108:111], a[72:75], v[8:11]\n\t"
"s_add_u32 m0, s26, 34816\n\t"
"s_nop 0\n\t"
"buffer_load_dwordx4 v241, s[12:15], s22 offen offset:256 nt lds\n\t"
"v_mfma_f32_16x16x32_bf16 v[16:19], v[110:113], a[52:55], 0\n\t"
"s_waitcnt lgkmcnt(8)\n\t"
"v_mfma_f32_16x16x32_bf16 v[16:19], v[110:113], a[76:79], v[16:19]\n\t"
"v_mfma_f32_16x16x32_bf16 v[24:27], v[108:111], a[52:55], 0\n\t"
"v_mfma_f32_16x16x32_bf16 v[24:27], v[108:111], a[76:79], v[24:27]\n\t"
"buffer_load_dwordx4 v242, s[12:15], s22 offen offset:1280 nt lds\n\t"
"s_waitcnt vmcnt(8)\n\t"
"ds_read_b64_tr_b16 a[0:1], v47 offset:0\n\t"
"ds_read_b64_tr_b16 a[2:3], v47 offset:1536\n\t"
"ds_read_b64_tr_b16 a[4:5], v47 offset:3072\n\t"
"ds_read_b64_tr_b16 a[6:7], v47 offset:4608\n\t"
"ds_read_b64_tr_b16 a[8:9], v47 offset:8\n\t"
"ds_read_b64_tr_b16 a[10:11], v47 offset:1544\n\t"
"ds_read_b64_tr_b16 a[12:13], v47 offset:3080\n\t"
"ds_read_b64_tr_b16 a[14:15], v47 offset:4616\n\t"
"ds_read_b64_tr_b16 a[16:17], v47 offset:16\n\t"
"ds_read_b64_tr_b16 a[18:19], v47 offset:1552\n\t"
"ds_read_b64_tr_b16 a[20:21], v47 offset:3088\n\t"
"ds_read_b64_tr_b16 a[22:23], v47 offset:4624\n\t"
"v_mfma_f32_16x16x32_bf16 v[32:35], v[110:113], a[56:59], 0\n\t"
"s_waitcnt lgkmcnt(15)\n\t"
"v_mfma_f32_16x16x32_bf16 v[32:35], v[110:113], a[80:83], v[32:35]\n\t"
"v_mfma_f32_16x16x32_bf16 v[96:99], v[108:111], a[56:59], 0\n\t"
"v_mfma_f32_16x16x32_bf16 v[96:99], v[108:111], a[80:83], v[96:99]\n\t"
"buffer_load_dwordx4 v243, s[12:15], s22 offen offset:2304 nt lds\n\t"
"v_mfma_f32_16x16x32_bf16 v[120:123], v[110:113], a[60:63], 0\n\t"
"v_mfma_f32_16x16x32_bf16 v[120:123], v[110:113], a[84:87], v[120:123]\n\t"
"v_mfma_f32_16x16x32_bf16 v[128:131], v[108:111], a[60:63], 0\n\t"
"v_mfma_f32_16x16x32_bf16 v[128:131], v[108:111], a[84:87], v[128:131]\n\t"
"buffer_load_dwordx4 v244, s[12:15], s22 offen offset:3328 nt lds\n\t"
"v_mfma_f32_16x16x32_bf16 v[136:139], v[110:113], a[64:67], 0\n\t"
"s_waitcnt lgkmcnt(14)\n\t"
"v_mfma_f32_16x16x32_bf16 v[136:139], v[110:113], a[88:91], v[136:139]\n\t"
"v_mfma_f32_16x16x32_bf16 v[152:155], v[108:111], a[64:67], 0\n\t"
"v_mfma_f32_16x16x32_bf16 v[152:155], v[108:111], a[88:91], v[152:155]\n\t"
"s_mov_b64 exec, s[36:37]\n\t"
"buffer_load_dwordx4 v245, s[12:15], s22 offen offset:3840 nt lds\n\t"
"s_mov_b64 exec, -1\n\t"
"v_mfma_f32_16x16x32_bf16 v[160:163], v[110:113], a[68:71], 0\n\t"
"s_waitcnt lgkmcnt(12)\n\t"
"v_mfma_f32_16x16x32_bf16 v[160:163], v[110:113], a[92:95], v[160:163]\n\t"
"v_mfma_f32_16x16x32_bf16 v[168:171], v[108:111], a[68:71], 0\n\t"
"v_mfma_f32_16x16x32_bf16 v[168:171], v[108:111], a[92:95], v[168:171]\n\t"
"s_add_u32 m0, s43, 32512\n\t"
"s_mov_b64 exec, s[44:45]\n\t"
"buffer_load_dwordx4 v246, s[12:15], 0 offen offset:256 nt lds\n\t"
"s_mov_b64 exec, -1\n\t"
"s_waitcnt lgkmcnt(10)\n\t"
"v_mfma_f32_16x16x32_bf16 v[4:7], v[110:113], a[0:3], v[0:3]\n\t"
"v_mfma_f32_16x16x32_bf16 v[0:3], v[114:117], a[0:3], v[0:3]\n\t"
"v_mfma_f32_16x16x32_bf16 v[12:15], v[108:111], a[0:3], v[8:11]\n\t"
"v_mfma_f32_16x16x32_bf16 v[8:11], v[112:115], a[0:3], v[8:11]\n\t"
"s_waitcnt lgkmcnt(8)\n\t"
"v_mfma_f32_16x16x32_bf16 v[20:23], v[110:113], a[4:7], v[16:19]\n\t"
"v_mfma_f32_16x16x32_bf16 v[16:19], v[114:117], a[4:7], v[16:19]\n\t"
"v_mfma_f32_16x16x32_bf16 v[28:31], v[108:111], a[4:7], v[24:27]\n\t"
"s_nop 0\n\t"
"v_cvt_pk_bf16_f32 v48, v4, v5\n\t"
"v_cvt_pk_bf16_f32 v49, v6, v7\n\t"
"v_mfma_f32_16x16x32_bf16 v[24:27], v[112:115], a[4:7], v[24:27]\n\t"
"v_cvt_pk_bf16_f32 v72, v0, v1\n\t"
"v_cvt_pk_bf16_f32 v73, v2, v3\n\t"
"s_waitcnt lgkmcnt(6)\n\t"
"v_mfma_f32_16x16x32_bf16 v[36:39], v[110:113], a[8:11], v[32:35]\n\t"
"v_cvt_pk_bf16_f32 v50, v12, v13\n\t"
"v_cvt_pk_bf16_f32 v51, v14, v15\n\t"
"v_mfma_f32_16x16x32_bf16 v[32:35], v[114:117], a[8:11], v[32:35]\n\t"
"v_cvt_pk_bf16_f32 v74, v8, v9\n\t"
"v_cvt_pk_bf16_f32 v75, v10, v11\n\t"
"v_mfma_f32_16x16x32_bf16 v[100:103], v[108:111], a[8:11], v[96:99]\n\t"
"v_cvt_pk_bf16_f32 v52, v20, v21\n\t"
"v_cvt_pk_bf16_f32 v53, v22, v23\n\t"
"v_mfma_f32_16x16x32_bf16 v[96:99], v[112:115], a[8:11], v[96:99]\n\t"
"v_cvt_pk_bf16_f32 v76, v16, v17\n\t"
"v_cvt_pk_bf16_f32 v77, v18, v19\n\t"
"s_waitcnt lgkmcnt(4)\n\t"
"v_mfma_f32_16x16x32_bf16 v[124:127], v[110:113], a[12:15], v[120:123]\n\t"
"v_cvt_pk_bf16_f32 v54, v28, v29\n\t"
"v_cvt_pk_bf16_f32 v55, v30, v31\n\t"
"v_mfma_f32_16x16x32_bf16 v[120:123], v[114:117], a[12:15], v[120:123]\n\t"
"v_cvt_pk_bf16_f32 v78, v24, v25\n\t"
"v_cvt_pk_bf16_f32 v79, v26, v27\n\t"
"v_mfma_f32_16x16x32_bf16 v[132:135], v[108:111], a[12:15], v[128:131]\n\t"
"v_cvt_pk_bf16_f32 v56, v36, v37\n\t"
"v_cvt_pk_bf16_f32 v57, v38, v39\n\t"
"v_mfma_f32_16x16x32_bf16 v[128:131], v[112:115], a[12:15], v[128:131]\n\t"
"v_cvt_pk_bf16_f32 v80, v32, v33\n\t"
"v_cvt_pk_bf16_f32 v81, v34, v35\n\t"
"s_waitcnt lgkmcnt(2)\n\t"
"v_mfma_f32_16x16x32_bf16 v[140:143], v[110:113], a[16:19], v[136:139]\n\t"
"v_cvt_pk_bf16_f32 v58, v100, v101\n\t"
"v_cvt_pk_bf16_f32 v59, v102, v103\n\t"
"v_mfma_f32_16x16x32_bf16 v[136:139], v[114:117], a[16:19], v[136:139]\n\t"
"v_cvt_pk_bf16_f32 v82, v96, v97\n\t"
"v_cvt_pk_bf16_f32 v83, v98, v99\n\t"
"v_mfma_f32_16x16x32_bf16 v[156:159], v[108:111], a[16:19], v[152:155]\n\t"
"v_cvt_pk_bf16_f32 v60, v124, v125\n\t"
"v_cvt_pk_bf16_f32 v61, v126, v127\n\t"
"v_mfma_f32_16x16x32_bf16 v[152:155], v[112:115], a[16:19], v[152:155]\n\t"
"v_cvt_pk_bf16_f32 v84, v120, v121\n\t"
"v_cvt_pk_bf16_f32 v85, v122, v123\n\t"
"s_waitcnt lgkmcnt(0)\n\t"
"v_mfma_f32_16x16x32_bf16 v[164:167], v[110:113], a[20:23], v[160:163]\n\t"
"v_cvt_pk_bf16_f32 v62, v132, v133\n\t"
"v_cvt_pk_bf16_f32 v63, v134, v135\n\t"
"v_mfma_f32_16x16x32_bf16 v[160:163], v[114:117], a[20:23], v[160:163]\n\t"
"v_cvt_pk_bf16_f32 v86, v128, v129\n\t"
"v_cvt_pk_bf16_f32 v87, v130, v131\n\t"
"v_mfma_f32_16x16x32_bf16 v[172:175], v[108:111], a[20:23], v[168:171]\n\t"
"v_cvt_pk_bf16_f32 v64, v140, v141\n\t"
"v_cvt_pk_bf16_f32 v65, v142, v143\n\t"
"v_mfma_f32_16x16x32_bf16 v[168:171], v[112:115], a[20:23], v[168:171]\n\t"
"v_cvt_pk_bf16_f32 v88, v136, v137\n\t"
"v_cvt_pk_bf16_f32 v89, v138, v139\n\t"
"v_cvt_pk_bf16_f32 v66, v156, v157\n\t"
"v_cvt_pk_bf16_f32 v67, v158, v159\n\t"
"v_cvt_pk_bf16_f32 v90, v152, v153\n\t"
"v_cvt_pk_bf16_f32 v91, v154, v155\n\t"
"v_cvt_pk_bf16_f32 v68, v164, v165\n\t"
"v_cvt_pk_bf16_f32 v69, v166, v167\n\t"
"v_cvt_pk_bf16_f32 v92, v160, v161\n\t"
"v_cvt_pk_bf16_f32 v93, v162, v163\n\t"
"v_cvt_pk_bf16_f32 v70, v172, v173\n\t"
"v_cvt_pk_bf16_f32 v71, v174, v175\n\t"
"v_cvt_pk_bf16_f32 v94, v168, v169\n\t"
"v_cvt_pk_bf16_f32 v95, v170, v171\n\t"
"s_barrier\n\t"
"v_mfma_f32_16x16x32_bf16 v[192:195], a[176:179], v[252:255], 0\n\t"
"s_nop 0\n\t"
"ds_read_b128 a[148:151], v216 offset:1024\n\t"
"ds_read_b128 a[164:167], v216 offset:9216\n\t"
"ds_read_b128 a[168:171], v216 offset:10240\n\t"
"v_mfma_f32_16x16x32_bf16 v[196:199], a[180:183], v[252:255], 0\n\t"
"s_nop 0\n\t"
"ds_read_b128 a[144:147], v216 offset:0\n\t"
"ds_read_b128 a[172:175], v216 offset:11264\n\t"
"v_mfma_f32_16x16x32_bf16 v[200:203], a[184:187], v[252:255], 0\n\t"
"s_mov_b32 s16, s80\n\t"
"ds_read_b128 a[160:163], v216 offset:8192\n\t"
"ds_read_b128 a[152:155], v216 offset:2048\n\t"
"v_mfma_f32_16x16x32_bf16 v[204:207], a[188:191], v[252:255], 0\n\t"
"s_and_b32 s17, s81, 0xffff\n\t"
"ds_read_b128 a[156:159], v216 offset:3072\n\t"
"v_add_u32 v218, 4096, v235\n\t"
"v_mfma_f32_16x16x32_bf16 v[208:211], a[192:195], v[148:151], 0\n\t"
"s_mov_b32 s18, 100663296\n\t"
"v_cvt_pk_bf16_f32 v192, v192, v193\n\t"
"v_cvt_pk_bf16_f32 v193, v194, v195\n\t"
"v_mfma_f32_16x16x32_bf16 v[212:215], a[196:199], v[148:151], 0\n\t"
"s_waitcnt lgkmcnt(2)\n\t"
"v_cvt_pk_bf16_f32 v194, v196, v197\n\t"
"v_cvt_pk_bf16_f32 v195, v198, v199\n\t"
"s_mov_b32 s19, 0x20000\n\t"
"v_mfma_f32_16x16x32_bf16 v[96:99], v[48:51], a[144:147], 0\n\t"
"s_mov_b32 s21, 4\n\t"
"v_cvt_pk_bf16_f32 v200, v200, v201\n\t"
"v_cvt_pk_bf16_f32 v201, v202, v203\n\t"
"v_mfma_f32_16x16x32_bf16 v[96:99], v[52:55], a[160:163], v[96:99]\n\t"
"s_mov_b32 s38, -2.0\n\t"
"ds_write_b128 v221, v[192:195] offset:16384\n\t"
"v_cvt_pk_bf16_f32 v202, v204, v205\n\t"
"v_mfma_f32_16x16x32_bf16 v[112:115], v[56:59], a[144:147], 0\n\t"
"s_mov_b32 s39, -2.0\n\t"
"v_mbcnt_lo_u32_b32 v176, -1, 0\n\t"
"v_mbcnt_hi_u32_b32 v176, -1, v176\n\t"
"v_mfma_f32_16x16x32_bf16 v[112:115], v[60:63], a[160:163], v[112:115]\n\t"
"v_and_b32 v153, 7, v176\n\t"
"v_and_b32 v158, 24, v176\n\t"
"v_cmp_eq_u32 vcc, 8, v158\n\t"
"v_mov_b32 v166, 0x3f80\n\t"
"v_mov_b32 v167, 0xbf80\n\t"
"v_cndmask_b32 v166, v166, v167, vcc\n\t"
"v_mov_b32 v168, v153\n\t"
"v_mov_b32 v167, 0\n\t"
"v_and_b32 v169, 1, v168\n\t"
"v_lshlrev_b32 v169, 4, v169\n\t"
"v_cvt_pk_bf16_f32 v203, v206, v207\n\t"
"v_cvt_pk_bf16_f32 v208, v208, v209\n\t"
"v_mfma_f32_16x16x32_bf16 v[128:131], v[64:67], a[144:147], 0\n\t"
"v_lshrrev_b32 v168, 1, v168\n\t"
"v_cvt_pk_bf16_f32 v209, v210, v211\n\t"
"v_mfma_f32_16x16x32_bf16 v[128:131], v[68:71], a[160:163], v[128:131]\n\t"
"ds_read_b128 a[144:147], v216 offset:4096\n\t"
"buffer_load_dword v190, v189, s[8:11], 0 offen sc1\n\t"
"ds_write_b128 v221, v[200:203] offset:24576\n\t"
"v_lshlrev_b64 v[166:167], v169, v[166:167]\n\t"
"v_cmp_eq_u32 vcc, 0, v168\n\t"
"s_nop 1\n\t"
"v_cndmask_b32 v172, 0, v166, vcc\n\t"
"v_accvgpr_write_b32 a212, v172\n\t"
"v_cvt_pk_bf16_f32 v210, v212, v213\n\t"
"v_mfma_f32_16x16x32_bf16 v[132:135], v[64:67], a[148:151], 0\n\t"
"v_cndmask_b32 v173, 0, v167, vcc\n\t"
"v_cmp_eq_u32 vcc, 1, v168\n\t"
"v_mfma_f32_16x16x32_bf16 v[132:135], v[68:71], a[164:167], v[132:135]\n\t"
"s_nop 0\n\t"
"v_cndmask_b32 v173, v173, v166, vcc\n\t"
"v_accvgpr_write_b32 a213, v173\n\t"
"v_cndmask_b32 v174, 0, v167, vcc\n\t"
"v_cmp_eq_u32 vcc, 2, v168\n\t"
"v_cvt_pk_bf16_f32 v211, v214, v215\n\t"
"v_mfma_f32_16x16x32_bf16 v[116:119], v[56:59], a[148:151], 0\n\t"
"v_cndmask_b32 v174, v174, v166, vcc\n\t"
"v_accvgpr_write_b32 a214, v174\n\t"
"v_mfma_f32_16x16x32_bf16 v[116:119], v[60:63], a[164:167], v[116:119]\n\t"
"ds_read_b128 a[160:163], v216 offset:12288\n\t"
"v_cndmask_b32 v175, 0, v167, vcc\n\t"
"s_waitcnt vmcnt(7)\n\t"
"s_barrier\n\t"
"v_mfma_f32_16x16x32_bf16 v[192:195], a[200:203], v[148:151], 0\n\t"
"ds_read_b64_tr_b16 a[176:177], v248 offset:41472\n\t"
"v_cmp_eq_u32 vcc, 3, v168\n\t"
"s_nop 1\n\t"
"v_cndmask_b32 v175, v175, v166, vcc\n\t"
"v_accvgpr_write_b32 a215, v175\n\t"
"v_mfma_f32_16x16x32_bf16 v[100:103], v[48:51], a[148:151], 0\n\t"
"ds_read_b128 a[148:151], v216 offset:5120\n\t"
"v_cmp_gt_u32 vcc, 32, v176\n\t"
"v_mfma_f32_16x16x32_bf16 v[100:103], v[52:55], a[164:167], v[100:103]\n\t"
"s_waitcnt lgkmcnt(7)\n\t"
"ds_read_b64_tr_b16 a[178:179], v248 offset:41984\n\t"
"v_lshlrev_b32 v166, 8, v172\n\t"
"v_mfma_f32_16x16x32_bf16 v[104:107], v[48:51], a[152:155], 0\n\t"
"v_and_b32 v166, 0x80008000, v166\n\t"
"v_xor_b32 v166, v166, v172\n\t"
"v_mfma_f32_16x16x32_bf16 v[104:107], v[52:55], a[168:171], v[104:107]\n\t"
"s_nop 0\n\t"
"ds_read_b128 a[164:167], v216 offset:13312\n\t"
"ds_read_b64_tr_b16 a[180:181], v249 offset:41472\n\t"
"ds_read_b64_tr_b16 a[182:183], v249 offset:41984\n\t"
"ds_read_b64_tr_b16 a[184:185], v250 offset:41472\n\t"
"ds_read_b64_tr_b16 a[186:187], v250 offset:41984\n\t"
"ds_read_b64_tr_b16 a[188:189], v251 offset:41472\n\t"
"ds_read_b64_tr_b16 a[190:191], v251 offset:41984\n\t"
"ds_write_b128 v221, v[208:211] offset:20480\n\t"
"v_mfma_f32_16x16x32_bf16 v[196:199], a[204:207], v[148:151], 0\n\t"
"v_cndmask_b32 v167, v166, v172, vcc\n\t"
"v_cvt_pk_bf16_f32 v192, v192, v193\n\t"
"v_mfma_f32_16x16x32_bf16 v[120:123], v[56:59], a[152:155], 0\n\t"
"v_cndmask_b32 v166, v172, v166, vcc\n\t"
"v_cvt_pk_bf16_f32 v193, v194, v195\n\t"
"v_mfma_f32_16x16x32_bf16 v[120:123], v[60:63], a[168:171], v[120:123]\n\t"
"v_accvgpr_write_b32 a208, v167\n\t"
"v_accvgpr_write_b32 a216, v166\n\t"
"v_mfma_f32_16x16x32_bf16 v[136:139], v[64:67], a[152:155], 0\n\t"
"ds_read_b64_tr_b16 a[192:193], v248 offset:42624\n\t"
"v_lshlrev_b32 v166, 8, v173\n\t"
"v_mfma_f32_16x16x32_bf16 v[136:139], v[68:71], a[168:171], v[136:139]\n\t"
"s_waitcnt lgkmcnt(15)\n\t"
"ds_read_b128 a[168:171], v216 offset:14336\n\t"
"v_cvt_pk_bf16_f32 v194, v196, v197\n\t"
"v_mfma_f32_16x16x32_bf16 v[140:143], v[64:67], a[156:159], 0\n\t"
"v_and_b32 v166, 0x80008000, v166\n\t"
"v_cvt_pk_bf16_f32 v195, v198, v199\n\t"
"v_mfma_f32_16x16x32_bf16 v[140:143], v[68:71], a[172:175], v[140:143]\n\t"
"ds_write_b128 v221, v[192:195] offset:28672\n\t"
"v_mfma_f32_16x16x32_bf16 v[124:127], v[56:59], a[156:159], 0\n\t"
"v_xor_b32 v166, v166, v173\n\t"
"v_cndmask_b32 v167, v166, v173, vcc\n\t"
"v_mfma_f32_16x16x32_bf16 v[124:127], v[60:63], a[172:175], v[124:127]\n\t"
"v_cndmask_b32 v166, v173, v166, vcc\n\t"
"v_accvgpr_write_b32 a209, v167\n\t"
"v_mfma_f32_16x16x32_bf16 v[108:111], v[48:51], a[156:159], 0\n\t"
"v_accvgpr_write_b32 a217, v166\n\t"
"v_lshlrev_b32 v166, 8, v174\n\t"
"v_mfma_f32_16x16x32_bf16 v[108:111], v[52:55], a[172:175], v[108:111]\n\t"
"s_waitcnt lgkmcnt(14)\n\t"
"ds_read_b64_tr_b16 a[194:195], v248 offset:43136\n\t"
"buffer_load_dword v191, v189, s[8:11], 0 offen sc1\n\t"
"v_mfma_f32_16x16x32_bf16 v[0:3], v[72:75], a[144:147], v[96:99]\n\t"
"ds_read_b64_tr_b16 a[196:197], v249 offset:42624\n\t"
"v_and_b32 v166, 0x80008000, v166\n\t"
"v_mfma_f32_16x16x32_bf16 v[0:3], v[76:79], a[160:163], v[0:3]\n\t"
"ds_read_b128 a[156:159], v216 offset:7168\n\t"
"v_xor_b32 v166, v166, v174\n\t"
"v_mfma_f32_16x16x32_bf16 v[16:19], v[80:83], a[144:147], v[112:115]\n\t"
"ds_read_b64_tr_b16 a[198:199], v249 offset:43136\n\t"
"v_cndmask_b32 v167, v166, v174, vcc\n\t"
"v_mfma_f32_16x16x32_bf16 v[16:19], v[84:87], a[160:163], v[16:19]\n\t"
"ds_read_b64_tr_b16 a[200:201], v250 offset:42624\n\t"
"v_cndmask_b32 v166, v174, v166, vcc\n\t"
"v_mfma_f32_16x16x32_bf16 v[32:35], v[88:91], a[144:147], v[128:131]\n\t"
"ds_read_b64_tr_b16 a[202:203], v250 offset:43136\n\t"
"v_accvgpr_write_b32 a210, v167\n\t"
"v_mfma_f32_16x16x32_bf16 v[32:35], v[92:95], a[160:163], v[32:35]\n\t"
"s_waitcnt lgkmcnt(15)\n\t"
"ds_read_b128 a[152:155], v216 offset:6144\n\t"
"v_accvgpr_write_b32 a218, v166\n\t"
"v_mfma_f32_16x16x32_bf16 v[36:39], v[88:91], a[148:151], v[132:135]\n\t"
"ds_read_b64_tr_b16 a[204:205], v251 offset:42624\n\t"
"v_lshlrev_b32 v166, 8, v175\n\t"
"v_mfma_f32_16x16x32_bf16 v[36:39], v[92:95], a[164:167], v[36:39]\n\t"
"ds_read_b64_tr_b16 a[206:207], v251 offset:43136\n\t"
"v_and_b32 v166, 0x80008000, v166\n\t"
"v_mfma_f32_16x16x32_bf16 v[20:23], v[80:83], a[148:151], v[116:119]\n\t"
"ds_read_b128 a[172:175], v216 offset:15360\n\t"
"v_xor_b32 v166, v166, v175\n\t"
"v_mfma_f32_16x16x32_bf16 v[20:23], v[84:87], a[164:167], v[20:23]\n\t"
"v_cndmask_b32 v167, v166, v175, vcc\n\t"
"v_cndmask_b32 v166, v175, v166, vcc\n\t"
"v_mfma_f32_16x16x32_bf16 v[4:7], v[72:75], a[148:151], v[100:103]\n\t"
"v_accvgpr_write_b32 a211, v167\n\t"
"v_accvgpr_write_b32 a219, v166\n\t"
"v_mfma_f32_16x16x32_bf16 v[4:7], v[76:79], a[164:167], v[4:7]\n\t"
"s_waitcnt lgkmcnt(0)\n\t"
"s_waitcnt vmcnt(1)\n\t"
"v_cmp_eq_u32 vcc, v190, v188\n\t"
"s_nop 4\n\t"
"s_cmp_eq_u64 vcc, 0\n\t"
"s_cbranch_scc1 Lgo_%=\n\t"
"s_sub_u32 s68, s68, 1\n\t"
"s_waitcnt vmcnt(0)\n\t"
"Lchk_%=:\n\t"
"v_cmp_eq_u32 vcc, v191, v188\n\t"
"s_nop 4\n\t"
"s_cmp_eq_u64 vcc, 0\n\t"
"s_cbranch_scc1 Lgo_%=\n\t"
"s_sub_u32 s68, s68, 1\n\t"
"s_cmp_eq_u32 s68, 0\n\t"
"s_cbranch_scc1 Lfb_%=\n\t"
"buffer_load_dword v191, v189, s[8:11], 0 offen sc1\n\t"
"s_waitcnt vmcnt(0)\n\t"
"s_branch Lchk_%=\n\t"
"Lgo_%=:\n\t"
"s_barrier\n\t"
"v_mfma_f32_16x16x32_bf16 v[8:11], v[72:75], a[152:155], v[104:107]\n\t"
"s_nop 0\n\t"
"ds_read_b128 a[144:147], v216 offset:16384\n\t"
"ds_read_b128 a[164:167], v216 offset:25600\n\t"
"v_mfma_f32_16x16x32_bf16 v[8:11], v[76:79], a[168:171], v[8:11]\n\t"
"ds_read_b128 a[160:163], v216 offset:24576\n\t"
"s_mov_b32 s54, 98304\n\t"
"buffer_load_dwordx4 a[96:99], v235, s[8:11], s54 offen offset:0 sc1\n\t"
"buffer_load_dwordx4 a[100:103], v235, s[8:11], s54 offen offset:1024 sc1\n\t"
"v_add_u32 v248, 32768, v227\n\t"
"v_mfma_f32_16x16x32_bf16 v[24:27], v[80:83], a[152:155], v[120:123]\n\t"
"ds_read_b128 a[148:151], v216 offset:17408\n\t"
"v_mbcnt_lo_u32_b32 v176, -1, 0\n\t"
"v_mfma_f32_16x16x32_bf16 v[24:27], v[84:87], a[168:171], v[24:27]\n\t"
"buffer_load_dwordx4 a[104:107], v235, s[8:11], s54 offen offset:2048 sc1\n\t"
"buffer_load_dwordx4 a[108:111], v235, s[8:11], s54 offen offset:3072 sc1\n\t"
"v_mbcnt_hi_u32_b32 v176, -1, v176\n\t"
"v_and_b32 v154, 15, v176\n\t"
"v_lshrrev_b32 v155, 4, v176\n\t"
"v_bfe_u32 v156, v176, 2, 2\n\t"
"v_and_b32 v157, 3, v176\n\t"
"v_mfma_f32_16x16x32_bf16 v[40:43], v[88:91], a[152:155], v[136:139]\n\t"
"v_lshlrev_b32 v157, 3, v157\n\t"
"v_xor_b32 v249, 32, v248\n\t"
"s_add_u32 s60, s43, 32768\n\t"
"v_mfma_f32_16x16x32_bf16 v[40:43], v[92:95], a[168:171], v[40:43]\n\t"
"s_add_u32 s61, s26, 20800\n\t"
"ds_read_b128 a[168:171], v216 offset:26624\n\t"
"v_xor_b32 v250, 64, v248\n\t"
"v_mfma_f32_16x16x32_bf16 v[200:203], a[176:179], v[252:255], 0\n\t"
"buffer_load_dwordx4 a[112:115], v218, s[8:11], s54 offen offset:0 sc1\n\t"
"buffer_load_dwordx4 a[116:119], v218, s[8:11], s54 offen offset:1024 sc1\n\t"
"s_mov_b32 s66, 172032\n\t"
"buffer_load_dwordx4 a[120:123], v235, s[8:11], s66 offen offset:0 sc1\n\t"
"v_mfma_f32_16x16x32_bf16 v[44:47], v[88:91], a[156:159], v[140:143]\n\t"
"s_mul_i32 s62, s73, 96\n\t"
"ds_read_b128 a[152:155], v216 offset:18432\n\t"
"v_xor_b32 v251, 96, v248\n\t"
"v_mfma_f32_16x16x32_bf16 v[44:47], v[92:95], a[172:175], v[44:47]\n\t"
"s_add_u32 s62, s62, 54272\n\t"
"v_min_u32 v158, 35, v176\n\t"
"v_lshl_add_u32 v222, v158, 4, s60\n\t"
"v_add_u32 v223, 66560, v222\n\t"
"v_and_b32 v158, 1, v155\n\t"
"v_add_u32 v159, v156, v158\n\t"
"v_mfma_f32_16x16x32_bf16 v[204:207], a[180:183], v[252:255], 0\n\t"
"buffer_load_dwordx4 a[124:127], v235, s[8:11], s66 offen offset:1024 sc1\n\t"
"buffer_load_dwordx4 a[128:131], v235, s[8:11], s66 offen offset:2048 sc1\n\t"
"buffer_load_dwordx4 a[132:135], v235, s[8:11], s66 offen offset:3072 sc1\n\t"
"v_lshl_add_u32 v166, v159, 7, v157\n\t"
"v_and_b32 v159, 2, v159\n\t"
"v_lshl_add_u32 v166, v159, 4, v166\n\t"
"v_xad_u32 v167, v158, 1, v156\n\t"
"v_mfma_f32_16x16x32_bf16 v[28:31], v[80:83], a[156:159], v[124:127]\n\t"
"v_lshl_add_u32 v168, v167, 7, v157\n\t"
"v_and_b32 v167, 2, v167\n\t"
"v_mfma_f32_16x16x32_bf16 v[28:31], v[84:87], a[172:175], v[28:31]\n\t"
"buffer_load_dwordx4 a[136:139], v218, s[8:11], s66 offen offset:0 sc1\n\t"
"buffer_load_dwordx4 a[140:143], v218, s[8:11], s66 offen offset:1024 sc1\n\t"
"v_lshl_add_u32 v168, v167, 4, v168\n\t"
"v_cvt_pk_bf16_f32 v200, v200, v201\n\t"
"v_mfma_f32_16x16x32_bf16 v[12:15], v[72:75], a[156:159], v[108:111]\n\t"
"v_sub_u32 v168, v168, v166\n\t"
"v_mul_u32_u24 v159, 20736, v158\n\t"
"v_mfma_f32_16x16x32_bf16 v[12:15], v[76:79], a[172:175], v[12:15]\n\t"
"v_sub_u32 v166, v166, v159\n\t"
"v_lshrrev_b32 v159, 5, v176\n\t"
"v_cvt_pk_bf16_f32 v201, v202, v203\n\t"
"v_mfma_f32_16x16x32_bf16 v[208:211], a[184:187], v[252:255], 0\n\t"
"s_waitcnt lgkmcnt(3)\n\t"
"ds_read_b128 a[172:175], v216 offset:27648\n\t"
"s_mov_b32 s69, 196608\n\t"
"buffer_load_dwordx4 a[0:3], v235, s[8:11], s69 offen offset:0 sc1\n\t"
"v_pk_fma_f32 v[96:97], v[96:97], s[38:39], v[0:1] neg_lo:[1,0,1] neg_hi:[1,0,1]\n\t"
"v_pk_fma_f32 v[98:99], v[98:99], s[38:39], v[2:3] neg_lo:[1,0,1] neg_hi:[1,0,1]\n\t"
"v_pk_fma_f32 v[112:113], v[112:113], s[38:39], v[16:17] neg_lo:[1,0,1] neg_hi:[1,0,1]\n\t"
"v_pk_fma_f32 v[114:115], v[114:115], s[38:39], v[18:19] neg_lo:[1,0,1] neg_hi:[1,0,1]\n\t"
"v_pk_fma_f32 v[128:129], v[128:129], s[38:39], v[32:33] neg_lo:[1,0,1] neg_hi:[1,0,1]\n\t"
"v_pk_fma_f32 v[130:131], v[130:131], s[38:39], v[34:35] neg_lo:[1,0,1] neg_hi:[1,0,1]\n\t"
"v_pk_fma_f32 v[132:133], v[132:133], s[38:39], v[36:37] neg_lo:[1,0,1] neg_hi:[1,0,1]\n\t"
"v_pk_fma_f32 v[134:135], v[134:135], s[38:39], v[38:39] neg_lo:[1,0,1] neg_hi:[1,0,1]\n\t"
"v_pk_fma_f32 v[116:117], v[116:117], s[38:39], v[20:21] neg_lo:[1,0,1] neg_hi:[1,0,1]\n\t"
"v_pk_fma_f32 v[118:119], v[118:119], s[38:39], v[22:23] neg_lo:[1,0,1] neg_hi:[1,0,1]\n\t"
"v_pk_fma_f32 v[100:101], v[100:101], s[38:39], v[4:5] neg_lo:[1,0,1] neg_hi:[1,0,1]\n\t"
"v_pk_fma_f32 v[102:103], v[102:103], s[38:39], v[6:7] neg_lo:[1,0,1] neg_hi:[1,0,1]\n\t"
"v_pk_fma_f32 v[104:105], v[104:105], s[38:39], v[8:9] neg_lo:[1,0,1] neg_hi:[1,0,1]\n\t"
"v_pk_fma_f32 v[106:107], v[106:107], s[38:39], v[10:11] neg_lo:[1,0,1] neg_hi:[1,0,1]\n\t"
"v_pk_fma_f32 v[120:121], v[120:121], s[38:39], v[24:25] neg_lo:[1,0,1] neg_hi:[1,0,1]\n\t"
"v_pk_fma_f32 v[122:123], v[122:123], s[38:39], v[26:27] neg_lo:[1,0,1] neg_hi:[1,0,1]\n\t"
"v_pk_fma_f32 v[136:137], v[136:137], s[38:39], v[40:41] neg_lo:[1,0,1] neg_hi:[1,0,1]\n\t"
"v_pk_fma_f32 v[138:139], v[138:139], s[38:39], v[42:43] neg_lo:[1,0,1] neg_hi:[1,0,1]\n\t"
"v_pk_fma_f32 v[140:141], v[140:141], s[38:39], v[44:45] neg_lo:[1,0,1] neg_hi:[1,0,1]\n\t"
"v_pk_fma_f32 v[142:143], v[142:143], s[38:39], v[46:47] neg_lo:[1,0,1] neg_hi:[1,0,1]\n\t"
"v_pk_fma_f32 v[124:125], v[124:125], s[38:39], v[28:29] neg_lo:[1,0,1] neg_hi:[1,0,1]\n\t"
"v_pk_fma_f32 v[126:127], v[126:127], s[38:39], v[30:31] neg_lo:[1,0,1] neg_hi:[1,0,1]\n\t"
"v_pk_fma_f32 v[108:109], v[108:109], s[38:39], v[12:13] neg_lo:[1,0,1] neg_hi:[1,0,1]\n\t"
"v_pk_fma_f32 v[110:111], v[110:111], s[38:39], v[14:15] neg_lo:[1,0,1] neg_hi:[1,0,1]\n\t"
"v_mfma_f32_16x16x32_bf16 v[0:3], a[48:51], a[144:147], v[0:3]\n\t"
"ds_read_b128 a[156:159], v216 offset:19456\n\t"
"v_xor_b32 v158, v158, v159\n\t"
"v_mfma_f32_16x16x32_bf16 v[0:3], a[52:55], a[160:163], v[0:3]\n\t"
"buffer_load_dwordx4 a[4:7], v235, s[8:11], s69 offen offset:1024 sc1\n\t"
"v_mfma_f32_16x16x32_bf16 v[212:215], a[188:191], v[252:255], 0\n\t"
"v_lshlrev_b32 v158, 6, v158\n\t"
"v_sub_u32 v166, v166, v158\n\t"
"v_mfma_f32_16x16x32_bf16 v[16:19], a[56:59], a[144:147], v[16:19]\n\t"
"v_cvt_pk_bf16_f32 v202, v204, v205\n\t"
"v_cvt_pk_bf16_f32 v208, v208, v209\n\t"
"v_mfma_f32_16x16x32_bf16 v[16:19], a[60:63], a[160:163], v[16:19]\n\t"
"buffer_load_dwordx4 a[8:11], v235, s[8:11], s69 offen offset:2048 sc1\n\t"
"v_mfma_f32_16x16x32_bf16 v[32:35], a[64:67], a[144:147], v[32:35]\n\t"
"v_lshl_add_u32 v168, v158, 1, v168\n\t"
"v_cvt_pk_bf16_f32 v203, v206, v207\n\t"
"v_mfma_f32_16x16x32_bf16 v[32:35], a[68:71], a[160:163], v[32:35]\n\t"
"v_subrev_u32 v220, 64, v168\n\t"
"v_cvt_pk_bf16_f32 v209, v210, v211\n\t"
"v_cvt_pk_bf16_f32 v210, v212, v213\n\t"
"v_mfma_f32_16x16x32_bf16 v[192:195], a[192:195], v[252:255], 0\n\t"
"s_waitcnt lgkmcnt(4)\n\t"
"ds_read_b128 a[144:147], v216 offset:20480\n\t"
"ds_write_b128 v221, v[200:203] offset:0\n\t"
"v_mul_u32_u24 v159, 1152, v159\n\t"
"v_mfma_f32_16x16x32_bf16 v[36:39], a[64:67], a[148:151], v[36:39]\n\t"
"v_add3_u32 v166, v166, v159, s61\n\t"
"v_cvt_pk_bf16_f32 v211, v214, v215\n\t"
"v_mfma_f32_16x16x32_bf16 v[36:39], a[68:71], a[164:167], v[36:39]\n\t"
"buffer_load_dwordx4 a[12:15], v235, s[8:11], s69 offen offset:3072 sc1\n\t"
"v_mfma_f32_16x16x32_bf16 v[20:23], a[56:59], a[148:151], v[20:23]\n\t"
"v_add_u32 v224, 1152, v166\n\t"
"v_add_u32 v225, v159, v166\n\t"
"v_mfma_f32_16x16x32_bf16 v[20:23], a[60:63], a[164:167], v[20:23]\n\t"
"ds_read_b128 a[160:163], v216 offset:28672\n\t"
"buffer_load_dwordx4 a[16:19], v218, s[8:11], s69 offen offset:0 sc1\n\t"
"ds_write_b128 v221, v[208:211] offset:8192\n\t"
"v_cvt_pk_bf16_f32 v192, v192, v193\n\t"
"v_mfma_f32_16x16x32_bf16 v[196:199], a[196:199], v[252:255], 0\n\t"
"s_barrier\n\t"
"v_mfma_f32_16x16x32_bf16 v[4:7], a[48:51], a[148:151], v[4:7]\n\t"
"v_mul_u32_u24 v158, 400, v154\n\t"
"v_mul_u32_u24 v159, 24, v155\n\t"
"v_mfma_f32_16x16x32_bf16 v[4:7], a[52:55], a[164:167], v[4:7]\n\t"
"v_add3_u32 v228, v158, v159, s62\n\t"
"v_cvt_pk_bf16_f32 v193, v194, v195\n\t"
"s_mul_i32 s60, s73, 3200\n\t"
"v_mfma_f32_16x16x32_bf16 v[200:203], a[200:203], v[252:255], 0\n\t"
"s_waitcnt lgkmcnt(6)\n\t"
"ds_read_b64_tr_b16 a[176:177], v248 offset:0\n\t"
"buffer_load_dwordx4 a[20:23], v218, s[8:11], s69 offen offset:1024 sc1\n\t"
"v_mfma_f32_16x16x32_bf16 v[8:11], a[48:51], a[152:155], v[8:11]\n\t"
"s_add_u32 s60, s60, 54272\n\t"
"ds_read_b128 a[148:151], v216 offset:21504\n\t"
"ds_read_b64_tr_b16 a[178:179], v248 offset:512\n\t"
"v_mfma_f32_16x16x32_bf16 v[8:11], a[52:55], a[168:171], v[8:11]\n\t"
"s_and_b32 s61, s73, 2\n\t"
"ds_read_b64_tr_b16 a[180:181], v249 offset:0\n\t"
"ds_read_b64_tr_b16 a[182:183], v249 offset:512\n\t"
"ds_read_b64_tr_b16 a[184:185], v250 offset:0\n\t"
"v_cvt_pk_bf16_f32 v194, v196, v197\n\t"
"v_mfma_f32_16x16x32_bf16 v[24:27], a[56:59], a[152:155], v[24:27]\n\t"
"s_lshl_b32 s61, s61, 7\n\t"
"ds_read_b128 a[164:167], v216 offset:29696\n\t"
"ds_read_b64_tr_b16 a[186:187], v250 offset:512\n\t"
"v_mfma_f32_16x16x32_bf16 v[24:27], a[60:63], a[168:171], v[24:27]\n\t"
"ds_read_b64_tr_b16 a[188:189], v251 offset:0\n\t"
"s_mov_b32 s81, 270336\n\t"
"buffer_load_dwordx4 a[24:27], v235, s[8:11], s81 offen offset:0 sc1\n\t"
"v_cvt_pk_bf16_f32 v195, v198, v199\n\t"
"v_mfma_f32_16x16x32_bf16 v[204:207], a[204:207], v[252:255], 0\n\t"
"s_add_u32 s60, s60, s61\n\t"
"ds_read_b64_tr_b16 a[190:191], v251 offset:512\n\t"
"ds_read_b64_tr_b16 a[192:193], v248 offset:1152\n\t"
"ds_read_b64_tr_b16 a[194:195], v248 offset:1664\n\t"
"ds_read_b64_tr_b16 a[196:197], v249 offset:1152\n\t"
"v_cvt_pk_bf16_f32 v200, v200, v201\n\t"
"v_mfma_f32_16x16x32_bf16 v[40:43], a[64:67], a[152:155], v[40:43]\n\t"
"s_mul_i32 s61, s73, 393216\n\t"
"ds_read_b64_tr_b16 a[198:199], v249 offset:1664\n\t"
"v_add_u32 v158, 0, v176\n\t"
"v_mfma_f32_16x16x32_bf16 v[40:43], a[68:71], a[168:171], v[40:43]\n\t"
"s_waitcnt lgkmcnt(15)\n\t"
"ds_read_b128 a[152:155], v216 offset:22528\n\t"
"buffer_load_dwordx4 a[28:31], v235, s[8:11], s81 offen offset:1024 sc1\n\t"
"v_mul_u32_u24 v159, 0xab, v158\n\t"
"v_lshrrev_b32 v159, 12, v159\n\t"
"v_add_lshl_u32 v166, v158, v159, 4\n\t"
"v_add_u32 v229, s60, v166\n\t"
"v_mfma_f32_16x16x32_bf16 v[44:47], a[64:67], a[156:159], v[44:47]\n\t"
"ds_read_b64_tr_b16 a[200:201], v250 offset:1152\n\t"
"v_cvt_pk_bf16_f32 v201, v202, v203\n\t"
"v_mfma_f32_16x16x32_bf16 v[44:47], a[68:71], a[172:175], v[44:47]\n\t"
"ds_read_b64_tr_b16 a[202:203], v250 offset:1664\n\t"
"v_mad_u32_u24 v158, v159, 24, v158\n\t"
"v_mfma_f32_16x16x32_bf16 v[28:31], a[56:59], a[156:159], v[28:31]\n\t"
"v_lshl_add_u32 v232, v158, 4, s61\n\t"
"v_add_u32 v158, 64, v176\n\t"
"v_mfma_f32_16x16x32_bf16 v[28:31], a[60:63], a[172:175], v[28:31]\n\t"
"ds_read_b64_tr_b16 a[204:205], v251 offset:1152\n\t"
"ds_write_b128 v221, v[192:195] offset:4096\n\t"
"v_mfma_f32_16x16x32_bf16 v[12:15], a[48:51], a[156:159], v[12:15]\n\t"
"v_mul_u32_u24 v159, 0xab, v158\n\t"
"v_cvt_pk_bf16_f32 v202, v204, v205\n\t"
"v_mfma_f32_16x16x32_bf16 v[12:15], a[52:55], a[172:175], v[12:15]\n\t"
"buffer_load_dwordx4 a[32:35], v235, s[8:11], s81 offen offset:2048 sc1\n\t"
"v_mfma_f32_16x16x32_bf16 v[96:99], a[72:75], a[144:147], v[96:99]\n\t"
"v_lshrrev_b32 v159, 12, v159\n\t"
"v_cvt_pk_bf16_f32 v203, v206, v207\n\t"
"v_mfma_f32_16x16x32_bf16 v[96:99], a[76:79], a[160:163], v[96:99]\n\t"
"ds_read_b128 a[172:175], v216 offset:31744\n\t"
"buffer_load_dwordx4 a[36:39], v235, s[8:11], s81 offen offset:3072 sc1\n\t"
"ds_write_b128 v221, v[200:203] offset:12288\n\t"
"v_mfma_f32_16x16x32_bf16 v[112:115], a[80:83], a[144:147], v[112:115]\n\t"
"v_add_lshl_u32 v166, v158, v159, 4\n\t"
"v_add_u32 v230, s60, v166\n\t"
"v_mfma_f32_16x16x32_bf16 v[112:115], a[84:87], a[160:163], v[112:115]\n\t"
"buffer_load_dwordx4 a[40:43], v218, s[8:11], s81 offen offset:0 sc1\n\t"
"v_mfma_f32_16x16x32_bf16 v[128:131], a[88:91], a[144:147], v[128:131]\n\t"
"v_mad_u32_u24 v158, v159, 24, v158\n\t"
"v_lshl_add_u32 v233, v158, 4, s61\n\t"
"v_mfma_f32_16x16x32_bf16 v[128:131], a[92:95], a[160:163], v[128:131]\n\t"
"s_waitcnt lgkmcnt(14)\n\t"
"ds_read_b128 a[168:171], v216 offset:30720\n\t"
"v_add_u32 v158, 128, v176\n\t"
"v_mfma_f32_16x16x32_bf16 v[132:135], a[88:91], a[148:151], v[132:135]\n\t"
"ds_read_b64_tr_b16 a[206:207], v251 offset:1664\n\t"
"v_mul_u32_u24 v159, 0xab, v158\n\t"
"v_mfma_f32_16x16x32_bf16 v[132:135], a[92:95], a[164:167], v[132:135]\n\t"
"buffer_load_dwordx4 a[44:47], v218, s[8:11], s81 offen offset:1024 sc1\n\t"
"v_mfma_f32_16x16x32_bf16 v[116:119], a[80:83], a[148:151], v[116:119]\n\t"
"ds_read_b128 a[156:159], v216 offset:23552\n\t"
"v_lshrrev_b32 v159, 12, v159\n\t"
"v_mfma_f32_16x16x32_bf16 v[116:119], a[84:87], a[164:167], v[116:119]\n\t"
"v_add_lshl_u32 v166, v158, v159, 4\n\t"
"v_add_u32 v231, s60, v166\n\t"
"v_mfma_f32_16x16x32_bf16 v[100:103], a[72:75], a[148:151], v[100:103]\n\t"
"v_mad_u32_u24 v158, v159, 24, v158\n\t"
"v_lshl_add_u32 v234, v158, 4, s61\n\t"
"v_mfma_f32_16x16x32_bf16 v[100:103], a[76:79], a[164:167], v[100:103]\n\t"
"s_mov_b32 s57, 122880\n\t"
"buffer_load_dwordx4 a[48:51], v235, s[8:11], s57 offen offset:0 sc1\n\t"
"s_waitcnt lgkmcnt(0)\n\t"
"s_add_u32 s71, s71, 1\n\t"
"v_mov_b32 v215, s71\n\t"
"s_mov_b64 exec, 1\n\t"
"buffer_store_dword v215, off, s[8:11], s70 sc1\n\t"
"s_mov_b64 exec, -1\n\t"
"s_barrier\n\t"
"v_mfma_f32_16x16x32_bf16 v[104:107], a[72:75], a[152:155], v[104:107]\n\t"
"ds_read_b128 a[160:163], v216 offset:8192\n\t"
"v_add_u32 v248, 122368, v226\n\t"
"v_mfma_f32_16x16x32_bf16 v[104:107], a[76:79], a[168:171], v[104:107]\n\t"
"ds_read_b128 a[144:147], v216 offset:0\n\t"
"v_xor_b32 v249, 32, v248\n\t"
"v_mfma_f32_16x16x32_bf16 v[120:123], a[80:83], a[152:155], v[120:123]\n\t"
"ds_read_b128 a[148:151], v216 offset:1024\n\t"
"v_xor_b32 v250, 64, v248\n\t"
"v_mfma_f32_16x16x32_bf16 v[120:123], a[84:87], a[168:171], v[120:123]\n\t"
"ds_read_b128 a[164:167], v216 offset:9216\n\t"
"v_accvgpr_read_b32 v172, a220\n\t"
"v_mfma_f32_16x16x32_bf16 v[136:139], a[88:91], a[152:155], v[136:139]\n\t"
"v_and_b32 v172, 0xff7fff7f, v172\n\t"
"v_xor_b32 v251, 96, v248\n\t"
"v_mfma_f32_16x16x32_bf16 v[136:139], a[92:95], a[168:171], v[136:139]\n\t"
"buffer_load_dwordx4 a[52:55], v235, s[8:11], s57 offen offset:1024 sc1\n\t"
"v_mfma_f32_16x16x32_bf16 v[208:211], a[176:179], v[252:255], 0\n\t"
"ds_read_b64_tr_b16 a[176:177], v248 offset:0\n\t"
"v_accvgpr_write_b32 a252, v172\n\t"
"v_mfma_f32_16x16x32_bf16 v[140:143], a[88:91], a[156:159], v[140:143]\n\t"
"ds_read_b64_tr_b16 a[178:179], v248 offset:512\n\t"
"v_accvgpr_read_b32 v173, a221\n\t"
"v_mfma_f32_16x16x32_bf16 v[140:143], a[92:95], a[172:175], v[140:143]\n\t"
"buffer_load_dwordx4 a[56:59], v235, s[8:11], s57 offen offset:2048 sc1\n\t"
"v_mfma_f32_16x16x32_bf16 v[212:215], a[180:183], v[252:255], 0\n\t"
"s_mov_b32 s40, 8192\n\t"
"ds_read_b128 a[152:155], v216 offset:2048\n\t"
"ds_read_b64_tr_b16 a[180:181], v249 offset:0\n\t"
"v_mfma_f32_16x16x32_bf16 v[124:127], a[80:83], a[156:159], v[124:127]\n\t"
"ds_read_b64_tr_b16 a[182:183], v249 offset:512\n\t"
"v_cvt_pk_bf16_f32 v208, v208, v209\n\t"
"v_mfma_f32_16x16x32_bf16 v[124:127], a[84:87], a[172:175], v[124:127]\n\t"
"buffer_load_dwordx4 a[60:63], v235, s[8:11], s57 offen offset:3072 sc1\n\t"
"v_mfma_f32_16x16x32_bf16 v[108:111], a[72:75], a[156:159], v[108:111]\n\t"
"ds_read_b128 a[168:171], v216 offset:10240\n\t"
"v_cvt_pk_bf16_f32 v209, v210, v211\n\t"
"v_mfma_f32_16x16x32_bf16 v[108:111], a[76:79], a[172:175], v[108:111]\n\t"
"buffer_load_dwordx4 a[64:67], v218, s[8:11], s57 offen offset:0 sc1\n\t"
"v_mfma_f32_16x16x32_bf16 v[192:195], a[184:187], v[252:255], 0\n\t"
"s_waitcnt vmcnt(28) lgkmcnt(8)\n\t"
"ds_read_b128 a[156:159], v216 offset:3072\n\t"
"v_and_b32 v173, 0xff7fff7f, v173\n\t"
"v_mfma_f32_16x16x32_bf16 v[0:3], a[96:99], a[144:147], v[0:3]\n\t"
"ds_read_b64_tr_b16 a[184:185], v250 offset:2304\n\t"
"v_cvt_pk_bf16_f32 v210, v212, v213\n\t"
"v_mfma_f32_16x16x32_bf16 v[0:3], a[100:103], a[160:163], v[0:3]\n\t"
"buffer_load_dwordx4 a[68:71], v218, s[8:11], s57 offen offset:1024 sc1\n\t"
"v_mfma_f32_16x16x32_bf16 v[196:199], a[188:191], v[252:255], 0\n\t"
"s_waitcnt vmcnt(27)\n\t"
"ds_read_b64_tr_b16 a[186:187], v250 offset:2816\n\t"
"v_accvgpr_write_b32 a253, v173\n\t"
"s_mov_b32 s41, 16384\n\t"
"v_mfma_f32_16x16x32_bf16 v[16:19], a[104:107], a[144:147], v[16:19]\n\t"
"ds_read_b128 a[172:175], v216 offset:11264\n\t"
"v_cvt_pk_bf16_f32 v211, v214, v215\n\t"
"v_mfma_f32_16x16x32_bf16 v[16:19], a[108:111], a[160:163], v[16:19]\n\t"
"s_waitcnt vmcnt(25)\n\t"
"s_add_u32 m0, s43, 142976\n\t"
"s_mov_b64 exec, s[44:45]\n\t"
"buffer_load_dwordx4 v247, s[12:15], 0 offen offset:128 nt lds\n\t"
"s_mov_b64 exec, -1\n\t"
"v_mfma_f32_16x16x32_bf16 v[32:35], a[112:115], a[144:147], v[32:35]\n\t"
"v_accvgpr_read_b32 v174, a222\n\t"
"v_and_b32 v174, 0xff7fff7f, v174\n\t"
"v_mfma_f32_16x16x32_bf16 v[32:35], a[116:119], a[160:163], v[32:35]\n\t"
"s_mov_b32 s42, 24576\n\t"
"ds_read_b128 a[160:163], v216 offset:12288\n\t"
"ds_read_b64_tr_b16 a[188:189], v251 offset:2304\n\t"
"v_mfma_f32_16x16x32_bf16 v[200:203], a[192:195], v[252:255], 0\n\t"
"s_waitcnt lgkmcnt(12)\n\t"
"ds_read_b128 a[144:147], v216 offset:4096\n\t"
"v_cvt_pk_bf16_f32 v192, v192, v193\n\t"
"v_mfma_f32_16x16x32_bf16 v[36:39], a[112:115], a[148:151], v[36:39]\n\t"
"v_cvt_pk_bf16_f32 v193, v194, v195\n\t"
"v_cvt_pk_bf16_f32 v194, v196, v197\n\t"
"v_mfma_f32_16x16x32_bf16 v[36:39], a[116:119], a[164:167], v[36:39]\n\t"
"s_mov_b32 s46, 32768\n\t"
"ds_write_b128 v221, v[208:211] offset:16384\n\t"
"v_mfma_f32_16x16x32_bf16 v[20:23], a[104:107], a[148:151], v[20:23]\n\t"
"v_accvgpr_write_b32 a254, v174\n\t"
"v_accvgpr_read_b32 v175, a223\n\t"
"v_mfma_f32_16x16x32_bf16 v[20:23], a[108:111], a[164:167], v[20:23]\n\t"
"v_and_b32 v175, 0xff7fff7f, v175\n\t"
"v_cvt_pk_bf16_f32 v195, v198, v199\n\t"
"v_mfma_f32_16x16x32_bf16 v[204:207], a[196:199], v[252:255], 0\n\t"
"ds_write_b128 v221, v[192:195] offset:24576\n\t"
"v_mfma_f32_16x16x32_bf16 v[4:7], a[96:99], a[148:151], v[4:7]\n\t"
"v_accvgpr_write_b32 a255, v175\n\t"
"v_cvt_pk_bf16_f32 v200, v200, v201\n\t"
"s_mov_b32 s90, 0.5\n\t"
"v_mfma_f32_16x16x32_bf16 v[4:7], a[100:103], a[164:167], v[4:7]\n\t"
"s_add_u32 m0, s26, 145280\n\t"
"s_nop 0\n\t"
"buffer_load_dwordx4 v236, s[12:15], s22 offen offset:128 nt lds\n\t"
"v_mfma_f32_16x16x32_bf16 v[208:211], a[200:203], v[252:255], 0\n\t"
"s_waitcnt lgkmcnt(9)\n\t"
"s_mov_b32 s91, 0.5\n\t"
"s_add_u32 s84, s8, 18432\n\t"
"s_addc_u32 s85, s9, 0\n\t"
"v_mfma_f32_16x16x32_bf16 v[8:11], a[96:99], a[152:155], v[8:11]\n\t"
"s_mov_b64 s[86:87], s[10:11]\n\t"
"ds_read_b64_tr_b16 a[190:191], v251 offset:2816\n\t"
"ds_read_b64_tr_b16 a[192:193], v250 offset:0\n\t"
"v_mfma_f32_16x16x32_bf16 v[8:11], a[100:103], a[168:171], v[8:11]\n\t"
"s_mov_b32 s47, 40960\n\t"
"ds_read_b64_tr_b16 a[194:195], v250 offset:512\n\t"
"v_cvt_pk_bf16_f32 v201, v202, v203\n\t"
"v_mfma_f32_16x16x32_bf16 v[24:27], a[104:107], a[152:155], v[24:27]\n\t"
"s_xor_b64 s[88:89], s[84:85], s[8:9]\n\t"
"ds_read_b128 a[164:167], v216 offset:13312\n\t"
"ds_read_b64_tr_b16 a[196:197], v251 offset:0\n\t"
"v_mfma_f32_16x16x32_bf16 v[24:27], a[108:111], a[168:171], v[24:27]\n\t"
"s_add_u32 s25, s24, 0xffeb0000\n\t"
"ds_read_b64_tr_b16 a[198:199], v251 offset:512\n\t"
"ds_read_b64_tr_b16 a[200:201], v248 offset:2304\n\t"
"v_mfma_f32_16x16x32_bf16 v[212:215], a[204:207], v[252:255], 0\n\t"
"s_mov_b32 s48, 49152\n\t"
"ds_read_b128 a[148:151], v216 offset:5120\n\t"
"v_cvt_pk_bf16_f32 v202, v204, v205\n\t"
"v_mfma_f32_16x16x32_bf16 v[40:43], a[112:115], a[152:155], v[40:43]\n\t"
"s_add_u32 s27, s24, 0xffe80000\n\t"
"ds_read_b64_tr_b16 a[202:203], v248 offset:2816\n\t"
"ds_read_b64_tr_b16 a[204:205], v249 offset:2304\n\t"
"v_mfma_f32_16x16x32_bf16 v[40:43], a[116:119], a[168:171], v[40:43]\n\t"
"s_waitcnt lgkmcnt(15)\n\t"
"ds_read_b128 a[152:155], v216 offset:6144\n\t"
"s_add_u32 s20, s24, 0xffd30000\n\t"
"v_mfma_f32_16x16x32_bf16 v[44:47], a[112:115], a[156:159], v[44:47]\n\t"
"v_cvt_pk_bf16_f32 v203, v206, v207\n\t"
"v_cvt_pk_bf16_f32 v208, v208, v209\n\t"
"v_mfma_f32_16x16x32_bf16 v[44:47], a[116:119], a[172:175], v[44:47]\n\t"
"ds_write_b128 v221, v[200:203] offset:20480\n\t"
"v_mfma_f32_16x16x32_bf16 v[28:31], a[104:107], a[156:159], v[28:31]\n\t"
"s_mov_b32 s49, 57344\n\t"
"v_mfma_f32_16x16x32_bf16 v[28:31], a[108:111], a[172:175], v[28:31]\n\t"
"v_cvt_pk_bf16_f32 v209, v210, v211\n\t"
"v_cvt_pk_bf16_f32 v210, v212, v213\n\t"
"v_mfma_f32_16x16x32_bf16 v[12:15], a[96:99], a[156:159], v[12:15]\n\t"
"v_cvt_pk_bf16_f32 v211, v214, v215\n\t"
"v_mfma_f32_16x16x32_bf16 v[12:15], a[100:103], a[172:175], v[12:15]\n\t"
"s_waitcnt vmcnt(25) lgkmcnt(14)\n\t"
"ds_write_b128 v221, v[208:211] offset:28672\n\t"
"v_mfma_f32_16x16x32_bf16 v[96:99], a[120:123], a[144:147], v[96:99]\n\t"
"v_mfma_f32_16x16x32_bf16 v[96:99], a[124:127], a[160:163], v[96:99]\n\t"
"s_waitcnt vmcnt(23)\n\t"
"buffer_load_dwordx4 v237, s[12:15], s22 offen offset:1152 nt lds\n\t"
"v_mfma_f32_16x16x32_bf16 v[112:115], a[128:131], a[144:147], v[112:115]\n\t"
"s_add_u32 s24, s24, 0xffd00000\n\t"
"v_mfma_f32_16x16x32_bf16 v[112:115], a[132:135], a[160:163], v[112:115]\n\t"
"s_waitcnt vmcnt(22)\n\t"
"ds_read_b128 a[156:159], v216 offset:7168\n\t"
"v_mfma_f32_16x16x32_bf16 v[128:131], a[136:139], a[144:147], v[128:131]\n\t"
"s_mov_b32 s50, 65536\n\t"
"ds_read_b128 a[168:171], v216 offset:14336\n\t"
"ds_read_b128 a[172:175], v216 offset:15360\n\t"
"v_mfma_f32_16x16x32_bf16 v[128:131], a[140:143], a[160:163], v[128:131]\n\t"
"s_waitcnt lgkmcnt(8)\n\t"
"ds_read_b64_tr_b16 a[206:207], v249 offset:2816\n\t"
"v_mfma_f32_16x16x32_bf16 v[132:135], a[136:139], a[148:151], v[132:135]\n\t"
"v_mfma_f32_16x16x32_bf16 v[132:135], a[140:143], a[164:167], v[132:135]\n\t"
"v_mfma_f32_16x16x32_bf16 v[116:119], a[128:131], a[148:151], v[116:119]\n\t"
"v_mfma_f32_16x16x32_bf16 v[116:119], a[132:135], a[164:167], v[116:119]\n\t"
"v_mfma_f32_16x16x32_bf16 v[100:103], a[120:123], a[148:151], v[100:103]\n\t"
"v_mfma_f32_16x16x32_bf16 v[100:103], a[124:127], a[164:167], v[100:103]\n\t"
"s_waitcnt lgkmcnt(1)\n\t"
"s_barrier\n\t"
"v_mfma_f32_16x16x32_bf16 v[104:107], a[120:123], a[152:155], v[104:107]\n\t"
"ds_read_b128 a[144:147], v216 offset:16384\n\t"
"v_add_u32 v248, 32768, v226\n\t"
"v_mfma_f32_16x16x32_bf16 v[104:107], a[124:127], a[168:171], v[104:107]\n\t"
"ds_read_b128 a[160:163], v216 offset:24576\n\t"
"v_xor_b32 v249, 32, v248\n\t"
"v_mfma_f32_16x16x32_bf16 v[120:123], a[128:131], a[152:155], v[120:123]\n\t"
"ds_read_b128 a[148:151], v216 offset:17408\n\t"
"v_xor_b32 v250, 64, v248\n\t"
"v_mfma_f32_16x16x32_bf16 v[120:123], a[132:135], a[168:171], v[120:123]\n\t"
"ds_read_b128 a[164:167], v216 offset:25600\n\t"
"v_mfma_f32_16x16x32_bf16 v[136:139], a[136:139], a[152:155], v[136:139]\n\t"
"s_mov_b32 s51, 73728\n\t"
"ds_read_b128 a[152:155], v216 offset:18432\n\t"
"v_xor_b32 v251, 96, v248\n\t"
"v_mfma_f32_16x16x32_bf16 v[136:139], a[140:143], a[168:171], v[136:139]\n\t"
"ds_read_b128 a[168:171], v216 offset:26624\n\t"
"v_mfma_f32_16x16x32_bf16 v[192:195], a[176:179], a[252:255], 0\n\t"
"buffer_load_dwordx4 v238, s[12:15], s22 offen offset:2176 nt lds\n\t"
"v_mfma_f32_16x16x32_bf16 v[140:143], a[136:139], a[156:159], v[140:143]\n\t"
"ds_read_b64_tr_b16 a[176:177], v248 offset:0\n\t"
"v_mfma_f32_16x16x32_bf16 v[140:143], a[140:143], a[172:175], v[140:143]\n\t"
"ds_read_b64_tr_b16 a[178:179], v248 offset:512\n\t"
"v_mfma_f32_16x16x32_bf16 v[196:199], a[180:183], a[252:255], 0\n\t"
"ds_read_b64_tr_b16 a[180:181], v249 offset:0\n\t"
"v_mfma_f32_16x16x32_bf16 v[124:127], a[128:131], a[156:159], v[124:127]\n\t"
"v_mfma_f32_16x16x32_bf16 v[124:127], a[132:135], a[172:175], v[124:127]\n\t"
"ds_read_b64_tr_b16 a[182:183], v249 offset:512\n\t"
"v_cvt_pk_bf16_f32 v192, v192, v193\n\t"
"v_mfma_f32_16x16x32_bf16 v[108:111], a[120:123], a[156:159], v[108:111]\n\t"
"ds_read_b128 a[156:159], v216 offset:19456\n\t"
"v_mfma_f32_16x16x32_bf16 v[108:111], a[124:127], a[172:175], v[108:111]\n\t"
"ds_read_b128 a[172:175], v216 offset:27648\n\t"
"v_cvt_pk_bf16_f32 v193, v194, v195\n\t"
"v_mfma_f32_16x16x32_bf16 v[200:203], a[184:187], a[252:255], 0\n\t"
"s_waitcnt vmcnt(21) lgkmcnt(10)\n\t"
"ds_read_b64_tr_b16 a[184:185], v250 offset:2304\n\t"
"v_cvt_pk_bf16_f32 v194, v196, v197\n\t"
"v_mfma_f32_16x16x32_bf16 v[0:3], a[0:3], a[144:147], v[0:3]\n\t"
"ds_read_b64_tr_b16 a[186:187], v250 offset:2816\n\t"
"v_mfma_f32_16x16x32_bf16 v[0:3], a[4:7], a[160:163], v[0:3]\n\t"
"v_cvt_pk_bf16_f32 v195, v198, v199\n\t"
"v_mfma_f32_16x16x32_bf16 v[204:207], a[188:191], a[252:255], 0\n\t"
"s_waitcnt vmcnt(19)\n\t"
"ds_read_b64_tr_b16 a[188:189], v251 offset:2304\n\t"
"s_mov_b32 s52, 81920\n\t"
"v_mfma_f32_16x16x32_bf16 v[16:19], a[8:11], a[144:147], v[16:19]\n\t"
"s_mov_b32 s53, 90112\n\t"
"v_cvt_pk_bf16_f32 v200, v200, v201\n\t"
"v_cvt_pk_bf16_f32 v201, v202, v203\n\t"
"v_mfma_f32_16x16x32_bf16 v[16:19], a[12:15], a[160:163], v[16:19]\n\t"
"s_waitcnt vmcnt(17)\n\t"
"ds_read_b64_tr_b16 a[190:191], v251 offset:2816\n\t"
"v_mfma_f32_16x16x32_bf16 v[32:35], a[16:19], a[144:147], v[32:35]\n\t"
"ds_read_b128 a[144:147], v216 offset:20480\n\t"
"v_mfma_f32_16x16x32_bf16 v[32:35], a[20:23], a[160:163], v[32:35]\n\t"
"ds_read_b128 a[160:163], v216 offset:28672\n\t"
"v_cvt_pk_bf16_f32 v202, v204, v205\n\t"
"v_mfma_f32_16x16x32_bf16 v[208:211], a[192:195], a[252:255], 0\n\t"
"s_waitcnt lgkmcnt(14)\n\t"
"ds_read_b64_tr_b16 a[192:193], v250 offset:0\n\t"
"ds_read_b64_tr_b16 a[194:195], v250 offset:512\n\t"
"s_mov_b32 s55, 106496\n\t"
"v_mfma_f32_16x16x32_bf16 v[36:39], a[16:19], a[148:151], v[36:39]\n\t"
"v_cvt_pk_bf16_f32 v203, v206, v207\n\t"
"v_mfma_f32_16x16x32_bf16 v[36:39], a[20:23], a[164:167], v[36:39]\n\t"
"ds_write_b128 v217, v[192:195] offset:0\n\t"
"v_mfma_f32_16x16x32_bf16 v[20:23], a[8:11], a[148:151], v[20:23]\n\t"
"v_mfma_f32_16x16x32_bf16 v[20:23], a[12:15], a[164:167], v[20:23]\n\t"
"buffer_load_dwordx4 v239, s[12:15], s22 offen offset:3200 nt lds\n\t"
"v_mfma_f32_16x16x32_bf16 v[212:215], a[196:199], a[252:255], 0\n\t"
"ds_write_b128 v219, v[200:203] offset:0\n\t"
"v_mfma_f32_16x16x32_bf16 v[4:7], a[0:3], a[148:151], v[4:7]\n\t"
"v_cvt_pk_bf16_f32 v208, v208, v209\n\t"
"v_cvt_pk_bf16_f32 v209, v210, v211\n\t"
"v_mfma_f32_16x16x32_bf16 v[4:7], a[4:7], a[164:167], v[4:7]\n\t"
"v_mfma_f32_16x16x32_bf16 v[192:195], a[200:203], a[252:255], 0\n\t"
"s_waitcnt lgkmcnt(15)\n\t"
"v_mfma_f32_16x16x32_bf16 v[8:11], a[0:3], a[152:155], v[8:11]\n\t"
"ds_read_b128 a[148:151], v216 offset:21504\n\t"
"v_cvt_pk_bf16_f32 v210, v212, v213\n\t"
"v_mfma_f32_16x16x32_bf16 v[8:11], a[4:7], a[168:171], v[8:11]\n\t"
"ds_read_b64_tr_b16 a[196:197], v251 offset:0\n\t"
"v_cvt_pk_bf16_f32 v211, v214, v215\n\t"
"v_mfma_f32_16x16x32_bf16 v[24:27], a[8:11], a[152:155], v[24:27]\n\t"
"s_mov_b32 s56, 114688\n\t"
"ds_read_b64_tr_b16 a[198:199], v251 offset:512\n\t"
"ds_read_b64_tr_b16 a[200:201], v248 offset:2304\n\t"
"v_mfma_f32_16x16x32_bf16 v[24:27], a[12:15], a[168:171], v[24:27]\n\t"
"ds_read_b128 a[164:167], v216 offset:29696\n\t"
"v_mfma_f32_16x16x32_bf16 v[196:199], a[204:207], a[252:255], 0\n\t"
"ds_read_b64_tr_b16 a[202:203], v248 offset:2816\n\t"
"v_cvt_pk_bf16_f32 v192, v192, v193\n\t"
"v_mfma_f32_16x16x32_bf16 v[40:43], a[16:19], a[152:155], v[40:43]\n\t"
"s_mov_b32 s58, 131072\n\t"
"ds_read_b128 a[152:155], v216 offset:22528\n\t"
"ds_read_b64_tr_b16 a[204:205], v249 offset:2304\n\t"
"v_mfma_f32_16x16x32_bf16 v[40:43], a[20:23], a[168:171], v[40:43]\n\t"
"s_waitcnt lgkmcnt(15)\n\t"
"ds_read_b64_tr_b16 a[206:207], v249 offset:2816\n\t"
"v_cvt_pk_bf16_f32 v193, v194, v195\n\t"
"v_mfma_f32_16x16x32_bf16 v[44:47], a[16:19], a[156:159], v[44:47]\n\t"
"ds_read_b128 a[168:171], v216 offset:30720\n\t"
"v_mfma_f32_16x16x32_bf16 v[44:47], a[20:23], a[172:175], v[44:47]\n\t"
"s_mov_b64 exec, s[36:37]\n\t"
"buffer_load_dwordx4 v240, s[12:15], s22 offen offset:3712 nt lds\n\t"
"s_mov_b64 exec, -1\n\t"
"v_mfma_f32_16x16x32_bf16 v[28:31], a[8:11], a[156:159], v[28:31]\n\t"
"v_cvt_pk_bf16_f32 v194, v196, v197\n\t"
"v_cvt_pk_bf16_f32 v195, v198, v199\n\t"
"v_mfma_f32_16x16x32_bf16 v[28:31], a[12:15], a[172:175], v[28:31]\n\t"
"s_mov_b32 s59, 139264\n\t"
"ds_write_b128 v217, v[208:211] offset:8192\n\t"
"v_mfma_f32_16x16x32_bf16 v[12:15], a[0:3], a[156:159], v[12:15]\n\t"
"v_mfma_f32_16x16x32_bf16 v[12:15], a[4:7], a[172:175], v[12:15]\n\t"
"s_waitcnt vmcnt(17) lgkmcnt(15)\n\t"
"v_mfma_f32_16x16x32_bf16 v[96:99], a[24:27], a[144:147], v[96:99]\n\t"
"v_mfma_f32_16x16x32_bf16 v[96:99], a[28:31], a[160:163], v[96:99]\n\t"
"s_waitcnt vmcnt(15)\n\t"
"ds_write_b128 v219, v[192:195] offset:8192\n\t"
"v_mfma_f32_16x16x32_bf16 v[112:115], a[32:35], a[144:147], v[112:115]\n\t"
"s_mov_b32 s63, 147456\n\t"
"v_mfma_f32_16x16x32_bf16 v[112:115], a[36:39], a[160:163], v[112:115]\n\t"
"s_waitcnt vmcnt(13)\n\t"
"v_mfma_f32_16x16x32_bf16 v[128:131], a[40:43], a[144:147], v[128:131]\n\t"
"v_mfma_f32_16x16x32_bf16 v[128:131], a[44:47], a[160:163], v[128:131]\n\t"
"s_waitcnt lgkmcnt(7)\n\t"
"buffer_load_dwordx4 a[72:75], v235, s[8:11], s63 offen offset:0 sc1\n\t"
"v_mfma_f32_16x16x32_bf16 v[132:135], a[40:43], a[148:151], v[132:135]\n\t"
"ds_read_b128 a[156:159], v216 offset:23552\n\t"
"v_mfma_f32_16x16x32_bf16 v[132:135], a[44:47], a[164:167], v[132:135]\n\t"
"buffer_load_dwordx4 a[76:79], v235, s[8:11], s63 offen offset:1024 sc1\n\t"
"v_mfma_f32_16x16x32_bf16 v[116:119], a[32:35], a[148:151], v[116:119]\n\t"
"ds_read_b128 a[172:175], v216 offset:31744\n\t"
"v_mfma_f32_16x16x32_bf16 v[116:119], a[36:39], a[164:167], v[116:119]\n\t"
"v_mfma_f32_16x16x32_bf16 v[100:103], a[24:27], a[148:151], v[100:103]\n\t"
"v_mfma_f32_16x16x32_bf16 v[100:103], a[28:31], a[164:167], v[100:103]\n\t"
"buffer_load_dwordx4 a[80:83], v235, s[8:11], s63 offen offset:2048 sc1\n\t"
"s_waitcnt lgkmcnt(0)\n\t"
"s_add_u32 s71, s71, 1\n\t"
"v_mov_b32 v215, s71\n\t"
"s_mov_b64 exec, 1\n\t"
"buffer_store_dword v215, off, s[8:11], s70 sc1\n\t"
"s_mov_b64 exec, -1\n\t"
"s_barrier\n\t"
"v_mfma_f32_16x16x32_bf16 v[104:107], a[24:27], a[152:155], v[104:107]\n\t"
"ds_read_b128 a[144:147], v216 offset:0\n\t"
"v_add_u32 v248, 80896, v224\n\t"
"v_mfma_f32_16x16x32_bf16 v[104:107], a[28:31], a[168:171], v[104:107]\n\t"
"ds_read_b128 a[148:151], v216 offset:1024\n\t"
"v_xor_b32 v249, 32, v248\n\t"
"v_mfma_f32_16x16x32_bf16 v[120:123], a[32:35], a[152:155], v[120:123]\n\t"
"ds_read_b128 a[160:163], v216 offset:8192\n\t"
"v_xor_b32 v250, 64, v248\n\t"
"v_mfma_f32_16x16x32_bf16 v[120:123], a[36:39], a[168:171], v[120:123]\n\t"
"ds_read_b128 a[164:167], v216 offset:9216\n\t"
"v_mfma_f32_16x16x32_bf16 v[136:139], a[40:43], a[152:155], v[136:139]\n\t"
"buffer_load_dwordx4 a[84:87], v235, s[8:11], s63 offen offset:3072 sc1\n\t"
"v_mfma_f32_16x16x32_bf16 v[136:139], a[44:47], a[168:171], v[136:139]\n\t"
"ds_read_b128 a[152:155], v216 offset:2048\n\t"
"v_xor_b32 v251, 96, v248\n\t"
"v_mfma_f32_16x16x32_bf16 v[200:203], a[176:179], a[252:255], 0\n\t"
"buffer_load_dwordx4 a[88:91], v218, s[8:11], s63 offen offset:0 sc1\n\t"
"v_mfma_f32_16x16x32_bf16 v[140:143], a[40:43], a[156:159], v[140:143]\n\t"
"ds_read_b128 a[168:171], v216 offset:10240\n\t"
"v_mfma_f32_16x16x32_bf16 v[140:143], a[44:47], a[172:175], v[140:143]\n\t"
"buffer_load_dwordx4 a[92:95], v218, s[8:11], s63 offen offset:1024 sc1\n\t"
"v_mfma_f32_16x16x32_bf16 v[204:207], a[180:183], a[252:255], 0\n\t"
"v_mfma_f32_16x16x32_bf16 v[124:127], a[32:35], a[156:159], v[124:127]\n\t"
"s_nop 0\n\t"
"v_cvt_pk_bf16_f32 v200, v200, v201\n\t"
"v_mfma_f32_16x16x32_bf16 v[124:127], a[36:39], a[172:175], v[124:127]\n\t"
"s_mov_b32 s75, 221184\n\t"
"buffer_load_dwordx4 a[96:99], v235, s[8:11], s75 offen offset:0 sc1\n\t"
"v_mfma_f32_16x16x32_bf16 v[108:111], a[24:27], a[156:159], v[108:111]\n\t"
"ds_read_b128 a[156:159], v216 offset:3072\n\t"
"v_cvt_pk_bf16_f32 v201, v202, v203\n\t"
"v_mfma_f32_16x16x32_bf16 v[108:111], a[28:31], a[172:175], v[108:111]\n\t"
"buffer_load_dwordx4 a[100:103], v235, s[8:11], s75 offen offset:1024 sc1\n\t"
"v_mfma_f32_16x16x32_bf16 v[208:211], a[184:187], a[252:255], 0\n\t"
"s_waitcnt vmcnt(19) lgkmcnt(4)\n\t"
"ds_read_b128 a[172:175], v216 offset:11264\n\t"
"v_pk_add_f32 v[96:97], v[0:1], v[96:97] neg_lo:[0,1] neg_hi:[0,1]\n\t"
"v_pk_add_f32 v[98:99], v[2:3], v[98:99] neg_lo:[0,1] neg_hi:[0,1]\n\t"
"v_pk_fma_f32 v[0:1], v[96:97], s[90:91], v[0:1] neg_lo:[1,0,0] neg_hi:[1,0,0]\n\t"
"v_pk_fma_f32 v[2:3], v[98:99], s[90:91], v[2:3] neg_lo:[1,0,0] neg_hi:[1,0,0]\n\t"
"v_pk_add_f32 v[112:113], v[16:17], v[112:113] neg_lo:[0,1] neg_hi:[0,1]\n\t"
"v_pk_add_f32 v[114:115], v[18:19], v[114:115] neg_lo:[0,1] neg_hi:[0,1]\n\t"
"v_pk_fma_f32 v[16:17], v[112:113], s[90:91], v[16:17] neg_lo:[1,0,0] neg_hi:[1,0,0]\n\t"
"v_pk_fma_f32 v[18:19], v[114:115], s[90:91], v[18:19] neg_lo:[1,0,0] neg_hi:[1,0,0]\n\t"
"v_pk_add_f32 v[128:129], v[32:33], v[128:129] neg_lo:[0,1] neg_hi:[0,1]\n\t"
"v_pk_add_f32 v[130:131], v[34:35], v[130:131] neg_lo:[0,1] neg_hi:[0,1]\n\t"
"v_pk_fma_f32 v[32:33], v[128:129], s[90:91], v[32:33] neg_lo:[1,0,0] neg_hi:[1,0,0]\n\t"
"v_pk_fma_f32 v[34:35], v[130:131], s[90:91], v[34:35] neg_lo:[1,0,0] neg_hi:[1,0,0]\n\t"
"v_pk_add_f32 v[132:133], v[36:37], v[132:133] neg_lo:[0,1] neg_hi:[0,1]\n\t"
"v_pk_add_f32 v[134:135], v[38:39], v[134:135] neg_lo:[0,1] neg_hi:[0,1]\n\t"
"v_pk_fma_f32 v[36:37], v[132:133], s[90:91], v[36:37] neg_lo:[1,0,0] neg_hi:[1,0,0]\n\t"
"v_pk_fma_f32 v[38:39], v[134:135], s[90:91], v[38:39] neg_lo:[1,0,0] neg_hi:[1,0,0]\n\t"
"v_pk_add_f32 v[116:117], v[20:21], v[116:117] neg_lo:[0,1] neg_hi:[0,1]\n\t"
"v_pk_add_f32 v[118:119], v[22:23], v[118:119] neg_lo:[0,1] neg_hi:[0,1]\n\t"
"v_pk_fma_f32 v[20:21], v[116:117], s[90:91], v[20:21] neg_lo:[1,0,0] neg_hi:[1,0,0]\n\t"
"v_pk_fma_f32 v[22:23], v[118:119], s[90:91], v[22:23] neg_lo:[1,0,0] neg_hi:[1,0,0]\n\t"
"v_pk_add_f32 v[100:101], v[4:5], v[100:101] neg_lo:[0,1] neg_hi:[0,1]\n\t"
"v_pk_add_f32 v[102:103], v[6:7], v[102:103] neg_lo:[0,1] neg_hi:[0,1]\n\t"
"v_pk_fma_f32 v[4:5], v[100:101], s[90:91], v[4:5] neg_lo:[1,0,0] neg_hi:[1,0,0]\n\t"
"v_pk_fma_f32 v[6:7], v[102:103], s[90:91], v[6:7] neg_lo:[1,0,0] neg_hi:[1,0,0]\n\t"
"v_pk_add_f32 v[104:105], v[8:9], v[104:105] neg_lo:[0,1] neg_hi:[0,1]\n\t"
"v_pk_add_f32 v[106:107], v[10:11], v[106:107] neg_lo:[0,1] neg_hi:[0,1]\n\t"
"v_pk_fma_f32 v[8:9], v[104:105], s[90:91], v[8:9] neg_lo:[1,0,0] neg_hi:[1,0,0]\n\t"
"v_pk_fma_f32 v[10:11], v[106:107], s[90:91], v[10:11] neg_lo:[1,0,0] neg_hi:[1,0,0]\n\t"
"v_pk_add_f32 v[120:121], v[24:25], v[120:121] neg_lo:[0,1] neg_hi:[0,1]\n\t"
"v_pk_add_f32 v[122:123], v[26:27], v[122:123] neg_lo:[0,1] neg_hi:[0,1]\n\t"
"v_pk_fma_f32 v[24:25], v[120:121], s[90:91], v[24:25] neg_lo:[1,0,0] neg_hi:[1,0,0]\n\t"
"v_pk_fma_f32 v[26:27], v[122:123], s[90:91], v[26:27] neg_lo:[1,0,0] neg_hi:[1,0,0]\n\t"
"v_pk_add_f32 v[136:137], v[40:41], v[136:137] neg_lo:[0,1] neg_hi:[0,1]\n\t"
"v_pk_add_f32 v[138:139], v[42:43], v[138:139] neg_lo:[0,1] neg_hi:[0,1]\n\t"
"v_pk_fma_f32 v[40:41], v[136:137], s[90:91], v[40:41] neg_lo:[1,0,0] neg_hi:[1,0,0]\n\t"
"v_pk_fma_f32 v[42:43], v[138:139], s[90:91], v[42:43] neg_lo:[1,0,0] neg_hi:[1,0,0]\n\t"
"v_pk_add_f32 v[140:141], v[44:45], v[140:141] neg_lo:[0,1] neg_hi:[0,1]\n\t"
"v_pk_add_f32 v[142:143], v[46:47], v[142:143] neg_lo:[0,1] neg_hi:[0,1]\n\t"
"v_pk_fma_f32 v[44:45], v[140:141], s[90:91], v[44:45] neg_lo:[1,0,0] neg_hi:[1,0,0]\n\t"
"v_pk_fma_f32 v[46:47], v[142:143], s[90:91], v[46:47] neg_lo:[1,0,0] neg_hi:[1,0,0]\n\t"
"v_pk_add_f32 v[124:125], v[28:29], v[124:125] neg_lo:[0,1] neg_hi:[0,1]\n\t"
"v_pk_add_f32 v[126:127], v[30:31], v[126:127] neg_lo:[0,1] neg_hi:[0,1]\n\t"
"v_pk_fma_f32 v[28:29], v[124:125], s[90:91], v[28:29] neg_lo:[1,0,0] neg_hi:[1,0,0]\n\t"
"v_pk_fma_f32 v[30:31], v[126:127], s[90:91], v[30:31] neg_lo:[1,0,0] neg_hi:[1,0,0]\n\t"
"v_pk_add_f32 v[108:109], v[12:13], v[108:109] neg_lo:[0,1] neg_hi:[0,1]\n\t"
"v_pk_add_f32 v[110:111], v[14:15], v[110:111] neg_lo:[0,1] neg_hi:[0,1]\n\t"
"v_pk_fma_f32 v[12:13], v[108:109], s[90:91], v[12:13] neg_lo:[1,0,0] neg_hi:[1,0,0]\n\t"
"v_pk_fma_f32 v[14:15], v[110:111], s[90:91], v[14:15] neg_lo:[1,0,0] neg_hi:[1,0,0]\n\t"
"v_mfma_f32_16x16x32_bf16 v[0:3], a[48:51], a[144:147], v[0:3]\n\t"
"v_cvt_pk_bf16_f32 v202, v204, v205\n\t"
"v_cvt_pk_bf16_f32 v203, v206, v207\n\t"
"v_mfma_f32_16x16x32_bf16 v[0:3], a[52:55], a[160:163], v[0:3]\n\t"
"ds_write_b128 v217, v[200:203] offset:16384\n\t"
"v_mfma_f32_16x16x32_bf16 v[212:215], a[188:191], a[252:255], 0\n\t"
"s_waitcnt vmcnt(17)\n\t"
"buffer_load_dwordx4 a[104:107], v235, s[8:11], s75 offen offset:2048 sc1\n\t"
"v_mfma_f32_16x16x32_bf16 v[16:19], a[56:59], a[144:147], v[16:19]\n\t"
"v_cvt_pk_bf16_f32 v208, v208, v209\n\t"
"v_cvt_pk_bf16_f32 v209, v210, v211\n\t"
"v_mfma_f32_16x16x32_bf16 v[16:19], a[60:63], a[160:163], v[16:19]\n\t"
"s_waitcnt vmcnt(16)\n\t"
"buffer_load_dwordx4 a[108:111], v235, s[8:11], s75 offen offset:3072 sc1\n\t"
"v_mfma_f32_16x16x32_bf16 v[32:35], a[64:67], a[144:147], v[32:35]\n\t"
"ds_read_b128 a[144:147], v216 offset:4096\n\t"
"v_mfma_f32_16x16x32_bf16 v[32:35], a[68:71], a[160:163], v[32:35]\n\t"
"ds_read_b128 a[160:163], v216 offset:12288\n\t"
"v_cvt_pk_bf16_f32 v210, v212, v213\n\t"
"v_mfma_f32_16x16x32_bf16 v[192:195], a[192:195], a[252:255], 0\n\t"
"s_waitcnt lgkmcnt(7)\n\t"
"buffer_load_dwordx4 a[112:115], v218, s[8:11], s75 offen offset:0 sc1\n\t"
"v_mfma_f32_16x16x32_bf16 v[36:39], a[64:67], a[148:151], v[36:39]\n\t"
"v_cvt_pk_bf16_f32 v211, v214, v215\n\t"
"v_mfma_f32_16x16x32_bf16 v[36:39], a[68:71], a[164:167], v[36:39]\n\t"
"ds_write_b128 v219, v[208:211] offset:16384\n\t"
"v_mfma_f32_16x16x32_bf16 v[20:23], a[56:59], a[148:151], v[20:23]\n\t"
"buffer_load_dwordx4 a[116:119], v218, s[8:11], s75 offen offset:1024 sc1\n\t"
"v_mfma_f32_16x16x32_bf16 v[20:23], a[60:63], a[164:167], v[20:23]\n\t"
"v_cvt_pk_bf16_f32 v192, v192, v193\n\t"
"v_cvt_pk_bf16_f32 v193, v194, v195\n\t"
"v_mfma_f32_16x16x32_bf16 v[196:199], a[196:199], a[252:255], 0\n\t"
"s_waitcnt vmcnt(13)\n\t"
"s_barrier\n\t"
"v_mfma_f32_16x16x32_bf16 v[4:7], a[48:51], a[148:151], v[4:7]\n\t"
"s_mov_b32 s78, 245760\n\t"
"buffer_load_dwordx4 a[120:123], v235, s[8:11], s78 offen offset:0 sc1\n\t"
"v_mfma_f32_16x16x32_bf16 v[4:7], a[52:55], a[164:167], v[4:7]\n\t"
"s_mov_b32 s68, 188416\n\t"
"ds_read_b64_tr_b16 a[176:177], v248 offset:41472\n\t"
"ds_read_b64_tr_b16 a[178:179], v248 offset:41984\n\t"
"v_mfma_f32_16x16x32_bf16 v[200:203], a[200:203], a[252:255], 0\n\t"
"s_waitcnt lgkmcnt(8)\n\t"
"buffer_load_dwordx4 a[124:127], v235, s[8:11], s78 offen offset:1024 sc1\n\t"
"v_mfma_f32_16x16x32_bf16 v[8:11], a[48:51], a[152:155], v[8:11]\n\t"
"s_mov_b32 s64, 155648\n\t"
"ds_read_b128 a[164:167], v216 offset:13312\n\t"
"ds_read_b64_tr_b16 a[180:181], v249 offset:41472\n\t"
"v_mfma_f32_16x16x32_bf16 v[8:11], a[52:55], a[168:171], v[8:11]\n\t"
"buffer_load_dwordx4 a[128:131], v235, s[8:11], s78 offen offset:2048 sc1\n\t"
"v_mfma_f32_16x16x32_bf16 v[24:27], a[56:59], a[152:155], v[24:27]\n\t"
"s_mov_b32 s72, 204800\n\t"
"ds_read_b64_tr_b16 a[182:183], v249 offset:41984\n\t"
"ds_read_b64_tr_b16 a[184:185], v250 offset:43776\n\t"
"v_mfma_f32_16x16x32_bf16 v[24:27], a[60:63], a[168:171], v[24:27]\n\t"
"buffer_load_dwordx4 a[132:135], v235, s[8:11], s78 offen offset:3072 sc1\n\t"
"v_mfma_f32_16x16x32_bf16 v[204:207], a[204:207], a[252:255], 0\n\t"
"s_mov_b32 s67, 180224\n\t"
"ds_read_b128 a[148:151], v216 offset:5120\n\t"
"ds_read_b64_tr_b16 a[186:187], v250 offset:44288\n\t"
"v_mfma_f32_16x16x32_bf16 v[40:43], a[64:67], a[152:155], v[40:43]\n\t"
"ds_read_b64_tr_b16 a[188:189], v251 offset:43776\n\t"
"v_cvt_pk_bf16_f32 v194, v196, v197\n\t"
"v_mfma_f32_16x16x32_bf16 v[40:43], a[68:71], a[168:171], v[40:43]\n\t"
"s_waitcnt lgkmcnt(13)\n\t"
"ds_read_b64_tr_b16 a[190:191], v251 offset:44288\n\t"
"v_cvt_pk_bf16_f32 v195, v198, v199\n\t"
"v_mfma_f32_16x16x32_bf16 v[44:47], a[64:67], a[156:159], v[44:47]\n\t"
"buffer_load_dwordx4 a[136:139], v218, s[8:11], s78 offen offset:0 sc1\n\t"
"v_mfma_f32_16x16x32_bf16 v[44:47], a[68:71], a[172:175], v[44:47]\n\t"
"ds_write_b128 v217, v[192:195] offset:24576\n\t"
"v_mfma_f32_16x16x32_bf16 v[28:31], a[56:59], a[156:159], v[28:31]\n\t"
"v_cvt_pk_bf16_f32 v200, v200, v201\n\t"
"v_cvt_pk_bf16_f32 v201, v202, v203\n\t"
"v_mfma_f32_16x16x32_bf16 v[28:31], a[60:63], a[172:175], v[28:31]\n\t"
"buffer_load_dwordx4 a[140:143], v218, s[8:11], s78 offen offset:1024 sc1\n\t"
"v_mfma_f32_16x16x32_bf16 v[12:15], a[48:51], a[156:159], v[12:15]\n\t"
"v_cvt_pk_bf16_f32 v202, v204, v205\n\t"
"v_cvt_pk_bf16_f32 v203, v206, v207\n\t"
"v_mfma_f32_16x16x32_bf16 v[12:15], a[52:55], a[172:175], v[12:15]\n\t"
"s_waitcnt vmcnt(17) lgkmcnt(12)\n\t"
"ds_write_b128 v219, v[200:203] offset:24576\n\t"
"v_mfma_f32_16x16x32_bf16 v[96:99], a[72:75], a[144:147], v[96:99]\n\t"
"v_mfma_f32_16x16x32_bf16 v[96:99], a[76:79], a[160:163], v[96:99]\n\t"
"s_waitcnt vmcnt(14)\n\t"
"s_add_u32 m0, s43, 101632\n\t"
"s_mov_b64 exec, s[44:45]\n\t"
"buffer_load_dwordx4 v247, s[12:15], 0 offen offset:0 nt lds\n\t"
"s_mov_b64 exec, -1\n\t"
"v_mfma_f32_16x16x32_bf16 v[112:115], a[80:83], a[144:147], v[112:115]\n\t"
"v_mfma_f32_16x16x32_bf16 v[112:115], a[84:87], a[160:163], v[112:115]\n\t"
"s_waitcnt vmcnt(13)\n\t"
"ds_read_b128 a[172:175], v216 offset:15360\n\t"
"ds_read_b64_tr_b16 a[192:193], v250 offset:41472\n\t"
"s_mov_b32 s65, 163840\n\t"
"v_mfma_f32_16x16x32_bf16 v[128:131], a[88:91], a[144:147], v[128:131]\n\t"
"s_nop 0\n\t"
"ds_read_b64_tr_b16 a[194:195], v250 offset:41984\n\t"
"ds_read_b64_tr_b16 a[196:197], v251 offset:41472\n\t"
"v_mfma_f32_16x16x32_bf16 v[128:131], a[92:95], a[160:163], v[128:131]\n\t"
"s_waitcnt lgkmcnt(9)\n\t"
"ds_read_b64_tr_b16 a[198:199], v251 offset:41984\n\t"
"ds_read_b64_tr_b16 a[200:201], v248 offset:43776\n\t"
"s_mov_b32 s74, 212992\n\t"
"v_mfma_f32_16x16x32_bf16 v[132:135], a[88:91], a[148:151], v[132:135]\n\t"
"s_nop 0\n\t"
"ds_read_b64_tr_b16 a[202:203], v248 offset:44288\n\t"
"ds_read_b64_tr_b16 a[204:205], v249 offset:43776\n\t"
"v_mfma_f32_16x16x32_bf16 v[132:135], a[92:95], a[164:167], v[132:135]\n\t"
"s_nop 0\n\t"
"ds_read_b128 a[168:171], v216 offset:14336\n\t"
"ds_read_b64_tr_b16 a[206:207], v249 offset:44288\n\t"
"v_mfma_f32_16x16x32_bf16 v[116:119], a[80:83], a[148:151], v[116:119]\n\t"
"s_nop 0\n\t"
"ds_read_b128 a[152:155], v216 offset:6144\n\t"
"ds_read_b128 a[156:159], v216 offset:7168\n\t"
"v_mfma_f32_16x16x32_bf16 v[116:119], a[84:87], a[164:167], v[116:119]\n\t"
"s_add_u32 m0, s26, 103936\n\t"
"s_nop 0\n\t"
"buffer_load_dwordx4 v236, s[12:15], s22 offen offset:0 nt lds\n\t"
"v_mfma_f32_16x16x32_bf16 v[100:103], a[72:75], a[148:151], v[100:103]\n\t"
"v_mfma_f32_16x16x32_bf16 v[100:103], a[76:79], a[164:167], v[100:103]\n\t"
"s_waitcnt lgkmcnt(0)\n\t"
"s_barrier\n\t"
"v_mfma_f32_16x16x32_bf16 v[104:107], a[72:75], a[152:155], v[104:107]\n\t"
"ds_read_b128 a[148:151], v216 offset:17408\n\t"
"v_mfma_f32_16x16x32_bf16 v[104:107], a[76:79], a[168:171], v[104:107]\n\t"
"ds_read_b128 a[144:147], v216 offset:16384\n\t"
"v_mfma_f32_16x16x32_bf16 v[120:123], a[80:83], a[152:155], v[120:123]\n\t"
"ds_read_b128 a[160:163], v216 offset:24576\n\t"
"v_mfma_f32_16x16x32_bf16 v[120:123], a[84:87], a[168:171], v[120:123]\n\t"
"ds_read_b128 a[164:167], v216 offset:25600\n\t"
"v_mfma_f32_16x16x32_bf16 v[136:139], a[88:91], a[152:155], v[136:139]\n\t"
"ds_read_b128 a[152:155], v216 offset:18432\n\t"
"v_mfma_f32_16x16x32_bf16 v[136:139], a[92:95], a[168:171], v[136:139]\n\t"
"v_mfma_f32_16x16x32_bf16 v[208:211], a[176:179], a[212:215], 0\n\t"
"ds_read_b128 a[168:171], v216 offset:26624\n\t"
"v_mfma_f32_16x16x32_bf16 v[140:143], a[88:91], a[156:159], v[140:143]\n\t"
"s_mov_b32 s76, 229376\n\t"
"v_mfma_f32_16x16x32_bf16 v[140:143], a[92:95], a[172:175], v[140:143]\n\t"
"v_mfma_f32_16x16x32_bf16 v[212:215], a[180:183], a[212:215], 0\n\t"
"v_mfma_f32_16x16x32_bf16 v[124:127], a[80:83], a[156:159], v[124:127]\n\t"
"buffer_load_dwordx4 a[248:251], v247, s[12:15], 0 offen offset:256 nt\n\t"
"v_mfma_f32_16x16x32_bf16 v[124:127], a[84:87], a[172:175], v[124:127]\n\t"
"v_cvt_pk_bf16_f32 v208, v208, v209\n\t"
"v_cvt_pk_bf16_f32 v209, v210, v211\n\t"
"v_mfma_f32_16x16x32_bf16 v[108:111], a[72:75], a[156:159], v[108:111]\n\t"
"buffer_load_dwordx4 v237, s[12:15], s22 offen offset:1024 nt lds\n\t"
"v_mfma_f32_16x16x32_bf16 v[108:111], a[76:79], a[172:175], v[108:111]\n\t"
"ds_read_b128 a[156:159], v216 offset:19456\n\t"
"v_cvt_pk_bf16_f32 v210, v212, v213\n\t"
"v_mfma_f32_16x16x32_bf16 v[192:195], a[184:187], a[212:215], 0\n\t"
"s_waitcnt vmcnt(14) lgkmcnt(4)\n\t"
"buffer_load_dwordx4 v238, s[12:15], s22 offen offset:2048 nt lds\n\t"
"v_mfma_f32_16x16x32_bf16 v[0:3], a[96:99], a[144:147], v[0:3]\n\t"
"ds_read_b128 a[172:175], v216 offset:27648\n\t"
"v_cvt_pk_bf16_f32 v211, v214, v215\n\t"
"v_mfma_f32_16x16x32_bf16 v[0:3], a[100:103], a[160:163], v[0:3]\n\t"
"buffer_load_dwordx4 v239, s[12:15], s22 offen offset:3072 nt lds\n\t"
"v_mfma_f32_16x16x32_bf16 v[196:199], a[188:191], a[212:215], 0\n\t"
"s_waitcnt vmcnt(14)\n\t"
"ds_write_b128 v217, v[208:211] offset:0\n\t"
"s_mov_b32 s77, 237568\n\t"
"v_mfma_f32_16x16x32_bf16 v[16:19], a[104:107], a[144:147], v[16:19]\n\t"
"v_cvt_pk_bf16_f32 v192, v192, v193\n\t"
"v_cvt_pk_bf16_f32 v193, v194, v195\n\t"
"v_mfma_f32_16x16x32_bf16 v[16:19], a[108:111], a[160:163], v[16:19]\n\t"
"s_waitcnt vmcnt(12)\n\t"
"s_mov_b64 exec, s[36:37]\n\t"
"buffer_load_dwordx4 v240, s[12:15], s22 offen offset:3584 nt lds\n\t"
"s_mov_b64 exec, -1\n\t"
"v_mfma_f32_16x16x32_bf16 v[32:35], a[112:115], a[144:147], v[32:35]\n\t"
"v_mfma_f32_16x16x32_bf16 v[32:35], a[116:119], a[160:163], v[32:35]\n\t"
"s_mov_b32 s79, 253952\n\t"
"ds_read_b128 a[144:147], v216 offset:20480\n\t"
"v_cvt_pk_bf16_f32 v194, v196, v197\n\t"
"v_mfma_f32_16x16x32_bf16 v[200:203], a[192:195], a[212:215], 0\n\t"
"s_waitcnt lgkmcnt(6)\n\t"
"ds_read_b128 a[160:163], v216 offset:28672\n\t"
"v_cvt_pk_bf16_f32 v195, v198, v199\n\t"
"v_mfma_f32_16x16x32_bf16 v[36:39], a[112:115], a[148:151], v[36:39]\n\t"
"v_mfma_f32_16x16x32_bf16 v[36:39], a[116:119], a[164:167], v[36:39]\n\t"
"buffer_load_dwordx4 a[0:3], v218, s[8:11], s58 offen offset:0 sc1\n\t"
"v_mfma_f32_16x16x32_bf16 v[20:23], a[104:107], a[148:151], v[20:23]\n\t"
"v_mfma_f32_16x16x32_bf16 v[20:23], a[108:111], a[164:167], v[20:23]\n\t"
"buffer_load_dwordx4 a[4:7], v218, s[8:11], s58 offen offset:1024 sc1\n\t"
"buffer_load_dwordx4 a[8:11], v218, s[8:11], s58 offen offset:2048 sc1\n\t"
"v_mfma_f32_16x16x32_bf16 v[204:207], a[196:199], a[212:215], 0\n\t"
"ds_write_b128 v219, v[192:195] offset:0\n\t"
"v_mfma_f32_16x16x32_bf16 v[4:7], a[96:99], a[148:151], v[4:7]\n\t"
"buffer_load_dwordx4 a[12:15], v218, s[8:11], s58 offen offset:3072 sc1\n\t"
"v_mfma_f32_16x16x32_bf16 v[4:7], a[100:103], a[164:167], v[4:7]\n\t"
"v_cvt_pk_bf16_f32 v200, v200, v201\n\t"
"v_cvt_pk_bf16_f32 v201, v202, v203\n\t"
"v_mfma_f32_16x16x32_bf16 v[208:211], a[200:203], a[212:215], 0\n\t"
"s_waitcnt lgkmcnt(6)\n\t"
"buffer_load_dwordx4 a[16:19], v235, s[8:11], s59 offen offset:0 sc1\n\t"
"buffer_load_dwordx4 a[20:23], v235, s[8:11], s59 offen offset:1024 sc1\n\t"
"buffer_load_dwordx4 a[24:27], v218, s[8:11], s57 offen offset:2048 sc1\n\t"
"v_mfma_f32_16x16x32_bf16 v[8:11], a[96:99], a[152:155], v[8:11]\n\t"
"s_mov_b32 s80, 262144\n\t"
"ds_read_b128 a[148:151], v216 offset:21504\n\t"
"v_cvt_pk_bf16_f32 v202, v204, v205\n\t"
"v_mfma_f32_16x16x32_bf16 v[8:11], a[100:103], a[168:171], v[8:11]\n\t"
"buffer_load_dwordx4 a[28:31], v218, s[8:11], s57 offen offset:3072 sc1\n\t"
"buffer_load_dwordx4 a[32:35], v235, s[8:11], s58 offen offset:0 sc1\n\t"
"v_mfma_f32_16x16x32_bf16 v[24:27], a[104:107], a[152:155], v[24:27]\n\t"
"ds_read_b128 a[164:167], v216 offset:29696\n\t"
"v_cvt_pk_bf16_f32 v203, v206, v207\n\t"
"v_mfma_f32_16x16x32_bf16 v[24:27], a[108:111], a[168:171], v[24:27]\n\t"
"buffer_load_dwordx4 a[36:39], v235, s[8:11], s58 offen offset:1024 sc1\n\t"
"buffer_load_dwordx4 a[40:43], v235, s[8:11], s58 offen offset:2048 sc1\n\t"
"v_mfma_f32_16x16x32_bf16 v[212:215], a[204:207], a[212:215], 0\n\t"
"ds_write_b128 v217, v[200:203] offset:8192\n\t"
"v_mfma_f32_16x16x32_bf16 v[40:43], a[112:115], a[152:155], v[40:43]\n\t"
"buffer_load_dwordx4 a[44:47], v235, s[8:11], s58 offen offset:3072 sc1\n\t"
"v_mfma_f32_16x16x32_bf16 v[40:43], a[116:119], a[168:171], v[40:43]\n\t"
"s_waitcnt lgkmcnt(7)\n\t"
"buffer_load_dwordx4 a[48:51], v218, s[8:11], s64 offen offset:0 sc1\n\t"
"buffer_load_dwordx4 a[52:55], v218, s[8:11], s64 offen offset:1024 sc1\n\t"
"v_mfma_f32_16x16x32_bf16 v[44:47], a[112:115], a[156:159], v[44:47]\n\t"
"s_mov_b32 s82, 278528\n\t"
"v_cvt_pk_bf16_f32 v208, v208, v209\n\t"
"v_cvt_pk_bf16_f32 v209, v210, v211\n\t"
"v_mfma_f32_16x16x32_bf16 v[44:47], a[116:119], a[172:175], v[44:47]\n\t"
"buffer_load_dwordx4 a[56:59], v218, s[8:11], s64 offen offset:2048 sc1\n\t"
"buffer_load_dwordx4 a[60:63], v218, s[8:11], s64 offen offset:3072 sc1\n\t"
"v_mfma_f32_16x16x32_bf16 v[28:31], a[104:107], a[156:159], v[28:31]\n\t"
"ds_read_b128 a[168:171], v216 offset:30720\n\t"
"v_cvt_pk_bf16_f32 v210, v212, v213\n\t"
"v_mfma_f32_16x16x32_bf16 v[28:31], a[108:111], a[172:175], v[28:31]\n\t"
"buffer_load_dwordx4 a[64:67], v235, s[8:11], s65 offen offset:0 sc1\n\t"
"buffer_load_dwordx4 a[68:71], v235, s[8:11], s65 offen offset:1024 sc1\n\t"
"v_mfma_f32_16x16x32_bf16 v[12:15], a[96:99], a[156:159], v[12:15]\n\t"
"ds_read_b128 a[152:155], v216 offset:22528\n\t"
"v_cvt_pk_bf16_f32 v211, v214, v215\n\t"
"v_mfma_f32_16x16x32_bf16 v[12:15], a[100:103], a[172:175], v[12:15]\n\t"
"s_waitcnt vmcnt(29) lgkmcnt(6)\n\t"
"buffer_load_dwordx4 a[72:75], v218, s[8:11], s63 offen offset:2048 sc1\n\t"
"buffer_load_dwordx4 a[76:79], v218, s[8:11], s63 offen offset:3072 sc1\n\t"
"v_mfma_f32_16x16x32_bf16 v[96:99], a[120:123], a[144:147], v[96:99]\n\t"
"s_mov_b32 s83, 286720\n\t"
"v_mfma_f32_16x16x32_bf16 v[96:99], a[124:127], a[160:163], v[96:99]\n\t"
"s_waitcnt vmcnt(29)\n\t"
"ds_write_b128 v219, v[208:211] offset:8192\n\t"
"v_mfma_f32_16x16x32_bf16 v[112:115], a[128:131], a[144:147], v[112:115]\n\t"
"buffer_load_dwordx4 a[80:83], v235, s[8:11], s64 offen offset:0 sc1\n\t"
"v_mfma_f32_16x16x32_bf16 v[112:115], a[132:135], a[160:163], v[112:115]\n\t"
"s_waitcnt vmcnt(28)\n\t"
"buffer_load_dwordx4 a[84:87], v235, s[8:11], s64 offen offset:1024 sc1\n\t"
"buffer_load_dwordx4 a[88:91], v235, s[8:11], s64 offen offset:2048 sc1\n\t"
"buffer_load_dwordx4 a[92:95], v235, s[8:11], s64 offen offset:3072 sc1\n\t"
"v_mfma_f32_16x16x32_bf16 v[128:131], a[136:139], a[144:147], v[128:131]\n\t"
"v_mfma_f32_16x16x32_bf16 v[128:131], a[140:143], a[160:163], v[128:131]\n\t"
"s_waitcnt lgkmcnt(4)\n\t"
"buffer_load_dwordx4 a[96:99], v218, s[8:11], s46 offen offset:0 sc1\n\t"
"buffer_load_dwordx4 a[100:103], v218, s[8:11], s46 offen offset:1024 sc1\n\t"
"v_mfma_f32_16x16x32_bf16 v[132:135], a[136:139], a[148:151], v[132:135]\n\t"
"ds_read_b128 a[156:159], v216 offset:23552\n\t"
"v_mfma_f32_16x16x32_bf16 v[132:135], a[140:143], a[164:167], v[132:135]\n\t"
"buffer_load_dwordx4 a[104:107], v218, s[8:11], s46 offen offset:2048 sc1\n\t"
"buffer_load_dwordx4 a[108:111], v218, s[8:11], s46 offen offset:3072 sc1\n\t"
"v_mfma_f32_16x16x32_bf16 v[116:119], a[128:131], a[148:151], v[116:119]\n\t"
"ds_read_b128 a[172:175], v216 offset:31744\n\t"
"v_mfma_f32_16x16x32_bf16 v[116:119], a[132:135], a[164:167], v[116:119]\n\t"
"buffer_load_dwordx4 a[112:115], v235, s[8:11], s47 offen offset:0 sc1\n\t"
"buffer_load_dwordx4 a[116:119], v235, s[8:11], s47 offen offset:1024 sc1\n\t"
"v_mfma_f32_16x16x32_bf16 v[100:103], a[120:123], a[148:151], v[100:103]\n\t"
"v_mfma_f32_16x16x32_bf16 v[100:103], a[124:127], a[164:167], v[100:103]\n\t"
"v_add_u32 v246, 8, v228\n\t"
"v_add_u32 v247, 16, v228\n\t"
"s_waitcnt vmcnt(30)\n\t"
"s_waitcnt lgkmcnt(0)\n\t"
"s_barrier\n\t"
"Lunit_%=:\n\t"
"v_mfma_f32_16x16x32_bf16 v[104:107], a[120:123], a[152:155], v[104:107]\n\t"
"s_add_u32 m0, s26, 55552\n\t"
"ds_read_b128 a[144:147], v216 offset:4096\n\t"
"ds_read_b128 a[148:151], v216 offset:5120\n\t"
"v_mfma_f32_16x16x32_bf16 v[104:107], a[124:127], a[168:171], v[104:107]\n\t"
"buffer_load_dwordx4 v236, s[12:15], s22 offen offset:256 nt lds\n\t"
"v_mfma_f32_16x16x32_bf16 v[120:123], a[128:131], a[152:155], v[120:123]\n\t"
"ds_read_b128 a[160:163], v216 offset:12288\n\t"
"v_mfma_f32_16x16x32_bf16 v[120:123], a[132:135], a[168:171], v[120:123]\n\t"
"ds_read_b128 a[164:167], v216 offset:13312\n\t"
"v_mfma_f32_16x16x32_bf16 v[136:139], a[136:139], a[152:155], v[136:139]\n\t"
"v_mfma_f32_16x16x32_bf16 v[136:139], a[140:143], a[168:171], v[136:139]\n\t"
"ds_write_b128 v222, a[248:251] offset:20736\n\t"
"v_mfma_f32_16x16x32_bf16 v[192:195], a[176:179], a[216:219], 0\n\t"
"v_mfma_f32_16x16x32_bf16 v[140:143], a[136:139], a[156:159], v[140:143]\n\t"
"v_mfma_f32_16x16x32_bf16 v[140:143], a[140:143], a[172:175], v[140:143]\n\t"
"buffer_load_dwordx4 v237, s[12:15], s22 offen offset:1280 nt lds\n\t"
"v_mfma_f32_16x16x32_bf16 v[196:199], a[180:183], a[216:219], 0\n\t"
"s_nop 0\n\t"
"ds_read_b128 a[168:171], v216 offset:14336\n\t"
"ds_read_b64_tr_b16 a[176:177], v248 offset:0\n\t"
"v_mfma_f32_16x16x32_bf16 v[124:127], a[128:131], a[156:159], v[124:127]\n\t"
"ds_read_b128 a[152:155], v216 offset:6144\n\t"
"v_cvt_pk_bf16_f32 v192, v192, v193\n\t"
"v_mfma_f32_16x16x32_bf16 v[124:127], a[132:135], a[172:175], v[124:127]\n\t"
"s_nop 0\n\t"
"ds_read_b64_tr_b16 a[178:179], v248 offset:512\n\t"
"ds_read_b64_tr_b16 a[180:181], v249 offset:0\n\t"
"v_mfma_f32_16x16x32_bf16 v[108:111], a[120:123], a[156:159], v[108:111]\n\t"
"ds_read_b64_tr_b16 a[182:183], v249 offset:512\n\t"
"v_cvt_pk_bf16_f32 v193, v194, v195\n\t"
"v_mfma_f32_16x16x32_bf16 v[108:111], a[124:127], a[172:175], v[108:111]\n\t"
"s_nop 0\n\t"
"ds_read_b128 a[156:159], v216 offset:7168\n\t"
"ds_read_b128 a[172:175], v216 offset:15360\n\t"
"v_mfma_f32_16x16x32_bf16 v[200:203], a[184:187], a[216:219], 0\n\t"
"s_waitcnt vmcnt(30) lgkmcnt(9)\n\t"
"ds_read_b64_tr_b16 a[184:185], v250 offset:2304\n\t"
"v_cvt_pk_bf16_f32 v194, v196, v197\n\t"
"v_mfma_f32_16x16x32_bf16 v[48:51], a[0:3], a[144:147], v[0:3]\n\t"
"ds_read_b64_tr_b16 a[186:187], v250 offset:2816\n\t"
"v_cvt_pk_bf16_f32 v195, v198, v199\n\t"
"v_mfma_f32_16x16x32_bf16 v[48:51], a[4:7], a[160:163], v[48:51]\n\t"
"buffer_load_dwordx4 v238, s[12:15], s22 offen offset:2304 nt lds\n\t"
"v_mfma_f32_16x16x32_bf16 v[204:207], a[188:191], a[216:219], 0\n\t"
"s_waitcnt vmcnt(29) lgkmcnt(10)\n\t"
"ds_write_b128 v217, v[192:195] offset:16384\n\t"
"v_mfma_f32_16x16x32_bf16 v[64:67], a[8:11], a[144:147], v[16:19]\n\t"
"v_cvt_pk_bf16_f32 v200, v200, v201\n\t"
"v_mfma_f32_16x16x32_bf16 v[64:67], a[12:15], a[160:163], v[64:67]\n\t"
"s_waitcnt vmcnt(27) lgkmcnt(9)\n\t"
"buffer_load_dwordx4 v239, s[12:15], s22 offen offset:3328 nt lds\n\t"
"v_mfma_f32_16x16x32_bf16 v[80:83], a[16:19], a[144:147], v[32:35]\n\t"
"v_cvt_pk_bf16_f32 v201, v202, v203\n\t"
"v_mfma_f32_16x16x32_bf16 v[80:83], a[20:23], a[160:163], v[80:83]\n\t"
"ds_read_b128 a[160:163], v216 offset:8192\n\t"
"v_cvt_pk_bf16_f32 v202, v204, v205\n\t"
"v_mfma_f32_16x16x32_bf16 v[208:211], a[192:195], a[216:219], 0\n\t"
"ds_read_b128 a[144:147], v216 offset:0\n\t"
"v_mfma_f32_16x16x32_bf16 v[84:87], a[16:19], a[148:151], v[36:39]\n\t"
"v_cvt_pk_bf16_f32 v203, v206, v207\n\t"
"v_mfma_f32_16x16x32_bf16 v[84:87], a[20:23], a[164:167], v[84:87]\n\t"
"ds_write_b128 v219, v[200:203] offset:16384\n\t"
"v_mfma_f32_16x16x32_bf16 v[68:71], a[8:11], a[148:151], v[20:23]\n\t"
"v_mfma_f32_16x16x32_bf16 v[68:71], a[12:15], a[164:167], v[68:71]\n\t"
"s_mov_b64 exec, s[36:37]\n\t"
"buffer_load_dwordx4 v240, s[12:15], s22 offen offset:3840 nt lds\n\t"
"s_mov_b64 exec, -1\n\t"
"v_mfma_f32_16x16x32_bf16 v[212:215], a[196:199], a[216:219], 0\n\t"
"v_cvt_pk_bf16_f32 v208, v208, v209\n\t"
"v_cvt_pk_bf16_f32 v209, v210, v211\n\t"
"v_mfma_f32_16x16x32_bf16 v[52:55], a[0:3], a[148:151], v[4:7]\n\t"
"ds_read_b64_tr_b16 a[188:189], v251 offset:2304\n\t"
"v_mfma_f32_16x16x32_bf16 v[52:55], a[4:7], a[164:167], v[52:55]\n\t"
"ds_read_b64_tr_b16 a[190:191], v251 offset:2816\n\t"
"v_mfma_f32_16x16x32_bf16 v[192:195], a[200:203], a[216:219], 0\n\t"
"s_waitcnt lgkmcnt(5)\n\t"
"ds_read_b64_tr_b16 a[192:193], v250 offset:0\n\t"
"v_mfma_f32_16x16x32_bf16 v[56:59], a[0:3], a[152:155], v[8:11]\n\t"
"ds_read_b128 a[164:167], v216 offset:9216\n\t"
"v_cvt_pk_bf16_f32 v210, v212, v213\n\t"
"v_mfma_f32_16x16x32_bf16 v[56:59], a[4:7], a[168:171], v[56:59]\n\t"
"ds_read_b64_tr_b16 a[194:195], v250 offset:512\n\t"
"v_mfma_f32_16x16x32_bf16 v[72:75], a[8:11], a[152:155], v[24:27]\n\t"
"ds_read_b64_tr_b16 a[196:197], v251 offset:0\n\t"
"v_cvt_pk_bf16_f32 v211, v214, v215\n\t"
"v_mfma_f32_16x16x32_bf16 v[72:75], a[12:15], a[168:171], v[72:75]\n\t"
"ds_read_b64_tr_b16 a[198:199], v251 offset:512\n\t"
"v_cvt_pk_bf16_f32 v192, v192, v193\n\t"
"v_mfma_f32_16x16x32_bf16 v[196:199], a[204:207], a[216:219], 0\n\t"
"ds_read_b128 a[148:151], v216 offset:1024\n\t"
"v_cvt_pk_bf16_f32 v193, v194, v195\n\t"
"v_mfma_f32_16x16x32_bf16 v[88:91], a[16:19], a[152:155], v[40:43]\n\t"
"v_mfma_f32_16x16x32_bf16 v[88:91], a[20:23], a[168:171], v[88:91]\n\t"
"ds_write_b128 v217, v[208:211] offset:24576\n\t"
"v_mfma_f32_16x16x32_bf16 v[92:95], a[16:19], a[156:159], v[44:47]\n\t"
"v_mfma_f32_16x16x32_bf16 v[92:95], a[20:23], a[172:175], v[92:95]\n\t"
"s_nop 0\n\t"
"v_cvt_pk_bf16_f32 v194, v196, v197\n\t"
"v_cvt_pk_bf16_f32 v195, v198, v199\n\t"
"v_mfma_f32_16x16x32_bf16 v[76:79], a[8:11], a[156:159], v[28:31]\n\t"
"v_mfma_f32_16x16x32_bf16 v[76:79], a[12:15], a[172:175], v[76:79]\n\t"
"ds_write_b128 v219, v[192:195] offset:24576\n\t"
"v_mfma_f32_16x16x32_bf16 v[60:63], a[0:3], a[156:159], v[12:15]\n\t"
"v_mfma_f32_16x16x32_bf16 v[60:63], a[4:7], a[172:175], v[60:63]\n\t"
"s_waitcnt vmcnt(27) lgkmcnt(4)\n\t"
"v_mfma_f32_16x16x32_bf16 v[0:3], a[24:27], a[144:147], v[0:3]\n\t"
"v_mfma_f32_16x16x32_bf16 v[0:3], a[28:31], a[160:163], v[0:3]\n\t"
"s_waitcnt vmcnt(25) lgkmcnt(2)\n\t"
"ds_read_b128 a[152:155], v216 offset:2048\n\t"
"v_mfma_f32_16x16x32_bf16 v[16:19], a[32:35], a[144:147], v[16:19]\n\t"
"ds_read_b128 a[156:159], v216 offset:3072\n\t"
"v_mfma_f32_16x16x32_bf16 v[16:19], a[36:39], a[160:163], v[16:19]\n\t"
"s_waitcnt vmcnt(23) lgkmcnt(3)\n\t"
"ds_read_b64_tr_b16 a[200:201], v248 offset:2304\n\t"
"v_mfma_f32_16x16x32_bf16 v[32:35], a[40:43], a[144:147], v[32:35]\n\t"
"ds_read_b64_tr_b16 a[202:203], v248 offset:2816\n\t"
"v_mfma_f32_16x16x32_bf16 v[32:35], a[44:47], a[160:163], v[32:35]\n\t"
"ds_read_b64_tr_b16 a[204:205], v249 offset:2304\n\t"
"v_mfma_f32_16x16x32_bf16 v[36:39], a[40:43], a[148:151], v[36:39]\n\t"
"ds_read_b128 a[172:175], v216 offset:11264\n\t"
"v_mfma_f32_16x16x32_bf16 v[36:39], a[44:47], a[164:167], v[36:39]\n\t"
"ds_read_b64_tr_b16 a[206:207], v249 offset:2816\n\t"
"v_mfma_f32_16x16x32_bf16 v[20:23], a[32:35], a[148:151], v[20:23]\n\t"
"ds_read_b128 a[168:171], v216 offset:10240\n\t"
"v_mfma_f32_16x16x32_bf16 v[20:23], a[36:39], a[164:167], v[20:23]\n\t"
"buffer_load_dwordx4 a[136:139], v235, s[8:11], s46 offen offset:2048 sc1\n\t"
"v_mfma_f32_16x16x32_bf16 v[4:7], a[24:27], a[148:151], v[4:7]\n\t"
"v_mfma_f32_16x16x32_bf16 v[4:7], a[28:31], a[164:167], v[4:7]\n\t"
"s_waitcnt lgkmcnt(0)\n\t"
"s_barrier\n\t"
"v_mfma_f32_16x16x32_bf16 v[8:11], a[24:27], a[152:155], v[8:11]\n\t"
"ds_read_b128 a[144:147], v216 offset:20480\n\t"
"v_mfma_f32_16x16x32_bf16 v[8:11], a[28:31], a[168:171], v[8:11]\n\t"
"ds_read_b128 a[164:167], v216 offset:29696\n\t"
"v_mfma_f32_16x16x32_bf16 v[24:27], a[32:35], a[152:155], v[24:27]\n\t"
"s_add_u32 s24, s24, 3145728\n\t"
"ds_read_b128 a[160:163], v216 offset:28672\n\t"
"ds_read_b128 a[148:151], v216 offset:21504\n\t"
"v_mfma_f32_16x16x32_bf16 v[24:27], a[36:39], a[168:171], v[24:27]\n\t"
"v_mfma_f32_16x16x32_bf16 v[40:43], a[40:43], a[152:155], v[40:43]\n\t"
"s_add_u32 s20, s20, 3145728\n\t"
"s_add_u32 s27, s27, 3145728\n\t"
"v_mfma_f32_16x16x32_bf16 v[40:43], a[44:47], a[168:171], v[40:43]\n\t"
"s_add_u32 s25, s25, 3145728\n\t"
"ds_read_b128 a[152:155], v216 offset:22528\n\t"
"v_mfma_f32_16x16x32_bf16 v[200:203], a[176:179], a[212:215], 0\n\t"
"ds_read_b128 a[168:171], v216 offset:30720\n\t"
"v_mfma_f32_16x16x32_bf16 v[44:47], a[40:43], a[156:159], v[44:47]\n\t"
"v_mfma_f32_16x16x32_bf16 v[44:47], a[44:47], a[172:175], v[44:47]\n\t"
"v_mfma_f32_16x16x32_bf16 v[204:207], a[180:183], a[212:215], 0\n\t"
"v_mfma_f32_16x16x32_bf16 v[28:31], a[32:35], a[156:159], v[28:31]\n\t"
"s_nop 2\n\t"
"v_cvt_pk_bf16_f32 v200, v200, v201\n\t"
"v_cvt_pk_bf16_f32 v201, v202, v203\n\t"
"v_mfma_f32_16x16x32_bf16 v[28:31], a[36:39], a[172:175], v[28:31]\n\t"
"v_mfma_f32_16x16x32_bf16 v[12:15], a[24:27], a[156:159], v[12:15]\n\t"
"v_mfma_f32_16x16x32_bf16 v[12:15], a[28:31], a[172:175], v[12:15]\n\t"
"v_cvt_pk_bf16_f32 v202, v204, v205\n\t"
"v_mfma_f32_16x16x32_bf16 v[208:211], a[184:187], a[212:215], 0\n\t"
"s_waitcnt vmcnt(22) lgkmcnt(2)\n\t"
"ds_read_b128 a[172:175], v216 offset:31744\n\t"
"v_mfma_f32_16x16x32_bf16 v[144:147], a[48:51], a[144:147], v[96:99]\n\t"
"ds_read_b128 a[156:159], v216 offset:23552\n\t"
"v_cvt_pk_bf16_f32 v203, v206, v207\n\t"
"v_mfma_f32_16x16x32_bf16 v[144:147], a[52:55], a[160:163], v[144:147]\n\t"
"v_mfma_f32_16x16x32_bf16 v[212:215], a[188:191], a[212:215], 0\n\t"
"s_waitcnt vmcnt(20) lgkmcnt(2)\n\t"
"ds_write_b128 v217, v[200:203] offset:0\n\t"
"v_mfma_f32_16x16x32_bf16 v[160:163], a[56:59], a[144:147], v[112:115]\n\t"
"v_cvt_pk_bf16_f32 v208, v208, v209\n\t"
"v_cvt_pk_bf16_f32 v209, v210, v211\n\t"
"v_mfma_f32_16x16x32_bf16 v[160:163], a[60:63], a[160:163], v[160:163]\n\t"
"s_waitcnt vmcnt(18)\n\t"
"v_mfma_f32_16x16x32_bf16 v[176:179], a[64:67], a[144:147], v[128:131]\n\t"
"v_mfma_f32_16x16x32_bf16 v[176:179], a[68:71], a[160:163], v[176:179]\n\t"
"v_cvt_pk_bf16_f32 v210, v212, v213\n\t"
"v_mfma_f32_16x16x32_bf16 v[192:195], a[192:195], a[212:215], 0\n\t"
"ds_read_b128 a[160:163], v216 offset:24576\n\t"
"v_cvt_pk_bf16_f32 v211, v214, v215\n\t"
"v_mfma_f32_16x16x32_bf16 v[180:183], a[64:67], a[148:151], v[132:135]\n\t"
"v_mfma_f32_16x16x32_bf16 v[180:183], a[68:71], a[164:167], v[180:183]\n\t"
"buffer_load_dwordx4 a[140:143], v235, s[8:11], s46 offen offset:3072 sc1\n\t"
"v_mfma_f32_16x16x32_bf16 v[164:167], a[56:59], a[148:151], v[116:119]\n\t"
"v_mfma_f32_16x16x32_bf16 v[164:167], a[60:63], a[164:167], v[164:167]\n\t"
"ds_read_b128 a[144:147], v216 offset:16384\n\t"
"v_mfma_f32_16x16x32_bf16 v[196:199], a[196:199], a[212:215], 0\n\t"
"v_cvt_pk_bf16_f32 v192, v192, v193\n\t"
"v_mfma_f32_16x16x32_bf16 v[148:151], a[48:51], a[148:151], v[100:103]\n\t"
"ds_read_b128 a[148:151], v216 offset:17408\n\t"
"v_cvt_pk_bf16_f32 v193, v194, v195\n\t"
"v_mfma_f32_16x16x32_bf16 v[148:151], a[52:55], a[164:167], v[148:151]\n\t"
"buffer_load_dwordx4 a[132:135], v235, s[8:11], s46 offen offset:1024 sc1\n\t"
"v_mfma_f32_16x16x32_bf16 v[200:203], a[200:203], a[212:215], 0\n\t"
"ds_read_b128 a[164:167], v216 offset:25600\n\t"
"v_mfma_f32_16x16x32_bf16 v[152:155], a[48:51], a[152:155], v[104:107]\n\t"
"v_mfma_f32_16x16x32_bf16 v[152:155], a[52:55], a[168:171], v[152:155]\n\t"
"ds_write_b128 v219, v[208:211] offset:0\n\t"
"v_mfma_f32_16x16x32_bf16 v[168:171], a[56:59], a[152:155], v[120:123]\n\t"
"v_cvt_pk_bf16_f32 v194, v196, v197\n\t"
"v_cvt_pk_bf16_f32 v195, v198, v199\n\t"
"v_mfma_f32_16x16x32_bf16 v[168:171], a[60:63], a[168:171], v[168:171]\n\t"
"buffer_load_dwordx4 a[128:131], v235, s[8:11], s46 offen offset:0 sc1\n\t"
"v_mfma_f32_16x16x32_bf16 v[204:207], a[204:207], a[212:215], 0\n\t"
"v_cvt_pk_bf16_f32 v200, v200, v201\n\t"
"v_mfma_f32_16x16x32_bf16 v[184:187], a[64:67], a[152:155], v[136:139]\n\t"
"ds_read_b128 a[152:155], v216 offset:18432\n\t"
"v_mfma_f32_16x16x32_bf16 v[184:187], a[68:71], a[168:171], v[184:187]\n\t"
"s_waitcnt lgkmcnt(4)\n\t"
"buffer_load_dwordx4 a[120:123], v218, s[8:11], s42 offen offset:2048 sc1\n\t"
"v_mfma_f32_16x16x32_bf16 v[188:191], a[64:67], a[156:159], v[140:143]\n\t"
"ds_read_b128 a[168:171], v216 offset:26624\n\t"
"v_cvt_pk_bf16_f32 v201, v202, v203\n\t"
"v_mfma_f32_16x16x32_bf16 v[188:191], a[68:71], a[172:175], v[188:191]\n\t"
"buffer_load_dwordx4 a[124:127], v218, s[8:11], s42 offen offset:3072 sc1\n\t"
"v_mfma_f32_16x16x32_bf16 v[172:175], a[56:59], a[156:159], v[124:127]\n\t"
"v_cvt_pk_bf16_f32 v202, v204, v205\n\t"
"v_mfma_f32_16x16x32_bf16 v[172:175], a[60:63], a[172:175], v[172:175]\n\t"
"ds_write_b128 v217, v[192:195] offset:8192\n\t"
"v_mfma_f32_16x16x32_bf16 v[156:159], a[48:51], a[156:159], v[108:111]\n\t"
"v_cvt_pk_bf16_f32 v203, v206, v207\n\t"
"v_mfma_f32_16x16x32_bf16 v[156:159], a[52:55], a[172:175], v[156:159]\n\t"
"s_waitcnt vmcnt(21) lgkmcnt(3)\n\t"
"buffer_load_dwordx4 a[16:19], v235, s[8:11], s50 offen offset:0 sc1\n\t"
"v_mfma_f32_16x16x32_bf16 v[96:99], a[72:75], a[144:147], v[96:99]\n\t"
"v_mfma_f32_16x16x32_bf16 v[96:99], a[76:79], a[160:163], v[96:99]\n\t"
"s_waitcnt vmcnt(20)\n\t"
"ds_write_b128 v219, v[200:203] offset:8192\n\t"
"v_mfma_f32_16x16x32_bf16 v[112:115], a[80:83], a[144:147], v[112:115]\n\t"
"v_mfma_f32_16x16x32_bf16 v[112:115], a[84:87], a[160:163], v[112:115]\n\t"
"s_waitcnt vmcnt(18) lgkmcnt(3)\n\t"
"buffer_load_dwordx4 a[20:23], v235, s[8:11], s50 offen offset:1024 sc1\n\t"
"v_mfma_f32_16x16x32_bf16 v[128:131], a[88:91], a[144:147], v[128:131]\n\t"
"v_mfma_f32_16x16x32_bf16 v[128:131], a[92:95], a[160:163], v[128:131]\n\t"
"ds_read_b128 a[172:175], v216 offset:27648\n\t"
"v_mfma_f32_16x16x32_bf16 v[132:135], a[88:91], a[148:151], v[132:135]\n\t"
"ds_read_b128 a[156:159], v216 offset:19456\n\t"
"v_mfma_f32_16x16x32_bf16 v[132:135], a[92:95], a[164:167], v[132:135]\n\t"
"buffer_load_dwordx4 a[12:15], v218, s[8:11], s49 offen offset:3072 sc1\n\t"
"v_mfma_f32_16x16x32_bf16 v[116:119], a[80:83], a[148:151], v[116:119]\n\t"
"v_mfma_f32_16x16x32_bf16 v[116:119], a[84:87], a[164:167], v[116:119]\n\t"
"v_mfma_f32_16x16x32_bf16 v[100:103], a[72:75], a[148:151], v[100:103]\n\t"
"buffer_load_dwordx4 a[8:11], v218, s[8:11], s49 offen offset:2048 sc1\n\t"
"v_mfma_f32_16x16x32_bf16 v[100:103], a[76:79], a[164:167], v[100:103]\n\t"
"s_waitcnt vmcnt(10) lgkmcnt(2)\n\t"
"s_waitcnt lgkmcnt(0)\n\t"
"s_barrier\n\t"
"v_mfma_f32_16x16x32_bf16 v[104:107], a[72:75], a[152:155], v[104:107]\n\t"
"ds_read_b128 a[144:147], v216 offset:4096\n\t"
"v_add_u32 v248, 32768, v224\n\t"
"v_mfma_f32_16x16x32_bf16 v[104:107], a[76:79], a[168:171], v[104:107]\n\t"
"buffer_load_dwordx4 a[0:3], v218, s[8:11], s49 offen offset:0 sc1\n\t"
"v_mfma_f32_16x16x32_bf16 v[120:123], a[80:83], a[152:155], v[120:123]\n\t"
"s_nop 0\n\t"
"ds_read_b128 a[160:163], v216 offset:12288\n\t"
"ds_read_b128 a[148:151], v216 offset:5120\n\t"
"v_mfma_f32_16x16x32_bf16 v[120:123], a[84:87], a[168:171], v[120:123]\n\t"
"ds_read_b128 a[164:167], v216 offset:13312\n\t"
"v_xor_b32 v249, 32, v248\n\t"
"v_mfma_f32_16x16x32_bf16 v[136:139], a[88:91], a[152:155], v[136:139]\n\t"
"ds_read_b128 a[152:155], v216 offset:6144\n\t"
"v_xor_b32 v250, 64, v248\n\t"
"v_mfma_f32_16x16x32_bf16 v[136:139], a[92:95], a[168:171], v[136:139]\n\t"
"buffer_load_dwordx4 a[4:7], v218, s[8:11], s49 offen offset:1024 sc1\n\t"
"v_mfma_f32_16x16x32_bf16 v[208:211], a[176:179], a[216:219], 0\n\t"
"ds_read_b128 a[248:251], v222 offset:18432\n\t"
"v_xor_b32 v251, 96, v248\n\t"
"v_mfma_f32_16x16x32_bf16 v[140:143], a[88:91], a[156:159], v[140:143]\n\t"
"ds_read_b64_tr_b16 a[176:177], v248 offset:0\n\t"
"v_xor_b32 v226, 64, v226\n\t"
"v_mfma_f32_16x16x32_bf16 v[140:143], a[92:95], a[172:175], v[140:143]\n\t"
"buffer_load_dwordx4 a[40:43], v235, s[8:11], s49 offen offset:2048 sc1\n\t"
"v_mfma_f32_16x16x32_bf16 v[212:215], a[180:183], a[216:219], 0\n\t"
"ds_read_b64_tr_b16 a[178:179], v248 offset:512\n\t"
"v_xor_b32 v227, 64, v227\n\t"
"v_mfma_f32_16x16x32_bf16 v[124:127], a[80:83], a[156:159], v[124:127]\n\t"
"ds_read_b64_tr_b16 a[180:181], v249 offset:0\n\t"
"v_cvt_pk_bf16_f32 v208, v208, v209\n\t"
"v_mfma_f32_16x16x32_bf16 v[124:127], a[84:87], a[172:175], v[124:127]\n\t"
"buffer_load_dwordx4 a[44:47], v235, s[8:11], s49 offen offset:3072 sc1\n\t"
"v_mfma_f32_16x16x32_bf16 v[108:111], a[72:75], a[156:159], v[108:111]\n\t"
"ds_read_b64_tr_b16 a[182:183], v249 offset:512\n\t"
"v_cvt_pk_bf16_f32 v209, v210, v211\n\t"
"v_mfma_f32_16x16x32_bf16 v[108:111], a[76:79], a[172:175], v[108:111]\n\t"
"ds_read_b128 a[172:175], v216 offset:15360\n\t"
"v_cvt_pk_bf16_f32 v210, v212, v213\n\t"
"v_mfma_f32_16x16x32_bf16 v[192:195], a[184:187], a[216:219], 0\n\t"
"s_waitcnt lgkmcnt(6)\n\t"
"buffer_load_dwordx4 a[36:39], v235, s[8:11], s49 offen offset:1024 sc1\n\t"
"v_mfma_f32_16x16x32_bf16 v[48:51], a[96:99], a[144:147], v[48:51]\n\t"
"s_add_u32 s22, s22, 3145728\n\t"
"ds_read_b128 a[168:171], v216 offset:14336\n\t"
"ds_read_b64_tr_b16 a[184:185], v250 offset:2304\n\t"
"v_mfma_f32_16x16x32_bf16 v[48:51], a[100:103], a[160:163], v[48:51]\n\t"
"ds_read_b64_tr_b16 a[186:187], v250 offset:2816\n\t"
"v_cvt_pk_bf16_f32 v211, v214, v215\n\t"
"v_mfma_f32_16x16x32_bf16 v[196:199], a[188:191], a[216:219], 0\n\t"
"s_cmp_eq_u32 s21, 1\n\t"
"s_cselect_b32 s22, 2.0, s22\n\t"
"ds_read_b64_tr_b16 a[188:189], v251 offset:2304\n\t"
"v_mfma_f32_16x16x32_bf16 v[64:67], a[104:107], a[144:147], v[64:67]\n\t"
"buffer_load_dwordx4 a[32:35], v235, s[8:11], s49 offen offset:0 sc1\n\t"
"v_mfma_f32_16x16x32_bf16 v[64:67], a[108:111], a[160:163], v[64:67]\n\t"
"ds_write_b128 v217, v[208:211] offset:16384\n\t"
"v_mfma_f32_16x16x32_bf16 v[80:83], a[112:115], a[144:147], v[80:83]\n\t"
"v_cvt_pk_bf16_f32 v192, v192, v193\n\t"
"v_cvt_pk_bf16_f32 v193, v194, v195\n\t"
"v_mfma_f32_16x16x32_bf16 v[80:83], a[116:119], a[160:163], v[80:83]\n\t"
"buffer_load_dwordx4 a[24:27], v218, s[8:11], s48 offen offset:2048 sc1\n\t"
"v_mfma_f32_16x16x32_bf16 v[200:203], a[192:195], a[216:219], 0\n\t"
"v_cvt_pk_bf16_f32 v194, v196, v197\n\t"
"v_mfma_f32_16x16x32_bf16 v[84:87], a[112:115], a[148:151], v[84:87]\n\t"
"s_nop 0\n\t"
"ds_read_b128 a[156:159], v216 offset:7168\n\t"
"ds_read_b128 a[144:147], v216 offset:0\n\t"
"v_mfma_f32_16x16x32_bf16 v[84:87], a[116:119], a[164:167], v[84:87]\n\t"
"s_nop 0\n\t"
"ds_read_b64_tr_b16 a[190:191], v251 offset:2816\n\t"
"ds_read_b64_tr_b16 a[192:193], v250 offset:0\n\t"
"v_mfma_f32_16x16x32_bf16 v[68:71], a[104:107], a[148:151], v[68:71]\n\t"
"ds_read_b64_tr_b16 a[194:195], v250 offset:512\n\t"
"v_cvt_pk_bf16_f32 v195, v198, v199\n\t"
"v_mfma_f32_16x16x32_bf16 v[68:71], a[108:111], a[164:167], v[68:71]\n\t"
"ds_read_b128 a[160:163], v216 offset:8192\n\t"
"v_cvt_pk_bf16_f32 v200, v200, v201\n\t"
"v_mfma_f32_16x16x32_bf16 v[204:207], a[196:199], a[216:219], 0\n\t"
"ds_read_b64_tr_b16 a[196:197], v251 offset:0\n\t"
"v_cvt_pk_bf16_f32 v201, v202, v203\n\t"
"v_mfma_f32_16x16x32_bf16 v[52:55], a[96:99], a[148:151], v[52:55]\n\t"
"ds_read_b64_tr_b16 a[198:199], v251 offset:512\n\t"
"v_mfma_f32_16x16x32_bf16 v[52:55], a[100:103], a[164:167], v[52:55]\n\t"
"buffer_load_dwordx4 a[28:31], v218, s[8:11], s48 offen offset:3072 sc1\n\t"
"v_mfma_f32_16x16x32_bf16 v[208:211], a[200:203], a[216:219], 0\n\t"
"s_waitcnt lgkmcnt(8)\n\t"
"ds_write_b128 v219, v[192:195] offset:16384\n\t"
"v_mfma_f32_16x16x32_bf16 v[56:59], a[96:99], a[152:155], v[56:59]\n\t"
"v_cvt_pk_bf16_f32 v202, v204, v205\n\t"
"v_mfma_f32_16x16x32_bf16 v[56:59], a[100:103], a[168:171], v[56:59]\n\t"
"buffer_load_dwordx4 a[64:67], v235, s[8:11], s77 offen offset:0 sc1\n\t"
"v_mfma_f32_16x16x32_bf16 v[72:75], a[104:107], a[152:155], v[72:75]\n\t"
"v_cvt_pk_bf16_f32 v203, v206, v207\n\t"
"v_mfma_f32_16x16x32_bf16 v[72:75], a[108:111], a[168:171], v[72:75]\n\t"
"ds_read_b128 a[148:151], v216 offset:1024\n\t"
"v_cvt_pk_bf16_f32 v208, v208, v209\n\t"
"v_mfma_f32_16x16x32_bf16 v[212:215], a[204:207], a[216:219], 0\n\t"
"ds_read_b128 a[164:167], v216 offset:9216\n\t"
"v_cvt_pk_bf16_f32 v209, v210, v211\n\t"
"v_mfma_f32_16x16x32_bf16 v[88:91], a[112:115], a[152:155], v[88:91]\n\t"
"s_nop 0\n\t"
"ds_read_b64_tr_b16 a[200:201], v248 offset:2304\n\t"
"ds_read_b64_tr_b16 a[202:203], v248 offset:2816\n\t"
"v_mfma_f32_16x16x32_bf16 v[88:91], a[116:119], a[168:171], v[88:91]\n\t"
"s_waitcnt lgkmcnt(6)\n\t"
"ds_read_b128 a[152:155], v216 offset:2048\n\t"
"v_mfma_f32_16x16x32_bf16 v[92:95], a[112:115], a[156:159], v[92:95]\n\t"
"buffer_load_dwordx4 a[68:71], v235, s[8:11], s77 offen offset:1024 sc1\n\t"
"v_mfma_f32_16x16x32_bf16 v[92:95], a[116:119], a[172:175], v[92:95]\n\t"
"ds_write_b128 v217, v[200:203] offset:24576\n\t"
"v_mfma_f32_16x16x32_bf16 v[76:79], a[104:107], a[156:159], v[76:79]\n\t"
"v_cvt_pk_bf16_f32 v210, v212, v213\n\t"
"v_mfma_f32_16x16x32_bf16 v[76:79], a[108:111], a[172:175], v[76:79]\n\t"
"buffer_load_dwordx4 a[60:63], v218, s[8:11], s76 offen offset:3072 sc1\n\t"
"v_mfma_f32_16x16x32_bf16 v[60:63], a[96:99], a[156:159], v[60:63]\n\t"
"v_cvt_pk_bf16_f32 v211, v214, v215\n\t"
"v_mfma_f32_16x16x32_bf16 v[60:63], a[100:103], a[172:175], v[60:63]\n\t"
"s_waitcnt vmcnt(15) lgkmcnt(6)\n\t"
"ds_write_b128 v219, v[208:211] offset:24576\n\t"
"v_mfma_f32_16x16x32_bf16 v[0:3], a[120:123], a[144:147], v[0:3]\n\t"
"buffer_load_dwordx4 a[56:59], v218, s[8:11], s76 offen offset:2048 sc1\n\t"
"v_mfma_f32_16x16x32_bf16 v[0:3], a[124:127], a[160:163], v[0:3]\n\t"
"v_mfma_f32_16x16x32_bf16 v[16:19], a[128:131], a[144:147], v[16:19]\n\t"
"buffer_load_dwordx4 a[48:51], v218, s[8:11], s76 offen offset:0 sc1\n\t"
"v_mfma_f32_16x16x32_bf16 v[16:19], a[132:135], a[160:163], v[16:19]\n\t"
"ds_read_b128 a[172:175], v216 offset:11264\n\t"
"v_mfma_f32_16x16x32_bf16 v[32:35], a[136:139], a[144:147], v[32:35]\n\t"
"ds_read_b64_tr_b16 a[204:205], v249 offset:2304\n\t"
"v_mfma_f32_16x16x32_bf16 v[32:35], a[140:143], a[160:163], v[32:35]\n\t"
"s_waitcnt lgkmcnt(3)\n\t"
"buffer_load_dwordx4 a[52:55], v218, s[8:11], s76 offen offset:1024 sc1\n\t"
"v_mfma_f32_16x16x32_bf16 v[36:39], a[136:139], a[148:151], v[36:39]\n\t"
"ds_read_b64_tr_b16 a[206:207], v249 offset:2816\n\t"
"v_mfma_f32_16x16x32_bf16 v[36:39], a[140:143], a[164:167], v[36:39]\n\t"
"ds_read_b128 a[168:171], v216 offset:10240\n\t"
"v_mfma_f32_16x16x32_bf16 v[20:23], a[128:131], a[148:151], v[20:23]\n\t"
"ds_read_b128 a[156:159], v216 offset:3072\n\t"
"v_mfma_f32_16x16x32_bf16 v[20:23], a[132:135], a[164:167], v[20:23]\n\t"
"buffer_load_dwordx4 a[88:91], v235, s[8:11], s76 offen offset:2048 sc1\n\t"
"v_mfma_f32_16x16x32_bf16 v[4:7], a[120:123], a[148:151], v[4:7]\n\t"
"v_mfma_f32_16x16x32_bf16 v[4:7], a[124:127], a[164:167], v[4:7]\n\t"
"buffer_load_dwordx4 a[92:95], v235, s[8:11], s76 offen offset:3072 sc1\n\t"
"s_waitcnt lgkmcnt(0)\n\t"
"s_barrier\n\t"
"v_mfma_f32_16x16x32_bf16 v[8:11], a[120:123], a[152:155], v[8:11]\n\t"
"ds_read_b128 a[160:163], v216 offset:28672\n\t"
"v_mfma_f32_16x16x32_bf16 v[8:11], a[124:127], a[168:171], v[8:11]\n\t"
"ds_read_b128 a[144:147], v216 offset:20480\n\t"
"v_mfma_f32_16x16x32_bf16 v[24:27], a[128:131], a[152:155], v[24:27]\n\t"
"ds_read_b128 a[148:151], v216 offset:21504\n\t"
"v_mfma_f32_16x16x32_bf16 v[24:27], a[132:135], a[168:171], v[24:27]\n\t"
"ds_read_b128 a[164:167], v216 offset:29696\n\t"
"v_mfma_f32_16x16x32_bf16 v[40:43], a[136:139], a[152:155], v[40:43]\n\t"
"ds_read_b128 a[244:247], v223 offset:41472\n\t"
"v_mfma_f32_16x16x32_bf16 v[40:43], a[140:143], a[168:171], v[40:43]\n\t"
"buffer_load_dwordx4 a[84:87], v235, s[8:11], s76 offen offset:1024 sc1\n\t"
"v_mfma_f32_16x16x32_bf16 v[192:195], a[176:179], a[212:215], 0\n\t"
"ds_read_b128 a[168:171], v216 offset:30720\n\t"
"v_mfma_f32_16x16x32_bf16 v[44:47], a[136:139], a[156:159], v[44:47]\n\t"
"ds_read_b128 a[152:155], v216 offset:22528\n\t"
"v_mfma_f32_16x16x32_bf16 v[44:47], a[140:143], a[172:175], v[44:47]\n\t"
"buffer_load_dwordx4 a[80:83], v235, s[8:11], s76 offen offset:0 sc1\n\t"
"v_mfma_f32_16x16x32_bf16 v[196:199], a[180:183], a[212:215], 0\n\t"
"v_mfma_f32_16x16x32_bf16 v[28:31], a[128:131], a[156:159], v[28:31]\n\t"
"s_nop 0\n\t"
"v_cvt_pk_bf16_f32 v192, v192, v193\n\t"
"v_cvt_pk_bf16_f32 v193, v194, v195\n\t"
"v_mfma_f32_16x16x32_bf16 v[28:31], a[132:135], a[172:175], v[28:31]\n\t"
"buffer_load_dwordx4 a[72:75], v218, s[8:11], s75 offen offset:2048 sc1\n\t"
"v_mfma_f32_16x16x32_bf16 v[12:15], a[120:123], a[156:159], v[12:15]\n\t"
"ds_read_b128 a[156:159], v216 offset:23552\n\t"
"v_mfma_f32_16x16x32_bf16 v[12:15], a[124:127], a[172:175], v[12:15]\n\t"
"buffer_load_dwordx4 a[76:79], v218, s[8:11], s75 offen offset:3072 sc1\n\t"
"v_mfma_f32_16x16x32_bf16 v[200:203], a[184:187], a[212:215], 0\n\t"
"s_waitcnt vmcnt(18) lgkmcnt(3)\n\t"
"v_mfma_f32_16x16x32_bf16 v[144:147], a[0:3], a[144:147], v[144:147]\n\t"
"v_cvt_pk_bf16_f32 v194, v196, v197\n\t"
"v_mfma_f32_16x16x32_bf16 v[144:147], a[4:7], a[160:163], v[144:147]\n\t"
"buffer_load_dwordx4 a[112:115], v235, s[8:11], s80 offen offset:0 sc1\n\t"
"v_mfma_f32_16x16x32_bf16 v[204:207], a[188:191], a[212:215], 0\n\t"
"ds_read_b128 a[172:175], v216 offset:31744\n\t"
"v_cvt_pk_bf16_f32 v195, v198, v199\n\t"
"v_mfma_f32_16x16x32_bf16 v[160:163], a[8:11], a[144:147], v[160:163]\n\t"
"v_cvt_pk_bf16_f32 v200, v200, v201\n\t"
"v_mfma_f32_16x16x32_bf16 v[160:163], a[12:15], a[160:163], v[160:163]\n\t"
"buffer_load_dwordx4 a[116:119], v235, s[8:11], s80 offen offset:1024 sc1\n\t"
"v_mfma_f32_16x16x32_bf16 v[176:179], a[16:19], a[144:147], v[176:179]\n\t"
"ds_read_b128 a[144:147], v216 offset:16384\n\t"
"v_cvt_pk_bf16_f32 v201, v202, v203\n\t"
"v_mfma_f32_16x16x32_bf16 v[176:179], a[20:23], a[160:163], v[176:179]\n\t"
"v_cvt_pk_bf16_f32 v202, v204, v205\n\t"
"v_cvt_pk_bf16_f32 v203, v206, v207\n\t"
"v_mfma_f32_16x16x32_bf16 v[208:211], a[192:195], a[212:215], 0\n\t"
"ds_write_b128 v217, v[192:195] offset:0\n\t"
"v_mfma_f32_16x16x32_bf16 v[180:183], a[16:19], a[148:151], v[180:183]\n\t"
"v_mfma_f32_16x16x32_bf16 v[180:183], a[20:23], a[164:167], v[180:183]\n\t"
"buffer_load_dwordx4 a[108:111], v218, s[8:11], s79 offen offset:3072 sc1\n\t"
"v_mfma_f32_16x16x32_bf16 v[164:167], a[8:11], a[148:151], v[164:167]\n\t"
"v_mfma_f32_16x16x32_bf16 v[164:167], a[12:15], a[164:167], v[164:167]\n\t"
"buffer_load_dwordx4 a[104:107], v218, s[8:11], s79 offen offset:2048 sc1\n\t"
"v_mfma_f32_16x16x32_bf16 v[212:215], a[196:199], a[212:215], 0\n\t"
"ds_read_b128 a[160:163], v216 offset:24576\n\t"
"v_cvt_pk_bf16_f32 v208, v208, v209\n\t"
"v_mfma_f32_16x16x32_bf16 v[148:151], a[0:3], a[148:151], v[148:151]\n\t"
"v_cvt_pk_bf16_f32 v209, v210, v211\n\t"
"v_mfma_f32_16x16x32_bf16 v[148:151], a[4:7], a[164:167], v[148:151]\n\t"
"ds_write_b128 v219, v[200:203] offset:0\n\t"
"v_mfma_f32_16x16x32_bf16 v[192:195], a[200:203], a[212:215], 0\n\t"
"s_waitcnt lgkmcnt(3)\n\t"
"buffer_load_dwordx4 a[96:99], v218, s[8:11], s79 offen offset:0 sc1\n\t"
"v_mfma_f32_16x16x32_bf16 v[152:155], a[0:3], a[152:155], v[152:155]\n\t"
"v_cvt_pk_bf16_f32 v210, v212, v213\n\t"
"v_mfma_f32_16x16x32_bf16 v[152:155], a[4:7], a[168:171], v[152:155]\n\t"
"v_mfma_f32_16x16x32_bf16 v[168:171], a[8:11], a[152:155], v[168:171]\n\t"
"ds_read_b128 a[148:151], v216 offset:17408\n\t"
"v_cvt_pk_bf16_f32 v211, v214, v215\n\t"
"v_mfma_f32_16x16x32_bf16 v[168:171], a[12:15], a[168:171], v[168:171]\n\t"
"buffer_load_dwordx4 a[100:103], v218, s[8:11], s79 offen offset:1024 sc1\n\t"
"v_mfma_f32_16x16x32_bf16 v[196:199], a[204:207], a[212:215], 0\n\t"
"ds_read_b128 a[164:167], v216 offset:25600\n\t"
"v_mfma_f32_16x16x32_bf16 v[184:187], a[16:19], a[152:155], v[184:187]\n\t"
"ds_read_b128 a[152:155], v216 offset:18432\n\t"
"v_cvt_pk_bf16_f32 v192, v192, v193\n\t"
"v_mfma_f32_16x16x32_bf16 v[184:187], a[20:23], a[168:171], v[184:187]\n\t"
"ds_read_b128 a[168:171], v216 offset:26624\n\t"
"v_cvt_pk_bf16_f32 v193, v194, v195\n\t"
"v_mfma_f32_16x16x32_bf16 v[188:191], a[16:19], a[156:159], v[188:191]\n\t"
"v_mfma_f32_16x16x32_bf16 v[188:191], a[20:23], a[172:175], v[188:191]\n\t"
"buffer_load_dwordx4 a[136:139], v235, s[8:11], s79 offen offset:2048 sc1\n\t"
"v_mfma_f32_16x16x32_bf16 v[172:175], a[8:11], a[156:159], v[172:175]\n\t"
"v_mfma_f32_16x16x32_bf16 v[172:175], a[12:15], a[172:175], v[172:175]\n\t"
"ds_write_b128 v217, v[208:211] offset:8192\n\t"
"v_mfma_f32_16x16x32_bf16 v[156:159], a[0:3], a[156:159], v[156:159]\n\t"
"v_cvt_pk_bf16_f32 v194, v196, v197\n\t"
"v_cvt_pk_bf16_f32 v195, v198, v199\n\t"
"v_mfma_f32_16x16x32_bf16 v[156:159], a[4:7], a[172:175], v[156:159]\n\t"
"s_waitcnt vmcnt(19) lgkmcnt(4)\n\t"
"buffer_load_dwordx4 a[140:143], v235, s[8:11], s79 offen offset:3072 sc1\n\t"
"v_mfma_f32_16x16x32_bf16 v[96:99], a[24:27], a[144:147], v[96:99]\n\t"
"v_mfma_f32_16x16x32_bf16 v[96:99], a[28:31], a[160:163], v[96:99]\n\t"
"ds_write_b128 v219, v[192:195] offset:8192\n\t"
"v_mfma_f32_16x16x32_bf16 v[112:115], a[32:35], a[144:147], v[112:115]\n\t"
"v_mfma_f32_16x16x32_bf16 v[112:115], a[36:39], a[160:163], v[112:115]\n\t"
"buffer_load_dwordx4 a[132:135], v235, s[8:11], s79 offen offset:1024 sc1\n\t"
"v_mfma_f32_16x16x32_bf16 v[128:131], a[40:43], a[144:147], v[128:131]\n\t"
"v_mfma_f32_16x16x32_bf16 v[128:131], a[44:47], a[160:163], v[128:131]\n\t"
"s_waitcnt lgkmcnt(2)\n\t"
"buffer_load_dwordx4 a[128:131], v235, s[8:11], s79 offen offset:0 sc1\n\t"
"v_mfma_f32_16x16x32_bf16 v[132:135], a[40:43], a[148:151], v[132:135]\n\t"
"ds_read_b128 a[172:175], v216 offset:27648\n\t"
"v_mfma_f32_16x16x32_bf16 v[132:135], a[44:47], a[164:167], v[132:135]\n\t"
"v_mfma_f32_16x16x32_bf16 v[116:119], a[32:35], a[148:151], v[116:119]\n\t"
"ds_read_b128 a[156:159], v216 offset:19456\n\t"
"v_mfma_f32_16x16x32_bf16 v[116:119], a[36:39], a[164:167], v[116:119]\n\t"
"buffer_load_dwordx4 a[120:123], v218, s[8:11], s78 offen offset:2048 sc1\n\t"
"v_mfma_f32_16x16x32_bf16 v[100:103], a[24:27], a[148:151], v[100:103]\n\t"
"v_mfma_f32_16x16x32_bf16 v[100:103], a[28:31], a[164:167], v[100:103]\n\t"
"s_waitcnt lgkmcnt(0)\n\t"
"s_barrier\n\t"
"v_mfma_f32_16x16x32_bf16 v[104:107], a[24:27], a[152:155], v[104:107]\n\t"
"ds_read_b128 a[144:147], v216 offset:4096\n\t"
"v_add_u32 v248, 32768, v225\n\t"
"v_mfma_f32_16x16x32_bf16 v[104:107], a[28:31], a[168:171], v[104:107]\n\t"
"ds_read_b128 a[160:163], v216 offset:12288\n\t"
"v_xor_b32 v249, 32, v248\n\t"
"v_mfma_f32_16x16x32_bf16 v[120:123], a[32:35], a[152:155], v[120:123]\n\t"
"ds_read_b128 a[148:151], v216 offset:5120\n\t"
"v_xor_b32 v250, 64, v248\n\t"
"v_mfma_f32_16x16x32_bf16 v[120:123], a[36:39], a[168:171], v[120:123]\n\t"
"ds_read_b128 a[164:167], v216 offset:13312\n\t"
"v_xor_b32 v251, 96, v248\n\t"
"v_mfma_f32_16x16x32_bf16 v[136:139], a[40:43], a[152:155], v[136:139]\n\t"
"buffer_load_dwordx4 a[124:127], v218, s[8:11], s78 offen offset:3072 sc1\n\t"
"v_mfma_f32_16x16x32_bf16 v[136:139], a[44:47], a[168:171], v[136:139]\n\t"
"ds_read_b128 a[240:243], v223 offset:0\n\t"
"v_mfma_f32_16x16x32_bf16 v[200:203], a[176:179], a[216:219], 0\n\t"
"buffer_load_dwordx4 a[16:19], v235, s[8:11], s74 offen offset:0 sc1\n\t"
"v_mfma_f32_16x16x32_bf16 v[140:143], a[40:43], a[156:159], v[140:143]\n\t"
"s_nop 0\n\t"
"ds_read_b64_tr_b16 a[176:177], v248 offset:0\n\t"
"ds_read_b64_tr_b16 a[178:179], v248 offset:512\n\t"
"v_mfma_f32_16x16x32_bf16 v[140:143], a[44:47], a[172:175], v[140:143]\n\t"
"buffer_load_dwordx4 a[20:23], v235, s[8:11], s74 offen offset:1024 sc1\n\t"
"v_mfma_f32_16x16x32_bf16 v[204:207], a[180:183], a[216:219], 0\n\t"
"ds_read_b64_tr_b16 a[180:181], v249 offset:0\n\t"
"v_mfma_f32_16x16x32_bf16 v[124:127], a[32:35], a[156:159], v[124:127]\n\t"
"ds_read_b128 a[152:155], v216 offset:6144\n\t"
"v_cvt_pk_bf16_f32 v200, v200, v201\n\t"
"v_mfma_f32_16x16x32_bf16 v[124:127], a[36:39], a[172:175], v[124:127]\n\t"
"buffer_load_dwordx4 a[12:15], v218, s[8:11], s72 offen offset:3072 sc1\n\t"
"v_mfma_f32_16x16x32_bf16 v[108:111], a[24:27], a[156:159], v[108:111]\n\t"
"ds_read_b64_tr_b16 a[182:183], v249 offset:512\n\t"
"v_cvt_pk_bf16_f32 v201, v202, v203\n\t"
"v_mfma_f32_16x16x32_bf16 v[108:111], a[28:31], a[172:175], v[108:111]\n\t"
"buffer_load_dwordx4 a[8:11], v218, s[8:11], s72 offen offset:2048 sc1\n\t"
"v_mfma_f32_16x16x32_bf16 v[208:211], a[184:187], a[216:219], 0\n\t"
"s_waitcnt vmcnt(22) lgkmcnt(6)\n\t"
"ds_read_b128 a[168:171], v216 offset:14336\n\t"
"ds_read_b128 a[172:175], v216 offset:15360\n\t"
"v_mfma_f32_16x16x32_bf16 v[48:51], a[48:51], a[144:147], v[48:51]\n\t"
"s_nop 0\n\t"
"ds_read_b64_tr_b16 a[184:185], v250 offset:2304\n\t"
"ds_read_b64_tr_b16 a[186:187], v250 offset:2816\n\t"
"v_mfma_f32_16x16x32_bf16 v[48:51], a[52:55], a[160:163], v[48:51]\n\t"
"buffer_load_dwordx4 a[0:3], v218, s[8:11], s72 offen offset:0 sc1\n\t"
"v_mfma_f32_16x16x32_bf16 v[212:215], a[188:191], a[216:219], 0\n\t"
"ds_read_b64_tr_b16 a[188:189], v251 offset:2304\n\t"
"v_cvt_pk_bf16_f32 v202, v204, v205\n\t"
"v_mfma_f32_16x16x32_bf16 v[64:67], a[56:59], a[144:147], v[64:67]\n\t"
"ds_read_b128 a[156:159], v216 offset:7168\n\t"
"v_cvt_pk_bf16_f32 v208, v208, v209\n\t"
"v_mfma_f32_16x16x32_bf16 v[64:67], a[60:63], a[160:163], v[64:67]\n\t"
"ds_read_b64_tr_b16 a[190:191], v251 offset:2816\n\t"
"v_cvt_pk_bf16_f32 v209, v210, v211\n\t"
"v_mfma_f32_16x16x32_bf16 v[80:83], a[64:67], a[144:147], v[80:83]\n\t"
"v_cvt_pk_bf16_f32 v203, v206, v207\n\t"
"v_mfma_f32_16x16x32_bf16 v[80:83], a[68:71], a[160:163], v[80:83]\n\t"
"ds_write_b128 v217, v[200:203] offset:16384\n\t"
"v_mfma_f32_16x16x32_bf16 v[192:195], a[192:195], a[216:219], 0\n\t"
"buffer_load_dwordx4 a[4:7], v218, s[8:11], s72 offen offset:1024 sc1\n\t"
"v_mfma_f32_16x16x32_bf16 v[84:87], a[64:67], a[148:151], v[84:87]\n\t"
"v_cvt_pk_bf16_f32 v210, v212, v213\n\t"
"v_cvt_pk_bf16_f32 v211, v214, v215\n\t"
"v_mfma_f32_16x16x32_bf16 v[84:87], a[68:71], a[164:167], v[84:87]\n\t"
"buffer_load_dwordx4 a[40:43], v235, s[8:11], s72 offen offset:2048 sc1\n\t"
"v_mfma_f32_16x16x32_bf16 v[68:71], a[56:59], a[148:151], v[68:71]\n\t"
"s_nop 0\n\t"
"ds_read_b128 a[160:163], v216 offset:8192\n\t"
"ds_read_b64_tr_b16 a[192:193], v250 offset:0\n\t"
"v_mfma_f32_16x16x32_bf16 v[68:71], a[60:63], a[164:167], v[68:71]\n\t"
"s_nop 0\n\t"
"ds_read_b128 a[144:147], v216 offset:0\n\t"
"ds_read_b64_tr_b16 a[194:195], v250 offset:512\n\t"
"v_mfma_f32_16x16x32_bf16 v[196:199], a[196:199], a[216:219], 0\n\t"
"ds_read_b64_tr_b16 a[196:197], v251 offset:0\n\t"
"v_cvt_pk_bf16_f32 v192, v192, v193\n\t"
"v_mfma_f32_16x16x32_bf16 v[52:55], a[48:51], a[148:151], v[52:55]\n\t"
"ds_read_b64_tr_b16 a[198:199], v251 offset:512\n\t"
"v_mfma_f32_16x16x32_bf16 v[52:55], a[52:55], a[164:167], v[52:55]\n\t"
"buffer_load_dwordx4 a[44:47], v235, s[8:11], s72 offen offset:3072 sc1\n\t"
"v_mfma_f32_16x16x32_bf16 v[200:203], a[200:203], a[216:219], 0\n\t"
"s_waitcnt lgkmcnt(7)\n\t"
"ds_write_b128 v219, v[208:211] offset:16384\n\t"
"v_mfma_f32_16x16x32_bf16 v[56:59], a[48:51], a[152:155], v[56:59]\n\t"
"v_cvt_pk_bf16_f32 v193, v194, v195\n\t"
"v_mfma_f32_16x16x32_bf16 v[56:59], a[52:55], a[168:171], v[56:59]\n\t"
"buffer_load_dwordx4 a[36:39], v235, s[8:11], s72 offen offset:1024 sc1\n\t"
"v_mfma_f32_16x16x32_bf16 v[72:75], a[56:59], a[152:155], v[72:75]\n\t"
"v_cvt_pk_bf16_f32 v194, v196, v197\n\t"
"v_cvt_pk_bf16_f32 v195, v198, v199\n\t"
"v_mfma_f32_16x16x32_bf16 v[72:75], a[60:63], a[168:171], v[72:75]\n\t"
"ds_read_b128 a[164:167], v216 offset:9216\n\t"
"v_cvt_pk_bf16_f32 v200, v200, v201\n\t"
"v_mfma_f32_16x16x32_bf16 v[204:207], a[204:207], a[216:219], 0\n\t"
"s_nop 0\n\t"
"ds_read_b128 a[148:151], v216 offset:1024\n\t"
"ds_read_b64_tr_b16 a[200:201], v248 offset:2304\n\t"
"v_mfma_f32_16x16x32_bf16 v[88:91], a[64:67], a[152:155], v[88:91]\n\t"
"ds_read_b128 a[152:155], v216 offset:2048\n\t"
"v_cvt_pk_bf16_f32 v201, v202, v203\n\t"
"v_mfma_f32_16x16x32_bf16 v[88:91], a[68:71], a[168:171], v[88:91]\n\t"
"s_nop 0\n\t"
"ds_read_b128 a[168:171], v216 offset:10240\n\t"
"ds_read_b64_tr_b16 a[202:203], v248 offset:2816\n\t"
"v_mfma_f32_16x16x32_bf16 v[92:95], a[64:67], a[156:159], v[92:95]\n\t"
"buffer_load_dwordx4 a[32:35], v235, s[8:11], s72 offen offset:0 sc1\n\t"
"v_mfma_f32_16x16x32_bf16 v[92:95], a[68:71], a[172:175], v[92:95]\n\t"
"ds_write_b128 v217, v[192:195] offset:24576\n\t"
"v_mfma_f32_16x16x32_bf16 v[76:79], a[56:59], a[156:159], v[76:79]\n\t"
"v_cvt_pk_bf16_f32 v202, v204, v205\n\t"
"v_mfma_f32_16x16x32_bf16 v[76:79], a[60:63], a[172:175], v[76:79]\n\t"
"buffer_load_dwordx4 a[24:27], v218, s[8:11], s69 offen offset:2048 sc1\n\t"
"v_mfma_f32_16x16x32_bf16 v[60:63], a[48:51], a[156:159], v[60:63]\n\t"
"v_cvt_pk_bf16_f32 v203, v206, v207\n\t"
"v_mfma_f32_16x16x32_bf16 v[60:63], a[52:55], a[172:175], v[60:63]\n\t"
"s_waitcnt vmcnt(23) lgkmcnt(7)\n\t"
"ds_write_b128 v219, v[200:203] offset:24576\n\t"
"v_mfma_f32_16x16x32_bf16 v[0:3], a[72:75], a[144:147], v[0:3]\n\t"
"v_mfma_f32_16x16x32_bf16 v[0:3], a[76:79], a[160:163], v[0:3]\n\t"
"buffer_load_dwordx4 a[28:31], v218, s[8:11], s69 offen offset:3072 sc1\n\t"
"v_mfma_f32_16x16x32_bf16 v[16:19], a[80:83], a[144:147], v[16:19]\n\t"
"v_mfma_f32_16x16x32_bf16 v[16:19], a[84:87], a[160:163], v[16:19]\n\t"
"buffer_load_dwordx4 a[64:67], v235, s[8:11], s83 offen offset:0 sc1\n\t"
"v_mfma_f32_16x16x32_bf16 v[32:35], a[88:91], a[144:147], v[32:35]\n\t"
"ds_read_b64_tr_b16 a[204:205], v249 offset:2304\n\t"
"v_mfma_f32_16x16x32_bf16 v[32:35], a[92:95], a[160:163], v[32:35]\n\t"
"s_waitcnt lgkmcnt(2)\n\t"
"ds_read_b128 a[172:175], v216 offset:11264\n\t"
"v_mfma_f32_16x16x32_bf16 v[36:39], a[88:91], a[148:151], v[36:39]\n\t"
"ds_read_b64_tr_b16 a[206:207], v249 offset:2816\n\t"
"v_mfma_f32_16x16x32_bf16 v[36:39], a[92:95], a[164:167], v[36:39]\n\t"
"buffer_load_dwordx4 a[68:71], v235, s[8:11], s83 offen offset:1024 sc1\n\t"
"v_mfma_f32_16x16x32_bf16 v[20:23], a[80:83], a[148:151], v[20:23]\n\t"
"ds_read_b128 a[156:159], v216 offset:3072\n\t"
"v_mfma_f32_16x16x32_bf16 v[20:23], a[84:87], a[164:167], v[20:23]\n\t"
"v_mfma_f32_16x16x32_bf16 v[4:7], a[72:75], a[148:151], v[4:7]\n\t"
"buffer_load_dwordx4 a[60:63], v218, s[8:11], s82 offen offset:3072 sc1\n\t"
"v_mfma_f32_16x16x32_bf16 v[4:7], a[76:79], a[164:167], v[4:7]\n\t"
"s_waitcnt lgkmcnt(0)\n\t"
"s_barrier\n\t"
"v_mfma_f32_16x16x32_bf16 v[8:11], a[72:75], a[152:155], v[8:11]\n\t"
"s_nop 0\n\t"
"ds_read_b128 a[144:147], v216 offset:20480\n\t"
"ds_read_b128 a[160:163], v216 offset:28672\n\t"
"v_mfma_f32_16x16x32_bf16 v[8:11], a[76:79], a[168:171], v[8:11]\n\t"
"buffer_load_dwordx4 a[56:59], v218, s[8:11], s82 offen offset:2048 sc1\n\t"
"v_mfma_f32_16x16x32_bf16 v[24:27], a[80:83], a[152:155], v[24:27]\n\t"
"ds_read_b128 a[148:151], v216 offset:21504\n\t"
"v_mfma_f32_16x16x32_bf16 v[24:27], a[84:87], a[168:171], v[24:27]\n\t"
"ds_read_b128 a[164:167], v216 offset:29696\n\t"
"v_mfma_f32_16x16x32_bf16 v[40:43], a[88:91], a[152:155], v[40:43]\n\t"
"ds_read_b128 a[152:155], v216 offset:22528\n\t"
"v_mfma_f32_16x16x32_bf16 v[40:43], a[92:95], a[168:171], v[40:43]\n\t"
"buffer_load_dwordx4 a[48:51], v218, s[8:11], s82 offen offset:0 sc1\n\t"
"v_mfma_f32_16x16x32_bf16 v[208:211], a[176:179], a[208:211], 0\n\t"
"ds_read_b64_tr_b16 a[176:177], v248 offset:1152\n\t"
"v_mfma_f32_16x16x32_bf16 v[44:47], a[88:91], a[156:159], v[44:47]\n\t"
"ds_read_b64_tr_b16 a[178:179], v248 offset:1664\n\t"
"v_mfma_f32_16x16x32_bf16 v[44:47], a[92:95], a[172:175], v[44:47]\n\t"
"buffer_load_dwordx4 a[52:55], v218, s[8:11], s82 offen offset:1024 sc1\n\t"
"v_mfma_f32_16x16x32_bf16 v[212:215], a[180:183], a[208:211], 0\n\t"
"ds_read_b64_tr_b16 a[180:181], v249 offset:1152\n\t"
"v_mfma_f32_16x16x32_bf16 v[28:31], a[80:83], a[156:159], v[28:31]\n\t"
"ds_read_b64_tr_b16 a[182:183], v249 offset:1664\n\t"
"v_cvt_pk_bf16_f32 v208, v208, v209\n\t"
"v_mfma_f32_16x16x32_bf16 v[28:31], a[84:87], a[172:175], v[28:31]\n\t"
"buffer_load_dwordx4 a[88:91], v235, s[8:11], s82 offen offset:2048 sc1\n\t"
"v_mfma_f32_16x16x32_bf16 v[12:15], a[72:75], a[156:159], v[12:15]\n\t"
"ds_read_b128 a[168:171], v216 offset:30720\n\t"
"v_cvt_pk_bf16_f32 v209, v210, v211\n\t"
"v_mfma_f32_16x16x32_bf16 v[12:15], a[76:79], a[172:175], v[12:15]\n\t"
"ds_read_b128 a[156:159], v216 offset:23552\n\t"
"v_cvt_pk_bf16_f32 v210, v212, v213\n\t"
"v_mfma_f32_16x16x32_bf16 v[192:195], a[184:187], a[208:211], 0\n\t"
"s_waitcnt vmcnt(25) lgkmcnt(6)\n\t"
"buffer_load_dwordx4 a[92:95], v235, s[8:11], s82 offen offset:3072 sc1\n\t"
"v_mfma_f32_16x16x32_bf16 v[144:147], a[96:99], a[144:147], v[144:147]\n\t"
"ds_read_b128 a[172:175], v216 offset:31744\n\t"
"v_cvt_pk_bf16_f32 v211, v214, v215\n\t"
"v_mfma_f32_16x16x32_bf16 v[144:147], a[100:103], a[160:163], v[144:147]\n\t"
"ds_read_b64_tr_b16 a[184:185], v250 offset:3456\n\t"
"v_mfma_f32_16x16x32_bf16 v[196:199], a[188:191], a[208:211], 0\n\t"
"ds_read_b64_tr_b16 a[186:187], v250 offset:3968\n\t"
"v_mfma_f32_16x16x32_bf16 v[160:163], a[104:107], a[144:147], v[160:163]\n\t"
"ds_read_b64_tr_b16 a[188:189], v251 offset:3456\n\t"
"v_cvt_pk_bf16_f32 v192, v192, v193\n\t"
"v_mfma_f32_16x16x32_bf16 v[160:163], a[108:111], a[160:163], v[160:163]\n\t"
"buffer_load_dwordx4 a[84:87], v235, s[8:11], s82 offen offset:1024 sc1\n\t"
"v_mfma_f32_16x16x32_bf16 v[176:179], a[112:115], a[144:147], v[176:179]\n\t"
"ds_read_b64_tr_b16 a[190:191], v251 offset:3968\n\t"
"v_cvt_pk_bf16_f32 v193, v194, v195\n\t"
"v_mfma_f32_16x16x32_bf16 v[176:179], a[116:119], a[160:163], v[176:179]\n\t"
"ds_read_b128 a[160:163], v216 offset:24576\n\t"
"v_cvt_pk_bf16_f32 v194, v196, v197\n\t"
"v_mfma_f32_16x16x32_bf16 v[200:203], a[192:195], a[208:211], 0\n\t"
"s_nop 0\n\t"
"ds_read_b128 a[144:147], v216 offset:16384\n\t"
"ds_read_b64_tr_b16 a[192:193], v250 offset:1152\n\t"
"v_mfma_f32_16x16x32_bf16 v[180:183], a[112:115], a[148:151], v[180:183]\n\t"
"buffer_load_dwordx4 a[80:83], v235, s[8:11], s82 offen offset:0 sc1\n\t"
"v_mfma_f32_16x16x32_bf16 v[180:183], a[116:119], a[164:167], v[180:183]\n\t"
"ds_write_b128 v217, v[208:211] offset:0\n\t"
"v_mfma_f32_16x16x32_bf16 v[164:167], a[104:107], a[148:151], v[164:167]\n\t"
"v_cvt_pk_bf16_f32 v195, v198, v199\n\t"
"v_mfma_f32_16x16x32_bf16 v[164:167], a[108:111], a[164:167], v[164:167]\n\t"
"buffer_load_dwordx4 a[72:75], v218, s[8:11], s81 offen offset:2048 sc1\n\t"
"v_mfma_f32_16x16x32_bf16 v[204:207], a[196:199], a[208:211], 0\n\t"
"ds_write_b128 v219, v[192:195] offset:0\n\t"
"v_mfma_f32_16x16x32_bf16 v[148:151], a[96:99], a[148:151], v[148:151]\n\t"
"v_cvt_pk_bf16_f32 v200, v200, v201\n\t"
"v_cvt_pk_bf16_f32 v201, v202, v203\n\t"
"v_mfma_f32_16x16x32_bf16 v[148:151], a[100:103], a[164:167], v[148:151]\n\t"
"buffer_load_dwordx4 a[76:79], v218, s[8:11], s81 offen offset:3072 sc1\n\t"
"v_mfma_f32_16x16x32_bf16 v[208:211], a[200:203], a[208:211], 0\n\t"
"s_waitcnt lgkmcnt(5)\n\t"
"v_mfma_f32_16x16x32_bf16 v[152:155], a[96:99], a[152:155], v[152:155]\n\t"
"ds_read_b128 a[148:151], v216 offset:17408\n\t"
"v_cvt_pk_bf16_f32 v202, v204, v205\n\t"
"v_mfma_f32_16x16x32_bf16 v[152:155], a[100:103], a[168:171], v[152:155]\n\t"
"ds_read_b64_tr_b16 a[194:195], v250 offset:1664\n\t"
"v_cvt_pk_bf16_f32 v203, v206, v207\n\t"
"v_mfma_f32_16x16x32_bf16 v[168:171], a[104:107], a[152:155], v[168:171]\n\t"
"s_nop 0\n\t"
"ds_read_b64_tr_b16 a[196:197], v251 offset:1152\n\t"
"ds_read_b64_tr_b16 a[198:199], v251 offset:1664\n\t"
"v_mfma_f32_16x16x32_bf16 v[168:171], a[108:111], a[168:171], v[168:171]\n\t"
"ds_read_b64_tr_b16 a[200:201], v248 offset:3456\n\t"
"v_cvt_pk_bf16_f32 v208, v208, v209\n\t"
"v_mfma_f32_16x16x32_bf16 v[212:215], a[204:207], a[208:211], 0\n\t"
"s_nop 0\n\t"
"ds_read_b128 a[164:167], v216 offset:25600\n\t"
"ds_read_b64_tr_b16 a[202:203], v248 offset:3968\n\t"
"v_mfma_f32_16x16x32_bf16 v[184:187], a[112:115], a[152:155], v[184:187]\n\t"
"ds_read_b128 a[152:155], v216 offset:18432\n\t"
"v_mfma_f32_16x16x32_bf16 v[184:187], a[116:119], a[168:171], v[184:187]\n\t"
"ds_read_b64_tr_b16 a[204:205], v249 offset:3456\n\t"
"v_cvt_pk_bf16_f32 v209, v210, v211\n\t"
"v_mfma_f32_16x16x32_bf16 v[188:191], a[112:115], a[156:159], v[188:191]\n\t"
"buffer_load_dwordx4 a[112:115], v235, s[8:11], s56 offen offset:0 sc1\n\t"
"v_mfma_f32_16x16x32_bf16 v[188:191], a[116:119], a[172:175], v[188:191]\n\t"
"ds_write_b128 v217, v[200:203] offset:8192\n\t"
"v_mfma_f32_16x16x32_bf16 v[172:175], a[104:107], a[156:159], v[172:175]\n\t"
"v_cvt_pk_bf16_f32 v210, v212, v213\n\t"
"v_cvt_pk_bf16_f32 v211, v214, v215\n\t"
"v_mfma_f32_16x16x32_bf16 v[172:175], a[108:111], a[172:175], v[172:175]\n\t"
"buffer_load_dwordx4 a[116:119], v235, s[8:11], s56 offen offset:1024 sc1\n\t"
"v_mfma_f32_16x16x32_bf16 v[156:159], a[96:99], a[156:159], v[156:159]\n\t"
"v_mfma_f32_16x16x32_bf16 v[156:159], a[100:103], a[172:175], v[156:159]\n\t"
"s_waitcnt vmcnt(26) lgkmcnt(6)\n\t"
"ds_read_b64_tr_b16 a[206:207], v249 offset:3968\n\t"
"v_mfma_f32_16x16x32_bf16 v[96:99], a[120:123], a[144:147], v[96:99]\n\t"
"buffer_load_dwordx4 a[108:111], v218, s[8:11], s55 offen offset:3072 sc1\n\t"
"v_mfma_f32_16x16x32_bf16 v[96:99], a[124:127], a[160:163], v[96:99]\n\t"
"ds_write_b128 v219, v[208:211] offset:8192\n\t"
"v_mfma_f32_16x16x32_bf16 v[112:115], a[128:131], a[144:147], v[112:115]\n\t"
"buffer_load_dwordx4 a[104:107], v218, s[8:11], s55 offen offset:2048 sc1\n\t"
"v_mfma_f32_16x16x32_bf16 v[112:115], a[132:135], a[160:163], v[112:115]\n\t"
"v_pk_fma_f32 v[48:49], v[144:145], s[90:91], v[48:49]\n\t"
"v_pk_fma_f32 v[50:51], v[146:147], s[90:91], v[50:51]\n\t"
"v_pk_add_f32 v[144:145], v[48:49], v[144:145] neg_lo:[0,1] neg_hi:[0,1]\n\t"
"v_pk_add_f32 v[146:147], v[50:51], v[146:147] neg_lo:[0,1] neg_hi:[0,1]\n\t"
"v_pk_fma_f32 v[64:65], v[160:161], s[90:91], v[64:65]\n\t"
"v_pk_fma_f32 v[66:67], v[162:163], s[90:91], v[66:67]\n\t"
"v_pk_add_f32 v[160:161], v[64:65], v[160:161] neg_lo:[0,1] neg_hi:[0,1]\n\t"
"v_pk_add_f32 v[162:163], v[66:67], v[162:163] neg_lo:[0,1] neg_hi:[0,1]\n\t"
"v_pk_fma_f32 v[80:81], v[176:177], s[90:91], v[80:81]\n\t"
"v_pk_fma_f32 v[82:83], v[178:179], s[90:91], v[82:83]\n\t"
"v_pk_add_f32 v[176:177], v[80:81], v[176:177] neg_lo:[0,1] neg_hi:[0,1]\n\t"
"v_pk_add_f32 v[178:179], v[82:83], v[178:179] neg_lo:[0,1] neg_hi:[0,1]\n\t"
"v_pk_fma_f32 v[84:85], v[180:181], s[90:91], v[84:85]\n\t"
"v_pk_fma_f32 v[86:87], v[182:183], s[90:91], v[86:87]\n\t"
"v_pk_add_f32 v[180:181], v[84:85], v[180:181] neg_lo:[0,1] neg_hi:[0,1]\n\t"
"v_pk_add_f32 v[182:183], v[86:87], v[182:183] neg_lo:[0,1] neg_hi:[0,1]\n\t"
"v_pk_fma_f32 v[68:69], v[164:165], s[90:91], v[68:69]\n\t"
"v_pk_fma_f32 v[70:71], v[166:167], s[90:91], v[70:71]\n\t"
"v_pk_add_f32 v[164:165], v[68:69], v[164:165] neg_lo:[0,1] neg_hi:[0,1]\n\t"
"v_pk_add_f32 v[166:167], v[70:71], v[166:167] neg_lo:[0,1] neg_hi:[0,1]\n\t"
"v_pk_fma_f32 v[52:53], v[148:149], s[90:91], v[52:53]\n\t"
"v_pk_fma_f32 v[54:55], v[150:151], s[90:91], v[54:55]\n\t"
"v_pk_add_f32 v[148:149], v[52:53], v[148:149] neg_lo:[0,1] neg_hi:[0,1]\n\t"
"v_pk_add_f32 v[150:151], v[54:55], v[150:151] neg_lo:[0,1] neg_hi:[0,1]\n\t"
"v_pk_fma_f32 v[56:57], v[152:153], s[90:91], v[56:57]\n\t"
"v_pk_fma_f32 v[58:59], v[154:155], s[90:91], v[58:59]\n\t"
"v_pk_add_f32 v[152:153], v[56:57], v[152:153] neg_lo:[0,1] neg_hi:[0,1]\n\t"
"v_pk_add_f32 v[154:155], v[58:59], v[154:155] neg_lo:[0,1] neg_hi:[0,1]\n\t"
"v_pk_fma_f32 v[72:73], v[168:169], s[90:91], v[72:73]\n\t"
"v_pk_fma_f32 v[74:75], v[170:171], s[90:91], v[74:75]\n\t"
"v_pk_add_f32 v[168:169], v[72:73], v[168:169] neg_lo:[0,1] neg_hi:[0,1]\n\t"
"v_pk_add_f32 v[170:171], v[74:75], v[170:171] neg_lo:[0,1] neg_hi:[0,1]\n\t"
"v_pk_fma_f32 v[88:89], v[184:185], s[90:91], v[88:89]\n\t"
"v_pk_fma_f32 v[90:91], v[186:187], s[90:91], v[90:91]\n\t"
"v_pk_add_f32 v[184:185], v[88:89], v[184:185] neg_lo:[0,1] neg_hi:[0,1]\n\t"
"v_pk_add_f32 v[186:187], v[90:91], v[186:187] neg_lo:[0,1] neg_hi:[0,1]\n\t"
"v_pk_fma_f32 v[92:93], v[188:189], s[90:91], v[92:93]\n\t"
"v_pk_fma_f32 v[94:95], v[190:191], s[90:91], v[94:95]\n\t"
"v_pk_add_f32 v[188:189], v[92:93], v[188:189] neg_lo:[0,1] neg_hi:[0,1]\n\t"
"v_pk_add_f32 v[190:191], v[94:95], v[190:191] neg_lo:[0,1] neg_hi:[0,1]\n\t"
"v_pk_fma_f32 v[76:77], v[172:173], s[90:91], v[76:77]\n\t"
"v_pk_fma_f32 v[78:79], v[174:175], s[90:91], v[78:79]\n\t"
"v_pk_add_f32 v[172:173], v[76:77], v[172:173] neg_lo:[0,1] neg_hi:[0,1]\n\t"
"v_pk_add_f32 v[174:175], v[78:79], v[174:175] neg_lo:[0,1] neg_hi:[0,1]\n\t"
"v_pk_fma_f32 v[60:61], v[156:157], s[90:91], v[60:61]\n\t"
"v_pk_fma_f32 v[62:63], v[158:159], s[90:91], v[62:63]\n\t"
"v_pk_add_f32 v[156:157], v[60:61], v[156:157] neg_lo:[0,1] neg_hi:[0,1]\n\t"
"v_pk_add_f32 v[158:159], v[62:63], v[158:159] neg_lo:[0,1] neg_hi:[0,1]\n\t"
"v_mfma_f32_16x16x32_bf16 v[128:131], a[136:139], a[144:147], v[128:131]\n\t"
"buffer_load_dwordx4 a[96:99], v218, s[8:11], s55 offen offset:0 sc1\n\t"
"v_mfma_f32_16x16x32_bf16 v[128:131], a[140:143], a[160:163], v[128:131]\n\t"
"s_waitcnt lgkmcnt(2)\n\t"
"ds_read_b128 a[168:171], v216 offset:26624\n\t"
"v_mfma_f32_16x16x32_bf16 v[132:135], a[136:139], a[148:151], v[132:135]\n\t"
"ds_read_b128 a[172:175], v216 offset:27648\n\t"
"v_mfma_f32_16x16x32_bf16 v[132:135], a[140:143], a[164:167], v[132:135]\n\t"
"buffer_load_dwordx4 a[100:103], v218, s[8:11], s55 offen offset:1024 sc1\n\t"
"v_mfma_f32_16x16x32_bf16 v[116:119], a[128:131], a[148:151], v[116:119]\n\t"
"ds_read_b128 a[156:159], v216 offset:19456\n\t"
"v_mfma_f32_16x16x32_bf16 v[116:119], a[132:135], a[164:167], v[116:119]\n\t"
"v_mfma_f32_16x16x32_bf16 v[100:103], a[120:123], a[148:151], v[100:103]\n\t"
"v_mfma_f32_16x16x32_bf16 v[100:103], a[124:127], a[164:167], v[100:103]\n\t"
"s_waitcnt lgkmcnt(0)\n\t"
"s_barrier\n\t"
"v_mfma_f32_16x16x32_bf16 v[104:107], a[120:123], a[152:155], v[104:107]\n\t"
"s_add_u32 m0, s26, 34816\n\t"
"ds_read_b128 a[144:147], v216 offset:4096\n\t"
"ds_read_b128 a[160:163], v216 offset:12288\n\t"
"v_mfma_f32_16x16x32_bf16 v[104:107], a[124:127], a[168:171], v[104:107]\n\t"
"buffer_load_dwordx4 v236, s[12:15], s22 offen offset:256 nt lds\n\t"
"v_mfma_f32_16x16x32_bf16 v[120:123], a[128:131], a[152:155], v[120:123]\n\t"
"ds_read_b128 a[148:151], v216 offset:5120\n\t"
"v_add_u32 v248, 80896, v225\n\t"
"v_mfma_f32_16x16x32_bf16 v[120:123], a[132:135], a[168:171], v[120:123]\n\t"
"ds_read_b128 a[164:167], v216 offset:13312\n\t"
"v_xor_b32 v249, 32, v248\n\t"
"v_mfma_f32_16x16x32_bf16 v[136:139], a[136:139], a[152:155], v[136:139]\n\t"
"ds_read_b128 a[228:231], v222 offset:39168\n\t"
"v_xor_b32 v250, 64, v248\n\t"
"v_mfma_f32_16x16x32_bf16 v[136:139], a[140:143], a[168:171], v[136:139]\n\t"
"ds_read_b128 a[168:171], v216 offset:14336\n\t"
"v_xor_b32 v251, 96, v248\n\t"
"v_mfma_f32_16x16x32_bf16 v[192:195], a[176:179], a[208:211], 0\n\t"
"ds_read_b128 a[152:155], v216 offset:6144\n\t"
"v_mfma_f32_16x16x32_bf16 v[140:143], a[136:139], a[156:159], v[140:143]\n\t"
"ds_read_b64_tr_b16 a[176:177], v248 offset:41472\n\t"
"v_mfma_f32_16x16x32_bf16 v[140:143], a[140:143], a[172:175], v[140:143]\n\t"
"buffer_load_dwordx4 v237, s[12:15], s22 offen offset:1280 nt lds\n\t"
"v_mfma_f32_16x16x32_bf16 v[196:199], a[180:183], a[208:211], 0\n\t"
"ds_read_b64_tr_b16 a[178:179], v248 offset:41984\n\t"
"v_mfma_f32_16x16x32_bf16 v[124:127], a[128:131], a[156:159], v[124:127]\n\t"
"ds_read_b64_tr_b16 a[180:181], v249 offset:41472\n\t"
"v_cvt_pk_bf16_f32 v192, v192, v193\n\t"
"v_mfma_f32_16x16x32_bf16 v[124:127], a[132:135], a[172:175], v[124:127]\n\t"
"ds_read_b64_tr_b16 a[182:183], v249 offset:41984\n\t"
"v_cvt_pk_bf16_f32 v193, v194, v195\n\t"
"v_mfma_f32_16x16x32_bf16 v[108:111], a[120:123], a[156:159], v[108:111]\n\t"
"ds_read_b128 a[156:159], v216 offset:7168\n\t"
"v_mfma_f32_16x16x32_bf16 v[108:111], a[124:127], a[172:175], v[108:111]\n\t"
"buffer_load_dwordx4 v238, s[12:15], s22 offen offset:2304 nt lds\n\t"
"v_mfma_f32_16x16x32_bf16 v[200:203], a[184:187], a[208:211], 0\n\t"
"s_waitcnt vmcnt(27) lgkmcnt(7)\n\t"
"ds_read_b128 a[172:175], v216 offset:15360\n\t"
"v_cvt_pk_bf16_f32 v194, v196, v197\n\t"
"v_mfma_f32_16x16x32_bf16 v[48:51], a[0:3], a[144:147], v[48:51]\n\t"
"ds_read_b64_tr_b16 a[184:185], v250 offset:43776\n\t"
"v_cvt_pk_bf16_f32 v195, v198, v199\n\t"
"v_mfma_f32_16x16x32_bf16 v[48:51], a[4:7], a[160:163], v[48:51]\n\t"
"ds_read_b64_tr_b16 a[186:187], v250 offset:44288\n\t"
"v_mfma_f32_16x16x32_bf16 v[204:207], a[188:191], a[208:211], 0\n\t"
"ds_read_b64_tr_b16 a[188:189], v251 offset:43776\n\t"
"v_pk_fma_f32 v[0:1], v[96:97], s[90:91], v[0:1]\n\t"
"v_pk_fma_f32 v[2:3], v[98:99], s[90:91], v[2:3]\n\t"
"v_pk_add_f32 v[96:97], v[0:1], v[96:97] neg_lo:[0,1] neg_hi:[0,1]\n\t"
"v_pk_add_f32 v[98:99], v[2:3], v[98:99] neg_lo:[0,1] neg_hi:[0,1]\n\t"
"v_pk_fma_f32 v[16:17], v[112:113], s[90:91], v[16:17]\n\t"
"v_pk_fma_f32 v[18:19], v[114:115], s[90:91], v[18:19]\n\t"
"v_pk_add_f32 v[112:113], v[16:17], v[112:113] neg_lo:[0,1] neg_hi:[0,1]\n\t"
"v_pk_add_f32 v[114:115], v[18:19], v[114:115] neg_lo:[0,1] neg_hi:[0,1]\n\t"
"v_pk_fma_f32 v[32:33], v[128:129], s[90:91], v[32:33]\n\t"
"v_pk_fma_f32 v[34:35], v[130:131], s[90:91], v[34:35]\n\t"
"v_pk_add_f32 v[128:129], v[32:33], v[128:129] neg_lo:[0,1] neg_hi:[0,1]\n\t"
"v_pk_add_f32 v[130:131], v[34:35], v[130:131] neg_lo:[0,1] neg_hi:[0,1]\n\t"
"v_pk_fma_f32 v[36:37], v[132:133], s[90:91], v[36:37]\n\t"
"v_pk_fma_f32 v[38:39], v[134:135], s[90:91], v[38:39]\n\t"
"v_pk_add_f32 v[132:133], v[36:37], v[132:133] neg_lo:[0,1] neg_hi:[0,1]\n\t"
"v_pk_add_f32 v[134:135], v[38:39], v[134:135] neg_lo:[0,1] neg_hi:[0,1]\n\t"
"v_pk_fma_f32 v[20:21], v[116:117], s[90:91], v[20:21]\n\t"
"v_pk_fma_f32 v[22:23], v[118:119], s[90:91], v[22:23]\n\t"
"v_pk_add_f32 v[116:117], v[20:21], v[116:117] neg_lo:[0,1] neg_hi:[0,1]\n\t"
"v_pk_add_f32 v[118:119], v[22:23], v[118:119] neg_lo:[0,1] neg_hi:[0,1]\n\t"
"v_pk_fma_f32 v[4:5], v[100:101], s[90:91], v[4:5]\n\t"
"v_pk_fma_f32 v[6:7], v[102:103], s[90:91], v[6:7]\n\t"
"v_pk_add_f32 v[100:101], v[4:5], v[100:101] neg_lo:[0,1] neg_hi:[0,1]\n\t"
"v_pk_add_f32 v[102:103], v[6:7], v[102:103] neg_lo:[0,1] neg_hi:[0,1]\n\t"
"v_pk_fma_f32 v[8:9], v[104:105], s[90:91], v[8:9]\n\t"
"v_pk_fma_f32 v[10:11], v[106:107], s[90:91], v[10:11]\n\t"
"v_pk_add_f32 v[104:105], v[8:9], v[104:105] neg_lo:[0,1] neg_hi:[0,1]\n\t"
"v_pk_add_f32 v[106:107], v[10:11], v[106:107] neg_lo:[0,1] neg_hi:[0,1]\n\t"
"v_pk_fma_f32 v[24:25], v[120:121], s[90:91], v[24:25]\n\t"
"v_pk_fma_f32 v[26:27], v[122:123], s[90:91], v[26:27]\n\t"
"v_pk_add_f32 v[120:121], v[24:25], v[120:121] neg_lo:[0,1] neg_hi:[0,1]\n\t"
"v_pk_add_f32 v[122:123], v[26:27], v[122:123] neg_lo:[0,1] neg_hi:[0,1]\n\t"
"v_pk_fma_f32 v[40:41], v[136:137], s[90:91], v[40:41]\n\t"
"v_pk_fma_f32 v[42:43], v[138:139], s[90:91], v[42:43]\n\t"
"v_pk_add_f32 v[136:137], v[40:41], v[136:137] neg_lo:[0,1] neg_hi:[0,1]\n\t"
"v_pk_add_f32 v[138:139], v[42:43], v[138:139] neg_lo:[0,1] neg_hi:[0,1]\n\t"
"v_pk_fma_f32 v[44:45], v[140:141], s[90:91], v[44:45]\n\t"
"v_pk_fma_f32 v[46:47], v[142:143], s[90:91], v[46:47]\n\t"
"v_pk_add_f32 v[140:141], v[44:45], v[140:141] neg_lo:[0,1] neg_hi:[0,1]\n\t"
"v_pk_add_f32 v[142:143], v[46:47], v[142:143] neg_lo:[0,1] neg_hi:[0,1]\n\t"
"v_pk_fma_f32 v[28:29], v[124:125], s[90:91], v[28:29]\n\t"
"v_pk_fma_f32 v[30:31], v[126:127], s[90:91], v[30:31]\n\t"
"v_pk_add_f32 v[124:125], v[28:29], v[124:125] neg_lo:[0,1] neg_hi:[0,1]\n\t"
"v_pk_add_f32 v[126:127], v[30:31], v[126:127] neg_lo:[0,1] neg_hi:[0,1]\n\t"
"v_pk_fma_f32 v[12:13], v[108:109], s[90:91], v[12:13]\n\t"
"v_pk_fma_f32 v[14:15], v[110:111], s[90:91], v[14:15]\n\t"
"v_pk_add_f32 v[108:109], v[12:13], v[108:109] neg_lo:[0,1] neg_hi:[0,1]\n\t"
"v_pk_add_f32 v[110:111], v[14:15], v[110:111] neg_lo:[0,1] neg_hi:[0,1]\n\t"
"v_mfma_f32_16x16x32_bf16 v[64:67], a[8:11], a[144:147], v[64:67]\n\t"
"ds_read_b64_tr_b16 a[190:191], v251 offset:44288\n\t"
"v_cvt_pk_bf16_f32 v200, v200, v201\n\t"
"v_mfma_f32_16x16x32_bf16 v[64:67], a[12:15], a[160:163], v[64:67]\n\t"
"buffer_load_dwordx4 v239, s[12:15], s22 offen offset:3328 nt lds\n\t"
"v_mfma_f32_16x16x32_bf16 v[80:83], a[16:19], a[144:147], v[80:83]\n\t"
"ds_read_b128 a[144:147], v216 offset:0\n\t"
"v_cvt_pk_bf16_f32 v201, v202, v203\n\t"
"v_mfma_f32_16x16x32_bf16 v[80:83], a[20:23], a[160:163], v[80:83]\n\t"
"ds_read_b128 a[160:163], v216 offset:8192\n\t"
"v_cvt_pk_bf16_f32 v202, v204, v205\n\t"
"v_mfma_f32_16x16x32_bf16 v[208:211], a[192:195], a[208:211], 0\n\t"
"s_nop 0\n\t"
"ds_read_b64_tr_b16 a[192:193], v250 offset:41472\n\t"
"ds_read_b64_tr_b16 a[194:195], v250 offset:41984\n\t"
"v_mfma_f32_16x16x32_bf16 v[84:87], a[16:19], a[148:151], v[84:87]\n\t"
"v_mfma_f32_16x16x32_bf16 v[84:87], a[20:23], a[164:167], v[84:87]\n\t"
"ds_write_b128 v217, v[192:195] offset:16384\n\t"
"v_mfma_f32_16x16x32_bf16 v[68:71], a[8:11], a[148:151], v[68:71]\n\t"
"v_cvt_pk_bf16_f32 v203, v206, v207\n\t"
"v_mfma_f32_16x16x32_bf16 v[68:71], a[12:15], a[164:167], v[68:71]\n\t"
"s_mov_b64 exec, s[36:37]\n\t"
"buffer_load_dwordx4 v240, s[12:15], s22 offen offset:3840 nt lds\n\t"
"s_mov_b64 exec, -1\n\t"
"v_mfma_f32_16x16x32_bf16 v[212:215], a[196:199], a[208:211], 0\n\t"
"ds_write_b128 v219, v[200:203] offset:16384\n\t"
"v_mfma_f32_16x16x32_bf16 v[52:55], a[0:3], a[148:151], v[52:55]\n\t"
"v_cvt_pk_bf16_f32 v208, v208, v209\n\t"
"v_mfma_f32_16x16x32_bf16 v[52:55], a[4:7], a[164:167], v[52:55]\n\t"
"v_cvt_pk_bf16_f32 v209, v210, v211\n\t"
"v_mfma_f32_16x16x32_bf16 v[192:195], a[200:203], a[208:211], 0\n\t"
"s_waitcnt lgkmcnt(5)\n\t"
"ds_write_b128 v222, a[228:231] offset:0\n\t"
"v_mfma_f32_16x16x32_bf16 v[56:59], a[0:3], a[152:155], v[56:59]\n\t"
"v_cvt_pk_bf16_f32 v210, v212, v213\n\t"
"v_mfma_f32_16x16x32_bf16 v[56:59], a[4:7], a[168:171], v[56:59]\n\t"
"buffer_load_dwordx4 a[136:139], v235, s[8:11], s55 offen offset:2048 sc1\n\t"
"v_mfma_f32_16x16x32_bf16 v[72:75], a[8:11], a[152:155], v[72:75]\n\t"
"v_cvt_pk_bf16_f32 v211, v214, v215\n\t"
"v_mfma_f32_16x16x32_bf16 v[72:75], a[12:15], a[168:171], v[72:75]\n\t"
"s_nop 0\n\t"
"ds_read_b128 a[148:151], v216 offset:1024\n\t"
"ds_read_b128 a[164:167], v216 offset:9216\n\t"
"v_mfma_f32_16x16x32_bf16 v[196:199], a[204:207], a[208:211], 0\n\t"
"ds_read_b64_tr_b16 a[196:197], v251 offset:41472\n\t"
"v_cvt_pk_bf16_f32 v192, v192, v193\n\t"
"v_mfma_f32_16x16x32_bf16 v[88:91], a[16:19], a[152:155], v[88:91]\n\t"
"s_nop 0\n\t"
"ds_read_b128 a[152:155], v216 offset:2048\n\t"
"ds_read_b64_tr_b16 a[198:199], v251 offset:41984\n\t"
"v_mfma_f32_16x16x32_bf16 v[88:91], a[20:23], a[168:171], v[88:91]\n\t"
"ds_read_b64_tr_b16 a[200:201], v248 offset:43776\n\t"
"v_cvt_pk_bf16_f32 v193, v194, v195\n\t"
"v_mfma_f32_16x16x32_bf16 v[92:95], a[16:19], a[156:159], v[92:95]\n\t"
"v_mfma_f32_16x16x32_bf16 v[92:95], a[20:23], a[172:175], v[92:95]\n\t"
"ds_write_b128 v217, v[208:211] offset:24576\n\t"
"v_mfma_f32_16x16x32_bf16 v[76:79], a[8:11], a[156:159], v[76:79]\n\t"
"v_cvt_pk_bf16_f32 v194, v196, v197\n\t"
"v_mfma_f32_16x16x32_bf16 v[76:79], a[12:15], a[172:175], v[76:79]\n\t"
"v_cvt_pk_bf16_f32 v195, v198, v199\n\t"
"v_mfma_f32_16x16x32_bf16 v[60:63], a[0:3], a[156:159], v[60:63]\n\t"
"v_mfma_f32_16x16x32_bf16 v[60:63], a[4:7], a[172:175], v[60:63]\n\t"
"s_waitcnt vmcnt(24) lgkmcnt(7)\n\t"
"ds_write_b128 v219, v[192:195] offset:24576\n\t"
"v_mfma_f32_16x16x32_bf16 v[0:3], a[24:27], a[144:147], v[0:3]\n\t"
"v_mfma_f32_16x16x32_bf16 v[0:3], a[28:31], a[160:163], v[0:3]\n\t"
"v_mfma_f32_16x16x32_bf16 v[16:19], a[32:35], a[144:147], v[16:19]\n\t"
"v_mfma_f32_16x16x32_bf16 v[16:19], a[36:39], a[160:163], v[16:19]\n\t"
"ds_read_b64_tr_b16 a[202:203], v248 offset:44288\n\t"
"v_mfma_f32_16x16x32_bf16 v[32:35], a[40:43], a[144:147], v[32:35]\n\t"
"ds_read_b64_tr_b16 a[204:205], v249 offset:43776\n\t"
"v_mfma_f32_16x16x32_bf16 v[32:35], a[44:47], a[160:163], v[32:35]\n\t"
"s_waitcnt lgkmcnt(3)\n\t"
"ds_read_b64_tr_b16 a[206:207], v249 offset:44288\n\t"
"v_mfma_f32_16x16x32_bf16 v[36:39], a[40:43], a[148:151], v[36:39]\n\t"
"ds_read_b128 a[156:159], v216 offset:3072\n\t"
"v_mfma_f32_16x16x32_bf16 v[36:39], a[44:47], a[164:167], v[36:39]\n\t"
"ds_read_b128 a[168:171], v216 offset:10240\n\t"
"v_mfma_f32_16x16x32_bf16 v[20:23], a[32:35], a[148:151], v[20:23]\n\t"
"ds_read_b128 a[172:175], v216 offset:11264\n\t"
"v_mfma_f32_16x16x32_bf16 v[20:23], a[36:39], a[164:167], v[20:23]\n\t"
"v_mfma_f32_16x16x32_bf16 v[4:7], a[24:27], a[148:151], v[4:7]\n\t"
"v_mfma_f32_16x16x32_bf16 v[4:7], a[28:31], a[164:167], v[4:7]\n\t"
"s_waitcnt lgkmcnt(0)\n\t"
"s_barrier\n\t"
"v_mfma_f32_16x16x32_bf16 v[8:11], a[24:27], a[152:155], v[8:11]\n\t"
"ds_read_b128 a[160:163], v216 offset:28672\n\t"
"v_mfma_f32_16x16x32_bf16 v[8:11], a[28:31], a[168:171], v[8:11]\n\t"
"ds_read_b128 a[144:147], v216 offset:20480\n\t"
"v_mfma_f32_16x16x32_bf16 v[24:27], a[32:35], a[152:155], v[24:27]\n\t"
"ds_read_b128 a[148:151], v216 offset:21504\n\t"
"v_mfma_f32_16x16x32_bf16 v[24:27], a[36:39], a[168:171], v[24:27]\n\t"
"ds_read_b128 a[164:167], v216 offset:29696\n\t"
"v_mfma_f32_16x16x32_bf16 v[40:43], a[40:43], a[152:155], v[40:43]\n\t"
"ds_read_b128 a[152:155], v216 offset:22528\n\t"
"v_mfma_f32_16x16x32_bf16 v[40:43], a[44:47], a[168:171], v[40:43]\n\t"
"ds_read_b128 a[168:171], v216 offset:30720\n\t"
"v_mfma_f32_16x16x32_bf16 v[200:203], a[176:179], a[208:211], 0\n\t"
"ds_read_b64_tr_b16 a[176:177], v248 offset:0\n\t"
"v_mfma_f32_16x16x32_bf16 v[44:47], a[40:43], a[156:159], v[44:47]\n\t"
"ds_read_b64_tr_b16 a[178:179], v248 offset:512\n\t"
"v_mfma_f32_16x16x32_bf16 v[44:47], a[44:47], a[172:175], v[44:47]\n\t"
"buffer_load_dwordx4 a[140:143], v235, s[8:11], s55 offen offset:3072 sc1\n\t"
"v_mfma_f32_16x16x32_bf16 v[204:207], a[180:183], a[208:211], 0\n\t"
"ds_read_b64_tr_b16 a[180:181], v249 offset:0\n\t"
"v_mfma_f32_16x16x32_bf16 v[28:31], a[32:35], a[156:159], v[28:31]\n\t"
"ds_read_b64_tr_b16 a[182:183], v249 offset:512\n\t"
"v_cvt_pk_bf16_f32 v200, v200, v201\n\t"
"v_mfma_f32_16x16x32_bf16 v[28:31], a[36:39], a[172:175], v[28:31]\n\t"
"buffer_load_dwordx4 a[132:135], v235, s[8:11], s55 offen offset:1024 sc1\n\t"
"v_mfma_f32_16x16x32_bf16 v[12:15], a[24:27], a[156:159], v[12:15]\n\t"
"ds_read_b128 a[156:159], v216 offset:23552\n\t"
"v_cvt_pk_bf16_f32 v201, v202, v203\n\t"
"v_mfma_f32_16x16x32_bf16 v[12:15], a[28:31], a[172:175], v[12:15]\n\t"
"ds_read_b128 a[172:175], v216 offset:31744\n\t"
"v_cvt_pk_bf16_f32 v202, v204, v205\n\t"
"v_mfma_f32_16x16x32_bf16 v[208:211], a[184:187], a[208:211], 0\n\t"
"s_waitcnt vmcnt(20) lgkmcnt(7)\n\t"
"buffer_load_dwordx4 a[128:131], v235, s[8:11], s55 offen offset:0 sc1\n\t"
"v_mfma_f32_16x16x32_bf16 v[144:147], a[48:51], a[144:147], v[144:147]\n\t"
"ds_read_b64_tr_b16 a[184:185], v250 offset:2304\n\t"
"v_cvt_pk_bf16_f32 v203, v206, v207\n\t"
"v_mfma_f32_16x16x32_bf16 v[144:147], a[52:55], a[160:163], v[144:147]\n\t"
"ds_read_b64_tr_b16 a[186:187], v250 offset:2816\n\t"
"v_mfma_f32_16x16x32_bf16 v[212:215], a[188:191], a[208:211], 0\n\t"
"ds_read_b64_tr_b16 a[188:189], v251 offset:2304\n\t"
"v_mfma_f32_16x16x32_bf16 v[160:163], a[56:59], a[144:147], v[160:163]\n\t"
"buffer_load_dwordx4 a[120:123], v218, s[8:11], s54 offen offset:2048 sc1\n\t"
"v_mfma_f32_16x16x32_bf16 v[160:163], a[60:63], a[160:163], v[160:163]\n\t"
"ds_write_b128 v217, v[200:203] offset:0\n\t"
"v_mfma_f32_16x16x32_bf16 v[176:179], a[64:67], a[144:147], v[176:179]\n\t"
"v_cvt_pk_bf16_f32 v208, v208, v209\n\t"
"v_cvt_pk_bf16_f32 v209, v210, v211\n\t"
"v_mfma_f32_16x16x32_bf16 v[176:179], a[68:71], a[160:163], v[176:179]\n\t"
"v_cvt_pk_bf16_f32 v210, v212, v213\n\t"
"v_cvt_pk_bf16_f32 v211, v214, v215\n\t"
"v_mfma_f32_16x16x32_bf16 v[192:195], a[192:195], a[208:211], 0\n\t"
"buffer_load_dwordx4 a[124:127], v218, s[8:11], s54 offen offset:3072 sc1\n\t"
"v_mfma_f32_16x16x32_bf16 v[180:183], a[64:67], a[148:151], v[180:183]\n\t"
"ds_read_b64_tr_b16 a[190:191], v251 offset:2816\n\t"
"v_mfma_f32_16x16x32_bf16 v[180:183], a[68:71], a[164:167], v[180:183]\n\t"
"s_nop 0\n\t"
"ds_read_b128 a[160:163], v216 offset:24576\n\t"
"ds_read_b64_tr_b16 a[192:193], v250 offset:0\n\t"
"v_mfma_f32_16x16x32_bf16 v[164:167], a[56:59], a[148:151], v[164:167]\n\t"
"ds_read_b64_tr_b16 a[194:195], v250 offset:512\n\t"
"v_mfma_f32_16x16x32_bf16 v[164:167], a[60:63], a[164:167], v[164:167]\n\t"
"ds_read_b128 a[144:147], v216 offset:16384\n\t"
"v_cvt_pk_bf16_f32 v192, v192, v193\n\t"
"v_mfma_f32_16x16x32_bf16 v[196:199], a[196:199], a[208:211], 0\n\t"
"s_nop 0\n\t"
"ds_read_b64_tr_b16 a[196:197], v251 offset:0\n\t"
"ds_read_b64_tr_b16 a[198:199], v251 offset:512\n\t"
"v_mfma_f32_16x16x32_bf16 v[148:151], a[48:51], a[148:151], v[148:151]\n\t"
"ds_read_b128 a[148:151], v216 offset:17408\n\t"
"v_cvt_pk_bf16_f32 v193, v194, v195\n\t"
"v_mfma_f32_16x16x32_bf16 v[148:151], a[52:55], a[164:167], v[148:151]\n\t"
"buffer_load_dwordx4 a[16:19], v235, s[8:11], s41 offen offset:0 sc1\n\t"
"v_mfma_f32_16x16x32_bf16 v[200:203], a[200:203], a[208:211], 0\n\t"
"s_waitcnt lgkmcnt(8)\n\t"
"ds_write_b128 v219, v[208:211] offset:0\n\t"
"v_mfma_f32_16x16x32_bf16 v[152:155], a[48:51], a[152:155], v[152:155]\n\t"
"v_cvt_pk_bf16_f32 v194, v196, v197\n\t"
"v_cvt_pk_bf16_f32 v195, v198, v199\n\t"
"v_mfma_f32_16x16x32_bf16 v[152:155], a[52:55], a[168:171], v[152:155]\n\t"
"buffer_load_dwordx4 a[20:23], v235, s[8:11], s41 offen offset:1024 sc1\n\t"
"v_mfma_f32_16x16x32_bf16 v[168:171], a[56:59], a[152:155], v[168:171]\n\t"
"v_mfma_f32_16x16x32_bf16 v[168:171], a[60:63], a[168:171], v[168:171]\n\t"
"buffer_load_dwordx4 a[12:15], v218, s[8:11], s40 offen offset:3072 sc1\n\t"
"v_mfma_f32_16x16x32_bf16 v[204:207], a[204:207], a[208:211], 0\n\t"
"ds_read_b128 a[164:167], v216 offset:25600\n\t"
"v_cvt_pk_bf16_f32 v200, v200, v201\n\t"
"v_mfma_f32_16x16x32_bf16 v[184:187], a[64:67], a[152:155], v[184:187]\n\t"
"ds_read_b64_tr_b16 a[200:201], v248 offset:2304\n\t"
"v_cvt_pk_bf16_f32 v201, v202, v203\n\t"
"v_mfma_f32_16x16x32_bf16 v[184:187], a[68:71], a[168:171], v[184:187]\n\t"
"ds_read_b64_tr_b16 a[202:203], v248 offset:2816\n\t"
"v_mfma_f32_16x16x32_bf16 v[188:191], a[64:67], a[156:159], v[188:191]\n\t"
"s_nop 0\n\t"
"ds_read_b128 a[152:155], v216 offset:18432\n\t"
"ds_read_b64_tr_b16 a[204:205], v249 offset:2304\n\t"
"v_mfma_f32_16x16x32_bf16 v[188:191], a[68:71], a[172:175], v[188:191]\n\t"
"ds_read_b64_tr_b16 a[206:207], v249 offset:2816\n\t"
"v_cvt_pk_bf16_f32 v202, v204, v205\n\t"
"v_mfma_f32_16x16x32_bf16 v[172:175], a[56:59], a[156:159], v[172:175]\n\t"
"buffer_load_dwordx4 a[8:11], v218, s[8:11], s40 offen offset:2048 sc1\n\t"
"v_mfma_f32_16x16x32_bf16 v[172:175], a[60:63], a[172:175], v[172:175]\n\t"
"ds_write_b128 v217, v[192:195] offset:8192\n\t"
"v_mfma_f32_16x16x32_bf16 v[156:159], a[48:51], a[156:159], v[156:159]\n\t"
"v_mfma_f32_16x16x32_bf16 v[156:159], a[52:55], a[172:175], v[156:159]\n\t"
"s_waitcnt vmcnt(21) lgkmcnt(7)\n\t"
"buffer_load_dwordx4 a[0:3], v218, s[8:11], s40 offen offset:0 sc1\n\t"
"v_mfma_f32_16x16x32_bf16 v[96:99], a[72:75], a[144:147], v[96:99]\n\t"
"v_cvt_pk_bf16_f32 v203, v206, v207\n\t"
"v_mfma_f32_16x16x32_bf16 v[96:99], a[76:79], a[160:163], v[96:99]\n\t"
"ds_write_b128 v219, v[200:203] offset:8192\n\t"
"v_mfma_f32_16x16x32_bf16 v[112:115], a[80:83], a[144:147], v[112:115]\n\t"
"v_mfma_f32_16x16x32_bf16 v[112:115], a[84:87], a[160:163], v[112:115]\n\t"
"buffer_load_dwordx4 a[4:7], v218, s[8:11], s40 offen offset:1024 sc1\n\t"
"v_mfma_f32_16x16x32_bf16 v[128:131], a[88:91], a[144:147], v[128:131]\n\t"
"v_mfma_f32_16x16x32_bf16 v[128:131], a[92:95], a[160:163], v[128:131]\n\t"
"s_waitcnt lgkmcnt(2)\n\t"
"buffer_load_dwordx4 a[40:43], v235, s[8:11], s40 offen offset:2048 sc1\n\t"
"v_mfma_f32_16x16x32_bf16 v[132:135], a[88:91], a[148:151], v[132:135]\n\t"
"ds_read_b128 a[172:175], v216 offset:27648\n\t"
"v_mfma_f32_16x16x32_bf16 v[132:135], a[92:95], a[164:167], v[132:135]\n\t"
"ds_read_b128 a[156:159], v216 offset:19456\n\t"
"v_mfma_f32_16x16x32_bf16 v[116:119], a[80:83], a[148:151], v[116:119]\n\t"
"ds_read_b128 a[168:171], v216 offset:26624\n\t"
"v_mfma_f32_16x16x32_bf16 v[116:119], a[84:87], a[164:167], v[116:119]\n\t"
"buffer_load_dwordx4 a[44:47], v235, s[8:11], s40 offen offset:3072 sc1\n\t"
"v_mfma_f32_16x16x32_bf16 v[100:103], a[72:75], a[148:151], v[100:103]\n\t"
"v_mfma_f32_16x16x32_bf16 v[100:103], a[76:79], a[164:167], v[100:103]\n\t"
"s_waitcnt lgkmcnt(0)\n\t"
"s_barrier\n\t"
"v_mfma_f32_16x16x32_bf16 v[104:107], a[72:75], a[152:155], v[104:107]\n\t"
"s_nop 0\n\t"
"ds_read_b128 a[144:147], v216 offset:4096\n\t"
"ds_read_b128 a[160:163], v216 offset:12288\n\t"
"v_mfma_f32_16x16x32_bf16 v[104:107], a[76:79], a[168:171], v[104:107]\n\t"
"buffer_load_dwordx4 a[36:39], v235, s[8:11], s40 offen offset:1024 sc1\n\t"
"v_mfma_f32_16x16x32_bf16 v[120:123], a[80:83], a[152:155], v[120:123]\n\t"
"ds_read_b128 a[148:151], v216 offset:5120\n\t"
"v_mfma_f32_16x16x32_bf16 v[120:123], a[84:87], a[168:171], v[120:123]\n\t"
"ds_read_b128 a[164:167], v216 offset:13312\n\t"
"v_mfma_f32_16x16x32_bf16 v[136:139], a[88:91], a[152:155], v[136:139]\n\t"
"ds_read_b128 a[152:155], v216 offset:6144\n\t"
"v_mfma_f32_16x16x32_bf16 v[136:139], a[92:95], a[168:171], v[136:139]\n\t"
"buffer_load_dwordx4 a[32:35], v235, s[8:11], s40 offen offset:0 sc1\n\t"
"v_mfma_f32_16x16x32_bf16 v[208:211], a[176:179], a[208:211], 0\n\t"
"s_nop 0\n\t"
"ds_read_b64_tr_b16 a[176:177], v248 offset:42624\n\t"
"ds_read_b64_tr_b16 a[178:179], v248 offset:43136\n\t"
"v_mfma_f32_16x16x32_bf16 v[140:143], a[88:91], a[156:159], v[140:143]\n\t"
"ds_read_b128 a[168:171], v216 offset:14336\n\t"
"v_mfma_f32_16x16x32_bf16 v[140:143], a[92:95], a[172:175], v[140:143]\n\t"
"buffer_load_dwordx4 a[24:27], v218, s[8:11], 0 offen offset:2048 sc1\n\t"
"v_mfma_f32_16x16x32_bf16 v[212:215], a[180:183], a[208:211], 0\n\t"
"ds_read_b64_tr_b16 a[180:181], v249 offset:42624\n\t"
"v_mfma_f32_16x16x32_bf16 v[124:127], a[80:83], a[156:159], v[124:127]\n\t"
"ds_read_b64_tr_b16 a[182:183], v249 offset:43136\n\t"
"v_cvt_pk_bf16_f32 v208, v208, v209\n\t"
"v_mfma_f32_16x16x32_bf16 v[124:127], a[84:87], a[172:175], v[124:127]\n\t"
"buffer_load_dwordx4 a[28:31], v218, s[8:11], 0 offen offset:3072 sc1\n\t"
"v_mfma_f32_16x16x32_bf16 v[108:111], a[72:75], a[156:159], v[108:111]\n\t"
"ds_read_b128 a[156:159], v216 offset:7168\n\t"
"v_cvt_pk_bf16_f32 v209, v210, v211\n\t"
"v_mfma_f32_16x16x32_bf16 v[108:111], a[76:79], a[172:175], v[108:111]\n\t"
"buffer_load_dwordx4 a[64:67], v235, s[8:11], s68 offen offset:0 sc1\n\t"
"v_mfma_f32_16x16x32_bf16 v[192:195], a[184:187], a[208:211], 0\n\t"
"s_waitcnt vmcnt(24) lgkmcnt(6)\n\t"
"ds_read_b64_tr_b16 a[184:185], v250 offset:44928\n\t"
"v_cvt_pk_bf16_f32 v210, v212, v213\n\t"
"v_mfma_f32_16x16x32_bf16 v[48:51], a[96:99], a[144:147], v[48:51]\n\t"
"ds_read_b128 a[172:175], v216 offset:15360\n\t"
"v_cvt_pk_bf16_f32 v211, v214, v215\n\t"
"v_mfma_f32_16x16x32_bf16 v[48:51], a[100:103], a[160:163], v[48:51]\n\t"
"buffer_load_dwordx4 a[68:71], v235, s[8:11], s68 offen offset:1024 sc1\n\t"
"v_mfma_f32_16x16x32_bf16 v[196:199], a[188:191], a[208:211], 0\n\t"
"ds_read_b64_tr_b16 a[186:187], v250 offset:45440\n\t"
"v_mfma_f32_16x16x32_bf16 v[64:67], a[104:107], a[144:147], v[64:67]\n\t"
"ds_read_b64_tr_b16 a[188:189], v251 offset:44928\n\t"
"v_cvt_pk_bf16_f32 v192, v192, v193\n\t"
"v_mfma_f32_16x16x32_bf16 v[64:67], a[108:111], a[160:163], v[64:67]\n\t"
"ds_read_b64_tr_b16 a[190:191], v251 offset:45440\n\t"
"v_cvt_pk_bf16_f32 v193, v194, v195\n\t"
"v_mfma_f32_16x16x32_bf16 v[80:83], a[112:115], a[144:147], v[80:83]\n\t"
"v_mfma_f32_16x16x32_bf16 v[80:83], a[116:119], a[160:163], v[80:83]\n\t"
"ds_write_b128 v217, v[208:211] offset:16384\n\t"
"v_mfma_f32_16x16x32_bf16 v[200:203], a[192:195], a[208:211], 0\n\t"
"buffer_load_dwordx4 a[60:63], v218, s[8:11], s67 offen offset:3072 sc1\n\t"
"v_mfma_f32_16x16x32_bf16 v[84:87], a[112:115], a[148:151], v[84:87]\n\t"
"v_cvt_pk_bf16_f32 v194, v196, v197\n\t"
"v_cvt_pk_bf16_f32 v195, v198, v199\n\t"
"v_mfma_f32_16x16x32_bf16 v[84:87], a[116:119], a[164:167], v[84:87]\n\t"
"ds_write_b128 v219, v[192:195] offset:16384\n\t"
"v_mfma_f32_16x16x32_bf16 v[68:71], a[104:107], a[148:151], v[68:71]\n\t"
"v_mfma_f32_16x16x32_bf16 v[68:71], a[108:111], a[164:167], v[68:71]\n\t"
"buffer_load_dwordx4 a[56:59], v218, s[8:11], s67 offen offset:2048 sc1\n\t"
"v_mfma_f32_16x16x32_bf16 v[204:207], a[196:199], a[208:211], 0\n\t"
"v_cvt_pk_bf16_f32 v200, v200, v201\n\t"
"v_cvt_pk_bf16_f32 v201, v202, v203\n\t"
"v_mfma_f32_16x16x32_bf16 v[52:55], a[96:99], a[148:151], v[52:55]\n\t"
"s_nop 0\n\t"
"ds_read_b128 a[144:147], v216 offset:0\n\t"
"ds_read_b128 a[160:163], v216 offset:8192\n\t"
"v_mfma_f32_16x16x32_bf16 v[52:55], a[100:103], a[164:167], v[52:55]\n\t"
"buffer_load_dwordx4 a[48:51], v218, s[8:11], s67 offen offset:0 sc1\n\t"
"v_mfma_f32_16x16x32_bf16 v[208:211], a[200:203], a[208:211], 0\n\t"
"s_waitcnt lgkmcnt(4)\n\t"
"ds_read_b128 a[164:167], v216 offset:9216\n\t"
"ds_read_b64_tr_b16 a[192:193], v250 offset:42624\n\t"
"v_mfma_f32_16x16x32_bf16 v[56:59], a[96:99], a[152:155], v[56:59]\n\t"
"s_nop 0\n\t"
"ds_read_b64_tr_b16 a[194:195], v250 offset:43136\n\t"
"ds_read_b64_tr_b16 a[196:197], v251 offset:42624\n\t"
"v_mfma_f32_16x16x32_bf16 v[56:59], a[100:103], a[168:171], v[56:59]\n\t"
"buffer_load_dwordx4 a[52:55], v218, s[8:11], s67 offen offset:1024 sc1\n\t"
"v_mfma_f32_16x16x32_bf16 v[72:75], a[104:107], a[152:155], v[72:75]\n\t"
"ds_read_b128 a[148:151], v216 offset:1024\n\t"
"v_mfma_f32_16x16x32_bf16 v[72:75], a[108:111], a[168:171], v[72:75]\n\t"
"buffer_load_dwordx4 a[88:91], v235, s[8:11], s67 offen offset:2048 sc1\n\t"
"v_mfma_f32_16x16x32_bf16 v[212:215], a[204:207], a[208:211], 0\n\t"
"ds_read_b64_tr_b16 a[198:199], v251 offset:43136\n\t"
"v_cvt_pk_bf16_f32 v208, v208, v209\n\t"
"v_mfma_f32_16x16x32_bf16 v[88:91], a[112:115], a[152:155], v[88:91]\n\t"
"ds_read_b64_tr_b16 a[200:201], v248 offset:44928\n\t"
"v_cvt_pk_bf16_f32 v202, v204, v205\n\t"
"v_mfma_f32_16x16x32_bf16 v[88:91], a[116:119], a[168:171], v[88:91]\n\t"
"ds_read_b64_tr_b16 a[202:203], v248 offset:45440\n\t"
"v_cvt_pk_bf16_f32 v203, v206, v207\n\t"
"v_mfma_f32_16x16x32_bf16 v[92:95], a[112:115], a[156:159], v[92:95]\n\t"
"s_nop 0\n\t"
"ds_read_b128 a[152:155], v216 offset:2048\n\t"
"ds_read_b128 a[168:171], v216 offset:10240\n\t"
"v_mfma_f32_16x16x32_bf16 v[92:95], a[116:119], a[172:175], v[92:95]\n\t"
"ds_read_b64_tr_b16 a[204:205], v249 offset:44928\n\t"
"v_cvt_pk_bf16_f32 v209, v210, v211\n\t"
"v_mfma_f32_16x16x32_bf16 v[76:79], a[104:107], a[156:159], v[76:79]\n\t"
"buffer_load_dwordx4 a[92:95], v235, s[8:11], s67 offen offset:3072 sc1\n\t"
"v_mfma_f32_16x16x32_bf16 v[76:79], a[108:111], a[172:175], v[76:79]\n\t"
"ds_write_b128 v217, v[200:203] offset:24576\n\t"
"v_mfma_f32_16x16x32_bf16 v[60:63], a[96:99], a[156:159], v[60:63]\n\t"
"v_cvt_pk_bf16_f32 v210, v212, v213\n\t"
"v_mfma_f32_16x16x32_bf16 v[60:63], a[100:103], a[172:175], v[60:63]\n\t"
"s_waitcnt vmcnt(20) lgkmcnt(7)\n\t"
"buffer_load_dwordx4 a[84:87], v235, s[8:11], s67 offen offset:1024 sc1\n\t"
"v_mfma_f32_16x16x32_bf16 v[0:3], a[120:123], a[144:147], v[0:3]\n\t"
"v_cvt_pk_bf16_f32 v211, v214, v215\n\t"
"v_mfma_f32_16x16x32_bf16 v[0:3], a[124:127], a[160:163], v[0:3]\n\t"
"ds_write_b128 v219, v[208:211] offset:24576\n\t"
"v_mfma_f32_16x16x32_bf16 v[16:19], a[128:131], a[144:147], v[16:19]\n\t"
"v_mfma_f32_16x16x32_bf16 v[16:19], a[132:135], a[160:163], v[16:19]\n\t"
"buffer_load_dwordx4 a[80:83], v235, s[8:11], s67 offen offset:0 sc1\n\t"
"v_mfma_f32_16x16x32_bf16 v[32:35], a[136:139], a[144:147], v[32:35]\n\t"
"v_mfma_f32_16x16x32_bf16 v[32:35], a[140:143], a[160:163], v[32:35]\n\t"
"ds_read_b128 a[156:159], v216 offset:3072\n\t"
"v_mfma_f32_16x16x32_bf16 v[36:39], a[136:139], a[148:151], v[36:39]\n\t"
"ds_read_b64_tr_b16 a[206:207], v249 offset:45440\n\t"
"v_mfma_f32_16x16x32_bf16 v[36:39], a[140:143], a[164:167], v[36:39]\n\t"
"buffer_load_dwordx4 a[72:75], v218, s[8:11], s66 offen offset:2048 sc1\n\t"
"v_mfma_f32_16x16x32_bf16 v[20:23], a[128:131], a[148:151], v[20:23]\n\t"
"ds_read_b128 a[172:175], v216 offset:11264\n\t"
"v_mfma_f32_16x16x32_bf16 v[20:23], a[132:135], a[164:167], v[20:23]\n\t"
"buffer_load_dwordx4 a[76:79], v218, s[8:11], s66 offen offset:3072 sc1\n\t"
"v_mfma_f32_16x16x32_bf16 v[4:7], a[120:123], a[148:151], v[4:7]\n\t"
"v_mfma_f32_16x16x32_bf16 v[4:7], a[124:127], a[164:167], v[4:7]\n\t"
"s_waitcnt lgkmcnt(0)\n\t"
"s_barrier\n\t"
"v_mfma_f32_16x16x32_bf16 v[8:11], a[120:123], a[152:155], v[8:11]\n\t"
"s_add_u32 m0, s26, 124544\n\t"
"ds_read_b128 a[144:147], v216 offset:20480\n\t"
"ds_read_b128 a[160:163], v216 offset:28672\n\t"
"v_mfma_f32_16x16x32_bf16 v[8:11], a[124:127], a[168:171], v[8:11]\n\t"
"buffer_load_dwordx4 v236, s[12:15], s22 offen offset:128 nt lds\n\t"
"v_mfma_f32_16x16x32_bf16 v[24:27], a[128:131], a[152:155], v[24:27]\n\t"
"ds_read_b128 a[148:151], v216 offset:21504\n\t"
"v_mfma_f32_16x16x32_bf16 v[24:27], a[132:135], a[168:171], v[24:27]\n\t"
"ds_read_b128 a[164:167], v216 offset:29696\n\t"
"v_mfma_f32_16x16x32_bf16 v[40:43], a[136:139], a[152:155], v[40:43]\n\t"
"ds_read_b128 a[152:155], v216 offset:22528\n\t"
"v_mfma_f32_16x16x32_bf16 v[40:43], a[140:143], a[168:171], v[40:43]\n\t"
"ds_read_b128 a[228:231], v223 offset:62208\n\t"
"v_mfma_f32_16x16x32_bf16 v[192:195], a[176:179], a[208:211], 0\n\t"
"ds_read_b64_tr_b16 a[176:177], v248 offset:1152\n\t"
"v_mfma_f32_16x16x32_bf16 v[44:47], a[136:139], a[156:159], v[44:47]\n\t"
"ds_read_b64_tr_b16 a[178:179], v248 offset:1664\n\t"
"v_mfma_f32_16x16x32_bf16 v[44:47], a[140:143], a[172:175], v[44:47]\n\t"
"buffer_load_dwordx4 v237, s[12:15], s22 offen offset:1152 nt lds\n\t"
"v_mfma_f32_16x16x32_bf16 v[196:199], a[180:183], a[208:211], 0\n\t"
"ds_read_b64_tr_b16 a[180:181], v249 offset:1152\n\t"
"v_mfma_f32_16x16x32_bf16 v[28:31], a[128:131], a[156:159], v[28:31]\n\t"
"ds_read_b128 a[168:171], v216 offset:30720\n\t"
"v_cvt_pk_bf16_f32 v192, v192, v193\n\t"
"v_mfma_f32_16x16x32_bf16 v[28:31], a[132:135], a[172:175], v[28:31]\n\t"
"ds_read_b64_tr_b16 a[182:183], v249 offset:1664\n\t"
"v_cvt_pk_bf16_f32 v193, v194, v195\n\t"
"v_mfma_f32_16x16x32_bf16 v[12:15], a[120:123], a[156:159], v[12:15]\n\t"
"v_mfma_f32_16x16x32_bf16 v[12:15], a[124:127], a[172:175], v[12:15]\n\t"
"s_waitcnt lgkmcnt(5)\n\t"
"ds_write_b128 v223, a[228:231] offset:23040\n\t"
"v_mfma_f32_16x16x32_bf16 v[200:203], a[184:187], a[208:211], 0\n\t"
"s_waitcnt vmcnt(20)\n\t"
"buffer_load_dwordx4 v238, s[12:15], s22 offen offset:2176 nt lds\n\t"
"v_mfma_f32_16x16x32_bf16 v[48:51], a[0:3], a[144:147], v[48:51]\n\t"
"v_cvt_pk_bf16_f32 v194, v196, v197\n\t"
"v_cvt_pk_bf16_f32 v195, v198, v199\n\t"
"v_mfma_f32_16x16x32_bf16 v[48:51], a[4:7], a[160:163], v[48:51]\n\t"
"v_mfma_f32_16x16x32_bf16 v[204:207], a[188:191], a[208:211], 0\n\t"
"s_nop 0\n\t"
"ds_read_b128 a[156:159], v216 offset:23552\n\t"
"ds_read_b64_tr_b16 a[184:185], v250 offset:3456\n\t"
"v_mfma_f32_16x16x32_bf16 v[64:67], a[8:11], a[144:147], v[64:67]\n\t"
"ds_read_b64_tr_b16 a[186:187], v250 offset:3968\n\t"
"v_cvt_pk_bf16_f32 v200, v200, v201\n\t"
"v_mfma_f32_16x16x32_bf16 v[64:67], a[12:15], a[160:163], v[64:67]\n\t"
"s_nop 0\n\t"
"ds_read_b128 a[172:175], v216 offset:31744\n\t"
"ds_read_b64_tr_b16 a[188:189], v251 offset:3456\n\t"
"v_mfma_f32_16x16x32_bf16 v[80:83], a[16:19], a[144:147], v[80:83]\n\t"
"ds_read_b64_tr_b16 a[190:191], v251 offset:3968\n\t"
"v_cvt_pk_bf16_f32 v201, v202, v203\n\t"
"v_mfma_f32_16x16x32_bf16 v[80:83], a[20:23], a[160:163], v[80:83]\n\t"
"buffer_load_dwordx4 v239, s[12:15], s22 offen offset:3200 nt lds\n\t"
"v_mfma_f32_16x16x32_bf16 v[208:211], a[192:195], a[208:211], 0\n\t"
"ds_write_b128 v217, v[192:195] offset:0\n\t"
"v_mfma_f32_16x16x32_bf16 v[84:87], a[16:19], a[148:151], v[84:87]\n\t"
"v_cvt_pk_bf16_f32 v202, v204, v205\n\t"
"v_mfma_f32_16x16x32_bf16 v[84:87], a[20:23], a[164:167], v[84:87]\n\t"
"v_cvt_pk_bf16_f32 v203, v206, v207\n\t"
"v_mfma_f32_16x16x32_bf16 v[68:71], a[8:11], a[148:151], v[68:71]\n\t"
"s_mov_b64 exec, s[36:37]\n\t"
"buffer_load_dwordx4 v240, s[12:15], s22 offen offset:3712 nt lds\n\t"
"s_mov_b64 exec, -1\n\t"
"v_mfma_f32_16x16x32_bf16 v[68:71], a[12:15], a[164:167], v[68:71]\n\t"
"s_nop 0\n\t"
"ds_read_b128 a[160:163], v216 offset:24576\n\t"
"ds_read_b64_tr_b16 a[192:193], v250 offset:1152\n\t"
"v_mfma_f32_16x16x32_bf16 v[212:215], a[196:199], a[208:211], 0\n\t"
"s_nop 0\n\t"
"ds_read_b128 a[144:147], v216 offset:16384\n\t"
"ds_read_b64_tr_b16 a[194:195], v250 offset:1664\n\t"
"v_mfma_f32_16x16x32_bf16 v[52:55], a[0:3], a[148:151], v[52:55]\n\t"
"ds_read_b64_tr_b16 a[196:197], v251 offset:1152\n\t"
"v_cvt_pk_bf16_f32 v208, v208, v209\n\t"
"v_mfma_f32_16x16x32_bf16 v[52:55], a[4:7], a[164:167], v[52:55]\n\t"
"s_nop 0\n\t"
"ds_read_b128 a[148:151], v216 offset:17408\n\t"
"ds_read_b128 a[164:167], v216 offset:25600\n\t"
"v_mfma_f32_16x16x32_bf16 v[192:195], a[200:203], a[208:211], 0\n\t"
"s_waitcnt lgkmcnt(8)\n\t"
"ds_read_b64_tr_b16 a[198:199], v251 offset:1664\n\t"
"v_cvt_pk_bf16_f32 v209, v210, v211\n\t"
"v_mfma_f32_16x16x32_bf16 v[56:59], a[0:3], a[152:155], v[56:59]\n\t"
"v_cvt_pk_bf16_f32 v210, v212, v213\n\t"
"v_mfma_f32_16x16x32_bf16 v[56:59], a[4:7], a[168:171], v[56:59]\n\t"
"ds_write_b128 v219, v[200:203] offset:0\n\t"
"v_mfma_f32_16x16x32_bf16 v[72:75], a[8:11], a[152:155], v[72:75]\n\t"
"v_mfma_f32_16x16x32_bf16 v[72:75], a[12:15], a[168:171], v[72:75]\n\t"
"v_mfma_f32_16x16x32_bf16 v[196:199], a[204:207], a[208:211], 0\n\t"
"v_cvt_pk_bf16_f32 v211, v214, v215\n\t"
"v_cvt_pk_bf16_f32 v192, v192, v193\n\t"
"v_mfma_f32_16x16x32_bf16 v[88:91], a[16:19], a[152:155], v[88:91]\n\t"
"ds_read_b64_tr_b16 a[200:201], v248 offset:3456\n\t"
"v_cvt_pk_bf16_f32 v193, v194, v195\n\t"
"v_mfma_f32_16x16x32_bf16 v[88:91], a[20:23], a[168:171], v[88:91]\n\t"
"ds_read_b64_tr_b16 a[202:203], v248 offset:3968\n\t"
"v_mfma_f32_16x16x32_bf16 v[92:95], a[16:19], a[156:159], v[92:95]\n\t"
"v_mfma_f32_16x16x32_bf16 v[92:95], a[20:23], a[172:175], v[92:95]\n\t"
"ds_write_b128 v217, v[208:211] offset:8192\n\t"
"v_mfma_f32_16x16x32_bf16 v[76:79], a[8:11], a[156:159], v[76:79]\n\t"
"v_cvt_pk_bf16_f32 v194, v196, v197\n\t"
"v_cvt_pk_bf16_f32 v195, v198, v199\n\t"
"v_mfma_f32_16x16x32_bf16 v[76:79], a[12:15], a[172:175], v[76:79]\n\t"
"v_mfma_f32_16x16x32_bf16 v[60:63], a[0:3], a[156:159], v[60:63]\n\t"
"v_mfma_f32_16x16x32_bf16 v[60:63], a[4:7], a[172:175], v[60:63]\n\t"
"s_waitcnt vmcnt(17) lgkmcnt(3)\n\t"
"ds_write_b128 v219, v[192:195] offset:8192\n\t"
"v_mfma_f32_16x16x32_bf16 v[0:3], a[24:27], a[144:147], v[0:3]\n\t"
"v_mfma_f32_16x16x32_bf16 v[0:3], a[28:31], a[160:163], v[0:3]\n\t"
"v_mfma_f32_16x16x32_bf16 v[16:19], a[32:35], a[144:147], v[16:19]\n\t"
"v_mfma_f32_16x16x32_bf16 v[16:19], a[36:39], a[160:163], v[16:19]\n\t"
"ds_read_b128 a[152:155], v216 offset:18432\n\t"
"v_mfma_f32_16x16x32_bf16 v[32:35], a[40:43], a[144:147], v[32:35]\n\t"
"ds_read_b128 a[172:175], v216 offset:27648\n\t"
"v_mfma_f32_16x16x32_bf16 v[32:35], a[44:47], a[160:163], v[32:35]\n\t"
"ds_read_b64_tr_b16 a[204:205], v249 offset:3456\n\t"
"v_mfma_f32_16x16x32_bf16 v[36:39], a[40:43], a[148:151], v[36:39]\n\t"
"ds_read_b64_tr_b16 a[206:207], v249 offset:3968\n\t"
"v_mfma_f32_16x16x32_bf16 v[36:39], a[44:47], a[164:167], v[36:39]\n\t"
"ds_read_b128 a[168:171], v216 offset:26624\n\t"
"v_mfma_f32_16x16x32_bf16 v[20:23], a[32:35], a[148:151], v[20:23]\n\t"
"ds_read_b128 a[156:159], v216 offset:19456\n\t"
"v_mfma_f32_16x16x32_bf16 v[20:23], a[36:39], a[164:167], v[20:23]\n\t"
"v_mfma_f32_16x16x32_bf16 v[4:7], a[24:27], a[148:151], v[4:7]\n\t"
"v_mfma_f32_16x16x32_bf16 v[4:7], a[28:31], a[164:167], v[4:7]\n\t"
"s_waitcnt lgkmcnt(0)\n\t"
"s_barrier\n\t"
"v_mfma_f32_16x16x32_bf16 v[8:11], a[24:27], a[152:155], v[8:11]\n\t"
"ds_read_b128 a[160:163], v216 offset:12288\n\t"
"v_add_u32 v248, 32768, v227\n\t"
"v_mfma_f32_16x16x32_bf16 v[8:11], a[28:31], a[168:171], v[8:11]\n\t"
"ds_read_b128 a[144:147], v216 offset:4096\n\t"
"v_xor_b32 v249, 32, v248\n\t"
"v_mfma_f32_16x16x32_bf16 v[24:27], a[32:35], a[152:155], v[24:27]\n\t"
"ds_read_b128 a[148:151], v216 offset:5120\n\t"
"v_mfma_f32_16x16x32_bf16 v[24:27], a[36:39], a[168:171], v[24:27]\n\t"
"ds_read_b128 a[164:167], v216 offset:13312\n\t"
"v_xor_b32 v250, 64, v248\n\t"
"v_mfma_f32_16x16x32_bf16 v[40:43], a[40:43], a[152:155], v[40:43]\n\t"
"v_mfma_f32_16x16x32_bf16 v[40:43], a[44:47], a[168:171], v[40:43]\n\t"
"ds_read_b128 a[168:171], v216 offset:14336\n\t"
"v_mfma_f32_16x16x32_bf16 v[200:203], a[176:179], a[208:211], 0\n\t"
"buffer_load_dwordx4 a[112:115], v235, s[8:11], s53 offen offset:0 sc1\n\t"
"v_mfma_f32_16x16x32_bf16 v[44:47], a[40:43], a[156:159], v[44:47]\n\t"
"ds_read_b128 a[152:155], v216 offset:6144\n\t"
"v_xor_b32 v251, 96, v248\n\t"
"v_mfma_f32_16x16x32_bf16 v[44:47], a[44:47], a[172:175], v[44:47]\n\t"
"ds_read_b64_tr_b16 a[176:177], v248 offset:20736\n\t"
"v_mfma_f32_16x16x32_bf16 v[204:207], a[180:183], a[208:211], 0\n\t"
"ds_read_b64_tr_b16 a[178:179], v248 offset:21248\n\t"
"v_mfma_f32_16x16x32_bf16 v[28:31], a[32:35], a[156:159], v[28:31]\n\t"
"ds_read_b64_tr_b16 a[180:181], v249 offset:20736\n\t"
"v_cvt_pk_bf16_f32 v200, v200, v201\n\t"
"v_mfma_f32_16x16x32_bf16 v[28:31], a[36:39], a[172:175], v[28:31]\n\t"
"ds_read_b64_tr_b16 a[182:183], v249 offset:21248\n\t"
"v_cvt_pk_bf16_f32 v201, v202, v203\n\t"
"v_mfma_f32_16x16x32_bf16 v[12:15], a[24:27], a[156:159], v[12:15]\n\t"
"ds_read_b128 a[156:159], v216 offset:7168\n\t"
"v_mfma_f32_16x16x32_bf16 v[12:15], a[28:31], a[172:175], v[12:15]\n\t"
"ds_read_b128 a[172:175], v216 offset:15360\n\t"
"v_cvt_pk_bf16_f32 v202, v204, v205\n\t"
"v_mfma_f32_16x16x32_bf16 v[208:211], a[184:187], a[208:211], 0\n\t"
"s_waitcnt vmcnt(12) lgkmcnt(8)\n\t"
"ds_read_b64_tr_b16 a[184:185], v250 offset:20736\n\t"
"v_mfma_f32_16x16x32_bf16 v[144:147], a[48:51], a[144:147], v[144:147]\n\t"
"ds_read_b64_tr_b16 a[186:187], v250 offset:21248\n\t"
"v_mfma_f32_16x16x32_bf16 v[144:147], a[52:55], a[160:163], v[144:147]\n\t"
"v_cvt_pk_bf16_f32 v203, v206, v207\n\t"
"v_mfma_f32_16x16x32_bf16 v[212:215], a[188:191], a[208:211], 0\n\t"
"ds_read_b64_tr_b16 a[188:189], v251 offset:20736\n\t"
"ds_write_b128 v217, v[200:203] offset:16384\n\t"
"v_pk_add_f32 v[0:1], v[0:1], v[48:49]\n\t"
"v_pk_add_f32 v[2:3], v[2:3], v[50:51]\n\t"
"v_pk_fma_f32 v[48:49], v[48:49], s[38:39], v[0:1]\n\t"
"v_pk_fma_f32 v[50:51], v[50:51], s[38:39], v[2:3]\n\t"
"v_pk_add_f32 v[16:17], v[16:17], v[64:65]\n\t"
"v_pk_add_f32 v[18:19], v[18:19], v[66:67]\n\t"
"v_pk_fma_f32 v[64:65], v[64:65], s[38:39], v[16:17]\n\t"
"v_pk_fma_f32 v[66:67], v[66:67], s[38:39], v[18:19]\n\t"
"v_pk_add_f32 v[32:33], v[32:33], v[80:81]\n\t"
"v_pk_add_f32 v[34:35], v[34:35], v[82:83]\n\t"
"v_pk_fma_f32 v[80:81], v[80:81], s[38:39], v[32:33]\n\t"
"v_pk_fma_f32 v[82:83], v[82:83], s[38:39], v[34:35]\n\t"
"v_pk_add_f32 v[36:37], v[36:37], v[84:85]\n\t"
"v_pk_add_f32 v[38:39], v[38:39], v[86:87]\n\t"
"v_pk_fma_f32 v[84:85], v[84:85], s[38:39], v[36:37]\n\t"
"v_pk_fma_f32 v[86:87], v[86:87], s[38:39], v[38:39]\n\t"
"v_pk_add_f32 v[20:21], v[20:21], v[68:69]\n\t"
"v_pk_add_f32 v[22:23], v[22:23], v[70:71]\n\t"
"v_pk_fma_f32 v[68:69], v[68:69], s[38:39], v[20:21]\n\t"
"v_pk_fma_f32 v[70:71], v[70:71], s[38:39], v[22:23]\n\t"
"v_pk_add_f32 v[4:5], v[4:5], v[52:53]\n\t"
"v_pk_add_f32 v[6:7], v[6:7], v[54:55]\n\t"
"v_pk_fma_f32 v[52:53], v[52:53], s[38:39], v[4:5]\n\t"
"v_pk_fma_f32 v[54:55], v[54:55], s[38:39], v[6:7]\n\t"
"v_pk_add_f32 v[8:9], v[8:9], v[56:57]\n\t"
"v_pk_add_f32 v[10:11], v[10:11], v[58:59]\n\t"
"v_pk_fma_f32 v[56:57], v[56:57], s[38:39], v[8:9]\n\t"
"v_pk_fma_f32 v[58:59], v[58:59], s[38:39], v[10:11]\n\t"
"v_pk_add_f32 v[24:25], v[24:25], v[72:73]\n\t"
"v_pk_add_f32 v[26:27], v[26:27], v[74:75]\n\t"
"v_pk_fma_f32 v[72:73], v[72:73], s[38:39], v[24:25]\n\t"
"v_pk_fma_f32 v[74:75], v[74:75], s[38:39], v[26:27]\n\t"
"v_pk_add_f32 v[40:41], v[40:41], v[88:89]\n\t"
"v_pk_add_f32 v[42:43], v[42:43], v[90:91]\n\t"
"v_pk_fma_f32 v[88:89], v[88:89], s[38:39], v[40:41]\n\t"
"v_pk_fma_f32 v[90:91], v[90:91], s[38:39], v[42:43]\n\t"
"v_pk_add_f32 v[44:45], v[44:45], v[92:93]\n\t"
"v_pk_add_f32 v[46:47], v[46:47], v[94:95]\n\t"
"v_pk_fma_f32 v[92:93], v[92:93], s[38:39], v[44:45]\n\t"
"v_pk_fma_f32 v[94:95], v[94:95], s[38:39], v[46:47]\n\t"
"v_pk_add_f32 v[28:29], v[28:29], v[76:77]\n\t"
"v_pk_add_f32 v[30:31], v[30:31], v[78:79]\n\t"
"v_pk_fma_f32 v[76:77], v[76:77], s[38:39], v[28:29]\n\t"
"v_pk_fma_f32 v[78:79], v[78:79], s[38:39], v[30:31]\n\t"
"v_pk_add_f32 v[12:13], v[12:13], v[60:61]\n\t"
"v_pk_add_f32 v[14:15], v[14:15], v[62:63]\n\t"
"v_pk_fma_f32 v[60:61], v[60:61], s[38:39], v[12:13]\n\t"
"v_pk_fma_f32 v[62:63], v[62:63], s[38:39], v[14:15]\n\t"
"v_cvt_pk_bf16_f32 v0, v0, v1\n\t"
"v_cvt_pk_bf16_f32 v4, v4, v5\n\t"
"v_cvt_pk_bf16_f32 v16, v16, v17\n\t"
"v_cvt_pk_bf16_f32 v20, v20, v21\n\t"
"v_cvt_pk_bf16_f32 v32, v32, v33\n\t"
"v_cvt_pk_bf16_f32 v40, v40, v41\n\t"
"v_mfma_f32_16x16x32_bf16 v[160:163], a[56:59], a[144:147], v[160:163]\n\t"
"v_cvt_pk_bf16_f32 v36, v36, v37\n\t"
"v_cvt_pk_bf16_f32 v28, v28, v29\n\t"
"v_mfma_f32_16x16x32_bf16 v[160:163], a[60:63], a[160:163], v[160:163]\n\t"
"v_cvt_pk_bf16_f32 v1, v2, v3\n\t"
"v_cvt_pk_bf16_f32 v5, v6, v7\n\t"
"v_cvt_pk_bf16_f32 v24, v24, v25\n\t"
"v_mfma_f32_16x16x32_bf16 v[176:179], a[64:67], a[144:147], v[176:179]\n\t"
"v_cvt_pk_bf16_f32 v208, v208, v209\n\t"
"v_cvt_pk_bf16_f32 v25, v26, v27\n\t"
"v_mfma_f32_16x16x32_bf16 v[176:179], a[68:71], a[160:163], v[176:179]\n\t"
"s_nop 0\n\t"
"ds_read_b128 a[144:147], v216 offset:0\n\t"
"ds_read_b128 a[160:163], v216 offset:8192\n\t"
"ds_read_b64_tr_b16 a[190:191], v251 offset:21248\n\t"
"ds_write2st64_b64 v228, v[0:1], v[4:5] offset0:39 offset1:52\n\t"
"v_cvt_pk_bf16_f32 v8, v8, v9\n\t"
"v_mfma_f32_16x16x32_bf16 v[192:195], a[192:195], a[208:211], 0\n\t"
"v_cvt_pk_bf16_f32 v209, v210, v211\n\t"
"v_cvt_pk_bf16_f32 v21, v22, v23\n\t"
"v_cvt_pk_bf16_f32 v37, v38, v39\n\t"
"v_mfma_f32_16x16x32_bf16 v[180:183], a[64:67], a[148:151], v[180:183]\n\t"
"v_cvt_pk_bf16_f32 v17, v18, v19\n\t"
"v_cvt_pk_bf16_f32 v12, v12, v13\n\t"
"v_mfma_f32_16x16x32_bf16 v[180:183], a[68:71], a[164:167], v[180:183]\n\t"
"buffer_load_dwordx4 a[116:119], v235, s[8:11], s53 offen offset:1024 sc1\n\t"
"ds_write2st64_b64 v246, v[16:17], v[20:21] offset0:39 offset1:52\n\t"
"v_cvt_pk_bf16_f32 v210, v212, v213\n\t"
"v_mfma_f32_16x16x32_bf16 v[164:167], a[56:59], a[148:151], v[164:167]\n\t"
"v_cvt_pk_bf16_f32 v33, v34, v35\n\t"
"v_cvt_pk_bf16_f32 v9, v10, v11\n\t"
"v_mfma_f32_16x16x32_bf16 v[164:167], a[60:63], a[164:167], v[164:167]\n\t"
"v_cvt_pk_bf16_f32 v211, v214, v215\n\t"
"v_cvt_pk_bf16_f32 v192, v192, v193\n\t"
"v_mfma_f32_16x16x32_bf16 v[196:199], a[196:199], a[208:211], 0\n\t"
"ds_write2st64_b64 v247, v[32:33], v[36:37] offset0:39 offset1:52\n\t"
"v_mfma_f32_16x16x32_bf16 v[148:151], a[48:51], a[148:151], v[148:151]\n\t"
"v_cvt_pk_bf16_f32 v193, v194, v195\n\t"
"v_cvt_pk_bf16_f32 v29, v30, v31\n\t"
"v_mfma_f32_16x16x32_bf16 v[148:151], a[52:55], a[164:167], v[148:151]\n\t"
"buffer_load_dwordx4 a[108:111], v218, s[8:11], s52 offen offset:3072 sc1\n\t"
"buffer_load_dwordx4 a[104:107], v218, s[8:11], s52 offen offset:2048 sc1\n\t"
"v_mfma_f32_16x16x32_bf16 v[200:203], a[200:203], a[208:211], 0\n\t"
"s_waitcnt lgkmcnt(6)\n\t"
"ds_write_b128 v219, v[208:211] offset:16384\n\t"
"v_mfma_f32_16x16x32_bf16 v[152:155], a[48:51], a[152:155], v[152:155]\n\t"
"buffer_load_dwordx4 a[96:99], v218, s[8:11], s52 offen offset:0 sc1\n\t"
"v_mfma_f32_16x16x32_bf16 v[152:155], a[52:55], a[168:171], v[152:155]\n\t"
"ds_write2st64_b64 v246, v[24:25], v[28:29] offset0:65 offset1:78\n\t"
"v_mfma_f32_16x16x32_bf16 v[168:171], a[56:59], a[152:155], v[168:171]\n\t"
"v_cvt_pk_bf16_f32 v194, v196, v197\n\t"
"v_cvt_pk_bf16_f32 v13, v14, v15\n\t"
"v_mfma_f32_16x16x32_bf16 v[168:171], a[60:63], a[168:171], v[168:171]\n\t"
"s_nop 0\n\t"
"ds_read_b128 a[148:151], v216 offset:1024\n\t"
"ds_read_b128 a[164:167], v216 offset:9216\n\t"
"ds_read_b64_tr_b16 a[192:193], v248 offset:21888\n\t"
"ds_read_b64_tr_b16 a[194:195], v248 offset:22400\n\t"
"ds_read_b64_tr_b16 a[196:197], v249 offset:21888\n\t"
"ds_read_b64_tr_b16 a[198:199], v249 offset:22400\n\t"
"v_mfma_f32_16x16x32_bf16 v[204:207], a[204:207], a[208:211], 0\n\t"
"ds_write2st64_b64 v228, v[8:9], v[12:13] offset0:65 offset1:78\n\t"
"v_mfma_f32_16x16x32_bf16 v[184:187], a[64:67], a[152:155], v[184:187]\n\t"
"v_cvt_pk_bf16_f32 v195, v198, v199\n\t"
"v_cvt_pk_bf16_f32 v200, v200, v201\n\t"
"v_mfma_f32_16x16x32_bf16 v[184:187], a[68:71], a[168:171], v[184:187]\n\t"
"buffer_load_dwordx4 a[100:103], v218, s[8:11], s52 offen offset:1024 sc1\n\t"
"v_mfma_f32_16x16x32_bf16 v[188:191], a[64:67], a[156:159], v[188:191]\n\t"
"v_cvt_pk_bf16_f32 v201, v202, v203\n\t"
"v_cvt_pk_bf16_f32 v44, v44, v45\n\t"
"v_mfma_f32_16x16x32_bf16 v[188:191], a[68:71], a[172:175], v[188:191]\n\t"
"ds_write_b128 v217, v[192:195] offset:24576\n\t"
"v_mfma_f32_16x16x32_bf16 v[172:175], a[56:59], a[156:159], v[172:175]\n\t"
"v_cvt_pk_bf16_f32 v202, v204, v205\n\t"
"v_cvt_pk_bf16_f32 v203, v206, v207\n\t"
"v_mfma_f32_16x16x32_bf16 v[172:175], a[60:63], a[172:175], v[172:175]\n\t"
"buffer_load_dwordx4 a[136:139], v235, s[8:11], s52 offen offset:2048 sc1\n\t"
"v_mfma_f32_16x16x32_bf16 v[156:159], a[48:51], a[156:159], v[156:159]\n\t"
"v_cvt_pk_bf16_f32 v41, v42, v43\n\t"
"v_cvt_pk_bf16_f32 v45, v46, v47\n\t"
"v_mfma_f32_16x16x32_bf16 v[156:159], a[52:55], a[172:175], v[156:159]\n\t"
"s_waitcnt vmcnt(12) lgkmcnt(8)\n\t"
"ds_write_b128 v219, v[200:203] offset:24576\n\t"
"v_mfma_f32_16x16x32_bf16 v[96:99], a[72:75], a[144:147], v[96:99]\n\t"
"buffer_load_dwordx4 a[140:143], v235, s[8:11], s52 offen offset:3072 sc1\n\t"
"v_mfma_f32_16x16x32_bf16 v[96:99], a[76:79], a[160:163], v[96:99]\n\t"
"ds_write2st64_b64 v247, v[40:41], v[44:45] offset0:65 offset1:78\n\t"
"v_mfma_f32_16x16x32_bf16 v[112:115], a[80:83], a[144:147], v[112:115]\n\t"
"buffer_load_dwordx4 a[132:135], v235, s[8:11], s52 offen offset:1024 sc1\n\t"
"v_mfma_f32_16x16x32_bf16 v[112:115], a[84:87], a[160:163], v[112:115]\n\t"
"s_nop 0\n\t"
"ds_read_b64_tr_b16 a[200:201], v250 offset:21888\n\t"
"ds_read_b64_tr_b16 a[202:203], v250 offset:22400\n\t"
"v_mfma_f32_16x16x32_bf16 v[128:131], a[88:91], a[144:147], v[128:131]\n\t"
"buffer_load_dwordx4 a[128:131], v235, s[8:11], s52 offen offset:0 sc1\n\t"
"v_mfma_f32_16x16x32_bf16 v[128:131], a[92:95], a[160:163], v[128:131]\n\t"
"s_waitcnt lgkmcnt(4)\n\t"
"ds_read_b128 a[152:155], v216 offset:2048\n\t"
"ds_read_b128 a[172:175], v216 offset:11264\n\t"
"v_mfma_f32_16x16x32_bf16 v[132:135], a[88:91], a[148:151], v[132:135]\n\t"
"s_nop 0\n\t"
"ds_read_b64_tr_b16 a[204:205], v251 offset:21888\n\t"
"ds_read_b64_tr_b16 a[206:207], v251 offset:22400\n\t"
"v_mfma_f32_16x16x32_bf16 v[132:135], a[92:95], a[164:167], v[132:135]\n\t"
"buffer_load_dwordx4 a[120:123], v218, s[8:11], s51 offen offset:2048 sc1\n\t"
"v_mfma_f32_16x16x32_bf16 v[116:119], a[80:83], a[148:151], v[116:119]\n\t"
"s_nop 0\n\t"
"ds_read_b128 a[168:171], v216 offset:10240\n\t"
"ds_read_b128 a[156:159], v216 offset:3072\n\t"
"v_mfma_f32_16x16x32_bf16 v[116:119], a[84:87], a[164:167], v[116:119]\n\t"
"v_mfma_f32_16x16x32_bf16 v[100:103], a[72:75], a[148:151], v[100:103]\n\t"
"buffer_load_dwordx4 a[124:127], v218, s[8:11], s51 offen offset:3072 sc1\n\t"
"v_mfma_f32_16x16x32_bf16 v[100:103], a[76:79], a[164:167], v[100:103]\n\t"
"s_waitcnt lgkmcnt(0)\n\t"
"s_barrier\n\t"
"s_cmp_eq_u32 s21, 1\n\t"
"s_cbranch_scc1 Ltail_%=\n\t"
"v_mfma_f32_16x16x32_bf16 v[104:107], a[72:75], a[152:155], v[104:107]\n\t"
"s_nop 0\n\t"
"ds_read_b128 a[144:147], v216 offset:20480\n\t"
"ds_read_b128 a[160:163], v216 offset:28672\n\t"
"v_mfma_f32_16x16x32_bf16 v[104:107], a[76:79], a[168:171], v[104:107]\n\t"
"buffer_load_dwordx4 a[16:19], v218, s[84:87], s69 offen offset:0 sc1\n\t"
"v_mfma_f32_16x16x32_bf16 v[120:123], a[80:83], a[152:155], v[120:123]\n\t"
"s_nop 0\n\t"
"ds_read_b128 a[148:151], v216 offset:21504\n\t"
"ds_read_b128 a[164:167], v216 offset:29696\n\t"
"v_mfma_f32_16x16x32_bf16 v[120:123], a[84:87], a[168:171], v[120:123]\n\t"
"buffer_load_dwordx4 a[20:23], v218, s[84:87], s69 offen offset:1024 sc1\n\t"
"v_mfma_f32_16x16x32_bf16 v[136:139], a[88:91], a[152:155], v[136:139]\n\t"
"ds_read_b128 v[8:11], v229 offset:19968\n\t"
"v_mfma_f32_16x16x32_bf16 v[136:139], a[92:95], a[168:171], v[136:139]\n\t"
"s_nop 0\n\t"
"ds_read_b128 a[152:155], v216 offset:22528\n\t"
"ds_read_b128 a[168:171], v216 offset:30720\n\t"
"v_mfma_f32_16x16x32_bf16 v[192:195], a[176:179], a[224:227], 0\n\t"
"buffer_load_dwordx4 a[12:15], v235, s[84:87], s69 offen offset:3072 sc1\n\t"
"v_mfma_f32_16x16x32_bf16 v[140:143], a[88:91], a[156:159], v[140:143]\n\t"
"ds_read_b64_tr_b16 a[176:177], v248 offset:0\n\t"
"v_mfma_f32_16x16x32_bf16 v[140:143], a[92:95], a[172:175], v[140:143]\n\t"
"ds_read_b64_tr_b16 a[178:179], v248 offset:512\n\t"
"v_mfma_f32_16x16x32_bf16 v[196:199], a[180:183], a[224:227], 0\n\t"
"ds_read_b64_tr_b16 a[180:181], v249 offset:0\n\t"
"v_mfma_f32_16x16x32_bf16 v[124:127], a[80:83], a[156:159], v[124:127]\n\t"
"ds_read_b64_tr_b16 a[182:183], v249 offset:512\n\t"
"v_cvt_pk_bf16_f32 v192, v192, v193\n\t"
"v_mfma_f32_16x16x32_bf16 v[124:127], a[84:87], a[172:175], v[124:127]\n\t"
"s_waitcnt lgkmcnt(6)\n\t"
"buffer_store_dwordx4 v[8:11], v232, s[16:19], s24 offen nt\n\t"
"v_mfma_f32_16x16x32_bf16 v[108:111], a[72:75], a[156:159], v[108:111]\n\t"
"ds_read_b128 a[156:159], v216 offset:23552\n\t"
"v_cvt_pk_bf16_f32 v193, v194, v195\n\t"
"v_mfma_f32_16x16x32_bf16 v[108:111], a[76:79], a[172:175], v[108:111]\n\t"
"ds_read_b128 v[24:27], v230 offset:19968\n\t"
"v_cvt_pk_bf16_f32 v194, v196, v197\n\t"
"v_mfma_f32_16x16x32_bf16 v[200:203], a[184:187], a[224:227], 0\n\t"
"s_waitcnt vmcnt(10)\n\t"
"buffer_load_dwordx4 a[8:11], v235, s[84:87], s69 offen offset:2048 sc1\n\t"
"v_mfma_f32_16x16x32_bf16 v[144:147], a[96:99], a[144:147], v[144:147]\n\t"
"ds_read_b64_tr_b16 a[184:185], v250 offset:0\n\t"
"v_cvt_pk_bf16_f32 v195, v198, v199\n\t"
"v_mfma_f32_16x16x32_bf16 v[144:147], a[100:103], a[160:163], v[144:147]\n\t"
"buffer_load_dwordx4 a[0:3], v235, s[84:87], s69 offen offset:0 sc1\n\t"
"v_mfma_f32_16x16x32_bf16 v[204:207], a[188:191], a[224:227], 0\n\t"
"s_nop 0\n\t"
"ds_read_b128 a[172:175], v216 offset:31744\n\t"
"ds_read_b64_tr_b16 a[186:187], v250 offset:512\n\t"
"v_mfma_f32_16x16x32_bf16 v[160:163], a[104:107], a[144:147], v[160:163]\n\t"
"ds_read_b64_tr_b16 a[188:189], v251 offset:0\n\t"
"v_cvt_pk_bf16_f32 v200, v200, v201\n\t"
"v_mfma_f32_16x16x32_bf16 v[160:163], a[108:111], a[160:163], v[160:163]\n\t"
"ds_read_b64_tr_b16 a[190:191], v251 offset:512\n\t"
"v_cvt_pk_bf16_f32 v201, v202, v203\n\t"
"v_mfma_f32_16x16x32_bf16 v[176:179], a[112:115], a[144:147], v[176:179]\n\t"
"ds_read_b128 a[144:147], v216 offset:16384\n\t"
"v_mfma_f32_16x16x32_bf16 v[176:179], a[116:119], a[160:163], v[176:179]\n\t"
"buffer_load_dwordx4 a[4:7], v235, s[84:87], s69 offen offset:1024 sc1\n\t"
"v_mfma_f32_16x16x32_bf16 v[208:211], a[192:195], a[224:227], 0\n\t"
"ds_write_b128 v221, v[192:195] offset:0\n\t"
"v_mfma_f32_16x16x32_bf16 v[180:183], a[112:115], a[148:151], v[180:183]\n\t"
"v_cvt_pk_bf16_f32 v202, v204, v205\n\t"
"v_cvt_pk_bf16_f32 v203, v206, v207\n\t"
"v_mfma_f32_16x16x32_bf16 v[180:183], a[116:119], a[164:167], v[180:183]\n\t"
"buffer_load_dwordx4 a[40:43], v218, s[84:87], s81 offen offset:0 sc1\n\t"
"v_mfma_f32_16x16x32_bf16 v[164:167], a[104:107], a[148:151], v[164:167]\n\t"
"v_mfma_f32_16x16x32_bf16 v[164:167], a[108:111], a[164:167], v[164:167]\n\t"
"s_nop 0\n\t"
"ds_read_b128 a[160:163], v216 offset:24576\n\t"
"ds_read_b128 v[28:31], v230 offset:33280\n\t"
"v_mfma_f32_16x16x32_bf16 v[212:215], a[196:199], a[224:227], 0\n\t"
"s_waitcnt lgkmcnt(6)\n\t"
"buffer_store_dwordx4 v[24:27], v233, s[16:19], s24 offen nt\n\t"
"v_mfma_f32_16x16x32_bf16 v[148:151], a[96:99], a[148:151], v[148:151]\n\t"
"v_cvt_pk_bf16_f32 v208, v208, v209\n\t"
"v_cvt_pk_bf16_f32 v209, v210, v211\n\t"
"v_mfma_f32_16x16x32_bf16 v[148:151], a[100:103], a[164:167], v[148:151]\n\t"
"ds_write_b128 v221, v[200:203] offset:8192\n\t"
"v_mfma_f32_16x16x32_bf16 v[192:195], a[200:203], a[224:227], 0\n\t"
"buffer_load_dwordx4 a[44:47], v218, s[84:87], s81 offen offset:1024 sc1\n\t"
"v_mfma_f32_16x16x32_bf16 v[152:155], a[96:99], a[152:155], v[152:155]\n\t"
"v_cvt_pk_bf16_f32 v210, v212, v213\n\t"
"v_cvt_pk_bf16_f32 v211, v214, v215\n\t"
"v_mfma_f32_16x16x32_bf16 v[152:155], a[100:103], a[168:171], v[152:155]\n\t"
"buffer_load_dwordx4 a[36:39], v235, s[84:87], s81 offen offset:3072 sc1\n\t"
"v_mfma_f32_16x16x32_bf16 v[168:171], a[104:107], a[152:155], v[168:171]\n\t"
"s_nop 0\n\t"
"ds_read_b64_tr_b16 a[192:193], v248 offset:1152\n\t"
"ds_read_b128 v[40:43], v231 offset:19968\n\t"
"v_mfma_f32_16x16x32_bf16 v[168:171], a[108:111], a[168:171], v[168:171]\n\t"
"buffer_load_dwordx4 a[32:35], v235, s[84:87], s81 offen offset:2048 sc1\n\t"
"v_mfma_f32_16x16x32_bf16 v[196:199], a[204:207], a[224:227], 0\n\t"
"s_nop 0\n\t"
"ds_read_b128 a[148:151], v216 offset:17408\n\t"
"ds_read_b128 a[164:167], v216 offset:25600\n\t"
"v_mfma_f32_16x16x32_bf16 v[184:187], a[112:115], a[152:155], v[184:187]\n\t"
"ds_read_b64_tr_b16 a[194:195], v248 offset:1664\n\t"
"v_cvt_pk_bf16_f32 v192, v192, v193\n\t"
"v_mfma_f32_16x16x32_bf16 v[184:187], a[116:119], a[168:171], v[184:187]\n\t"
"s_nop 0\n\t"
"ds_read_b64_tr_b16 a[196:197], v249 offset:1152\n\t"
"ds_read_b64_tr_b16 a[198:199], v249 offset:1664\n\t"
"v_mfma_f32_16x16x32_bf16 v[188:191], a[112:115], a[156:159], v[188:191]\n\t"
"s_nop 0\n\t"
"ds_read_b64_tr_b16 a[200:201], v250 offset:1152\n\t"
"ds_read_b128 v[44:47], v231 offset:33280\n\t"
"v_mfma_f32_16x16x32_bf16 v[188:191], a[116:119], a[172:175], v[188:191]\n\t"
"ds_read_b64_tr_b16 a[202:203], v250 offset:1664\n\t"
"v_cvt_pk_bf16_f32 v193, v194, v195\n\t"
"v_mfma_f32_16x16x32_bf16 v[172:175], a[104:107], a[156:159], v[172:175]\n\t"
"buffer_load_dwordx4 a[24:27], v235, s[84:87], s81 offen offset:0 sc1\n\t"
"v_mfma_f32_16x16x32_bf16 v[172:175], a[108:111], a[172:175], v[172:175]\n\t"
"ds_read_b128 v[12:15], v229 offset:33280\n\t"
"ds_write_b128 v221, v[208:211] offset:4096\n\t"
"v_mfma_f32_16x16x32_bf16 v[156:159], a[96:99], a[156:159], v[156:159]\n\t"
"v_cvt_pk_bf16_f32 v194, v196, v197\n\t"
"v_cvt_pk_bf16_f32 v195, v198, v199\n\t"
"v_mfma_f32_16x16x32_bf16 v[156:159], a[100:103], a[172:175], v[156:159]\n\t"
"s_waitcnt vmcnt(13) lgkmcnt(10)\n\t"
"buffer_store_dwordx4 v[40:43], v234, s[16:19], s24 offen nt\n\t"
"v_mfma_f32_16x16x32_bf16 v[96:99], a[120:123], a[144:147], v[96:99]\n\t"
"v_mfma_f32_16x16x32_bf16 v[96:99], a[124:127], a[160:163], v[96:99]\n\t"
"ds_write_b128 v221, v[192:195] offset:12288\n\t"
"v_mfma_f32_16x16x32_bf16 v[112:115], a[128:131], a[144:147], v[112:115]\n\t"
"buffer_load_dwordx4 a[28:31], v235, s[84:87], s81 offen offset:1024 sc1\n\t"
"v_mfma_f32_16x16x32_bf16 v[112:115], a[132:135], a[160:163], v[112:115]\n\t"
"v_mfma_f32_16x16x32_bf16 v[128:131], a[136:139], a[144:147], v[128:131]\n\t"
"buffer_load_dwordx4 a[64:67], v218, s[84:87], s54 offen offset:0 sc1\n\t"
"v_mfma_f32_16x16x32_bf16 v[128:131], a[140:143], a[160:163], v[128:131]\n\t"
"s_waitcnt lgkmcnt(3)\n\t"
"ds_read_b128 a[152:155], v216 offset:18432\n\t"
"ds_read_b64_tr_b16 a[204:205], v251 offset:1152\n\t"
"v_mfma_f32_16x16x32_bf16 v[132:135], a[136:139], a[148:151], v[132:135]\n\t"
"buffer_load_dwordx4 a[68:71], v218, s[84:87], s54 offen offset:1024 sc1\n\t"
"v_mfma_f32_16x16x32_bf16 v[132:135], a[140:143], a[164:167], v[132:135]\n\t"
"s_nop 0\n\t"
"ds_read_b128 a[168:171], v216 offset:26624\n\t"
"ds_read_b64_tr_b16 a[206:207], v251 offset:1664\n\t"
"v_mfma_f32_16x16x32_bf16 v[116:119], a[128:131], a[148:151], v[116:119]\n\t"
"s_nop 0\n\t"
"ds_read_b128 a[156:159], v216 offset:19456\n\t"
"ds_read_b128 a[172:175], v216 offset:27648\n\t"
"v_mfma_f32_16x16x32_bf16 v[116:119], a[132:135], a[164:167], v[116:119]\n\t"
"s_waitcnt lgkmcnt(7)\n\t"
"buffer_store_dwordx4 v[12:15], v232, s[16:19], s27 offen nt\n\t"
"v_mfma_f32_16x16x32_bf16 v[100:103], a[120:123], a[148:151], v[100:103]\n\t"
"v_mfma_f32_16x16x32_bf16 v[100:103], a[124:127], a[164:167], v[100:103]\n\t"
"buffer_load_dwordx4 a[60:63], v235, s[84:87], s54 offen offset:3072 sc1\n\t"
"s_waitcnt lgkmcnt(0)\n\t"
"s_barrier\n\t"
"v_mfma_f32_16x16x32_bf16 v[104:107], a[120:123], a[152:155], v[104:107]\n\t"
"ds_read_b128 a[144:147], v216 offset:0\n\t"
"v_add_u32 v248, 101632, v227\n\t"
"v_mfma_f32_16x16x32_bf16 v[104:107], a[124:127], a[168:171], v[104:107]\n\t"
"buffer_load_dwordx4 a[56:59], v235, s[84:87], s54 offen offset:2048 sc1\n\t"
"v_mfma_f32_16x16x32_bf16 v[120:123], a[128:131], a[152:155], v[120:123]\n\t"
"s_nop 0\n\t"
"ds_read_b128 a[160:163], v216 offset:8192\n\t"
"ds_read_b128 a[148:151], v216 offset:1024\n\t"
"v_mfma_f32_16x16x32_bf16 v[120:123], a[132:135], a[168:171], v[120:123]\n\t"
"ds_read_b128 a[164:167], v216 offset:9216\n\t"
"v_xor_b32 v249, 32, v248\n\t"
"v_mfma_f32_16x16x32_bf16 v[136:139], a[136:139], a[152:155], v[136:139]\n\t"
"ds_read_b128 a[152:155], v216 offset:2048\n\t"
"v_xor_b32 v250, 64, v248\n\t"
"v_mfma_f32_16x16x32_bf16 v[136:139], a[140:143], a[168:171], v[136:139]\n\t"
"buffer_load_dwordx4 a[48:51], v235, s[84:87], s54 offen offset:0 sc1\n\t"
"v_mfma_f32_16x16x32_bf16 v[200:203], a[176:179], v[252:255], 0\n\t"
"ds_read_b128 a[168:171], v216 offset:10240\n\t"
"v_xor_b32 v251, 96, v248\n\t"
"v_mfma_f32_16x16x32_bf16 v[140:143], a[136:139], a[156:159], v[140:143]\n\t"
"s_nop 0\n\t"
"ds_read_b64_tr_b16 a[176:177], v248 offset:41472\n\t"
"ds_read_b64_tr_b16 a[178:179], v248 offset:41984\n\t"
"v_mfma_f32_16x16x32_bf16 v[140:143], a[140:143], a[172:175], v[140:143]\n\t"
"buffer_load_dwordx4 a[52:55], v235, s[84:87], s54 offen offset:1024 sc1\n\t"
"v_mfma_f32_16x16x32_bf16 v[204:207], a[180:183], v[252:255], 0\n\t"
"ds_read_b64_tr_b16 a[180:181], v249 offset:41472\n\t"
"v_mfma_f32_16x16x32_bf16 v[124:127], a[128:131], a[156:159], v[124:127]\n\t"
"ds_read_b64_tr_b16 a[182:183], v249 offset:41984\n\t"
"v_cvt_pk_bf16_f32 v200, v200, v201\n\t"
"v_mfma_f32_16x16x32_bf16 v[124:127], a[132:135], a[172:175], v[124:127]\n\t"
"buffer_load_dwordx4 a[88:91], v218, s[84:87], s66 offen offset:0 sc1\n\t"
"v_mfma_f32_16x16x32_bf16 v[108:111], a[120:123], a[156:159], v[108:111]\n\t"
"ds_read_b128 a[156:159], v216 offset:3072\n\t"
"v_cvt_pk_bf16_f32 v201, v202, v203\n\t"
"v_mfma_f32_16x16x32_bf16 v[108:111], a[124:127], a[172:175], v[108:111]\n\t"
"ds_read_b128 a[172:175], v216 offset:11264\n\t"
"buffer_load_dwordx4 a[92:95], v218, s[84:87], s66 offen offset:1024 sc1\n\t"
"v_mfma_f32_16x16x32_bf16 v[208:211], a[184:187], v[252:255], 0\n\t"
"s_waitcnt vmcnt(17) lgkmcnt(7)\n\t"
"ds_read_b64_tr_b16 a[184:185], v250 offset:41472\n\t"
"v_cvt_pk_bf16_f32 v202, v204, v205\n\t"
"v_mfma_f32_16x16x32_bf16 v[48:51], a[0:3], a[144:147], v[48:51]\n\t"
"ds_read_b64_tr_b16 a[186:187], v250 offset:41984\n\t"
"v_cvt_pk_bf16_f32 v203, v206, v207\n\t"
"v_mfma_f32_16x16x32_bf16 v[48:51], a[4:7], a[160:163], v[48:51]\n\t"
"buffer_store_dwordx4 v[28:31], v233, s[16:19], s27 offen nt\n\t"
"v_mfma_f32_16x16x32_bf16 v[212:215], a[188:191], v[252:255], 0\n\t"
"ds_read_b64_tr_b16 a[188:189], v251 offset:41472\n\t"
"ds_write_b128 v221, v[200:203] offset:16384\n\t"
"v_pk_add_f32 v[96:97], v[96:97], v[144:145]\n\t"
"v_pk_add_f32 v[98:99], v[98:99], v[146:147]\n\t"
"v_pk_fma_f32 v[144:145], v[144:145], s[38:39], v[96:97]\n\t"
"v_pk_fma_f32 v[146:147], v[146:147], s[38:39], v[98:99]\n\t"
"v_pk_add_f32 v[112:113], v[112:113], v[160:161]\n\t"
"v_pk_add_f32 v[114:115], v[114:115], v[162:163]\n\t"
"v_pk_fma_f32 v[160:161], v[160:161], s[38:39], v[112:113]\n\t"
"v_pk_fma_f32 v[162:163], v[162:163], s[38:39], v[114:115]\n\t"
"v_pk_add_f32 v[128:129], v[128:129], v[176:177]\n\t"
"v_pk_add_f32 v[130:131], v[130:131], v[178:179]\n\t"
"v_pk_fma_f32 v[176:177], v[176:177], s[38:39], v[128:129]\n\t"
"v_pk_fma_f32 v[178:179], v[178:179], s[38:39], v[130:131]\n\t"
"v_pk_add_f32 v[132:133], v[132:133], v[180:181]\n\t"
"v_pk_add_f32 v[134:135], v[134:135], v[182:183]\n\t"
"v_pk_fma_f32 v[180:181], v[180:181], s[38:39], v[132:133]\n\t"
"v_pk_fma_f32 v[182:183], v[182:183], s[38:39], v[134:135]\n\t"
"v_pk_add_f32 v[116:117], v[116:117], v[164:165]\n\t"
"v_pk_add_f32 v[118:119], v[118:119], v[166:167]\n\t"
"v_pk_fma_f32 v[164:165], v[164:165], s[38:39], v[116:117]\n\t"
"v_pk_fma_f32 v[166:167], v[166:167], s[38:39], v[118:119]\n\t"
"v_pk_add_f32 v[100:101], v[100:101], v[148:149]\n\t"
"v_pk_add_f32 v[102:103], v[102:103], v[150:151]\n\t"
"v_pk_fma_f32 v[148:149], v[148:149], s[38:39], v[100:101]\n\t"
"v_pk_fma_f32 v[150:151], v[150:151], s[38:39], v[102:103]\n\t"
"v_pk_add_f32 v[104:105], v[104:105], v[152:153]\n\t"
"v_pk_add_f32 v[106:107], v[106:107], v[154:155]\n\t"
"v_pk_fma_f32 v[152:153], v[152:153], s[38:39], v[104:105]\n\t"
"v_pk_fma_f32 v[154:155], v[154:155], s[38:39], v[106:107]\n\t"
"v_pk_add_f32 v[120:121], v[120:121], v[168:169]\n\t"
"v_pk_add_f32 v[122:123], v[122:123], v[170:171]\n\t"
"v_pk_fma_f32 v[168:169], v[168:169], s[38:39], v[120:121]\n\t"
"v_pk_fma_f32 v[170:171], v[170:171], s[38:39], v[122:123]\n\t"
"v_pk_add_f32 v[136:137], v[136:137], v[184:185]\n\t"
"v_pk_add_f32 v[138:139], v[138:139], v[186:187]\n\t"
"v_pk_fma_f32 v[184:185], v[184:185], s[38:39], v[136:137]\n\t"
"v_pk_fma_f32 v[186:187], v[186:187], s[38:39], v[138:139]\n\t"
"v_pk_add_f32 v[140:141], v[140:141], v[188:189]\n\t"
"v_pk_add_f32 v[142:143], v[142:143], v[190:191]\n\t"
"v_pk_fma_f32 v[188:189], v[188:189], s[38:39], v[140:141]\n\t"
"v_pk_fma_f32 v[190:191], v[190:191], s[38:39], v[142:143]\n\t"
"v_pk_add_f32 v[124:125], v[124:125], v[172:173]\n\t"
"v_pk_add_f32 v[126:127], v[126:127], v[174:175]\n\t"
"v_pk_fma_f32 v[172:173], v[172:173], s[38:39], v[124:125]\n\t"
"v_pk_fma_f32 v[174:175], v[174:175], s[38:39], v[126:127]\n\t"
"v_pk_add_f32 v[108:109], v[108:109], v[156:157]\n\t"
"v_pk_add_f32 v[110:111], v[110:111], v[158:159]\n\t"
"v_pk_fma_f32 v[156:157], v[156:157], s[38:39], v[108:109]\n\t"
"v_pk_fma_f32 v[158:159], v[158:159], s[38:39], v[110:111]\n\t"
"v_cvt_pk_bf16_f32 v96, v96, v97\n\t"
"v_cvt_pk_bf16_f32 v100, v100, v101\n\t"
"v_cvt_pk_bf16_f32 v101, v102, v103\n\t"
"v_cvt_pk_bf16_f32 v112, v112, v113\n\t"
"v_cvt_pk_bf16_f32 v132, v132, v133\n\t"
"v_mfma_f32_16x16x32_bf16 v[64:67], a[8:11], a[144:147], v[64:67]\n\t"
"v_cvt_pk_bf16_f32 v128, v128, v129\n\t"
"v_cvt_pk_bf16_f32 v129, v130, v131\n\t"
"v_mfma_f32_16x16x32_bf16 v[64:67], a[12:15], a[160:163], v[64:67]\n\t"
"buffer_load_dwordx4 a[84:87], v235, s[84:87], s66 offen offset:3072 sc1\n\t"
"buffer_load_dwordx4 a[80:83], v235, s[84:87], s66 offen offset:2048 sc1\n\t"
"buffer_load_dwordx4 a[72:75], v235, s[84:87], s66 offen offset:0 sc1\n\t"
"buffer_load_dwordx4 a[76:79], v235, s[84:87], s66 offen offset:1024 sc1\n\t"
"buffer_load_dwordx4 a[112:115], v218, s[84:87], 0 offen offset:0 sc1\n\t"
"v_cvt_pk_bf16_f32 v97, v98, v99\n\t"
"v_cvt_pk_bf16_f32 v116, v116, v117\n\t"
"v_cvt_pk_bf16_f32 v108, v108, v109\n\t"
"v_cvt_pk_bf16_f32 v124, v124, v125\n\t"
"v_mfma_f32_16x16x32_bf16 v[80:83], a[16:19], a[144:147], v[80:83]\n\t"
"v_cvt_pk_bf16_f32 v113, v114, v115\n\t"
"v_cvt_pk_bf16_f32 v136, v136, v137\n\t"
"v_mfma_f32_16x16x32_bf16 v[80:83], a[20:23], a[160:163], v[80:83]\n\t"
"ds_read_b64_tr_b16 a[190:191], v251 offset:41984\n\t"
"ds_write2st64_b64 v228, v[96:97], v[100:101] offset0:39 offset1:52\n\t"
"v_cvt_pk_bf16_f32 v208, v208, v209\n\t"
"v_mfma_f32_16x16x32_bf16 v[192:195], a[192:195], v[252:255], 0\n\t"
"v_cvt_pk_bf16_f32 v117, v118, v119\n\t"
"v_cvt_pk_bf16_f32 v104, v104, v105\n\t"
"v_mfma_f32_16x16x32_bf16 v[84:87], a[16:19], a[148:151], v[84:87]\n\t"
"v_cvt_pk_bf16_f32 v209, v210, v211\n\t"
"v_cvt_pk_bf16_f32 v120, v120, v121\n\t"
"v_mfma_f32_16x16x32_bf16 v[84:87], a[20:23], a[164:167], v[84:87]\n\t"
"buffer_load_dwordx4 a[116:119], v218, s[84:87], 0 offen offset:1024 sc1\n\t"
"ds_write2st64_b64 v246, v[112:113], v[116:117] offset0:39 offset1:52\n\t"
"v_mfma_f32_16x16x32_bf16 v[68:71], a[8:11], a[148:151], v[68:71]\n\t"
"v_cvt_pk_bf16_f32 v210, v212, v213\n\t"
"v_cvt_pk_bf16_f32 v211, v214, v215\n\t"
"v_mfma_f32_16x16x32_bf16 v[68:71], a[12:15], a[164:167], v[68:71]\n\t"
"v_cvt_pk_bf16_f32 v192, v192, v193\n\t"
"v_cvt_pk_bf16_f32 v140, v140, v141\n\t"
"v_mfma_f32_16x16x32_bf16 v[196:199], a[196:199], v[252:255], 0\n\t"
"buffer_load_dwordx4 a[108:111], v235, s[84:87], 0 offen offset:3072 sc1\n\t"
"v_mfma_f32_16x16x32_bf16 v[52:55], a[0:3], a[148:151], v[52:55]\n\t"
"v_cvt_pk_bf16_f32 v193, v194, v195\n\t"
"v_cvt_pk_bf16_f32 v133, v134, v135\n\t"
"v_mfma_f32_16x16x32_bf16 v[52:55], a[4:7], a[164:167], v[52:55]\n\t"
"s_nop 0\n\t"
"ds_read_b128 a[144:147], v216 offset:4096\n\t"
"ds_read_b128 a[160:163], v216 offset:12288\n\t"
"ds_read_b128 a[148:151], v216 offset:5120\n\t"
"ds_read_b128 a[164:167], v216 offset:13312\n\t"
"ds_read_b64_tr_b16 a[192:193], v248 offset:42624\n\t"
"ds_read_b64_tr_b16 a[194:195], v248 offset:43136\n\t"
"ds_write2st64_b64 v247, v[128:129], v[132:133] offset0:39 offset1:52\n\t"
"v_mfma_f32_16x16x32_bf16 v[200:203], a[200:203], v[252:255], 0\n\t"
"s_waitcnt lgkmcnt(10)\n\t"
"buffer_store_dwordx4 v[44:47], v234, s[16:19], s27 offen nt\n\t"
"v_mfma_f32_16x16x32_bf16 v[56:59], a[0:3], a[152:155], v[56:59]\n\t"
"v_cvt_pk_bf16_f32 v194, v196, v197\n\t"
"v_cvt_pk_bf16_f32 v105, v106, v107\n\t"
"v_mfma_f32_16x16x32_bf16 v[56:59], a[4:7], a[168:171], v[56:59]\n\t"
"ds_write_b128 v221, v[208:211] offset:24576\n\t"
"v_mfma_f32_16x16x32_bf16 v[72:75], a[8:11], a[152:155], v[72:75]\n\t"
"v_cvt_pk_bf16_f32 v109, v110, v111\n\t"
"v_cvt_pk_bf16_f32 v125, v126, v127\n\t"
"v_mfma_f32_16x16x32_bf16 v[72:75], a[12:15], a[168:171], v[72:75]\n\t"
"buffer_load_dwordx4 a[104:107], v235, s[84:87], 0 offen offset:2048 sc1\n\t"
"v_mfma_f32_16x16x32_bf16 v[204:207], a[204:207], v[252:255], 0\n\t"
"ds_write2st64_b64 v228, v[104:105], v[108:109] offset0:65 offset1:78\n\t"
"v_mfma_f32_16x16x32_bf16 v[88:91], a[16:19], a[152:155], v[88:91]\n\t"
"v_cvt_pk_bf16_f32 v195, v198, v199\n\t"
"v_cvt_pk_bf16_f32 v200, v200, v201\n\t"
"v_mfma_f32_16x16x32_bf16 v[88:91], a[20:23], a[168:171], v[88:91]\n\t"
"buffer_load_dwordx4 a[96:99], v235, s[84:87], 0 offen offset:0 sc1\n\t"
"v_mfma_f32_16x16x32_bf16 v[92:95], a[16:19], a[156:159], v[92:95]\n\t"
"v_cvt_pk_bf16_f32 v121, v122, v123\n\t"
"v_cvt_pk_bf16_f32 v137, v138, v139\n\t"
"v_mfma_f32_16x16x32_bf16 v[92:95], a[20:23], a[172:175], v[92:95]\n\t"
"ds_write2st64_b64 v246, v[120:121], v[124:125] offset0:65 offset1:78\n\t"
"v_mfma_f32_16x16x32_bf16 v[76:79], a[8:11], a[156:159], v[76:79]\n\t"
"v_cvt_pk_bf16_f32 v201, v202, v203\n\t"
"v_cvt_pk_bf16_f32 v141, v142, v143\n\t"
"v_mfma_f32_16x16x32_bf16 v[76:79], a[12:15], a[172:175], v[76:79]\n\t"
"ds_write_b128 v221, v[192:195] offset:20480\n\t"
"v_mfma_f32_16x16x32_bf16 v[60:63], a[0:3], a[156:159], v[60:63]\n\t"
"buffer_load_dwordx4 a[100:103], v235, s[84:87], 0 offen offset:1024 sc1\n\t"
"v_mfma_f32_16x16x32_bf16 v[60:63], a[4:7], a[172:175], v[60:63]\n\t"
"s_waitcnt vmcnt(21) lgkmcnt(3)\n\t"
"ds_write2st64_b64 v247, v[136:137], v[140:141] offset0:65 offset1:78\n\t"
"v_mfma_f32_16x16x32_bf16 v[144:147], a[24:27], a[144:147], v[144:147]\n\t"
"v_cvt_pk_bf16_f32 v202, v204, v205\n\t"
"v_cvt_pk_bf16_f32 v203, v206, v207\n\t"
"v_mfma_f32_16x16x32_bf16 v[144:147], a[28:31], a[160:163], v[144:147]\n\t"
"ds_write_b128 v221, v[200:203] offset:28672\n\t"
"v_mfma_f32_16x16x32_bf16 v[160:163], a[32:35], a[144:147], v[160:163]\n\t"
"buffer_load_dwordx4 a[136:139], v218, s[84:87], s51 offen offset:0 sc1\n\t"
"v_mfma_f32_16x16x32_bf16 v[160:163], a[36:39], a[160:163], v[160:163]\n\t"
"s_nop 0\n\t"
"ds_read_b64_tr_b16 a[196:197], v249 offset:42624\n\t"
"ds_read_b64_tr_b16 a[198:199], v249 offset:43136\n\t"
"v_mfma_f32_16x16x32_bf16 v[176:179], a[40:43], a[144:147], v[176:179]\n\t"
"buffer_load_dwordx4 a[140:143], v218, s[84:87], s51 offen offset:1024 sc1\n\t"
"v_mfma_f32_16x16x32_bf16 v[176:179], a[44:47], a[160:163], v[176:179]\n\t"
"s_nop 0\n\t"
"ds_read_b64_tr_b16 a[200:201], v250 offset:42624\n\t"
"ds_read_b64_tr_b16 a[202:203], v250 offset:43136\n\t"
"v_mfma_f32_16x16x32_bf16 v[180:183], a[40:43], a[148:151], v[180:183]\n\t"
"s_nop 0\n\t"
"ds_read_b128 a[168:171], v216 offset:14336\n\t"
"ds_read_b128 a[156:159], v216 offset:7168\n\t"
"v_mfma_f32_16x16x32_bf16 v[180:183], a[44:47], a[164:167], v[180:183]\n\t"
"s_nop 0\n\t"
"ds_read_b64_tr_b16 a[204:205], v251 offset:42624\n\t"
"ds_read_b64_tr_b16 a[206:207], v251 offset:43136\n\t"
"v_mfma_f32_16x16x32_bf16 v[164:167], a[32:35], a[148:151], v[164:167]\n\t"
"s_nop 0\n\t"
"ds_read_b128 a[152:155], v216 offset:6144\n\t"
"ds_read_b128 a[172:175], v216 offset:15360\n\t"
"v_mfma_f32_16x16x32_bf16 v[164:167], a[36:39], a[164:167], v[164:167]\n\t"
"buffer_load_dwordx4 a[132:135], v235, s[84:87], s51 offen offset:3072 sc1\n\t"
"v_mfma_f32_16x16x32_bf16 v[148:151], a[24:27], a[148:151], v[148:151]\n\t"
"v_mfma_f32_16x16x32_bf16 v[148:151], a[28:31], a[164:167], v[148:151]\n\t"
"buffer_load_dwordx4 a[128:131], v235, s[84:87], s51 offen offset:2048 sc1\n\t"
"s_waitcnt lgkmcnt(0)\n\t"
"s_barrier\n\t"
"v_mfma_f32_16x16x32_bf16 v[152:155], a[24:27], a[152:155], v[152:155]\n\t"
"s_nop 0\n\t"
"ds_read_b128 a[144:147], v216 offset:16384\n\t"
"ds_read_b128 a[160:163], v216 offset:24576\n\t"
"v_mfma_f32_16x16x32_bf16 v[152:155], a[28:31], a[168:171], v[152:155]\n\t"
"buffer_load_dwordx4 a[120:123], v235, s[84:87], s51 offen offset:0 sc1\n\t"
"v_mfma_f32_16x16x32_bf16 v[168:171], a[32:35], a[152:155], v[168:171]\n\t"
"ds_read_b128 a[148:151], v216 offset:17408\n\t"
"v_mfma_f32_16x16x32_bf16 v[168:171], a[36:39], a[168:171], v[168:171]\n\t"
"ds_read_b128 a[164:167], v216 offset:25600\n\t"
"v_mfma_f32_16x16x32_bf16 v[184:187], a[40:43], a[152:155], v[184:187]\n\t"
"ds_read_b128 v[104:107], v229 offset:19968\n\t"
"v_mfma_f32_16x16x32_bf16 v[184:187], a[44:47], a[168:171], v[184:187]\n\t"
"buffer_load_dwordx4 a[124:127], v235, s[84:87], s51 offen offset:1024 sc1\n\t"
"v_mfma_f32_16x16x32_bf16 v[208:211], a[176:179], a[224:227], 0\n\t"
"ds_read_b64_tr_b16 a[176:177], v248 offset:20736\n\t"
"v_mfma_f32_16x16x32_bf16 v[188:191], a[40:43], a[156:159], v[188:191]\n\t"
"ds_read_b64_tr_b16 a[178:179], v248 offset:21248\n\t"
"v_mfma_f32_16x16x32_bf16 v[188:191], a[44:47], a[172:175], v[188:191]\n\t"
"ds_read_b128 a[152:155], v216 offset:18432\n\t"
"v_mfma_f32_16x16x32_bf16 v[212:215], a[180:183], a[224:227], 0\n\t"
"ds_read_b64_tr_b16 a[180:181], v249 offset:20736\n\t"
"v_mfma_f32_16x16x32_bf16 v[172:175], a[32:35], a[156:159], v[172:175]\n\t"
"ds_read_b64_tr_b16 a[182:183], v249 offset:21248\n\t"
"v_mfma_f32_16x16x32_bf16 v[172:175], a[36:39], a[172:175], v[172:175]\n\t"
"ds_read_b128 v[120:123], v230 offset:19968\n\t"
"v_cvt_pk_bf16_f32 v208, v208, v209\n\t"
"v_mfma_f32_16x16x32_bf16 v[156:159], a[24:27], a[156:159], v[156:159]\n\t"
"ds_read_b128 a[168:171], v216 offset:26624\n\t"
"v_cvt_pk_bf16_f32 v209, v210, v211\n\t"
"v_mfma_f32_16x16x32_bf16 v[156:159], a[28:31], a[172:175], v[156:159]\n\t"
"ds_read_b128 a[172:175], v216 offset:27648\n\t"
"v_cvt_pk_bf16_f32 v210, v212, v213\n\t"
"v_mfma_f32_16x16x32_bf16 v[192:195], a[184:187], a[224:227], 0\n\t"
"s_waitcnt lgkmcnt(8)\n\t"
"ds_read_b64_tr_b16 a[184:185], v250 offset:20736\n\t"
"v_mfma_f32_16x16x32_bf16 v[0:3], a[0:3], a[144:147], 0\n\t"
"ds_read_b64_tr_b16 a[186:187], v250 offset:21248\n\t"
"v_cvt_pk_bf16_f32 v211, v214, v215\n\t"
"v_mfma_f32_16x16x32_bf16 v[0:3], a[4:7], a[160:163], v[0:3]\n\t"
"ds_read_b128 a[156:159], v216 offset:19456\n\t"
"v_mfma_f32_16x16x32_bf16 v[196:199], a[188:191], a[224:227], 0\n\t"
"ds_read_b64_tr_b16 a[188:189], v251 offset:20736\n\t"
"v_mfma_f32_16x16x32_bf16 v[16:19], a[8:11], a[144:147], 0\n\t"
"ds_read_b64_tr_b16 a[190:191], v251 offset:21248\n\t"
"v_cvt_pk_bf16_f32 v192, v192, v193\n\t"
"v_mfma_f32_16x16x32_bf16 v[16:19], a[12:15], a[160:163], v[16:19]\n\t"
"ds_read_b128 v[136:139], v231 offset:19968\n\t"
"v_cvt_pk_bf16_f32 v193, v194, v195\n\t"
"v_mfma_f32_16x16x32_bf16 v[32:35], a[16:19], a[144:147], 0\n\t"
"ds_read_b128 a[144:147], v216 offset:20480\n\t"
"v_mfma_f32_16x16x32_bf16 v[32:35], a[20:23], a[160:163], v[32:35]\n\t"
"buffer_store_dwordx4 v[104:107], v232, s[16:19], s20 offen nt\n\t"
"v_mfma_f32_16x16x32_bf16 v[200:203], a[192:195], a[224:227], 0\n\t"
"ds_write_b128 v221, v[208:211] offset:0\n\t"
"v_mfma_f32_16x16x32_bf16 v[36:39], a[16:19], a[148:151], 0\n\t"
"v_mfma_f32_16x16x32_bf16 v[36:39], a[20:23], a[164:167], v[36:39]\n\t"
"s_waitcnt lgkmcnt(7)\n\t"
"buffer_store_dwordx4 v[120:123], v233, s[16:19], s20 offen nt\n\t"
"v_mfma_f32_16x16x32_bf16 v[20:23], a[8:11], a[148:151], 0\n\t"
"v_cvt_pk_bf16_f32 v194, v196, v197\n\t"
"v_cvt_pk_bf16_f32 v195, v198, v199\n\t"
"v_mfma_f32_16x16x32_bf16 v[20:23], a[12:15], a[164:167], v[20:23]\n\t"
"s_nop 0\n\t"
"ds_read_b128 a[160:163], v216 offset:28672\n\t"
"ds_read_b64_tr_b16 a[192:193], v248 offset:21888\n\t"
"v_mfma_f32_16x16x32_bf16 v[204:207], a[196:199], a[224:227], 0\n\t"
"s_nop 0\n\t"
"ds_read_b64_tr_b16 a[194:195], v248 offset:22400\n\t"
"ds_read_b128 a[232:235], v230 offset:33280\n\t"
"v_mfma_f32_16x16x32_bf16 v[4:7], a[0:3], a[148:151], 0\n\t"
"v_cvt_pk_bf16_f32 v200, v200, v201\n\t"
"v_cvt_pk_bf16_f32 v201, v202, v203\n\t"
"v_mfma_f32_16x16x32_bf16 v[4:7], a[4:7], a[164:167], v[4:7]\n\t"
"ds_write_b128 v221, v[192:195] offset:8192\n\t"
"v_mfma_f32_16x16x32_bf16 v[208:211], a[200:203], a[224:227], 0\n\t"
"v_mfma_f32_16x16x32_bf16 v[8:11], a[0:3], a[152:155], 0\n\t"
"v_cvt_pk_bf16_f32 v202, v204, v205\n\t"
"v_cvt_pk_bf16_f32 v203, v206, v207\n\t"
"v_mfma_f32_16x16x32_bf16 v[8:11], a[4:7], a[168:171], v[8:11]\n\t"
"s_waitcnt lgkmcnt(5)\n\t"
"buffer_store_dwordx4 v[136:139], v234, s[16:19], s20 offen nt\n\t"
"v_mfma_f32_16x16x32_bf16 v[24:27], a[8:11], a[152:155], 0\n\t"
"s_nop 0\n\t"
"ds_read_b64_tr_b16 a[196:197], v249 offset:21888\n\t"
"ds_read_b128 v[108:111], v229 offset:33280\n\t"
"v_mfma_f32_16x16x32_bf16 v[24:27], a[12:15], a[168:171], v[24:27]\n\t"
"s_nop 0\n\t"
"ds_read_b128 a[164:167], v216 offset:29696\n\t"
"ds_read_b128 a[236:239], v231 offset:33280\n\t"
"v_mfma_f32_16x16x32_bf16 v[212:215], a[204:207], a[224:227], 0\n\t"
"s_nop 0\n\t"
"ds_read_b128 a[148:151], v216 offset:21504\n\t"
"ds_read_b64_tr_b16 a[198:199], v249 offset:22400\n\t"
"v_mfma_f32_16x16x32_bf16 v[40:43], a[16:19], a[152:155], 0\n\t"
"ds_read_b64_tr_b16 a[200:201], v250 offset:21888\n\t"
"v_cvt_pk_bf16_f32 v208, v208, v209\n\t"
"v_mfma_f32_16x16x32_bf16 v[40:43], a[20:23], a[168:171], v[40:43]\n\t"
"ds_read_b128 a[168:171], v216 offset:30720\n\t"
"v_cvt_pk_bf16_f32 v209, v210, v211\n\t"
"v_mfma_f32_16x16x32_bf16 v[44:47], a[16:19], a[156:159], 0\n\t"
"buffer_load_dwordx4 a[16:19], v218, s[84:87], s57 offen offset:0 sc1\n\t"
"v_mfma_f32_16x16x32_bf16 v[44:47], a[20:23], a[172:175], v[44:47]\n\t"
"ds_read_b128 a[152:155], v216 offset:22528\n\t"
"v_cvt_pk_bf16_f32 v210, v212, v213\n\t"
"v_mfma_f32_16x16x32_bf16 v[28:31], a[8:11], a[156:159], 0\n\t"
"buffer_load_dwordx4 a[20:23], v218, s[84:87], s57 offen offset:1024 sc1\n\t"
"v_mfma_f32_16x16x32_bf16 v[28:31], a[12:15], a[172:175], v[28:31]\n\t"
"ds_write_b128 v221, v[200:203] offset:4096\n\t"
"v_mfma_f32_16x16x32_bf16 v[12:15], a[0:3], a[156:159], 0\n\t"
"buffer_load_dwordx4 a[12:15], v235, s[84:87], s57 offen offset:3072 sc1\n\t"
"v_mfma_f32_16x16x32_bf16 v[12:15], a[4:7], a[172:175], v[12:15]\n\t"
"s_waitcnt lgkmcnt(8)\n\t"
"v_cvt_pk_bf16_f32 v211, v214, v215\n\t"
"v_mfma_f32_16x16x32_bf16 v[96:99], a[24:27], a[144:147], 0\n\t"
"buffer_load_dwordx4 a[8:11], v235, s[84:87], s57 offen offset:2048 sc1\n\t"
"v_mfma_f32_16x16x32_bf16 v[96:99], a[28:31], a[160:163], v[96:99]\n\t"
"ds_write_b128 v221, v[208:211] offset:12288\n\t"
"v_mfma_f32_16x16x32_bf16 v[112:115], a[32:35], a[144:147], 0\n\t"
"buffer_load_dwordx4 a[0:3], v235, s[84:87], s57 offen offset:0 sc1\n\t"
"v_mfma_f32_16x16x32_bf16 v[112:115], a[36:39], a[160:163], v[112:115]\n\t"
"v_mfma_f32_16x16x32_bf16 v[128:131], a[40:43], a[144:147], 0\n\t"
"buffer_load_dwordx4 a[4:7], v235, s[84:87], s57 offen offset:1024 sc1\n\t"
"v_mfma_f32_16x16x32_bf16 v[128:131], a[44:47], a[160:163], v[128:131]\n\t"
"s_waitcnt lgkmcnt(2)\n\t"
"ds_read_b128 a[172:175], v216 offset:31744\n\t"
"ds_read_b64_tr_b16 a[202:203], v250 offset:22400\n\t"
"v_mfma_f32_16x16x32_bf16 v[132:135], a[40:43], a[148:151], 0\n\t"
"ds_read_b64_tr_b16 a[204:205], v251 offset:21888\n\t"
"v_mfma_f32_16x16x32_bf16 v[132:135], a[44:47], a[164:167], v[132:135]\n\t"
"ds_read_b64_tr_b16 a[206:207], v251 offset:22400\n\t"
"v_mfma_f32_16x16x32_bf16 v[116:119], a[32:35], a[148:151], 0\n\t"
"ds_read_b128 a[156:159], v216 offset:23552\n\t"
"v_mfma_f32_16x16x32_bf16 v[116:119], a[36:39], a[164:167], v[116:119]\n\t"
"buffer_store_dwordx4 v[108:111], v232, s[16:19], s25 offen nt\n\t"
"v_mfma_f32_16x16x32_bf16 v[100:103], a[24:27], a[148:151], 0\n\t"
"v_mfma_f32_16x16x32_bf16 v[100:103], a[28:31], a[164:167], v[100:103]\n\t"
"s_waitcnt lgkmcnt(0)\n\t"
"s_barrier\n\t"
"v_mfma_f32_16x16x32_bf16 v[104:107], a[24:27], a[152:155], 0\n\t"
"s_add_u32 m0, s26, 83200\n\t"
"ds_read_b128 a[144:147], v216 offset:0\n\t"
"ds_read_b128 a[160:163], v216 offset:8192\n\t"
"v_mfma_f32_16x16x32_bf16 v[104:107], a[28:31], a[168:171], v[104:107]\n\t"
"buffer_load_dwordx4 v236, s[12:15], s22 offen offset:0 nt lds\n\t"
"v_mfma_f32_16x16x32_bf16 v[120:123], a[32:35], a[152:155], 0\n\t"
"ds_read_b128 a[148:151], v216 offset:1024\n\t"
"v_mfma_f32_16x16x32_bf16 v[120:123], a[36:39], a[168:171], v[120:123]\n\t"
"ds_read_b128 a[164:167], v216 offset:9216\n\t"
"v_mfma_f32_16x16x32_bf16 v[136:139], a[40:43], a[152:155], 0\n\t"
"ds_read_b128 a[152:155], v216 offset:2048\n\t"
"v_mfma_f32_16x16x32_bf16 v[136:139], a[44:47], a[168:171], v[136:139]\n\t"
"ds_read_b128 a[228:231], v223 offset:20736\n\t"
"v_mfma_f32_16x16x32_bf16 v[192:195], a[176:179], v[252:255], 0\n\t"
"ds_read_b64_tr_b16 a[176:177], v248 offset:0\n\t"
"v_mfma_f32_16x16x32_bf16 v[140:143], a[40:43], a[156:159], 0\n\t"
"ds_read_b128 a[168:171], v216 offset:10240\n\t"
"v_mfma_f32_16x16x32_bf16 v[140:143], a[44:47], a[172:175], v[140:143]\n\t"
"buffer_load_dwordx4 v237, s[12:15], s22 offen offset:1024 nt lds\n\t"
"v_mfma_f32_16x16x32_bf16 v[196:199], a[180:183], v[252:255], 0\n\t"
"ds_read_b64_tr_b16 a[178:179], v248 offset:512\n\t"
"v_mfma_f32_16x16x32_bf16 v[124:127], a[32:35], a[156:159], 0\n\t"
"ds_read_b64_tr_b16 a[180:181], v249 offset:0\n\t"
"v_cvt_pk_bf16_f32 v192, v192, v193\n\t"
"v_mfma_f32_16x16x32_bf16 v[124:127], a[36:39], a[172:175], v[124:127]\n\t"
"ds_read_b64_tr_b16 a[182:183], v249 offset:512\n\t"
"v_mfma_f32_16x16x32_bf16 v[108:111], a[24:27], a[156:159], 0\n\t"
"ds_read_b128 a[156:159], v216 offset:3072\n\t"
"v_cvt_pk_bf16_f32 v193, v194, v195\n\t"
"v_mfma_f32_16x16x32_bf16 v[108:111], a[28:31], a[172:175], v[108:111]\n\t"
"ds_read_b128 a[172:175], v216 offset:11264\n\t"
"v_cvt_pk_bf16_f32 v194, v196, v197\n\t"
"v_mfma_f32_16x16x32_bf16 v[200:203], a[184:187], v[252:255], 0\n\t"
"s_waitcnt vmcnt(32) lgkmcnt(8)\n\t"
"ds_read_b64_tr_b16 a[184:185], v250 offset:0\n\t"
"v_cvt_pk_bf16_f32 v195, v198, v199\n\t"
"v_mfma_f32_16x16x32_bf16 v[48:51], a[48:51], a[144:147], v[48:51]\n\t"
"ds_read_b64_tr_b16 a[186:187], v250 offset:512\n\t"
"v_mfma_f32_16x16x32_bf16 v[48:51], a[52:55], a[160:163], v[48:51]\n\t"
"buffer_load_dwordx4 v238, s[12:15], s22 offen offset:2048 nt lds\n\t"
"v_mfma_f32_16x16x32_bf16 v[204:207], a[188:191], v[252:255], 0\n\t"
"ds_write_b128 v221, v[192:195] offset:16384\n\t"
"v_mfma_f32_16x16x32_bf16 v[64:67], a[56:59], a[144:147], v[64:67]\n\t"
"v_cvt_pk_bf16_f32 v200, v200, v201\n\t"
"v_cvt_pk_bf16_f32 v201, v202, v203\n\t"
"v_mfma_f32_16x16x32_bf16 v[64:67], a[60:63], a[160:163], v[64:67]\n\t"
"buffer_load_dwordx4 v239, s[12:15], s22 offen offset:3072 nt lds\n\t"
"v_mfma_f32_16x16x32_bf16 v[80:83], a[64:67], a[144:147], v[80:83]\n\t"
"v_mfma_f32_16x16x32_bf16 v[80:83], a[68:71], a[160:163], v[80:83]\n\t"
"s_waitcnt lgkmcnt(5)\n\t"
"ds_write_b128 v222, a[228:231] offset:48128\n\t"
"v_mfma_f32_16x16x32_bf16 v[208:211], a[192:195], v[252:255], 0\n\t"
"v_cvt_pk_bf16_f32 v202, v204, v205\n\t"
"v_mfma_f32_16x16x32_bf16 v[84:87], a[64:67], a[148:151], v[84:87]\n\t"
"v_cvt_pk_bf16_f32 v203, v206, v207\n\t"
"v_mfma_f32_16x16x32_bf16 v[84:87], a[68:71], a[164:167], v[84:87]\n\t"
"ds_write_b128 v221, v[200:203] offset:24576\n\t"
"v_mfma_f32_16x16x32_bf16 v[68:71], a[56:59], a[148:151], v[68:71]\n\t"
"v_mfma_f32_16x16x32_bf16 v[68:71], a[60:63], a[164:167], v[68:71]\n\t"
"s_mov_b64 exec, s[36:37]\n\t"
"buffer_load_dwordx4 v240, s[12:15], s22 offen offset:3584 nt lds\n\t"
"s_mov_b64 exec, -1\n\t"
"v_mfma_f32_16x16x32_bf16 v[212:215], a[196:199], v[252:255], 0\n\t"
"v_cvt_pk_bf16_f32 v208, v208, v209\n\t"
"v_mfma_f32_16x16x32_bf16 v[52:55], a[48:51], a[148:151], v[52:55]\n\t"
"s_nop 0\n\t"
"ds_read_b128 a[144:147], v216 offset:4096\n\t"
"ds_read_b128 a[160:163], v216 offset:12288\n\t"
"v_mfma_f32_16x16x32_bf16 v[52:55], a[52:55], a[164:167], v[52:55]\n\t"
"ds_read_b64_tr_b16 a[188:189], v251 offset:0\n\t"
"v_cvt_pk_bf16_f32 v209, v210, v211\n\t"
"v_mfma_f32_16x16x32_bf16 v[192:195], a[200:203], v[252:255], 0\n\t"
"ds_read_b64_tr_b16 a[190:191], v251 offset:512\n\t"
"v_mfma_f32_16x16x32_bf16 v[56:59], a[48:51], a[152:155], v[56:59]\n\t"
"ds_read_b64_tr_b16 a[192:193], v248 offset:1152\n\t"
"v_cvt_pk_bf16_f32 v210, v212, v213\n\t"
"v_mfma_f32_16x16x32_bf16 v[56:59], a[52:55], a[168:171], v[56:59]\n\t"
"s_nop 0\n\t"
"ds_read_b128 a[148:151], v216 offset:5120\n\t"
"ds_read_b64_tr_b16 a[194:195], v248 offset:1664\n\t"
"v_mfma_f32_16x16x32_bf16 v[72:75], a[56:59], a[152:155], v[72:75]\n\t"
"ds_read_b64_tr_b16 a[196:197], v249 offset:1152\n\t"
"v_cvt_pk_bf16_f32 v211, v214, v215\n\t"
"v_mfma_f32_16x16x32_bf16 v[72:75], a[60:63], a[168:171], v[72:75]\n\t"
"ds_read_b128 a[164:167], v216 offset:13312\n\t"
"v_cvt_pk_bf16_f32 v192, v192, v193\n\t"
"v_mfma_f32_16x16x32_bf16 v[196:199], a[204:207], v[252:255], 0\n\t"
"ds_read_b64_tr_b16 a[198:199], v249 offset:1664\n\t"
"v_mfma_f32_16x16x32_bf16 v[88:91], a[64:67], a[152:155], v[88:91]\n\t"
"ds_read_b64_tr_b16 a[200:201], v250 offset:1152\n\t"
"v_cvt_pk_bf16_f32 v193, v194, v195\n\t"
"v_mfma_f32_16x16x32_bf16 v[88:91], a[68:71], a[168:171], v[88:91]\n\t"
"s_waitcnt lgkmcnt(11)\n\t"
"ds_read_b64_tr_b16 a[202:203], v250 offset:1664\n\t"
"v_mfma_f32_16x16x32_bf16 v[92:95], a[64:67], a[156:159], v[92:95]\n\t"
"v_mfma_f32_16x16x32_bf16 v[92:95], a[68:71], a[172:175], v[92:95]\n\t"
"ds_write_b128 v221, v[208:211] offset:20480\n\t"
"v_mfma_f32_16x16x32_bf16 v[76:79], a[56:59], a[156:159], v[76:79]\n\t"
"v_cvt_pk_bf16_f32 v194, v196, v197\n\t"
"v_mfma_f32_16x16x32_bf16 v[76:79], a[60:63], a[172:175], v[76:79]\n\t"
"v_cvt_pk_bf16_f32 v195, v198, v199\n\t"
"v_mfma_f32_16x16x32_bf16 v[60:63], a[48:51], a[156:159], v[60:63]\n\t"
"v_mfma_f32_16x16x32_bf16 v[60:63], a[52:55], a[172:175], v[60:63]\n\t"
"s_waitcnt vmcnt(28) lgkmcnt(5)\n\t"
"ds_write_b128 v221, v[192:195] offset:28672\n\t"
"v_mfma_f32_16x16x32_bf16 v[144:147], a[72:75], a[144:147], v[144:147]\n\t"
"v_mfma_f32_16x16x32_bf16 v[144:147], a[76:79], a[160:163], v[144:147]\n\t"
"v_mfma_f32_16x16x32_bf16 v[160:163], a[80:83], a[144:147], v[160:163]\n\t"
"v_mfma_f32_16x16x32_bf16 v[160:163], a[84:87], a[160:163], v[160:163]\n\t"
"ds_read_b64_tr_b16 a[204:205], v251 offset:1152\n\t"
"v_mfma_f32_16x16x32_bf16 v[176:179], a[88:91], a[144:147], v[176:179]\n\t"
"ds_read_b128 a[168:171], v216 offset:14336\n\t"
"v_mfma_f32_16x16x32_bf16 v[176:179], a[92:95], a[160:163], v[176:179]\n\t"
"s_waitcnt lgkmcnt(3)\n\t"
"ds_read_b128 a[156:159], v216 offset:7168\n\t"
"v_mfma_f32_16x16x32_bf16 v[180:183], a[88:91], a[148:151], v[180:183]\n\t"
"ds_read_b64_tr_b16 a[206:207], v251 offset:1664\n\t"
"v_mfma_f32_16x16x32_bf16 v[180:183], a[92:95], a[164:167], v[180:183]\n\t"
"ds_read_b128 a[172:175], v216 offset:15360\n\t"
"v_mfma_f32_16x16x32_bf16 v[164:167], a[80:83], a[148:151], v[164:167]\n\t"
"ds_read_b128 a[152:155], v216 offset:6144\n\t"
"v_mfma_f32_16x16x32_bf16 v[164:167], a[84:87], a[164:167], v[164:167]\n\t"
"buffer_load_dwordx4 a[40:43], v218, s[84:87], s63 offen offset:0 sc1\n\t"
"v_mfma_f32_16x16x32_bf16 v[148:151], a[72:75], a[148:151], v[148:151]\n\t"
"v_mfma_f32_16x16x32_bf16 v[148:151], a[76:79], a[164:167], v[148:151]\n\t"
"s_waitcnt lgkmcnt(0)\n\t"
"s_barrier\n\t"
"v_mfma_f32_16x16x32_bf16 v[152:155], a[72:75], a[152:155], v[152:155]\n\t"
"ds_read_b128 a[160:163], v216 offset:24576\n\t"
"v_add_u32 v248, 143104, v226\n\t"
"v_mfma_f32_16x16x32_bf16 v[152:155], a[76:79], a[168:171], v[152:155]\n\t"
"ds_read_b128 a[144:147], v216 offset:16384\n\t"
"v_xor_b32 v249, 32, v248\n\t"
"v_mfma_f32_16x16x32_bf16 v[168:171], a[80:83], a[152:155], v[168:171]\n\t"
"ds_read_b128 a[148:151], v216 offset:17408\n\t"
"v_swap_b32 v236, v241\n\t"
"v_mfma_f32_16x16x32_bf16 v[168:171], a[84:87], a[168:171], v[168:171]\n\t"
"ds_read_b128 a[164:167], v216 offset:25600\n\t"
"v_xor_b32 v250, 64, v248\n\t"
"v_mfma_f32_16x16x32_bf16 v[184:187], a[88:91], a[152:155], v[184:187]\n\t"
"ds_read_b128 a[152:155], v216 offset:18432\n\t"
"v_swap_b32 v237, v242\n\t"
"v_mfma_f32_16x16x32_bf16 v[184:187], a[92:95], a[168:171], v[184:187]\n\t"
"v_swap_b32 v238, v243\n\t"
"v_xor_b32 v251, 96, v248\n\t"
"v_mfma_f32_16x16x32_bf16 v[200:203], a[176:179], a[224:227], 0\n\t"
"ds_read_b64_tr_b16 a[176:177], v248 offset:0\n\t"
"v_swap_b32 v239, v244\n\t"
"v_mfma_f32_16x16x32_bf16 v[188:191], a[88:91], a[156:159], v[188:191]\n\t"
"v_swap_b32 v240, v245\n\t"
"v_add_u32 v224, v224, v220\n\t"
"v_mfma_f32_16x16x32_bf16 v[188:191], a[92:95], a[172:175], v[188:191]\n\t"
"ds_read_b64_tr_b16 a[178:179], v248 offset:512\n\t"
"v_mfma_f32_16x16x32_bf16 v[204:207], a[180:183], a[224:227], 0\n\t"
"ds_read_b64_tr_b16 a[180:181], v249 offset:0\n\t"
"v_mfma_f32_16x16x32_bf16 v[172:175], a[80:83], a[156:159], v[172:175]\n\t"
"ds_read_b128 a[168:171], v216 offset:26624\n\t"
"v_cvt_pk_bf16_f32 v200, v200, v201\n\t"
"v_mfma_f32_16x16x32_bf16 v[172:175], a[84:87], a[172:175], v[172:175]\n\t"
"ds_read_b64_tr_b16 a[182:183], v249 offset:512\n\t"
"v_cvt_pk_bf16_f32 v201, v202, v203\n\t"
"v_mfma_f32_16x16x32_bf16 v[156:159], a[72:75], a[156:159], v[156:159]\n\t"
"ds_read_b128 a[156:159], v216 offset:19456\n\t"
"v_add_u32 v225, v225, v220\n\t"
"v_mfma_f32_16x16x32_bf16 v[156:159], a[76:79], a[172:175], v[156:159]\n\t"
"ds_read_b128 a[172:175], v216 offset:27648\n\t"
"v_mfma_f32_16x16x32_bf16 v[208:211], a[184:187], a[224:227], 0\n\t"
"s_waitcnt lgkmcnt(7)\n\t"
"ds_read_b64_tr_b16 a[184:185], v250 offset:2304\n\t"
"v_cvt_pk_bf16_f32 v202, v204, v205\n\t"
"v_mfma_f32_16x16x32_bf16 v[0:3], a[48:51], a[144:147], v[0:3]\n\t"
"ds_read_b64_tr_b16 a[186:187], v250 offset:2816\n\t"
"v_cvt_pk_bf16_f32 v203, v206, v207\n\t"
"v_mfma_f32_16x16x32_bf16 v[0:3], a[52:55], a[160:163], v[0:3]\n\t"
"v_sub_u32 v220, 0, v220\n\t"
"v_mfma_f32_16x16x32_bf16 v[212:215], a[188:191], a[224:227], 0\n\t"
"s_nop 0\n\t"
"ds_read_b64_tr_b16 a[188:189], v251 offset:2304\n\t"
"ds_read_b64_tr_b16 a[190:191], v251 offset:2816\n\t"
"v_mfma_f32_16x16x32_bf16 v[16:19], a[56:59], a[144:147], v[16:19]\n\t"
"v_cvt_pk_bf16_f32 v208, v208, v209\n\t"
"v_mfma_f32_16x16x32_bf16 v[16:19], a[60:63], a[160:163], v[16:19]\n\t"
"ds_write_b128 v221, v[200:203] offset:0\n\t"
"v_mfma_f32_16x16x32_bf16 v[32:35], a[64:67], a[144:147], v[32:35]\n\t"
"v_cvt_pk_bf16_f32 v209, v210, v211\n\t"
"v_mfma_f32_16x16x32_bf16 v[32:35], a[68:71], a[160:163], v[32:35]\n\t"
"buffer_load_dwordx4 a[44:47], v218, s[84:87], s63 offen offset:1024 sc1\n\t"
"v_mfma_f32_16x16x32_bf16 v[192:195], a[192:195], a[224:227], 0\n\t"
"v_cvt_pk_bf16_f32 v210, v212, v213\n\t"
"v_mfma_f32_16x16x32_bf16 v[36:39], a[64:67], a[148:151], v[36:39]\n\t"
"s_nop 0\n\t"
"ds_read_b128 a[160:163], v216 offset:28672\n\t"
"ds_read_b64_tr_b16 a[192:193], v250 offset:0\n\t"
"v_mfma_f32_16x16x32_bf16 v[36:39], a[68:71], a[164:167], v[36:39]\n\t"
"ds_read_b128 a[144:147], v216 offset:20480\n\t"
"v_cvt_pk_bf16_f32 v211, v214, v215\n\t"
"v_mfma_f32_16x16x32_bf16 v[20:23], a[56:59], a[148:151], v[20:23]\n\t"
"ds_read_b64_tr_b16 a[194:195], v250 offset:512\n\t"
"v_mfma_f32_16x16x32_bf16 v[20:23], a[60:63], a[164:167], v[20:23]\n\t"
"buffer_load_dwordx4 a[36:39], v235, s[84:87], s63 offen offset:3072 sc1\n\t"
"v_mfma_f32_16x16x32_bf16 v[196:199], a[196:199], a[224:227], 0\n\t"
"ds_write_b128 v221, v[208:211] offset:8192\n\t"
"v_mfma_f32_16x16x32_bf16 v[4:7], a[48:51], a[148:151], v[4:7]\n\t"
"v_cvt_pk_bf16_f32 v192, v192, v193\n\t"
"v_mfma_f32_16x16x32_bf16 v[4:7], a[52:55], a[164:167], v[4:7]\n\t"
"buffer_load_dwordx4 a[32:35], v235, s[84:87], s63 offen offset:2048 sc1\n\t"
"v_mfma_f32_16x16x32_bf16 v[200:203], a[200:203], a[224:227], 0\n\t"
"s_waitcnt lgkmcnt(5)\n\t"
"v_cvt_pk_bf16_f32 v193, v194, v195\n\t"
"v_mfma_f32_16x16x32_bf16 v[8:11], a[48:51], a[152:155], v[8:11]\n\t"
"s_nop 0\n\t"
"ds_read_b64_tr_b16 a[196:197], v251 offset:0\n\t"
"ds_read_b64_tr_b16 a[198:199], v251 offset:512\n\t"
"v_mfma_f32_16x16x32_bf16 v[8:11], a[52:55], a[168:171], v[8:11]\n\t"
"buffer_load_dwordx4 a[24:27], v235, s[84:87], s63 offen offset:0 sc1\n\t"
"v_mfma_f32_16x16x32_bf16 v[24:27], a[56:59], a[152:155], v[24:27]\n\t"
"ds_read_b64_tr_b16 a[200:201], v248 offset:2304\n\t"
"v_cvt_pk_bf16_f32 v194, v196, v197\n\t"
"v_mfma_f32_16x16x32_bf16 v[24:27], a[60:63], a[168:171], v[24:27]\n\t"
"ds_read_b128 a[148:151], v216 offset:21504\n\t"
"v_cvt_pk_bf16_f32 v200, v200, v201\n\t"
"v_mfma_f32_16x16x32_bf16 v[204:207], a[204:207], a[224:227], 0\n\t"
"ds_read_b128 a[164:167], v216 offset:29696\n\t"
"v_cvt_pk_bf16_f32 v195, v198, v199\n\t"
"v_mfma_f32_16x16x32_bf16 v[40:43], a[64:67], a[152:155], v[40:43]\n\t"
"ds_read_b64_tr_b16 a[202:203], v248 offset:2816\n\t"
"v_cvt_pk_bf16_f32 v201, v202, v203\n\t"
"v_mfma_f32_16x16x32_bf16 v[40:43], a[68:71], a[168:171], v[40:43]\n\t"
"ds_read_b128 a[152:155], v216 offset:22528\n\t"
"v_mfma_f32_16x16x32_bf16 v[44:47], a[64:67], a[156:159], v[44:47]\n\t"
"ds_read_b64_tr_b16 a[204:205], v249 offset:2304\n\t"
"v_mfma_f32_16x16x32_bf16 v[44:47], a[68:71], a[172:175], v[44:47]\n\t"
"ds_read_b64_tr_b16 a[206:207], v249 offset:2816\n\t"
"v_cvt_pk_bf16_f32 v202, v204, v205\n\t"
"v_mfma_f32_16x16x32_bf16 v[28:31], a[56:59], a[156:159], v[28:31]\n\t"
"buffer_load_dwordx4 a[28:31], v235, s[84:87], s63 offen offset:1024 sc1\n\t"
"v_mfma_f32_16x16x32_bf16 v[28:31], a[60:63], a[172:175], v[28:31]\n\t"
"ds_write_b128 v221, v[192:195] offset:4096\n\t"
"v_mfma_f32_16x16x32_bf16 v[12:15], a[48:51], a[156:159], v[12:15]\n\t"
"v_cvt_pk_bf16_f32 v203, v206, v207\n\t"
"v_mfma_f32_16x16x32_bf16 v[12:15], a[52:55], a[172:175], v[12:15]\n\t"
"s_waitcnt lgkmcnt(7)\n\t"
"buffer_load_dwordx4 a[64:67], v218, s[84:87], s42 offen offset:0 sc1\n\t"
"v_mfma_f32_16x16x32_bf16 v[96:99], a[72:75], a[144:147], v[96:99]\n\t"
"v_mfma_f32_16x16x32_bf16 v[96:99], a[76:79], a[160:163], v[96:99]\n\t"
"ds_write_b128 v221, v[200:203] offset:12288\n\t"
"v_mfma_f32_16x16x32_bf16 v[112:115], a[80:83], a[144:147], v[112:115]\n\t"
"v_mfma_f32_16x16x32_bf16 v[112:115], a[84:87], a[160:163], v[112:115]\n\t"
"buffer_load_dwordx4 a[68:71], v218, s[84:87], s42 offen offset:1024 sc1\n\t"
"v_mfma_f32_16x16x32_bf16 v[128:131], a[88:91], a[144:147], v[128:131]\n\t"
"v_mfma_f32_16x16x32_bf16 v[128:131], a[92:95], a[160:163], v[128:131]\n\t"
"s_waitcnt lgkmcnt(2)\n\t"
"buffer_load_dwordx4 a[60:63], v235, s[84:87], s42 offen offset:3072 sc1\n\t"
"v_mfma_f32_16x16x32_bf16 v[132:135], a[88:91], a[148:151], v[132:135]\n\t"
"ds_read_b128 a[168:171], v216 offset:30720\n\t"
"v_mfma_f32_16x16x32_bf16 v[132:135], a[92:95], a[164:167], v[132:135]\n\t"
"ds_read_b128 a[156:159], v216 offset:23552\n\t"
"v_mfma_f32_16x16x32_bf16 v[116:119], a[80:83], a[148:151], v[116:119]\n\t"
"ds_read_b128 a[172:175], v216 offset:31744\n\t"
"v_mfma_f32_16x16x32_bf16 v[116:119], a[84:87], a[164:167], v[116:119]\n\t"
"buffer_load_dwordx4 a[56:59], v235, s[84:87], s42 offen offset:2048 sc1\n\t"
"v_mfma_f32_16x16x32_bf16 v[100:103], a[72:75], a[148:151], v[100:103]\n\t"
"v_mfma_f32_16x16x32_bf16 v[100:103], a[76:79], a[164:167], v[100:103]\n\t"
"buffer_load_dwordx4 a[48:51], v235, s[84:87], s42 offen offset:0 sc1\n\t"
"s_waitcnt vmcnt(11) lgkmcnt(3)\n\t"
"s_waitcnt lgkmcnt(0)\n\t"
"s_barrier\n\t"
"v_mfma_f32_16x16x32_bf16 v[104:107], a[72:75], a[152:155], v[104:107]\n\t"
"ds_read_b128 a[148:151], v216 offset:1024\n\t"
"v_add_u32 v248, 80896, v227\n\t"
"v_mfma_f32_16x16x32_bf16 v[104:107], a[76:79], a[168:171], v[104:107]\n\t"
"ds_read_b128 a[144:147], v216 offset:0\n\t"
"v_xor_b32 v249, 32, v248\n\t"
"v_mfma_f32_16x16x32_bf16 v[120:123], a[80:83], a[152:155], v[120:123]\n\t"
"s_nop 0\n\t"
"ds_read_b128 a[160:163], v216 offset:8192\n\t"
"ds_read_b128 a[164:167], v216 offset:9216\n\t"
"v_mfma_f32_16x16x32_bf16 v[120:123], a[84:87], a[168:171], v[120:123]\n\t"
"buffer_store_dwordx4 a[232:235], v233, s[16:19], s25 offen nt\n\t"
"v_mfma_f32_16x16x32_bf16 v[136:139], a[88:91], a[152:155], v[136:139]\n\t"
"v_mfma_f32_16x16x32_bf16 v[136:139], a[92:95], a[168:171], v[136:139]\n\t"
"ds_write_b128 v223, a[244:247] offset:43776\n\t"
"v_mfma_f32_16x16x32_bf16 v[208:211], a[176:179], a[220:223], 0\n\t"
"buffer_load_dwordx4 a[52:55], v235, s[84:87], s42 offen offset:1024 sc1\n\t"
"v_mfma_f32_16x16x32_bf16 v[140:143], a[88:91], a[156:159], v[140:143]\n\t"
"v_xor_b32 v250, 64, v248\n\t"
"v_xor_b32 v251, 96, v248\n\t"
"v_mfma_f32_16x16x32_bf16 v[140:143], a[92:95], a[172:175], v[140:143]\n\t"
"buffer_load_dwordx4 a[88:91], v218, s[84:87], s48 offen offset:0 sc1\n\t"
"v_mfma_f32_16x16x32_bf16 v[212:215], a[180:183], a[220:223], 0\n\t"
"s_nop 0\n\t"
"ds_read_b128 a[152:155], v216 offset:2048\n\t"
"ds_read_b64_tr_b16 a[176:177], v248 offset:0\n\t"
"v_mfma_f32_16x16x32_bf16 v[124:127], a[80:83], a[156:159], v[124:127]\n\t"
"ds_read_b64_tr_b16 a[178:179], v248 offset:512\n\t"
"v_mfma_f32_16x16x32_bf16 v[124:127], a[84:87], a[172:175], v[124:127]\n\t"
"buffer_load_dwordx4 a[92:95], v218, s[84:87], s48 offen offset:1024 sc1\n\t"
"v_mfma_f32_16x16x32_bf16 v[108:111], a[72:75], a[156:159], v[108:111]\n\t"
"ds_read_b64_tr_b16 a[180:181], v249 offset:0\n\t"
"v_cvt_pk_bf16_f32 v208, v208, v209\n\t"
"v_mfma_f32_16x16x32_bf16 v[108:111], a[76:79], a[172:175], v[108:111]\n\t"
"ds_read_b128 a[172:175], v216 offset:11264\n\t"
"v_cvt_pk_bf16_f32 v209, v210, v211\n\t"
"v_mfma_f32_16x16x32_bf16 v[192:195], a[184:187], a[220:223], 0\n\t"
"s_waitcnt lgkmcnt(6)\n\t"
"buffer_load_dwordx4 a[84:87], v235, s[84:87], s48 offen offset:3072 sc1\n\t"
"v_mfma_f32_16x16x32_bf16 v[48:51], a[96:99], a[144:147], v[48:51]\n\t"
"s_nop 0\n\t"
"ds_read_b128 a[156:159], v216 offset:3072\n\t"
"ds_read_b64_tr_b16 a[182:183], v249 offset:512\n\t"
"v_mfma_f32_16x16x32_bf16 v[48:51], a[100:103], a[160:163], v[48:51]\n\t"
"ds_read_b128 a[168:171], v216 offset:10240\n\t"
"v_cvt_pk_bf16_f32 v210, v212, v213\n\t"
"v_mfma_f32_16x16x32_bf16 v[196:199], a[188:191], a[220:223], 0\n\t"
"ds_read_b64_tr_b16 a[184:185], v250 offset:0\n\t"
"v_cvt_pk_bf16_f32 v211, v214, v215\n\t"
"v_mfma_f32_16x16x32_bf16 v[64:67], a[104:107], a[144:147], v[64:67]\n\t"
"buffer_load_dwordx4 a[80:83], v235, s[84:87], s48 offen offset:2048 sc1\n\t"
"v_mfma_f32_16x16x32_bf16 v[64:67], a[108:111], a[160:163], v[64:67]\n\t"
"ds_write_b128 v217, v[208:211] offset:16384\n\t"
"v_mfma_f32_16x16x32_bf16 v[80:83], a[112:115], a[144:147], v[80:83]\n\t"
"v_cvt_pk_bf16_f32 v192, v192, v193\n\t"
"v_cvt_pk_bf16_f32 v193, v194, v195\n\t"
"v_mfma_f32_16x16x32_bf16 v[80:83], a[116:119], a[160:163], v[80:83]\n\t"
"v_cvt_pk_bf16_f32 v194, v196, v197\n\t"
"v_cvt_pk_bf16_f32 v195, v198, v199\n\t"
"v_mfma_f32_16x16x32_bf16 v[200:203], a[192:195], a[220:223], 0\n\t"
"buffer_store_dwordx4 a[236:239], v234, s[16:19], s25 offen nt\n\t"
"v_mfma_f32_16x16x32_bf16 v[84:87], a[112:115], a[148:151], v[84:87]\n\t"
"s_nop 0\n\t"
"ds_read_b64_tr_b16 a[186:187], v250 offset:512\n\t"
"ds_read_b64_tr_b16 a[188:189], v251 offset:0\n\t"
"v_mfma_f32_16x16x32_bf16 v[84:87], a[116:119], a[164:167], v[84:87]\n\t"
"s_nop 0\n\t"
"ds_read_b128 a[160:163], v216 offset:12288\n\t"
"ds_read_b64_tr_b16 a[190:191], v251 offset:512\n\t"
"v_mfma_f32_16x16x32_bf16 v[68:71], a[104:107], a[148:151], v[68:71]\n\t"
"ds_read_b64_tr_b16 a[192:193], v248 offset:1152\n\t"
"v_mfma_f32_16x16x32_bf16 v[68:71], a[108:111], a[164:167], v[68:71]\n\t"
"ds_read_b128 a[144:147], v216 offset:4096\n\t"
"v_cvt_pk_bf16_f32 v200, v200, v201\n\t"
"v_mfma_f32_16x16x32_bf16 v[204:207], a[196:199], a[220:223], 0\n\t"
"buffer_load_dwordx4 a[72:75], v235, s[84:87], s48 offen offset:0 sc1\n\t"
"v_mfma_f32_16x16x32_bf16 v[52:55], a[96:99], a[148:151], v[52:55]\n\t"
"v_cvt_pk_bf16_f32 v201, v202, v203\n\t"
"v_mfma_f32_16x16x32_bf16 v[52:55], a[100:103], a[164:167], v[52:55]\n\t"
"ds_write_b128 v219, v[192:195] offset:16384\n\t"
"v_mfma_f32_16x16x32_bf16 v[208:211], a[200:203], a[220:223], 0\n\t"
"s_waitcnt lgkmcnt(7)\n\t"
"buffer_load_dwordx4 a[76:79], v235, s[84:87], s48 offen offset:1024 sc1\n\t"
"v_mfma_f32_16x16x32_bf16 v[56:59], a[96:99], a[152:155], v[56:59]\n\t"
"v_mfma_f32_16x16x32_bf16 v[56:59], a[100:103], a[168:171], v[56:59]\n\t"
"v_cvt_pk_bf16_f32 v202, v204, v205\n\t"
"v_cvt_pk_bf16_f32 v203, v206, v207\n\t"
"v_mfma_f32_16x16x32_bf16 v[72:75], a[104:107], a[152:155], v[72:75]\n\t"
"ds_read_b128 a[148:151], v216 offset:5120\n\t"
"v_mfma_f32_16x16x32_bf16 v[72:75], a[108:111], a[168:171], v[72:75]\n\t"
"ds_read_b128 a[164:167], v216 offset:13312\n\t"
"v_mfma_f32_16x16x32_bf16 v[212:215], a[204:207], a[220:223], 0\n\t"
"ds_read_b64_tr_b16 a[194:195], v248 offset:1664\n\t"
"v_cvt_pk_bf16_f32 v208, v208, v209\n\t"
"v_mfma_f32_16x16x32_bf16 v[88:91], a[112:115], a[152:155], v[88:91]\n\t"
"s_nop 0\n\t"
"ds_read_b64_tr_b16 a[196:197], v249 offset:1152\n\t"
"ds_read_b64_tr_b16 a[198:199], v249 offset:1664\n\t"
"v_mfma_f32_16x16x32_bf16 v[88:91], a[116:119], a[168:171], v[88:91]\n\t"
"s_nop 0\n\t"
"ds_read_b128 a[152:155], v216 offset:6144\n\t"
"ds_read_b64_tr_b16 a[200:201], v250 offset:1152\n\t"
"v_mfma_f32_16x16x32_bf16 v[92:95], a[112:115], a[156:159], v[92:95]\n\t"
"v_mfma_f32_16x16x32_bf16 v[92:95], a[116:119], a[172:175], v[92:95]\n\t"
"ds_write_b128 v217, v[200:203] offset:24576\n\t"
"v_mfma_f32_16x16x32_bf16 v[76:79], a[104:107], a[156:159], v[76:79]\n\t"
"v_cvt_pk_bf16_f32 v209, v210, v211\n\t"
"v_mfma_f32_16x16x32_bf16 v[76:79], a[108:111], a[172:175], v[76:79]\n\t"
"v_cvt_pk_bf16_f32 v210, v212, v213\n\t"
"v_mfma_f32_16x16x32_bf16 v[60:63], a[96:99], a[156:159], v[60:63]\n\t"
"v_cvt_pk_bf16_f32 v211, v214, v215\n\t"
"v_mfma_f32_16x16x32_bf16 v[60:63], a[100:103], a[172:175], v[60:63]\n\t"
"s_waitcnt lgkmcnt(7)\n\t"
"ds_write_b128 v219, v[208:211] offset:24576\n\t"
"v_mfma_f32_16x16x32_bf16 v[144:147], a[120:123], a[144:147], v[144:147]\n\t"
"v_mfma_f32_16x16x32_bf16 v[144:147], a[124:127], a[160:163], v[144:147]\n\t"
"v_mfma_f32_16x16x32_bf16 v[160:163], a[128:131], a[144:147], v[160:163]\n\t"
"v_mfma_f32_16x16x32_bf16 v[160:163], a[132:135], a[160:163], v[160:163]\n\t"
"ds_read_b64_tr_b16 a[202:203], v250 offset:1664\n\t"
"v_mfma_f32_16x16x32_bf16 v[176:179], a[136:139], a[144:147], v[176:179]\n\t"
"ds_read_b64_tr_b16 a[204:205], v251 offset:1152\n\t"
"v_mfma_f32_16x16x32_bf16 v[176:179], a[140:143], a[160:163], v[176:179]\n\t"
"s_waitcnt lgkmcnt(3)\n\t"
"ds_read_b128 a[172:175], v216 offset:15360\n\t"
"v_mfma_f32_16x16x32_bf16 v[180:183], a[136:139], a[148:151], v[180:183]\n\t"
"ds_read_b128 a[156:159], v216 offset:7168\n\t"
"v_mfma_f32_16x16x32_bf16 v[180:183], a[140:143], a[164:167], v[180:183]\n\t"
"ds_read_b64_tr_b16 a[206:207], v251 offset:1664\n\t"
"v_mfma_f32_16x16x32_bf16 v[164:167], a[128:131], a[148:151], v[164:167]\n\t"
"ds_read_b128 a[168:171], v216 offset:14336\n\t"
"v_mfma_f32_16x16x32_bf16 v[164:167], a[132:135], a[164:167], v[164:167]\n\t"
"v_mfma_f32_16x16x32_bf16 v[148:151], a[120:123], a[148:151], v[148:151]\n\t"
"v_mfma_f32_16x16x32_bf16 v[148:151], a[124:127], a[164:167], v[148:151]\n\t"
"s_waitcnt lgkmcnt(0)\n\t"
"s_barrier\n\t"
"v_mfma_f32_16x16x32_bf16 v[152:155], a[120:123], a[152:155], v[152:155]\n\t"
"s_add_u32 m0, s26, 145280\n\t"
"ds_read_b128 a[144:147], v216 offset:16384\n\t"
"ds_read_b128 a[148:151], v216 offset:17408\n\t"
"v_mfma_f32_16x16x32_bf16 v[152:155], a[124:127], a[168:171], v[152:155]\n\t"
"buffer_load_dwordx4 v236, s[12:15], s22 offen offset:128 nt lds\n\t"
"v_mfma_f32_16x16x32_bf16 v[168:171], a[128:131], a[152:155], v[168:171]\n\t"
"s_xor_b32 s24, s24, 384\n\t"
"ds_read_b128 a[160:163], v216 offset:24576\n\t"
"s_xor_b32 s20, s20, 384\n\t"
"v_mfma_f32_16x16x32_bf16 v[168:171], a[132:135], a[168:171], v[168:171]\n\t"
"ds_read_b128 a[164:167], v216 offset:25600\n\t"
"v_add_u32 v248, 53504, v226\n\t"
"v_mfma_f32_16x16x32_bf16 v[184:187], a[136:139], a[152:155], v[184:187]\n\t"
"v_xor_b32 v249, 32, v248\n\t"
"v_xor_b32 v250, 64, v248\n\t"
"v_mfma_f32_16x16x32_bf16 v[184:187], a[140:143], a[168:171], v[184:187]\n\t"
"v_xor_b32 v251, 96, v248\n\t"
"v_mfma_f32_16x16x32_bf16 v[192:195], a[176:179], v[252:255], 0\n\t"
"s_xor_b32 s27, s27, 384\n\t"
"ds_read_b128 a[168:171], v216 offset:26624\n\t"
"ds_read_b64_tr_b16 a[176:177], v248 offset:48128\n\t"
"v_mfma_f32_16x16x32_bf16 v[188:191], a[136:139], a[156:159], v[188:191]\n\t"
"ds_read_b64_tr_b16 a[178:179], v248 offset:48640\n\t"
"v_mfma_f32_16x16x32_bf16 v[188:191], a[140:143], a[172:175], v[188:191]\n\t"
"buffer_load_dwordx4 v237, s[12:15], s22 offen offset:1152 nt lds\n\t"
"v_mfma_f32_16x16x32_bf16 v[196:199], a[180:183], v[252:255], 0\n\t"
"ds_read_b64_tr_b16 a[180:181], v249 offset:48128\n\t"
"v_mfma_f32_16x16x32_bf16 v[172:175], a[128:131], a[156:159], v[172:175]\n\t"
"ds_read_b64_tr_b16 a[182:183], v249 offset:48640\n\t"
"v_cvt_pk_bf16_f32 v192, v192, v193\n\t"
"v_mfma_f32_16x16x32_bf16 v[172:175], a[132:135], a[172:175], v[172:175]\n\t"
"ds_read_b128 a[152:155], v216 offset:18432\n\t"
"v_mfma_f32_16x16x32_bf16 v[156:159], a[120:123], a[156:159], v[156:159]\n\t"
"ds_read_b128 a[156:159], v216 offset:19456\n\t"
"v_cvt_pk_bf16_f32 v193, v194, v195\n\t"
"v_mfma_f32_16x16x32_bf16 v[156:159], a[124:127], a[172:175], v[156:159]\n\t"
"buffer_load_dwordx4 v238, s[12:15], s22 offen offset:2176 nt lds\n\t"
"v_mfma_f32_16x16x32_bf16 v[200:203], a[184:187], v[252:255], 0\n\t"
"s_waitcnt lgkmcnt(7)\n\t"
"ds_read_b64_tr_b16 a[184:185], v250 offset:50432\n\t"
"v_pk_add_f32 v[144:145], v[48:49], v[144:145] neg_lo:[0,1] neg_hi:[0,1]\n\t"
"v_pk_add_f32 v[146:147], v[50:51], v[146:147] neg_lo:[0,1] neg_hi:[0,1]\n\t"
"v_pk_fma_f32 v[48:49], v[144:145], s[90:91], v[48:49] neg_lo:[1,0,0] neg_hi:[1,0,0]\n\t"
"v_pk_fma_f32 v[50:51], v[146:147], s[90:91], v[50:51] neg_lo:[1,0,0] neg_hi:[1,0,0]\n\t"
"v_pk_add_f32 v[160:161], v[64:65], v[160:161] neg_lo:[0,1] neg_hi:[0,1]\n\t"
"v_pk_add_f32 v[162:163], v[66:67], v[162:163] neg_lo:[0,1] neg_hi:[0,1]\n\t"
"v_pk_fma_f32 v[64:65], v[160:161], s[90:91], v[64:65] neg_lo:[1,0,0] neg_hi:[1,0,0]\n\t"
"v_pk_fma_f32 v[66:67], v[162:163], s[90:91], v[66:67] neg_lo:[1,0,0] neg_hi:[1,0,0]\n\t"
"v_pk_add_f32 v[176:177], v[80:81], v[176:177] neg_lo:[0,1] neg_hi:[0,1]\n\t"
"v_pk_add_f32 v[178:179], v[82:83], v[178:179] neg_lo:[0,1] neg_hi:[0,1]\n\t"
"v_pk_fma_f32 v[80:81], v[176:177], s[90:91], v[80:81] neg_lo:[1,0,0] neg_hi:[1,0,0]\n\t"
"v_pk_fma_f32 v[82:83], v[178:179], s[90:91], v[82:83] neg_lo:[1,0,0] neg_hi:[1,0,0]\n\t"
"v_pk_add_f32 v[180:181], v[84:85], v[180:181] neg_lo:[0,1] neg_hi:[0,1]\n\t"
"v_pk_add_f32 v[182:183], v[86:87], v[182:183] neg_lo:[0,1] neg_hi:[0,1]\n\t"
"v_pk_fma_f32 v[84:85], v[180:181], s[90:91], v[84:85] neg_lo:[1,0,0] neg_hi:[1,0,0]\n\t"
"v_pk_fma_f32 v[86:87], v[182:183], s[90:91], v[86:87] neg_lo:[1,0,0] neg_hi:[1,0,0]\n\t"
"v_pk_add_f32 v[164:165], v[68:69], v[164:165] neg_lo:[0,1] neg_hi:[0,1]\n\t"
"v_pk_add_f32 v[166:167], v[70:71], v[166:167] neg_lo:[0,1] neg_hi:[0,1]\n\t"
"v_pk_fma_f32 v[68:69], v[164:165], s[90:91], v[68:69] neg_lo:[1,0,0] neg_hi:[1,0,0]\n\t"
"v_pk_fma_f32 v[70:71], v[166:167], s[90:91], v[70:71] neg_lo:[1,0,0] neg_hi:[1,0,0]\n\t"
"v_pk_add_f32 v[148:149], v[52:53], v[148:149] neg_lo:[0,1] neg_hi:[0,1]\n\t"
"v_pk_add_f32 v[150:151], v[54:55], v[150:151] neg_lo:[0,1] neg_hi:[0,1]\n\t"
"v_pk_fma_f32 v[52:53], v[148:149], s[90:91], v[52:53] neg_lo:[1,0,0] neg_hi:[1,0,0]\n\t"
"v_pk_fma_f32 v[54:55], v[150:151], s[90:91], v[54:55] neg_lo:[1,0,0] neg_hi:[1,0,0]\n\t"
"v_pk_add_f32 v[152:153], v[56:57], v[152:153] neg_lo:[0,1] neg_hi:[0,1]\n\t"
"v_pk_add_f32 v[154:155], v[58:59], v[154:155] neg_lo:[0,1] neg_hi:[0,1]\n\t"
"v_pk_fma_f32 v[56:57], v[152:153], s[90:91], v[56:57] neg_lo:[1,0,0] neg_hi:[1,0,0]\n\t"
"v_pk_fma_f32 v[58:59], v[154:155], s[90:91], v[58:59] neg_lo:[1,0,0] neg_hi:[1,0,0]\n\t"
"v_pk_add_f32 v[168:169], v[72:73], v[168:169] neg_lo:[0,1] neg_hi:[0,1]\n\t"
"v_pk_add_f32 v[170:171], v[74:75], v[170:171] neg_lo:[0,1] neg_hi:[0,1]\n\t"
"v_pk_fma_f32 v[72:73], v[168:169], s[90:91], v[72:73] neg_lo:[1,0,0] neg_hi:[1,0,0]\n\t"
"v_pk_fma_f32 v[74:75], v[170:171], s[90:91], v[74:75] neg_lo:[1,0,0] neg_hi:[1,0,0]\n\t"
"v_pk_add_f32 v[184:185], v[88:89], v[184:185] neg_lo:[0,1] neg_hi:[0,1]\n\t"
"v_pk_add_f32 v[186:187], v[90:91], v[186:187] neg_lo:[0,1] neg_hi:[0,1]\n\t"
"v_pk_fma_f32 v[88:89], v[184:185], s[90:91], v[88:89] neg_lo:[1,0,0] neg_hi:[1,0,0]\n\t"
"v_pk_fma_f32 v[90:91], v[186:187], s[90:91], v[90:91] neg_lo:[1,0,0] neg_hi:[1,0,0]\n\t"
"v_pk_add_f32 v[188:189], v[92:93], v[188:189] neg_lo:[0,1] neg_hi:[0,1]\n\t"
"v_pk_add_f32 v[190:191], v[94:95], v[190:191] neg_lo:[0,1] neg_hi:[0,1]\n\t"
"v_pk_fma_f32 v[92:93], v[188:189], s[90:91], v[92:93] neg_lo:[1,0,0] neg_hi:[1,0,0]\n\t"
"v_pk_fma_f32 v[94:95], v[190:191], s[90:91], v[94:95] neg_lo:[1,0,0] neg_hi:[1,0,0]\n\t"
"v_pk_add_f32 v[172:173], v[76:77], v[172:173] neg_lo:[0,1] neg_hi:[0,1]\n\t"
"v_pk_add_f32 v[174:175], v[78:79], v[174:175] neg_lo:[0,1] neg_hi:[0,1]\n\t"
"v_pk_fma_f32 v[76:77], v[172:173], s[90:91], v[76:77] neg_lo:[1,0,0] neg_hi:[1,0,0]\n\t"
"v_pk_fma_f32 v[78:79], v[174:175], s[90:91], v[78:79] neg_lo:[1,0,0] neg_hi:[1,0,0]\n\t"
"v_pk_add_f32 v[156:157], v[60:61], v[156:157] neg_lo:[0,1] neg_hi:[0,1]\n\t"
"v_pk_add_f32 v[158:159], v[62:63], v[158:159] neg_lo:[0,1] neg_hi:[0,1]\n\t"
"v_pk_fma_f32 v[60:61], v[156:157], s[90:91], v[60:61] neg_lo:[1,0,0] neg_hi:[1,0,0]\n\t"
"v_pk_fma_f32 v[62:63], v[158:159], s[90:91], v[62:63] neg_lo:[1,0,0] neg_hi:[1,0,0]\n\t"
"v_mfma_f32_16x16x32_bf16 v[48:51], a[0:3], a[144:147], v[48:51]\n\t"
"ds_read_b128 a[172:175], v216 offset:27648\n\t"
"v_cvt_pk_bf16_f32 v194, v196, v197\n\t"
"v_mfma_f32_16x16x32_bf16 v[48:51], a[4:7], a[160:163], v[48:51]\n\t"
"ds_read_b64_tr_b16 a[186:187], v250 offset:50944\n\t"
"v_mfma_f32_16x16x32_bf16 v[204:207], a[188:191], v[252:255], 0\n\t"
"ds_read_b64_tr_b16 a[188:189], v251 offset:50432\n\t"
"v_cvt_pk_bf16_f32 v195, v198, v199\n\t"
"v_mfma_f32_16x16x32_bf16 v[64:67], a[8:11], a[144:147], v[64:67]\n\t"
"ds_read_b64_tr_b16 a[190:191], v251 offset:50944\n\t"
"v_mfma_f32_16x16x32_bf16 v[64:67], a[12:15], a[160:163], v[64:67]\n\t"
"buffer_load_dwordx4 v239, s[12:15], s22 offen offset:3200 nt lds\n\t"
"v_mfma_f32_16x16x32_bf16 v[80:83], a[16:19], a[144:147], v[80:83]\n\t"
"ds_read_b128 a[144:147], v216 offset:20480\n\t"
"v_mfma_f32_16x16x32_bf16 v[80:83], a[20:23], a[160:163], v[80:83]\n\t"
"ds_read_b128 a[160:163], v216 offset:28672\n\t"
"v_cvt_pk_bf16_f32 v200, v200, v201\n\t"
"v_mfma_f32_16x16x32_bf16 v[208:211], a[192:195], v[252:255], 0\n\t"
"s_xor_b32 s25, s25, 384\n\t"
"ds_read_b64_tr_b16 a[192:193], v250 offset:48128\n\t"
"ds_read_b64_tr_b16 a[194:195], v250 offset:48640\n\t"
"v_mfma_f32_16x16x32_bf16 v[84:87], a[16:19], a[148:151], v[84:87]\n\t"
"v_cvt_pk_bf16_f32 v201, v202, v203\n\t"
"v_mfma_f32_16x16x32_bf16 v[84:87], a[20:23], a[164:167], v[84:87]\n\t"
"ds_write_b128 v221, v[192:195] offset:0\n\t"
"v_mfma_f32_16x16x32_bf16 v[68:71], a[8:11], a[148:151], v[68:71]\n\t"
"v_cvt_pk_bf16_f32 v202, v204, v205\n\t"
"v_cvt_pk_bf16_f32 v203, v206, v207\n\t"
"v_mfma_f32_16x16x32_bf16 v[68:71], a[12:15], a[164:167], v[68:71]\n\t"
"s_mov_b64 exec, s[36:37]\n\t"
"buffer_load_dwordx4 v240, s[12:15], s22 offen offset:3712 nt lds\n\t"
"s_mov_b64 exec, -1\n\t"
"v_mfma_f32_16x16x32_bf16 v[212:215], a[196:199], v[252:255], 0\n\t"
"v_cvt_pk_bf16_f32 v208, v208, v209\n\t"
"v_mfma_f32_16x16x32_bf16 v[52:55], a[0:3], a[148:151], v[52:55]\n\t"
"ds_read_b64_tr_b16 a[196:197], v251 offset:48128\n\t"
"v_mfma_f32_16x16x32_bf16 v[52:55], a[4:7], a[164:167], v[52:55]\n\t"
"ds_read_b128 a[164:167], v216 offset:29696\n\t"
"v_cvt_pk_bf16_f32 v209, v210, v211\n\t"
"v_mfma_f32_16x16x32_bf16 v[192:195], a[200:203], v[252:255], 0\n\t"
"s_waitcnt lgkmcnt(6)\n\t"
"ds_read_b128 a[148:151], v216 offset:21504\n\t"
"v_mfma_f32_16x16x32_bf16 v[56:59], a[0:3], a[152:155], v[56:59]\n\t"
"v_cvt_pk_bf16_f32 v210, v212, v213\n\t"
"v_cvt_pk_bf16_f32 v211, v214, v215\n\t"
"v_mfma_f32_16x16x32_bf16 v[56:59], a[4:7], a[168:171], v[56:59]\n\t"
"ds_write_b128 v221, v[200:203] offset:8192\n\t"
"v_mfma_f32_16x16x32_bf16 v[72:75], a[8:11], a[152:155], v[72:75]\n\t"
"v_mfma_f32_16x16x32_bf16 v[72:75], a[12:15], a[168:171], v[72:75]\n\t"
"v_cvt_pk_bf16_f32 v192, v192, v193\n\t"
"v_mfma_f32_16x16x32_bf16 v[196:199], a[204:207], v[252:255], 0\n\t"
"ds_write_b128 v221, v[208:211] offset:4096\n\t"
"v_mfma_f32_16x16x32_bf16 v[88:91], a[16:19], a[152:155], v[88:91]\n\t"
"v_mfma_f32_16x16x32_bf16 v[88:91], a[20:23], a[168:171], v[88:91]\n\t"
"v_cvt_pk_bf16_f32 v193, v194, v195\n\t"
"v_mfma_f32_16x16x32_bf16 v[92:95], a[16:19], a[156:159], v[92:95]\n\t"
"v_mfma_f32_16x16x32_bf16 v[92:95], a[20:23], a[172:175], v[92:95]\n\t"
"ds_read_b64_tr_b16 a[198:199], v251 offset:48640\n\t"
"v_mfma_f32_16x16x32_bf16 v[76:79], a[8:11], a[156:159], v[76:79]\n\t"
"v_cvt_pk_bf16_f32 v194, v196, v197\n\t"
"v_cvt_pk_bf16_f32 v195, v198, v199\n\t"
"v_mfma_f32_16x16x32_bf16 v[76:79], a[12:15], a[172:175], v[76:79]\n\t"
"ds_write_b128 v221, v[192:195] offset:12288\n\t"
"v_mfma_f32_16x16x32_bf16 v[60:63], a[0:3], a[156:159], v[60:63]\n\t"
"v_mfma_f32_16x16x32_bf16 v[60:63], a[4:7], a[172:175], v[60:63]\n\t"
"s_waitcnt vmcnt(19) lgkmcnt(3)\n\t"
"v_mfma_f32_16x16x32_bf16 v[144:147], a[24:27], a[144:147], v[144:147]\n\t"
"v_mfma_f32_16x16x32_bf16 v[144:147], a[28:31], a[160:163], v[144:147]\n\t"
"ds_read_b128 a[168:171], v216 offset:30720\n\t"
"v_mfma_f32_16x16x32_bf16 v[160:163], a[32:35], a[144:147], v[160:163]\n\t"
"ds_read_b64_tr_b16 a[200:201], v248 offset:50432\n\t"
"v_mfma_f32_16x16x32_bf16 v[160:163], a[36:39], a[160:163], v[160:163]\n\t"
"ds_read_b64_tr_b16 a[202:203], v248 offset:50944\n\t"
"v_mfma_f32_16x16x32_bf16 v[176:179], a[40:43], a[144:147], v[176:179]\n\t"
"ds_read_b64_tr_b16 a[204:205], v249 offset:50432\n\t"
"v_mfma_f32_16x16x32_bf16 v[176:179], a[44:47], a[160:163], v[176:179]\n\t"
"ds_read_b64_tr_b16 a[206:207], v249 offset:50944\n\t"
"v_mfma_f32_16x16x32_bf16 v[180:183], a[40:43], a[148:151], v[180:183]\n\t"
"ds_read_b128 a[156:159], v216 offset:23552\n\t"
"v_mfma_f32_16x16x32_bf16 v[180:183], a[44:47], a[164:167], v[180:183]\n\t"
"ds_read_b128 a[172:175], v216 offset:31744\n\t"
"v_mfma_f32_16x16x32_bf16 v[164:167], a[32:35], a[148:151], v[164:167]\n\t"
"ds_read_b128 a[152:155], v216 offset:22528\n\t"
"v_mfma_f32_16x16x32_bf16 v[164:167], a[36:39], a[164:167], v[164:167]\n\t"
"v_mfma_f32_16x16x32_bf16 v[148:151], a[24:27], a[148:151], v[148:151]\n\t"
"v_mfma_f32_16x16x32_bf16 v[148:151], a[28:31], a[164:167], v[148:151]\n\t"
"s_waitcnt lgkmcnt(0)\n\t"
"s_barrier\n\t"
"v_mfma_f32_16x16x32_bf16 v[152:155], a[24:27], a[152:155], v[152:155]\n\t"
"ds_read_b128 a[160:163], v216 offset:8192\n\t"
"v_mfma_f32_16x16x32_bf16 v[152:155], a[28:31], a[168:171], v[152:155]\n\t"
"ds_read_b128 a[144:147], v216 offset:0\n\t"
"v_mfma_f32_16x16x32_bf16 v[168:171], a[32:35], a[152:155], v[168:171]\n\t"
"ds_read_b128 a[148:151], v216 offset:1024\n\t"
"v_mfma_f32_16x16x32_bf16 v[168:171], a[36:39], a[168:171], v[168:171]\n\t"
"ds_read_b128 a[164:167], v216 offset:9216\n\t"
"v_mfma_f32_16x16x32_bf16 v[184:187], a[40:43], a[152:155], v[184:187]\n\t"
"v_mfma_f32_16x16x32_bf16 v[184:187], a[44:47], a[168:171], v[184:187]\n\t"
"ds_write_b128 v223, a[240:243] offset:2304\n\t"
"v_mfma_f32_16x16x32_bf16 v[200:203], a[176:179], a[220:223], 0\n\t"
"v_mfma_f32_16x16x32_bf16 v[188:191], a[40:43], a[156:159], v[188:191]\n\t"
"v_mfma_f32_16x16x32_bf16 v[188:191], a[44:47], a[172:175], v[188:191]\n\t"
"v_mfma_f32_16x16x32_bf16 v[204:207], a[180:183], a[220:223], 0\n\t"
"s_nop 0\n\t"
"ds_read_b128 a[152:155], v216 offset:2048\n\t"
"ds_read_b128 a[168:171], v216 offset:10240\n\t"
"v_mfma_f32_16x16x32_bf16 v[172:175], a[32:35], a[156:159], v[172:175]\n\t"
"ds_read_b64_tr_b16 a[176:177], v248 offset:0\n\t"
"v_cvt_pk_bf16_f32 v200, v200, v201\n\t"
"v_mfma_f32_16x16x32_bf16 v[172:175], a[36:39], a[172:175], v[172:175]\n\t"
"ds_read_b64_tr_b16 a[178:179], v248 offset:512\n\t"
"v_mfma_f32_16x16x32_bf16 v[156:159], a[24:27], a[156:159], v[156:159]\n\t"
"ds_read_b128 a[156:159], v216 offset:3072\n\t"
"v_cvt_pk_bf16_f32 v201, v202, v203\n\t"
"v_mfma_f32_16x16x32_bf16 v[156:159], a[28:31], a[172:175], v[156:159]\n\t"
"ds_read_b128 a[172:175], v216 offset:11264\n\t"
"v_mfma_f32_16x16x32_bf16 v[208:211], a[184:187], a[220:223], 0\n\t"
"s_waitcnt lgkmcnt(7)\n\t"
"ds_read_b64_tr_b16 a[180:181], v249 offset:0\n\t"
"v_cvt_pk_bf16_f32 v202, v204, v205\n\t"
"v_mfma_f32_16x16x32_bf16 v[0:3], a[96:99], a[144:147], v[0:3]\n\t"
"ds_read_b64_tr_b16 a[182:183], v249 offset:512\n\t"
"v_mfma_f32_16x16x32_bf16 v[0:3], a[100:103], a[160:163], v[0:3]\n\t"
"ds_read_b64_tr_b16 a[184:185], v250 offset:2304\n\t"
"v_cvt_pk_bf16_f32 v203, v206, v207\n\t"
"v_mfma_f32_16x16x32_bf16 v[212:215], a[188:191], a[220:223], 0\n\t"
"ds_read_b64_tr_b16 a[186:187], v250 offset:2816\n\t"
"v_mfma_f32_16x16x32_bf16 v[16:19], a[104:107], a[144:147], v[16:19]\n\t"
"ds_read_b64_tr_b16 a[188:189], v251 offset:2304\n\t"
"v_cvt_pk_bf16_f32 v208, v208, v209\n\t"
"v_mfma_f32_16x16x32_bf16 v[16:19], a[108:111], a[160:163], v[16:19]\n\t"
"ds_read_b64_tr_b16 a[190:191], v251 offset:2816\n\t"
"v_cvt_pk_bf16_f32 v209, v210, v211\n\t"
"v_mfma_f32_16x16x32_bf16 v[32:35], a[112:115], a[144:147], v[32:35]\n\t"
"ds_read_b128 a[144:147], v216 offset:4096\n\t"
"v_mfma_f32_16x16x32_bf16 v[32:35], a[116:119], a[160:163], v[32:35]\n\t"
"ds_read_b128 a[160:163], v216 offset:12288\n\t"
"v_cvt_pk_bf16_f32 v210, v212, v213\n\t"
"v_mfma_f32_16x16x32_bf16 v[192:195], a[192:195], a[220:223], 0\n\t"
"s_nop 0\n\t"
"ds_read_b64_tr_b16 a[192:193], v250 offset:0\n\t"
"ds_read_b64_tr_b16 a[194:195], v250 offset:512\n\t"
"v_mfma_f32_16x16x32_bf16 v[36:39], a[112:115], a[148:151], v[36:39]\n\t"
"v_cvt_pk_bf16_f32 v211, v214, v215\n\t"
"v_mfma_f32_16x16x32_bf16 v[36:39], a[116:119], a[164:167], v[36:39]\n\t"
"ds_write_b128 v217, v[200:203] offset:16384\n\t"
"v_mfma_f32_16x16x32_bf16 v[20:23], a[104:107], a[148:151], v[20:23]\n\t"
"v_mfma_f32_16x16x32_bf16 v[20:23], a[108:111], a[164:167], v[20:23]\n\t"
"v_cvt_pk_bf16_f32 v192, v192, v193\n\t"
"v_cvt_pk_bf16_f32 v193, v194, v195\n\t"
"v_mfma_f32_16x16x32_bf16 v[196:199], a[196:199], a[220:223], 0\n\t"
"ds_write_b128 v219, v[208:211] offset:16384\n\t"
"v_mfma_f32_16x16x32_bf16 v[4:7], a[96:99], a[148:151], v[4:7]\n\t"
"v_mfma_f32_16x16x32_bf16 v[4:7], a[100:103], a[164:167], v[4:7]\n\t"
"v_mfma_f32_16x16x32_bf16 v[200:203], a[200:203], a[220:223], 0\n\t"
"s_waitcnt lgkmcnt(5)\n\t"
"v_mfma_f32_16x16x32_bf16 v[8:11], a[96:99], a[152:155], v[8:11]\n\t"
"ds_read_b128 a[148:151], v216 offset:5120\n\t"
"s_nop 0\n\t"
"v_cvt_pk_bf16_f32 v194, v196, v197\n\t"
"v_mfma_f32_16x16x32_bf16 v[8:11], a[100:103], a[168:171], v[8:11]\n\t"
"s_nop 0\n\t"
"ds_read_b128 a[164:167], v216 offset:13312\n\t"
"ds_read_b64_tr_b16 a[196:197], v251 offset:0\n\t"
"v_mfma_f32_16x16x32_bf16 v[24:27], a[104:107], a[152:155], v[24:27]\n\t"
"ds_read_b64_tr_b16 a[198:199], v251 offset:512\n\t"
"v_mfma_f32_16x16x32_bf16 v[24:27], a[108:111], a[168:171], v[24:27]\n\t"
"ds_read_b64_tr_b16 a[200:201], v248 offset:2304\n\t"
"v_cvt_pk_bf16_f32 v200, v200, v201\n\t"
"v_mfma_f32_16x16x32_bf16 v[204:207], a[204:207], a[220:223], 0\n\t"
"ds_read_b64_tr_b16 a[202:203], v248 offset:2816\n\t"
"v_mfma_f32_16x16x32_bf16 v[40:43], a[112:115], a[152:155], v[40:43]\n\t"
"ds_read_b64_tr_b16 a[204:205], v249 offset:2304\n\t"
"v_cvt_pk_bf16_f32 v201, v202, v203\n\t"
"v_mfma_f32_16x16x32_bf16 v[40:43], a[116:119], a[168:171], v[40:43]\n\t"
"ds_read_b128 a[168:171], v216 offset:14336\n\t"
"v_cvt_pk_bf16_f32 v195, v198, v199\n\t"
"v_mfma_f32_16x16x32_bf16 v[44:47], a[112:115], a[156:159], v[44:47]\n\t"
"buffer_load_dwordx4 a[112:115], v218, s[84:87], s75 offen offset:0 sc1\n\t"
"v_mfma_f32_16x16x32_bf16 v[44:47], a[116:119], a[172:175], v[44:47]\n\t"
"ds_write_b128 v217, v[192:195] offset:24576\n\t"
"v_mfma_f32_16x16x32_bf16 v[28:31], a[104:107], a[156:159], v[28:31]\n\t"
"buffer_load_dwordx4 a[116:119], v218, s[84:87], s75 offen offset:1024 sc1\n\t"
"v_mfma_f32_16x16x32_bf16 v[28:31], a[108:111], a[172:175], v[28:31]\n\t"
"v_cvt_pk_bf16_f32 v202, v204, v205\n\t"
"v_cvt_pk_bf16_f32 v203, v206, v207\n\t"
"v_mfma_f32_16x16x32_bf16 v[12:15], a[96:99], a[156:159], v[12:15]\n\t"
"buffer_load_dwordx4 a[108:111], v235, s[84:87], s75 offen offset:3072 sc1\n\t"
"v_mfma_f32_16x16x32_bf16 v[12:15], a[100:103], a[172:175], v[12:15]\n\t"
"s_waitcnt lgkmcnt(5)\n\t"
"ds_read_b128 a[156:159], v216 offset:7168\n\t"
"v_mfma_f32_16x16x32_bf16 v[96:99], a[120:123], a[144:147], v[96:99]\n\t"
"buffer_load_dwordx4 a[104:107], v235, s[84:87], s75 offen offset:2048 sc1\n\t"
"v_mfma_f32_16x16x32_bf16 v[96:99], a[124:127], a[160:163], v[96:99]\n\t"
"ds_write_b128 v219, v[200:203] offset:24576\n\t"
"v_mfma_f32_16x16x32_bf16 v[112:115], a[128:131], a[144:147], v[112:115]\n\t"
"v_mfma_f32_16x16x32_bf16 v[112:115], a[132:135], a[160:163], v[112:115]\n\t"
"buffer_load_dwordx4 a[96:99], v235, s[84:87], s75 offen offset:0 sc1\n\t"
"v_mfma_f32_16x16x32_bf16 v[128:131], a[136:139], a[144:147], v[128:131]\n\t"
"v_mfma_f32_16x16x32_bf16 v[128:131], a[140:143], a[160:163], v[128:131]\n\t"
"buffer_load_dwordx4 a[100:103], v235, s[84:87], s75 offen offset:1024 sc1\n\t"
"v_mfma_f32_16x16x32_bf16 v[132:135], a[136:139], a[148:151], v[132:135]\n\t"
"ds_read_b128 a[152:155], v216 offset:6144\n\t"
"v_mfma_f32_16x16x32_bf16 v[132:135], a[140:143], a[164:167], v[132:135]\n\t"
"ds_read_b64_tr_b16 a[206:207], v249 offset:2816\n\t"
"v_mfma_f32_16x16x32_bf16 v[116:119], a[128:131], a[148:151], v[116:119]\n\t"
"ds_read_b128 a[172:175], v216 offset:15360\n\t"
"v_mfma_f32_16x16x32_bf16 v[116:119], a[132:135], a[164:167], v[116:119]\n\t"
"v_mfma_f32_16x16x32_bf16 v[100:103], a[120:123], a[148:151], v[100:103]\n\t"
"v_mfma_f32_16x16x32_bf16 v[100:103], a[124:127], a[164:167], v[100:103]\n\t"
"s_waitcnt lgkmcnt(0)\n\t"
"s_barrier\n\t"
"v_mfma_f32_16x16x32_bf16 v[104:107], a[120:123], a[152:155], v[104:107]\n\t"
"ds_read_b128 a[160:163], v216 offset:24576\n\t"
"v_add_u32 v248, 80896, v226\n\t"
"v_mfma_f32_16x16x32_bf16 v[104:107], a[124:127], a[168:171], v[104:107]\n\t"
"ds_read_b128 a[144:147], v216 offset:16384\n\t"
"v_mfma_f32_16x16x32_bf16 v[120:123], a[128:131], a[152:155], v[120:123]\n\t"
"ds_read_b128 a[148:151], v216 offset:17408\n\t"
"v_xor_b32 v249, 32, v248\n\t"
"v_mfma_f32_16x16x32_bf16 v[120:123], a[132:135], a[168:171], v[120:123]\n\t"
"ds_read_b128 a[164:167], v216 offset:25600\n\t"
"v_xor_b32 v250, 64, v248\n\t"
"v_mfma_f32_16x16x32_bf16 v[136:139], a[136:139], a[152:155], v[136:139]\n\t"
"ds_read_b128 a[152:155], v216 offset:18432\n\t"
"v_xor_b32 v251, 96, v248\n\t"
"v_mfma_f32_16x16x32_bf16 v[136:139], a[140:143], a[168:171], v[136:139]\n\t"
"ds_read_b128 a[168:171], v216 offset:26624\n\t"
"v_mfma_f32_16x16x32_bf16 v[208:211], a[176:179], a[220:223], 0\n\t"
"ds_read_b64_tr_b16 a[176:177], v248 offset:41472\n\t"
"v_mfma_f32_16x16x32_bf16 v[140:143], a[136:139], a[156:159], v[140:143]\n\t"
"ds_read_b64_tr_b16 a[178:179], v248 offset:41984\n\t"
"v_mfma_f32_16x16x32_bf16 v[140:143], a[140:143], a[172:175], v[140:143]\n\t"
"buffer_load_dwordx4 a[136:139], v218, s[84:87], s78 offen offset:0 sc1\n\t"
"v_mfma_f32_16x16x32_bf16 v[212:215], a[180:183], a[220:223], 0\n\t"
"ds_read_b64_tr_b16 a[180:181], v249 offset:41472\n\t"
"v_mfma_f32_16x16x32_bf16 v[124:127], a[128:131], a[156:159], v[124:127]\n\t"
"ds_read_b64_tr_b16 a[182:183], v249 offset:41984\n\t"
"v_cvt_pk_bf16_f32 v208, v208, v209\n\t"
"v_mfma_f32_16x16x32_bf16 v[124:127], a[132:135], a[172:175], v[124:127]\n\t"
"buffer_load_dwordx4 a[140:143], v218, s[84:87], s78 offen offset:1024 sc1\n\t"
"v_mfma_f32_16x16x32_bf16 v[108:111], a[120:123], a[156:159], v[108:111]\n\t"
"ds_read_b128 a[156:159], v216 offset:19456\n\t"
"v_cvt_pk_bf16_f32 v209, v210, v211\n\t"
"v_mfma_f32_16x16x32_bf16 v[108:111], a[124:127], a[172:175], v[108:111]\n\t"
"buffer_load_dwordx4 a[132:135], v235, s[84:87], s78 offen offset:3072 sc1\n\t"
"v_mfma_f32_16x16x32_bf16 v[192:195], a[184:187], a[220:223], 0\n\t"
"s_waitcnt vmcnt(21) lgkmcnt(6)\n\t"
"ds_read_b64_tr_b16 a[184:185], v250 offset:43776\n\t"
"v_cvt_pk_bf16_f32 v210, v212, v213\n\t"
"v_mfma_f32_16x16x32_bf16 v[48:51], a[48:51], a[144:147], v[48:51]\n\t"
"ds_read_b64_tr_b16 a[186:187], v250 offset:44288\n\t"
"v_cvt_pk_bf16_f32 v211, v214, v215\n\t"
"v_mfma_f32_16x16x32_bf16 v[48:51], a[52:55], a[160:163], v[48:51]\n\t"
"buffer_load_dwordx4 a[128:131], v235, s[84:87], s78 offen offset:2048 sc1\n\t"
"v_mfma_f32_16x16x32_bf16 v[196:199], a[188:191], a[220:223], 0\n\t"
"ds_write_b128 v217, v[208:211] offset:0\n\t"
"v_pk_add_f32 v[96:97], v[0:1], v[96:97] neg_lo:[0,1] neg_hi:[0,1]\n\t"
"v_pk_add_f32 v[98:99], v[2:3], v[98:99] neg_lo:[0,1] neg_hi:[0,1]\n\t"
"v_pk_fma_f32 v[0:1], v[96:97], s[90:91], v[0:1] neg_lo:[1,0,0] neg_hi:[1,0,0]\n\t"
"v_pk_fma_f32 v[2:3], v[98:99], s[90:91], v[2:3] neg_lo:[1,0,0] neg_hi:[1,0,0]\n\t"
"v_pk_add_f32 v[112:113], v[16:17], v[112:113] neg_lo:[0,1] neg_hi:[0,1]\n\t"
"v_pk_add_f32 v[114:115], v[18:19], v[114:115] neg_lo:[0,1] neg_hi:[0,1]\n\t"
"v_pk_fma_f32 v[16:17], v[112:113], s[90:91], v[16:17] neg_lo:[1,0,0] neg_hi:[1,0,0]\n\t"
"v_pk_fma_f32 v[18:19], v[114:115], s[90:91], v[18:19] neg_lo:[1,0,0] neg_hi:[1,0,0]\n\t"
"v_pk_add_f32 v[128:129], v[32:33], v[128:129] neg_lo:[0,1] neg_hi:[0,1]\n\t"
"v_pk_add_f32 v[130:131], v[34:35], v[130:131] neg_lo:[0,1] neg_hi:[0,1]\n\t"
"v_pk_fma_f32 v[32:33], v[128:129], s[90:91], v[32:33] neg_lo:[1,0,0] neg_hi:[1,0,0]\n\t"
"v_pk_fma_f32 v[34:35], v[130:131], s[90:91], v[34:35] neg_lo:[1,0,0] neg_hi:[1,0,0]\n\t"
"v_pk_add_f32 v[132:133], v[36:37], v[132:133] neg_lo:[0,1] neg_hi:[0,1]\n\t"
"v_pk_add_f32 v[134:135], v[38:39], v[134:135] neg_lo:[0,1] neg_hi:[0,1]\n\t"
"v_pk_fma_f32 v[36:37], v[132:133], s[90:91], v[36:37] neg_lo:[1,0,0] neg_hi:[1,0,0]\n\t"
"v_pk_fma_f32 v[38:39], v[134:135], s[90:91], v[38:39] neg_lo:[1,0,0] neg_hi:[1,0,0]\n\t"
"v_pk_add_f32 v[116:117], v[20:21], v[116:117] neg_lo:[0,1] neg_hi:[0,1]\n\t"
"v_pk_add_f32 v[118:119], v[22:23], v[118:119] neg_lo:[0,1] neg_hi:[0,1]\n\t"
"v_pk_fma_f32 v[20:21], v[116:117], s[90:91], v[20:21] neg_lo:[1,0,0] neg_hi:[1,0,0]\n\t"
"v_pk_fma_f32 v[22:23], v[118:119], s[90:91], v[22:23] neg_lo:[1,0,0] neg_hi:[1,0,0]\n\t"
"v_pk_add_f32 v[100:101], v[4:5], v[100:101] neg_lo:[0,1] neg_hi:[0,1]\n\t"
"v_pk_add_f32 v[102:103], v[6:7], v[102:103] neg_lo:[0,1] neg_hi:[0,1]\n\t"
"v_pk_fma_f32 v[4:5], v[100:101], s[90:91], v[4:5] neg_lo:[1,0,0] neg_hi:[1,0,0]\n\t"
"v_pk_fma_f32 v[6:7], v[102:103], s[90:91], v[6:7] neg_lo:[1,0,0] neg_hi:[1,0,0]\n\t"
"v_pk_add_f32 v[104:105], v[8:9], v[104:105] neg_lo:[0,1] neg_hi:[0,1]\n\t"
"v_pk_add_f32 v[106:107], v[10:11], v[106:107] neg_lo:[0,1] neg_hi:[0,1]\n\t"
"v_pk_fma_f32 v[8:9], v[104:105], s[90:91], v[8:9] neg_lo:[1,0,0] neg_hi:[1,0,0]\n\t"
"v_pk_fma_f32 v[10:11], v[106:107], s[90:91], v[10:11] neg_lo:[1,0,0] neg_hi:[1,0,0]\n\t"
"v_pk_add_f32 v[120:121], v[24:25], v[120:121] neg_lo:[0,1] neg_hi:[0,1]\n\t"
"v_pk_add_f32 v[122:123], v[26:27], v[122:123] neg_lo:[0,1] neg_hi:[0,1]\n\t"
"v_pk_fma_f32 v[24:25], v[120:121], s[90:91], v[24:25] neg_lo:[1,0,0] neg_hi:[1,0,0]\n\t"
"v_pk_fma_f32 v[26:27], v[122:123], s[90:91], v[26:27] neg_lo:[1,0,0] neg_hi:[1,0,0]\n\t"
"v_pk_add_f32 v[136:137], v[40:41], v[136:137] neg_lo:[0,1] neg_hi:[0,1]\n\t"
"v_pk_add_f32 v[138:139], v[42:43], v[138:139] neg_lo:[0,1] neg_hi:[0,1]\n\t"
"v_pk_fma_f32 v[40:41], v[136:137], s[90:91], v[40:41] neg_lo:[1,0,0] neg_hi:[1,0,0]\n\t"
"v_pk_fma_f32 v[42:43], v[138:139], s[90:91], v[42:43] neg_lo:[1,0,0] neg_hi:[1,0,0]\n\t"
"v_pk_add_f32 v[140:141], v[44:45], v[140:141] neg_lo:[0,1] neg_hi:[0,1]\n\t"
"v_pk_add_f32 v[142:143], v[46:47], v[142:143] neg_lo:[0,1] neg_hi:[0,1]\n\t"
"v_pk_fma_f32 v[44:45], v[140:141], s[90:91], v[44:45] neg_lo:[1,0,0] neg_hi:[1,0,0]\n\t"
"v_pk_fma_f32 v[46:47], v[142:143], s[90:91], v[46:47] neg_lo:[1,0,0] neg_hi:[1,0,0]\n\t"
"v_pk_add_f32 v[124:125], v[28:29], v[124:125] neg_lo:[0,1] neg_hi:[0,1]\n\t"
"v_pk_add_f32 v[126:127], v[30:31], v[126:127] neg_lo:[0,1] neg_hi:[0,1]\n\t"
"v_pk_fma_f32 v[28:29], v[124:125], s[90:91], v[28:29] neg_lo:[1,0,0] neg_hi:[1,0,0]\n\t"
"v_pk_fma_f32 v[30:31], v[126:127], s[90:91], v[30:31] neg_lo:[1,0,0] neg_hi:[1,0,0]\n\t"
"v_pk_add_f32 v[108:109], v[12:13], v[108:109] neg_lo:[0,1] neg_hi:[0,1]\n\t"
"v_pk_add_f32 v[110:111], v[14:15], v[110:111] neg_lo:[0,1] neg_hi:[0,1]\n\t"
"v_pk_fma_f32 v[12:13], v[108:109], s[90:91], v[12:13] neg_lo:[1,0,0] neg_hi:[1,0,0]\n\t"
"v_pk_fma_f32 v[14:15], v[110:111], s[90:91], v[14:15] neg_lo:[1,0,0] neg_hi:[1,0,0]\n\t"
"v_mfma_f32_16x16x32_bf16 v[64:67], a[56:59], a[144:147], v[64:67]\n\t"
"buffer_load_dwordx4 a[120:123], v235, s[84:87], s78 offen offset:0 sc1\n\t"
"v_mfma_f32_16x16x32_bf16 v[64:67], a[60:63], a[160:163], v[64:67]\n\t"
"v_cvt_pk_bf16_f32 v192, v192, v193\n\t"
"v_cvt_pk_bf16_f32 v193, v194, v195\n\t"
"v_mfma_f32_16x16x32_bf16 v[80:83], a[64:67], a[144:147], v[80:83]\n\t"
"buffer_load_dwordx4 a[124:127], v235, s[84:87], s78 offen offset:1024 sc1\n\t"
"v_mfma_f32_16x16x32_bf16 v[80:83], a[68:71], a[160:163], v[80:83]\n\t"
"s_nop 0\n\t"
"ds_read_b64_tr_b16 a[188:189], v251 offset:43776\n\t"
"ds_read_b64_tr_b16 a[190:191], v251 offset:44288\n\t"
"v_mfma_f32_16x16x32_bf16 v[200:203], a[192:195], a[220:223], 0\n\t"
"ds_read_b128 a[172:175], v216 offset:27648\n\t"
"v_cvt_pk_bf16_f32 v194, v196, v197\n\t"
"v_mfma_f32_16x16x32_bf16 v[84:87], a[64:67], a[148:151], v[84:87]\n\t"
"ds_read_b64_tr_b16 a[192:193], v250 offset:41472\n\t"
"v_mfma_f32_16x16x32_bf16 v[84:87], a[68:71], a[164:167], v[84:87]\n\t"
"ds_read_b64_tr_b16 a[194:195], v250 offset:41984\n\t"
"v_mfma_f32_16x16x32_bf16 v[68:71], a[56:59], a[148:151], v[68:71]\n\t"
"ds_read_b128 a[144:147], v216 offset:20480\n\t"
"v_cvt_pk_bf16_f32 v195, v198, v199\n\t"
"v_mfma_f32_16x16x32_bf16 v[68:71], a[60:63], a[164:167], v[68:71]\n\t"
"ds_read_b128 a[160:163], v216 offset:28672\n\t"
"v_cvt_pk_bf16_f32 v200, v200, v201\n\t"
"v_mfma_f32_16x16x32_bf16 v[204:207], a[196:199], a[220:223], 0\n\t"
"ds_read_b64_tr_b16 a[196:197], v251 offset:41472\n\t"
"v_cvt_pk_bf16_f32 v201, v202, v203\n\t"
"v_mfma_f32_16x16x32_bf16 v[52:55], a[48:51], a[148:151], v[52:55]\n\t"
"v_mfma_f32_16x16x32_bf16 v[52:55], a[52:55], a[164:167], v[52:55]\n\t"
"ds_write_b128 v219, v[192:195] offset:0\n\t"
"v_mfma_f32_16x16x32_bf16 v[208:211], a[200:203], a[220:223], 0\n\t"
"s_waitcnt lgkmcnt(9)\n\t"
"v_mfma_f32_16x16x32_bf16 v[56:59], a[48:51], a[152:155], v[56:59]\n\t"
"v_cvt_pk_bf16_f32 v202, v204, v205\n\t"
"v_mfma_f32_16x16x32_bf16 v[56:59], a[52:55], a[168:171], v[56:59]\n\t"
"v_cvt_pk_bf16_f32 v203, v206, v207\n\t"
"v_mfma_f32_16x16x32_bf16 v[72:75], a[56:59], a[152:155], v[72:75]\n\t"
"ds_read_b128 a[148:151], v216 offset:21504\n\t"
"v_mfma_f32_16x16x32_bf16 v[72:75], a[60:63], a[168:171], v[72:75]\n\t"
"ds_read_b128 a[164:167], v216 offset:29696\n\t"
"v_cvt_pk_bf16_f32 v208, v208, v209\n\t"
"v_mfma_f32_16x16x32_bf16 v[212:215], a[204:207], a[220:223], 0\n\t"
"ds_read_b64_tr_b16 a[198:199], v251 offset:41984\n\t"
"v_mfma_f32_16x16x32_bf16 v[88:91], a[64:67], a[152:155], v[88:91]\n\t"
"ds_read_b128 a[152:155], v216 offset:22528\n\t"
"v_cvt_pk_bf16_f32 v209, v210, v211\n\t"
"v_mfma_f32_16x16x32_bf16 v[88:91], a[68:71], a[168:171], v[88:91]\n\t"
"s_waitcnt lgkmcnt(5)\n\t"
"ds_read_b64_tr_b16 a[200:201], v248 offset:43776\n\t"
"v_mfma_f32_16x16x32_bf16 v[92:95], a[64:67], a[156:159], v[92:95]\n\t"
"v_mfma_f32_16x16x32_bf16 v[92:95], a[68:71], a[172:175], v[92:95]\n\t"
"ds_write_b128 v217, v[200:203] offset:8192\n\t"
"v_mfma_f32_16x16x32_bf16 v[76:79], a[56:59], a[156:159], v[76:79]\n\t"
"v_cvt_pk_bf16_f32 v210, v212, v213\n\t"
"v_cvt_pk_bf16_f32 v211, v214, v215\n\t"
"v_mfma_f32_16x16x32_bf16 v[76:79], a[60:63], a[172:175], v[76:79]\n\t"
"v_mfma_f32_16x16x32_bf16 v[60:63], a[48:51], a[156:159], v[60:63]\n\t"
"v_mfma_f32_16x16x32_bf16 v[60:63], a[52:55], a[172:175], v[60:63]\n\t"
"s_waitcnt vmcnt(17) lgkmcnt(5)\n\t"
"ds_write_b128 v219, v[208:211] offset:8192\n\t"
"v_mfma_f32_16x16x32_bf16 v[144:147], a[72:75], a[144:147], v[144:147]\n\t"
"v_mfma_f32_16x16x32_bf16 v[144:147], a[76:79], a[160:163], v[144:147]\n\t"
"v_mfma_f32_16x16x32_bf16 v[160:163], a[80:83], a[144:147], v[160:163]\n\t"
"v_mfma_f32_16x16x32_bf16 v[160:163], a[84:87], a[160:163], v[160:163]\n\t"
"ds_read_b128 a[172:175], v216 offset:31744\n\t"
"v_mfma_f32_16x16x32_bf16 v[176:179], a[88:91], a[144:147], v[176:179]\n\t"
"ds_read_b128 a[168:171], v216 offset:30720\n\t"
"v_mfma_f32_16x16x32_bf16 v[176:179], a[92:95], a[160:163], v[176:179]\n\t"
"s_waitcnt lgkmcnt(3)\n\t"
"ds_read_b64_tr_b16 a[202:203], v248 offset:44288\n\t"
"v_mfma_f32_16x16x32_bf16 v[180:183], a[88:91], a[148:151], v[180:183]\n\t"
"ds_read_b64_tr_b16 a[204:205], v249 offset:43776\n\t"
"v_mfma_f32_16x16x32_bf16 v[180:183], a[92:95], a[164:167], v[180:183]\n\t"
"ds_read_b64_tr_b16 a[206:207], v249 offset:44288\n\t"
"v_mfma_f32_16x16x32_bf16 v[164:167], a[80:83], a[148:151], v[164:167]\n\t"
"ds_read_b128 a[156:159], v216 offset:23552\n\t"
"v_mfma_f32_16x16x32_bf16 v[164:167], a[84:87], a[164:167], v[164:167]\n\t"
"v_mfma_f32_16x16x32_bf16 v[148:151], a[72:75], a[148:151], v[148:151]\n\t"
"v_mfma_f32_16x16x32_bf16 v[148:151], a[76:79], a[164:167], v[148:151]\n\t"
"s_waitcnt lgkmcnt(0)\n\t"
"s_barrier\n\t"
"v_mfma_f32_16x16x32_bf16 v[152:155], a[72:75], a[152:155], v[152:155]\n\t"
"s_add_u32 m0, s26, 103936\n\t"
"ds_read_b128 a[144:147], v216 offset:0\n\t"
"ds_read_b128 a[148:151], v216 offset:1024\n\t"
"v_mfma_f32_16x16x32_bf16 v[152:155], a[76:79], a[168:171], v[152:155]\n\t"
"buffer_load_dwordx4 v236, s[12:15], s22 offen offset:0 nt lds\n\t"
"v_mfma_f32_16x16x32_bf16 v[168:171], a[80:83], a[152:155], v[168:171]\n\t"
"ds_read_b128 a[160:163], v216 offset:8192\n\t"
"v_mfma_f32_16x16x32_bf16 v[168:171], a[84:87], a[168:171], v[168:171]\n\t"
"ds_read_b128 a[164:167], v216 offset:9216\n\t"
"v_mfma_f32_16x16x32_bf16 v[184:187], a[88:91], a[152:155], v[184:187]\n\t"
"ds_read_b128 a[152:155], v216 offset:2048\n\t"
"v_mfma_f32_16x16x32_bf16 v[184:187], a[92:95], a[168:171], v[184:187]\n\t"
"ds_read_b128 a[168:171], v216 offset:10240\n\t"
"v_mfma_f32_16x16x32_bf16 v[192:195], a[176:179], a[252:255], 0\n\t"
"ds_read_b64_tr_b16 a[176:177], v248 offset:0\n\t"
"v_mfma_f32_16x16x32_bf16 v[188:191], a[88:91], a[156:159], v[188:191]\n\t"
"ds_read_b64_tr_b16 a[178:179], v248 offset:512\n\t"
"v_mfma_f32_16x16x32_bf16 v[188:191], a[92:95], a[172:175], v[188:191]\n\t"
"buffer_load_dwordx4 v237, s[12:15], s22 offen offset:1024 nt lds\n\t"
"v_mfma_f32_16x16x32_bf16 v[196:199], a[180:183], a[252:255], 0\n\t"
"ds_read_b64_tr_b16 a[180:181], v249 offset:0\n\t"
"v_mfma_f32_16x16x32_bf16 v[172:175], a[80:83], a[156:159], v[172:175]\n\t"
"ds_read_b64_tr_b16 a[182:183], v249 offset:512\n\t"
"v_cvt_pk_bf16_f32 v192, v192, v193\n\t"
"v_mfma_f32_16x16x32_bf16 v[172:175], a[84:87], a[172:175], v[172:175]\n\t"
"v_mfma_f32_16x16x32_bf16 v[156:159], a[72:75], a[156:159], v[156:159]\n\t"
"ds_read_b128 a[156:159], v216 offset:3072\n\t"
"v_cvt_pk_bf16_f32 v193, v194, v195\n\t"
"v_mfma_f32_16x16x32_bf16 v[156:159], a[76:79], a[172:175], v[156:159]\n\t"
"buffer_load_dwordx4 v238, s[12:15], s22 offen offset:2048 nt lds\n\t"
"v_mfma_f32_16x16x32_bf16 v[200:203], a[184:187], a[252:255], 0\n\t"
"s_waitcnt vmcnt(9) lgkmcnt(6)\n\t"
"ds_read_b128 a[172:175], v216 offset:11264\n\t"
"v_cvt_pk_bf16_f32 v194, v196, v197\n\t"
"v_mfma_f32_16x16x32_bf16 v[48:51], a[96:99], a[144:147], v[48:51]\n\t"
"ds_read_b64_tr_b16 a[184:185], v250 offset:2304\n\t"
"v_cvt_pk_bf16_f32 v195, v198, v199\n\t"
"v_mfma_f32_16x16x32_bf16 v[48:51], a[100:103], a[160:163], v[48:51]\n\t"
"ds_read_b64_tr_b16 a[186:187], v250 offset:2816\n\t"
"v_mfma_f32_16x16x32_bf16 v[204:207], a[188:191], a[252:255], 0\n\t"
"ds_read_b64_tr_b16 a[188:189], v251 offset:2304\n\t"
"v_mfma_f32_16x16x32_bf16 v[64:67], a[104:107], a[144:147], v[64:67]\n\t"
"ds_read_b64_tr_b16 a[190:191], v251 offset:2816\n\t"
"v_cvt_pk_bf16_f32 v200, v200, v201\n\t"
"v_mfma_f32_16x16x32_bf16 v[64:67], a[108:111], a[160:163], v[64:67]\n\t"
"buffer_load_dwordx4 v239, s[12:15], s22 offen offset:3072 nt lds\n\t"
"v_mfma_f32_16x16x32_bf16 v[80:83], a[112:115], a[144:147], v[80:83]\n\t"
"ds_read_b128 a[144:147], v216 offset:4096\n\t"
"v_cvt_pk_bf16_f32 v201, v202, v203\n\t"
"v_mfma_f32_16x16x32_bf16 v[80:83], a[116:119], a[160:163], v[80:83]\n\t"
"ds_read_b128 a[160:163], v216 offset:12288\n\t"
"v_cvt_pk_bf16_f32 v202, v204, v205\n\t"
"v_mfma_f32_16x16x32_bf16 v[208:211], a[192:195], a[252:255], 0\n\t"
"ds_read_b64_tr_b16 a[192:193], v250 offset:0\n\t"
"v_mfma_f32_16x16x32_bf16 v[84:87], a[112:115], a[148:151], v[84:87]\n\t"
"v_cvt_pk_bf16_f32 v203, v206, v207\n\t"
"v_mfma_f32_16x16x32_bf16 v[84:87], a[116:119], a[164:167], v[84:87]\n\t"
"ds_write_b128 v217, v[192:195] offset:16384\n\t"
"v_mfma_f32_16x16x32_bf16 v[68:71], a[104:107], a[148:151], v[68:71]\n\t"
"v_mfma_f32_16x16x32_bf16 v[68:71], a[108:111], a[164:167], v[68:71]\n\t"
"s_mov_b64 exec, s[36:37]\n\t"
"buffer_load_dwordx4 v240, s[12:15], s22 offen offset:3584 nt lds\n\t"
"s_mov_b64 exec, -1\n\t"
"v_mfma_f32_16x16x32_bf16 v[212:215], a[196:199], a[252:255], 0\n\t"
"ds_write_b128 v219, v[200:203] offset:16384\n\t"
"v_mfma_f32_16x16x32_bf16 v[52:55], a[96:99], a[148:151], v[52:55]\n\t"
"v_cvt_pk_bf16_f32 v208, v208, v209\n\t"
"v_cvt_pk_bf16_f32 v209, v210, v211\n\t"
"v_mfma_f32_16x16x32_bf16 v[52:55], a[100:103], a[164:167], v[52:55]\n\t"
"v_mfma_f32_16x16x32_bf16 v[192:195], a[200:203], a[252:255], 0\n\t"
"s_waitcnt lgkmcnt(4)\n\t"
"v_mfma_f32_16x16x32_bf16 v[56:59], a[96:99], a[152:155], v[56:59]\n\t"
"ds_read_b64_tr_b16 a[194:195], v250 offset:512\n\t"
"v_cvt_pk_bf16_f32 v210, v212, v213\n\t"
"v_mfma_f32_16x16x32_bf16 v[56:59], a[100:103], a[168:171], v[56:59]\n\t"
"ds_read_b64_tr_b16 a[196:197], v251 offset:0\n\t"
"v_mfma_f32_16x16x32_bf16 v[72:75], a[104:107], a[152:155], v[72:75]\n\t"
"ds_read_b64_tr_b16 a[198:199], v251 offset:512\n\t"
"v_mfma_f32_16x16x32_bf16 v[72:75], a[108:111], a[168:171], v[72:75]\n\t"
"ds_read_b128 a[164:167], v216 offset:13312\n\t"
"v_cvt_pk_bf16_f32 v192, v192, v193\n\t"
"v_mfma_f32_16x16x32_bf16 v[196:199], a[204:207], a[252:255], 0\n\t"
"ds_read_b128 a[148:151], v216 offset:5120\n\t"
"v_cvt_pk_bf16_f32 v193, v194, v195\n\t"
"v_mfma_f32_16x16x32_bf16 v[88:91], a[112:115], a[152:155], v[88:91]\n\t"
"ds_read_b64_tr_b16 a[200:201], v248 offset:2304\n\t"
"v_cvt_pk_bf16_f32 v211, v214, v215\n\t"
"v_mfma_f32_16x16x32_bf16 v[88:91], a[116:119], a[168:171], v[88:91]\n\t"
"ds_read_b64_tr_b16 a[202:203], v248 offset:2816\n\t"
"v_mfma_f32_16x16x32_bf16 v[92:95], a[112:115], a[156:159], v[92:95]\n\t"
"ds_read_b64_tr_b16 a[204:205], v249 offset:2304\n\t"
"v_mfma_f32_16x16x32_bf16 v[92:95], a[116:119], a[172:175], v[92:95]\n\t"
"ds_read_b128 a[152:155], v216 offset:6144\n\t"
"v_mfma_f32_16x16x32_bf16 v[76:79], a[104:107], a[156:159], v[76:79]\n\t"
"v_cvt_pk_bf16_f32 v194, v196, v197\n\t"
"v_cvt_pk_bf16_f32 v195, v198, v199\n\t"
"v_mfma_f32_16x16x32_bf16 v[76:79], a[108:111], a[172:175], v[76:79]\n\t"
"ds_write_b128 v217, v[208:211] offset:24576\n\t"
"v_mfma_f32_16x16x32_bf16 v[60:63], a[96:99], a[156:159], v[60:63]\n\t"
"v_mfma_f32_16x16x32_bf16 v[60:63], a[100:103], a[172:175], v[60:63]\n\t"
"s_waitcnt vmcnt(5) lgkmcnt(7)\n\t"
"v_mfma_f32_16x16x32_bf16 v[144:147], a[120:123], a[144:147], v[144:147]\n\t"
"v_mfma_f32_16x16x32_bf16 v[144:147], a[124:127], a[160:163], v[144:147]\n\t"
"ds_write_b128 v219, v[192:195] offset:24576\n\t"
"v_mfma_f32_16x16x32_bf16 v[160:163], a[128:131], a[144:147], v[160:163]\n\t"
"v_mfma_f32_16x16x32_bf16 v[160:163], a[132:135], a[160:163], v[160:163]\n\t"
"v_mfma_f32_16x16x32_bf16 v[176:179], a[136:139], a[144:147], v[176:179]\n\t"
"v_mfma_f32_16x16x32_bf16 v[176:179], a[140:143], a[160:163], v[176:179]\n\t"
"s_waitcnt lgkmcnt(2)\n\t"
"ds_read_b128 a[168:171], v216 offset:14336\n\t"
"v_mfma_f32_16x16x32_bf16 v[180:183], a[136:139], a[148:151], v[180:183]\n\t"
"ds_read_b64_tr_b16 a[206:207], v249 offset:2816\n\t"
"v_mfma_f32_16x16x32_bf16 v[180:183], a[140:143], a[164:167], v[180:183]\n\t"
"ds_read_b128 a[156:159], v216 offset:7168\n\t"
"v_mfma_f32_16x16x32_bf16 v[164:167], a[128:131], a[148:151], v[164:167]\n\t"
"ds_read_b128 a[172:175], v216 offset:15360\n\t"
"v_mfma_f32_16x16x32_bf16 v[164:167], a[132:135], a[164:167], v[164:167]\n\t"
"v_mfma_f32_16x16x32_bf16 v[148:151], a[120:123], a[148:151], v[148:151]\n\t"
"v_mfma_f32_16x16x32_bf16 v[148:151], a[124:127], a[164:167], v[148:151]\n\t"
"s_waitcnt lgkmcnt(0)\n\t"
"s_barrier\n\t"
"v_mfma_f32_16x16x32_bf16 v[152:155], a[120:123], a[152:155], v[152:155]\n\t"
"ds_read_b128 a[144:147], v216 offset:16384\n\t"
"v_add_u32 v248, 32768, v226\n\t"
"v_mfma_f32_16x16x32_bf16 v[152:155], a[124:127], a[168:171], v[152:155]\n\t"
"ds_read_b128 a[160:163], v216 offset:24576\n\t"
"v_xor_b32 v249, 32, v248\n\t"
"v_mfma_f32_16x16x32_bf16 v[168:171], a[128:131], a[152:155], v[168:171]\n\t"
"ds_read_b128 a[148:151], v216 offset:17408\n\t"
"v_xor_b32 v250, 64, v248\n\t"
"v_mfma_f32_16x16x32_bf16 v[168:171], a[132:135], a[168:171], v[168:171]\n\t"
"ds_read_b128 a[164:167], v216 offset:25600\n\t"
"v_mfma_f32_16x16x32_bf16 v[184:187], a[136:139], a[152:155], v[184:187]\n\t"
"v_xor_b32 v251, 96, v248\n\t"
"v_mfma_f32_16x16x32_bf16 v[184:187], a[140:143], a[168:171], v[184:187]\n\t"
"ds_read_b128 a[152:155], v216 offset:18432\n\t"
"v_mfma_f32_16x16x32_bf16 v[200:203], a[176:179], a[252:255], 0\n\t"
"ds_read_b64_tr_b16 a[176:177], v248 offset:0\n\t"
"v_mfma_f32_16x16x32_bf16 v[188:191], a[136:139], a[156:159], v[188:191]\n\t"
"ds_read_b64_tr_b16 a[178:179], v248 offset:512\n\t"
"v_mfma_f32_16x16x32_bf16 v[188:191], a[140:143], a[172:175], v[188:191]\n\t"
"ds_read_b128 a[168:171], v216 offset:26624\n\t"
"v_mfma_f32_16x16x32_bf16 v[204:207], a[180:183], a[252:255], 0\n\t"
"ds_read_b64_tr_b16 a[180:181], v249 offset:0\n\t"
"v_mfma_f32_16x16x32_bf16 v[172:175], a[128:131], a[156:159], v[172:175]\n\t"
"ds_read_b64_tr_b16 a[182:183], v249 offset:512\n\t"
"v_cvt_pk_bf16_f32 v200, v200, v201\n\t"
"v_mfma_f32_16x16x32_bf16 v[172:175], a[132:135], a[172:175], v[172:175]\n\t"
"v_cvt_pk_bf16_f32 v201, v202, v203\n\t"
"v_mfma_f32_16x16x32_bf16 v[156:159], a[120:123], a[156:159], v[156:159]\n\t"
"ds_read_b128 a[156:159], v216 offset:19456\n\t"
"v_mfma_f32_16x16x32_bf16 v[156:159], a[124:127], a[172:175], v[156:159]\n\t"
"ds_read_b128 a[172:175], v216 offset:27648\n\t"
"v_cvt_pk_bf16_f32 v202, v204, v205\n\t"
"v_mfma_f32_16x16x32_bf16 v[208:211], a[184:187], a[252:255], 0\n\t"
"s_waitcnt lgkmcnt(8)\n\t"
"ds_read_b64_tr_b16 a[184:185], v250 offset:2304\n\t"
"v_mfma_f32_16x16x32_bf16 v[0:3], a[0:3], a[144:147], v[0:3]\n\t"
"ds_read_b64_tr_b16 a[186:187], v250 offset:2816\n\t"
"v_cvt_pk_bf16_f32 v203, v206, v207\n\t"
"v_mfma_f32_16x16x32_bf16 v[0:3], a[4:7], a[160:163], v[0:3]\n\t"
"v_mfma_f32_16x16x32_bf16 v[212:215], a[188:191], a[252:255], 0\n\t"
"ds_read_b64_tr_b16 a[188:189], v251 offset:2304\n\t"
"ds_write_b128 v217, v[200:203] offset:0\n\t"
"v_pk_fma_f32 v[48:49], v[144:145], s[90:91], v[48:49]\n\t"
"v_pk_fma_f32 v[50:51], v[146:147], s[90:91], v[50:51]\n\t"
"v_pk_add_f32 v[144:145], v[48:49], v[144:145] neg_lo:[0,1] neg_hi:[0,1]\n\t"
"v_pk_add_f32 v[146:147], v[50:51], v[146:147] neg_lo:[0,1] neg_hi:[0,1]\n\t"
"v_pk_fma_f32 v[64:65], v[160:161], s[90:91], v[64:65]\n\t"
"v_pk_fma_f32 v[66:67], v[162:163], s[90:91], v[66:67]\n\t"
"v_pk_add_f32 v[160:161], v[64:65], v[160:161] neg_lo:[0,1] neg_hi:[0,1]\n\t"
"v_pk_add_f32 v[162:163], v[66:67], v[162:163] neg_lo:[0,1] neg_hi:[0,1]\n\t"
"v_pk_fma_f32 v[80:81], v[176:177], s[90:91], v[80:81]\n\t"
"v_pk_fma_f32 v[82:83], v[178:179], s[90:91], v[82:83]\n\t"
"v_pk_add_f32 v[176:177], v[80:81], v[176:177] neg_lo:[0,1] neg_hi:[0,1]\n\t"
"v_pk_add_f32 v[178:179], v[82:83], v[178:179] neg_lo:[0,1] neg_hi:[0,1]\n\t"
"v_pk_fma_f32 v[84:85], v[180:181], s[90:91], v[84:85]\n\t"
"v_pk_fma_f32 v[86:87], v[182:183], s[90:91], v[86:87]\n\t"
"v_pk_add_f32 v[180:181], v[84:85], v[180:181] neg_lo:[0,1] neg_hi:[0,1]\n\t"
"v_pk_add_f32 v[182:183], v[86:87], v[182:183] neg_lo:[0,1] neg_hi:[0,1]\n\t"
"v_pk_fma_f32 v[68:69], v[164:165], s[90:91], v[68:69]\n\t"
"v_pk_fma_f32 v[70:71], v[166:167], s[90:91], v[70:71]\n\t"
"v_pk_add_f32 v[164:165], v[68:69], v[164:165] neg_lo:[0,1] neg_hi:[0,1]\n\t"
"v_pk_add_f32 v[166:167], v[70:71], v[166:167] neg_lo:[0,1] neg_hi:[0,1]\n\t"
"v_pk_fma_f32 v[52:53], v[148:149], s[90:91], v[52:53]\n\t"
"v_pk_fma_f32 v[54:55], v[150:151], s[90:91], v[54:55]\n\t"
"v_pk_add_f32 v[148:149], v[52:53], v[148:149] neg_lo:[0,1] neg_hi:[0,1]\n\t"
"v_pk_add_f32 v[150:151], v[54:55], v[150:151] neg_lo:[0,1] neg_hi:[0,1]\n\t"
"v_pk_fma_f32 v[56:57], v[152:153], s[90:91], v[56:57]\n\t"
"v_pk_fma_f32 v[58:59], v[154:155], s[90:91], v[58:59]\n\t"
"v_pk_add_f32 v[152:153], v[56:57], v[152:153] neg_lo:[0,1] neg_hi:[0,1]\n\t"
"v_pk_add_f32 v[154:155], v[58:59], v[154:155] neg_lo:[0,1] neg_hi:[0,1]\n\t"
"v_pk_fma_f32 v[72:73], v[168:169], s[90:91], v[72:73]\n\t"
"v_pk_fma_f32 v[74:75], v[170:171], s[90:91], v[74:75]\n\t"
"v_pk_add_f32 v[168:169], v[72:73], v[168:169] neg_lo:[0,1] neg_hi:[0,1]\n\t"
"v_pk_add_f32 v[170:171], v[74:75], v[170:171] neg_lo:[0,1] neg_hi:[0,1]\n\t"
"v_pk_fma_f32 v[88:89], v[184:185], s[90:91], v[88:89]\n\t"
"v_pk_fma_f32 v[90:91], v[186:187], s[90:91], v[90:91]\n\t"
"v_pk_add_f32 v[184:185], v[88:89], v[184:185] neg_lo:[0,1] neg_hi:[0,1]\n\t"
"v_pk_add_f32 v[186:187], v[90:91], v[186:187] neg_lo:[0,1] neg_hi:[0,1]\n\t"
"v_pk_fma_f32 v[92:93], v[188:189], s[90:91], v[92:93]\n\t"
"v_pk_fma_f32 v[94:95], v[190:191], s[90:91], v[94:95]\n\t"
"v_pk_add_f32 v[188:189], v[92:93], v[188:189] neg_lo:[0,1] neg_hi:[0,1]\n\t"
"v_pk_add_f32 v[190:191], v[94:95], v[190:191] neg_lo:[0,1] neg_hi:[0,1]\n\t"
"v_pk_fma_f32 v[76:77], v[172:173], s[90:91], v[76:77]\n\t"
"v_pk_fma_f32 v[78:79], v[174:175], s[90:91], v[78:79]\n\t"
"v_pk_add_f32 v[172:173], v[76:77], v[172:173] neg_lo:[0,1] neg_hi:[0,1]\n\t"
"v_pk_add_f32 v[174:175], v[78:79], v[174:175] neg_lo:[0,1] neg_hi:[0,1]\n\t"
"v_pk_fma_f32 v[60:61], v[156:157], s[90:91], v[60:61]\n\t"
"v_pk_fma_f32 v[62:63], v[158:159], s[90:91], v[62:63]\n\t"
"v_pk_add_f32 v[156:157], v[60:61], v[156:157] neg_lo:[0,1] neg_hi:[0,1]\n\t"
"v_pk_add_f32 v[158:159], v[62:63], v[158:159] neg_lo:[0,1] neg_hi:[0,1]\n\t"
"v_cvt_pk_bf16_f32 v52, v52, v53\n\t"
"v_cvt_pk_bf16_f32 v53, v54, v55\n\t"
"v_cvt_pk_bf16_f32 v68, v68, v69\n\t"
"v_cvt_pk_bf16_f32 v72, v72, v73\n\t"
"v_cvt_pk_bf16_f32 v92, v92, v93\n\t"
"v_mfma_f32_16x16x32_bf16 v[16:19], a[8:11], a[144:147], v[16:19]\n\t"
"v_cvt_pk_bf16_f32 v48, v48, v49\n\t"
"v_cvt_pk_bf16_f32 v84, v84, v85\n\t"
"v_mfma_f32_16x16x32_bf16 v[16:19], a[12:15], a[160:163], v[16:19]\n\t"
"v_cvt_pk_bf16_f32 v208, v208, v209\n\t"
"v_cvt_pk_bf16_f32 v64, v64, v65\n\t"
"v_cvt_pk_bf16_f32 v69, v70, v71\n\t"
"v_mfma_f32_16x16x32_bf16 v[32:35], a[16:19], a[144:147], v[32:35]\n\t"
"v_cvt_pk_bf16_f32 v49, v50, v51\n\t"
"v_cvt_pk_bf16_f32 v65, v66, v67\n\t"
"v_mfma_f32_16x16x32_bf16 v[32:35], a[20:23], a[160:163], v[32:35]\n\t"
"ds_read_b128 a[160:163], v216 offset:28672\n\t"
"ds_write2st64_b64 v228, v[48:49], v[52:53] offset0:0 offset1:13\n\t"
"v_mfma_f32_16x16x32_bf16 v[192:195], a[192:195], a[252:255], 0\n\t"
"v_cvt_pk_bf16_f32 v209, v210, v211\n\t"
"v_cvt_pk_bf16_f32 v88, v88, v89\n\t"
"v_mfma_f32_16x16x32_bf16 v[36:39], a[16:19], a[148:151], v[36:39]\n\t"
"v_cvt_pk_bf16_f32 v80, v80, v81\n\t"
"v_cvt_pk_bf16_f32 v60, v60, v61\n\t"
"v_mfma_f32_16x16x32_bf16 v[36:39], a[20:23], a[164:167], v[36:39]\n\t"
"ds_write2st64_b64 v246, v[64:65], v[68:69] offset0:0 offset1:13\n\t"
"v_mfma_f32_16x16x32_bf16 v[20:23], a[8:11], a[148:151], v[20:23]\n\t"
"v_cvt_pk_bf16_f32 v210, v212, v213\n\t"
"v_cvt_pk_bf16_f32 v93, v94, v95\n\t"
"v_mfma_f32_16x16x32_bf16 v[20:23], a[12:15], a[164:167], v[20:23]\n\t"
"v_cvt_pk_bf16_f32 v192, v192, v193\n\t"
"v_cvt_pk_bf16_f32 v85, v86, v87\n\t"
"v_mfma_f32_16x16x32_bf16 v[196:199], a[196:199], a[252:255], 0\n\t"
"v_cvt_pk_bf16_f32 v56, v56, v57\n\t"
"v_cvt_pk_bf16_f32 v57, v58, v59\n\t"
"v_mfma_f32_16x16x32_bf16 v[4:7], a[0:3], a[148:151], v[4:7]\n\t"
"v_cvt_pk_bf16_f32 v211, v214, v215\n\t"
"v_cvt_pk_bf16_f32 v81, v82, v83\n\t"
"v_mfma_f32_16x16x32_bf16 v[4:7], a[4:7], a[164:167], v[4:7]\n\t"
"s_nop 0\n\t"
"ds_read_b128 a[144:147], v216 offset:20480\n\t"
"ds_read_b128 a[148:151], v216 offset:21504\n\t"
"ds_read_b128 a[164:167], v216 offset:29696\n\t"
"ds_read_b64_tr_b16 a[190:191], v251 offset:2816\n\t"
"ds_read_b64_tr_b16 a[192:193], v250 offset:0\n\t"
"ds_read_b64_tr_b16 a[194:195], v250 offset:512\n\t"
"ds_write2st64_b64 v247, v[80:81], v[84:85] offset0:0 offset1:13\n\t"
"v_mfma_f32_16x16x32_bf16 v[200:203], a[200:203], a[252:255], 0\n\t"
"s_waitcnt lgkmcnt(10)\n\t"
"v_cvt_pk_bf16_f32 v193, v194, v195\n\t"
"v_cvt_pk_bf16_f32 v89, v90, v91\n\t"
"v_mfma_f32_16x16x32_bf16 v[8:11], a[0:3], a[152:155], v[8:11]\n\t"
"v_cvt_pk_bf16_f32 v61, v62, v63\n\t"
"v_cvt_pk_bf16_f32 v73, v74, v75\n\t"
"v_mfma_f32_16x16x32_bf16 v[8:11], a[4:7], a[168:171], v[8:11]\n\t"
"ds_write_b128 v219, v[208:211] offset:0\n\t"
"v_mfma_f32_16x16x32_bf16 v[24:27], a[8:11], a[152:155], v[24:27]\n\t"
"v_cvt_pk_bf16_f32 v194, v196, v197\n\t"
"v_cvt_pk_bf16_f32 v76, v76, v77\n\t"
"v_mfma_f32_16x16x32_bf16 v[24:27], a[12:15], a[168:171], v[24:27]\n\t"
"v_cvt_pk_bf16_f32 v195, v198, v199\n\t"
"v_cvt_pk_bf16_f32 v77, v78, v79\n\t"
"v_mfma_f32_16x16x32_bf16 v[204:207], a[204:207], a[252:255], 0\n\t"
"ds_write2st64_b64 v228, v[56:57], v[60:61] offset0:26 offset1:39\n\t"
"v_mfma_f32_16x16x32_bf16 v[40:43], a[16:19], a[152:155], v[40:43]\n\t"
"v_cvt_pk_bf16_f32 v200, v200, v201\n\t"
"v_cvt_pk_bf16_f32 v201, v202, v203\n\t"
"v_mfma_f32_16x16x32_bf16 v[40:43], a[20:23], a[168:171], v[40:43]\n\t"
"ds_write2st64_b64 v246, v[72:73], v[76:77] offset0:26 offset1:39\n\t"
"v_mfma_f32_16x16x32_bf16 v[44:47], a[16:19], a[156:159], v[44:47]\n\t"
"buffer_load_dwordx4 a[16:19], v235, s[8:11], s59 offen offset:0 sc1\n\t"
"v_mfma_f32_16x16x32_bf16 v[44:47], a[20:23], a[172:175], v[44:47]\n\t"
"ds_write2st64_b64 v247, v[88:89], v[92:93] offset0:26 offset1:39\n\t"
"v_mfma_f32_16x16x32_bf16 v[28:31], a[8:11], a[156:159], v[28:31]\n\t"
"buffer_load_dwordx4 a[20:23], v235, s[8:11], s59 offen offset:1024 sc1\n\t"
"v_mfma_f32_16x16x32_bf16 v[28:31], a[12:15], a[172:175], v[28:31]\n\t"
"ds_write_b128 v217, v[192:195] offset:8192\n\t"
"v_mfma_f32_16x16x32_bf16 v[12:15], a[0:3], a[156:159], v[12:15]\n\t"
"buffer_load_dwordx4 a[12:15], v218, s[8:11], s58 offen offset:3072 sc1\n\t"
"v_mfma_f32_16x16x32_bf16 v[12:15], a[4:7], a[172:175], v[12:15]\n\t"
"s_waitcnt lgkmcnt(4)\n\t"
"v_cvt_pk_bf16_f32 v202, v204, v205\n\t"
"v_cvt_pk_bf16_f32 v203, v206, v207\n\t"
"v_mfma_f32_16x16x32_bf16 v[96:99], a[24:27], a[144:147], v[96:99]\n\t"
"buffer_load_dwordx4 a[8:11], v218, s[8:11], s58 offen offset:2048 sc1\n\t"
"v_mfma_f32_16x16x32_bf16 v[96:99], a[28:31], a[160:163], v[96:99]\n\t"
"ds_write_b128 v219, v[200:203] offset:8192\n\t"
"v_mfma_f32_16x16x32_bf16 v[112:115], a[32:35], a[144:147], v[112:115]\n\t"
"buffer_load_dwordx4 a[0:3], v218, s[8:11], s58 offen offset:0 sc1\n\t"
"v_mfma_f32_16x16x32_bf16 v[112:115], a[36:39], a[160:163], v[112:115]\n\t"
"s_nop 0\n\t"
"ds_read_b64_tr_b16 a[196:197], v251 offset:0\n\t"
"ds_read_b64_tr_b16 a[198:199], v251 offset:512\n\t"
"v_mfma_f32_16x16x32_bf16 v[128:131], a[40:43], a[144:147], v[128:131]\n\t"
"buffer_load_dwordx4 a[4:7], v218, s[8:11], s58 offen offset:1024 sc1\n\t"
"v_mfma_f32_16x16x32_bf16 v[128:131], a[44:47], a[160:163], v[128:131]\n\t"
"s_nop 0\n\t"
"ds_read_b128 a[152:155], v216 offset:22528\n\t"
"ds_read_b128 a[168:171], v216 offset:30720\n\t"
"v_mfma_f32_16x16x32_bf16 v[132:135], a[40:43], a[148:151], v[132:135]\n\t"
"s_nop 0\n\t"
"ds_read_b64_tr_b16 a[200:201], v248 offset:2304\n\t"
"ds_read_b64_tr_b16 a[202:203], v248 offset:2816\n\t"
"v_mfma_f32_16x16x32_bf16 v[132:135], a[44:47], a[164:167], v[132:135]\n\t"
"s_nop 0\n\t"
"ds_read_b64_tr_b16 a[204:205], v249 offset:2304\n\t"
"ds_read_b64_tr_b16 a[206:207], v249 offset:2816\n\t"
"v_mfma_f32_16x16x32_bf16 v[116:119], a[32:35], a[148:151], v[116:119]\n\t"
"s_nop 0\n\t"
"ds_read_b128 a[156:159], v216 offset:23552\n\t"
"ds_read_b128 a[172:175], v216 offset:31744\n\t"
"v_mfma_f32_16x16x32_bf16 v[116:119], a[36:39], a[164:167], v[116:119]\n\t"
"v_mfma_f32_16x16x32_bf16 v[100:103], a[24:27], a[148:151], v[100:103]\n\t"
"v_mfma_f32_16x16x32_bf16 v[100:103], a[28:31], a[164:167], v[100:103]\n\t"
"s_waitcnt lgkmcnt(0)\n\t"
"s_barrier\n\t"
"v_mfma_f32_16x16x32_bf16 v[104:107], a[24:27], a[152:155], v[104:107]\n\t"
"ds_read_b128 v[60:63], v229 offset:13312\n\t"
"v_add_u32 v248, 80896, v224\n\t"
"v_mfma_f32_16x16x32_bf16 v[104:107], a[28:31], a[168:171], v[104:107]\n\t"
"s_nop 0\n\t"
"ds_read_b128 a[144:147], v216 offset:0\n\t"
"ds_read_b128 a[148:151], v216 offset:1024\n\t"
"ds_read_b128 a[164:167], v216 offset:9216\n\t"
"ds_read_b128 v[56:59], v229 offset:0\n\t"
"ds_read_b128 v[72:75], v230 offset:0\n\t"
"ds_read_b128 v[88:91], v231 offset:0\n\t"
"ds_read_b128 v[76:79], v230 offset:13312\n\t"
"ds_read_b128 v[92:95], v231 offset:13312\n\t"
"v_cvt_pk_bf16_f32 v144, v144, v145\n\t"
"v_cvt_pk_bf16_f32 v176, v176, v177\n\t"
"v_cvt_pk_bf16_f32 v156, v156, v157\n\t"
"v_mfma_f32_16x16x32_bf16 v[120:123], a[32:35], a[152:155], v[120:123]\n\t"
"ds_read_b128 a[160:163], v216 offset:8192\n\t"
"v_cvt_pk_bf16_f32 v148, v148, v149\n\t"
"v_mfma_f32_16x16x32_bf16 v[120:123], a[36:39], a[168:171], v[120:123]\n\t"
"v_xor_b32 v249, 32, v248\n\t"
"v_xor_b32 v250, 64, v248\n\t"
"v_xor_b32 v251, 96, v248\n\t"
"v_cvt_pk_bf16_f32 v164, v164, v165\n\t"
"v_mfma_f32_16x16x32_bf16 v[136:139], a[40:43], a[152:155], v[136:139]\n\t"
"v_cvt_pk_bf16_f32 v149, v150, v151\n\t"
"v_cvt_pk_bf16_f32 v168, v168, v169\n\t"
"v_mfma_f32_16x16x32_bf16 v[136:139], a[44:47], a[168:171], v[136:139]\n\t"
"ds_read_b128 a[152:155], v216 offset:2048\n\t"
"v_cvt_pk_bf16_f32 v160, v160, v161\n\t"
"v_mfma_f32_16x16x32_bf16 v[208:211], a[176:179], a[252:255], 0\n\t"
"ds_read_b64_tr_b16 a[176:177], v248 offset:41472\n\t"
"v_cvt_pk_bf16_f32 v161, v162, v163\n\t"
"v_mfma_f32_16x16x32_bf16 v[140:143], a[40:43], a[156:159], v[140:143]\n\t"
"ds_read_b64_tr_b16 a[178:179], v248 offset:41984\n\t"
"v_cvt_pk_bf16_f32 v188, v188, v189\n\t"
"v_mfma_f32_16x16x32_bf16 v[140:143], a[44:47], a[172:175], v[140:143]\n\t"
"ds_read_b128 a[168:171], v216 offset:10240\n\t"
"buffer_load_dwordx4 a[40:43], v235, s[8:11], s58 offen offset:2048 sc1\n\t"
"buffer_load_dwordx4 a[44:47], v235, s[8:11], s58 offen offset:3072 sc1\n\t"
"v_mfma_f32_16x16x32_bf16 v[212:215], a[180:183], a[252:255], 0\n\t"
"ds_read_b64_tr_b16 a[180:181], v249 offset:41472\n\t"
"s_waitcnt lgkmcnt(6)\n\t"
"s_barrier\n\t"
"v_mfma_f32_16x16x32_bf16 v[124:127], a[32:35], a[156:159], v[124:127]\n\t"
"v_cvt_pk_bf16_f32 v145, v146, v147\n\t"
"v_cvt_pk_bf16_f32 v165, v166, v167\n\t"
"v_mfma_f32_16x16x32_bf16 v[124:127], a[36:39], a[172:175], v[124:127]\n\t"
"ds_read_b64_tr_b16 a[182:183], v249 offset:41984\n\t"
"buffer_load_dwordx4 a[36:39], v235, s[8:11], s58 offen offset:1024 sc1\n\t"
"ds_write2st64_b64 v246, v[160:161], v[164:165] offset0:0 offset1:13\n\t"
"v_mfma_f32_16x16x32_bf16 v[108:111], a[24:27], a[156:159], v[108:111]\n\t"
"v_cvt_pk_bf16_f32 v208, v208, v209\n\t"
"v_cvt_pk_bf16_f32 v209, v210, v211\n\t"
"v_mfma_f32_16x16x32_bf16 v[108:111], a[28:31], a[172:175], v[108:111]\n\t"
"buffer_load_dwordx4 a[32:35], v235, s[8:11], s58 offen offset:0 sc1\n\t"
"buffer_load_dwordx4 a[24:27], v218, s[8:11], s57 offen offset:2048 sc1\n\t"
"v_mfma_f32_16x16x32_bf16 v[192:195], a[184:187], a[252:255], 0\n\t"
"s_waitcnt lgkmcnt(7)\n\t"
"ds_write2st64_b64 v228, v[144:145], v[148:149] offset0:0 offset1:13\n\t"
"v_mfma_f32_16x16x32_bf16 v[0:3], a[48:51], a[144:147], v[0:3]\n\t"
"buffer_load_dwordx4 a[28:31], v218, s[8:11], s57 offen offset:3072 sc1\n\t"
"v_mfma_f32_16x16x32_bf16 v[0:3], a[52:55], a[160:163], v[0:3]\n\t"
"v_cvt_pk_bf16_f32 v177, v178, v179\n\t"
"v_cvt_pk_bf16_f32 v157, v158, v159\n\t"
"v_mfma_f32_16x16x32_bf16 v[196:199], a[188:191], a[252:255], 0\n\t"
"buffer_store_dwordx4 v[56:59], v232, s[16:19], s24 offen nt\n\t"
"v_mfma_f32_16x16x32_bf16 v[16:19], a[56:59], a[144:147], v[16:19]\n\t"
"ds_read_b128 a[156:159], v216 offset:3072\n\t"
"v_cvt_pk_bf16_f32 v180, v180, v181\n\t"
"v_mfma_f32_16x16x32_bf16 v[16:19], a[60:63], a[160:163], v[16:19]\n\t"
"ds_read_b128 a[172:175], v216 offset:11264\n\t"
"v_cvt_pk_bf16_f32 v181, v182, v183\n\t"
"v_mfma_f32_16x16x32_bf16 v[32:35], a[64:67], a[144:147], v[32:35]\n\t"
"v_cvt_pk_bf16_f32 v210, v212, v213\n\t"
"v_cvt_pk_bf16_f32 v211, v214, v215\n\t"
"v_mfma_f32_16x16x32_bf16 v[32:35], a[68:71], a[160:163], v[32:35]\n\t"
"ds_read_b64_tr_b16 a[184:185], v250 offset:43776\n\t"
"ds_write2st64_b64 v247, v[176:177], v[180:181] offset0:0 offset1:13\n\t"
"v_mfma_f32_16x16x32_bf16 v[200:203], a[192:195], a[252:255], 0\n\t"
"buffer_store_dwordx4 v[72:75], v233, s[16:19], s24 offen nt\n\t"
"v_mfma_f32_16x16x32_bf16 v[36:39], a[64:67], a[148:151], v[36:39]\n\t"
"v_cvt_pk_bf16_f32 v192, v192, v193\n\t"
"v_cvt_pk_bf16_f32 v193, v194, v195\n\t"
"v_mfma_f32_16x16x32_bf16 v[36:39], a[68:71], a[164:167], v[36:39]\n\t"
"ds_write_b128 v217, v[208:211] offset:16384\n\t"
"v_mfma_f32_16x16x32_bf16 v[20:23], a[56:59], a[148:151], v[20:23]\n\t"
"v_cvt_pk_bf16_f32 v184, v184, v185\n\t"
"v_cvt_pk_bf16_f32 v189, v190, v191\n\t"
"v_mfma_f32_16x16x32_bf16 v[20:23], a[60:63], a[164:167], v[20:23]\n\t"
"buffer_store_dwordx4 v[88:91], v234, s[16:19], s24 offen nt\n\t"
"v_mfma_f32_16x16x32_bf16 v[204:207], a[196:199], a[252:255], 0\n\t"
"v_cvt_pk_bf16_f32 v194, v196, v197\n\t"
"v_cvt_pk_bf16_f32 v152, v152, v153\n\t"
"v_mfma_f32_16x16x32_bf16 v[4:7], a[48:51], a[148:151], v[4:7]\n\t"
"v_cvt_pk_bf16_f32 v195, v198, v199\n\t"
"v_cvt_pk_bf16_f32 v185, v186, v187\n\t"
"v_mfma_f32_16x16x32_bf16 v[4:7], a[52:55], a[164:167], v[4:7]\n\t"
"s_nop 0\n\t"
"ds_read_b128 a[144:147], v216 offset:4096\n\t"
"ds_read_b128 a[160:163], v216 offset:12288\n\t"
"ds_read_b128 a[148:151], v216 offset:5120\n\t"
"ds_read_b128 a[164:167], v216 offset:13312\n\t"
"ds_read_b64_tr_b16 a[186:187], v250 offset:44288\n\t"
"ds_read_b64_tr_b16 a[188:189], v251 offset:43776\n\t"
"ds_read_b64_tr_b16 a[190:191], v251 offset:44288\n\t"
"ds_read_b64_tr_b16 a[192:193], v250 offset:41472\n\t"
"ds_read_b64_tr_b16 a[194:195], v250 offset:41984\n\t"
"ds_read_b64_tr_b16 a[196:197], v251 offset:41472\n\t"
"ds_read_b64_tr_b16 a[198:199], v251 offset:41984\n\t"
"ds_write_b128 v219, v[192:195] offset:16384\n\t"
"v_mfma_f32_16x16x32_bf16 v[208:211], a[200:203], a[252:255], 0\n\t"
"s_waitcnt lgkmcnt(15)\n\t"
"v_cvt_pk_bf16_f32 v172, v172, v173\n\t"
"v_cvt_pk_bf16_f32 v173, v174, v175\n\t"
"v_mfma_f32_16x16x32_bf16 v[8:11], a[48:51], a[152:155], v[8:11]\n\t"
"v_cvt_pk_bf16_f32 v153, v154, v155\n\t"
"v_cvt_pk_bf16_f32 v169, v170, v171\n\t"
"v_mfma_f32_16x16x32_bf16 v[8:11], a[52:55], a[168:171], v[8:11]\n\t"
"ds_write2st64_b64 v228, v[152:153], v[156:157] offset0:26 offset1:39\n\t"
"v_mfma_f32_16x16x32_bf16 v[24:27], a[56:59], a[152:155], v[24:27]\n\t"
"v_cvt_pk_bf16_f32 v200, v200, v201\n\t"
"v_cvt_pk_bf16_f32 v201, v202, v203\n\t"
"v_mfma_f32_16x16x32_bf16 v[24:27], a[60:63], a[168:171], v[24:27]\n\t"
"buffer_store_dwordx4 v[60:63], v232, s[16:19], s27 offen nt\n\t"
"v_mfma_f32_16x16x32_bf16 v[212:215], a[204:207], a[252:255], 0\n\t"
"ds_write2st64_b64 v246, v[168:169], v[172:173] offset0:26 offset1:39\n\t"
"v_mfma_f32_16x16x32_bf16 v[40:43], a[64:67], a[152:155], v[40:43]\n\t"
"v_cvt_pk_bf16_f32 v208, v208, v209\n\t"
"v_cvt_pk_bf16_f32 v209, v210, v211\n\t"
"v_mfma_f32_16x16x32_bf16 v[40:43], a[68:71], a[168:171], v[40:43]\n\t"
"v_cvt_pk_bf16_f32 v202, v204, v205\n\t"
"v_cvt_pk_bf16_f32 v203, v206, v207\n\t"
"v_mfma_f32_16x16x32_bf16 v[44:47], a[64:67], a[156:159], v[44:47]\n\t"
"buffer_load_dwordx4 a[64:67], v235, s[8:11], s65 offen offset:0 sc1\n\t"
"v_mfma_f32_16x16x32_bf16 v[44:47], a[68:71], a[172:175], v[44:47]\n\t"
"ds_write_b128 v217, v[200:203] offset:24576\n\t"
"v_mfma_f32_16x16x32_bf16 v[28:31], a[56:59], a[156:159], v[28:31]\n\t"
"buffer_load_dwordx4 a[68:71], v235, s[8:11], s65 offen offset:1024 sc1\n\t"
"v_mfma_f32_16x16x32_bf16 v[28:31], a[60:63], a[172:175], v[28:31]\n\t"
"buffer_store_dwordx4 v[76:79], v233, s[16:19], s27 offen nt\n\t"
"v_mfma_f32_16x16x32_bf16 v[12:15], a[48:51], a[156:159], v[12:15]\n\t"
"v_cvt_pk_bf16_f32 v210, v212, v213\n\t"
"v_cvt_pk_bf16_f32 v211, v214, v215\n\t"
"v_mfma_f32_16x16x32_bf16 v[12:15], a[52:55], a[172:175], v[12:15]\n\t"
"s_waitcnt lgkmcnt(2)\n\t"
"ds_write_b128 v219, v[208:211] offset:24576\n\t"
"v_mfma_f32_16x16x32_bf16 v[96:99], a[72:75], a[144:147], v[96:99]\n\t"
"buffer_load_dwordx4 a[60:63], v218, s[8:11], s64 offen offset:3072 sc1\n\t"
"v_mfma_f32_16x16x32_bf16 v[96:99], a[76:79], a[160:163], v[96:99]\n\t"
"ds_write2st64_b64 v247, v[184:185], v[188:189] offset0:26 offset1:39\n\t"
"v_mfma_f32_16x16x32_bf16 v[112:115], a[80:83], a[144:147], v[112:115]\n\t"
"buffer_load_dwordx4 a[56:59], v218, s[8:11], s64 offen offset:2048 sc1\n\t"
"v_mfma_f32_16x16x32_bf16 v[112:115], a[84:87], a[160:163], v[112:115]\n\t"
"s_nop 0\n\t"
"ds_read_b128 a[152:155], v216 offset:6144\n\t"
"ds_read_b64_tr_b16 a[200:201], v248 offset:43776\n\t"
"v_mfma_f32_16x16x32_bf16 v[128:131], a[88:91], a[144:147], v[128:131]\n\t"
"buffer_load_dwordx4 a[48:51], v218, s[8:11], s64 offen offset:0 sc1\n\t"
"v_mfma_f32_16x16x32_bf16 v[128:131], a[92:95], a[160:163], v[128:131]\n\t"
"s_nop 0\n\t"
"ds_read_b64_tr_b16 a[202:203], v248 offset:44288\n\t"
"ds_read_b64_tr_b16 a[204:205], v249 offset:43776\n\t"
"v_mfma_f32_16x16x32_bf16 v[132:135], a[88:91], a[148:151], v[132:135]\n\t"
"buffer_load_dwordx4 a[52:55], v218, s[8:11], s64 offen offset:1024 sc1\n\t"
"v_mfma_f32_16x16x32_bf16 v[132:135], a[92:95], a[164:167], v[132:135]\n\t"
"s_nop 0\n\t"
"ds_read_b128 a[156:159], v216 offset:7168\n\t"
"ds_read_b64_tr_b16 a[206:207], v249 offset:44288\n\t"
"v_mfma_f32_16x16x32_bf16 v[116:119], a[80:83], a[148:151], v[116:119]\n\t"
"s_nop 0\n\t"
"ds_read_b128 a[168:171], v216 offset:14336\n\t"
"ds_read_b128 a[172:175], v216 offset:15360\n\t"
"v_mfma_f32_16x16x32_bf16 v[116:119], a[84:87], a[164:167], v[116:119]\n\t"
"buffer_store_dwordx4 v[92:95], v234, s[16:19], s27 offen nt\n\t"
"v_mfma_f32_16x16x32_bf16 v[100:103], a[72:75], a[148:151], v[100:103]\n\t"
"v_mfma_f32_16x16x32_bf16 v[100:103], a[76:79], a[164:167], v[100:103]\n\t"
"s_waitcnt lgkmcnt(0)\n\t"
"s_barrier\n\t"
"v_mfma_f32_16x16x32_bf16 v[104:107], a[72:75], a[152:155], v[104:107]\n\t"
"ds_read_b128 a[144:147], v216 offset:16384\n\t"
"v_mfma_f32_16x16x32_bf16 v[104:107], a[76:79], a[168:171], v[104:107]\n\t"
"ds_read_b128 a[160:163], v216 offset:24576\n\t"
"v_mfma_f32_16x16x32_bf16 v[120:123], a[80:83], a[152:155], v[120:123]\n\t"
"ds_read_b128 a[148:151], v216 offset:17408\n\t"
"v_mfma_f32_16x16x32_bf16 v[120:123], a[84:87], a[168:171], v[120:123]\n\t"
"ds_read_b128 a[164:167], v216 offset:25600\n\t"
"v_mfma_f32_16x16x32_bf16 v[136:139], a[88:91], a[152:155], v[136:139]\n\t"
"ds_read_b128 v[152:155], v229 offset:0\n\t"
"v_mfma_f32_16x16x32_bf16 v[136:139], a[92:95], a[168:171], v[136:139]\n\t"
"ds_read_b128 a[168:171], v216 offset:26624\n\t"
"v_mfma_f32_16x16x32_bf16 v[192:195], a[176:179], a[212:215], 0\n\t"
"ds_read_b128 v[168:171], v230 offset:0\n\t"
"v_mfma_f32_16x16x32_bf16 v[140:143], a[88:91], a[156:159], v[140:143]\n\t"
"buffer_load_dwordx4 a[88:91], v235, s[8:11], s64 offen offset:2048 sc1\n\t"
"v_mfma_f32_16x16x32_bf16 v[140:143], a[92:95], a[172:175], v[140:143]\n\t"
"ds_read_b128 a[152:155], v216 offset:18432\n\t"
"v_mfma_f32_16x16x32_bf16 v[196:199], a[180:183], a[212:215], 0\n\t"
"buffer_load_dwordx4 a[92:95], v235, s[8:11], s64 offen offset:3072 sc1\n\t"
"v_mfma_f32_16x16x32_bf16 v[124:127], a[80:83], a[156:159], v[124:127]\n\t"
"v_cvt_pk_bf16_f32 v192, v192, v193\n\t"
"v_cvt_pk_bf16_f32 v193, v194, v195\n\t"
"v_mfma_f32_16x16x32_bf16 v[124:127], a[84:87], a[172:175], v[124:127]\n\t"
"buffer_load_dwordx4 a[84:87], v235, s[8:11], s64 offen offset:1024 sc1\n\t"
"v_mfma_f32_16x16x32_bf16 v[108:111], a[72:75], a[156:159], v[108:111]\n\t"
"ds_read_b128 v[184:187], v231 offset:0\n\t"
"v_mfma_f32_16x16x32_bf16 v[108:111], a[76:79], a[172:175], v[108:111]\n\t"
"buffer_load_dwordx4 a[80:83], v235, s[8:11], s64 offen offset:0 sc1\n\t"
"v_mfma_f32_16x16x32_bf16 v[200:203], a[184:187], a[212:215], 0\n\t"
"s_waitcnt lgkmcnt(4)\n\t"
"buffer_load_dwordx4 a[72:75], v218, s[8:11], s63 offen offset:2048 sc1\n\t"
"v_mfma_f32_16x16x32_bf16 v[0:3], a[96:99], a[144:147], v[0:3]\n\t"
"v_cvt_pk_bf16_f32 v194, v196, v197\n\t"
"v_mfma_f32_16x16x32_bf16 v[0:3], a[100:103], a[160:163], v[0:3]\n\t"
"ds_read_b128 a[156:159], v216 offset:19456\n\t"
"v_cvt_pk_bf16_f32 v195, v198, v199\n\t"
"v_mfma_f32_16x16x32_bf16 v[204:207], a[188:191], a[212:215], 0\n\t"
"buffer_store_dwordx4 v[152:155], v232, s[16:19], s20 offen nt\n\t"
"v_mfma_f32_16x16x32_bf16 v[16:19], a[104:107], a[144:147], v[16:19]\n\t"
"ds_read_b128 a[172:175], v216 offset:27648\n\t"
"v_cvt_pk_bf16_f32 v200, v200, v201\n\t"
"v_mfma_f32_16x16x32_bf16 v[16:19], a[108:111], a[160:163], v[16:19]\n\t"
"ds_read_b128 v[156:159], v229 offset:13312\n\t"
"v_cvt_pk_bf16_f32 v201, v202, v203\n\t"
"v_mfma_f32_16x16x32_bf16 v[32:35], a[112:115], a[144:147], v[32:35]\n\t"
"buffer_load_dwordx4 a[76:79], v218, s[8:11], s63 offen offset:3072 sc1\n\t"
"v_mfma_f32_16x16x32_bf16 v[32:35], a[116:119], a[160:163], v[32:35]\n\t"
"ds_write_b128 v217, v[192:195] offset:0\n\t"
"v_mfma_f32_16x16x32_bf16 v[208:211], a[192:195], a[212:215], 0\n\t"
"v_cvt_pk_bf16_f32 v202, v204, v205\n\t"
"v_cvt_pk_bf16_f32 v203, v206, v207\n\t"
"v_mfma_f32_16x16x32_bf16 v[36:39], a[112:115], a[148:151], v[36:39]\n\t"
"v_mfma_f32_16x16x32_bf16 v[36:39], a[116:119], a[164:167], v[36:39]\n\t"
"s_waitcnt lgkmcnt(4)\n\t"
"buffer_store_dwordx4 v[168:171], v233, s[16:19], s20 offen nt\n\t"
"v_mfma_f32_16x16x32_bf16 v[20:23], a[104:107], a[148:151], v[20:23]\n\t"
"ds_read_b128 a[144:147], v216 offset:20480\n\t"
"v_mfma_f32_16x16x32_bf16 v[20:23], a[108:111], a[164:167], v[20:23]\n\t"
"ds_read_b128 v[172:175], v230 offset:13312\n\t"
"v_cvt_pk_bf16_f32 v208, v208, v209\n\t"
"v_mfma_f32_16x16x32_bf16 v[212:215], a[196:199], a[212:215], 0\n\t"
"buffer_store_dwordx4 v[184:187], v234, s[16:19], s20 offen nt\n\t"
"v_mfma_f32_16x16x32_bf16 v[4:7], a[96:99], a[148:151], v[4:7]\n\t"
"ds_read_b128 a[160:163], v216 offset:28672\n\t"
"v_cvt_pk_bf16_f32 v209, v210, v211\n\t"
"v_mfma_f32_16x16x32_bf16 v[4:7], a[100:103], a[164:167], v[4:7]\n\t"
"v_mfma_f32_16x16x32_bf16 v[192:195], a[200:203], a[212:215], 0\n\t"
"ds_write_b128 v219, v[200:203] offset:0\n\t"
"v_mfma_f32_16x16x32_bf16 v[8:11], a[96:99], a[152:155], v[8:11]\n\t"
"v_cvt_pk_bf16_f32 v210, v212, v213\n\t"
"v_cvt_pk_bf16_f32 v211, v214, v215\n\t"
"v_mfma_f32_16x16x32_bf16 v[8:11], a[100:103], a[168:171], v[8:11]\n\t"
"s_waitcnt lgkmcnt(4)\n\t"
"buffer_store_dwordx4 v[156:159], v232, s[16:19], s25 offen nt\n\t"
"v_mfma_f32_16x16x32_bf16 v[24:27], a[104:107], a[152:155], v[24:27]\n\t"
"v_mfma_f32_16x16x32_bf16 v[24:27], a[108:111], a[168:171], v[24:27]\n\t"
"ds_read_b128 a[148:151], v216 offset:21504\n\t"
"v_cvt_pk_bf16_f32 v192, v192, v193\n\t"
"v_mfma_f32_16x16x32_bf16 v[196:199], a[204:207], a[212:215], 0\n\t"
"ds_read_b128 a[164:167], v216 offset:29696\n\t"
"v_mfma_f32_16x16x32_bf16 v[40:43], a[112:115], a[152:155], v[40:43]\n\t"
"ds_read_b128 v[188:191], v231 offset:13312\n\t"
"v_cvt_pk_bf16_f32 v193, v194, v195\n\t"
"v_mfma_f32_16x16x32_bf16 v[40:43], a[116:119], a[168:171], v[40:43]\n\t"
"ds_read_b128 a[168:171], v216 offset:30720\n\t"
"v_mfma_f32_16x16x32_bf16 v[44:47], a[112:115], a[156:159], v[44:47]\n\t"
"buffer_load_dwordx4 a[112:115], v235, s[8:11], s47 offen offset:0 sc1\n\t"
"v_mfma_f32_16x16x32_bf16 v[44:47], a[116:119], a[172:175], v[44:47]\n\t"
"ds_write_b128 v217, v[208:211] offset:8192\n\t"
"v_mfma_f32_16x16x32_bf16 v[28:31], a[104:107], a[156:159], v[28:31]\n\t"
"buffer_load_dwordx4 a[116:119], v235, s[8:11], s47 offen offset:1024 sc1\n\t"
"v_mfma_f32_16x16x32_bf16 v[28:31], a[108:111], a[172:175], v[28:31]\n\t"
"s_waitcnt lgkmcnt(5)\n\t"
"buffer_store_dwordx4 v[172:175], v233, s[16:19], s25 offen nt\n\t"
"v_mfma_f32_16x16x32_bf16 v[12:15], a[96:99], a[156:159], v[12:15]\n\t"
"v_cvt_pk_bf16_f32 v194, v196, v197\n\t"
"v_cvt_pk_bf16_f32 v195, v198, v199\n\t"
"v_mfma_f32_16x16x32_bf16 v[12:15], a[100:103], a[172:175], v[12:15]\n\t"
"ds_read_b128 a[156:159], v216 offset:23552\n\t"
"v_mfma_f32_16x16x32_bf16 v[96:99], a[120:123], a[144:147], v[96:99]\n\t"
"buffer_load_dwordx4 a[108:111], v218, s[8:11], s46 offen offset:3072 sc1\n\t"
"v_mfma_f32_16x16x32_bf16 v[96:99], a[124:127], a[160:163], v[96:99]\n\t"
"ds_write_b128 v219, v[192:195] offset:8192\n\t"
"v_mfma_f32_16x16x32_bf16 v[112:115], a[128:131], a[144:147], v[112:115]\n\t"
"buffer_load_dwordx4 a[104:107], v218, s[8:11], s46 offen offset:2048 sc1\n\t"
"v_mfma_f32_16x16x32_bf16 v[112:115], a[132:135], a[160:163], v[112:115]\n\t"
"v_mfma_f32_16x16x32_bf16 v[128:131], a[136:139], a[144:147], v[128:131]\n\t"
"buffer_load_dwordx4 a[96:99], v218, s[8:11], s46 offen offset:0 sc1\n\t"
"v_mfma_f32_16x16x32_bf16 v[128:131], a[140:143], a[160:163], v[128:131]\n\t"
"s_waitcnt lgkmcnt(2)\n\t"
"ds_read_b128 a[152:155], v216 offset:22528\n\t"
"v_mfma_f32_16x16x32_bf16 v[132:135], a[136:139], a[148:151], v[132:135]\n\t"
"buffer_load_dwordx4 a[100:103], v218, s[8:11], s46 offen offset:1024 sc1\n\t"
"v_mfma_f32_16x16x32_bf16 v[132:135], a[140:143], a[164:167], v[132:135]\n\t"
"v_mfma_f32_16x16x32_bf16 v[116:119], a[128:131], a[148:151], v[116:119]\n\t"
"ds_read_b128 a[172:175], v216 offset:31744\n\t"
"v_mfma_f32_16x16x32_bf16 v[116:119], a[132:135], a[164:167], v[116:119]\n\t"
"buffer_store_dwordx4 v[188:191], v234, s[16:19], s25 offen nt\n\t"
"v_mfma_f32_16x16x32_bf16 v[100:103], a[120:123], a[148:151], v[100:103]\n\t"
"v_mfma_f32_16x16x32_bf16 v[100:103], a[124:127], a[164:167], v[100:103]\n\t"
"s_waitcnt vmcnt(42) lgkmcnt(2)\n\t"
"s_waitcnt lgkmcnt(0)\n\t"
"s_barrier\n\t"
"s_add_u32 s71, s71, 1\n\t"
"v_mov_b32 v215, s71\n\t"
"s_mov_b64 exec, 1\n\t"
"buffer_store_dword v215, off, s[8:11], s70 sc1\n\t"
"s_mov_b64 exec, -1\n\t"
"s_sub_u32 s21, s21, 1\n\t"
"s_xor_b64 s[84:85], s[84:85], s[88:89]\n\t"
"s_branch Lunit_%=\n\t"
"Lfb_%=:\n\t"
"v_mbcnt_lo_u32_b32 v162, -1, 0\n\t"
"v_mbcnt_hi_u32_b32 v162, -1, v162\n\t"
"v_and_b32 v161, 3, v162\n\t"
"v_lshlrev_b32 v163, 2, v162\n\t"
"v_and_or_b32 v161, v163, 16, v161\n\t"
"v_mul_u32_u24 v161, 0xc0, v161\n\t"
"v_lshrrev_b32 v163, 5, v162\n\t"
"v_mul_u32_u24 v163, 12, v163\n\t"
"v_bfe_u32 v164, v162, 3, 2\n\t"
"v_add3_u32 v161, v161, v163, v164\n\t"
"v_lshlrev_b32 v161, 1, v161\n\t"
"v_lshlrev_b32 v162, 1, v162\n\t"
"v_mov_b32 v152, 0\n\t"
"v_mov_b32 v153, 0\n\t"
"v_mov_b32 v154, 0\n\t"
"v_mov_b32 v155, 0\n\t"
"v_mov_b32 v156, 0\n\t"
"v_mov_b32 v157, 0\n\t"
"v_mov_b32 v158, 0\n\t"
"v_mov_b32 v159, 0\n\t"
"v_mov_b32 v160, 0\n\t"
"s_mov_b32 s67, 0\n\t"
"Lfb0_%=:\n\t"
"s_mov_b32 s69, 0\n\t"
"Lfb1_%=:\n\t"
"s_lshr_b32 s60, s69, 5\n\t"
"s_mul_i32 s56, s60, 12288\n\t"
"s_bfe_u32 s61, s69, 0x20001\n\t"
"s_mul_i32 s61, s61, 1536\n\t"
"s_add_u32 s56, s56, s61\n\t"
"s_bitcmp1_b32 s69, 0\n\t"
"s_cselect_b32 s61, 48, 0\n\t"
"s_add_u32 s56, s56, s61\n\t"
"s_bfe_u32 s62, s69, 0x20003\n\t"
"s_mul_i32 s61, s62, 96\n\t"
"s_add_u32 s56, s56, s61\n\t"
"s_lshl3_add_u32 s56, s67, s56\n\t"
"s_lshr_b32 s60, s69, 5\n\t"
"s_bfe_u32 s62, s69, 0x20003\n\t"
"s_and_b32 s61, s60, 1\n\t"
"s_lshl1_add_u32 s61, s67, s61\n\t"
"s_mul_i32 s62, s62, 288\n\t"
"s_add_u32 s61, s61, s62\n\t"
"s_lshr_b32 s60, s60, 1\n\t"
"s_mul_i32 s62, s60, 96\n\t"
"s_add_u32 s61, s61, s62\n\t"
"s_lshl_b32 s57, s61, 10\n\t"
"s_and_b32 s61, s69, 7\n\t"
"s_lshl_b32 s61, s61, 7\n\t"
"s_add_u32 s57, s57, s61\n\t"
"s_add_u32 s60, s56, 0\n\t"
"buffer_load_short_d16_hi v152, v161, s[52:55], s60 offen\n\t"
"s_add_u32 s60, s56, 73728\n\t"
"buffer_load_short_d16_hi v153, v161, s[52:55], s60 offen\n\t"
"s_add_u32 s60, s56, 147456\n\t"
"buffer_load_short_d16_hi v154, v161, s[52:55], s60 offen\n\t"
"s_add_u32 s60, s56, 221184\n\t"
"buffer_load_short_d16_hi v155, v161, s[52:55], s60 offen\n\t"
"s_add_u32 s60, s56, 294912\n\t"
"buffer_load_short_d16_hi v156, v161, s[52:55], s60 offen\n\t"
"s_add_u32 s60, s56, 368640\n\t"
"buffer_load_short_d16_hi v157, v161, s[52:55], s60 offen\n\t"
"s_add_u32 s60, s56, 442368\n\t"
"buffer_load_short_d16_hi v158, v161, s[52:55], s60 offen\n\t"
"s_add_u32 s60, s56, 516096\n\t"
"buffer_load_short_d16_hi v159, v161, s[52:55], s60 offen\n\t"
"s_add_u32 s60, s56, 589824\n\t"
"buffer_load_short_d16_hi v160, v161, s[52:55], s60 offen\n\t"
"s_waitcnt vmcnt(2)\n\t"
"v_add_f32 v168, v152, v158\n\t"
"s_waitcnt vmcnt(1)\n\t"
"v_add_f32 v169, v153, v159\n\t"
"s_waitcnt vmcnt(0)\n\t"
"v_add_f32 v170, v154, v160\n\t"
"v_add_f32 v171, v168, v155\n\t"
"v_sub_f32 v174, v168, v155\n\t"
"v_add_f32 v172, v169, v156\n\t"
"v_sub_f32 v175, v169, v156\n\t"
"v_add_f32 v173, v170, v157\n\t"
"v_sub_f32 v176, v170, v157\n\t"
"v_add_f32 v177, v152, v154\n\t"
"v_add_f32 v178, v171, v173\n\t"
"v_add_f32 v179, v174, v176\n\t"
"v_add_f32 v180, v158, v160\n\t"
"v_add_f32 v182, v177, v153\n\t"
"v_sub_f32 v183, v177, v153\n\t"
"v_add_f32 v184, v178, v172\n\t"
"v_sub_f32 v185, v178, v172\n\t"
"v_add_f32 v186, v179, v175\n\t"
"v_sub_f32 v187, v179, v175\n\t"
"v_add_f32 v188, v180, v159\n\t"
"v_sub_f32 v189, v180, v159\n\t"
"v_mul_f32 v191, 0.5, v182\n\t"
"v_mul_f32 v192, 0.5, v183\n\t"
"v_mul_f32 v194, 0.5, v171\n\t"
"v_mul_f32 v195, 0x3e800000, v184\n\t"
"v_mul_f32 v196, 0x3e800000, v185\n\t"
"v_mul_f32 v197, -0.5, v173\n\t"
"v_mul_f32 v199, 0.5, v186\n\t"
"v_mul_f32 v200, 0.5, v187\n\t"
"v_mul_f32 v203, -0.5, v188\n\t"
"v_mul_f32 v204, -0.5, v189\n\t"
"v_cvt_pk_bf16_f32 v206, v152, v191\n\t"
"v_cvt_pk_bf16_f32 v207, v192, -v154\n\t"
"v_cvt_pk_bf16_f32 v208, v194, v195\n\t"
"v_cvt_pk_bf16_f32 v209, v196, v197\n\t"
"v_cvt_pk_bf16_f32 v210, v174, v199\n\t"
"v_cvt_pk_bf16_f32 v211, v200, -v176\n\t"
"v_cvt_pk_bf16_f32 v212, -v158, v203\n\t"
"v_cvt_pk_bf16_f32 v213, v204, v160\n\t"
"s_add_u32 s60, s57, 0\n\t"
"buffer_store_short v206, v162, s[8:11], s60 offen sc1\n\t"
"s_add_u32 s60, s57, 6144\n\t"
"buffer_store_short_d16_hi v206, v162, s[8:11], s60 offen sc1\n\t"
"s_add_u32 s60, s57, 12288\n\t"
"buffer_store_short v207, v162, s[8:11], s60 offen sc1\n\t"
"s_add_u32 s60, s57, 18432\n\t"
"buffer_store_short_d16_hi v207, v162, s[8:11], s60 offen sc1\n\t"
"s_add_u32 s60, s57, 24576\n\t"
"buffer_store_short v208, v162, s[8:11], s60 offen sc1\n\t"
"s_add_u32 s60, s57, 30720\n\t"
"buffer_store_short_d16_hi v208, v162, s[8:11], s60 offen sc1\n\t"
"s_add_u32 s60, s57, 36864\n\t"
"buffer_store_short v209, v162, s[8:11], s60 offen sc1\n\t"
"s_add_u32 s60, s57, 43008\n\t"
"buffer_store_short_d16_hi v209, v162, s[8:11], s60 offen sc1\n\t"
"s_add_u32 s60, s57, 49152\n\t"
"buffer_store_short v210, v162, s[8:11], s60 offen sc1\n\t"
"s_add_u32 s60, s57, 55296\n\t"
"buffer_store_short_d16_hi v210, v162, s[8:11], s60 offen sc1\n\t"
"s_add_u32 s60, s57, 61440\n\t"
"buffer_store_short v211, v162, s[8:11], s60 offen sc1\n\t"
"s_add_u32 s60, s57, 67584\n\t"
"buffer_store_short_d16_hi v211, v162, s[8:11], s60 offen sc1\n\t"
"s_add_u32 s60, s57, 73728\n\t"
"buffer_store_short v212, v162, s[8:11], s60 offen sc1\n\t"
"s_add_u32 s60, s57, 79872\n\t"
"buffer_store_short_d16_hi v212, v162, s[8:11], s60 offen sc1\n\t"
"s_add_u32 s60, s57, 86016\n\t"
"buffer_store_short v213, v162, s[8:11], s60 offen sc1\n\t"
"s_add_u32 s60, s57, 92160\n\t"
"buffer_store_short_d16_hi v213, v162, s[8:11], s60 offen sc1\n\t"
"s_waitcnt vmcnt(0)\n\t"
"s_add_u32 s69, s69, 1\n\t"
"s_cmp_lt_u32 s69, 192\n\t"
"s_cbranch_scc1 Lfb1_%=\n\t"
"s_add_u32 s67, s67, 1\n\t"
"s_cmp_lt_u32 s67, 3\n\t"
"s_cbranch_scc1 Lfb0_%=\n\t"
"s_branch Lgo_%=\n\t"
"Ltail_%=:\n\t"
"v_mfma_f32_16x16x32_bf16 v[104:107], a[72:75], a[152:155], v[104:107]\n\t"
"ds_read_b128 a[148:151], v216 offset:21504\n\t"
"v_add_u32 v248, 143104, v227\n\t"
"v_mfma_f32_16x16x32_bf16 v[104:107], a[76:79], a[168:171], v[104:107]\n\t"
"ds_read_b128 a[144:147], v216 offset:20480\n\t"
"buffer_load_dwordx4 a[16:19], v218, s[84:87], s69 offen offset:0 sc1\n\t"
"buffer_load_dwordx4 a[20:23], v218, s[84:87], s69 offen offset:1024 sc1\n\t"
"v_mfma_f32_16x16x32_bf16 v[120:123], a[80:83], a[152:155], v[120:123]\n\t"
"ds_read_b128 a[160:163], v216 offset:28672\n\t"
"v_xor_b32 v249, 32, v248\n\t"
"v_mfma_f32_16x16x32_bf16 v[120:123], a[84:87], a[168:171], v[120:123]\n\t"
"ds_read_b128 a[164:167], v216 offset:29696\n\t"
"buffer_load_dwordx4 a[12:15], v235, s[84:87], s69 offen offset:3072 sc1\n\t"
"buffer_load_dwordx4 a[8:11], v235, s[84:87], s69 offen offset:2048 sc1\n\t"
"v_mfma_f32_16x16x32_bf16 v[136:139], a[88:91], a[152:155], v[136:139]\n\t"
"ds_read_b128 v[8:11], v229 offset:19968\n\t"
"v_xor_b32 v250, 64, v248\n\t"
"v_mfma_f32_16x16x32_bf16 v[136:139], a[92:95], a[168:171], v[136:139]\n\t"
"ds_read_b128 a[152:155], v216 offset:22528\n\t"
"buffer_load_dwordx4 a[0:3], v235, s[84:87], s69 offen offset:0 sc1\n\t"
"v_mfma_f32_16x16x32_bf16 v[192:195], a[176:179], a[224:227], 0\n\t"
"ds_read_b128 a[168:171], v216 offset:30720\n\t"
"v_xor_b32 v251, 96, v248\n\t"
"v_mfma_f32_16x16x32_bf16 v[140:143], a[88:91], a[156:159], v[140:143]\n\t"
"buffer_load_dwordx4 a[4:7], v235, s[84:87], s69 offen offset:1024 sc1\n\t"
"v_mfma_f32_16x16x32_bf16 v[140:143], a[92:95], a[172:175], v[140:143]\n\t"
"s_nop 0\n\t"
"ds_read_b64_tr_b16 a[176:177], v248 offset:0\n\t"
"ds_read_b64_tr_b16 a[178:179], v248 offset:512\n\t"
"ds_read_b128 v[24:27], v230 offset:19968\n\t"
"v_mfma_f32_16x16x32_bf16 v[196:199], a[180:183], a[224:227], 0\n\t"
"buffer_load_dwordx4 a[40:43], v218, s[84:87], s81 offen offset:0 sc1\n\t"
"v_mfma_f32_16x16x32_bf16 v[124:127], a[80:83], a[156:159], v[124:127]\n\t"
"s_nop 0\n\t"
"ds_read_b64_tr_b16 a[180:181], v249 offset:0\n\t"
"ds_read_b64_tr_b16 a[182:183], v249 offset:512\n\t"
"v_mfma_f32_16x16x32_bf16 v[124:127], a[84:87], a[172:175], v[124:127]\n\t"
"buffer_load_dwordx4 a[44:47], v218, s[84:87], s81 offen offset:1024 sc1\n\t"
"v_mfma_f32_16x16x32_bf16 v[108:111], a[72:75], a[156:159], v[108:111]\n\t"
"ds_read_b128 a[156:159], v216 offset:23552\n\t"
"v_cvt_pk_bf16_f32 v192, v192, v193\n\t"
"v_mfma_f32_16x16x32_bf16 v[108:111], a[76:79], a[172:175], v[108:111]\n\t"
"s_nop 0\n\t"
"ds_read_b128 a[172:175], v216 offset:31744\n\t"
"ds_read_b128 v[40:43], v231 offset:19968\n\t"
"v_mfma_f32_16x16x32_bf16 v[200:203], a[184:187], a[224:227], 0\n\t"
"s_waitcnt vmcnt(14) lgkmcnt(10)\n\t"
"buffer_store_dwordx4 v[8:11], v232, s[16:19], s24 offen nt\n\t"
"v_mfma_f32_16x16x32_bf16 v[144:147], a[96:99], a[144:147], v[144:147]\n\t"
"ds_read_b64_tr_b16 a[184:185], v250 offset:0\n\t"
"v_cvt_pk_bf16_f32 v193, v194, v195\n\t"
"v_mfma_f32_16x16x32_bf16 v[144:147], a[100:103], a[160:163], v[144:147]\n\t"
"ds_read_b64_tr_b16 a[186:187], v250 offset:512\n\t"
"v_cvt_pk_bf16_f32 v194, v196, v197\n\t"
"v_mfma_f32_16x16x32_bf16 v[204:207], a[188:191], a[224:227], 0\n\t"
"buffer_load_dwordx4 a[36:39], v235, s[84:87], s81 offen offset:3072 sc1\n\t"
"v_mfma_f32_16x16x32_bf16 v[160:163], a[104:107], a[144:147], v[160:163]\n\t"
"ds_read_b64_tr_b16 a[188:189], v251 offset:0\n\t"
"v_cvt_pk_bf16_f32 v195, v198, v199\n\t"
"v_mfma_f32_16x16x32_bf16 v[160:163], a[108:111], a[160:163], v[160:163]\n\t"
"buffer_load_dwordx4 a[32:35], v235, s[84:87], s81 offen offset:2048 sc1\n\t"
"v_mfma_f32_16x16x32_bf16 v[176:179], a[112:115], a[144:147], v[176:179]\n\t"
"s_nop 0\n\t"
"ds_read_b128 a[144:147], v216 offset:16384\n\t"
"ds_read_b64_tr_b16 a[190:191], v251 offset:512\n\t"
"v_mfma_f32_16x16x32_bf16 v[176:179], a[116:119], a[160:163], v[176:179]\n\t"
"s_nop 0\n\t"
"ds_read_b128 a[160:163], v216 offset:24576\n\t"
"ds_read_b128 v[12:15], v229 offset:33280\n\t"
"v_mfma_f32_16x16x32_bf16 v[208:211], a[192:195], a[224:227], 0\n\t"
"ds_read_b64_tr_b16 a[192:193], v248 offset:1152\n\t"
"s_waitcnt lgkmcnt(10)\n\t"
"buffer_store_dwordx4 v[24:27], v233, s[16:19], s24 offen nt\n\t"
"v_mfma_f32_16x16x32_bf16 v[180:183], a[112:115], a[148:151], v[180:183]\n\t"
"v_cvt_pk_bf16_f32 v200, v200, v201\n\t"
"v_cvt_pk_bf16_f32 v201, v202, v203\n\t"
"v_mfma_f32_16x16x32_bf16 v[180:183], a[116:119], a[164:167], v[180:183]\n\t"
"ds_read_b64_tr_b16 a[194:195], v248 offset:1664\n\t"
"ds_write_b128 v221, v[192:195] offset:0\n\t"
"v_mfma_f32_16x16x32_bf16 v[164:167], a[104:107], a[148:151], v[164:167]\n\t"
"buffer_load_dwordx4 a[24:27], v235, s[84:87], s81 offen offset:0 sc1\n\t"
"v_mfma_f32_16x16x32_bf16 v[164:167], a[108:111], a[164:167], v[164:167]\n\t"
"s_waitcnt lgkmcnt(8)\n\t"
"buffer_store_dwordx4 v[40:43], v234, s[16:19], s24 offen nt\n\t"
"v_mfma_f32_16x16x32_bf16 v[212:215], a[196:199], a[224:227], 0\n\t"
"v_cvt_pk_bf16_f32 v202, v204, v205\n\t"
"v_cvt_pk_bf16_f32 v203, v206, v207\n\t"
"v_mfma_f32_16x16x32_bf16 v[148:151], a[96:99], a[148:151], v[148:151]\n\t"
"s_nop 0\n\t"
"ds_read_b128 a[148:151], v216 offset:17408\n\t"
"ds_read_b128 v[28:31], v230 offset:33280\n\t"
"v_mfma_f32_16x16x32_bf16 v[148:151], a[100:103], a[164:167], v[148:151]\n\t"
"buffer_load_dwordx4 a[28:31], v235, s[84:87], s81 offen offset:1024 sc1\n\t"
"v_mfma_f32_16x16x32_bf16 v[192:195], a[200:203], a[224:227], 0\n\t"
"s_nop 0\n\t"
"ds_read_b128 a[164:167], v216 offset:25600\n\t"
"ds_read_b64_tr_b16 a[196:197], v249 offset:1152\n\t"
"v_mfma_f32_16x16x32_bf16 v[152:155], a[96:99], a[152:155], v[152:155]\n\t"
"buffer_load_dwordx4 a[64:67], v218, s[84:87], s54 offen offset:0 sc1\n\t"
"v_mfma_f32_16x16x32_bf16 v[152:155], a[100:103], a[168:171], v[152:155]\n\t"
"ds_write_b128 v221, v[200:203] offset:8192\n\t"
"v_mfma_f32_16x16x32_bf16 v[168:171], a[104:107], a[152:155], v[168:171]\n\t"
"v_cvt_pk_bf16_f32 v208, v208, v209\n\t"
"v_cvt_pk_bf16_f32 v209, v210, v211\n\t"
"v_mfma_f32_16x16x32_bf16 v[168:171], a[108:111], a[168:171], v[168:171]\n\t"
"s_waitcnt lgkmcnt(5)\n\t"
"buffer_store_dwordx4 v[12:15], v232, s[16:19], s27 offen nt\n\t"
"v_mfma_f32_16x16x32_bf16 v[196:199], a[204:207], a[224:227], 0\n\t"
"v_cvt_pk_bf16_f32 v210, v212, v213\n\t"
"v_cvt_pk_bf16_f32 v192, v192, v193\n\t"
"v_mfma_f32_16x16x32_bf16 v[184:187], a[112:115], a[152:155], v[184:187]\n\t"
"ds_read_b64_tr_b16 a[198:199], v249 offset:1664\n\t"
"v_cvt_pk_bf16_f32 v193, v194, v195\n\t"
"v_mfma_f32_16x16x32_bf16 v[184:187], a[116:119], a[168:171], v[184:187]\n\t"
"ds_read_b128 v[44:47], v231 offset:33280\n\t"
"v_cvt_pk_bf16_f32 v211, v214, v215\n\t"
"v_mfma_f32_16x16x32_bf16 v[188:191], a[112:115], a[156:159], v[188:191]\n\t"
"buffer_load_dwordx4 a[68:71], v218, s[84:87], s54 offen offset:1024 sc1\n\t"
"v_mfma_f32_16x16x32_bf16 v[188:191], a[116:119], a[172:175], v[188:191]\n\t"
"ds_write_b128 v221, v[208:211] offset:4096\n\t"
"v_mfma_f32_16x16x32_bf16 v[172:175], a[104:107], a[156:159], v[172:175]\n\t"
"buffer_load_dwordx4 a[60:63], v235, s[84:87], s54 offen offset:3072 sc1\n\t"
"v_mfma_f32_16x16x32_bf16 v[172:175], a[108:111], a[172:175], v[172:175]\n\t"
"s_waitcnt lgkmcnt(4)\n\t"
"buffer_store_dwordx4 v[28:31], v233, s[16:19], s27 offen nt\n\t"
"v_mfma_f32_16x16x32_bf16 v[156:159], a[96:99], a[156:159], v[156:159]\n\t"
"v_cvt_pk_bf16_f32 v194, v196, v197\n\t"
"v_cvt_pk_bf16_f32 v195, v198, v199\n\t"
"v_mfma_f32_16x16x32_bf16 v[156:159], a[100:103], a[172:175], v[156:159]\n\t"
"s_waitcnt vmcnt(20) lgkmcnt(3)\n\t"
"ds_write_b128 v221, v[192:195] offset:12288\n\t"
"v_mfma_f32_16x16x32_bf16 v[96:99], a[120:123], a[144:147], v[96:99]\n\t"
"buffer_load_dwordx4 a[56:59], v235, s[84:87], s54 offen offset:2048 sc1\n\t"
"v_mfma_f32_16x16x32_bf16 v[96:99], a[124:127], a[160:163], v[96:99]\n\t"
"v_mfma_f32_16x16x32_bf16 v[112:115], a[128:131], a[144:147], v[112:115]\n\t"
"buffer_load_dwordx4 a[48:51], v235, s[84:87], s54 offen offset:0 sc1\n\t"
"v_mfma_f32_16x16x32_bf16 v[112:115], a[132:135], a[160:163], v[112:115]\n\t"
"s_nop 0\n\t"
"ds_read_b128 a[168:171], v216 offset:26624\n\t"
"ds_read_b64_tr_b16 a[200:201], v250 offset:1152\n\t"
"v_mfma_f32_16x16x32_bf16 v[128:131], a[136:139], a[144:147], v[128:131]\n\t"
"s_nop 0\n\t"
"ds_read_b128 a[152:155], v216 offset:18432\n\t"
"ds_read_b64_tr_b16 a[202:203], v250 offset:1664\n\t"
"v_mfma_f32_16x16x32_bf16 v[128:131], a[140:143], a[160:163], v[128:131]\n\t"
"s_waitcnt lgkmcnt(5)\n\t"
"buffer_store_dwordx4 v[44:47], v234, s[16:19], s27 offen nt\n\t"
"v_mfma_f32_16x16x32_bf16 v[132:135], a[136:139], a[148:151], v[132:135]\n\t"
"s_nop 0\n\t"
"ds_read_b128 a[156:159], v216 offset:19456\n\t"
"ds_read_b64_tr_b16 a[204:205], v251 offset:1152\n\t"
"v_mfma_f32_16x16x32_bf16 v[132:135], a[140:143], a[164:167], v[132:135]\n\t"
"s_nop 0\n\t"
"ds_read_b128 a[172:175], v216 offset:27648\n\t"
"ds_read_b64_tr_b16 a[206:207], v251 offset:1664\n\t"
"v_mfma_f32_16x16x32_bf16 v[116:119], a[128:131], a[148:151], v[116:119]\n\t"
"buffer_load_dwordx4 a[52:55], v235, s[84:87], s54 offen offset:1024 sc1\n\t"
"v_mfma_f32_16x16x32_bf16 v[116:119], a[132:135], a[164:167], v[116:119]\n\t"
"v_mfma_f32_16x16x32_bf16 v[100:103], a[120:123], a[148:151], v[100:103]\n\t"
"buffer_load_dwordx4 a[88:91], v218, s[84:87], s66 offen offset:0 sc1\n\t"
"v_mfma_f32_16x16x32_bf16 v[100:103], a[124:127], a[164:167], v[100:103]\n\t"
"s_waitcnt lgkmcnt(0)\n\t"
"s_barrier\n\t"
"v_mfma_f32_16x16x32_bf16 v[104:107], a[120:123], a[152:155], v[104:107]\n\t"
"s_nop 0\n\t"
"ds_read_b128 a[144:147], v216 offset:0\n\t"
"ds_read_b128 a[148:151], v216 offset:1024\n\t"
"v_mfma_f32_16x16x32_bf16 v[104:107], a[124:127], a[168:171], v[104:107]\n\t"
"buffer_load_dwordx4 a[92:95], v218, s[84:87], s66 offen offset:1024 sc1\n\t"
"v_mfma_f32_16x16x32_bf16 v[120:123], a[128:131], a[152:155], v[120:123]\n\t"
"ds_read_b128 a[160:163], v216 offset:8192\n\t"
"v_add_u32 v248, 101632, v226\n\t"
"v_mfma_f32_16x16x32_bf16 v[120:123], a[132:135], a[168:171], v[120:123]\n\t"
"ds_read_b128 a[164:167], v216 offset:9216\n\t"
"v_xor_b32 v249, 32, v248\n\t"
"v_mfma_f32_16x16x32_bf16 v[136:139], a[136:139], a[152:155], v[136:139]\n\t"
"ds_read_b128 a[152:155], v216 offset:2048\n\t"
"v_xor_b32 v250, 64, v248\n\t"
"v_mfma_f32_16x16x32_bf16 v[136:139], a[140:143], a[168:171], v[136:139]\n\t"
"buffer_load_dwordx4 a[84:87], v235, s[84:87], s66 offen offset:3072 sc1\n\t"
"v_mfma_f32_16x16x32_bf16 v[200:203], a[176:179], a[224:227], 0\n\t"
"ds_read_b128 a[168:171], v216 offset:10240\n\t"
"v_xor_b32 v251, 96, v248\n\t"
"v_mfma_f32_16x16x32_bf16 v[140:143], a[136:139], a[156:159], v[140:143]\n\t"
"ds_read_b64_tr_b16 a[176:177], v248 offset:41472\n\t"
"v_mfma_f32_16x16x32_bf16 v[140:143], a[140:143], a[172:175], v[140:143]\n\t"
"buffer_load_dwordx4 a[80:83], v235, s[84:87], s66 offen offset:2048 sc1\n\t"
"v_mfma_f32_16x16x32_bf16 v[204:207], a[180:183], a[224:227], 0\n\t"
"ds_read_b64_tr_b16 a[178:179], v248 offset:41984\n\t"
"v_mfma_f32_16x16x32_bf16 v[124:127], a[128:131], a[156:159], v[124:127]\n\t"
"ds_read_b64_tr_b16 a[180:181], v249 offset:41472\n\t"
"v_cvt_pk_bf16_f32 v200, v200, v201\n\t"
"v_mfma_f32_16x16x32_bf16 v[124:127], a[132:135], a[172:175], v[124:127]\n\t"
"ds_read_b64_tr_b16 a[182:183], v249 offset:41984\n\t"
"v_cvt_pk_bf16_f32 v201, v202, v203\n\t"
"v_mfma_f32_16x16x32_bf16 v[108:111], a[120:123], a[156:159], v[108:111]\n\t"
"buffer_load_dwordx4 a[72:75], v235, s[84:87], s66 offen offset:0 sc1\n\t"
"v_mfma_f32_16x16x32_bf16 v[108:111], a[124:127], a[172:175], v[108:111]\n\t"
"ds_read_b128 a[156:159], v216 offset:3072\n\t"
"v_cvt_pk_bf16_f32 v202, v204, v205\n\t"
"v_mfma_f32_16x16x32_bf16 v[208:211], a[184:187], a[224:227], 0\n\t"
"s_waitcnt vmcnt(23) lgkmcnt(6)\n\t"
"ds_read_b64_tr_b16 a[184:185], v250 offset:43776\n\t"
"buffer_load_dwordx4 a[76:79], v235, s[84:87], s66 offen offset:1024 sc1\n\t"
"buffer_load_dwordx4 a[112:115], v218, s[84:87], s57 offen offset:0 sc1\n\t"
"v_mfma_f32_16x16x32_bf16 v[48:51], a[0:3], a[144:147], v[48:51]\n\t"
"ds_read_b64_tr_b16 a[186:187], v250 offset:44288\n\t"
"v_cvt_pk_bf16_f32 v203, v206, v207\n\t"
"v_mfma_f32_16x16x32_bf16 v[48:51], a[4:7], a[160:163], v[48:51]\n\t"
"buffer_load_dwordx4 a[116:119], v218, s[84:87], s57 offen offset:1024 sc1\n\t"
"buffer_load_dwordx4 a[108:111], v235, s[84:87], s57 offen offset:3072 sc1\n\t"
"v_mfma_f32_16x16x32_bf16 v[212:215], a[188:191], a[224:227], 0\n\t"
"ds_read_b64_tr_b16 a[188:189], v251 offset:43776\n\t"
"ds_write_b128 v221, v[200:203] offset:16384\n\t"
"v_pk_add_f32 v[96:97], v[96:97], v[144:145]\n\t"
"v_pk_add_f32 v[98:99], v[98:99], v[146:147]\n\t"
"v_pk_fma_f32 v[144:145], v[144:145], s[38:39], v[96:97]\n\t"
"v_pk_fma_f32 v[146:147], v[146:147], s[38:39], v[98:99]\n\t"
"v_pk_add_f32 v[112:113], v[112:113], v[160:161]\n\t"
"v_pk_add_f32 v[114:115], v[114:115], v[162:163]\n\t"
"v_pk_fma_f32 v[160:161], v[160:161], s[38:39], v[112:113]\n\t"
"v_pk_fma_f32 v[162:163], v[162:163], s[38:39], v[114:115]\n\t"
"v_pk_add_f32 v[128:129], v[128:129], v[176:177]\n\t"
"v_pk_add_f32 v[130:131], v[130:131], v[178:179]\n\t"
"v_pk_fma_f32 v[176:177], v[176:177], s[38:39], v[128:129]\n\t"
"v_pk_fma_f32 v[178:179], v[178:179], s[38:39], v[130:131]\n\t"
"v_pk_add_f32 v[132:133], v[132:133], v[180:181]\n\t"
"v_pk_add_f32 v[134:135], v[134:135], v[182:183]\n\t"
"v_pk_fma_f32 v[180:181], v[180:181], s[38:39], v[132:133]\n\t"
"v_pk_fma_f32 v[182:183], v[182:183], s[38:39], v[134:135]\n\t"
"v_pk_add_f32 v[116:117], v[116:117], v[164:165]\n\t"
"v_pk_add_f32 v[118:119], v[118:119], v[166:167]\n\t"
"v_pk_fma_f32 v[164:165], v[164:165], s[38:39], v[116:117]\n\t"
"v_pk_fma_f32 v[166:167], v[166:167], s[38:39], v[118:119]\n\t"
"v_pk_add_f32 v[100:101], v[100:101], v[148:149]\n\t"
"v_pk_add_f32 v[102:103], v[102:103], v[150:151]\n\t"
"v_pk_fma_f32 v[148:149], v[148:149], s[38:39], v[100:101]\n\t"
"v_pk_fma_f32 v[150:151], v[150:151], s[38:39], v[102:103]\n\t"
"v_pk_add_f32 v[104:105], v[104:105], v[152:153]\n\t"
"v_pk_add_f32 v[106:107], v[106:107], v[154:155]\n\t"
"v_pk_fma_f32 v[152:153], v[152:153], s[38:39], v[104:105]\n\t"
"v_pk_fma_f32 v[154:155], v[154:155], s[38:39], v[106:107]\n\t"
"v_pk_add_f32 v[120:121], v[120:121], v[168:169]\n\t"
"v_pk_add_f32 v[122:123], v[122:123], v[170:171]\n\t"
"v_pk_fma_f32 v[168:169], v[168:169], s[38:39], v[120:121]\n\t"
"v_pk_fma_f32 v[170:171], v[170:171], s[38:39], v[122:123]\n\t"
"v_pk_add_f32 v[136:137], v[136:137], v[184:185]\n\t"
"v_pk_add_f32 v[138:139], v[138:139], v[186:187]\n\t"
"v_pk_fma_f32 v[184:185], v[184:185], s[38:39], v[136:137]\n\t"
"v_pk_fma_f32 v[186:187], v[186:187], s[38:39], v[138:139]\n\t"
"v_pk_add_f32 v[140:141], v[140:141], v[188:189]\n\t"
"v_pk_add_f32 v[142:143], v[142:143], v[190:191]\n\t"
"v_pk_fma_f32 v[188:189], v[188:189], s[38:39], v[140:141]\n\t"
"v_pk_fma_f32 v[190:191], v[190:191], s[38:39], v[142:143]\n\t"
"v_pk_add_f32 v[124:125], v[124:125], v[172:173]\n\t"
"v_pk_add_f32 v[126:127], v[126:127], v[174:175]\n\t"
"v_pk_fma_f32 v[172:173], v[172:173], s[38:39], v[124:125]\n\t"
"v_pk_fma_f32 v[174:175], v[174:175], s[38:39], v[126:127]\n\t"
"v_pk_add_f32 v[108:109], v[108:109], v[156:157]\n\t"
"v_pk_add_f32 v[110:111], v[110:111], v[158:159]\n\t"
"v_pk_fma_f32 v[156:157], v[156:157], s[38:39], v[108:109]\n\t"
"v_pk_fma_f32 v[158:159], v[158:159], s[38:39], v[110:111]\n\t"
"v_cvt_pk_bf16_f32 v112, v112, v113\n\t"
"v_cvt_pk_bf16_f32 v113, v114, v115\n\t"
"v_cvt_pk_bf16_f32 v116, v116, v117\n\t"
"v_cvt_pk_bf16_f32 v132, v132, v133\n\t"
"v_cvt_pk_bf16_f32 v108, v108, v109\n\t"
"v_mfma_f32_16x16x32_bf16 v[64:67], a[8:11], a[144:147], v[64:67]\n\t"
"v_cvt_pk_bf16_f32 v208, v208, v209\n\t"
"v_cvt_pk_bf16_f32 v96, v96, v97\n\t"
"v_mfma_f32_16x16x32_bf16 v[64:67], a[12:15], a[160:163], v[64:67]\n\t"
"buffer_load_dwordx4 a[104:107], v235, s[84:87], s57 offen offset:2048 sc1\n\t"
"buffer_load_dwordx4 a[96:99], v235, s[84:87], s57 offen offset:0 sc1\n\t"
"v_cvt_pk_bf16_f32 v209, v210, v211\n\t"
"v_cvt_pk_bf16_f32 v100, v100, v101\n\t"
"v_cvt_pk_bf16_f32 v117, v118, v119\n\t"
"v_cvt_pk_bf16_f32 v109, v110, v111\n\t"
"v_mfma_f32_16x16x32_bf16 v[80:83], a[16:19], a[144:147], v[80:83]\n\t"
"v_cvt_pk_bf16_f32 v97, v98, v99\n\t"
"v_cvt_pk_bf16_f32 v101, v102, v103\n\t"
"v_mfma_f32_16x16x32_bf16 v[80:83], a[20:23], a[160:163], v[80:83]\n\t"
"s_nop 0\n\t"
"ds_read_b128 a[172:175], v216 offset:11264\n\t"
"ds_read_b128 a[144:147], v216 offset:4096\n\t"
"ds_read_b128 a[160:163], v216 offset:12288\n\t"
"ds_read_b64_tr_b16 a[190:191], v251 offset:44288\n\t"
"ds_write2st64_b64 v228, v[96:97], v[100:101] offset0:39 offset1:52\n\t"
"v_cvt_pk_bf16_f32 v133, v134, v135\n\t"
"v_mfma_f32_16x16x32_bf16 v[192:195], a[192:195], a[224:227], 0\n\t"
"buffer_load_dwordx4 a[100:103], v235, s[84:87], s57 offen offset:1024 sc1\n\t"
"v_mfma_f32_16x16x32_bf16 v[84:87], a[16:19], a[148:151], v[84:87]\n\t"
"v_cvt_pk_bf16_f32 v128, v128, v129\n\t"
"v_cvt_pk_bf16_f32 v124, v124, v125\n\t"
"v_mfma_f32_16x16x32_bf16 v[84:87], a[20:23], a[164:167], v[84:87]\n\t"
"ds_write2st64_b64 v246, v[112:113], v[116:117] offset0:39 offset1:52\n\t"
"v_mfma_f32_16x16x32_bf16 v[68:71], a[8:11], a[148:151], v[68:71]\n\t"
"v_cvt_pk_bf16_f32 v210, v212, v213\n\t"
"v_cvt_pk_bf16_f32 v129, v130, v131\n\t"
"v_mfma_f32_16x16x32_bf16 v[68:71], a[12:15], a[164:167], v[68:71]\n\t"
"buffer_load_dwordx4 a[136:139], v218, s[84:87], s63 offen offset:0 sc1\n\t"
"v_mfma_f32_16x16x32_bf16 v[196:199], a[196:199], a[224:227], 0\n\t"
"ds_write2st64_b64 v247, v[128:129], v[132:133] offset0:39 offset1:52\n\t"
"v_mfma_f32_16x16x32_bf16 v[52:55], a[0:3], a[148:151], v[52:55]\n\t"
"v_cvt_pk_bf16_f32 v211, v214, v215\n\t"
"v_cvt_pk_bf16_f32 v192, v192, v193\n\t"
"v_mfma_f32_16x16x32_bf16 v[52:55], a[4:7], a[164:167], v[52:55]\n\t"
"ds_write_b128 v221, v[208:211] offset:24576\n\t"
"v_mfma_f32_16x16x32_bf16 v[200:203], a[200:203], a[224:227], 0\n\t"
"s_waitcnt lgkmcnt(8)\n\t"
"v_cvt_pk_bf16_f32 v104, v104, v105\n\t"
"v_cvt_pk_bf16_f32 v105, v106, v107\n\t"
"v_mfma_f32_16x16x32_bf16 v[56:59], a[0:3], a[152:155], v[56:59]\n\t"
"v_cvt_pk_bf16_f32 v193, v194, v195\n\t"
"v_cvt_pk_bf16_f32 v120, v120, v121\n\t"
"v_mfma_f32_16x16x32_bf16 v[56:59], a[4:7], a[168:171], v[56:59]\n\t"
"s_nop 0\n\t"
"ds_read_b128 a[148:151], v216 offset:5120\n\t"
"ds_read_b128 a[164:167], v216 offset:13312\n\t"
"ds_read_b64_tr_b16 a[192:193], v250 offset:41472\n\t"
"ds_read_b64_tr_b16 a[194:195], v250 offset:41984\n\t"
"ds_write2st64_b64 v228, v[104:105], v[108:109] offset0:65 offset1:78\n\t"
"v_mfma_f32_16x16x32_bf16 v[72:75], a[8:11], a[152:155], v[72:75]\n\t"
"v_cvt_pk_bf16_f32 v121, v122, v123\n\t"
"v_cvt_pk_bf16_f32 v125, v126, v127\n\t"
"v_mfma_f32_16x16x32_bf16 v[72:75], a[12:15], a[168:171], v[72:75]\n\t"
"buffer_load_dwordx4 a[140:143], v218, s[84:87], s63 offen offset:1024 sc1\n\t"
"v_mfma_f32_16x16x32_bf16 v[204:207], a[204:207], a[224:227], 0\n\t"
"ds_write2st64_b64 v246, v[120:121], v[124:125] offset0:65 offset1:78\n\t"
"v_mfma_f32_16x16x32_bf16 v[88:91], a[16:19], a[152:155], v[88:91]\n\t"
"v_cvt_pk_bf16_f32 v194, v196, v197\n\t"
"v_cvt_pk_bf16_f32 v195, v198, v199\n\t"
"v_mfma_f32_16x16x32_bf16 v[88:91], a[20:23], a[168:171], v[88:91]\n\t"
"s_waitcnt lgkmcnt(7)\n\t"
"buffer_load_dwordx4 a[132:135], v235, s[84:87], s63 offen offset:3072 sc1\n\t"
"v_mfma_f32_16x16x32_bf16 v[92:95], a[16:19], a[156:159], v[92:95]\n\t"
"v_cvt_pk_bf16_f32 v200, v200, v201\n\t"
"v_cvt_pk_bf16_f32 v201, v202, v203\n\t"
"v_mfma_f32_16x16x32_bf16 v[92:95], a[20:23], a[172:175], v[92:95]\n\t"
"ds_read_b64_tr_b16 a[196:197], v251 offset:41472\n\t"
"ds_write_b128 v221, v[192:195] offset:20480\n\t"
"v_mfma_f32_16x16x32_bf16 v[76:79], a[8:11], a[156:159], v[76:79]\n\t"
"v_cvt_pk_bf16_f32 v136, v136, v137\n\t"
"v_cvt_pk_bf16_f32 v140, v140, v141\n\t"
"v_mfma_f32_16x16x32_bf16 v[76:79], a[12:15], a[172:175], v[76:79]\n\t"
"buffer_load_dwordx4 a[128:131], v235, s[84:87], s63 offen offset:2048 sc1\n\t"
"v_mfma_f32_16x16x32_bf16 v[60:63], a[0:3], a[156:159], v[60:63]\n\t"
"v_cvt_pk_bf16_f32 v137, v138, v139\n\t"
"v_cvt_pk_bf16_f32 v141, v142, v143\n\t"
"v_mfma_f32_16x16x32_bf16 v[60:63], a[4:7], a[172:175], v[60:63]\n\t"
"s_waitcnt vmcnt(25) lgkmcnt(3)\n\t"
"ds_read_b64_tr_b16 a[198:199], v251 offset:41984\n\t"
"ds_write2st64_b64 v247, v[136:137], v[140:141] offset0:65 offset1:78\n\t"
"v_mfma_f32_16x16x32_bf16 v[144:147], a[24:27], a[144:147], v[144:147]\n\t"
"v_cvt_pk_bf16_f32 v202, v204, v205\n\t"
"v_cvt_pk_bf16_f32 v203, v206, v207\n\t"
"v_mfma_f32_16x16x32_bf16 v[144:147], a[28:31], a[160:163], v[144:147]\n\t"
"ds_write_b128 v221, v[200:203] offset:28672\n\t"
"v_mfma_f32_16x16x32_bf16 v[160:163], a[32:35], a[144:147], v[160:163]\n\t"
"buffer_load_dwordx4 a[120:123], v235, s[84:87], s63 offen offset:0 sc1\n\t"
"v_mfma_f32_16x16x32_bf16 v[160:163], a[36:39], a[160:163], v[160:163]\n\t"
"s_nop 0\n\t"
"ds_read_b128 a[156:159], v216 offset:7168\n\t"
"ds_read_b64_tr_b16 a[200:201], v248 offset:43776\n\t"
"v_mfma_f32_16x16x32_bf16 v[176:179], a[40:43], a[144:147], v[176:179]\n\t"
"buffer_load_dwordx4 a[124:127], v235, s[84:87], s63 offen offset:1024 sc1\n\t"
"v_mfma_f32_16x16x32_bf16 v[176:179], a[44:47], a[160:163], v[176:179]\n\t"
"s_nop 0\n\t"
"ds_read_b128 a[168:171], v216 offset:14336\n\t"
"ds_read_b64_tr_b16 a[202:203], v248 offset:44288\n\t"
"v_mfma_f32_16x16x32_bf16 v[180:183], a[40:43], a[148:151], v[180:183]\n\t"
"s_nop 0\n\t"
"ds_read_b64_tr_b16 a[204:205], v249 offset:43776\n\t"
"ds_read_b64_tr_b16 a[206:207], v249 offset:44288\n\t"
"v_mfma_f32_16x16x32_bf16 v[180:183], a[44:47], a[164:167], v[180:183]\n\t"
"buffer_load_dwordx4 a[16:19], v218, s[84:87], s42 offen offset:0 sc1\n\t"
"v_mfma_f32_16x16x32_bf16 v[164:167], a[32:35], a[148:151], v[164:167]\n\t"
"s_nop 0\n\t"
"ds_read_b128 a[152:155], v216 offset:6144\n\t"
"ds_read_b128 a[172:175], v216 offset:15360\n\t"
"v_mfma_f32_16x16x32_bf16 v[164:167], a[36:39], a[164:167], v[164:167]\n\t"
"buffer_load_dwordx4 a[20:23], v218, s[84:87], s42 offen offset:1024 sc1\n\t"
"v_mfma_f32_16x16x32_bf16 v[148:151], a[24:27], a[148:151], v[148:151]\n\t"
"v_mfma_f32_16x16x32_bf16 v[148:151], a[28:31], a[164:167], v[148:151]\n\t"
"s_waitcnt lgkmcnt(0)\n\t"
"s_barrier\n\t"
"v_mfma_f32_16x16x32_bf16 v[152:155], a[24:27], a[152:155], v[152:155]\n\t"
"s_nop 0\n\t"
"ds_read_b128 a[144:147], v216 offset:16384\n\t"
"ds_read_b128 a[148:151], v216 offset:17408\n\t"
"v_mfma_f32_16x16x32_bf16 v[152:155], a[28:31], a[168:171], v[152:155]\n\t"
"buffer_load_dwordx4 a[12:15], v235, s[84:87], s42 offen offset:3072 sc1\n\t"
"v_mfma_f32_16x16x32_bf16 v[168:171], a[32:35], a[152:155], v[168:171]\n\t"
"s_nop 0\n\t"
"ds_read_b128 a[160:163], v216 offset:24576\n\t"
"ds_read_b128 a[164:167], v216 offset:25600\n\t"
"v_mfma_f32_16x16x32_bf16 v[168:171], a[36:39], a[168:171], v[168:171]\n\t"
"ds_read_b128 v[104:107], v229 offset:19968\n\t"
"v_mfma_f32_16x16x32_bf16 v[184:187], a[40:43], a[152:155], v[184:187]\n\t"
"ds_read_b128 a[152:155], v216 offset:18432\n\t"
"v_mfma_f32_16x16x32_bf16 v[184:187], a[44:47], a[168:171], v[184:187]\n\t"
"buffer_load_dwordx4 a[8:11], v235, s[84:87], s42 offen offset:2048 sc1\n\t"
"v_mfma_f32_16x16x32_bf16 v[208:211], a[176:179], a[220:223], 0\n\t"
"s_nop 0\n\t"
"ds_read_b128 a[168:171], v216 offset:26624\n\t"
"ds_read_b128 v[120:123], v230 offset:19968\n\t"
"v_mfma_f32_16x16x32_bf16 v[188:191], a[40:43], a[156:159], v[188:191]\n\t"
"s_nop 0\n\t"
"ds_read_b64_tr_b16 a[176:177], v248 offset:0\n\t"
"ds_read_b64_tr_b16 a[178:179], v248 offset:512\n\t"
"v_mfma_f32_16x16x32_bf16 v[188:191], a[44:47], a[172:175], v[188:191]\n\t"
"buffer_load_dwordx4 a[0:3], v235, s[84:87], s42 offen offset:0 sc1\n\t"
"v_mfma_f32_16x16x32_bf16 v[212:215], a[180:183], a[220:223], 0\n\t"
"ds_read_b64_tr_b16 a[180:181], v249 offset:0\n\t"
"v_mfma_f32_16x16x32_bf16 v[172:175], a[32:35], a[156:159], v[172:175]\n\t"
"ds_read_b64_tr_b16 a[182:183], v249 offset:512\n\t"
"v_cvt_pk_bf16_f32 v208, v208, v209\n\t"
"v_mfma_f32_16x16x32_bf16 v[172:175], a[36:39], a[172:175], v[172:175]\n\t"
"s_waitcnt lgkmcnt(7)\n\t"
"buffer_store_dwordx4 v[104:107], v232, s[16:19], s20 offen nt\n\t"
"v_mfma_f32_16x16x32_bf16 v[156:159], a[24:27], a[156:159], v[156:159]\n\t"
"ds_read_b128 v[136:139], v231 offset:19968\n\t"
"v_cvt_pk_bf16_f32 v209, v210, v211\n\t"
"v_mfma_f32_16x16x32_bf16 v[156:159], a[28:31], a[172:175], v[156:159]\n\t"
"ds_read_b128 a[172:175], v216 offset:27648\n\t"
"v_cvt_pk_bf16_f32 v210, v212, v213\n\t"
"v_mfma_f32_16x16x32_bf16 v[192:195], a[184:187], a[220:223], 0\n\t"
"s_waitcnt vmcnt(24) lgkmcnt(8)\n\t"
"buffer_load_dwordx4 a[4:7], v235, s[84:87], s42 offen offset:1024 sc1\n\t"
"v_mfma_f32_16x16x32_bf16 v[48:51], a[48:51], a[144:147], v[48:51]\n\t"
"ds_read_b128 a[156:159], v216 offset:19456\n\t"
"v_cvt_pk_bf16_f32 v211, v214, v215\n\t"
"v_mfma_f32_16x16x32_bf16 v[48:51], a[52:55], a[160:163], v[48:51]\n\t"
"s_nop 0\n\t"
"ds_read_b64_tr_b16 a[184:185], v250 offset:2304\n\t"
"ds_read_b64_tr_b16 a[186:187], v250 offset:2816\n\t"
"v_mfma_f32_16x16x32_bf16 v[196:199], a[188:191], a[220:223], 0\n\t"
"s_waitcnt lgkmcnt(7)\n\t"
"buffer_store_dwordx4 v[120:123], v233, s[16:19], s20 offen nt\n\t"
"v_mfma_f32_16x16x32_bf16 v[64:67], a[56:59], a[144:147], v[64:67]\n\t"
"ds_read_b64_tr_b16 a[188:189], v251 offset:2304\n\t"
"v_cvt_pk_bf16_f32 v192, v192, v193\n\t"
"v_mfma_f32_16x16x32_bf16 v[64:67], a[60:63], a[160:163], v[64:67]\n\t"
"ds_read_b64_tr_b16 a[190:191], v251 offset:2816\n\t"
"v_cvt_pk_bf16_f32 v193, v194, v195\n\t"
"v_mfma_f32_16x16x32_bf16 v[80:83], a[64:67], a[144:147], v[80:83]\n\t"
"s_nop 0\n\t"
"ds_read_b128 a[144:147], v216 offset:20480\n\t"
"ds_read_b128 v[108:111], v229 offset:33280\n\t"
"v_mfma_f32_16x16x32_bf16 v[80:83], a[68:71], a[160:163], v[80:83]\n\t"
"buffer_load_dwordx4 a[40:43], v218, s[84:87], s48 offen offset:0 sc1\n\t"
"v_mfma_f32_16x16x32_bf16 v[200:203], a[192:195], a[220:223], 0\n\t"
"s_nop 0\n\t"
"ds_read_b128 a[160:163], v216 offset:28672\n\t"
"ds_read_b64_tr_b16 a[192:193], v250 offset:0\n\t"
"v_mfma_f32_16x16x32_bf16 v[84:87], a[64:67], a[148:151], v[84:87]\n\t"
"buffer_load_dwordx4 a[44:47], v218, s[84:87], s48 offen offset:1024 sc1\n\t"
"v_mfma_f32_16x16x32_bf16 v[84:87], a[68:71], a[164:167], v[84:87]\n\t"
"ds_write_b128 v217, v[208:211] offset:0\n\t"
"v_mfma_f32_16x16x32_bf16 v[68:71], a[56:59], a[148:151], v[68:71]\n\t"
"v_cvt_pk_bf16_f32 v194, v196, v197\n\t"
"v_cvt_pk_bf16_f32 v195, v198, v199\n\t"
"v_mfma_f32_16x16x32_bf16 v[68:71], a[60:63], a[164:167], v[68:71]\n\t"
"s_waitcnt lgkmcnt(7)\n\t"
"buffer_store_dwordx4 v[136:139], v234, s[16:19], s20 offen nt\n\t"
"v_mfma_f32_16x16x32_bf16 v[204:207], a[196:199], a[220:223], 0\n\t"
"ds_write_b128 v219, v[192:195] offset:0\n\t"
"v_mfma_f32_16x16x32_bf16 v[52:55], a[48:51], a[148:151], v[52:55]\n\t"
"v_cvt_pk_bf16_f32 v200, v200, v201\n\t"
"v_cvt_pk_bf16_f32 v201, v202, v203\n\t"
"v_mfma_f32_16x16x32_bf16 v[52:55], a[52:55], a[164:167], v[52:55]\n\t"
"buffer_load_dwordx4 a[36:39], v235, s[84:87], s48 offen offset:3072 sc1\n\t"
"v_mfma_f32_16x16x32_bf16 v[208:211], a[200:203], a[220:223], 0\n\t"
"buffer_load_dwordx4 a[32:35], v235, s[84:87], s48 offen offset:2048 sc1\n\t"
"v_mfma_f32_16x16x32_bf16 v[56:59], a[48:51], a[152:155], v[56:59]\n\t"
"s_nop 0\n\t"
"ds_read_b128 a[148:151], v216 offset:21504\n\t"
"ds_read_b64_tr_b16 a[194:195], v250 offset:512\n\t"
"v_mfma_f32_16x16x32_bf16 v[56:59], a[52:55], a[168:171], v[56:59]\n\t"
"ds_read_b128 a[232:235], v230 offset:33280\n\t"
"buffer_load_dwordx4 a[24:27], v235, s[84:87], s48 offen offset:0 sc1\n\t"
"v_mfma_f32_16x16x32_bf16 v[72:75], a[56:59], a[152:155], v[72:75]\n\t"
"ds_read_b128 a[164:167], v216 offset:29696\n\t"
"v_cvt_pk_bf16_f32 v202, v204, v205\n\t"
"v_mfma_f32_16x16x32_bf16 v[72:75], a[60:63], a[168:171], v[72:75]\n\t"
"ds_read_b128 a[236:239], v231 offset:33280\n\t"
"v_cvt_pk_bf16_f32 v208, v208, v209\n\t"
"v_mfma_f32_16x16x32_bf16 v[212:215], a[204:207], a[220:223], 0\n\t"
"s_waitcnt lgkmcnt(6)\n\t"
"buffer_store_dwordx4 v[108:111], v232, s[16:19], s25 offen nt\n\t"
"v_mfma_f32_16x16x32_bf16 v[88:91], a[64:67], a[152:155], v[88:91]\n\t"
"v_cvt_pk_bf16_f32 v203, v206, v207\n\t"
"v_cvt_pk_bf16_f32 v209, v210, v211\n\t"
"v_mfma_f32_16x16x32_bf16 v[88:91], a[68:71], a[168:171], v[88:91]\n\t"
"ds_write_b128 v217, v[200:203] offset:8192\n\t"
"v_mfma_f32_16x16x32_bf16 v[92:95], a[64:67], a[156:159], v[92:95]\n\t"
"buffer_load_dwordx4 a[28:31], v235, s[84:87], s48 offen offset:1024 sc1\n\t"
"v_mfma_f32_16x16x32_bf16 v[92:95], a[68:71], a[172:175], v[92:95]\n\t"
"v_cvt_pk_bf16_f32 v210, v212, v213\n\t"
"v_cvt_pk_bf16_f32 v211, v214, v215\n\t"
"v_mfma_f32_16x16x32_bf16 v[76:79], a[56:59], a[156:159], v[76:79]\n\t"
"buffer_load_dwordx4 a[64:67], v218, s[84:87], s75 offen offset:0 sc1\n\t"
"v_mfma_f32_16x16x32_bf16 v[76:79], a[60:63], a[172:175], v[76:79]\n\t"
"ds_write_b128 v219, v[208:211] offset:8192\n\t"
"v_mfma_f32_16x16x32_bf16 v[60:63], a[48:51], a[156:159], v[60:63]\n\t"
"buffer_load_dwordx4 a[68:71], v218, s[84:87], s75 offen offset:1024 sc1\n\t"
"v_mfma_f32_16x16x32_bf16 v[60:63], a[52:55], a[172:175], v[60:63]\n\t"
"s_waitcnt vmcnt(30) lgkmcnt(3)\n\t"
"buffer_store_dwordx4 a[232:235], v233, s[16:19], s25 offen nt\n\t"
"v_mfma_f32_16x16x32_bf16 v[144:147], a[72:75], a[144:147], v[144:147]\n\t"
"v_mfma_f32_16x16x32_bf16 v[144:147], a[76:79], a[160:163], v[144:147]\n\t"
"s_nop 0\n\t"
"ds_read_b128 a[156:159], v216 offset:23552\n\t"
"ds_read_b64_tr_b16 a[196:197], v251 offset:0\n\t"
"v_mfma_f32_16x16x32_bf16 v[160:163], a[80:83], a[144:147], v[160:163]\n\t"
"s_nop 0\n\t"
"ds_read_b64_tr_b16 a[198:199], v251 offset:512\n\t"
"ds_read_b64_tr_b16 a[200:201], v248 offset:2304\n\t"
"v_mfma_f32_16x16x32_bf16 v[160:163], a[84:87], a[160:163], v[160:163]\n\t"
"s_waitcnt lgkmcnt(5)\n\t"
"buffer_store_dwordx4 a[236:239], v234, s[16:19], s25 offen nt\n\t"
"v_mfma_f32_16x16x32_bf16 v[176:179], a[88:91], a[144:147], v[176:179]\n\t"
"s_nop 0\n\t"
"ds_read_b128 a[168:171], v216 offset:30720\n\t"
"ds_read_b64_tr_b16 a[202:203], v248 offset:2816\n\t"
"v_mfma_f32_16x16x32_bf16 v[176:179], a[92:95], a[160:163], v[176:179]\n\t"
"s_nop 0\n\t"
"ds_read_b128 a[172:175], v216 offset:31744\n\t"
"ds_read_b64_tr_b16 a[204:205], v249 offset:2304\n\t"
"v_mfma_f32_16x16x32_bf16 v[180:183], a[88:91], a[148:151], v[180:183]\n\t"
"buffer_load_dwordx4 a[60:63], v235, s[84:87], s75 offen offset:3072 sc1\n\t"
"v_mfma_f32_16x16x32_bf16 v[180:183], a[92:95], a[164:167], v[180:183]\n\t"
"s_nop 0\n\t"
"ds_read_b128 a[152:155], v216 offset:22528\n\t"
"ds_read_b64_tr_b16 a[206:207], v249 offset:2816\n\t"
"v_mfma_f32_16x16x32_bf16 v[164:167], a[80:83], a[148:151], v[164:167]\n\t"
"buffer_load_dwordx4 a[56:59], v235, s[84:87], s75 offen offset:2048 sc1\n\t"
"v_mfma_f32_16x16x32_bf16 v[164:167], a[84:87], a[164:167], v[164:167]\n\t"
"v_mfma_f32_16x16x32_bf16 v[148:151], a[72:75], a[148:151], v[148:151]\n\t"
"buffer_load_dwordx4 a[48:51], v235, s[84:87], s75 offen offset:0 sc1\n\t"
"v_mfma_f32_16x16x32_bf16 v[148:151], a[76:79], a[164:167], v[148:151]\n\t"
"s_waitcnt lgkmcnt(0)\n\t"
"s_barrier\n\t"
"v_mfma_f32_16x16x32_bf16 v[152:155], a[72:75], a[152:155], v[152:155]\n\t"
"ds_read_b128 a[148:151], v216 offset:1024\n\t"
"v_add_u32 v248, 53504, v226\n\t"
"v_mfma_f32_16x16x32_bf16 v[152:155], a[76:79], a[168:171], v[152:155]\n\t"
"ds_read_b128 a[144:147], v216 offset:0\n\t"
"v_xor_b32 v249, 32, v248\n\t"
"v_mfma_f32_16x16x32_bf16 v[168:171], a[80:83], a[152:155], v[168:171]\n\t"
"ds_read_b128 a[160:163], v216 offset:8192\n\t"
"v_xor_b32 v250, 64, v248\n\t"
"v_mfma_f32_16x16x32_bf16 v[168:171], a[84:87], a[168:171], v[168:171]\n\t"
"ds_read_b128 a[164:167], v216 offset:9216\n\t"
"v_xor_b32 v251, 96, v248\n\t"
"v_mfma_f32_16x16x32_bf16 v[184:187], a[88:91], a[152:155], v[184:187]\n\t"
"ds_read_b128 a[152:155], v216 offset:2048\n\t"
"v_mfma_f32_16x16x32_bf16 v[184:187], a[92:95], a[168:171], v[184:187]\n\t"
"buffer_load_dwordx4 a[52:55], v235, s[84:87], s75 offen offset:1024 sc1\n\t"
"v_mfma_f32_16x16x32_bf16 v[192:195], a[176:179], a[220:223], 0\n\t"
"ds_read_b64_tr_b16 a[176:177], v248 offset:0\n\t"
"v_mfma_f32_16x16x32_bf16 v[188:191], a[88:91], a[156:159], v[188:191]\n\t"
"ds_read_b64_tr_b16 a[178:179], v248 offset:512\n\t"
"v_mfma_f32_16x16x32_bf16 v[188:191], a[92:95], a[172:175], v[188:191]\n\t"
"buffer_load_dwordx4 a[88:91], v218, s[84:87], s78 offen offset:0 sc1\n\t"
"v_mfma_f32_16x16x32_bf16 v[196:199], a[180:183], a[220:223], 0\n\t"
"ds_read_b64_tr_b16 a[180:181], v249 offset:0\n\t"
"v_mfma_f32_16x16x32_bf16 v[172:175], a[80:83], a[156:159], v[172:175]\n\t"
"ds_read_b64_tr_b16 a[182:183], v249 offset:512\n\t"
"v_cvt_pk_bf16_f32 v192, v192, v193\n\t"
"v_mfma_f32_16x16x32_bf16 v[172:175], a[84:87], a[172:175], v[172:175]\n\t"
"buffer_load_dwordx4 a[92:95], v218, s[84:87], s78 offen offset:1024 sc1\n\t"
"v_mfma_f32_16x16x32_bf16 v[156:159], a[72:75], a[156:159], v[156:159]\n\t"
"ds_read_b128 a[168:171], v216 offset:10240\n\t"
"v_cvt_pk_bf16_f32 v193, v194, v195\n\t"
"v_mfma_f32_16x16x32_bf16 v[156:159], a[76:79], a[172:175], v[156:159]\n\t"
"buffer_load_dwordx4 a[84:87], v235, s[84:87], s78 offen offset:3072 sc1\n\t"
"v_mfma_f32_16x16x32_bf16 v[200:203], a[184:187], a[220:223], 0\n\t"
"s_waitcnt vmcnt(33) lgkmcnt(5)\n\t"
"ds_read_b128 a[156:159], v216 offset:3072\n\t"
"ds_read_b128 a[172:175], v216 offset:11264\n\t"
"v_pk_add_f32 v[144:145], v[48:49], v[144:145] neg_lo:[0,1] neg_hi:[0,1]\n\t"
"v_pk_add_f32 v[146:147], v[50:51], v[146:147] neg_lo:[0,1] neg_hi:[0,1]\n\t"
"v_pk_fma_f32 v[48:49], v[144:145], s[90:91], v[48:49] neg_lo:[1,0,0] neg_hi:[1,0,0]\n\t"
"v_pk_fma_f32 v[50:51], v[146:147], s[90:91], v[50:51] neg_lo:[1,0,0] neg_hi:[1,0,0]\n\t"
"v_pk_add_f32 v[160:161], v[64:65], v[160:161] neg_lo:[0,1] neg_hi:[0,1]\n\t"
"v_pk_add_f32 v[162:163], v[66:67], v[162:163] neg_lo:[0,1] neg_hi:[0,1]\n\t"
"v_pk_fma_f32 v[64:65], v[160:161], s[90:91], v[64:65] neg_lo:[1,0,0] neg_hi:[1,0,0]\n\t"
"v_pk_fma_f32 v[66:67], v[162:163], s[90:91], v[66:67] neg_lo:[1,0,0] neg_hi:[1,0,0]\n\t"
"v_pk_add_f32 v[176:177], v[80:81], v[176:177] neg_lo:[0,1] neg_hi:[0,1]\n\t"
"v_pk_add_f32 v[178:179], v[82:83], v[178:179] neg_lo:[0,1] neg_hi:[0,1]\n\t"
"v_pk_fma_f32 v[80:81], v[176:177], s[90:91], v[80:81] neg_lo:[1,0,0] neg_hi:[1,0,0]\n\t"
"v_pk_fma_f32 v[82:83], v[178:179], s[90:91], v[82:83] neg_lo:[1,0,0] neg_hi:[1,0,0]\n\t"
"v_pk_add_f32 v[180:181], v[84:85], v[180:181] neg_lo:[0,1] neg_hi:[0,1]\n\t"
"v_pk_add_f32 v[182:183], v[86:87], v[182:183] neg_lo:[0,1] neg_hi:[0,1]\n\t"
"v_pk_fma_f32 v[84:85], v[180:181], s[90:91], v[84:85] neg_lo:[1,0,0] neg_hi:[1,0,0]\n\t"
"v_pk_fma_f32 v[86:87], v[182:183], s[90:91], v[86:87] neg_lo:[1,0,0] neg_hi:[1,0,0]\n\t"
"v_pk_add_f32 v[164:165], v[68:69], v[164:165] neg_lo:[0,1] neg_hi:[0,1]\n\t"
"v_pk_add_f32 v[166:167], v[70:71], v[166:167] neg_lo:[0,1] neg_hi:[0,1]\n\t"
"v_pk_fma_f32 v[68:69], v[164:165], s[90:91], v[68:69] neg_lo:[1,0,0] neg_hi:[1,0,0]\n\t"
"v_pk_fma_f32 v[70:71], v[166:167], s[90:91], v[70:71] neg_lo:[1,0,0] neg_hi:[1,0,0]\n\t"
"v_pk_add_f32 v[148:149], v[52:53], v[148:149] neg_lo:[0,1] neg_hi:[0,1]\n\t"
"v_pk_add_f32 v[150:151], v[54:55], v[150:151] neg_lo:[0,1] neg_hi:[0,1]\n\t"
"v_pk_fma_f32 v[52:53], v[148:149], s[90:91], v[52:53] neg_lo:[1,0,0] neg_hi:[1,0,0]\n\t"
"v_pk_fma_f32 v[54:55], v[150:151], s[90:91], v[54:55] neg_lo:[1,0,0] neg_hi:[1,0,0]\n\t"
"v_pk_add_f32 v[152:153], v[56:57], v[152:153] neg_lo:[0,1] neg_hi:[0,1]\n\t"
"v_pk_add_f32 v[154:155], v[58:59], v[154:155] neg_lo:[0,1] neg_hi:[0,1]\n\t"
"v_pk_fma_f32 v[56:57], v[152:153], s[90:91], v[56:57] neg_lo:[1,0,0] neg_hi:[1,0,0]\n\t"
"v_pk_fma_f32 v[58:59], v[154:155], s[90:91], v[58:59] neg_lo:[1,0,0] neg_hi:[1,0,0]\n\t"
"v_pk_add_f32 v[168:169], v[72:73], v[168:169] neg_lo:[0,1] neg_hi:[0,1]\n\t"
"v_pk_add_f32 v[170:171], v[74:75], v[170:171] neg_lo:[0,1] neg_hi:[0,1]\n\t"
"v_pk_fma_f32 v[72:73], v[168:169], s[90:91], v[72:73] neg_lo:[1,0,0] neg_hi:[1,0,0]\n\t"
"v_pk_fma_f32 v[74:75], v[170:171], s[90:91], v[74:75] neg_lo:[1,0,0] neg_hi:[1,0,0]\n\t"
"v_pk_add_f32 v[184:185], v[88:89], v[184:185] neg_lo:[0,1] neg_hi:[0,1]\n\t"
"v_pk_add_f32 v[186:187], v[90:91], v[186:187] neg_lo:[0,1] neg_hi:[0,1]\n\t"
"v_pk_fma_f32 v[88:89], v[184:185], s[90:91], v[88:89] neg_lo:[1,0,0] neg_hi:[1,0,0]\n\t"
"v_pk_fma_f32 v[90:91], v[186:187], s[90:91], v[90:91] neg_lo:[1,0,0] neg_hi:[1,0,0]\n\t"
"v_pk_add_f32 v[188:189], v[92:93], v[188:189] neg_lo:[0,1] neg_hi:[0,1]\n\t"
"v_pk_add_f32 v[190:191], v[94:95], v[190:191] neg_lo:[0,1] neg_hi:[0,1]\n\t"
"v_pk_fma_f32 v[92:93], v[188:189], s[90:91], v[92:93] neg_lo:[1,0,0] neg_hi:[1,0,0]\n\t"
"v_pk_fma_f32 v[94:95], v[190:191], s[90:91], v[94:95] neg_lo:[1,0,0] neg_hi:[1,0,0]\n\t"
"v_pk_add_f32 v[172:173], v[76:77], v[172:173] neg_lo:[0,1] neg_hi:[0,1]\n\t"
"v_pk_add_f32 v[174:175], v[78:79], v[174:175] neg_lo:[0,1] neg_hi:[0,1]\n\t"
"v_pk_fma_f32 v[76:77], v[172:173], s[90:91], v[76:77] neg_lo:[1,0,0] neg_hi:[1,0,0]\n\t"
"v_pk_fma_f32 v[78:79], v[174:175], s[90:91], v[78:79] neg_lo:[1,0,0] neg_hi:[1,0,0]\n\t"
"v_pk_add_f32 v[156:157], v[60:61], v[156:157] neg_lo:[0,1] neg_hi:[0,1]\n\t"
"v_pk_add_f32 v[158:159], v[62:63], v[158:159] neg_lo:[0,1] neg_hi:[0,1]\n\t"
"v_pk_fma_f32 v[60:61], v[156:157], s[90:91], v[60:61] neg_lo:[1,0,0] neg_hi:[1,0,0]\n\t"
"v_pk_fma_f32 v[62:63], v[158:159], s[90:91], v[62:63] neg_lo:[1,0,0] neg_hi:[1,0,0]\n\t"
"v_mfma_f32_16x16x32_bf16 v[48:51], a[96:99], a[144:147], v[48:51]\n\t"
"ds_read_b64_tr_b16 a[184:185], v250 offset:2304\n\t"
"v_cvt_pk_bf16_f32 v194, v196, v197\n\t"
"v_mfma_f32_16x16x32_bf16 v[48:51], a[100:103], a[160:163], v[48:51]\n\t"
"buffer_load_dwordx4 a[80:83], v235, s[84:87], s78 offen offset:2048 sc1\n\t"
"v_mfma_f32_16x16x32_bf16 v[204:207], a[188:191], a[220:223], 0\n\t"
"ds_read_b64_tr_b16 a[186:187], v250 offset:2816\n\t"
"v_cvt_pk_bf16_f32 v195, v198, v199\n\t"
"v_mfma_f32_16x16x32_bf16 v[64:67], a[104:107], a[144:147], v[64:67]\n\t"
"ds_read_b64_tr_b16 a[188:189], v251 offset:2304\n\t"
"v_cvt_pk_bf16_f32 v200, v200, v201\n\t"
"v_mfma_f32_16x16x32_bf16 v[64:67], a[108:111], a[160:163], v[64:67]\n\t"
"buffer_load_dwordx4 a[72:75], v235, s[84:87], s78 offen offset:0 sc1\n\t"
"v_mfma_f32_16x16x32_bf16 v[80:83], a[112:115], a[144:147], v[80:83]\n\t"
"ds_read_b64_tr_b16 a[190:191], v251 offset:2816\n\t"
"v_cvt_pk_bf16_f32 v201, v202, v203\n\t"
"v_mfma_f32_16x16x32_bf16 v[80:83], a[116:119], a[160:163], v[80:83]\n\t"
"buffer_load_dwordx4 a[76:79], v235, s[84:87], s78 offen offset:1024 sc1\n\t"
"v_mfma_f32_16x16x32_bf16 v[208:211], a[192:195], a[220:223], 0\n\t"
"ds_write_b128 v217, v[192:195] offset:16384\n\t"
"v_mfma_f32_16x16x32_bf16 v[84:87], a[112:115], a[148:151], v[84:87]\n\t"
"v_cvt_pk_bf16_f32 v202, v204, v205\n\t"
"v_mfma_f32_16x16x32_bf16 v[84:87], a[116:119], a[164:167], v[84:87]\n\t"
"v_cvt_pk_bf16_f32 v203, v206, v207\n\t"
"v_mfma_f32_16x16x32_bf16 v[68:71], a[104:107], a[148:151], v[68:71]\n\t"
"v_mfma_f32_16x16x32_bf16 v[68:71], a[108:111], a[164:167], v[68:71]\n\t"
"s_nop 0\n\t"
"ds_read_b64_tr_b16 a[192:193], v250 offset:0\n\t"
"ds_read_b64_tr_b16 a[194:195], v250 offset:512\n\t"
"v_mfma_f32_16x16x32_bf16 v[212:215], a[196:199], a[220:223], 0\n\t"
"ds_read_b128 a[160:163], v216 offset:12288\n\t"
"v_cvt_pk_bf16_f32 v208, v208, v209\n\t"
"v_mfma_f32_16x16x32_bf16 v[52:55], a[96:99], a[148:151], v[52:55]\n\t"
"ds_read_b64_tr_b16 a[196:197], v251 offset:0\n\t"
"v_cvt_pk_bf16_f32 v209, v210, v211\n\t"
"v_mfma_f32_16x16x32_bf16 v[52:55], a[100:103], a[164:167], v[52:55]\n\t"
"s_nop 0\n\t"
"ds_read_b128 a[144:147], v216 offset:4096\n\t"
"ds_read_b64_tr_b16 a[198:199], v251 offset:512\n\t"
"v_mfma_f32_16x16x32_bf16 v[192:195], a[200:203], a[220:223], 0\n\t"
"s_waitcnt lgkmcnt(7)\n\t"
"ds_read_b128 a[148:151], v216 offset:5120\n\t"
"ds_read_b128 a[164:167], v216 offset:13312\n\t"
"v_mfma_f32_16x16x32_bf16 v[56:59], a[96:99], a[152:155], v[56:59]\n\t"
"v_cvt_pk_bf16_f32 v210, v212, v213\n\t"
"v_cvt_pk_bf16_f32 v211, v214, v215\n\t"
"v_mfma_f32_16x16x32_bf16 v[56:59], a[100:103], a[168:171], v[56:59]\n\t"
"ds_write_b128 v219, v[200:203] offset:16384\n\t"
"v_mfma_f32_16x16x32_bf16 v[72:75], a[104:107], a[152:155], v[72:75]\n\t"
"v_mfma_f32_16x16x32_bf16 v[72:75], a[108:111], a[168:171], v[72:75]\n\t"
"v_cvt_pk_bf16_f32 v192, v192, v193\n\t"
"v_mfma_f32_16x16x32_bf16 v[196:199], a[204:207], a[220:223], 0\n\t"
"ds_write_b128 v217, v[208:211] offset:24576\n\t"
"v_mfma_f32_16x16x32_bf16 v[88:91], a[112:115], a[152:155], v[88:91]\n\t"
"v_mfma_f32_16x16x32_bf16 v[88:91], a[116:119], a[168:171], v[88:91]\n\t"
"v_cvt_pk_bf16_f32 v193, v194, v195\n\t"
"v_mfma_f32_16x16x32_bf16 v[92:95], a[112:115], a[156:159], v[92:95]\n\t"
"buffer_load_dwordx4 a[112:115], v218, s[84:87], 0 offen offset:0 sc1\n\t"
"v_mfma_f32_16x16x32_bf16 v[92:95], a[116:119], a[172:175], v[92:95]\n\t"
"s_nop 0\n\t"
"ds_read_b64_tr_b16 a[200:201], v248 offset:2304\n\t"
"ds_read_b64_tr_b16 a[202:203], v248 offset:2816\n\t"
"v_mfma_f32_16x16x32_bf16 v[76:79], a[104:107], a[156:159], v[76:79]\n\t"
"ds_read_b64_tr_b16 a[204:205], v249 offset:2304\n\t"
"v_cvt_pk_bf16_f32 v194, v196, v197\n\t"
"v_mfma_f32_16x16x32_bf16 v[76:79], a[108:111], a[172:175], v[76:79]\n\t"
"buffer_load_dwordx4 a[116:119], v218, s[84:87], 0 offen offset:1024 sc1\n\t"
"v_mfma_f32_16x16x32_bf16 v[60:63], a[96:99], a[156:159], v[60:63]\n\t"
"ds_read_b64_tr_b16 a[206:207], v249 offset:2816\n\t"
"v_cvt_pk_bf16_f32 v195, v198, v199\n\t"
"v_mfma_f32_16x16x32_bf16 v[60:63], a[100:103], a[172:175], v[60:63]\n\t"
"s_waitcnt vmcnt(32) lgkmcnt(5)\n\t"
"ds_read_b128 a[172:175], v216 offset:15360\n\t"
"v_mfma_f32_16x16x32_bf16 v[144:147], a[120:123], a[144:147], v[144:147]\n\t"
"buffer_load_dwordx4 a[108:111], v235, s[84:87], 0 offen offset:3072 sc1\n\t"
"v_mfma_f32_16x16x32_bf16 v[144:147], a[124:127], a[160:163], v[144:147]\n\t"
"ds_write_b128 v219, v[192:195] offset:24576\n\t"
"v_mfma_f32_16x16x32_bf16 v[160:163], a[128:131], a[144:147], v[160:163]\n\t"
"buffer_load_dwordx4 a[104:107], v235, s[84:87], 0 offen offset:2048 sc1\n\t"
"v_mfma_f32_16x16x32_bf16 v[160:163], a[132:135], a[160:163], v[160:163]\n\t"
"v_mfma_f32_16x16x32_bf16 v[176:179], a[136:139], a[144:147], v[176:179]\n\t"
"buffer_load_dwordx4 a[96:99], v235, s[84:87], 0 offen offset:0 sc1\n\t"
"v_mfma_f32_16x16x32_bf16 v[176:179], a[140:143], a[160:163], v[176:179]\n\t"
"ds_read_b128 a[156:159], v216 offset:7168\n\t"
"v_mfma_f32_16x16x32_bf16 v[180:183], a[136:139], a[148:151], v[180:183]\n\t"
"buffer_load_dwordx4 a[100:103], v235, s[84:87], 0 offen offset:1024 sc1\n\t"
"v_mfma_f32_16x16x32_bf16 v[180:183], a[140:143], a[164:167], v[180:183]\n\t"
"ds_read_b128 a[168:171], v216 offset:14336\n\t"
"v_mfma_f32_16x16x32_bf16 v[164:167], a[128:131], a[148:151], v[164:167]\n\t"
"ds_read_b128 a[152:155], v216 offset:6144\n\t"
"v_mfma_f32_16x16x32_bf16 v[164:167], a[132:135], a[164:167], v[164:167]\n\t"
"v_mfma_f32_16x16x32_bf16 v[148:151], a[120:123], a[148:151], v[148:151]\n\t"
"v_mfma_f32_16x16x32_bf16 v[148:151], a[124:127], a[164:167], v[148:151]\n\t"
"s_waitcnt lgkmcnt(0)\n\t"
"s_barrier\n\t"
"v_mfma_f32_16x16x32_bf16 v[152:155], a[120:123], a[152:155], v[152:155]\n\t"
"ds_read_b128 a[144:147], v216 offset:16384\n\t"
"v_add_u32 v248, 101632, v227\n\t"
"v_mfma_f32_16x16x32_bf16 v[152:155], a[124:127], a[168:171], v[152:155]\n\t"
"ds_read_b128 a[148:151], v216 offset:17408\n\t"
"v_xor_b32 v249, 32, v248\n\t"
"v_mfma_f32_16x16x32_bf16 v[168:171], a[128:131], a[152:155], v[168:171]\n\t"
"ds_read_b128 a[160:163], v216 offset:24576\n\t"
"v_xor_b32 v250, 64, v248\n\t"
"v_mfma_f32_16x16x32_bf16 v[168:171], a[132:135], a[168:171], v[168:171]\n\t"
"ds_read_b128 a[164:167], v216 offset:25600\n\t"
"v_mfma_f32_16x16x32_bf16 v[184:187], a[136:139], a[152:155], v[184:187]\n\t"
"s_xor_b32 s24, s24, 384\n\t"
"v_xor_b32 v251, 96, v248\n\t"
"v_mfma_f32_16x16x32_bf16 v[184:187], a[140:143], a[168:171], v[184:187]\n\t"
"ds_read_b128 a[152:155], v216 offset:18432\n\t"
"v_mfma_f32_16x16x32_bf16 v[200:203], a[176:179], a[220:223], 0\n\t"
"s_xor_b32 s20, s20, 384\n\t"
"ds_read_b128 a[168:171], v216 offset:26624\n\t"
"ds_read_b64_tr_b16 a[176:177], v248 offset:0\n\t"
"v_mfma_f32_16x16x32_bf16 v[188:191], a[136:139], a[156:159], v[188:191]\n\t"
"buffer_load_dwordx4 a[136:139], v218, s[84:87], s51 offen offset:0 sc1\n\t"
"v_mfma_f32_16x16x32_bf16 v[188:191], a[140:143], a[172:175], v[188:191]\n\t"
"ds_read_b64_tr_b16 a[178:179], v248 offset:512\n\t"
"v_mfma_f32_16x16x32_bf16 v[204:207], a[180:183], a[220:223], 0\n\t"
"buffer_load_dwordx4 a[140:143], v218, s[84:87], s51 offen offset:1024 sc1\n\t"
"v_mfma_f32_16x16x32_bf16 v[172:175], a[128:131], a[156:159], v[172:175]\n\t"
"ds_read_b64_tr_b16 a[180:181], v249 offset:0\n\t"
"v_cvt_pk_bf16_f32 v200, v200, v201\n\t"
"v_mfma_f32_16x16x32_bf16 v[172:175], a[132:135], a[172:175], v[172:175]\n\t"
"buffer_load_dwordx4 a[132:135], v235, s[84:87], s51 offen offset:3072 sc1\n\t"
"v_mfma_f32_16x16x32_bf16 v[156:159], a[120:123], a[156:159], v[156:159]\n\t"
"ds_read_b64_tr_b16 a[182:183], v249 offset:512\n\t"
"v_cvt_pk_bf16_f32 v201, v202, v203\n\t"
"v_mfma_f32_16x16x32_bf16 v[156:159], a[124:127], a[172:175], v[156:159]\n\t"
"buffer_load_dwordx4 a[128:131], v235, s[84:87], s51 offen offset:2048 sc1\n\t"
"v_mfma_f32_16x16x32_bf16 v[208:211], a[184:187], a[220:223], 0\n\t"
"s_waitcnt vmcnt(33) lgkmcnt(6)\n\t"
"ds_read_b128 a[156:159], v216 offset:19456\n\t"
"ds_read_b64_tr_b16 a[184:185], v250 offset:0\n\t"
"s_xor_b32 s27, s27, 384\n\t"
"v_mfma_f32_16x16x32_bf16 v[48:51], a[0:3], a[144:147], v[48:51]\n\t"
"v_cvt_pk_bf16_f32 v202, v204, v205\n\t"
"v_cvt_pk_bf16_f32 v203, v206, v207\n\t"
"v_mfma_f32_16x16x32_bf16 v[48:51], a[4:7], a[160:163], v[48:51]\n\t"
"ds_write_b128 v217, v[200:203] offset:0\n\t"
"v_mfma_f32_16x16x32_bf16 v[212:215], a[188:191], a[220:223], 0\n\t"
"buffer_load_dwordx4 a[120:123], v235, s[84:87], s51 offen offset:0 sc1\n\t"
"v_mfma_f32_16x16x32_bf16 v[64:67], a[8:11], a[144:147], v[64:67]\n\t"
"v_cvt_pk_bf16_f32 v208, v208, v209\n\t"
"v_cvt_pk_bf16_f32 v209, v210, v211\n\t"
"v_mfma_f32_16x16x32_bf16 v[64:67], a[12:15], a[160:163], v[64:67]\n\t"
"buffer_load_dwordx4 a[124:127], v235, s[84:87], s51 offen offset:1024 sc1\n\t"
"v_mfma_f32_16x16x32_bf16 v[80:83], a[16:19], a[144:147], v[80:83]\n\t"
"s_xor_b32 s25, s25, 384\n\t"
"ds_read_b64_tr_b16 a[186:187], v250 offset:512\n\t"
"ds_read_b64_tr_b16 a[188:189], v251 offset:0\n\t"
"v_mfma_f32_16x16x32_bf16 v[80:83], a[20:23], a[160:163], v[80:83]\n\t"
"ds_read_b64_tr_b16 a[190:191], v251 offset:512\n\t"
"v_cvt_pk_bf16_f32 v210, v212, v213\n\t"
"v_mfma_f32_16x16x32_bf16 v[192:195], a[192:195], a[220:223], 0\n\t"
"ds_read_b128 a[172:175], v216 offset:27648\n\t"
"v_cvt_pk_bf16_f32 v211, v214, v215\n\t"
"v_mfma_f32_16x16x32_bf16 v[84:87], a[16:19], a[148:151], v[84:87]\n\t"
"ds_read_b128 a[160:163], v216 offset:28672\n\t"
"v_mfma_f32_16x16x32_bf16 v[84:87], a[20:23], a[164:167], v[84:87]\n\t"
"ds_read_b64_tr_b16 a[192:193], v248 offset:1152\n\t"
"v_mfma_f32_16x16x32_bf16 v[68:71], a[8:11], a[148:151], v[68:71]\n\t"
"ds_read_b64_tr_b16 a[194:195], v248 offset:1664\n\t"
"v_mfma_f32_16x16x32_bf16 v[68:71], a[12:15], a[164:167], v[68:71]\n\t"
"ds_read_b128 a[144:147], v216 offset:20480\n\t"
"v_cvt_pk_bf16_f32 v192, v192, v193\n\t"
"v_mfma_f32_16x16x32_bf16 v[196:199], a[196:199], a[220:223], 0\n\t"
"ds_read_b64_tr_b16 a[196:197], v249 offset:1152\n\t"
"v_cvt_pk_bf16_f32 v193, v194, v195\n\t"
"v_mfma_f32_16x16x32_bf16 v[52:55], a[0:3], a[148:151], v[52:55]\n\t"
"ds_read_b128 a[148:151], v216 offset:21504\n\t"
"v_mfma_f32_16x16x32_bf16 v[52:55], a[4:7], a[164:167], v[52:55]\n\t"
"v_mfma_f32_16x16x32_bf16 v[200:203], a[200:203], a[220:223], 0\n\t"
"s_waitcnt lgkmcnt(8)\n\t"
"ds_write_b128 v219, v[208:211] offset:0\n\t"
"v_mfma_f32_16x16x32_bf16 v[56:59], a[0:3], a[152:155], v[56:59]\n\t"
"v_mfma_f32_16x16x32_bf16 v[56:59], a[4:7], a[168:171], v[56:59]\n\t"
"v_cvt_pk_bf16_f32 v194, v196, v197\n\t"
"v_mfma_f32_16x16x32_bf16 v[72:75], a[8:11], a[152:155], v[72:75]\n\t"
"v_cvt_pk_bf16_f32 v195, v198, v199\n\t"
"v_mfma_f32_16x16x32_bf16 v[72:75], a[12:15], a[168:171], v[72:75]\n\t"
"ds_read_b64_tr_b16 a[198:199], v249 offset:1664\n\t"
"v_mfma_f32_16x16x32_bf16 v[204:207], a[204:207], a[220:223], 0\n\t"
"ds_read_b128 a[164:167], v216 offset:29696\n\t"
"v_cvt_pk_bf16_f32 v200, v200, v201\n\t"
"v_mfma_f32_16x16x32_bf16 v[88:91], a[16:19], a[152:155], v[88:91]\n\t"
"ds_read_b64_tr_b16 a[200:201], v250 offset:1152\n\t"
"v_cvt_pk_bf16_f32 v201, v202, v203\n\t"
"v_mfma_f32_16x16x32_bf16 v[88:91], a[20:23], a[168:171], v[88:91]\n\t"
"s_waitcnt lgkmcnt(5)\n\t"
"ds_read_b128 a[168:171], v216 offset:30720\n\t"
"v_mfma_f32_16x16x32_bf16 v[92:95], a[16:19], a[156:159], v[92:95]\n\t"
"v_mfma_f32_16x16x32_bf16 v[92:95], a[20:23], a[172:175], v[92:95]\n\t"
"ds_write_b128 v217, v[192:195] offset:8192\n\t"
"v_mfma_f32_16x16x32_bf16 v[76:79], a[8:11], a[156:159], v[76:79]\n\t"
"v_cvt_pk_bf16_f32 v202, v204, v205\n\t"
"v_cvt_pk_bf16_f32 v203, v206, v207\n\t"
"v_mfma_f32_16x16x32_bf16 v[76:79], a[12:15], a[172:175], v[76:79]\n\t"
"v_mfma_f32_16x16x32_bf16 v[60:63], a[0:3], a[156:159], v[60:63]\n\t"
"v_mfma_f32_16x16x32_bf16 v[60:63], a[4:7], a[172:175], v[60:63]\n\t"
"s_waitcnt vmcnt(26) lgkmcnt(5)\n\t"
"ds_write_b128 v219, v[200:203] offset:8192\n\t"
"v_mfma_f32_16x16x32_bf16 v[144:147], a[24:27], a[144:147], v[144:147]\n\t"
"v_mfma_f32_16x16x32_bf16 v[144:147], a[28:31], a[160:163], v[144:147]\n\t"
"v_mfma_f32_16x16x32_bf16 v[160:163], a[32:35], a[144:147], v[160:163]\n\t"
"v_mfma_f32_16x16x32_bf16 v[160:163], a[36:39], a[160:163], v[160:163]\n\t"
"ds_read_b64_tr_b16 a[202:203], v250 offset:1664\n\t"
"v_mfma_f32_16x16x32_bf16 v[176:179], a[40:43], a[144:147], v[176:179]\n\t"
"ds_read_b64_tr_b16 a[204:205], v251 offset:1152\n\t"
"v_mfma_f32_16x16x32_bf16 v[176:179], a[44:47], a[160:163], v[176:179]\n\t"
"s_waitcnt lgkmcnt(3)\n\t"
"ds_read_b128 a[172:175], v216 offset:31744\n\t"
"v_mfma_f32_16x16x32_bf16 v[180:183], a[40:43], a[148:151], v[180:183]\n\t"
"ds_read_b64_tr_b16 a[206:207], v251 offset:1664\n\t"
"v_mfma_f32_16x16x32_bf16 v[180:183], a[44:47], a[164:167], v[180:183]\n\t"
"ds_read_b128 a[152:155], v216 offset:22528\n\t"
"v_mfma_f32_16x16x32_bf16 v[164:167], a[32:35], a[148:151], v[164:167]\n\t"
"ds_read_b128 a[156:159], v216 offset:23552\n\t"
"v_mfma_f32_16x16x32_bf16 v[164:167], a[36:39], a[164:167], v[164:167]\n\t"
"v_mfma_f32_16x16x32_bf16 v[148:151], a[24:27], a[148:151], v[148:151]\n\t"
"v_mfma_f32_16x16x32_bf16 v[148:151], a[28:31], a[164:167], v[148:151]\n\t"
"s_waitcnt lgkmcnt(0)\n\t"
"s_barrier\n\t"
"v_mfma_f32_16x16x32_bf16 v[152:155], a[24:27], a[152:155], v[152:155]\n\t"
"ds_read_b128 a[144:147], v216 offset:0\n\t"
"v_mfma_f32_16x16x32_bf16 v[152:155], a[28:31], a[168:171], v[152:155]\n\t"
"ds_read_b128 a[148:151], v216 offset:1024\n\t"
"v_mfma_f32_16x16x32_bf16 v[168:171], a[32:35], a[152:155], v[168:171]\n\t"
"ds_read_b128 a[160:163], v216 offset:8192\n\t"
"v_mfma_f32_16x16x32_bf16 v[168:171], a[36:39], a[168:171], v[168:171]\n\t"
"ds_read_b128 a[164:167], v216 offset:9216\n\t"
"v_mfma_f32_16x16x32_bf16 v[184:187], a[40:43], a[152:155], v[184:187]\n\t"
"v_mfma_f32_16x16x32_bf16 v[184:187], a[44:47], a[168:171], v[184:187]\n\t"
"ds_read_b128 a[152:155], v216 offset:2048\n\t"
"v_mfma_f32_16x16x32_bf16 v[208:211], a[176:179], a[224:227], 0\n\t"
"ds_read_b128 a[168:171], v216 offset:10240\n\t"
"v_mfma_f32_16x16x32_bf16 v[188:191], a[40:43], a[156:159], v[188:191]\n\t"
"v_mfma_f32_16x16x32_bf16 v[188:191], a[44:47], a[172:175], v[188:191]\n\t"
"v_mfma_f32_16x16x32_bf16 v[212:215], a[180:183], a[224:227], 0\n\t"
"v_mfma_f32_16x16x32_bf16 v[172:175], a[32:35], a[156:159], v[172:175]\n\t"
"s_nop 2\n\t"
"v_cvt_pk_bf16_f32 v208, v208, v209\n\t"
"v_mfma_f32_16x16x32_bf16 v[172:175], a[36:39], a[172:175], v[172:175]\n\t"
"v_cvt_pk_bf16_f32 v209, v210, v211\n\t"
"v_mfma_f32_16x16x32_bf16 v[156:159], a[24:27], a[156:159], v[156:159]\n\t"
"v_mfma_f32_16x16x32_bf16 v[156:159], a[28:31], a[172:175], v[156:159]\n\t"
"v_cvt_pk_bf16_f32 v210, v212, v213\n\t"
"v_mfma_f32_16x16x32_bf16 v[192:195], a[184:187], a[224:227], 0\n\t"
"s_waitcnt vmcnt(18) lgkmcnt(2)\n\t"
"ds_read_b128 a[172:175], v216 offset:11264\n\t"
"v_mfma_f32_16x16x32_bf16 v[48:51], a[48:51], a[144:147], v[48:51]\n\t"
"ds_read_b128 a[156:159], v216 offset:3072\n\t"
"v_mfma_f32_16x16x32_bf16 v[48:51], a[52:55], a[160:163], v[48:51]\n\t"
"v_cvt_pk_bf16_f32 v211, v214, v215\n\t"
"v_mfma_f32_16x16x32_bf16 v[196:199], a[188:191], a[224:227], 0\n\t"
"ds_write_b128 v221, v[208:211] offset:16384\n\t"
"v_mfma_f32_16x16x32_bf16 v[64:67], a[56:59], a[144:147], v[64:67]\n\t"
"v_mfma_f32_16x16x32_bf16 v[64:67], a[60:63], a[160:163], v[64:67]\n\t"
"v_cvt_pk_bf16_f32 v192, v192, v193\n\t"
"v_cvt_pk_bf16_f32 v193, v194, v195\n\t"
"v_mfma_f32_16x16x32_bf16 v[80:83], a[64:67], a[144:147], v[80:83]\n\t"
"v_mfma_f32_16x16x32_bf16 v[80:83], a[68:71], a[160:163], v[80:83]\n\t"
"ds_read_b128 a[144:147], v216 offset:4096\n\t"
"v_cvt_pk_bf16_f32 v194, v196, v197\n\t"
"v_mfma_f32_16x16x32_bf16 v[200:203], a[192:195], a[224:227], 0\n\t"
"ds_read_b128 a[160:163], v216 offset:12288\n\t"
"v_cvt_pk_bf16_f32 v195, v198, v199\n\t"
"v_mfma_f32_16x16x32_bf16 v[84:87], a[64:67], a[148:151], v[84:87]\n\t"
"v_mfma_f32_16x16x32_bf16 v[84:87], a[68:71], a[164:167], v[84:87]\n\t"
"ds_write_b128 v221, v[192:195] offset:24576\n\t"
"v_mfma_f32_16x16x32_bf16 v[68:71], a[56:59], a[148:151], v[68:71]\n\t"
"v_mfma_f32_16x16x32_bf16 v[68:71], a[60:63], a[164:167], v[68:71]\n\t"
"s_nop 0\n\t"
"v_cvt_pk_bf16_f32 v200, v200, v201\n\t"
"v_cvt_pk_bf16_f32 v201, v202, v203\n\t"
"v_mfma_f32_16x16x32_bf16 v[204:207], a[196:199], a[224:227], 0\n\t"
"v_mfma_f32_16x16x32_bf16 v[52:55], a[48:51], a[148:151], v[52:55]\n\t"
"ds_read_b128 a[148:151], v216 offset:5120\n\t"
"v_mfma_f32_16x16x32_bf16 v[52:55], a[52:55], a[164:167], v[52:55]\n\t"
"ds_read_b128 a[164:167], v216 offset:13312\n\t"
"v_mfma_f32_16x16x32_bf16 v[208:211], a[200:203], a[224:227], 0\n\t"
"s_waitcnt lgkmcnt(5)\n\t"
"v_mfma_f32_16x16x32_bf16 v[56:59], a[48:51], a[152:155], v[56:59]\n\t"
"s_nop 0\n\t"
"v_cvt_pk_bf16_f32 v202, v204, v205\n\t"
"v_cvt_pk_bf16_f32 v203, v206, v207\n\t"
"v_mfma_f32_16x16x32_bf16 v[56:59], a[52:55], a[168:171], v[56:59]\n\t"
"ds_write_b128 v221, v[200:203] offset:20480\n\t"
"v_mfma_f32_16x16x32_bf16 v[72:75], a[56:59], a[152:155], v[72:75]\n\t"
"v_mfma_f32_16x16x32_bf16 v[72:75], a[60:63], a[168:171], v[72:75]\n\t"
"v_cvt_pk_bf16_f32 v208, v208, v209\n\t"
"v_mfma_f32_16x16x32_bf16 v[212:215], a[204:207], a[224:227], 0\n\t"
"v_mfma_f32_16x16x32_bf16 v[88:91], a[64:67], a[152:155], v[88:91]\n\t"
"v_mfma_f32_16x16x32_bf16 v[88:91], a[68:71], a[168:171], v[88:91]\n\t"
"v_mfma_f32_16x16x32_bf16 v[92:95], a[64:67], a[156:159], v[92:95]\n\t"
"v_mfma_f32_16x16x32_bf16 v[92:95], a[68:71], a[172:175], v[92:95]\n\t"
"ds_read_b128 a[152:155], v216 offset:6144\n\t"
"v_mfma_f32_16x16x32_bf16 v[76:79], a[56:59], a[156:159], v[76:79]\n\t"
"ds_read_b128 a[168:171], v216 offset:14336\n\t"
"v_mfma_f32_16x16x32_bf16 v[76:79], a[60:63], a[172:175], v[76:79]\n\t"
"v_cvt_pk_bf16_f32 v209, v210, v211\n\t"
"v_mfma_f32_16x16x32_bf16 v[60:63], a[48:51], a[156:159], v[60:63]\n\t"
"ds_read_b128 a[156:159], v216 offset:7168\n\t"
"v_cvt_pk_bf16_f32 v210, v212, v213\n\t"
"v_mfma_f32_16x16x32_bf16 v[60:63], a[52:55], a[172:175], v[60:63]\n\t"
"s_waitcnt vmcnt(12) lgkmcnt(3)\n\t"
"ds_read_b128 a[172:175], v216 offset:15360\n\t"
"v_mfma_f32_16x16x32_bf16 v[144:147], a[72:75], a[144:147], v[144:147]\n\t"
"v_cvt_pk_bf16_f32 v211, v214, v215\n\t"
"v_mfma_f32_16x16x32_bf16 v[144:147], a[76:79], a[160:163], v[144:147]\n\t"
"ds_write_b128 v221, v[208:211] offset:28672\n\t"
"v_mfma_f32_16x16x32_bf16 v[160:163], a[80:83], a[144:147], v[160:163]\n\t"
"v_mfma_f32_16x16x32_bf16 v[160:163], a[84:87], a[160:163], v[160:163]\n\t"
"v_mfma_f32_16x16x32_bf16 v[176:179], a[88:91], a[144:147], v[176:179]\n\t"
"v_mfma_f32_16x16x32_bf16 v[176:179], a[92:95], a[160:163], v[176:179]\n\t"
"v_mfma_f32_16x16x32_bf16 v[180:183], a[88:91], a[148:151], v[180:183]\n\t"
"v_mfma_f32_16x16x32_bf16 v[180:183], a[92:95], a[164:167], v[180:183]\n\t"
"v_mfma_f32_16x16x32_bf16 v[164:167], a[80:83], a[148:151], v[164:167]\n\t"
"v_mfma_f32_16x16x32_bf16 v[164:167], a[84:87], a[164:167], v[164:167]\n\t"
"v_mfma_f32_16x16x32_bf16 v[148:151], a[72:75], a[148:151], v[148:151]\n\t"
"v_mfma_f32_16x16x32_bf16 v[148:151], a[76:79], a[164:167], v[148:151]\n\t"
"s_waitcnt lgkmcnt(0)\n\t"
"s_barrier\n\t"
"v_mfma_f32_16x16x32_bf16 v[152:155], a[72:75], a[152:155], v[152:155]\n\t"
"ds_read_b128 a[144:147], v216 offset:16384\n\t"
"v_mfma_f32_16x16x32_bf16 v[152:155], a[76:79], a[168:171], v[152:155]\n\t"
"ds_read_b128 a[160:163], v216 offset:24576\n\t"
"v_mfma_f32_16x16x32_bf16 v[168:171], a[80:83], a[152:155], v[168:171]\n\t"
"ds_read_b128 a[148:151], v216 offset:17408\n\t"
"v_mfma_f32_16x16x32_bf16 v[168:171], a[84:87], a[168:171], v[168:171]\n\t"
"ds_read_b128 a[164:167], v216 offset:25600\n\t"
"v_mfma_f32_16x16x32_bf16 v[184:187], a[88:91], a[152:155], v[184:187]\n\t"
"v_mfma_f32_16x16x32_bf16 v[184:187], a[92:95], a[168:171], v[184:187]\n\t"
"ds_read_b128 a[168:171], v216 offset:26624\n\t"
"v_mfma_f32_16x16x32_bf16 v[188:191], a[88:91], a[156:159], v[188:191]\n\t"
"v_mfma_f32_16x16x32_bf16 v[188:191], a[92:95], a[172:175], v[188:191]\n\t"
"v_mfma_f32_16x16x32_bf16 v[172:175], a[80:83], a[156:159], v[172:175]\n\t"
"ds_read_b128 a[152:155], v216 offset:18432\n\t"
"v_mfma_f32_16x16x32_bf16 v[172:175], a[84:87], a[172:175], v[172:175]\n\t"
"v_mfma_f32_16x16x32_bf16 v[156:159], a[72:75], a[156:159], v[156:159]\n\t"
"ds_read_b128 a[156:159], v216 offset:19456\n\t"
"v_mfma_f32_16x16x32_bf16 v[156:159], a[76:79], a[172:175], v[156:159]\n\t"
"s_waitcnt vmcnt(6) lgkmcnt(5)\n\t"
"v_pk_fma_f32 v[48:49], v[144:145], s[90:91], v[48:49]\n\t"
"v_pk_fma_f32 v[50:51], v[146:147], s[90:91], v[50:51]\n\t"
"v_pk_add_f32 v[144:145], v[48:49], v[144:145] neg_lo:[0,1] neg_hi:[0,1]\n\t"
"v_pk_add_f32 v[146:147], v[50:51], v[146:147] neg_lo:[0,1] neg_hi:[0,1]\n\t"
"v_pk_fma_f32 v[64:65], v[160:161], s[90:91], v[64:65]\n\t"
"v_pk_fma_f32 v[66:67], v[162:163], s[90:91], v[66:67]\n\t"
"v_pk_add_f32 v[160:161], v[64:65], v[160:161] neg_lo:[0,1] neg_hi:[0,1]\n\t"
"v_pk_add_f32 v[162:163], v[66:67], v[162:163] neg_lo:[0,1] neg_hi:[0,1]\n\t"
"v_pk_fma_f32 v[80:81], v[176:177], s[90:91], v[80:81]\n\t"
"v_pk_fma_f32 v[82:83], v[178:179], s[90:91], v[82:83]\n\t"
"v_pk_add_f32 v[176:177], v[80:81], v[176:177] neg_lo:[0,1] neg_hi:[0,1]\n\t"
"v_pk_add_f32 v[178:179], v[82:83], v[178:179] neg_lo:[0,1] neg_hi:[0,1]\n\t"
"v_pk_fma_f32 v[84:85], v[180:181], s[90:91], v[84:85]\n\t"
"v_pk_fma_f32 v[86:87], v[182:183], s[90:91], v[86:87]\n\t"
"v_pk_add_f32 v[180:181], v[84:85], v[180:181] neg_lo:[0,1] neg_hi:[0,1]\n\t"
"v_pk_add_f32 v[182:183], v[86:87], v[182:183] neg_lo:[0,1] neg_hi:[0,1]\n\t"
"v_pk_fma_f32 v[68:69], v[164:165], s[90:91], v[68:69]\n\t"
"v_pk_fma_f32 v[70:71], v[166:167], s[90:91], v[70:71]\n\t"
"v_pk_add_f32 v[164:165], v[68:69], v[164:165] neg_lo:[0,1] neg_hi:[0,1]\n\t"
"v_pk_add_f32 v[166:167], v[70:71], v[166:167] neg_lo:[0,1] neg_hi:[0,1]\n\t"
"v_pk_fma_f32 v[52:53], v[148:149], s[90:91], v[52:53]\n\t"
"v_pk_fma_f32 v[54:55], v[150:151], s[90:91], v[54:55]\n\t"
"v_pk_add_f32 v[148:149], v[52:53], v[148:149] neg_lo:[0,1] neg_hi:[0,1]\n\t"
"v_pk_add_f32 v[150:151], v[54:55], v[150:151] neg_lo:[0,1] neg_hi:[0,1]\n\t"
"v_pk_fma_f32 v[56:57], v[152:153], s[90:91], v[56:57]\n\t"
"v_pk_fma_f32 v[58:59], v[154:155], s[90:91], v[58:59]\n\t"
"v_pk_add_f32 v[152:153], v[56:57], v[152:153] neg_lo:[0,1] neg_hi:[0,1]\n\t"
"v_pk_add_f32 v[154:155], v[58:59], v[154:155] neg_lo:[0,1] neg_hi:[0,1]\n\t"
"v_pk_fma_f32 v[72:73], v[168:169], s[90:91], v[72:73]\n\t"
"v_pk_fma_f32 v[74:75], v[170:171], s[90:91], v[74:75]\n\t"
"v_pk_add_f32 v[168:169], v[72:73], v[168:169] neg_lo:[0,1] neg_hi:[0,1]\n\t"
"v_pk_add_f32 v[170:171], v[74:75], v[170:171] neg_lo:[0,1] neg_hi:[0,1]\n\t"
"v_pk_fma_f32 v[88:89], v[184:185], s[90:91], v[88:89]\n\t"
"v_pk_fma_f32 v[90:91], v[186:187], s[90:91], v[90:91]\n\t"
"v_pk_add_f32 v[184:185], v[88:89], v[184:185] neg_lo:[0,1] neg_hi:[0,1]\n\t"
"v_pk_add_f32 v[186:187], v[90:91], v[186:187] neg_lo:[0,1] neg_hi:[0,1]\n\t"
"v_pk_fma_f32 v[92:93], v[188:189], s[90:91], v[92:93]\n\t"
"v_pk_fma_f32 v[94:95], v[190:191], s[90:91], v[94:95]\n\t"
"v_pk_add_f32 v[188:189], v[92:93], v[188:189] neg_lo:[0,1] neg_hi:[0,1]\n\t"
"v_pk_add_f32 v[190:191], v[94:95], v[190:191] neg_lo:[0,1] neg_hi:[0,1]\n\t"
"v_pk_fma_f32 v[76:77], v[172:173], s[90:91], v[76:77]\n\t"
"v_pk_fma_f32 v[78:79], v[174:175], s[90:91], v[78:79]\n\t"
"v_pk_add_f32 v[172:173], v[76:77], v[172:173] neg_lo:[0,1] neg_hi:[0,1]\n\t"
"v_pk_add_f32 v[174:175], v[78:79], v[174:175] neg_lo:[0,1] neg_hi:[0,1]\n\t"
"v_pk_fma_f32 v[60:61], v[156:157], s[90:91], v[60:61]\n\t"
"v_pk_fma_f32 v[62:63], v[158:159], s[90:91], v[62:63]\n\t"
"v_pk_add_f32 v[156:157], v[60:61], v[156:157] neg_lo:[0,1] neg_hi:[0,1]\n\t"
"v_pk_add_f32 v[158:159], v[62:63], v[158:159] neg_lo:[0,1] neg_hi:[0,1]\n\t"
"v_mfma_f32_16x16x32_bf16 v[48:51], a[96:99], a[144:147], v[48:51]\n\t"
"ds_read_b128 a[172:175], v216 offset:27648\n\t"
"v_mfma_f32_16x16x32_bf16 v[48:51], a[100:103], a[160:163], v[48:51]\n\t"
"v_mfma_f32_16x16x32_bf16 v[64:67], a[104:107], a[144:147], v[64:67]\n\t"
"v_mfma_f32_16x16x32_bf16 v[64:67], a[108:111], a[160:163], v[64:67]\n\t"
"v_mfma_f32_16x16x32_bf16 v[80:83], a[112:115], a[144:147], v[80:83]\n\t"
"v_mfma_f32_16x16x32_bf16 v[80:83], a[116:119], a[160:163], v[80:83]\n\t"
"s_waitcnt lgkmcnt(3)\n\t"
"ds_read_b128 a[144:147], v216 offset:20480\n\t"
"v_mfma_f32_16x16x32_bf16 v[84:87], a[112:115], a[148:151], v[84:87]\n\t"
"ds_read_b128 a[160:163], v216 offset:28672\n\t"
"v_mfma_f32_16x16x32_bf16 v[84:87], a[116:119], a[164:167], v[84:87]\n\t"
"v_mfma_f32_16x16x32_bf16 v[68:71], a[104:107], a[148:151], v[68:71]\n\t"
"v_mfma_f32_16x16x32_bf16 v[68:71], a[108:111], a[164:167], v[68:71]\n\t"
"v_cvt_pk_bf16_f32 v64, v64, v65\n\t"
"v_cvt_pk_bf16_f32 v80, v80, v81\n\t"
"v_mfma_f32_16x16x32_bf16 v[52:55], a[96:99], a[148:151], v[52:55]\n\t"
"ds_read_b128 a[148:151], v216 offset:21504\n\t"
"v_cvt_pk_bf16_f32 v81, v82, v83\n\t"
"v_mfma_f32_16x16x32_bf16 v[52:55], a[100:103], a[164:167], v[52:55]\n\t"
"s_waitcnt lgkmcnt(3)\n\t"
"ds_read_b128 a[164:167], v216 offset:29696\n\t"
"v_cvt_pk_bf16_f32 v48, v48, v49\n\t"
"v_mfma_f32_16x16x32_bf16 v[56:59], a[96:99], a[152:155], v[56:59]\n\t"
"v_cvt_pk_bf16_f32 v49, v50, v51\n\t"
"v_cvt_pk_bf16_f32 v65, v66, v67\n\t"
"v_mfma_f32_16x16x32_bf16 v[56:59], a[100:103], a[168:171], v[56:59]\n\t"
"v_mfma_f32_16x16x32_bf16 v[72:75], a[104:107], a[152:155], v[72:75]\n\t"
"v_mfma_f32_16x16x32_bf16 v[72:75], a[108:111], a[168:171], v[72:75]\n\t"
"v_cvt_pk_bf16_f32 v52, v52, v53\n\t"
"v_cvt_pk_bf16_f32 v68, v68, v69\n\t"
"v_mfma_f32_16x16x32_bf16 v[88:91], a[112:115], a[152:155], v[88:91]\n\t"
"ds_read_b128 a[152:155], v216 offset:22528\n\t"
"v_cvt_pk_bf16_f32 v69, v70, v71\n\t"
"v_mfma_f32_16x16x32_bf16 v[88:91], a[116:119], a[168:171], v[88:91]\n\t"
"ds_read_b128 a[168:171], v216 offset:30720\n\t"
"v_cvt_pk_bf16_f32 v53, v54, v55\n\t"
"v_mfma_f32_16x16x32_bf16 v[92:95], a[112:115], a[156:159], v[92:95]\n\t"
"v_cvt_pk_bf16_f32 v84, v84, v85\n\t"
"v_mfma_f32_16x16x32_bf16 v[92:95], a[116:119], a[172:175], v[92:95]\n\t"
"ds_write2st64_b64 v246, v[64:65], v[68:69] offset0:0 offset1:13\n\t"
"v_mfma_f32_16x16x32_bf16 v[76:79], a[104:107], a[156:159], v[76:79]\n\t"
"v_cvt_pk_bf16_f32 v85, v86, v87\n\t"
"v_mfma_f32_16x16x32_bf16 v[76:79], a[108:111], a[172:175], v[76:79]\n\t"
"v_cvt_pk_bf16_f32 v56, v56, v57\n\t"
"v_cvt_pk_bf16_f32 v72, v72, v73\n\t"
"v_cvt_pk_bf16_f32 v88, v88, v89\n\t"
"v_cvt_pk_bf16_f32 v89, v90, v91\n\t"
"v_mfma_f32_16x16x32_bf16 v[60:63], a[96:99], a[156:159], v[60:63]\n\t"
"v_cvt_pk_bf16_f32 v57, v58, v59\n\t"
"v_cvt_pk_bf16_f32 v73, v74, v75\n\t"
"v_mfma_f32_16x16x32_bf16 v[60:63], a[100:103], a[172:175], v[60:63]\n\t"
"s_waitcnt vmcnt(0) lgkmcnt(3)\n\t"
"ds_read_b128 a[156:159], v216 offset:23552\n\t"
"ds_read_b128 a[172:175], v216 offset:31744\n\t"
"ds_write2st64_b64 v228, v[48:49], v[52:53] offset0:0 offset1:13\n\t"
"v_mfma_f32_16x16x32_bf16 v[144:147], a[120:123], a[144:147], v[144:147]\n\t"
"v_mfma_f32_16x16x32_bf16 v[144:147], a[124:127], a[160:163], v[144:147]\n\t"
"ds_write2st64_b64 v247, v[80:81], v[84:85] offset0:0 offset1:13\n\t"
"v_mfma_f32_16x16x32_bf16 v[160:163], a[128:131], a[144:147], v[160:163]\n\t"
"v_mfma_f32_16x16x32_bf16 v[160:163], a[132:135], a[160:163], v[160:163]\n\t"
"v_cvt_pk_bf16_f32 v60, v60, v61\n\t"
"v_cvt_pk_bf16_f32 v61, v62, v63\n\t"
"v_cvt_pk_bf16_f32 v76, v76, v77\n\t"
"v_cvt_pk_bf16_f32 v77, v78, v79\n\t"
"v_cvt_pk_bf16_f32 v92, v92, v93\n\t"
"v_cvt_pk_bf16_f32 v93, v94, v95\n\t"
"v_mfma_f32_16x16x32_bf16 v[176:179], a[136:139], a[144:147], v[176:179]\n\t"
"ds_write2st64_b64 v228, v[56:57], v[60:61] offset0:26 offset1:39\n\t"
"ds_write2st64_b64 v246, v[72:73], v[76:77] offset0:26 offset1:39\n\t"
"ds_write2st64_b64 v247, v[88:89], v[92:93] offset0:26 offset1:39\n\t"
"v_mfma_f32_16x16x32_bf16 v[176:179], a[140:143], a[160:163], v[176:179]\n\t"
"v_mfma_f32_16x16x32_bf16 v[180:183], a[136:139], a[148:151], v[180:183]\n\t"
"v_mfma_f32_16x16x32_bf16 v[180:183], a[140:143], a[164:167], v[180:183]\n\t"
"v_mfma_f32_16x16x32_bf16 v[164:167], a[128:131], a[148:151], v[164:167]\n\t"
"v_mfma_f32_16x16x32_bf16 v[164:167], a[132:135], a[164:167], v[164:167]\n\t"
"v_cvt_pk_bf16_f32 v144, v144, v145\n\t"
"v_cvt_pk_bf16_f32 v160, v160, v161\n\t"
"s_nop 1\n\t"
"v_cvt_pk_bf16_f32 v176, v176, v177\n\t"
"v_mfma_f32_16x16x32_bf16 v[148:151], a[120:123], a[148:151], v[148:151]\n\t"
"v_cvt_pk_bf16_f32 v161, v162, v163\n\t"
"v_cvt_pk_bf16_f32 v177, v178, v179\n\t"
"v_mfma_f32_16x16x32_bf16 v[148:151], a[124:127], a[164:167], v[148:151]\n\t"
"s_waitcnt lgkmcnt(3)\n\t"
"v_cvt_pk_bf16_f32 v145, v146, v147\n\t"
"s_waitcnt lgkmcnt(0)\n\t"
"s_barrier\n\t"
"v_mfma_f32_16x16x32_bf16 v[152:155], a[120:123], a[152:155], v[152:155]\n\t"
"s_nop 0\n\t"
"ds_read_b128 a[0:3], v229 offset:0\n\t"
"ds_read_b128 a[20:23], v231 offset:13312\n\t"
"v_mfma_f32_16x16x32_bf16 v[152:155], a[124:127], a[168:171], v[152:155]\n\t"
"s_nop 0\n\t"
"ds_read_b128 a[8:11], v231 offset:0\n\t"
"ds_read_b128 a[16:19], v230 offset:13312\n\t"
"v_mfma_f32_16x16x32_bf16 v[168:171], a[128:131], a[152:155], v[168:171]\n\t"
"s_nop 0\n\t"
"ds_read_b128 a[4:7], v230 offset:0\n\t"
"ds_read_b128 a[12:15], v229 offset:13312\n\t"
"v_mfma_f32_16x16x32_bf16 v[168:171], a[132:135], a[168:171], v[168:171]\n\t"
"v_cvt_pk_bf16_f32 v148, v148, v149\n\t"
"v_cvt_pk_bf16_f32 v149, v150, v151\n\t"
"v_cvt_pk_bf16_f32 v164, v164, v165\n\t"
"v_cvt_pk_bf16_f32 v165, v166, v167\n\t"
"v_cvt_pk_bf16_f32 v180, v180, v181\n\t"
"v_cvt_pk_bf16_f32 v181, v182, v183\n\t"
"v_mfma_f32_16x16x32_bf16 v[184:187], a[136:139], a[152:155], v[184:187]\n\t"
"ds_write2st64_b64 v228, v[144:145], v[148:149] offset0:52 offset1:65\n\t"
"ds_write2st64_b64 v246, v[160:161], v[164:165] offset0:52 offset1:65\n\t"
"ds_write2st64_b64 v247, v[176:177], v[180:181] offset0:52 offset1:65\n\t"
"v_mfma_f32_16x16x32_bf16 v[184:187], a[140:143], a[168:171], v[184:187]\n\t"
"v_mfma_f32_16x16x32_bf16 v[188:191], a[136:139], a[156:159], v[188:191]\n\t"
"s_waitcnt lgkmcnt(8)\n\t"
"buffer_store_dwordx4 a[0:3], v232, s[16:19], s24 offen nt\n\t"
"v_mfma_f32_16x16x32_bf16 v[188:191], a[140:143], a[172:175], v[188:191]\n\t"
"s_waitcnt lgkmcnt(6)\n\t"
"buffer_store_dwordx4 a[8:11], v234, s[16:19], s24 offen nt\n\t"
"v_mfma_f32_16x16x32_bf16 v[172:175], a[128:131], a[156:159], v[172:175]\n\t"
"v_mfma_f32_16x16x32_bf16 v[172:175], a[132:135], a[172:175], v[172:175]\n\t"
"s_waitcnt lgkmcnt(4)\n\t"
"buffer_store_dwordx4 a[4:7], v233, s[16:19], s24 offen nt\n\t"
"v_cvt_pk_bf16_f32 v152, v152, v153\n\t"
"v_cvt_pk_bf16_f32 v168, v168, v169\n\t"
"v_cvt_pk_bf16_f32 v169, v170, v171\n\t"
"v_cvt_pk_bf16_f32 v184, v184, v185\n\t"
"v_mfma_f32_16x16x32_bf16 v[156:159], a[120:123], a[156:159], v[156:159]\n\t"
"v_cvt_pk_bf16_f32 v153, v154, v155\n\t"
"v_cvt_pk_bf16_f32 v185, v186, v187\n\t"
"v_mfma_f32_16x16x32_bf16 v[156:159], a[124:127], a[172:175], v[156:159]\n\t"
"s_waitcnt lgkmcnt(3)\n\t"
"buffer_store_dwordx4 a[12:15], v232, s[16:19], s27 offen nt\n\t"
"buffer_store_dwordx4 a[16:19], v233, s[16:19], s27 offen nt\n\t"
"buffer_store_dwordx4 a[20:23], v234, s[16:19], s27 offen nt\n\t"
"s_nop 3\n\t"
"v_cvt_pk_bf16_f32 v156, v156, v157\n\t"
"v_cvt_pk_bf16_f32 v157, v158, v159\n\t"
"ds_write2st64_b64 v228, v[152:153], v[156:157] offset0:78 offset1:91\n\t"
"v_cvt_pk_bf16_f32 v172, v172, v173\n\t"
"v_cvt_pk_bf16_f32 v173, v174, v175\n\t"
"ds_write2st64_b64 v246, v[168:169], v[172:173] offset0:78 offset1:91\n\t"
"v_cvt_pk_bf16_f32 v188, v188, v189\n\t"
"v_cvt_pk_bf16_f32 v189, v190, v191\n\t"
"ds_write2st64_b64 v247, v[184:185], v[188:189] offset0:78 offset1:91\n\t"
"s_waitcnt lgkmcnt(0)\n\t"
"s_barrier\n\t"
"ds_read_b128 a[24:27], v229 offset:26624\n\t"
"ds_read_b128 a[28:31], v230 offset:26624\n\t"
"ds_read_b128 a[32:35], v231 offset:26624\n\t"
"ds_read_b128 a[36:39], v229 offset:39936\n\t"
"ds_read_b128 a[40:43], v230 offset:39936\n\t"
"ds_read_b128 a[44:47], v231 offset:39936\n\t"
"s_waitcnt lgkmcnt(5)\n\t"
"buffer_store_dwordx4 a[24:27], v232, s[16:19], s20 offen nt\n\t"
"s_waitcnt lgkmcnt(4)\n\t"
"buffer_store_dwordx4 a[28:31], v233, s[16:19], s20 offen nt\n\t"
"s_waitcnt lgkmcnt(3)\n\t"
"buffer_store_dwordx4 a[32:35], v234, s[16:19], s20 offen nt\n\t"
"s_waitcnt lgkmcnt(2)\n\t"
"buffer_store_dwordx4 a[36:39], v232, s[16:19], s25 offen nt\n\t"
"s_waitcnt lgkmcnt(1)\n\t"
"buffer_store_dwordx4 a[40:43], v233, s[16:19], s25 offen nt\n\t"
"s_waitcnt lgkmcnt(0)\n\t"
"buffer_store_dwordx4 a[44:47], v234, s[16:19], s25 offen nt\n\t"
"s_waitcnt vmcnt(0)\n\t"
"Lwino_end:\n\t"
  : "+{v205}"(tid)
  : "s"(__builtin_amdgcn_kernarg_segment_ptr()), "s"(blockIdx.x), "s"((unsigned)(size_t)(__attribute__((address_space(3))) char*)smem)
  : "v0", "v1", "v2", "v3", "v4", "v5", "v6", "v7", "v8", "v9", "v10", "v11", "v12", "v13", "v14", "v15", "v16", "v17", "v18", "v19", "v20", "v21", "v22", "v23", "v24", "v25", "v26", "v27", "v28", "v29", "v30", "v31", "v32", "v33", "v34", "v35", "v36", "v37", "v38", "v39", "v40", "v41", "v42", "v43", "v44", "v45", "v46", "v47", "v48", "v49", "v50", "v51", "v52", "v53", "v54", "v55", "v56", "v57", "v58", "v59", "v60", "v61", "v62", "v63", "v64", "v65", "v66", "v67", "v68", "v69", "v70", "v71", "v72", "v73", "v74", "v75", "v76", "v77", "v78", "v79", "v80", "v81", "v82", "v83", "v84", "v85", "v86", "v87", "v88", "v89", "v90", "v91", "v92", "v93", "v94", "v95", "v96", "v97", "v98", "v99", "v100", "v101", "v102", "v103", "v104", "v105", "v106", "v107", "v108", "v109", "v110", "v111", "v112", "v113", "v114", "v115", "v116", "v117", "v118", "v119", "v120", "v121", "v122", "v123", "v124", "v125", "v126", "v127", "v128", "v129", "v130", "v131", "v132", "v133", "v134", "v135", "v136", "v137", "v138", "v139", "v140", "v141", "v142", "v143", "v144", "v145", "v146", "v147", "v148", "v149", "v150", "v151", "v152", "v153", "v154", "v155", "v156", "v157", "v158", "v159", "v160", "v161", "v162", "v163", "v164", "v165", "v166", "v167", "v168", "v169", "v170", "v171", "v172", "v173", "v174", "v175", "v176", "v177", "v178", "v179", "v180", "v181", "v182", "v183", "v184", "v185", "v186", "v187", "v188", "v189", "v190", "v191", "v192", "v193", "v194", "v195", "v196", "v197", "v198", "v199", "v200", "v201", "v202", "v203", "v204", "v206", "v207", "v208", "v209", "v210", "v211", "v212", "v213", "v214", "v215", "v216", "v217", "v218", "v219", "v220", "v221", "v222", "v223", "v224", "v225", "v226", "v227", "v228", "v229", "v230", "v231", "v232", "v233", "v234", "v235", "v236", "v237", "v238", "v239", "v240", "v241", "v242", "v243", "v244", "v245", "v246", "v247", "v248", "v249", "v250", "v251", "v252", "v253", "v254", "v255", "a0", "a1", "a2", "a3", "a4", "a5", "a6", "a7", "a8", "a9", "a10", "a11", "a12", "a13", "a14", "a15", "a16", "a17", "a18", "a19", "a20", "a21", "a22", "a23", "a24", "a25", "a26", "a27", "a28", "a29", "a30", "a31", "a32", "a33", "a34", "a35", "a36", "a37", "a38", "a39", "a40", "a41", "a42", "a43", "a44", "a45", "a46", "a47", "a48", "a49", "a50", "a51", "a52", "a53", "a54", "a55", "a56", "a57", "a58", "a59", "a60", "a61", "a62", "a63", "a64", "a65", "a66", "a67", "a68", "a69", "a70", "a71", "a72", "a73", "a74", "a75", "a76", "a77", "a78", "a79", "a80", "a81", "a82", "a83", "a84", "a85", "a86", "a87", "a88", "a89", "a90", "a91", "a92", "a93", "a94", "a95", "a96", "a97", "a98", "a99", "a100", "a101", "a102", "a103", "a104", "a105", "a106", "a107", "a108", "a109", "a110", "a111", "a112", "a113", "a114", "a115", "a116", "a117", "a118", "a119", "a120", "a121", "a122", "a123", "a124", "a125", "a126", "a127", "a128", "a129", "a130", "a131", "a132", "a133", "a134", "a135", "a136", "a137", "a138", "a139", "a140", "a141", "a142", "a143", "a144", "a145", "a146", "a147", "a148", "a149", "a150", "a151", "a152", "a153", "a154", "a155", "a156", "a157", "a158", "a159", "a160", "a161", "a162", "a163", "a164", "a165", "a166", "a167", "a168", "a169", "a170", "a171", "a172", "a173", "a174", "a175", "a176", "a177", "a178", "a179", "a180", "a181", "a182", "a183", "a184", "a185", "a186", "a187", "a188", "a189", "a190", "a191", "a192", "a193", "a194", "a195", "a196", "a197", "a198", "a199", "a200", "a201", "a202", "a203", "a204", "a205", "a206", "a207", "a208", "a209", "a210", "a211", "a212", "a213", "a214", "a215", "a216", "a217", "a218", "a219", "a220", "a221", "a222", "a223", "a224", "a225", "a226", "a227", "a228", "a229", "a230", "a231", "a232", "a233", "a234", "a235", "a236", "a237", "a238", "a239", "a240", "a241", "a242", "a243", "a244", "a245", "a246", "a247", "a248", "a249", "a250", "a251", "a252", "a253", "a254", "a255", "s6", "s7", "s8", "s9", "s10", "s11", "s12", "s13", "s14", "s15", "s16", "s17", "s18", "s19", "s20", "s21", "s22", "s23", "s24", "s25", "s26", "s27", "s28", "s29", "s30", "s31", "s32", "s33", "s34", "s35", "s36", "s37", "s38", "s39", "s40", "s41", "s42", "s43", "s44", "s45", "s46", "s47", "s48", "s49", "s50", "s51", "s52", "s53", "s54", "s55", "s56", "s57", "s58", "s59", "s60", "s61", "s62", "s63", "s64", "s65", "s66", "s67", "s68", "s69", "s70", "s71", "s72", "s73", "s74", "s75", "s76", "s77", "s78", "s79", "s80", "s81", "s82", "s83", "s84", "s85", "s86", "s87", "s88", "s89", "s90", "s91", "scc", "vcc", "memory");
}

extern "C" void kernel_launch(void* const* d_in, const int* in_sizes, int n_in, void* d_out, int out_size, void* d_ws,
                              size_t ws_size, hipStream_t stream) {
  wino_main_kernel<<<256, 256, 0, stream>>>((const char*)d_in[0], (char*)d_ws, (char*)d_out, (const char*)d_in[1]);
}
```

```cpp
#include <hip/hip_runtime.h>
#include <hip/hip_bf16.h>
#include <stdint.h>

#define U_BYTES 1179648

__global__ __launch_bounds__(256, 1) void wino_main_kernel(const char* __restrict__ x, char* __restrict__ Uq, char* __restrict__ out, const char* __restrict__ wt) {
  __shared__ __attribute__((aligned(1024))) char smem[163840];
  unsigned tid = threadIdx.x;
  asm volatile(
"s_load_dwordx8 s[76:83], %1, 0x0\n\t"
"s_mov_b32 s66, %2\n\t"
"s_getpc_b64 s[6:7]\n\t"
"Lwino_pc:\n\t"
"s_bfe_u32 s42, %2, 0x50003\n\t"
"s_lshl_b32 s42, s42, 12\n\t"
"v_lshl_add_u32 v203, v205, 4, s42\n\t"
"v_min_u32 v203, (Lwino_end-Lwino_pc-16)&-16, v203\n\t"
"global_load_dwordx4 v[196:199], v203, s[6:7]\n\t"
"v_and_b32 v204, 63, v205\n\t"
"v_readfirstlane_b32 s73, v205\n\t"
"v_lshrrev_b32 v120, 3, v204\n\t"
"v_and_b32 v121, 7, v204\n\t"
"s_lshr_b32 s73, s73, 6\n\t"
"s_lshr_b32 s56, s66, 5\n\t"
"s_mul_i32 s56, s56, 12288\n\t"
"s_bfe_u32 s61, s66, 0x20001\n\t"
"s_mul_i32 s61, s61, 1536\n\t"
"s_add_u32 s56, s56, s61\n\t"
"s_bitcmp1_b32 s66, 0\n\t"
"s_cselect_b32 s61, 48, 0\n\t"
"s_add_u32 s56, s56, s61\n\t"
"s_bfe_u32 s62, s66, 0x20003\n\t"
"s_mul_i32 s61, s62, 96\n\t"
"s_add_u32 s56, s56, s61\n\t"
"s_cmp_lt_u32 s66, 192\n\t"
"s_cselect_b32 s56, s56, 2.0\n\t"
"v_bfe_u32 v42, v204, 3, 2\n\t"
"v_lshlrev_b32 v43, 2, v204\n\t"
"v_and_or_b32 v42, v43, 16, v42\n\t"
"v_mul_u32_u24 v42, 0x180, v42\n\t"
"v_lshrrev_b32 v44, 5, v204\n\t"
"v_mul_u32_u24 v43, 0x12000, v44\n\t"
"v_and_b32 v45, 3, v204\n\t"
"v_min_u32 v41, 2, v45\n\t"
"v_lshl_add_u32 v41, v41, 4, v42\n\t"
"v_add_u32 v41, v41, v43\n\t"
"v_lshlrev_b32 v43, 4, v204\n\t"
"v_bfe_u32 v45, v204, 1, 2\n\t"
"v_and_or_b32 v45, v43, 16, v45\n\t"
"v_mul_u32_u24 v45, 0x180, v45\n\t"
"v_lshl_add_u32 v44, v44, 3, v120\n\t"
"v_lshl_add_u32 v45, v44, 1, v45\n\t"
"v_mov_b32 v40, 0\n\t"
"s_mul_i32 s61, s73, 147456\n\t"
"s_add_u32 s61, s61, s56\n\t"
"s_lshl3_add_u32 s62, s73, s56\n\t"
"s_add_u32 s62, s62, 589824\n\t"
"s_lshl_b32 s64, s73, 10\n\t"
"s_and_b32 s60, s66, 7\n\t"
"s_lshr_b32 s65, s66, 3\n\t"
"s_mul_i32 s24, s60, 12582912\n\t"
"s_mul_i32 s63, s65, 6144\n\t"
"s_add_u32 s24, s24, s63\n\t"
"s_add_u32 s22, s24, 196224\n\t"
"s_mul_i32 s63, s73, 786432\n\t"
"s_mul_i32 s26, s73, 4608\n\t"
"s_mov_b32 s48, 189696\n\t"
"s_cmp_eq_u32 s65, 0\n\t"
"s_cselect_b64 s[40:41], 0xff, 0\n\t"
"s_cselect_b64 s[46:47], -1, 0\n\t"
"v_mul_u32_u24 v122, 0x300, v120\n\t"
"v_xor_b32 v123, 4, v121\n\t"
"v_add_u32 v122, s63, v122\n\t"
"v_and_b32 v124, 2, v120\n\t"
"v_xor_b32 v124, v124, v123\n\t"
"v_lshl_add_u32 v128, v124, 4, v122\n\t"
"v_cndmask_b32 v241, v128, 2.0, s[40:41]\n\t"
"v_add_u32 v122, 194816, v122\n\t"
"v_add_u32 v124, -1, v120\n\t"
"v_ashrrev_i32 v125, 31, v124\n\t"
"v_sub_u32 v124, v124, v125\n\t"
"v_and_b32 v124, 2, v124\n\t"
"v_xor_b32 v124, v124, v123\n\t"
"v_mad_i32_i24 v125, v125, s48, v122\n\t"
"v_lshl_add_u32 v129, v124, 4, v125\n\t"
"s_lshl_b64 s[40:41], s[40:41], 8\n\t"
"v_cndmask_b32 v242, v129, 2.0, s[40:41]\n\t"
"v_add_u32 v122, 194816, v122\n\t"
"v_add_u32 v124, -2, v120\n\t"
"v_ashrrev_i32 v125, 31, v124\n\t"
"v_sub_u32 v124, v124, v125\n\t"
"v_and_b32 v124, 2, v124\n\t"
"v_and_or_b32 v124, v125, 4, v124\n\t"
"v_xor_b32 v124, v124, v121\n\t"
"v_mad_i32_i24 v125, v125, s48, v122\n\t"
"v_lshl_add_u32 v130, v124, 4, v125\n\t"
"s_lshl_b64 s[40:41], s[40:41], 8\n\t"
"v_cndmask_b32 v243, v130, 2.0, s[40:41]\n\t"
"v_add_u32 v122, 194816, v122\n\t"
"v_add_u32 v124, -3, v120\n\t"
"v_ashrrev_i32 v125, 31, v124\n\t"
"v_sub_u32 v124, v124, v125\n\t"
"v_and_b32 v124, 2, v124\n\t"
"v_xor_b32 v124, v124, v121\n\t"
"v_mad_i32_i24 v125, v125, s48, v122\n\t"
"v_lshl_add_u32 v131, v124, 4, v125\n\t"
"s_lshl_b64 s[40:41], s[40:41], 8\n\t"
"v_cndmask_b32 v244, v131, 2.0, s[40:41]\n\t"
"v_add_u32 v124, 5, v120\n\t"
"v_and_b32 v124, 2, v124\n\t"
"v_xor_b32 v124, v124, v121\n\t"
"v_add_u32 v122, 2560, v122\n\t"
"v_lshl_add_u32 v245, v124, 4, v122\n\t"
"s_mov_b32 s37, -1\n\t"
"s_mov_b32 s36, 0\n\t"
"s_mul_i32 s67, s73, 48\n\t"
"s_add_u32 s67, s67, 118784\n\t"
"s_mov_b32 s10, 0x200000\n\t"
"s_mov_b32 s11, 0x20000\n\t"
"v_min_u32 v189, 47, v204\n\t"
"v_add_u32 v189, s67, v189\n\t"
"v_mul_u32_u24 v189, 16, v189\n\t"
"s_mov_b32 s54, 663552\n\t"
"s_mov_b32 s55, 0x20000\n\t"
"s_mov_b32 s14, 100663296\n\t"
"s_mov_b32 s15, 0x20000\n\t"
"s_add_u32 m0, s64, 0\n\t"
"s_waitcnt lgkmcnt(0)\n\t"
"s_mov_b32 s52, s82\n\t"
"s_and_b32 s53, s83, 0xffff\n\t"
"buffer_load_dwordx4 v41, s[52:55], s61 offen lds\n\t"
"buffer_load_short_d16_hi v40, v45, s[52:55], s62 offen\n\t"
"s_mov_b32 s8, s78\n\t"
"s_and_b32 s9, s79, 0xffff\n\t"
"buffer_load_dword v188, v189, s[8:11], 0 offen sc1\n\t"
"s_mov_b32 s12, s76\n\t"
"s_and_b32 s13, s77, 0xffff\n\t"
"s_add_u32 m0, s26, 83200\n\t"
"s_nop 0\n\t"
"buffer_load_dwordx4 v241, s[12:15], s22 offen offset:0 nt lds\n\t"
"buffer_load_dwordx4 v242, s[12:15], s22 offen offset:1024 nt lds\n\t"
"buffer_load_dwordx4 v243, s[12:15], s22 offen offset:2048 nt lds\n\t"
"buffer_load_dwordx4 v244, s[12:15], s22 offen offset:3072 nt lds\n\t"
"s_mov_b64 exec, s[36:37]\n\t"
"buffer_load_dwordx4 v245, s[12:15], s22 offen offset:3584 nt lds\n\t"
"s_mov_b64 exec, -1\n\t"
"s_mul_i32 s49, s73, 36\n\t"
"s_mul_i32 s43, s73, 576\n\t"
"s_sub_u32 s42, s22, 203520\n\t"
"s_mov_b32 s44, -1\n\t"
"s_mov_b32 s45, 15\n\t"
"v_min_u32 v124, 35, v204\n\t"
"v_add_u32 v124, s49, v124\n\t"
"v_lshrrev_b32 v119, 3, v124\n\t"
"v_and_b32 v124, 7, v124\n\t"
"v_mul_u32_u24 v127, 0x300, v119\n\t"
"v_cmp_gt_u32 vcc, 9, v119\n\t"
"v_add_u32 v119, -9, v119\n\t"
"v_ashrrev_i32 v125, 31, v119\n\t"
"v_add_u32 v127, s42, v127\n\t"
"v_mad_i32_i24 v127, v125, s48, v127\n\t"
"v_and_b32 v125, 9, v125\n\t"
"v_add_u32 v119, v119, v125\n\t"
"v_and_b32 v125, 2, v119\n\t"
"v_xor_b32 v124, v124, v125\n\t"
"v_lshl_add_u32 v133, v124, 4, v127\n\t"
"s_cmp_eq_u32 s60, 0\n\t"
"s_cselect_b64 s[50:51], vcc, 0\n\t"
"v_cmp_eq_u32 vcc, 0, v119\n\t"
"v_cndmask_b32 v246, v133, 2.0, s[50:51]\n\t"
"s_and_b64 vcc, vcc, s[46:47]\n\t"
"v_cndmask_b32 v246, v246, 2.0, vcc\n\t"
"s_add_u32 m0, s43, 80896\n\t"
"s_mov_b64 exec, s[44:45]\n\t"
"buffer_load_dwordx4 v246, s[12:15], 0 offen offset:0 nt lds\n\t"
"s_mov_b64 exec, -1\n\t"
"s_mul_i32 s70, s66, 16\n\t"
"s_add_u32 s70, s70, 1900544\n\t"
"s_lshl_b32 s61, s73, 8\n\t"
"s_or_b32 s61, s61, s66\n\t"
"s_cmp_lt_u32 s61, 192\n\t"
"s_cselect_b32 s70, s70, 2.0\n\t"
"s_lshr_b32 s61, s66, 3\n\t"
"s_lshl2_add_u32 s61, s61, s73\n\t"
"s_lshl_b32 s60, s61, 8\n\t"
"s_cmp_lt_u32 s61, 96\n\t"
"s_cselect_b32 s60, s60, 2.0\n\t"
"v_lshrrev_b32 v118, 1, v204\n\t"
"v_mul_u32_u24 v118, 220928, v118\n\t"
"v_lshl_add_u32 v118, v204, 7, v118\n\t"
"s_mov_b64 exec, 63\n\t"
"buffer_load_dword v118, v118, s[52:55], s60 offen\n\t"
"s_mov_b64 exec, -1\n\t"
"buffer_load_dword v187, off, s[8:11], s70 sc1\n\t"
"v_add_u32 v107, 0, v204\n\t"
"v_mul_u32_u24 v104, 0xab, v107\n\t"
"v_lshrrev_b32 v104, 10, v104\n\t"
"v_mul_u32_u24 v104, 0x120, v104\n\t"
"v_lshl_add_u32 v104, v107, 4, v104\n\t"
"v_add_u32 v107, 64, v204\n\t"
"v_mul_u32_u24 v105, 0xab, v107\n\t"
"v_lshrrev_b32 v105, 10, v105\n\t"
"v_mul_u32_u24 v105, 0x120, v105\n\t"
"v_lshl_add_u32 v105, v107, 4, v105\n\t"
"v_add_u32 v107, 128, v204\n\t"
"v_mul_u32_u24 v106, 0xab, v107\n\t"
"v_lshrrev_b32 v106, 10, v106\n\t"
"v_mul_u32_u24 v106, 0x120, v106\n\t"
"v_lshl_add_u32 v106, v107, 4, v106\n\t"
"s_mul_i32 s74, s73, 41472\n\t"
"s_mul_i32 s72, s73, 96\n\t"
"s_add_u32 s74, s74, 60160\n\t"
"s_cmp_eq_u32 s73, 0\n\t"
"s_cselect_b32 s74, 53632, s74\n\t"
"s_add_u32 s75, s74, 12288\n\t"
"s_cmp_eq_u32 s73, 3\n\t"
"s_cselect_b32 s74, 16448, s74\n\t"
"s_cselect_b32 s75, 74368, s75\n\t"
"s_lshr_b32 s60, s66, 5\n\t"
"s_bfe_u32 s62, s66, 0x20003\n\t"
"s_and_b32 s61, s60, 1\n\t"
"s_lshl1_add_u32 s61, s73, s61\n\t"
"s_mul_i32 s62, s62, 288\n\t"
"s_add_u32 s61, s61, s62\n\t"
"s_lshr_b32 s60, s60, 1\n\t"
"s_mul_i32 s62, s60, 96\n\t"
"s_add_u32 s61, s61, s62\n\t"
"s_lshl_b32 s57, s61, 10\n\t"
"s_and_b32 s61, s66, 7\n\t"
"s_lshl_b32 s61, s61, 7\n\t"
"s_add_u32 s57, s57, s61\n\t"
"s_cmp_lt_u32 s73, 3\n\t"
"s_cselect_b32 s67, s66, 0xff\n\t"
"s_cmp_lt_u32 s67, 192\n\t"
"s_cselect_b32 s57, s57, 2.0\n\t"
"s_lshl_b32 s60, s73, 3\n\t"
"s_add_u32 s60, s60, 0\n\t"
"s_lshl_b32 s61, s73, 11\n\t"
"s_add_u32 s61, s61, 4096\n\t"
"v_lshl_add_u32 v46, v121, 6, s60\n\t"
"v_lshl_add_u32 v46, v44, 1, v46\n\t"
"v_lshl_add_u32 v42, v204, 1, s61\n\t"
"v_lshl_add_u32 v43, v204, 4, s61\n\t"
"v_mul_u32_u24 v44, 6144, v120\n\t"
"v_lshl_add_u32 v44, v121, 4, v44\n\t"
"v_mov_b32 v32, 0\n\t"
"v_mov_b32 v33, 0\n\t"
"v_mov_b32 v34, 0\n\t"
"v_mov_b32 v35, 0\n\t"
"v_mov_b32 v36, 0\n\t"
"v_mov_b32 v37, 0\n\t"
"v_mov_b32 v38, 0\n\t"
"v_mov_b32 v39, 0\n\t"
"s_waitcnt vmcnt(9)\n\t"
"s_barrier\n\t"
"ds_read_u16_d16_hi v32, v46 offset:0\n\t"
"ds_read_u16_d16_hi v33, v46 offset:512\n\t"
"ds_read_u16_d16_hi v34, v46 offset:1024\n\t"
"ds_read_u16_d16_hi v35, v46 offset:1536\n\t"
"ds_read_u16_d16_hi v36, v46 offset:2048\n\t"
"ds_read_u16_d16_hi v37, v46 offset:2560\n\t"
"ds_read_u16_d16_hi v38, v46 offset:3072\n\t"
"ds_read_u16_d16_hi v39, v46 offset:3584\n\t"
"s_waitcnt lgkmcnt(1)\n\t"
"v_add_f32 v48, v32, v38\n\t"
"s_waitcnt lgkmcnt(0)\n\t"
"v_add_f32 v49, v33, v39\n\t"
"v_add_f32 v50, v34, v40\n\t"
"v_add_f32 v51, v48, v35\n\t"
"v_sub_f32 v54, v48, v35\n\t"
"v_add_f32 v52, v49, v36\n\t"
"v_sub_f32 v55, v49, v36\n\t"
"v_add_f32 v53, v50, v37\n\t"
"v_sub_f32 v56, v50, v37\n\t"
"v_add_f32 v57, v32, v34\n\t"
"v_add_f32 v58, v51, v53\n\t"
"v_add_f32 v59, v54, v56\n\t"
"v_add_f32 v60, v38, v40\n\t"
"v_add_f32 v62, v57, v33\n\t"
"v_sub_f32 v63, v57, v33\n\t"
"v_add_f32 v64, v58, v52\n\t"
"v_sub_f32 v65, v58, v52\n\t"
"v_add_f32 v66, v59, v55\n\t"
"v_sub_f32 v67, v59, v55\n\t"
"v_add_f32 v68, v60, v39\n\t"
"v_sub_f32 v69, v60, v39\n\t"
"v_mul_f32 v71, 0.5, v62\n\t"
"v_mul_f32 v72, 0.5, v63\n\t"
"v_mul_f32 v74, 0.5, v51\n\t"
"v_mul_f32 v75, 0x3e800000, v64\n\t"
"v_mul_f32 v76, 0x3e800000, v65\n\t"
"v_mul_f32 v77, -0.5, v53\n\t"
"v_mul_f32 v79, 0.5, v66\n\t"
"v_mul_f32 v80, 0.5, v67\n\t"
"v_mul_f32 v83, -0.5, v68\n\t"
"v_mul_f32 v84, -0.5, v69\n\t"
"v_cvt_pk_bf16_f32 v86, v32, v71\n\t"
"v_cvt_pk_bf16_f32 v87, v72, -v34\n\t"
"v_cvt_pk_bf16_f32 v88, v74, v75\n\t"
"v_cvt_pk_bf16_f32 v89, v76, v77\n\t"
"v_cvt_pk_bf16_f32 v90, v54, v79\n\t"
"v_cvt_pk_bf16_f32 v91, v80, -v56\n\t"
"v_cvt_pk_bf16_f32 v92, -v38, v83\n\t"
"v_cvt_pk_bf16_f32 v93, v84, v40\n\t"
"ds_write_b16 v42, v86 offset:0\n\t"
"ds_write_b16_d16_hi v42, v86 offset:128\n\t"
"ds_write_b16 v42, v87 offset:256\n\t"
"ds_write_b16_d16_hi v42, v87 offset:384\n\t"
"ds_write_b16 v42, v88 offset:512\n\t"
"ds_write_b16_d16_hi v42, v88 offset:640\n\t"
"ds_write_b16 v42, v89 offset:768\n\t"
"ds_write_b16_d16_hi v42, v89 offset:896\n\t"
"ds_write_b16 v42, v90 offset:1024\n\t"
"ds_write_b16_d16_hi v42, v90 offset:1152\n\t"
"ds_write_b16 v42, v91 offset:1280\n\t"
"ds_write_b16_d16_hi v42, v91 offset:1408\n\t"
"ds_write_b16 v42, v92 offset:1536\n\t"
"ds_write_b16_d16_hi v42, v92 offset:1664\n\t"
"ds_write_b16 v42, v93 offset:1792\n\t"
"ds_write_b16_d16_hi v42, v93 offset:1920\n\t"
"ds_read_b128 v[96:99], v43 offset:0\n\t"
"ds_read_b128 v[100:103], v43 offset:1024\n\t"
"s_add_u32 s60, s57, 0\n\t"
"s_waitcnt lgkmcnt(1)\n\t"
"buffer_store_dwordx4 v[96:99], v44, s[8:11], s60 offen sc1\n\t"
"s_add_u32 s60, s57, 49152\n\t"
"s_waitcnt lgkmcnt(0)\n\t"
"buffer_store_dwordx4 v[100:103], v44, s[8:11], s60 offen sc1\n\t"
"v_lshrrev_b32 v123, 4, v204\n\t"
"v_bfe_u32 v124, v204, 2, 2\n\t"
"v_and_b32 v125, 3, v204\n\t"
"v_lshlrev_b32 v125, 3, v125\n\t"
"v_and_or_b32 v138, v204, 48, v124\n\t"
"v_lshrrev_b32 v139, 1, v125\n\t"
"v_or_b32 v138, v138, v139\n\t"
"s_add_u32 s63, s26, 1152\n\t"
"v_and_b32 v126, 48, v204\n\t"
"v_lshl_add_u32 v126, v121, 6, v126\n\t"
"v_and_b32 v127, 8, v204\n\t"
"v_mul_u32_u24 v134, 1028, v127\n\t"
"v_add_u32 v224, v134, v126\n\t"
"s_add_u32 m0, s74, 0\n\t"
"s_add_u32 s27, s72, 0\n\t"
"buffer_load_dwordx4 v104, s[52:55], s27 offen lds\n\t"
"v_lshl_add_u32 v127, v127, 2, v125\n\t"
"v_lshl_add_u32 v127, v124, 7, v127\n\t"
"v_add_u32 v126, 16, v204\n\t"
"v_and_b32 v126, 32, v126\n\t"
"v_lshl_add_u32 v127, v126, 1, v127\n\t"
"v_mul_u32_u24 v126, 1152, v123\n\t"
"v_add3_u32 v226, v126, v127, s63\n\t"
"v_min_u32 v126, 2, v124\n\t"
"v_cmp_gt_u32 vcc, 48, v204\n\t"
"v_mov_b32 v135, 0x3f80bf80\n\t"
"v_lshlrev_b32 v137, 4, v138\n\t"
"v_cndmask_b32 v126, v126, v123, vcc\n\t"
"v_cndmask_b32 v127, 4, v124, vcc\n\t"
"s_add_u32 m0, s74, 1024\n\t"
"s_add_u32 s27, s72, 0\n\t"
"buffer_load_dwordx4 v105, s[52:55], s27 offen lds\n\t"
"v_and_b32 v134, 1, v126\n\t"
"v_lshl_add_u32 v134, v134, 6, v125\n\t"
"v_lshl_add_u32 v134, v127, 7, v134\n\t"
"v_and_b32 v127, 2, v127\n\t"
"v_lshl_add_u32 v134, v127, 4, v134\n\t"
"v_mul_u32_u24 v126, 2304, v126\n\t"
"v_add3_u32 v227, v126, v134, s26\n\t"
"v_and_b32 v126, 24, v138\n\t"
"v_cmp_eq_u32 vcc, 8, v126\n\t"
"v_mov_b32 v134, 0xbf803f80\n\t"
"v_lshrrev_b32 v126, 3, v138\n\t"
"v_cndmask_b32 v134, v134, v135, vcc\n\t"
"s_add_u32 m0, s74, 2048\n\t"
"s_add_u32 s27, s72, 0\n\t"
"buffer_load_dwordx4 v106, s[52:55], s27 offen lds\n\t"
"v_xor_b32 v126, v126, v138\n\t"
"v_and_b32 v126, 4, v126\n\t"
"v_cmp_eq_u32 vcc, 0, v126\n\t"
"v_mov_b32 v135, 0\n\t"
"v_and_b32 v126, 3, v138\n\t"
"v_cndmask_b32 v134, 0, v134, vcc\n\t"
"v_cmp_eq_u32 vcc, 3, v126\n\t"
"v_lshrrev_b32 v136, 16, v134\n\t"
"v_lshlrev_b64 v[134:135], v137, v[134:135]\n\t"
"v_cndmask_b32 v136, 0, v136, vcc\n\t"
"v_accvgpr_write_b32 a220, v134\n\t"
"v_accvgpr_write_b32 a221, v135\n\t"
"v_accvgpr_write_b32 a222, v136\n\t"
"s_add_u32 m0, s74, 3072\n\t"
"s_add_u32 s27, s72, 12288\n\t"
"buffer_load_dwordx4 v104, s[52:55], s27 offen lds\n\t"
"v_accvgpr_write_b32 a223, 0\n\t"
"s_mov_b32 s48, 384\n\t"
"s_cmp_eq_u32 s65, 31\n\t"
"s_cselect_b64 s[40:41], 0xff, 0\n\t"
"s_cselect_b64 s[46:47], -1, 0\n\t"
"v_xad_u32 v236, v128, 64, s48\n\t"
"v_xad_u32 v237, v129, 64, s48\n\t"
"v_cndmask_b32 v237, v237, 2.0, s[40:41]\n\t"
"s_lshl_b64 s[40:41], s[40:41], 8\n\t"
"v_xad_u32 v238, v130, 64, s48\n\t"
"v_cndmask_b32 v238, v238, 2.0, s[40:41]\n\t"
"s_lshl_b64 s[40:41], s[40:41], 8\n\t"
"v_xad_u32 v239, v131, 64, s48\n\t"
"s_add_u32 m0, s74, 4096\n\t"
"s_add_u32 s27, s72, 12288\n\t"
"buffer_load_dwordx4 v105, s[52:55], s27 offen lds\n\t"
"v_cndmask_b32 v239, v239, 2.0, s[40:41]\n\t"
"s_lshl_b64 s[40:41], s[40:41], 40\n\t"
"v_xad_u32 v240, v245, 64, s48\n\t"
"v_cndmask_b32 v240, v240, 2.0, s[40:41]\n\t"
"v_cmp_eq_u32 vcc, 8, v119\n\t"
"v_xad_u32 v247, v133, 64, s48\n\t"
"s_and_b64 vcc, vcc, s[46:47]\n\t"
"v_cndmask_b32 v247, v247, 2.0, s[50:51]\n\t"
"v_cndmask_b32 v247, v247, 2.0, vcc\n\t"
"v_and_b32 v139, 7, v138\n\t"
"v_bfe_u32 v126, v138, 3, 1\n\t"
"v_lshrrev_b32 v127, 4, v138\n\t"
"s_add_u32 m0, s74, 5120\n\t"
"s_add_u32 s27, s72, 12288\n\t"
"buffer_load_dwordx4 v106, s[52:55], s27 offen lds\n\t"
"v_sub_u32 v127, v127, v126\n\t"
"v_mov_b32 v134, 0xbf803f80\n\t"
"v_mov_b32 v135, 0x3f80bf80\n\t"
"v_cmp_eq_u32 vcc, 0, v127\n\t"
"v_add_u32 v126, 4, v126\n\t"
"s_nop 0\n\t"
"v_cndmask_b32 v134, 0, v134, vcc\n\t"
"v_cmp_eq_u32 vcc, 1, v127\n\t"
"s_nop 1\n\t"
"v_cndmask_b32 v134, v134, v135, vcc\n\t"
"v_cmp_eq_u32 vcc, 7, v139\n\t"
"s_nop 1\n\t"
"v_cndmask_b32 v135, 0, v135, vcc\n\t"
"v_cmp_gt_u32 vcc, 48, v138\n\t"
"s_nop 1\n\t"
"v_cndmask_b32 v134, v135, v134, vcc\n\t"
"v_mov_b32 v135, 0\n\t"
"s_add_u32 m0, s74, 6144\n\t"
"s_add_u32 s27, s72, 442368\n\t"
"buffer_load_dwordx4 v104, s[52:55], s27 offen lds\n\t"
"v_cndmask_b32 v136, v126, v139, vcc\n\t"
"v_and_b32 v137, 1, v136\n\t"
"v_lshlrev_b32 v137, 4, v137\n\t"
"v_lshrrev_b32 v136, 1, v136\n\t"
"v_lshlrev_b64 v[134:135], v137, v[134:135]\n\t"
"v_cmp_eq_u32 vcc, 0, v136\n\t"
"s_nop 1\n\t"
"v_cndmask_b32 v140, 0, v134, vcc\n\t"
"v_accvgpr_write_b32 a224, v140\n\t"
"v_cndmask_b32 v141, 0, v135, vcc\n\t"
"v_cmp_eq_u32 vcc, 1, v136\n\t"
"v_and_b32 v140, 0xff7fff7f, v140\n\t"
"v_xor_b32 v148, 0x80008000, v140\n\t"
"v_cndmask_b32 v141, v141, v134, vcc\n\t"
"s_add_u32 m0, s74, 7168\n\t"
"s_add_u32 s27, s72, 442368\n\t"
"buffer_load_dwordx4 v105, s[52:55], s27 offen lds\n\t"
"v_cndmask_b32 v142, 0, v135, vcc\n\t"
"v_cmp_eq_u32 vcc, 2, v136\n\t"
"v_accvgpr_write_b32 a225, v141\n\t"
"v_accvgpr_write_b32 a216, v140\n\t"
"v_cndmask_b32 v142, v142, v134, vcc\n\t"
"v_cndmask_b32 v143, 0, v135, vcc\n\t"
"v_cmp_eq_u32 vcc, 3, v136\n\t"
"v_accvgpr_write_b32 a226, v142\n\t"
"v_and_b32 v141, 0xff7fff7f, v141\n\t"
"v_cndmask_b32 v143, v143, v134, vcc\n\t"
"v_accvgpr_write_b32 a227, v143\n\t"
"v_xor_b32 v149, 0x80008000, v141\n\t"
"s_add_u32 m0, s74, 8192\n\t"
"s_add_u32 s27, s72, 442368\n\t"
"buffer_load_dwordx4 v106, s[52:55], s27 offen lds\n\t"
"v_accvgpr_write_b32 a217, v141\n\t"
"v_and_b32 v142, 0xff7fff7f, v142\n\t"
"v_xor_b32 v150, 0x80008000, v142\n\t"
"v_accvgpr_write_b32 a218, v142\n\t"
"v_and_b32 v143, 0xff7fff7f, v143\n\t"
"v_xor_b32 v151, 0x80008000, v143\n\t"
"v_accvgpr_write_b32 a219, v143\n\t"
"s_mov_b32 s68, 3000\n\t"
"s_mul_i32 s61, s73, 294912\n\t"
"v_lshl_add_u32 v235, v204, 4, s61\n\t"
"v_and_b32 v107, 51, v204\n\t"
"v_mul_u32_u24 v107, 24, v107\n\t"
"v_and_b32 v47, 8, v204\n\t"
"s_add_u32 m0, s74, 9216\n\t"
"s_add_u32 s27, s72, 454656\n\t"
"buffer_load_dwordx4 v104, s[52:55], s27 offen lds\n\t"
"v_mad_u32_u24 v107, v47, 12, v107\n\t"
"v_and_b32 v47, 4, v204\n\t"
"v_mul_u32_u24 v47, 0x180, v47\n\t"
"v_add_u32 v107, v47, v107\n\t"
"v_add_u32 v47, s75, v107\n\t"
"v_add_u32 v107, s74, v107\n\t"
"v_lshrrev_b32 v112, 4, v204\n\t"
"v_bfe_u32 v113, v204, 2, 2\n\t"
"v_cmp_eq_u32 vcc, v112, v113\n\t"
"v_lshlrev_b32 v112, 4, v204\n\t"
"v_mov_b32 v113, 0x3e80\n\t"
"v_lshlrev_b32 v113, v112, v113\n\t"
"v_cndmask_b32 v113, 0, v113, vcc\n\t"
"s_add_u32 m0, s74, 10240\n\t"
"s_add_u32 s27, s72, 454656\n\t"
"buffer_load_dwordx4 v105, s[52:55], s27 offen lds\n\t"
"v_and_b32 v112, 2, v204\n\t"
"v_cmp_eq_u32 vcc, 0, v112\n\t"
"v_mov_b32 v108, 0\n\t"
"v_mov_b32 v109, 0\n\t"
"v_cndmask_b32 v110, 0, v113, vcc\n\t"
"v_cndmask_b32 v111, v113, 0, vcc\n\t"
"v_xor_b32 v114, 0x80008000, v110\n\t"
"v_xor_b32 v115, 0x80008000, v111\n\t"
"v_mov_b32 v112, 0\n\t"
"v_mov_b32 v113, 0\n\t"
"v_mov_b32 v116, 0\n\t"
"v_mov_b32 v117, 0\n\t"
"s_add_u32 m0, s74, 11264\n\t"
"s_add_u32 s27, s72, 454656\n\t"
"buffer_load_dwordx4 v106, s[52:55], s27 offen lds\n\t"
"s_waitcnt vmcnt(16)\n\t"
"s_waitcnt vmcnt(12)\n\t"
"v_readfirstlane_b32 s71, v187\n\t"
"s_barrier\n\t"
"s_add_u32 s71, s71, 1\n\t"
"v_mov_b32 v215, s71\n\t"
"s_mov_b64 exec, 1\n\t"
"buffer_store_dword v215, off, s[8:11], s70 sc1\n\t"
"s_mov_b64 exec, -1\n\t"
"v_add_u32 v248, 80896, v226\n\t"
"v_xor_b32 v249, 32, v248\n\t"
"v_xor_b32 v250, 64, v248\n\t"
"v_xor_b32 v251, 96, v248\n\t"
"ds_read_b64_tr_b16 a[176:177], v248 offset:0\n\t"
"ds_read_b64_tr_b16 a[178:179], v248 offset:512\n\t"
"ds_read_b64_tr_b16 a[184:185], v249 offset:0\n\t"
"ds_read_b64_tr_b16 a[186:187], v249 offset:512\n\t"
"s_add_u32 m0, s75, 0\n\t"
"s_add_u32 s27, s72, 221184\n\t"
"buffer_load_dwordx4 v104, s[52:55], s27 offen lds\n\t"
"ds_read_b64_tr_b16 a[180:181], v248 offset:1024\n\t"
"ds_read_b64_tr_b16 a[188:189], v249 offset:1024\n\t"
"s_add_u32 m0, s75, 1024\n\t"
"s_add_u32 s27, s72, 221184\n\t"
"buffer_load_dwordx4 v105, s[52:55], s27 offen lds\n\t"
"ds_read_b64_tr_b16 a[192:193], v250 offset:0\n\t"
"ds_read_b64_tr_b16 a[194:195], v250 offset:512\n\t"
"ds_read_b64_tr_b16 a[200:201], v251 offset:0\n\t"
"ds_read_b64_tr_b16 a[202:203], v251 offset:512\n\t"
"s_add_u32 m0, s75, 2048\n\t"
"s_add_u32 s27, s72, 221184\n\t"
"buffer_load_dwordx4 v106, s[52:55], s27 offen lds\n\t"
"ds_read_b64_tr_b16 a[196:197], v250 offset:1024\n\t"
"ds_read_b64_tr_b16 a[204:205], v251 offset:1024\n\t"
"s_add_u32 m0, s75, 3072\n\t"
"s_add_u32 s27, s72, 233472\n\t"
"buffer_load_dwordx4 v104, s[52:55], s27 offen lds\n\t"
"s_waitcnt lgkmcnt(10)\n\t"
"v_mfma_f32_16x16x32_bf16 v[0:3], a[220:223], a[176:179], 0\n\t"
"s_waitcnt lgkmcnt(8)\n\t"
"v_mfma_f32_16x16x32_bf16 v[4:7], a[220:223], a[184:187], 0\n\t"
"s_waitcnt lgkmcnt(7)\n\t"
"v_mfma_f32_16x16x32_bf16 v[8:11], a[220:223], a[178:181], 0\n\t"
"s_waitcnt lgkmcnt(6)\n\t"
"v_mfma_f32_16x16x32_bf16 v[12:15], a[220:223], a[186:189], 0\n\t"
"s_add_u32 m0, s75, 4096\n\t"
"s_add_u32 s27, s72, 233472\n\t"
"buffer_load_dwordx4 v105, s[52:55], s27 offen lds\n\t"
"s_add_u32 m0, s75, 5120\n\t"
"s_add_u32 s27, s72, 233472\n\t"
"buffer_load_dwordx4 v106, s[52:55], s27 offen lds\n\t"
"s_waitcnt lgkmcnt(4)\n\t"
"v_mfma_f32_16x16x32_bf16 v[16:19], a[220:223], a[192:195], 0\n\t"
"s_waitcnt lgkmcnt(2)\n\t"
"v_mfma_f32_16x16x32_bf16 v[20:23], a[220:223], a[200:203], 0\n\t"
"s_waitcnt lgkmcnt(1)\n\t"
"v_mfma_f32_16x16x32_bf16 v[24:27], a[220:223], a[194:197], 0\n\t"
"s_waitcnt lgkmcnt(0)\n\t"
"v_mfma_f32_16x16x32_bf16 v[28:31], a[220:223], a[202:205], 0\n\t"
"s_add_u32 m0, s26, 124544\n\t"
"s_nop 0\n\t"
"buffer_load_dwordx4 v241, s[12:15], s22 offen offset:128 nt lds\n\t"
"buffer_load_dwordx4 v242, s[12:15], s22 offen offset:1152 nt lds\n\t"
"v_cvt_pk_bf16_f32 v0, v0, v4\n\t"
"v_cvt_pk_bf16_f32 v1, v1, v5\n\t"
"v_cvt_pk_bf16_f32 v2, v2, v6\n\t"
"v_cvt_pk_bf16_f32 v3, v3, v7\n\t"
"s_lshl_b32 m0, s73, 9\n\t"
"s_nop 0\n\t"
"ds_write_addtid_b32 v0 offset:0\n\t"
"ds_write_addtid_b32 v1 offset:8224\n\t"
"ds_write_addtid_b32 v2 offset:2048\n\t"
"ds_write_addtid_b32 v3 offset:10272\n\t"
"v_cvt_pk_bf16_f32 v8, v8, v12\n\t"
"v_cvt_pk_bf16_f32 v9, v9, v13\n\t"
"v_cvt_pk_bf16_f32 v10, v10, v14\n\t"
"v_cvt_pk_bf16_f32 v11, v11, v15\n\t"
"ds_write_addtid_b32 v8 offset:256\n\t"
"ds_write_addtid_b32 v9 offset:8480\n\t"
"ds_write_addtid_b32 v10 offset:2304\n\t"
"ds_write_addtid_b32 v11 offset:10528\n\t"
"v_cvt_pk_bf16_f32 v16, v16, v20\n\t"
"v_cvt_pk_bf16_f32 v17, v17, v21\n\t"
"v_cvt_pk_bf16_f32 v18, v18, v22\n\t"
"v_cvt_pk_bf16_f32 v19, v19, v23\n\t"
"ds_write_addtid_b32 v16 offset:4096\n\t"
"ds_write_addtid_b32 v17 offset:12320\n\t"
"ds_write_addtid_b32 v18 offset:6144\n\t"
"ds_write_addtid_b32 v19 offset:14368\n\t"
"v_cvt_pk_bf16_f32 v24, v24, v28\n\t"
"v_cvt_pk_bf16_f32 v25, v25, v29\n\t"
"v_cvt_pk_bf16_f32 v26, v26, v30\n\t"
"v_cvt_pk_bf16_f32 v27, v27, v31\n\t"
"ds_write_addtid_b32 v24 offset:4352\n\t"
"ds_write_addtid_b32 v25 offset:12576\n\t"
"ds_write_addtid_b32 v26 offset:6400\n\t"
"ds_write_addtid_b32 v27 offset:14624\n\t"
"v_add_u32 v248, 80896, v227\n\t"
"v_xor_b32 v249, 32, v248\n\t"
"v_xor_b32 v250, 64, v248\n\t"
"v_xor_b32 v251, 96, v248\n\t"
"ds_read_b64_tr_b16 a[176:177], v248 offset:0\n\t"
"ds_read_b64_tr_b16 a[178:179], v248 offset:512\n\t"
"ds_read_b64_tr_b16 a[180:181], v249 offset:0\n\t"
"ds_read_b64_tr_b16 a[182:183], v249 offset:512\n\t"
"s_add_u32 m0, s26, 124544\n\t"
"s_nop 0\n\t"
"buffer_load_dwordx4 v243, s[12:15], s22 offen offset:2176 nt lds\n\t"
"ds_read_b64_tr_b16 a[184:185], v250 offset:0\n\t"
"ds_read_b64_tr_b16 a[186:187], v250 offset:512\n\t"
"ds_read_b64_tr_b16 a[188:189], v251 offset:0\n\t"
"ds_read_b64_tr_b16 a[190:191], v251 offset:512\n\t"
"buffer_load_dwordx4 v244, s[12:15], s22 offen offset:3200 nt lds\n\t"
"ds_read_b64_tr_b16 a[192:193], v248 offset:1152\n\t"
"ds_read_b64_tr_b16 a[194:195], v248 offset:1664\n\t"
"ds_read_b64_tr_b16 a[196:197], v249 offset:1152\n\t"
"ds_read_b64_tr_b16 a[198:199], v249 offset:1664\n\t"
"s_mov_b64 exec, s[36:37]\n\t"
"buffer_load_dwordx4 v245, s[12:15], s22 offen offset:3712 nt lds\n\t"
"s_mov_b64 exec, -1\n\t"
"ds_read_b64_tr_b16 a[200:201], v250 offset:1152\n\t"
"ds_read_b64_tr_b16 a[202:203], v250 offset:1664\n\t"
"ds_read_b64_tr_b16 a[204:205], v251 offset:1152\n\t"
"ds_read_b64_tr_b16 a[206:207], v251 offset:1664\n\t"
"s_add_u32 m0, s43, 122240\n\t"
"s_mov_b64 exec, s[44:45]\n\t"
"buffer_load_dwordx4 v246, s[12:15], 0 offen offset:128 nt lds\n\t"
"s_mov_b64 exec, -1\n\t"
"s_waitcnt vmcnt(19)\n\t"
"ds_read_b64_tr_b16 a[48:49], v107 offset:0\n\t"
"ds_read_b64_tr_b16 a[50:51], v107 offset:192\n\t"
"ds_read_b64_tr_b16 a[52:53], v107 offset:3072\n\t"
"ds_read_b64_tr_b16 a[54:55], v107 offset:3264\n\t"
"ds_read_b64_tr_b16 a[56:57], v107 offset:8\n\t"
"ds_read_b64_tr_b16 a[58:59], v107 offset:200\n\t"
"ds_read_b64_tr_b16 a[60:61], v107 offset:3080\n\t"
"ds_read_b64_tr_b16 a[62:63], v107 offset:3272\n\t"
"ds_read_b64_tr_b16 a[64:65], v107 offset:16\n\t"
"ds_read_b64_tr_b16 a[66:67], v107 offset:208\n\t"
"ds_read_b64_tr_b16 a[68:69], v107 offset:3088\n\t"
"ds_read_b64_tr_b16 a[70:71], v107 offset:3280\n\t"
"s_waitcnt vmcnt(13)\n\t"
"ds_read_b64_tr_b16 a[72:73], v107 offset:6144\n\t"
"ds_read_b64_tr_b16 a[74:75], v107 offset:6336\n\t"
"ds_read_b64_tr_b16 a[76:77], v107 offset:9216\n\t"
"ds_read_b64_tr_b16 a[78:79], v107 offset:9408\n\t"
"ds_read_b64_tr_b16 a[80:81], v107 offset:6152\n\t"
"ds_read_b64_tr_b16 a[82:83], v107 offset:6344\n\t"
"ds_read_b64_tr_b16 a[84:85], v107 offset:9224\n\t"
"ds_read_b64_tr_b16 a[86:87], v107 offset:9416\n\t"
"ds_read_b64_tr_b16 a[88:89], v107 offset:6160\n\t"
"ds_read_b64_tr_b16 a[90:91], v107 offset:6352\n\t"
"ds_read_b64_tr_b16 a[92:93], v107 offset:9232\n\t"
"ds_read_b64_tr_b16 a[94:95], v107 offset:9424\n\t"
"s_waitcnt lgkmcnt(15)\n\t"
"v_mfma_f32_16x16x32_bf16 v[0:3], v[110:113], a[48:51], 0\n\t"
"s_waitcnt lgkmcnt(10)\n\t"
"v_mfma_f32_16x16x32_bf16 v[0:3], v[110:113], a[72:75], v[0:3]\n\t"
"v_mfma_f32_16x16x32_bf16 v[8:11], v[108:111], a[48:51], 0\n\t"
"v_mfma_f32_16x16x32_bf16 v[8:11], v[108:111], a[72:75], v[8:11]\n\t"
"s_add_u32 m0, s26, 34944\n\t"
"s_nop 0\n\t"
"buffer_load_dwordx4 v241, s[12:15], s22 offen offset:256 nt lds\n\t"
"v_mfma_f32_16x16x32_bf16 v[16:19], v[110:113], a[52:55], 0\n\t"
"s_waitcnt lgkmcnt(8)\n\t"
"v_mfma_f32_16x16x32_bf16 v[16:19], v[110:113], a[76:79], v[16:19]\n\t"
"v_mfma_f32_16x16x32_bf16 v[24:27], v[108:111], a[52:55], 0\n\t"
"v_mfma_f32_16x16x32_bf16 v[24:27], v[108:111], a[76:79], v[24:27]\n\t"
"buffer_load_dwordx4 v242, s[12:15], s22 offen offset:1280 nt lds\n\t"
"s_waitcnt vmcnt(8)\n\t"
"ds_read_b64_tr_b16 a[0:1], v47 offset:0\n\t"
"ds_read_b64_tr_b16 a[2:3], v47 offset:192\n\t"
"ds_read_b64_tr_b16 a[4:5], v47 offset:3072\n\t"
"ds_read_b64_tr_b16 a[6:7], v47 offset:3264\n\t"
"ds_read_b64_tr_b16 a[8:9], v47 offset:8\n\t"
"ds_read_b64_tr_b16 a[10:11], v47 offset:200\n\t"
"ds_read_b64_tr_b16 a[12:13], v47 offset:3080\n\t"
"ds_read_b64_tr_b16 a[14:15], v47 offset:3272\n\t"
"ds_read_b64_tr_b16 a[16:17], v47 offset:16\n\t"
"ds_read_b64_tr_b16 a[18:19], v47 offset:208\n\t"
"ds_read_b64_tr_b16 a[20:21], v47 offset:3088\n\t"
"ds_read_b64_tr_b16 a[22:23], v47 offset:3280\n\t"
"v_mfma_f32_16x16x32_bf16 v[32:35], v[110:113], a[56:59], 0\n\t"
"s_waitcnt lgkmcnt(15)\n\t"
"v_mfma_f32_16x16x32_bf16 v[32:35], v[110:113], a[80:83], v[32:35]\n\t"
"v_mfma_f32_16x16x32_bf16 v[96:99], v[108:111], a[56:59], 0\n\t"
"v_mfma_f32_16x16x32_bf16 v[96:99], v[108:111], a[80:83], v[96:99]\n\t"
"buffer_load_dwordx4 v243, s[12:15], s22 offen offset:2304 nt lds\n\t"
"v_mfma_f32_16x16x32_bf16 v[120:123], v[110:113], a[60:63], 0\n\t"
"v_mfma_f32_16x16x32_bf16 v[120:123], v[110:113], a[84:87], v[120:123]\n\t"
"v_mfma_f32_16x16x32_bf16 v[128:131], v[108:111], a[60:63], 0\n\t"
"v_mfma_f32_16x16x32_bf16 v[128:131], v[108:111], a[84:87], v[128:131]\n\t"
"buffer_load_dwordx4 v244, s[12:15], s22 offen offset:3328 nt lds\n\t"
"v_mfma_f32_16x16x32_bf16 v[136:139], v[110:113], a[64:67], 0\n\t"
"s_waitcnt lgkmcnt(14)\n\t"
"v_mfma_f32_16x16x32_bf16 v[136:139], v[110:113], a[88:91], v[136:139]\n\t"
"v_mfma_f32_16x16x32_bf16 v[152:155], v[108:111], a[64:67], 0\n\t"
"v_mfma_f32_16x16x32_bf16 v[152:155], v[108:111], a[88:91], v[152:155]\n\t"
"s_mov_b64 exec, s[36:37]\n\t"
"buffer_load_dwordx4 v245, s[12:15], s22 offen offset:3840 nt lds\n\t"
"s_mov_b64 exec, -1\n\t"
"v_mfma_f32_16x16x32_bf16 v[160:163], v[110:113], a[68:71], 0\n\t"
"s_waitcnt lgkmcnt(12)\n\t"
"v_mfma_f32_16x16x32_bf16 v[160:163], v[110:113], a[92:95], v[160:163]\n\t"
"v_mfma_f32_16x16x32_bf16 v[168:171], v[108:111], a[68:71], 0\n\t"
"v_mfma_f32_16x16x32_bf16 v[168:171], v[108:111], a[92:95], v[168:171]\n\t"
"s_add_u32 m0, s43, 32640\n\t"
"s_mov_b64 exec, s[44:45]\n\t"
"buffer_load_dwordx4 v246, s[12:15], 0 offen offset:256 nt lds\n\t"
"s_mov_b64 exec, -1\n\t"
"s_waitcnt lgkmcnt(10)\n\t"
"v_mfma_f32_16x16x32_bf16 v[4:7], v[110:113], a[0:3], v[0:3]\n\t"
"v_mfma_f32_16x16x32_bf16 v[0:3], v[114:117], a[0:3], v[0:3]\n\t"
"v_mfma_f32_16x16x32_bf16 v[12:15], v[108:111], a[0:3], v[8:11]\n\t"
"v_mfma_f32_16x16x32_bf16 v[8:11], v[112:115], a[0:3], v[8:11]\n\t"
"s_waitcnt lgkmcnt(8)\n\t"
"v_mfma_f32_16x16x32_bf16 v[20:23], v[110:113], a[4:7], v[16:19]\n\t"
"v_mfma_f32_16x16x32_bf16 v[16:19], v[114:117], a[4:7], v[16:19]\n\t"
"v_mfma_f32_16x16x32_bf16 v[28:31], v[108:111], a[4:7], v[24:27]\n\t"
"s_nop 0\n\t"
"v_cvt_pk_bf16_f32 v48, v4, v5\n\t"
"v_cvt_pk_bf16_f32 v49, v6, v7\n\t"
"v_mfma_f32_16x16x32_bf16 v[24:27], v[112:115], a[4:7], v[24:27]\n\t"
"v_cvt_pk_bf16_f32 v72, v0, v1\n\t"
"v_cvt_pk_bf16_f32 v73, v2, v3\n\t"
"s_waitcnt lgkmcnt(6)\n\t"
"v_mfma_f32_16x16x32_bf16 v[36:39], v[110:113], a[8:11], v[32:35]\n\t"
"v_cvt_pk_bf16_f32 v50, v12, v13\n\t"
"v_cvt_pk_bf16_f32 v51, v14, v15\n\t"
"v_mfma_f32_16x16x32_bf16 v[32:35], v[114:117], a[8:11], v[32:35]\n\t"
"v_cvt_pk_bf16_f32 v74, v8, v9\n\t"
"v_cvt_pk_bf16_f32 v75, v10, v11\n\t"
"v_mfma_f32_16x16x32_bf16 v[100:103], v[108:111], a[8:11], v[96:99]\n\t"
"v_cvt_pk_bf16_f32 v52, v20, v21\n\t"
"v_cvt_pk_bf16_f32 v53, v22, v23\n\t"
"v_mfma_f32_16x16x32_bf16 v[96:99], v[112:115], a[8:11], v[96:99]\n\t"
"v_cvt_pk_bf16_f32 v76, v16, v17\n\t"
"v_cvt_pk_bf16_f32 v77, v18, v19\n\t"
"s_waitcnt lgkmcnt(4)\n\t"
"v_mfma_f32_16x16x32_bf16 v[124:127], v[110:113], a[12:15], v[120:123]\n\t"
"v_cvt_pk_bf16_f32 v54, v28, v29\n\t"
"v_cvt_pk_bf16_f32 v55, v30, v31\n\t"
"v_mfma_f32_16x16x32_bf16 v[120:123], v[114:117], a[12:15], v[120:123]\n\t"
"v_cvt_pk_bf16_f32 v78, v24, v25\n\t"
"v_cvt_pk_bf16_f32 v79, v26, v27\n\t"
"v_mfma_f32_16x16x32_bf16 v[132:135], v[108:111], a[12:15], v[128:131]\n\t"
"v_cvt_pk_bf16_f32 v56, v36, v37\n\t"
"v_cvt_pk_bf16_f32 v57, v38, v39\n\t"
"v_mfma_f32_16x16x32_bf16 v[128:131], v[112:115], a[12:15], v[128:131]\n\t"
"v_cvt_pk_bf16_f32 v80, v32, v33\n\t"
"v_cvt_pk_bf16_f32 v81, v34, v35\n\t"
"s_waitcnt lgkmcnt(2)\n\t"
"v_mfma_f32_16x16x32_bf16 v[140:143], v[110:113], a[16:19], v[136:139]\n\t"
"v_cvt_pk_bf16_f32 v58, v100, v101\n\t"
"v_cvt_pk_bf16_f32 v59, v102, v103\n\t"
"v_mfma_f32_16x16x32_bf16 v[136:139], v[114:117], a[16:19], v[136:139]\n\t"
"v_cvt_pk_bf16_f32 v82, v96, v97\n\t"
"v_cvt_pk_bf16_f32 v83, v98, v99\n\t"
"v_mfma_f32_16x16x32_bf16 v[156:159], v[108:111], a[16:19], v[152:155]\n\t"
"v_cvt_pk_bf16_f32 v60, v124, v125\n\t"
"v_cvt_pk_bf16_f32 v61, v126, v127\n\t"
"v_mfma_f32_16x16x32_bf16 v[152:155], v[112:115], a[16:19], v[152:155]\n\t"
"v_cvt_pk_bf16_f32 v84, v120, v121\n\t"
"v_cvt_pk_bf16_f32 v85, v122, v123\n\t"
"s_waitcnt lgkmcnt(0)\n\t"
"v_mfma_f32_16x16x32_bf16 v[164:167], v[110:113], a[20:23], v[160:163]\n\t"
"v_cvt_pk_bf16_f32 v62, v132, v133\n\t"
"v_cvt_pk_bf16_f32 v63, v134, v135\n\t"
"v_mfma_f32_16x16x32_bf16 v[160:163], v[114:117], a[20:23], v[160:163]\n\t"
"v_cvt_pk_bf16_f32 v86, v128, v129\n\t"
"v_cvt_pk_bf16_f32 v87, v130, v131\n\t"
"v_mfma_f32_16x16x32_bf16 v[172:175], v[108:111], a[20:23], v[168:171]\n\t"
"v_cvt_pk_bf16_f32 v64, v140, v141\n\t"
"v_cvt_pk_bf16_f32 v65, v142, v143\n\t"
"v_mfma_f32_16x16x32_bf16 v[168:171], v[112:115], a[20:23], v[168:171]\n\t"
"v_cvt_pk_bf16_f32 v88, v136, v137\n\t"
"v_cvt_pk_bf16_f32 v89, v138, v139\n\t"
"v_cvt_pk_bf16_f32 v66, v156, v157\n\t"
"v_cvt_pk_bf16_f32 v67, v158, v159\n\t"
"v_cvt_pk_bf16_f32 v90, v152, v153\n\t"
"v_cvt_pk_bf16_f32 v91, v154, v155\n\t"
"v_cvt_pk_bf16_f32 v68, v164, v165\n\t"
"v_cvt_pk_bf16_f32 v69, v166, v167\n\t"
"v_cvt_pk_bf16_f32 v92, v160, v161\n\t"
"v_cvt_pk_bf16_f32 v93, v162, v163\n\t"
"v_cvt_pk_bf16_f32 v70, v172, v173\n\t"
"v_cvt_pk_bf16_f32 v71, v174, v175\n\t"
"v_cvt_pk_bf16_f32 v94, v168, v169\n\t"
"v_cvt_pk_bf16_f32 v95, v170, v171\n\t"
"s_barrier\n\t"
"v_mfma_f32_16x16x32_bf16 v[192:195], a[216:219], a[176:179], 0\n\t"
"s_lshl_b32 m0, s73, 9\n\t"
"ds_read_b128 a[144:147], v224 offset:0\n\t"
"v_mfma_f32_16x16x32_bf16 v[196:199], a[216:219], a[180:183], 0\n\t"
"s_nop 0\n\t"
"ds_read_b128 a[160:163], v224 offset:4096\n\t"
"ds_read_b128 a[168:171], v224 offset:5120\n\t"
"v_mfma_f32_16x16x32_bf16 v[200:203], a[216:219], a[184:187], 0\n\t"
"s_mov_b32 s16, s80\n\t"
"ds_read_b128 a[148:151], v224 offset:512\n\t"
"ds_read_b128 a[152:155], v224 offset:1024\n\t"
"v_mfma_f32_16x16x32_bf16 v[204:207], a[216:219], a[188:191], 0\n\t"
"s_and_b32 s17, s81, 0xffff\n\t"
"ds_read_b128 a[164:167], v224 offset:4608\n\t"
"ds_read_b128 a[172:175], v224 offset:5632\n\t"
"v_mfma_f32_16x16x32_bf16 v[208:211], v[148:151], a[192:195], 0\n\t"
"s_mov_b32 s18, 100663296\n\t"
"ds_read_b128 a[156:159], v224 offset:1536\n\t"
"v_add_u32 v252, 4096, v235\n\t"
"v_mfma_f32_16x16x32_bf16 v[212:215], v[148:151], a[196:199], 0\n\t"
"s_waitcnt lgkmcnt(6)\n\t"
"v_cvt_pk_bf16_f32 v194, v194, v198\n\t"
"v_cvt_pk_bf16_f32 v195, v195, v199\n\t"
"s_mov_b32 s19, 0x20000\n\t"
"v_mfma_f32_16x16x32_bf16 v[96:99], v[48:51], a[144:147], 0\n\t"
"s_mov_b32 s21, 4\n\t"
"ds_write_addtid_b32 v194 offset:24672\n\t"
"v_cvt_pk_bf16_f32 v192, v192, v196\n\t"
"v_mfma_f32_16x16x32_bf16 v[96:99], v[52:55], a[160:163], v[96:99]\n\t"
"s_mov_b32 s38, -2.0\n\t"
"ds_write_addtid_b32 v192 offset:16448\n\t"
"ds_write_addtid_b32 v195 offset:24928\n\t"
"v_cvt_pk_bf16_f32 v203, v203, v207\n\t"
"v_mfma_f32_16x16x32_bf16 v[112:115], v[56:59], a[144:147], 0\n\t"
"s_mov_b32 s39, -2.0\n\t"
"v_mbcnt_lo_u32_b32 v176, -1, 0\n\t"
"v_mbcnt_hi_u32_b32 v176, -1, v176\n\t"
"v_mfma_f32_16x16x32_bf16 v[112:115], v[60:63], a[160:163], v[112:115]\n\t"
"ds_write_addtid_b32 v203 offset:29024\n\t"
"v_cvt_pk_bf16_f32 v208, v208, v212\n\t"
"v_mfma_f32_16x16x32_bf16 v[128:131], v[64:67], a[144:147], 0\n\t"
"ds_read_b128 a[144:147], v224 offset:2048\n\t"
"v_bfe_u32 v156, v176, 2, 2\n\t"
"v_mfma_f32_16x16x32_bf16 v[128:131], v[68:71], a[160:163], v[128:131]\n\t"
"s_waitcnt lgkmcnt(7)\n\t"
"ds_write_addtid_b32 v208 offset:18496\n\t"
"buffer_load_dword v190, v189, s[8:11], 0 offen sc1\n\t"
"v_and_b32 v157, 3, v176\n\t"
"v_lshlrev_b32 v157, 3, v157\n\t"
"v_and_or_b32 v170, v176, 48, v156\n\t"
"v_lshrrev_b32 v171, 1, v157\n\t"
"v_or_b32 v170, v170, v171\n\t"
"v_and_b32 v158, 24, v170\n\t"
"v_cvt_pk_bf16_f32 v193, v193, v197\n\t"
"v_cvt_pk_bf16_f32 v200, v200, v204\n\t"
"v_cvt_pk_bf16_f32 v209, v209, v213\n\t"
"v_mfma_f32_16x16x32_bf16 v[132:135], v[64:67], a[148:151], 0\n\t"
"v_cmp_eq_u32 vcc, 8, v158\n\t"
"v_cvt_pk_bf16_f32 v210, v210, v214\n\t"
"v_mfma_f32_16x16x32_bf16 v[132:135], v[68:71], a[164:167], v[132:135]\n\t"
"ds_read_b128 a[160:163], v224 offset:6144\n\t"
"v_mov_b32 v166, 0x3f80\n\t"
"v_cvt_pk_bf16_f32 v202, v202, v206\n\t"
"v_mfma_f32_16x16x32_bf16 v[116:119], v[56:59], a[148:151], 0\n\t"
"v_mov_b32 v167, 0xbf80\n\t"
"v_cndmask_b32 v168, v167, v166, vcc\n\t"
"v_mfma_f32_16x16x32_bf16 v[116:119], v[60:63], a[164:167], v[116:119]\n\t"
"ds_write_addtid_b32 v193 offset:16704\n\t"
"v_cvt_pk_bf16_f32 v211, v211, v215\n\t"
"s_waitcnt vmcnt(7)\n\t"
"s_barrier\n\t"
"v_mfma_f32_16x16x32_bf16 v[216:219], v[148:151], a[200:203], 0\n\t"
"ds_write_addtid_b32 v202 offset:28768\n\t"
"v_cndmask_b32 v166, v166, v167, vcc\n\t"
"v_cvt_pk_bf16_f32 v201, v201, v205\n\t"
"v_mfma_f32_16x16x32_bf16 v[100:103], v[48:51], a[148:151], 0\n\t"
"ds_write_addtid_b32 v210 offset:26720\n\t"
"v_lshrrev_b32 v158, 3, v170\n\t"
"v_mfma_f32_16x16x32_bf16 v[100:103], v[52:55], a[164:167], v[100:103]\n\t"
"v_xor_b32 v158, v158, v170\n\t"
"v_and_b32 v158, 4, v158\n\t"
"v_cmp_eq_u32 vcc, 0, v158\n\t"
"v_mfma_f32_16x16x32_bf16 v[104:107], v[48:51], a[152:155], 0\n\t"
"ds_read_b64_tr_b16 a[176:177], v248 offset:41472\n\t"
"v_mov_b32 v167, 0\n\t"
"v_mfma_f32_16x16x32_bf16 v[104:107], v[52:55], a[168:171], v[104:107]\n\t"
"s_nop 0\n\t"
"ds_read_b128 a[148:151], v224 offset:2560\n\t"
"ds_read_b128 a[164:167], v224 offset:6656\n\t"
"ds_write_addtid_b32 v200 offset:20544\n\t"
"ds_write_addtid_b32 v201 offset:20800\n\t"
"ds_write_addtid_b32 v211 offset:26976\n\t"
"ds_read_b64_tr_b16 a[178:179], v248 offset:41984\n\t"
"v_mov_b32 v169, 0\n\t"
"v_cndmask_b32 v166, 0, v166, vcc\n\t"
"v_mfma_f32_16x16x32_bf16 v[220:223], v[148:151], a[204:207], 0\n\t"
"ds_read_b64_tr_b16 a[180:181], v249 offset:41472\n\t"
"v_cndmask_b32 v168, 0, v168, vcc\n\t"
"v_lshlrev_b32 v158, 4, v170\n\t"
"v_mfma_f32_16x16x32_bf16 v[120:123], v[56:59], a[152:155], 0\n\t"
"ds_read_b64_tr_b16 a[182:183], v249 offset:41984\n\t"
"v_lshlrev_b64 v[166:167], v158, v[166:167]\n\t"
"v_mfma_f32_16x16x32_bf16 v[120:123], v[60:63], a[168:171], v[120:123]\n\t"
"ds_write_addtid_b32 v209 offset:18752\n\t"
"v_lshlrev_b64 v[168:169], v158, v[168:169]\n\t"
"v_accvgpr_write_b32 a212, v166\n\t"
"v_mfma_f32_16x16x32_bf16 v[136:139], v[64:67], a[152:155], 0\n\t"
"ds_read_b64_tr_b16 a[184:185], v250 offset:41472\n\t"
"v_accvgpr_write_b32 a213, v167\n\t"
"v_mfma_f32_16x16x32_bf16 v[136:139], v[68:71], a[168:171], v[136:139]\n\t"
"s_waitcnt lgkmcnt(15)\n\t"
"ds_read_b64_tr_b16 a[186:187], v250 offset:41984\n\t"
"v_accvgpr_write_b32 a214, v168\n\t"
"v_accvgpr_write_b32 a215, v169\n\t"
"v_lshlrev_b32 v158, 15, v176\n\t"
"v_and_b32 v158, 0x8000, v158\n\t"
"v_cvt_pk_bf16_f32 v217, v217, v221\n\t"
"v_mfma_f32_16x16x32_bf16 v[140:143], v[64:67], a[156:159], 0\n\t"
"v_or_b32 v167, 0x3f803f80, v158\n\t"
"v_cvt_pk_bf16_f32 v219, v219, v223\n\t"
"v_mfma_f32_16x16x32_bf16 v[140:143], v[68:71], a[172:175], v[140:143]\n\t"
"ds_read_b64_tr_b16 a[188:189], v251 offset:41472\n\t"
"v_and_b32 v158, 2, v176\n\t"
"v_mul_u32_u24 v158, 0x4000, v158\n\t"
"v_mfma_f32_16x16x32_bf16 v[124:127], v[56:59], a[156:159], 0\n\t"
"ds_read_b64_tr_b16 a[190:191], v251 offset:41984\n\t"
"v_lshl_or_b32 v158, v158, 16, v158\n\t"
"v_mfma_f32_16x16x32_bf16 v[124:127], v[60:63], a[172:175], v[124:127]\n\t"
"s_nop 0\n\t"
"ds_read_b128 a[152:155], v224 offset:3072\n\t"
"ds_read_b128 a[168:171], v224 offset:7168\n\t"
"ds_read_b64_tr_b16 a[192:193], v248 offset:42624\n\t"
"ds_read_b64_tr_b16 a[194:195], v248 offset:43136\n\t"
"ds_read_b64_tr_b16 a[196:197], v249 offset:42624\n\t"
"v_xor_b32 v166, v167, v158\n\t"
"v_accvgpr_write_b32 a210, v166\n\t"
"v_accvgpr_write_b32 a211, v167\n\t"
"s_add_u32 s61, s26, 21888\n\t"
"v_mfma_f32_16x16x32_bf16 v[108:111], v[48:51], a[156:159], 0\n\t"
"ds_read_b64_tr_b16 a[198:199], v249 offset:43136\n\t"
"v_lshrrev_b32 v155, 4, v176\n\t"
"v_mfma_f32_16x16x32_bf16 v[108:111], v[52:55], a[172:175], v[108:111]\n\t"
"s_waitcnt lgkmcnt(15)\n\t"
"ds_read_b64_tr_b16 a[200:201], v250 offset:42624\n\t"
"buffer_load_dword v191, v189, s[8:11], 0 offen sc1\n\t"
"v_and_b32 v158, 1, v156\n\t"
"v_mfma_f32_16x16x32_bf16 v[0:3], v[72:75], a[144:147], v[96:99]\n\t"
"ds_write_addtid_b32 v217 offset:22848\n\t"
"v_cvt_pk_bf16_f32 v216, v216, v220\n\t"
"v_mfma_f32_16x16x32_bf16 v[0:3], v[76:79], a[160:163], v[0:3]\n\t"
"ds_write_addtid_b32 v219 offset:31072\n\t"
"v_lshrrev_b32 v159, 1, v156\n\t"
"v_cvt_pk_bf16_f32 v218, v218, v222\n\t"
"v_mfma_f32_16x16x32_bf16 v[16:19], v[80:83], a[144:147], v[112:115]\n\t"
"ds_write_addtid_b32 v218 offset:30816\n\t"
"v_mul_u32_u24 v166, 1152, v159\n\t"
"v_mfma_f32_16x16x32_bf16 v[16:19], v[84:87], a[160:163], v[16:19]\n\t"
"ds_write_addtid_b32 v216 offset:22592\n\t"
"v_mul_u32_u24 v167, 20736, v158\n\t"
"v_add3_u32 v166, v166, v157, s61\n\t"
"v_mfma_f32_16x16x32_bf16 v[32:35], v[88:91], a[144:147], v[128:131]\n\t"
"ds_read_b64_tr_b16 a[202:203], v250 offset:43136\n\t"
"v_sub_u32 v166, v166, v167\n\t"
"v_mfma_f32_16x16x32_bf16 v[32:35], v[92:95], a[160:163], v[32:35]\n\t"
"ds_read_b64_tr_b16 a[204:205], v251 offset:42624\n\t"
"v_xor_b32 v159, v159, v158\n\t"
"v_lshl_add_u32 v166, v159, 6, v166\n\t"
"v_xad_u32 v167, v158, 0, v155\n\t"
"v_lshl_add_u32 v168, v167, 7, v166\n\t"
"v_mfma_f32_16x16x32_bf16 v[36:39], v[88:91], a[148:151], v[132:135]\n\t"
"ds_read_b128 a[156:159], v224 offset:3584\n\t"
"v_and_b32 v167, 2, v167\n\t"
"v_mfma_f32_16x16x32_bf16 v[36:39], v[92:95], a[164:167], v[36:39]\n\t"
"ds_read_b64_tr_b16 a[206:207], v251 offset:43136\n\t"
"v_lshl_add_u32 v168, v167, 4, v168\n\t"
"v_xor_b32 v168, 64, v168\n\t"
"v_mfma_f32_16x16x32_bf16 v[20:23], v[80:83], a[148:151], v[116:119]\n\t"
"ds_read_b128 a[172:175], v224 offset:7680\n\t"
"v_accvgpr_write_b32 a208, v168\n\t"
"v_mfma_f32_16x16x32_bf16 v[20:23], v[84:87], a[164:167], v[20:23]\n\t"
"v_xad_u32 v167, v158, 1, v155\n\t"
"v_lshl_add_u32 v168, v167, 7, v166\n\t"
"v_and_b32 v167, 2, v167\n\t"
"v_mfma_f32_16x16x32_bf16 v[4:7], v[72:75], a[148:151], v[100:103]\n\t"
"v_lshl_add_u32 v168, v167, 4, v168\n\t"
"v_accvgpr_write_b32 a209, v168\n\t"
"v_mfma_f32_16x16x32_bf16 v[4:7], v[76:79], a[164:167], v[4:7]\n\t"
"s_waitcnt lgkmcnt(0)\n\t"
"s_waitcnt vmcnt(1)\n\t"
"v_cmp_eq_u32 vcc, v190, v188\n\t"
"s_nop 4\n\t"
"s_cmp_eq_u64 vcc, 0\n\t"
"s_cbranch_scc1 Lgo_%=\n\t"
"s_sub_u32 s68, s68, 1\n\t"
"s_waitcnt vmcnt(0)\n\t"
"Lchk_%=:\n\t"
"v_cmp_eq_u32 vcc, v191, v188\n\t"
"s_nop 4\n\t"
"s_cmp_eq_u64 vcc, 0\n\t"
"s_cbranch_scc1 Lgo_%=\n\t"
"s_sub_u32 s68, s68, 1\n\t"
"s_cmp_eq_u32 s68, 0\n\t"
"s_cbranch_scc1 Lfb_%=\n\t"
"buffer_load_dword v191, v189, s[8:11], 0 offen sc1\n\t"
"s_waitcnt vmcnt(0)\n\t"
"s_branch Lchk_%=\n\t"
"Lgo_%=:\n\t"
"s_barrier\n\t"
"v_mfma_f32_16x16x32_bf16 v[8:11], v[72:75], a[152:155], v[104:107]\n\t"
"ds_read_b128 a[160:163], v224 offset:20544\n\t"
"v_add_u32 v248, 32896, v227\n\t"
"v_mfma_f32_16x16x32_bf16 v[8:11], v[76:79], a[168:171], v[8:11]\n\t"
"ds_read_b128 a[144:147], v224 offset:16448\n\t"
"s_mov_b32 s54, 98304\n\t"
"buffer_load_dwordx4 a[96:99], v235, s[8:11], s54 offen offset:0 sc1\n\t"
"buffer_load_dwordx4 a[100:103], v235, s[8:11], s54 offen offset:1024 sc1\n\t"
"v_mfma_f32_16x16x32_bf16 v[24:27], v[80:83], a[152:155], v[120:123]\n\t"
"ds_read_b128 a[148:151], v224 offset:16960\n\t"
"v_mbcnt_lo_u32_b32 v176, -1, 0\n\t"
"v_mfma_f32_16x16x32_bf16 v[24:27], v[84:87], a[168:171], v[24:27]\n\t"
"ds_read_b128 a[164:167], v224 offset:21056\n\t"
"buffer_load_dwordx4 a[104:107], v235, s[8:11], s54 offen offset:2048 sc1\n\t"
"buffer_load_dwordx4 a[108:111], v235, s[8:11], s54 offen offset:3072 sc1\n\t"
"v_mfma_f32_16x16x32_bf16 v[40:43], v[88:91], a[152:155], v[136:139]\n\t"
"v_mbcnt_hi_u32_b32 v176, -1, v176\n\t"
"v_and_b32 v154, 15, v176\n\t"
"v_mfma_f32_16x16x32_bf16 v[40:43], v[92:95], a[168:171], v[40:43]\n\t"
"buffer_load_dwordx4 a[112:115], v252, s[8:11], s54 offen offset:0 sc1\n\t"
"buffer_load_dwordx4 a[116:119], v252, s[8:11], s54 offen offset:1024 sc1\n\t"
"v_lshrrev_b32 v155, 4, v176\n\t"
"v_bfe_u32 v156, v176, 2, 2\n\t"
"v_mfma_f32_16x16x32_bf16 v[192:195], a[216:219], a[176:179], 0\n\t"
"v_and_b32 v157, 3, v176\n\t"
"v_lshlrev_b32 v157, 3, v157\n\t"
"v_xor_b32 v249, 32, v248\n\t"
"s_add_u32 s60, s43, 32896\n\t"
"v_mfma_f32_16x16x32_bf16 v[44:47], v[88:91], a[156:159], v[140:143]\n\t"
"s_add_u32 s61, s26, 20800\n\t"
"v_xor_b32 v250, 64, v248\n\t"
"v_xor_b32 v251, 96, v248\n\t"
"v_mfma_f32_16x16x32_bf16 v[44:47], v[92:95], a[172:175], v[44:47]\n\t"
"s_mov_b32 s66, 172032\n\t"
"buffer_load_dwordx4 a[120:123], v235, s[8:11], s66 offen offset:0 sc1\n\t"
"buffer_load_dwordx4 a[124:127], v235, s[8:11], s66 offen offset:1024 sc1\n\t"
"buffer_load_dwordx4 a[128:131], v235, s[8:11], s66 offen offset:2048 sc1\n\t"
"v_mfma_f32_16x16x32_bf16 v[196:199], a[216:219], a[180:183], 0\n\t"
"s_mul_i32 s62, s73, 96\n\t"
"ds_read_b128 a[152:155], v224 offset:17472\n\t"
"ds_read_b128 a[168:171], v224 offset:21568\n\t"
"v_mfma_f32_16x16x32_bf16 v[28:31], v[80:83], a[156:159], v[124:127]\n\t"
"s_add_u32 s62, s62, 54400\n\t"
"v_min_u32 v158, 35, v176\n\t"
"v_lshl_add_u32 v254, v158, 4, s60\n\t"
"v_mfma_f32_16x16x32_bf16 v[28:31], v[84:87], a[172:175], v[28:31]\n\t"
"buffer_load_dwordx4 a[132:135], v235, s[8:11], s66 offen offset:3072 sc1\n\t"
"buffer_load_dwordx4 a[136:139], v252, s[8:11], s66 offen offset:0 sc1\n\t"
"buffer_load_dwordx4 a[140:143], v252, s[8:11], s66 offen offset:1024 sc1\n\t"
"v_add_u32 v255, 66432, v254\n\t"
"v_and_b32 v158, 1, v155\n\t"
"v_mfma_f32_16x16x32_bf16 v[12:15], v[72:75], a[156:159], v[108:111]\n\t"
"v_add_u32 v159, v156, v158\n\t"
"v_lshl_add_u32 v166, v159, 7, v157\n\t"
"v_mfma_f32_16x16x32_bf16 v[12:15], v[76:79], a[172:175], v[12:15]\n\t"
"v_and_b32 v159, 2, v159\n\t"
"v_cvt_pk_bf16_f32 v195, v195, v199\n\t"
"v_mfma_f32_16x16x32_bf16 v[200:203], a[216:219], a[184:187], 0\n\t"
"s_waitcnt lgkmcnt(4)\n\t"
"s_mov_b32 s69, 196608\n\t"
"buffer_load_dwordx4 a[0:3], v235, s[8:11], s69 offen offset:0 sc1\n\t"
"v_pk_fma_f32 v[96:97], v[96:97], s[38:39], v[0:1] neg_lo:[1,0,1] neg_hi:[1,0,1]\n\t"
"v_pk_fma_f32 v[98:99], v[98:99], s[38:39], v[2:3] neg_lo:[1,0,1] neg_hi:[1,0,1]\n\t"
"v_pk_fma_f32 v[112:113], v[112:113], s[38:39], v[16:17] neg_lo:[1,0,1] neg_hi:[1,0,1]\n\t"
"v_pk_fma_f32 v[114:115], v[114:115], s[38:39], v[18:19] neg_lo:[1,0,1] neg_hi:[1,0,1]\n\t"
"v_pk_fma_f32 v[128:129], v[128:129], s[38:39], v[32:33] neg_lo:[1,0,1] neg_hi:[1,0,1]\n\t"
"v_pk_fma_f32 v[130:131], v[130:131], s[38:39], v[34:35] neg_lo:[1,0,1] neg_hi:[1,0,1]\n\t"
"v_pk_fma_f32 v[132:133], v[132:133], s[38:39], v[36:37] neg_lo:[1,0,1] neg_hi:[1,0,1]\n\t"
"v_pk_fma_f32 v[134:135], v[134:135], s[38:39], v[38:39] neg_lo:[1,0,1] neg_hi:[1,0,1]\n\t"
"v_pk_fma_f32 v[116:117], v[116:117], s[38:39], v[20:21] neg_lo:[1,0,1] neg_hi:[1,0,1]\n\t"
"v_pk_fma_f32 v[118:119], v[118:119], s[38:39], v[22:23] neg_lo:[1,0,1] neg_hi:[1,0,1]\n\t"
"v_pk_fma_f32 v[100:101], v[100:101], s[38:39], v[4:5] neg_lo:[1,0,1] neg_hi:[1,0,1]\n\t"
"v_pk_fma_f32 v[102:103], v[102:103], s[38:39], v[6:7] neg_lo:[1,0,1] neg_hi:[1,0,1]\n\t"
"v_pk_fma_f32 v[104:105], v[104:105], s[38:39], v[8:9] neg_lo:[1,0,1] neg_hi:[1,0,1]\n\t"
"v_pk_fma_f32 v[106:107], v[106:107], s[38:39], v[10:11] neg_lo:[1,0,1] neg_hi:[1,0,1]\n\t"
"v_pk_fma_f32 v[120:121], v[120:121], s[38:39], v[24:25] neg_lo:[1,0,1] neg_hi:[1,0,1]\n\t"
"v_pk_fma_f32 v[122:123], v[122:123], s[38:39], v[26:27] neg_lo:[1,0,1] neg_hi:[1,0,1]\n\t"
"v_pk_fma_f32 v[136:137], v[136:137], s[38:39], v[40:41] neg_lo:[1,0,1] neg_hi:[1,0,1]\n\t"
"v_pk_fma_f32 v[138:139], v[138:139], s[38:39], v[42:43] neg_lo:[1,0,1] neg_hi:[1,0,1]\n\t"
"v_pk_fma_f32 v[140:141], v[140:141], s[38:39], v[44:45] neg_lo:[1,0,1] neg_hi:[1,0,1]\n\t"
"v_pk_fma_f32 v[142:143], v[142:143], s[38:39], v[46:47] neg_lo:[1,0,1] neg_hi:[1,0,1]\n\t"
"v_pk_fma_f32 v[124:125], v[124:125], s[38:39], v[28:29] neg_lo:[1,0,1] neg_hi:[1,0,1]\n\t"
"v_pk_fma_f32 v[126:127], v[126:127], s[38:39], v[30:31] neg_lo:[1,0,1] neg_hi:[1,0,1]\n\t"
"v_pk_fma_f32 v[108:109], v[108:109], s[38:39], v[12:13] neg_lo:[1,0,1] neg_hi:[1,0,1]\n\t"
"v_pk_fma_f32 v[110:111], v[110:111], s[38:39], v[14:15] neg_lo:[1,0,1] neg_hi:[1,0,1]\n\t"
"v_mfma_f32_16x16x32_bf16 v[0:3], a[48:51], a[144:147], v[0:3]\n\t"
"v_lshl_add_u32 v166, v159, 4, v166\n\t"
"v_cvt_pk_bf16_f32 v192, v192, v196\n\t"
"v_mfma_f32_16x16x32_bf16 v[0:3], a[52:55], a[160:163], v[0:3]\n\t"
"buffer_load_dwordx4 a[4:7], v235, s[8:11], s69 offen offset:1024 sc1\n\t"
"v_mfma_f32_16x16x32_bf16 v[204:207], a[216:219], a[188:191], 0\n\t"
"ds_write_addtid_b32 v192 offset:0\n\t"
"v_xad_u32 v167, v158, 1, v156\n\t"
"v_lshl_add_u32 v168, v167, 7, v157\n\t"
"v_and_b32 v167, 2, v167\n\t"
"v_lshl_add_u32 v168, v167, 4, v168\n\t"
"v_mfma_f32_16x16x32_bf16 v[16:19], a[56:59], a[144:147], v[16:19]\n\t"
"ds_write_addtid_b32 v195 offset:8480\n\t"
"v_sub_u32 v168, v168, v166\n\t"
"v_mfma_f32_16x16x32_bf16 v[16:19], a[60:63], a[160:163], v[16:19]\n\t"
"buffer_load_dwordx4 a[8:11], v235, s[8:11], s69 offen offset:2048 sc1\n\t"
"v_mul_u32_u24 v159, 20800, v158\n\t"
"v_sub_u32 v166, v166, v159\n\t"
"v_cvt_pk_bf16_f32 v193, v193, v197\n\t"
"v_mfma_f32_16x16x32_bf16 v[32:35], a[64:67], a[144:147], v[32:35]\n\t"
"ds_read_b128 a[172:175], v224 offset:22080\n\t"
"v_lshl_add_u32 v168, v158, 7, v168\n\t"
"v_mfma_f32_16x16x32_bf16 v[32:35], a[68:71], a[160:163], v[32:35]\n\t"
"ds_read_b128 a[156:159], v224 offset:17984\n\t"
"v_subrev_u32 v253, 64, v168\n\t"
"v_cvt_pk_bf16_f32 v203, v203, v207\n\t"
"v_mfma_f32_16x16x32_bf16 v[208:211], a[216:219], a[192:195], 0\n\t"
"s_waitcnt lgkmcnt(6)\n\t"
"ds_read_b128 a[144:147], v224 offset:18496\n\t"
"v_and_b32 v159, 32, v176\n\t"
"v_cvt_pk_bf16_f32 v194, v194, v198\n\t"
"v_mfma_f32_16x16x32_bf16 v[36:39], a[64:67], a[148:151], v[36:39]\n\t"
"ds_write_addtid_b32 v193 offset:256\n\t"
"v_lshlrev_b32 v159, 4, v159\n\t"
"v_mfma_f32_16x16x32_bf16 v[36:39], a[68:71], a[164:167], v[36:39]\n\t"
"buffer_load_dwordx4 a[12:15], v235, s[8:11], s69 offen offset:3072 sc1\n\t"
"v_add3_u32 v225, v166, v159, s61\n\t"
"v_mfma_f32_16x16x32_bf16 v[20:23], a[56:59], a[148:151], v[20:23]\n\t"
"v_mul_u32_u24 v158, 400, v154\n\t"
"v_cvt_pk_bf16_f32 v202, v202, v206\n\t"
"v_mfma_f32_16x16x32_bf16 v[20:23], a[60:63], a[164:167], v[20:23]\n\t"
"ds_write_addtid_b32 v202 offset:12320\n\t"
"buffer_load_dwordx4 a[16:19], v252, s[8:11], s69 offen offset:0 sc1\n\t"
"v_mul_u32_u24 v159, 24, v155\n\t"
"v_cvt_pk_bf16_f32 v200, v200, v204\n\t"
"v_mfma_f32_16x16x32_bf16 v[212:215], a[216:219], a[196:199], 0\n\t"
"v_add3_u32 v228, v158, v159, s62\n\t"
"s_barrier\n\t"
"v_mfma_f32_16x16x32_bf16 v[4:7], a[48:51], a[148:151], v[4:7]\n\t"
"s_mul_i32 s60, s73, 3200\n\t"
"ds_write_addtid_b32 v194 offset:8224\n\t"
"ds_read_b64_tr_b16 a[176:177], v248 offset:0\n\t"
"v_mfma_f32_16x16x32_bf16 v[4:7], a[52:55], a[164:167], v[4:7]\n\t"
"s_add_u32 s60, s60, 54400\n\t"
"ds_read_b128 a[160:163], v224 offset:22592\n\t"
"ds_read_b128 a[164:167], v224 offset:23104\n\t"
"ds_write_addtid_b32 v200 offset:4096\n\t"
"ds_read_b64_tr_b16 a[178:179], v248 offset:512\n\t"
"ds_read_b64_tr_b16 a[180:181], v249 offset:0\n\t"
"ds_read_b64_tr_b16 a[182:183], v249 offset:512\n\t"
"v_cvt_pk_bf16_f32 v201, v201, v205\n\t"
"v_mfma_f32_16x16x32_bf16 v[216:219], a[216:219], a[200:203], 0\n\t"
"s_waitcnt lgkmcnt(15)\n\t"
"buffer_load_dwordx4 a[20:23], v252, s[8:11], s69 offen offset:1024 sc1\n\t"
"v_mfma_f32_16x16x32_bf16 v[8:11], a[48:51], a[152:155], v[8:11]\n\t"
"s_and_b32 s61, s73, 2\n\t"
"ds_read_b64_tr_b16 a[184:185], v250 offset:0\n\t"
"v_cvt_pk_bf16_f32 v210, v210, v214\n\t"
"v_mfma_f32_16x16x32_bf16 v[8:11], a[52:55], a[168:171], v[8:11]\n\t"
"s_lshl_b32 s61, s61, 7\n\t"
"ds_write_addtid_b32 v201 offset:4352\n\t"
"ds_write_addtid_b32 v203 offset:12576\n\t"
"ds_read_b64_tr_b16 a[186:187], v250 offset:512\n\t"
"ds_read_b64_tr_b16 a[188:189], v251 offset:0\n\t"
"ds_read_b64_tr_b16 a[190:191], v251 offset:512\n\t"
"v_cvt_pk_bf16_f32 v208, v208, v212\n\t"
"v_mfma_f32_16x16x32_bf16 v[24:27], a[56:59], a[152:155], v[24:27]\n\t"
"ds_read_b64_tr_b16 a[192:193], v248 offset:1152\n\t"
"v_cvt_pk_bf16_f32 v211, v211, v215\n\t"
"v_mfma_f32_16x16x32_bf16 v[24:27], a[60:63], a[168:171], v[24:27]\n\t"
"ds_read_b64_tr_b16 a[194:195], v248 offset:1664\n\t"
"s_mov_b32 s81, 270336\n\t"
"buffer_load_dwordx4 a[24:27], v235, s[8:11], s81 offen offset:0 sc1\n\t"
"v_cvt_pk_bf16_f32 v209, v209, v213\n\t"
"v_mfma_f32_16x16x32_bf16 v[220:223], a[216:219], a[204:207], 0\n\t"
"s_add_u32 s60, s60, s61\n\t"
"ds_read_b128 a[148:151], v224 offset:19008\n\t"
"ds_write_addtid_b32 v210 offset:10272\n\t"
"ds_read_b64_tr_b16 a[196:197], v249 offset:1152\n\t"
"v_mfma_f32_16x16x32_bf16 v[40:43], a[64:67], a[152:155], v[40:43]\n\t"
"s_mul_i32 s61, s73, 393216\n\t"
"ds_write_addtid_b32 v208 offset:2048\n\t"
"ds_write_addtid_b32 v211 offset:10528\n\t"
"v_mfma_f32_16x16x32_bf16 v[40:43], a[68:71], a[168:171], v[40:43]\n\t"
"s_waitcnt lgkmcnt(15)\n\t"
"ds_read_b64_tr_b16 a[198:199], v249 offset:1664\n\t"
"buffer_load_dwordx4 a[28:31], v235, s[8:11], s81 offen offset:1024 sc1\n\t"
"v_mfma_f32_16x16x32_bf16 v[44:47], a[64:67], a[156:159], v[44:47]\n\t"
"ds_read_b64_tr_b16 a[200:201], v250 offset:1152\n\t"
"v_add_u32 v158, 0, v176\n\t"
"v_mfma_f32_16x16x32_bf16 v[44:47], a[68:71], a[172:175], v[44:47]\n\t"
"ds_write_addtid_b32 v209 offset:2304\n\t"
"v_mul_u32_u24 v159, 0xab, v158\n\t"
"v_mfma_f32_16x16x32_bf16 v[28:31], a[56:59], a[156:159], v[28:31]\n\t"
"ds_read_b64_tr_b16 a[202:203], v250 offset:1664\n\t"
"v_cvt_pk_bf16_f32 v219, v219, v223\n\t"
"v_mfma_f32_16x16x32_bf16 v[28:31], a[60:63], a[172:175], v[28:31]\n\t"
"ds_read_b128 a[152:155], v224 offset:19520\n\t"
"buffer_load_dwordx4 a[32:35], v235, s[8:11], s81 offen offset:2048 sc1\n\t"
"v_lshrrev_b32 v159, 12, v159\n\t"
"v_cvt_pk_bf16_f32 v216, v216, v220\n\t"
"v_mfma_f32_16x16x32_bf16 v[12:15], a[48:51], a[156:159], v[12:15]\n\t"
"v_add_lshl_u32 v166, v158, v159, 4\n\t"
"v_add_u32 v229, s60, v166\n\t"
"v_mfma_f32_16x16x32_bf16 v[12:15], a[52:55], a[172:175], v[12:15]\n\t"
"ds_read_b128 a[156:159], v224 offset:20032\n\t"
"v_mad_u32_u24 v158, v159, 24, v158\n\t"
"v_cvt_pk_bf16_f32 v218, v218, v222\n\t"
"v_mfma_f32_16x16x32_bf16 v[96:99], a[72:75], a[144:147], v[96:99]\n\t"
"ds_read_b64_tr_b16 a[204:205], v251 offset:1152\n\t"
"v_lshl_add_u32 v232, v158, 4, s61\n\t"
"v_mfma_f32_16x16x32_bf16 v[96:99], a[76:79], a[160:163], v[96:99]\n\t"
"ds_write_addtid_b32 v219 offset:14624\n\t"
"buffer_load_dwordx4 a[36:39], v235, s[8:11], s81 offen offset:3072 sc1\n\t"
"v_add_u32 v158, 64, v176\n\t"
"v_cvt_pk_bf16_f32 v217, v217, v221\n\t"
"v_mfma_f32_16x16x32_bf16 v[112:115], a[80:83], a[144:147], v[112:115]\n\t"
"ds_write_addtid_b32 v216 offset:6144\n\t"
"v_mul_u32_u24 v159, 0xab, v158\n\t"
"v_mfma_f32_16x16x32_bf16 v[112:115], a[84:87], a[160:163], v[112:115]\n\t"
"ds_read_b64_tr_b16 a[206:207], v251 offset:1664\n\t"
"buffer_load_dwordx4 a[40:43], v252, s[8:11], s81 offen offset:0 sc1\n\t"
"v_lshrrev_b32 v159, 12, v159\n\t"
"v_mfma_f32_16x16x32_bf16 v[128:131], a[88:91], a[144:147], v[128:131]\n\t"
"ds_write_addtid_b32 v217 offset:6400\n\t"
"v_add_lshl_u32 v166, v158, v159, 4\n\t"
"v_mfma_f32_16x16x32_bf16 v[128:131], a[92:95], a[160:163], v[128:131]\n\t"
"s_waitcnt lgkmcnt(15)\n\t"
"ds_write_addtid_b32 v218 offset:14368\n\t"
"v_add_u32 v230, s60, v166\n\t"
"v_mad_u32_u24 v158, v159, 24, v158\n\t"
"v_mfma_f32_16x16x32_bf16 v[132:135], a[88:91], a[148:151], v[132:135]\n\t"
"v_lshl_add_u32 v233, v158, 4, s61\n\t"
"v_add_u32 v158, 128, v176\n\t"
"v_mfma_f32_16x16x32_bf16 v[132:135], a[92:95], a[164:167], v[132:135]\n\t"
"ds_read_b128 a[172:175], v224 offset:24128\n\t"
"buffer_load_dwordx4 a[44:47], v252, s[8:11], s81 offen offset:1024 sc1\n\t"
"v_mfma_f32_16x16x32_bf16 v[116:119], a[80:83], a[148:151], v[116:119]\n\t"
"ds_read_b128 a[168:171], v224 offset:23616\n\t"
"v_mul_u32_u24 v159, 0xab, v158\n\t"
"v_mfma_f32_16x16x32_bf16 v[116:119], a[84:87], a[164:167], v[116:119]\n\t"
"v_lshrrev_b32 v159, 12, v159\n\t"
"v_add_lshl_u32 v166, v158, v159, 4\n\t"
"v_add_u32 v231, s60, v166\n\t"
"v_mfma_f32_16x16x32_bf16 v[100:103], a[72:75], a[148:151], v[100:103]\n\t"
"v_mad_u32_u24 v158, v159, 24, v158\n\t"
"v_lshl_add_u32 v234, v158, 4, s61\n\t"
"v_mfma_f32_16x16x32_bf16 v[100:103], a[76:79], a[164:167], v[100:103]\n\t"
"s_mov_b32 s57, 122880\n\t"
"buffer_load_dwordx4 a[48:51], v235, s[8:11], s57 offen offset:0 sc1\n\t"
"s_waitcnt lgkmcnt(0)\n\t"
"s_add_u32 s71, s71, 1\n\t"
"v_mov_b32 v215, s71\n\t"
"s_mov_b64 exec, 1\n\t"
"buffer_store_dword v215, off, s[8:11], s70 sc1\n\t"
"s_mov_b64 exec, -1\n\t"
"s_barrier\n\t"
"v_mfma_f32_16x16x32_bf16 v[104:107], a[72:75], a[152:155], v[104:107]\n\t"
"ds_read_b128 a[148:151], v224 offset:512\n\t"
"v_add_u32 v248, 122368, v226\n\t"
"v_mfma_f32_16x16x32_bf16 v[104:107], a[76:79], a[168:171], v[104:107]\n\t"
"ds_read_b128 a[144:147], v224 offset:0\n\t"
"v_mfma_f32_16x16x32_bf16 v[120:123], a[80:83], a[152:155], v[120:123]\n\t"
"ds_read_b128 a[160:163], v224 offset:4096\n\t"
"v_accvgpr_read_b32 v172, a220\n\t"
"v_mfma_f32_16x16x32_bf16 v[120:123], a[84:87], a[168:171], v[120:123]\n\t"
"ds_read_b128 a[164:167], v224 offset:4608\n\t"
"v_xor_b32 v249, 32, v248\n\t"
"v_mfma_f32_16x16x32_bf16 v[136:139], a[88:91], a[152:155], v[136:139]\n\t"
"ds_read_b128 a[152:155], v224 offset:1024\n\t"
"v_and_b32 v172, 0xff7fff7f, v172\n\t"
"v_mfma_f32_16x16x32_bf16 v[136:139], a[92:95], a[168:171], v[136:139]\n\t"
"buffer_load_dwordx4 a[52:55], v235, s[8:11], s57 offen offset:1024 sc1\n\t"
"v_mfma_f32_16x16x32_bf16 v[192:195], a[216:219], a[176:179], 0\n\t"
"v_accvgpr_write_b32 a252, v172\n\t"
"v_xor_b32 v250, 64, v248\n\t"
"v_mfma_f32_16x16x32_bf16 v[140:143], a[88:91], a[156:159], v[140:143]\n\t"
"v_accvgpr_read_b32 v173, a221\n\t"
"v_xor_b32 v251, 96, v248\n\t"
"v_mfma_f32_16x16x32_bf16 v[140:143], a[92:95], a[172:175], v[140:143]\n\t"
"buffer_load_dwordx4 a[56:59], v235, s[8:11], s57 offen offset:2048 sc1\n\t"
"v_mfma_f32_16x16x32_bf16 v[196:199], a[216:219], a[180:183], 0\n\t"
"ds_read_b128 a[168:171], v224 offset:5120\n\t"
"v_and_b32 v173, 0xff7fff7f, v173\n\t"
"v_mfma_f32_16x16x32_bf16 v[124:127], a[80:83], a[156:159], v[124:127]\n\t"
"v_accvgpr_write_b32 a253, v173\n\t"
"v_mfma_f32_16x16x32_bf16 v[124:127], a[84:87], a[172:175], v[124:127]\n\t"
"buffer_load_dwordx4 a[60:63], v235, s[8:11], s57 offen offset:3072 sc1\n\t"
"v_mfma_f32_16x16x32_bf16 v[108:111], a[72:75], a[156:159], v[108:111]\n\t"
"ds_read_b64_tr_b16 a[176:177], v248 offset:0\n\t"
"v_mfma_f32_16x16x32_bf16 v[108:111], a[76:79], a[172:175], v[108:111]\n\t"
"ds_read_b64_tr_b16 a[178:179], v248 offset:512\n\t"
"v_accvgpr_read_b32 v174, a222\n\t"
"v_mfma_f32_16x16x32_bf16 v[200:203], a[216:219], a[184:187], 0\n\t"
"s_waitcnt vmcnt(27) lgkmcnt(5)\n\t"
"buffer_load_dwordx4 a[64:67], v252, s[8:11], s57 offen offset:0 sc1\n\t"
"v_mfma_f32_16x16x32_bf16 v[0:3], a[96:99], a[144:147], v[0:3]\n\t"
"ds_read_b128 a[156:159], v224 offset:1536\n\t"
"v_cvt_pk_bf16_f32 v193, v193, v197\n\t"
"v_mfma_f32_16x16x32_bf16 v[0:3], a[100:103], a[160:163], v[0:3]\n\t"
"buffer_load_dwordx4 a[68:71], v252, s[8:11], s57 offen offset:1024 sc1\n\t"
"v_mfma_f32_16x16x32_bf16 v[204:207], a[216:219], a[188:191], 0\n\t"
"s_waitcnt vmcnt(27)\n\t"
"v_and_b32 v174, 0xff7fff7f, v174\n\t"
"v_accvgpr_write_b32 a254, v174\n\t"
"s_mov_b32 s42, 24576\n\t"
"v_mfma_f32_16x16x32_bf16 v[16:19], a[104:107], a[144:147], v[16:19]\n\t"
"ds_read_b128 a[172:175], v224 offset:5632\n\t"
"v_cvt_pk_bf16_f32 v194, v194, v198\n\t"
"v_mfma_f32_16x16x32_bf16 v[16:19], a[108:111], a[160:163], v[16:19]\n\t"
"s_waitcnt vmcnt(25)\n\t"
"ds_read_b64_tr_b16 a[180:181], v248 offset:1024\n\t"
"v_cvt_pk_bf16_f32 v195, v195, v199\n\t"
"v_mfma_f32_16x16x32_bf16 v[32:35], a[112:115], a[144:147], v[32:35]\n\t"
"ds_read_b64_tr_b16 a[184:185], v249 offset:0\n\t"
"v_cvt_pk_bf16_f32 v192, v192, v196\n\t"
"v_mfma_f32_16x16x32_bf16 v[32:35], a[116:119], a[160:163], v[32:35]\n\t"
"ds_read_b128 a[160:163], v224 offset:6144\n\t"
"v_accvgpr_read_b32 v175, a223\n\t"
"v_mfma_f32_16x16x32_bf16 v[208:211], a[216:219], a[192:195], 0\n\t"
"s_waitcnt lgkmcnt(9)\n\t"
"s_mov_b32 s63, 147456\n\t"
"buffer_load_dwordx4 a[72:75], v235, s[8:11], s63 offen offset:0 sc1\n\t"
"v_mfma_f32_16x16x32_bf16 v[36:39], a[112:115], a[148:151], v[36:39]\n\t"
"v_and_b32 v175, 0xff7fff7f, v175\n\t"
"v_cvt_pk_bf16_f32 v200, v200, v204\n\t"
"v_mfma_f32_16x16x32_bf16 v[36:39], a[116:119], a[164:167], v[36:39]\n\t"
"buffer_load_dwordx4 a[76:79], v235, s[8:11], s63 offen offset:1024 sc1\n\t"
"v_mfma_f32_16x16x32_bf16 v[20:23], a[104:107], a[148:151], v[20:23]\n\t"
"ds_write_addtid_b32 v192 offset:16448\n\t"
"v_cvt_pk_bf16_f32 v202, v202, v206\n\t"
"v_mfma_f32_16x16x32_bf16 v[20:23], a[108:111], a[164:167], v[20:23]\n\t"
"s_mov_b32 s40, 8192\n\t"
"ds_write_addtid_b32 v194 offset:24672\n\t"
"v_accvgpr_write_b32 a255, v175\n\t"
"v_mfma_f32_16x16x32_bf16 v[212:215], a[216:219], a[196:199], 0\n\t"
"s_mov_b32 s90, 0.5\n\t"
"ds_write_addtid_b32 v193 offset:16704\n\t"
"ds_read_b64_tr_b16 a[186:187], v249 offset:512\n\t"
"v_mfma_f32_16x16x32_bf16 v[4:7], a[96:99], a[148:151], v[4:7]\n\t"
"s_mov_b32 s91, 0.5\n\t"
"ds_read_b128 a[144:147], v224 offset:2048\n\t"
"ds_write_addtid_b32 v195 offset:24928\n\t"
"v_mfma_f32_16x16x32_bf16 v[4:7], a[100:103], a[164:167], v[4:7]\n\t"
"ds_write_addtid_b32 v202 offset:28768\n\t"
"v_cvt_pk_bf16_f32 v203, v203, v207\n\t"
"v_mfma_f32_16x16x32_bf16 v[216:219], a[216:219], a[200:203], 0\n\t"
"s_waitcnt lgkmcnt(14)\n\t"
"buffer_load_dwordx4 a[80:83], v235, s[8:11], s63 offen offset:2048 sc1\n\t"
"v_mfma_f32_16x16x32_bf16 v[8:11], a[96:99], a[152:155], v[8:11]\n\t"
"s_add_u32 s84, s8, 18432\n\t"
"s_addc_u32 s85, s9, 0\n\t"
"ds_read_b128 a[164:167], v224 offset:6656\n\t"
"v_mfma_f32_16x16x32_bf16 v[8:11], a[100:103], a[168:171], v[8:11]\n\t"
"buffer_load_dwordx4 a[84:87], v235, s[8:11], s63 offen offset:3072 sc1\n\t"
"v_mfma_f32_16x16x32_bf16 v[24:27], a[104:107], a[152:155], v[24:27]\n\t"
"s_mov_b64 s[86:87], s[10:11]\n\t"
"ds_read_b128 a[148:151], v224 offset:2560\n\t"
"ds_read_b64_tr_b16 a[188:189], v249 offset:1024\n\t"
"v_mfma_f32_16x16x32_bf16 v[24:27], a[108:111], a[168:171], v[24:27]\n\t"
"s_mov_b32 s46, 32768\n\t"
"ds_write_addtid_b32 v200 offset:20544\n\t"
"v_cvt_pk_bf16_f32 v201, v201, v205\n\t"
"v_mfma_f32_16x16x32_bf16 v[220:223], a[216:219], a[204:207], 0\n\t"
"v_cvt_pk_bf16_f32 v209, v209, v213\n\t"
"v_cvt_pk_bf16_f32 v211, v211, v215\n\t"
"v_mfma_f32_16x16x32_bf16 v[40:43], a[112:115], a[152:155], v[40:43]\n\t"
"s_xor_b64 s[88:89], s[84:85], s[8:9]\n\t"
"ds_write_addtid_b32 v201 offset:20800\n\t"
"ds_write_addtid_b32 v203 offset:29024\n\t"
"v_mfma_f32_16x16x32_bf16 v[40:43], a[116:119], a[168:171], v[40:43]\n\t"
"s_waitcnt lgkmcnt(15)\n\t"
"buffer_load_dwordx4 a[88:91], v252, s[8:11], s63 offen offset:0 sc1\n\t"
"v_mfma_f32_16x16x32_bf16 v[44:47], a[112:115], a[156:159], v[44:47]\n\t"
"ds_write_addtid_b32 v209 offset:18752\n\t"
"v_cvt_pk_bf16_f32 v208, v208, v212\n\t"
"v_mfma_f32_16x16x32_bf16 v[44:47], a[116:119], a[172:175], v[44:47]\n\t"
"buffer_load_dwordx4 a[92:95], v252, s[8:11], s63 offen offset:1024 sc1\n\t"
"v_mfma_f32_16x16x32_bf16 v[28:31], a[104:107], a[156:159], v[28:31]\n\t"
"ds_write_addtid_b32 v208 offset:18496\n\t"
"v_cvt_pk_bf16_f32 v218, v218, v222\n\t"
"v_mfma_f32_16x16x32_bf16 v[28:31], a[108:111], a[172:175], v[28:31]\n\t"
"s_add_u32 s25, s24, 0xffeb0000\n\t"
"ds_write_addtid_b32 v211 offset:26976\n\t"
"ds_read_b64_tr_b16 a[192:193], v250 offset:0\n\t"
"v_mfma_f32_16x16x32_bf16 v[12:15], a[96:99], a[156:159], v[12:15]\n\t"
"s_mov_b32 s41, 16384\n\t"
"ds_read_b64_tr_b16 a[194:195], v250 offset:512\n\t"
"v_cvt_pk_bf16_f32 v217, v217, v221\n\t"
"v_mfma_f32_16x16x32_bf16 v[12:15], a[100:103], a[172:175], v[12:15]\n\t"
"s_waitcnt vmcnt(29) lgkmcnt(13)\n\t"
"ds_read_b128 a[152:155], v224 offset:3072\n\t"
"ds_read_b64_tr_b16 a[196:197], v250 offset:1024\n\t"
"s_add_u32 s27, s24, 0xffe80000\n\t"
"v_mfma_f32_16x16x32_bf16 v[96:99], a[120:123], a[144:147], v[96:99]\n\t"
"ds_write_addtid_b32 v217 offset:22848\n\t"
"v_cvt_pk_bf16_f32 v210, v210, v214\n\t"
"s_add_u32 m0, s43, 142976\n\t"
"v_mfma_f32_16x16x32_bf16 v[96:99], a[124:127], a[160:163], v[96:99]\n\t"
"s_waitcnt vmcnt(27)\n\t"
"s_mov_b64 exec, s[44:45]\n\t"
"buffer_load_dwordx4 v247, s[12:15], 0 offen offset:128 nt lds\n\t"
"s_mov_b64 exec, -1\n\t"
"s_lshl_b32 m0, s73, 9\n\t"
"v_mfma_f32_16x16x32_bf16 v[112:115], a[128:131], a[144:147], v[112:115]\n\t"
"ds_write_addtid_b32 v210 offset:26720\n\t"
"v_cvt_pk_bf16_f32 v216, v216, v220\n\t"
"v_mfma_f32_16x16x32_bf16 v[112:115], a[132:135], a[160:163], v[112:115]\n\t"
"s_waitcnt vmcnt(26)\n\t"
"ds_read_b128 a[168:171], v224 offset:7168\n\t"
"v_cvt_pk_bf16_f32 v219, v219, v223\n\t"
"v_mfma_f32_16x16x32_bf16 v[128:131], a[136:139], a[144:147], v[128:131]\n\t"
"s_add_u32 s20, s24, 0xffd30000\n\t"
"ds_write_addtid_b32 v216 offset:22592\n\t"
"ds_read_b64_tr_b16 a[200:201], v251 offset:0\n\t"
"v_mfma_f32_16x16x32_bf16 v[128:131], a[140:143], a[160:163], v[128:131]\n\t"
"s_waitcnt lgkmcnt(15)\n\t"
"ds_write_addtid_b32 v218 offset:30816\n\t"
"ds_write_addtid_b32 v219 offset:31072\n\t"
"s_mov_b32 s47, 40960\n\t"
"v_mfma_f32_16x16x32_bf16 v[132:135], a[136:139], a[148:151], v[132:135]\n\t"
"s_add_u32 m0, s26, 145280\n\t"
"ds_read_b64_tr_b16 a[202:203], v251 offset:512\n\t"
"s_mov_b32 s48, 49152\n\t"
"s_mov_b32 s50, 65536\n\t"
"v_mfma_f32_16x16x32_bf16 v[132:135], a[140:143], a[164:167], v[132:135]\n\t"
"s_mov_b32 s49, 57344\n\t"
"ds_read_b64_tr_b16 a[204:205], v251 offset:1024\n\t"
"v_mfma_f32_16x16x32_bf16 v[116:119], a[128:131], a[148:151], v[116:119]\n\t"
"s_add_u32 s24, s24, 0xffd00000\n\t"
"ds_read_b128 a[156:159], v224 offset:3584\n\t"
"ds_read_b128 a[172:175], v224 offset:7680\n\t"
"v_mfma_f32_16x16x32_bf16 v[116:119], a[132:135], a[164:167], v[116:119]\n\t"
"buffer_load_dwordx4 v236, s[12:15], s22 offen offset:128 nt lds\n\t"
"v_mfma_f32_16x16x32_bf16 v[100:103], a[120:123], a[148:151], v[100:103]\n\t"
"s_lshl_b32 m0, s73, 9\n\t"
"v_mfma_f32_16x16x32_bf16 v[100:103], a[124:127], a[164:167], v[100:103]\n\t"
"s_waitcnt lgkmcnt(0)\n\t"
"s_barrier\n\t"
"v_mfma_f32_16x16x32_bf16 v[104:107], a[120:123], a[152:155], v[104:107]\n\t"
"s_add_u32 m0, s26, 145280\n\t"
"ds_read_b128 a[144:147], v224 offset:16448\n\t"
"v_add_u32 v248, 32896, v226\n\t"
"v_mfma_f32_16x16x32_bf16 v[104:107], a[124:127], a[168:171], v[104:107]\n\t"
"ds_read_b128 a[148:151], v224 offset:16960\n\t"
"v_xor_b32 v249, 32, v248\n\t"
"v_mfma_f32_16x16x32_bf16 v[120:123], a[128:131], a[152:155], v[120:123]\n\t"
"ds_read_b128 a[160:163], v224 offset:20544\n\t"
"v_mfma_f32_16x16x32_bf16 v[120:123], a[132:135], a[168:171], v[120:123]\n\t"
"ds_read_b128 a[164:167], v224 offset:21056\n\t"
"v_xor_b32 v250, 64, v248\n\t"
"v_mfma_f32_16x16x32_bf16 v[136:139], a[136:139], a[152:155], v[136:139]\n\t"
"v_mfma_f32_16x16x32_bf16 v[136:139], a[140:143], a[168:171], v[136:139]\n\t"
"v_xor_b32 v251, 96, v248\n\t"
"v_mfma_f32_16x16x32_bf16 v[192:195], a[252:255], a[176:179], 0\n\t"
"ds_read_b128 a[152:155], v224 offset:17472\n\t"
"v_mfma_f32_16x16x32_bf16 v[140:143], a[136:139], a[156:159], v[140:143]\n\t"
"v_mfma_f32_16x16x32_bf16 v[140:143], a[140:143], a[172:175], v[140:143]\n\t"
"ds_read_b128 a[168:171], v224 offset:21568\n\t"
"v_mfma_f32_16x16x32_bf16 v[196:199], a[252:255], a[184:187], 0\n\t"
"v_mfma_f32_16x16x32_bf16 v[124:127], a[128:131], a[156:159], v[124:127]\n\t"
"v_mfma_f32_16x16x32_bf16 v[124:127], a[132:135], a[172:175], v[124:127]\n\t"
"buffer_load_dwordx4 v237, s[12:15], s22 offen offset:1152 nt lds\n\t"
"v_mfma_f32_16x16x32_bf16 v[108:111], a[120:123], a[156:159], v[108:111]\n\t"
"s_lshl_b32 m0, s73, 9\n\t"
"ds_read_b128 a[156:159], v224 offset:17984\n\t"
"v_mfma_f32_16x16x32_bf16 v[108:111], a[124:127], a[172:175], v[108:111]\n\t"
"ds_read_b128 a[172:175], v224 offset:22080\n\t"
"v_mfma_f32_16x16x32_bf16 v[200:203], a[252:255], a[178:181], 0\n\t"
"s_waitcnt vmcnt(26) lgkmcnt(5)\n\t"
"ds_read_b64_tr_b16 a[176:177], v248 offset:0\n\t"
"v_cvt_pk_bf16_f32 v193, v193, v197\n\t"
"v_mfma_f32_16x16x32_bf16 v[0:3], a[0:3], a[144:147], v[0:3]\n\t"
"ds_read_b64_tr_b16 a[184:185], v249 offset:0\n\t"
"v_cvt_pk_bf16_f32 v195, v195, v199\n\t"
"v_mfma_f32_16x16x32_bf16 v[0:3], a[4:7], a[160:163], v[0:3]\n\t"
"ds_write_addtid_b32 v195 offset:10272\n\t"
"v_cvt_pk_bf16_f32 v194, v194, v198\n\t"
"v_mfma_f32_16x16x32_bf16 v[204:207], a[252:255], a[186:189], 0\n\t"
"s_waitcnt vmcnt(24)\n\t"
"ds_write_addtid_b32 v194 offset:2048\n\t"
"v_mfma_f32_16x16x32_bf16 v[16:19], a[8:11], a[144:147], v[16:19]\n\t"
"s_mov_b32 s51, 73728\n\t"
"v_mfma_f32_16x16x32_bf16 v[16:19], a[12:15], a[160:163], v[16:19]\n\t"
"s_waitcnt vmcnt(22)\n\t"
"ds_write_addtid_b32 v193 offset:8224\n\t"
"v_cvt_pk_bf16_f32 v192, v192, v196\n\t"
"v_mfma_f32_16x16x32_bf16 v[32:35], a[16:19], a[144:147], v[32:35]\n\t"
"v_mfma_f32_16x16x32_bf16 v[32:35], a[20:23], a[160:163], v[32:35]\n\t"
"ds_read_b128 a[144:147], v224 offset:18496\n\t"
"v_cvt_pk_bf16_f32 v202, v202, v206\n\t"
"v_mfma_f32_16x16x32_bf16 v[208:211], a[252:255], a[192:195], 0\n\t"
"s_waitcnt lgkmcnt(10)\n\t"
"ds_read_b64_tr_b16 a[178:179], v248 offset:512\n\t"
"v_mfma_f32_16x16x32_bf16 v[36:39], a[16:19], a[148:151], v[36:39]\n\t"
"ds_read_b128 a[160:163], v224 offset:22592\n\t"
"v_cvt_pk_bf16_f32 v201, v201, v205\n\t"
"v_mfma_f32_16x16x32_bf16 v[36:39], a[20:23], a[164:167], v[36:39]\n\t"
"ds_read_b64_tr_b16 a[180:181], v248 offset:1024\n\t"
"v_cvt_pk_bf16_f32 v203, v203, v207\n\t"
"v_mfma_f32_16x16x32_bf16 v[20:23], a[8:11], a[148:151], v[20:23]\n\t"
"ds_write_addtid_b32 v192 offset:0\n\t"
"v_mfma_f32_16x16x32_bf16 v[20:23], a[12:15], a[164:167], v[20:23]\n\t"
"s_add_u32 m0, s26, 145280\n\t"
"ds_read_b64_tr_b16 a[186:187], v249 offset:512\n\t"
"ds_read_b64_tr_b16 a[188:189], v249 offset:1024\n\t"
"s_mov_b32 s52, 81920\n\t"
"v_mfma_f32_16x16x32_bf16 v[212:215], a[252:255], a[200:203], 0\n\t"
"buffer_load_dwordx4 v238, s[12:15], s22 offen offset:2176 nt lds\n\t"
"v_mfma_f32_16x16x32_bf16 v[4:7], a[0:3], a[148:151], v[4:7]\n\t"
"s_lshl_b32 m0, s73, 9\n\t"
"v_mfma_f32_16x16x32_bf16 v[4:7], a[4:7], a[164:167], v[4:7]\n\t"
"ds_write_addtid_b32 v201 offset:8480\n\t"
"v_mfma_f32_16x16x32_bf16 v[216:219], a[252:255], a[194:197], 0\n\t"
"s_waitcnt lgkmcnt(15)\n\t"
"ds_write_addtid_b32 v203 offset:10528\n\t"
"v_mfma_f32_16x16x32_bf16 v[8:11], a[0:3], a[152:155], v[8:11]\n\t"
"ds_write_addtid_b32 v202 offset:2304\n\t"
"v_cvt_pk_bf16_f32 v200, v200, v204\n\t"
"v_mfma_f32_16x16x32_bf16 v[8:11], a[4:7], a[168:171], v[8:11]\n\t"
"ds_read_b128 a[148:151], v224 offset:19008\n\t"
"v_cvt_pk_bf16_f32 v211, v211, v215\n\t"
"v_mfma_f32_16x16x32_bf16 v[24:27], a[8:11], a[152:155], v[24:27]\n\t"
"v_cvt_pk_bf16_f32 v210, v210, v214\n\t"
"v_mfma_f32_16x16x32_bf16 v[24:27], a[12:15], a[168:171], v[24:27]\n\t"
"s_mov_b32 s55, 106496\n\t"
"ds_read_b64_tr_b16 a[192:193], v250 offset:0\n\t"
"v_cvt_pk_bf16_f32 v209, v209, v213\n\t"
"v_mfma_f32_16x16x32_bf16 v[220:223], a[252:255], a[202:205], 0\n\t"
"s_mov_b32 s53, 90112\n\t"
"ds_read_b128 a[164:167], v224 offset:23104\n\t"
"ds_write_addtid_b32 v200 offset:256\n\t"
"v_mfma_f32_16x16x32_bf16 v[40:43], a[16:19], a[152:155], v[40:43]\n\t"
"ds_read_b64_tr_b16 a[200:201], v251 offset:0\n\t"
"v_cvt_pk_bf16_f32 v208, v208, v212\n\t"
"v_mfma_f32_16x16x32_bf16 v[40:43], a[20:23], a[168:171], v[40:43]\n\t"
"s_waitcnt lgkmcnt(15)\n\t"
"ds_read_b128 a[152:155], v224 offset:19520\n\t"
"v_mfma_f32_16x16x32_bf16 v[44:47], a[16:19], a[156:159], v[44:47]\n\t"
"ds_write_addtid_b32 v209 offset:12320\n\t"
"v_mfma_f32_16x16x32_bf16 v[44:47], a[20:23], a[172:175], v[44:47]\n\t"
"ds_write_addtid_b32 v210 offset:6144\n\t"
"v_cvt_pk_bf16_f32 v216, v216, v220\n\t"
"v_mfma_f32_16x16x32_bf16 v[28:31], a[8:11], a[156:159], v[28:31]\n\t"
"ds_read_b64_tr_b16 a[194:195], v250 offset:512\n\t"
"v_cvt_pk_bf16_f32 v218, v218, v222\n\t"
"v_mfma_f32_16x16x32_bf16 v[28:31], a[12:15], a[172:175], v[28:31]\n\t"
"ds_write_addtid_b32 v211 offset:14368\n\t"
"v_cvt_pk_bf16_f32 v217, v217, v221\n\t"
"v_mfma_f32_16x16x32_bf16 v[12:15], a[0:3], a[156:159], v[12:15]\n\t"
"s_add_u32 m0, s26, 145280\n\t"
"ds_read_b64_tr_b16 a[196:197], v250 offset:1024\n\t"
"v_cvt_pk_bf16_f32 v219, v219, v223\n\t"
"v_mfma_f32_16x16x32_bf16 v[12:15], a[4:7], a[172:175], v[12:15]\n\t"
"s_waitcnt vmcnt(21) lgkmcnt(15)\n\t"
"buffer_load_dwordx4 v239, s[12:15], s22 offen offset:3200 nt lds\n\t"
"v_mfma_f32_16x16x32_bf16 v[96:99], a[24:27], a[144:147], v[96:99]\n\t"
"s_lshl_b32 m0, s73, 9\n\t"
"ds_read_b64_tr_b16 a[202:203], v251 offset:512\n\t"
"v_mfma_f32_16x16x32_bf16 v[96:99], a[28:31], a[160:163], v[96:99]\n\t"
"s_waitcnt vmcnt(20)\n\t"
"ds_write_addtid_b32 v217 offset:12576\n\t"
"s_mov_b32 s56, 114688\n\t"
"v_mfma_f32_16x16x32_bf16 v[112:115], a[32:35], a[144:147], v[112:115]\n\t"
"s_mov_b32 s58, 131072\n\t"
"ds_write_addtid_b32 v208 offset:4096\n\t"
"ds_write_addtid_b32 v219 offset:14624\n\t"
"v_mfma_f32_16x16x32_bf16 v[112:115], a[36:39], a[160:163], v[112:115]\n\t"
"s_waitcnt vmcnt(18)\n\t"
"ds_write_addtid_b32 v216 offset:4352\n\t"
"v_mfma_f32_16x16x32_bf16 v[128:131], a[40:43], a[144:147], v[128:131]\n\t"
"ds_read_b128 a[168:171], v224 offset:23616\n\t"
"v_mfma_f32_16x16x32_bf16 v[128:131], a[44:47], a[160:163], v[128:131]\n\t"
"s_waitcnt lgkmcnt(14)\n\t"
"ds_read_b128 a[156:159], v224 offset:20032\n\t"
"ds_write_addtid_b32 v218 offset:6400\n\t"
"s_mov_b32 s59, 139264\n\t"
"v_mfma_f32_16x16x32_bf16 v[132:135], a[40:43], a[148:151], v[132:135]\n\t"
"ds_read_b64_tr_b16 a[204:205], v251 offset:1024\n\t"
"v_mfma_f32_16x16x32_bf16 v[132:135], a[44:47], a[164:167], v[132:135]\n\t"
"ds_read_b128 a[172:175], v224 offset:24128\n\t"
"v_mfma_f32_16x16x32_bf16 v[116:119], a[32:35], a[148:151], v[116:119]\n\t"
"v_mfma_f32_16x16x32_bf16 v[116:119], a[36:39], a[164:167], v[116:119]\n\t"
"v_mfma_f32_16x16x32_bf16 v[100:103], a[24:27], a[148:151], v[100:103]\n\t"
"v_mfma_f32_16x16x32_bf16 v[100:103], a[28:31], a[164:167], v[100:103]\n\t"
"s_waitcnt lgkmcnt(0)\n\t"
"s_add_u32 s71, s71, 1\n\t"
"v_mov_b32 v215, s71\n\t"
"s_mov_b64 exec, 1\n\t"
"buffer_store_dword v215, off, s[8:11], s70 sc1\n\t"
"s_mov_b64 exec, -1\n\t"
"s_barrier\n\t"
"v_mfma_f32_16x16x32_bf16 v[104:107], a[24:27], a[152:155], v[104:107]\n\t"
"s_add_u32 m0, s26, 145280\n\t"
"ds_read_b128 a[160:163], v224 offset:4096\n\t"
"v_mfma_f32_16x16x32_bf16 v[104:107], a[28:31], a[168:171], v[104:107]\n\t"
"ds_read_b128 a[144:147], v224 offset:0\n\t"
"v_mfma_f32_16x16x32_bf16 v[120:123], a[32:35], a[152:155], v[120:123]\n\t"
"ds_read_b128 a[148:151], v224 offset:512\n\t"
"v_accvgpr_read_b32 v248, a208\n\t"
"v_mfma_f32_16x16x32_bf16 v[120:123], a[36:39], a[168:171], v[120:123]\n\t"
"ds_read_b128 a[164:167], v224 offset:4608\n\t"
"v_add_u32 v248, 80896, v248\n\t"
"v_mfma_f32_16x16x32_bf16 v[136:139], a[40:43], a[152:155], v[136:139]\n\t"
"v_mfma_f32_16x16x32_bf16 v[136:139], a[44:47], a[168:171], v[136:139]\n\t"
"s_mov_b64 exec, s[36:37]\n\t"
"buffer_load_dwordx4 v240, s[12:15], s22 offen offset:3712 nt lds\n\t"
"s_mov_b64 exec, -1\n\t"
"v_mfma_f32_16x16x32_bf16 v[192:195], a[252:255], a[176:179], 0\n\t"
"s_lshl_b32 m0, s73, 9\n\t"
"ds_read_b128 a[152:155], v224 offset:1024\n\t"
"v_mfma_f32_16x16x32_bf16 v[140:143], a[40:43], a[156:159], v[140:143]\n\t"
"v_mfma_f32_16x16x32_bf16 v[140:143], a[44:47], a[172:175], v[140:143]\n\t"
"v_xor_b32 v249, 32, v248\n\t"
"v_mfma_f32_16x16x32_bf16 v[196:199], a[252:255], a[184:187], 0\n\t"
"v_mfma_f32_16x16x32_bf16 v[124:127], a[32:35], a[156:159], v[124:127]\n\t"
"ds_read_b128 a[168:171], v224 offset:5120\n\t"
"v_mfma_f32_16x16x32_bf16 v[124:127], a[36:39], a[172:175], v[124:127]\n\t"
"v_mfma_f32_16x16x32_bf16 v[108:111], a[24:27], a[156:159], v[108:111]\n\t"
"v_mfma_f32_16x16x32_bf16 v[108:111], a[28:31], a[172:175], v[108:111]\n\t"
"s_mov_b32 s75, 221184\n\t"
"buffer_load_dwordx4 a[96:99], v235, s[8:11], s75 offen offset:0 sc1\n\t"
"v_mfma_f32_16x16x32_bf16 v[200:203], a[252:255], a[178:181], 0\n\t"
"s_waitcnt vmcnt(18) lgkmcnt(4)\n\t"
"v_pk_add_f32 v[96:97], v[0:1], v[96:97] neg_lo:[0,1] neg_hi:[0,1]\n\t"
"v_pk_add_f32 v[98:99], v[2:3], v[98:99] neg_lo:[0,1] neg_hi:[0,1]\n\t"
"v_pk_fma_f32 v[0:1], v[96:97], s[90:91], v[0:1] neg_lo:[1,0,0] neg_hi:[1,0,0]\n\t"
"v_pk_fma_f32 v[2:3], v[98:99], s[90:91], v[2:3] neg_lo:[1,0,0] neg_hi:[1,0,0]\n\t"
"v_pk_add_f32 v[112:113], v[16:17], v[112:113] neg_lo:[0,1] neg_hi:[0,1]\n\t"
"v_pk_add_f32 v[114:115], v[18:19], v[114:115] neg_lo:[0,1] neg_hi:[0,1]\n\t"
"v_pk_fma_f32 v[16:17], v[112:113], s[90:91], v[16:17] neg_lo:[1,0,0] neg_hi:[1,0,0]\n\t"
"v_pk_fma_f32 v[18:19], v[114:115], s[90:91], v[18:19] neg_lo:[1,0,0] neg_hi:[1,0,0]\n\t"
"v_pk_add_f32 v[128:129], v[32:33], v[128:129] neg_lo:[0,1] neg_hi:[0,1]\n\t"
"v_pk_add_f32 v[130:131], v[34:35], v[130:131] neg_lo:[0,1] neg_hi:[0,1]\n\t"
"v_pk_fma_f32 v[32:33], v[128:129], s[90:91], v[32:33] neg_lo:[1,0,0] neg_hi:[1,0,0]\n\t"
"v_pk_fma_f32 v[34:35], v[130:131], s[90:91], v[34:35] neg_lo:[1,0,0] neg_hi:[1,0,0]\n\t"
"v_pk_add_f32 v[132:133], v[36:37], v[132:133] neg_lo:[0,1] neg_hi:[0,1]\n\t"
"v_pk_add_f32 v[134:135], v[38:39], v[134:135] neg_lo:[0,1] neg_hi:[0,1]\n\t"
"v_pk_fma_f32 v[36:37], v[132:133], s[90:91], v[36:37] neg_lo:[1,0,0] neg_hi:[1,0,0]\n\t"
"v_pk_fma_f32 v[38:39], v[134:135], s[90:91], v[38:39] neg_lo:[1,0,0] neg_hi:[1,0,0]\n\t"
"v_pk_add_f32 v[116:117], v[20:21], v[116:117] neg_lo:[0,1] neg_hi:[0,1]\n\t"
"v_pk_add_f32 v[118:119], v[22:23], v[118:119] neg_lo:[0,1] neg_hi:[0,1]\n\t"
"v_pk_fma_f32 v[20:21], v[116:117], s[90:91], v[20:21] neg_lo:[1,0,0] neg_hi:[1,0,0]\n\t"
"v_pk_fma_f32 v[22:23], v[118:119], s[90:91], v[22:23] neg_lo:[1,0,0] neg_hi:[1,0,0]\n\t"
"v_pk_add_f32 v[100:101], v[4:5], v[100:101] neg_lo:[0,1] neg_hi:[0,1]\n\t"
"v_pk_add_f32 v[102:103], v[6:7], v[102:103] neg_lo:[0,1] neg_hi:[0,1]\n\t"
"v_pk_fma_f32 v[4:5], v[100:101], s[90:91], v[4:5] neg_lo:[1,0,0] neg_hi:[1,0,0]\n\t"
"v_pk_fma_f32 v[6:7], v[102:103], s[90:91], v[6:7] neg_lo:[1,0,0] neg_hi:[1,0,0]\n\t"
"v_pk_add_f32 v[104:105], v[8:9], v[104:105] neg_lo:[0,1] neg_hi:[0,1]\n\t"
"v_pk_add_f32 v[106:107], v[10:11], v[106:107] neg_lo:[0,1] neg_hi:[0,1]\n\t"
"v_pk_fma_f32 v[8:9], v[104:105], s[90:91], v[8:9] neg_lo:[1,0,0] neg_hi:[1,0,0]\n\t"
"v_pk_fma_f32 v[10:11], v[106:107], s[90:91], v[10:11] neg_lo:[1,0,0] neg_hi:[1,0,0]\n\t"
"v_pk_add_f32 v[120:121], v[24:25], v[120:121] neg_lo:[0,1] neg_hi:[0,1]\n\t"
"v_pk_add_f32 v[122:123], v[26:27], v[122:123] neg_lo:[0,1] neg_hi:[0,1]\n\t"
"v_pk_fma_f32 v[24:25], v[120:121], s[90:91], v[24:25] neg_lo:[1,0,0] neg_hi:[1,0,0]\n\t"
"v_pk_fma_f32 v[26:27], v[122:123], s[90:91], v[26:27] neg_lo:[1,0,0] neg_hi:[1,0,0]\n\t"
"v_pk_add_f32 v[136:137], v[40:41], v[136:137] neg_lo:[0,1] neg_hi:[0,1]\n\t"
"v_pk_add_f32 v[138:139], v[42:43], v[138:139] neg_lo:[0,1] neg_hi:[0,1]\n\t"
"v_pk_fma_f32 v[40:41], v[136:137], s[90:91], v[40:41] neg_lo:[1,0,0] neg_hi:[1,0,0]\n\t"
"v_pk_fma_f32 v[42:43], v[138:139], s[90:91], v[42:43] neg_lo:[1,0,0] neg_hi:[1,0,0]\n\t"
"v_pk_add_f32 v[140:141], v[44:45], v[140:141] neg_lo:[0,1] neg_hi:[0,1]\n\t"
"v_pk_add_f32 v[142:143], v[46:47], v[142:143] neg_lo:[0,1] neg_hi:[0,1]\n\t"
"v_pk_fma_f32 v[44:45], v[140:141], s[90:91], v[44:45] neg_lo:[1,0,0] neg_hi:[1,0,0]\n\t"
"v_pk_fma_f32 v[46:47], v[142:143], s[90:91], v[46:47] neg_lo:[1,0,0] neg_hi:[1,0,0]\n\t"
"v_pk_add_f32 v[124:125], v[28:29], v[124:125] neg_lo:[0,1] neg_hi:[0,1]\n\t"
"v_pk_add_f32 v[126:127], v[30:31], v[126:127] neg_lo:[0,1] neg_hi:[0,1]\n\t"
"v_pk_fma_f32 v[28:29], v[124:125], s[90:91], v[28:29] neg_lo:[1,0,0] neg_hi:[1,0,0]\n\t"
"v_pk_fma_f32 v[30:31], v[126:127], s[90:91], v[30:31] neg_lo:[1,0,0] neg_hi:[1,0,0]\n\t"
"v_pk_add_f32 v[108:109], v[12:13], v[108:109] neg_lo:[0,1] neg_hi:[0,1]\n\t"
"v_pk_add_f32 v[110:111], v[14:15], v[110:111] neg_lo:[0,1] neg_hi:[0,1]\n\t"
"v_pk_fma_f32 v[12:13], v[108:109], s[90:91], v[12:13] neg_lo:[1,0,0] neg_hi:[1,0,0]\n\t"
"v_pk_fma_f32 v[14:15], v[110:111], s[90:91], v[14:15] neg_lo:[1,0,0] neg_hi:[1,0,0]\n\t"
"v_mfma_f32_16x16x32_bf16 v[0:3], a[48:51], a[144:147], v[0:3]\n\t"
"v_cvt_pk_bf16_f32 v194, v194, v198\n\t"
"v_mfma_f32_16x16x32_bf16 v[0:3], a[52:55], a[160:163], v[0:3]\n\t"
"buffer_load_dwordx4 a[100:103], v235, s[8:11], s75 offen offset:1024 sc1\n\t"
"v_mfma_f32_16x16x32_bf16 v[204:207], a[252:255], a[186:189], 0\n\t"
"s_waitcnt vmcnt(17)\n\t"
"v_cvt_pk_bf16_f32 v193, v193, v197\n\t"
"v_mfma_f32_16x16x32_bf16 v[16:19], a[56:59], a[144:147], v[16:19]\n\t"
"v_cvt_pk_bf16_f32 v192, v192, v196\n\t"
"v_xor_b32 v250, 64, v248\n\t"
"v_mfma_f32_16x16x32_bf16 v[16:19], a[60:63], a[160:163], v[16:19]\n\t"
"s_waitcnt vmcnt(15)\n\t"
"ds_write_addtid_b32 v192 offset:16448\n\t"
"v_mfma_f32_16x16x32_bf16 v[32:35], a[64:67], a[144:147], v[32:35]\n\t"
"ds_write_addtid_b32 v193 offset:24672\n\t"
"v_cvt_pk_bf16_f32 v195, v195, v199\n\t"
"v_mfma_f32_16x16x32_bf16 v[32:35], a[68:71], a[160:163], v[32:35]\n\t"
"buffer_load_dwordx4 a[104:107], v235, s[8:11], s75 offen offset:2048 sc1\n\t"
"v_mfma_f32_16x16x32_bf16 v[208:211], a[252:255], a[192:195], 0\n\t"
"s_waitcnt lgkmcnt(4)\n\t"
"ds_read_b128 a[156:159], v224 offset:1536\n\t"
"v_mfma_f32_16x16x32_bf16 v[36:39], a[64:67], a[148:151], v[36:39]\n\t"
"s_mov_b32 s65, 163840\n\t"
"ds_read_b128 a[172:175], v224 offset:5632\n\t"
"ds_write_addtid_b32 v195 offset:26720\n\t"
"v_mfma_f32_16x16x32_bf16 v[36:39], a[68:71], a[164:167], v[36:39]\n\t"
"buffer_load_dwordx4 a[108:111], v235, s[8:11], s75 offen offset:3072 sc1\n\t"
"v_mfma_f32_16x16x32_bf16 v[20:23], a[56:59], a[148:151], v[20:23]\n\t"
"ds_read_b128 a[160:163], v224 offset:6144\n\t"
"v_cvt_pk_bf16_f32 v203, v203, v207\n\t"
"v_mfma_f32_16x16x32_bf16 v[20:23], a[60:63], a[164:167], v[20:23]\n\t"
"ds_write_addtid_b32 v194 offset:18496\n\t"
"v_cvt_pk_bf16_f32 v201, v201, v205\n\t"
"v_mfma_f32_16x16x32_bf16 v[212:215], a[252:255], a[200:203], 0\n\t"
"buffer_load_dwordx4 a[112:115], v252, s[8:11], s75 offen offset:0 sc1\n\t"
"v_mfma_f32_16x16x32_bf16 v[4:7], a[48:51], a[148:151], v[4:7]\n\t"
"s_mov_b32 s68, 188416\n\t"
"ds_read_b128 a[144:147], v224 offset:2048\n\t"
"ds_write_addtid_b32 v201 offset:24928\n\t"
"v_mfma_f32_16x16x32_bf16 v[4:7], a[52:55], a[164:167], v[4:7]\n\t"
"ds_write_addtid_b32 v203 offset:26976\n\t"
"v_cvt_pk_bf16_f32 v202, v202, v206\n\t"
"v_mfma_f32_16x16x32_bf16 v[216:219], a[252:255], a[194:197], 0\n\t"
"s_waitcnt lgkmcnt(10)\n\t"
"buffer_load_dwordx4 a[116:119], v252, s[8:11], s75 offen offset:1024 sc1\n\t"
"v_mfma_f32_16x16x32_bf16 v[8:11], a[48:51], a[152:155], v[8:11]\n\t"
"s_mov_b32 s67, 180224\n\t"
"ds_read_b128 a[148:151], v224 offset:2560\n\t"
"ds_write_addtid_b32 v202 offset:18752\n\t"
"v_mfma_f32_16x16x32_bf16 v[8:11], a[52:55], a[168:171], v[8:11]\n\t"
"v_cvt_pk_bf16_f32 v200, v200, v204\n\t"
"v_cvt_pk_bf16_f32 v208, v208, v212\n\t"
"v_mfma_f32_16x16x32_bf16 v[24:27], a[56:59], a[152:155], v[24:27]\n\t"
"ds_read_b128 a[164:167], v224 offset:6656\n\t"
"v_cvt_pk_bf16_f32 v209, v209, v213\n\t"
"v_mfma_f32_16x16x32_bf16 v[24:27], a[60:63], a[168:171], v[24:27]\n\t"
"s_mov_b32 s78, 245760\n\t"
"buffer_load_dwordx4 a[120:123], v235, s[8:11], s78 offen offset:0 sc1\n\t"
"v_mfma_f32_16x16x32_bf16 v[220:223], a[252:255], a[202:205], 0\n\t"
"ds_write_addtid_b32 v208 offset:20544\n\t"
"v_mfma_f32_16x16x32_bf16 v[40:43], a[64:67], a[152:155], v[40:43]\n\t"
"ds_write_addtid_b32 v200 offset:16704\n\t"
"v_cvt_pk_bf16_f32 v210, v210, v214\n\t"
"v_mfma_f32_16x16x32_bf16 v[40:43], a[68:71], a[168:171], v[40:43]\n\t"
"s_waitcnt lgkmcnt(11)\n\t"
"buffer_load_dwordx4 a[124:127], v235, s[8:11], s78 offen offset:1024 sc1\n\t"
"v_mfma_f32_16x16x32_bf16 v[44:47], a[64:67], a[156:159], v[44:47]\n\t"
"ds_read_b128 a[168:171], v224 offset:7168\n\t"
"v_mfma_f32_16x16x32_bf16 v[44:47], a[68:71], a[172:175], v[44:47]\n\t"
"s_mov_b32 s74, 212992\n\t"
"ds_read_b128 a[152:155], v224 offset:3072\n\t"
"v_mfma_f32_16x16x32_bf16 v[28:31], a[56:59], a[156:159], v[28:31]\n\t"
"v_cvt_pk_bf16_f32 v218, v218, v222\n\t"
"v_cvt_pk_bf16_f32 v219, v219, v223\n\t"
"v_mfma_f32_16x16x32_bf16 v[28:31], a[60:63], a[172:175], v[28:31]\n\t"
"buffer_load_dwordx4 a[128:131], v235, s[8:11], s78 offen offset:2048 sc1\n\t"
"v_mfma_f32_16x16x32_bf16 v[12:15], a[48:51], a[156:159], v[12:15]\n\t"
"ds_read_b128 a[156:159], v224 offset:3584\n\t"
"v_cvt_pk_bf16_f32 v211, v211, v215\n\t"
"v_mfma_f32_16x16x32_bf16 v[12:15], a[52:55], a[172:175], v[12:15]\n\t"
"s_waitcnt vmcnt(20) lgkmcnt(10)\n\t"
"ds_write_addtid_b32 v211 offset:30816\n\t"
"v_cvt_pk_bf16_f32 v217, v217, v221\n\t"
"v_mfma_f32_16x16x32_bf16 v[96:99], a[72:75], a[144:147], v[96:99]\n\t"
"ds_write_addtid_b32 v210 offset:22592\n\t"
"v_xor_b32 v251, 96, v248\n\t"
"v_mfma_f32_16x16x32_bf16 v[96:99], a[76:79], a[160:163], v[96:99]\n\t"
"s_waitcnt vmcnt(18)\n\t"
"buffer_load_dwordx4 a[132:135], v235, s[8:11], s78 offen offset:3072 sc1\n\t"
"v_mfma_f32_16x16x32_bf16 v[112:115], a[80:83], a[144:147], v[112:115]\n\t"
"s_mov_b32 s72, 204800\n\t"
"ds_write_addtid_b32 v209 offset:28768\n\t"
"ds_write_addtid_b32 v218 offset:22848\n\t"
"v_mfma_f32_16x16x32_bf16 v[112:115], a[84:87], a[160:163], v[112:115]\n\t"
"s_waitcnt vmcnt(17)\n\t"
"ds_write_addtid_b32 v217 offset:29024\n\t"
"v_cvt_pk_bf16_f32 v216, v216, v220\n\t"
"v_mfma_f32_16x16x32_bf16 v[128:131], a[88:91], a[144:147], v[128:131]\n\t"
"s_nop 0\n\t"
"ds_write_addtid_b32 v216 offset:20800\n\t"
"ds_write_addtid_b32 v219 offset:31072\n\t"
"v_mfma_f32_16x16x32_bf16 v[128:131], a[92:95], a[160:163], v[128:131]\n\t"
"s_waitcnt lgkmcnt(12)\n\t"
"buffer_load_dwordx4 a[136:139], v252, s[8:11], s78 offen offset:0 sc1\n\t"
"v_mfma_f32_16x16x32_bf16 v[132:135], a[88:91], a[148:151], v[132:135]\n\t"
"s_mov_b32 s64, 155648\n\t"
"ds_read_b128 a[172:175], v224 offset:7680\n\t"
"v_mfma_f32_16x16x32_bf16 v[132:135], a[92:95], a[164:167], v[132:135]\n\t"
"buffer_load_dwordx4 a[140:143], v252, s[8:11], s78 offen offset:1024 sc1\n\t"
"v_mfma_f32_16x16x32_bf16 v[116:119], a[80:83], a[148:151], v[116:119]\n\t"
"v_mfma_f32_16x16x32_bf16 v[116:119], a[84:87], a[164:167], v[116:119]\n\t"
"v_mfma_f32_16x16x32_bf16 v[100:103], a[72:75], a[148:151], v[100:103]\n\t"
"v_mfma_f32_16x16x32_bf16 v[100:103], a[76:79], a[164:167], v[100:103]\n\t"
"buffer_load_dwordx4 a[248:251], v247, s[12:15], 0 offen offset:256 nt\n\t"
"s_waitcnt vmcnt(13)\n\t"
"s_waitcnt lgkmcnt(0)\n\t"
"s_barrier\n\t"
"v_mfma_f32_16x16x32_bf16 v[104:107], a[72:75], a[152:155], v[104:107]\n\t"
"s_nop 0\n\t"
"ds_read_b128 a[144:147], v224 offset:16448\n\t"
"ds_read_b128 a[160:163], v224 offset:20544\n\t"
"v_mfma_f32_16x16x32_bf16 v[104:107], a[76:79], a[168:171], v[104:107]\n\t"
"buffer_load_dwordx4 a[0:3], v252, s[8:11], s58 offen offset:0 sc1\n\t"
"v_mfma_f32_16x16x32_bf16 v[120:123], a[80:83], a[152:155], v[120:123]\n\t"
"s_nop 0\n\t"
"ds_read_b128 a[148:151], v224 offset:16960\n\t"
"ds_read_b64_tr_b16 a[176:177], v248 offset:41472\n\t"
"v_mfma_f32_16x16x32_bf16 v[120:123], a[84:87], a[168:171], v[120:123]\n\t"
"s_nop 0\n\t"
"ds_read_b128 a[164:167], v224 offset:21056\n\t"
"ds_read_b64_tr_b16 a[178:179], v249 offset:41472\n\t"
"v_mfma_f32_16x16x32_bf16 v[136:139], a[88:91], a[152:155], v[136:139]\n\t"
"s_nop 0\n\t"
"ds_read_b64_tr_b16 a[180:181], v248 offset:41984\n\t"
"ds_read_b64_tr_b16 a[182:183], v249 offset:41984\n\t"
"v_mfma_f32_16x16x32_bf16 v[136:139], a[92:95], a[168:171], v[136:139]\n\t"
"buffer_load_dwordx4 a[4:7], v252, s[8:11], s58 offen offset:1024 sc1\n\t"
"v_mfma_f32_16x16x32_bf16 v[140:143], a[88:91], a[156:159], v[140:143]\n\t"
"s_mov_b32 s76, 229376\n\t"
"ds_read_b64_tr_b16 a[184:185], v250 offset:43776\n\t"
"ds_read_b64_tr_b16 a[186:187], v251 offset:43776\n\t"
"v_mfma_f32_16x16x32_bf16 v[140:143], a[92:95], a[172:175], v[140:143]\n\t"
"s_nop 0\n\t"
"ds_read_b128 a[152:155], v224 offset:17472\n\t"
"ds_read_b64_tr_b16 a[188:189], v250 offset:44288\n\t"
"v_mfma_f32_16x16x32_bf16 v[124:127], a[80:83], a[156:159], v[124:127]\n\t"
"s_nop 0\n\t"
"ds_read_b64_tr_b16 a[190:191], v251 offset:44288\n\t"
"ds_read_b64_tr_b16 a[192:193], v250 offset:41472\n\t"
"v_mfma_f32_16x16x32_bf16 v[124:127], a[84:87], a[172:175], v[124:127]\n\t"
"s_waitcnt lgkmcnt(6)\n\t"
"buffer_load_dwordx4 a[8:11], v252, s[8:11], s58 offen offset:2048 sc1\n\t"
"v_mfma_f32_4x4x4_16b_bf16 v[192:195], a[210:211], a[176:177], 0\n\t"
"ds_read_b64_tr_b16 a[194:195], v251 offset:41472\n\t"
"v_mfma_f32_4x4x4_16b_bf16 v[196:199], a[210:211], a[178:179], 0\n\t"
"ds_read_b128 a[168:171], v224 offset:21568\n\t"
"v_mfma_f32_4x4x4_16b_bf16 v[200:203], a[210:211], a[180:181], 0\n\t"
"ds_read_b64_tr_b16 a[196:197], v250 offset:41984\n\t"
"v_mfma_f32_4x4x4_16b_bf16 v[204:207], a[210:211], a[182:183], 0\n\t"
"buffer_load_dwordx4 a[12:15], v252, s[8:11], s58 offen offset:3072 sc1\n\t"
"v_mfma_f32_16x16x32_bf16 v[108:111], a[72:75], a[156:159], v[108:111]\n\t"
"ds_read_b64_tr_b16 a[198:199], v251 offset:41984\n\t"
"s_nop 0\n\t"
"v_cvt_pk_bf16_f32 v195, v195, v199\n\t"
"v_mfma_f32_16x16x32_bf16 v[108:111], a[76:79], a[172:175], v[108:111]\n\t"
"s_waitcnt vmcnt(15)\n\t"
"buffer_load_dwordx4 a[16:19], v235, s[8:11], s59 offen offset:0 sc1\n\t"
"v_mfma_f32_16x16x32_bf16 v[0:3], a[96:99], a[144:147], v[0:3]\n\t"
"ds_read_b64_tr_b16 a[200:201], v248 offset:43776\n\t"
"v_cvt_pk_bf16_f32 v194, v194, v198\n\t"
"v_mfma_f32_16x16x32_bf16 v[0:3], a[100:103], a[160:163], v[0:3]\n\t"
"s_waitcnt vmcnt(14)\n\t"
"ds_read_b128 a[156:159], v224 offset:17984\n\t"
"v_cvt_pk_bf16_f32 v197, v193, v197\n\t"
"v_mfma_f32_16x16x32_bf16 v[16:19], a[104:107], a[144:147], v[16:19]\n\t"
"ds_write_addtid_b32 v197 offset:2048\n\t"
"v_cvt_pk_bf16_f32 v199, v201, v205\n\t"
"v_mfma_f32_16x16x32_bf16 v[16:19], a[108:111], a[160:163], v[16:19]\n\t"
"s_waitcnt vmcnt(12)\n\t"
"buffer_load_dwordx4 a[20:23], v235, s[8:11], s59 offen offset:1024 sc1\n\t"
"v_mfma_f32_16x16x32_bf16 v[32:35], a[112:115], a[144:147], v[32:35]\n\t"
"ds_read_b64_tr_b16 a[202:203], v249 offset:43776\n\t"
"v_cvt_pk_bf16_f32 v196, v192, v196\n\t"
"v_mfma_f32_16x16x32_bf16 v[32:35], a[116:119], a[160:163], v[32:35]\n\t"
"s_waitcnt lgkmcnt(9)\n\t"
"ds_read_b128 a[172:175], v224 offset:22080\n\t"
"v_cvt_pk_bf16_f32 v192, v202, v206\n\t"
"v_mfma_f32_4x4x4_16b_bf16 v[208:211], a[210:211], a[184:185], 0\n\t"
"buffer_load_dwordx4 a[24:27], v252, s[8:11], s57 offen offset:2048 sc1\n\t"
"v_mfma_f32_4x4x4_16b_bf16 v[212:215], a[210:211], a[186:187], 0\n\t"
"ds_read_b128 a[144:147], v224 offset:18496\n\t"
"v_mfma_f32_4x4x4_16b_bf16 v[216:219], a[210:211], a[188:189], 0\n\t"
"v_cvt_pk_bf16_f32 v193, v203, v207\n\t"
"v_mfma_f32_4x4x4_16b_bf16 v[220:223], a[210:211], a[190:191], 0\n\t"
"buffer_load_dwordx4 a[28:31], v252, s[8:11], s57 offen offset:3072 sc1\n\t"
"v_mfma_f32_16x16x32_bf16 v[36:39], a[112:115], a[148:151], v[36:39]\n\t"
"ds_write_addtid_b32 v196 offset:0\n\t"
"s_nop 0\n\t"
"v_cvt_pk_bf16_f32 v210, v210, v214\n\t"
"v_mfma_f32_16x16x32_bf16 v[36:39], a[116:119], a[164:167], v[36:39]\n\t"
"ds_write_addtid_b32 v199 offset:2304\n\t"
"v_cvt_pk_bf16_f32 v211, v211, v215\n\t"
"v_mfma_f32_16x16x32_bf16 v[20:23], a[104:107], a[148:151], v[20:23]\n\t"
"s_mov_b32 s79, 253952\n\t"
"ds_read_b128 a[160:163], v224 offset:22592\n\t"
"v_cvt_pk_bf16_f32 v212, v208, v212\n\t"
"v_mfma_f32_16x16x32_bf16 v[20:23], a[108:111], a[164:167], v[20:23]\n\t"
"buffer_load_dwordx4 a[32:35], v235, s[8:11], s58 offen offset:0 sc1\n\t"
"v_mfma_f32_16x16x32_bf16 v[4:7], a[96:99], a[148:151], v[4:7]\n\t"
"ds_read_b64_tr_b16 a[204:205], v248 offset:44288\n\t"
"v_cvt_pk_bf16_f32 v208, v218, v222\n\t"
"v_mfma_f32_16x16x32_bf16 v[4:7], a[100:103], a[164:167], v[4:7]\n\t"
"s_waitcnt lgkmcnt(12)\n\t"
"ds_read_b128 a[164:167], v224 offset:23104\n\t"
"v_cvt_pk_bf16_f32 v213, v209, v213\n\t"
"v_mfma_f32_16x16x32_bf16 v[8:11], a[96:99], a[152:155], v[8:11]\n\t"
"v_cvt_pk_bf16_f32 v198, v200, v204\n\t"
"v_cvt_pk_bf16_f32 v209, v219, v223\n\t"
"v_mfma_f32_16x16x32_bf16 v[8:11], a[100:103], a[168:171], v[8:11]\n\t"
"s_waitcnt lgkmcnt(11)\n\t"
"buffer_load_dwordx4 a[36:39], v235, s[8:11], s58 offen offset:1024 sc1\n\t"
"v_mfma_f32_4x4x4_16b_bf16 v[200:203], a[210:211], a[192:193], 0\n\t"
"ds_read_b128 a[148:151], v224 offset:19008\n\t"
"v_cvt_pk_bf16_f32 v215, v217, v221\n\t"
"v_mfma_f32_4x4x4_16b_bf16 v[204:207], a[210:211], a[194:195], 0\n\t"
"ds_write_addtid_b32 v198 offset:256\n\t"
"v_mfma_f32_4x4x4_16b_bf16 v[196:199], a[210:211], a[196:197], 0\n\t"
"v_cvt_pk_bf16_f32 v214, v216, v220\n\t"
"v_mfma_f32_4x4x4_16b_bf16 v[216:219], a[210:211], a[198:199], 0\n\t"
"buffer_load_dwordx4 a[40:43], v235, s[8:11], s58 offen offset:2048 sc1\n\t"
"buffer_load_dwordx4 a[44:47], v235, s[8:11], s58 offen offset:3072 sc1\n\t"
"v_mfma_f32_16x16x32_bf16 v[24:27], a[104:107], a[152:155], v[24:27]\n\t"
"ds_write_addtid_b32 v215 offset:10528\n\t"
"v_cvt_pk_bf16_f32 v205, v201, v205\n\t"
"s_add_u32 m0, s43, 101632\n\t"
"v_mfma_f32_16x16x32_bf16 v[24:27], a[108:111], a[168:171], v[24:27]\n\t"
"s_mov_b64 exec, s[44:45]\n\t"
"buffer_load_dwordx4 v247, s[12:15], 0 offen offset:0 nt lds\n\t"
"s_mov_b64 exec, -1\n\t"
"s_add_u32 m0, s26, 103936\n\t"
"s_nop 0\n\t"
"buffer_load_dwordx4 v236, s[12:15], s22 offen offset:0 nt lds\n\t"
"v_mfma_f32_16x16x32_bf16 v[40:43], a[112:115], a[152:155], v[40:43]\n\t"
"ds_read_b64_tr_b16 a[206:207], v249 offset:44288\n\t"
"v_cvt_pk_bf16_f32 v202, v202, v206\n\t"
"v_mfma_f32_16x16x32_bf16 v[40:43], a[116:119], a[168:171], v[40:43]\n\t"
"s_waitcnt lgkmcnt(10)\n\t"
"buffer_load_dwordx4 v237, s[12:15], s22 offen offset:1024 nt lds\n\t"
"buffer_load_dwordx4 v238, s[12:15], s22 offen offset:2048 nt lds\n\t"
"s_lshl_b32 m0, s73, 9\n\t"
"v_mfma_f32_16x16x32_bf16 v[44:47], a[112:115], a[156:159], v[44:47]\n\t"
"ds_write_addtid_b32 v212 offset:8224\n\t"
"v_cvt_pk_bf16_f32 v204, v200, v204\n\t"
"s_add_u32 m0, s26, 103936\n\t"
"v_mfma_f32_16x16x32_bf16 v[44:47], a[116:119], a[172:175], v[44:47]\n\t"
"buffer_load_dwordx4 v239, s[12:15], s22 offen offset:3072 nt lds\n\t"
"s_mov_b64 exec, s[36:37]\n\t"
"buffer_load_dwordx4 v240, s[12:15], s22 offen offset:3584 nt lds\n\t"
"s_mov_b64 exec, -1\n\t"
"v_cvt_pk_bf16_f32 v203, v203, v207\n\t"
"s_lshl_b32 m0, s73, 9\n\t"
"v_mfma_f32_16x16x32_bf16 v[28:31], a[104:107], a[156:159], v[28:31]\n\t"
"s_mov_b32 s80, 262144\n\t"
"ds_write_addtid_b32 v214 offset:8480\n\t"
"v_cvt_pk_bf16_f32 v201, v199, v219\n\t"
"v_mfma_f32_16x16x32_bf16 v[28:31], a[108:111], a[172:175], v[28:31]\n\t"
"s_waitcnt lgkmcnt(2)\n\t"
"buffer_load_dwordx4 a[48:51], v252, s[8:11], s64 offen offset:0 sc1\n\t"
"buffer_load_dwordx4 a[52:55], v252, s[8:11], s64 offen offset:1024 sc1\n\t"
"v_cvt_pk_bf16_f32 v200, v198, v218\n\t"
"v_mfma_f32_4x4x4_16b_bf16 v[220:223], a[210:211], a[200:201], 0\n\t"
"ds_write_addtid_b32 v213 offset:10272\n\t"
"v_cvt_pk_bf16_f32 v207, v197, v217\n\t"
"v_mfma_f32_4x4x4_16b_bf16 v[212:215], a[210:211], a[202:203], 0\n\t"
"buffer_load_dwordx4 a[56:59], v252, s[8:11], s64 offen offset:2048 sc1\n\t"
"buffer_load_dwordx4 a[60:63], v252, s[8:11], s64 offen offset:3072 sc1\n\t"
"v_cvt_pk_bf16_f32 v206, v196, v216\n\t"
"v_mfma_f32_4x4x4_16b_bf16 v[196:199], a[210:211], a[204:205], 0\n\t"
"ds_write_addtid_b32 v207 offset:6400\n\t"
"v_mfma_f32_4x4x4_16b_bf16 v[216:219], a[210:211], a[206:207], 0\n\t"
"buffer_load_dwordx4 a[64:67], v235, s[8:11], s65 offen offset:0 sc1\n\t"
"buffer_load_dwordx4 a[68:71], v235, s[8:11], s65 offen offset:1024 sc1\n\t"
"v_mfma_f32_16x16x32_bf16 v[12:15], a[96:99], a[156:159], v[12:15]\n\t"
"ds_write_addtid_b32 v204 offset:4096\n\t"
"v_cvt_pk_bf16_f32 v223, v223, v215\n\t"
"v_mfma_f32_16x16x32_bf16 v[12:15], a[100:103], a[172:175], v[12:15]\n\t"
"s_waitcnt vmcnt(29)\n\t"
"ds_write_addtid_b32 v205 offset:6144\n\t"
"buffer_load_dwordx4 a[72:75], v252, s[8:11], s63 offen offset:2048 sc1\n\t"
"buffer_load_dwordx4 a[76:79], v252, s[8:11], s63 offen offset:3072 sc1\n\t"
"v_cvt_pk_bf16_f32 v212, v220, v212\n\t"
"v_mfma_f32_16x16x32_bf16 v[96:99], a[120:123], a[144:147], v[96:99]\n\t"
"ds_write_addtid_b32 v206 offset:4352\n\t"
"v_cvt_pk_bf16_f32 v215, v197, v217\n\t"
"v_mfma_f32_16x16x32_bf16 v[96:99], a[124:127], a[160:163], v[96:99]\n\t"
"s_waitcnt vmcnt(29)\n\t"
"ds_write_addtid_b32 v212 offset:12320\n\t"
"buffer_load_dwordx4 a[80:83], v235, s[8:11], s64 offen offset:0 sc1\n\t"
"buffer_load_dwordx4 a[84:87], v235, s[8:11], s64 offen offset:1024 sc1\n\t"
"v_cvt_pk_bf16_f32 v222, v222, v214\n\t"
"v_mfma_f32_16x16x32_bf16 v[112:115], a[128:131], a[144:147], v[112:115]\n\t"
"s_mov_b32 s83, 286720\n\t"
"ds_write_addtid_b32 v215 offset:14624\n\t"
"v_cvt_pk_bf16_f32 v214, v196, v216\n\t"
"v_mfma_f32_16x16x32_bf16 v[112:115], a[132:135], a[160:163], v[112:115]\n\t"
"s_waitcnt vmcnt(29)\n\t"
"buffer_load_dwordx4 a[88:91], v235, s[8:11], s64 offen offset:2048 sc1\n\t"
"buffer_load_dwordx4 a[92:95], v235, s[8:11], s64 offen offset:3072 sc1\n\t"
"v_mfma_f32_16x16x32_bf16 v[128:131], a[136:139], a[144:147], v[128:131]\n\t"
"ds_write_addtid_b32 v214 offset:12576\n\t"
"v_cvt_pk_bf16_f32 v213, v221, v213\n\t"
"v_mfma_f32_16x16x32_bf16 v[128:131], a[140:143], a[160:163], v[128:131]\n\t"
"ds_write_addtid_b32 v213 offset:14368\n\t"
"buffer_load_dwordx4 a[96:99], v252, s[8:11], s46 offen offset:0 sc1\n\t"
"buffer_load_dwordx4 a[100:103], v252, s[8:11], s46 offen offset:1024 sc1\n\t"
"v_mfma_f32_16x16x32_bf16 v[132:135], a[136:139], a[148:151], v[132:135]\n\t"
"s_mov_b32 s82, 278528\n\t"
"ds_read_b128 a[152:155], v224 offset:19520\n\t"
"ds_read_b128 a[172:175], v224 offset:24128\n\t"
"v_mfma_f32_16x16x32_bf16 v[132:135], a[140:143], a[164:167], v[132:135]\n\t"
"buffer_load_dwordx4 a[104:107], v252, s[8:11], s46 offen offset:2048 sc1\n\t"
"buffer_load_dwordx4 a[108:111], v252, s[8:11], s46 offen offset:3072 sc1\n\t"
"v_mfma_f32_16x16x32_bf16 v[116:119], a[128:131], a[148:151], v[116:119]\n\t"
"s_mov_b32 s77, 237568\n\t"
"ds_read_b128 a[168:171], v224 offset:23616\n\t"
"ds_read_b128 a[156:159], v224 offset:20032\n\t"
"v_mfma_f32_16x16x32_bf16 v[116:119], a[132:135], a[164:167], v[116:119]\n\t"
"buffer_load_dwordx4 a[112:115], v235, s[8:11], s47 offen offset:0 sc1\n\t"
"buffer_load_dwordx4 a[116:119], v235, s[8:11], s47 offen offset:1024 sc1\n\t"
"v_mfma_f32_16x16x32_bf16 v[100:103], a[120:123], a[148:151], v[100:103]\n\t"
"v_cvt_pk_bf16_f32 v220, v198, v218\n\t"
"v_cvt_pk_bf16_f32 v221, v199, v219\n\t"
"v_mfma_f32_16x16x32_bf16 v[100:103], a[124:127], a[164:167], v[100:103]\n\t"
"v_add_u32 v246, 8, v228\n\t"
"v_add_u32 v247, 16, v228\n\t"
"s_waitcnt lgkmcnt(0)\n\t"
"s_barrier\n\t"
"Lunit_%=:\n\t"
"v_mfma_f32_16x16x32_bf16 v[104:107], a[120:123], a[152:155], v[104:107]\n\t"
"ds_read_b128 a[144:147], v224 offset:2048\n\t"
"v_mfma_f32_16x16x32_bf16 v[104:107], a[124:127], a[168:171], v[104:107]\n\t"
"ds_read_b128 a[148:151], v224 offset:2560\n\t"
"v_mfma_f32_16x16x32_bf16 v[120:123], a[128:131], a[152:155], v[120:123]\n\t"
"s_nop 0\n\t"
"ds_read_b128 a[160:163], v224 offset:6144\n\t"
"ds_read_b128 a[164:167], v224 offset:6656\n\t"
"v_mfma_f32_16x16x32_bf16 v[120:123], a[132:135], a[168:171], v[120:123]\n\t"
"ds_write_addtid_b32 v201 offset:22848\n\t"
"v_mfma_f32_16x16x32_bf16 v[136:139], a[136:139], a[152:155], v[136:139]\n\t"
"ds_write_addtid_b32 v203 offset:22592\n\t"
"v_mfma_f32_16x16x32_bf16 v[136:139], a[140:143], a[168:171], v[136:139]\n\t"
"ds_read_b128 a[152:155], v224 offset:3072\n\t"
"v_mfma_f32_16x16x32_bf16 v[140:143], a[136:139], a[156:159], v[140:143]\n\t"
"ds_read_b128 a[168:171], v224 offset:7168\n\t"
"v_mfma_f32_16x16x32_bf16 v[140:143], a[140:143], a[172:175], v[140:143]\n\t"
"buffer_load_dwordx4 a[136:139], v235, s[8:11], s46 offen offset:2048 sc1\n\t"
"v_mfma_f32_16x16x32_bf16 v[124:127], a[128:131], a[156:159], v[124:127]\n\t"
"ds_write_addtid_b32 v194 offset:16448\n\t"
"v_mfma_f32_16x16x32_bf16 v[124:127], a[132:135], a[172:175], v[124:127]\n\t"
"buffer_load_dwordx4 a[140:143], v235, s[8:11], s46 offen offset:3072 sc1\n\t"
"v_mfma_f32_16x16x32_bf16 v[108:111], a[120:123], a[156:159], v[108:111]\n\t"
"ds_read_b128 a[156:159], v224 offset:3584\n\t"
"v_mfma_f32_16x16x32_bf16 v[108:111], a[124:127], a[172:175], v[108:111]\n\t"
"s_waitcnt vmcnt(36) lgkmcnt(7)\n\t"
"buffer_load_dwordx4 a[132:135], v235, s[8:11], s46 offen offset:1024 sc1\n\t"
"v_mfma_f32_16x16x32_bf16 v[48:51], a[0:3], a[144:147], v[0:3]\n\t"
"ds_read_b128 a[172:175], v224 offset:7680\n\t"
"v_mfma_f32_16x16x32_bf16 v[48:51], a[4:7], a[160:163], v[48:51]\n\t"
"s_waitcnt vmcnt(35) lgkmcnt(6)\n\t"
"buffer_load_dwordx4 a[128:131], v235, s[8:11], s46 offen offset:0 sc1\n\t"
"v_mfma_f32_16x16x32_bf16 v[64:67], a[8:11], a[144:147], v[16:19]\n\t"
"ds_write_addtid_b32 v210 offset:24672\n\t"
"v_mfma_f32_16x16x32_bf16 v[64:67], a[12:15], a[160:163], v[64:67]\n\t"
"s_waitcnt vmcnt(34) lgkmcnt(5)\n\t"
"buffer_load_dwordx4 a[120:123], v252, s[8:11], s42 offen offset:2048 sc1\n\t"
"v_mfma_f32_16x16x32_bf16 v[80:83], a[16:19], a[144:147], v[32:35]\n\t"
"ds_write_addtid_b32 v211 offset:26720\n\t"
"v_mfma_f32_16x16x32_bf16 v[80:83], a[20:23], a[160:163], v[80:83]\n\t"
"buffer_load_dwordx4 a[124:127], v252, s[8:11], s42 offen offset:3072 sc1\n\t"
"v_mfma_f32_16x16x32_bf16 v[84:87], a[16:19], a[148:151], v[36:39]\n\t"
"ds_write_addtid_b32 v200 offset:20800\n\t"
"v_mfma_f32_16x16x32_bf16 v[84:87], a[20:23], a[164:167], v[84:87]\n\t"
"ds_read_b128 a[160:163], v224 offset:4096\n\t"
"v_mfma_f32_16x16x32_bf16 v[68:71], a[8:11], a[148:151], v[20:23]\n\t"
"ds_write_addtid_b32 v208 offset:24928\n\t"
"v_mfma_f32_16x16x32_bf16 v[68:71], a[12:15], a[164:167], v[68:71]\n\t"
"ds_read_b128 a[144:147], v224 offset:0\n\t"
"v_mfma_f32_16x16x32_bf16 v[52:55], a[0:3], a[148:151], v[4:7]\n\t"
"ds_write_addtid_b32 v223 offset:30816\n\t"
"v_mfma_f32_16x16x32_bf16 v[52:55], a[4:7], a[164:167], v[52:55]\n\t"
"s_waitcnt lgkmcnt(7)\n\t"
"ds_read_b128 a[148:151], v224 offset:512\n\t"
"v_mfma_f32_16x16x32_bf16 v[56:59], a[0:3], a[152:155], v[8:11]\n\t"
"ds_write_addtid_b32 v195 offset:18496\n\t"
"v_mfma_f32_16x16x32_bf16 v[56:59], a[4:7], a[168:171], v[56:59]\n\t"
"ds_write_addtid_b32 v209 offset:26976\n\t"
"v_mfma_f32_16x16x32_bf16 v[72:75], a[8:11], a[152:155], v[24:27]\n\t"
"ds_read_b128 a[164:167], v224 offset:4608\n\t"
"v_mfma_f32_16x16x32_bf16 v[72:75], a[12:15], a[168:171], v[72:75]\n\t"
"ds_write_addtid_b32 v222 offset:28768\n\t"
"v_mfma_f32_16x16x32_bf16 v[88:91], a[16:19], a[152:155], v[40:43]\n\t"
"ds_write_addtid_b32 v192 offset:16704\n\t"
"v_mfma_f32_16x16x32_bf16 v[88:91], a[20:23], a[168:171], v[88:91]\n\t"
"ds_write_addtid_b32 v202 offset:20544\n\t"
"v_mfma_f32_16x16x32_bf16 v[92:95], a[16:19], a[156:159], v[44:47]\n\t"
"ds_write_addtid_b32 v193 offset:18752\n\t"
"v_mfma_f32_16x16x32_bf16 v[92:95], a[20:23], a[172:175], v[92:95]\n\t"
"buffer_load_dwordx4 a[16:19], v235, s[8:11], s50 offen offset:0 sc1\n\t"
"v_mfma_f32_16x16x32_bf16 v[76:79], a[8:11], a[156:159], v[28:31]\n\t"
"ds_write_addtid_b32 v220 offset:29024\n\t"
"v_mfma_f32_16x16x32_bf16 v[76:79], a[12:15], a[172:175], v[76:79]\n\t"
"buffer_load_dwordx4 a[20:23], v235, s[8:11], s50 offen offset:1024 sc1\n\t"
"v_mfma_f32_16x16x32_bf16 v[60:63], a[0:3], a[156:159], v[12:15]\n\t"
"s_add_u32 m0, s26, 55680\n\t"
"ds_read_b128 a[152:155], v224 offset:1024\n\t"
"v_mfma_f32_16x16x32_bf16 v[60:63], a[4:7], a[172:175], v[60:63]\n\t"
"s_waitcnt vmcnt(36) lgkmcnt(7)\n\t"
"buffer_load_dwordx4 v236, s[12:15], s22 offen offset:256 nt lds\n\t"
"v_mfma_f32_16x16x32_bf16 v[0:3], a[24:27], a[144:147], v[0:3]\n\t"
"ds_read_b128 a[168:171], v224 offset:5120\n\t"
"v_mfma_f32_16x16x32_bf16 v[0:3], a[28:31], a[160:163], v[0:3]\n\t"
"s_waitcnt vmcnt(35) lgkmcnt(6)\n\t"
"buffer_load_dwordx4 v237, s[12:15], s22 offen offset:1280 nt lds\n\t"
"s_lshl_b32 m0, s73, 9\n\t"
"v_mfma_f32_16x16x32_bf16 v[16:19], a[32:35], a[144:147], v[16:19]\n\t"
"ds_write_addtid_b32 v221 offset:31072\n\t"
"s_add_u32 m0, s26, 55680\n\t"
"v_mfma_f32_16x16x32_bf16 v[16:19], a[36:39], a[160:163], v[16:19]\n\t"
"s_waitcnt vmcnt(34) lgkmcnt(5)\n\t"
"buffer_load_dwordx4 v238, s[12:15], s22 offen offset:2304 nt lds\n\t"
"v_mfma_f32_16x16x32_bf16 v[32:35], a[40:43], a[144:147], v[32:35]\n\t"
"ds_read_b128 a[172:175], v224 offset:5632\n\t"
"v_mfma_f32_16x16x32_bf16 v[32:35], a[44:47], a[160:163], v[32:35]\n\t"
"ds_read_b128 a[156:159], v224 offset:1536\n\t"
"v_mfma_f32_16x16x32_bf16 v[36:39], a[40:43], a[148:151], v[36:39]\n\t"
"buffer_load_dwordx4 v239, s[12:15], s22 offen offset:3328 nt lds\n\t"
"v_mfma_f32_16x16x32_bf16 v[36:39], a[44:47], a[164:167], v[36:39]\n\t"
"ds_write_b128 v254, a[248:251] offset:20736\n\t"
"v_mfma_f32_16x16x32_bf16 v[20:23], a[32:35], a[148:151], v[20:23]\n\t"
"v_mfma_f32_16x16x32_bf16 v[20:23], a[36:39], a[164:167], v[20:23]\n\t"
"s_mov_b64 exec, s[36:37]\n\t"
"buffer_load_dwordx4 v240, s[12:15], s22 offen offset:3840 nt lds\n\t"
"s_mov_b64 exec, -1\n\t"
"v_mfma_f32_16x16x32_bf16 v[4:7], a[24:27], a[148:151], v[4:7]\n\t"
"s_lshl_b32 m0, s73, 9\n\t"
"v_mfma_f32_16x16x32_bf16 v[4:7], a[28:31], a[164:167], v[4:7]\n\t"
"s_waitcnt vmcnt(31) lgkmcnt(4)\n\t"
"s_waitcnt lgkmcnt(0)\n\t"
"s_barrier\n\t"
"v_mfma_f32_16x16x32_bf16 v[8:11], a[24:27], a[152:155], v[8:11]\n\t"
"ds_read_b128 a[144:147], v224 offset:18496\n\t"
"v_mfma_f32_16x16x32_bf16 v[8:11], a[28:31], a[168:171], v[8:11]\n\t"
"ds_read_b128 a[160:163], v224 offset:22592\n\t"
"v_mfma_f32_16x16x32_bf16 v[24:27], a[32:35], a[152:155], v[24:27]\n\t"
"s_add_u32 s24, s24, 3145728\n\t"
"ds_read_b128 a[148:151], v224 offset:19008\n\t"
"ds_read_b64_tr_b16 a[176:177], v248 offset:0\n\t"
"v_mfma_f32_16x16x32_bf16 v[24:27], a[36:39], a[168:171], v[24:27]\n\t"
"s_add_u32 s20, s20, 3145728\n\t"
"ds_read_b128 a[164:167], v224 offset:23104\n\t"
"ds_read_b64_tr_b16 a[178:179], v249 offset:0\n\t"
"v_mfma_f32_16x16x32_bf16 v[40:43], a[40:43], a[152:155], v[40:43]\n\t"
"s_add_u32 s27, s27, 3145728\n\t"
"ds_read_b64_tr_b16 a[180:181], v248 offset:512\n\t"
"ds_read_b64_tr_b16 a[182:183], v249 offset:512\n\t"
"v_mfma_f32_16x16x32_bf16 v[40:43], a[44:47], a[168:171], v[40:43]\n\t"
"ds_read_b64_tr_b16 a[184:185], v250 offset:2304\n\t"
"v_mfma_f32_16x16x32_bf16 v[44:47], a[40:43], a[156:159], v[44:47]\n\t"
"ds_read_b128 a[168:171], v224 offset:23616\n\t"
"v_mfma_f32_16x16x32_bf16 v[44:47], a[44:47], a[172:175], v[44:47]\n\t"
"v_mfma_f32_16x16x32_bf16 v[28:31], a[32:35], a[156:159], v[28:31]\n\t"
"v_mfma_f32_16x16x32_bf16 v[28:31], a[36:39], a[172:175], v[28:31]\n\t"
"s_waitcnt lgkmcnt(2)\n\t"
"ds_read_b64_tr_b16 a[186:187], v251 offset:2304\n\t"
"v_mfma_f32_4x4x4_16b_bf16 v[192:195], a[210:211], a[176:177], 0\n\t"
"ds_read_b64_tr_b16 a[188:189], v250 offset:2816\n\t"
"v_mfma_f32_4x4x4_16b_bf16 v[196:199], a[210:211], a[178:179], 0\n\t"
"ds_read_b64_tr_b16 a[190:191], v251 offset:2816\n\t"
"v_mfma_f32_4x4x4_16b_bf16 v[200:203], a[210:211], a[180:181], 0\n\t"
"ds_read_b64_tr_b16 a[192:193], v250 offset:0\n\t"
"v_mfma_f32_4x4x4_16b_bf16 v[204:207], a[210:211], a[182:183], 0\n\t"
"ds_read_b128 a[152:155], v224 offset:19520\n\t"
"v_mfma_f32_16x16x32_bf16 v[12:15], a[24:27], a[156:159], v[12:15]\n\t"
"ds_read_b64_tr_b16 a[194:195], v251 offset:0\n\t"
"s_nop 0\n\t"
"v_cvt_pk_bf16_f32 v194, v194, v198\n\t"
"v_mfma_f32_16x16x32_bf16 v[12:15], a[28:31], a[172:175], v[12:15]\n\t"
"s_waitcnt vmcnt(29) lgkmcnt(7)\n\t"
"buffer_load_dwordx4 a[12:15], v252, s[8:11], s49 offen offset:3072 sc1\n\t"
"v_mfma_f32_16x16x32_bf16 v[144:147], a[48:51], a[144:147], v[96:99]\n\t"
"ds_read_b64_tr_b16 a[196:197], v250 offset:512\n\t"
"v_cvt_pk_bf16_f32 v197, v193, v197\n\t"
"v_mfma_f32_16x16x32_bf16 v[144:147], a[52:55], a[160:163], v[144:147]\n\t"
"s_waitcnt vmcnt(28) lgkmcnt(7)\n\t"
"ds_read_b64_tr_b16 a[198:199], v251 offset:512\n\t"
"v_mfma_f32_16x16x32_bf16 v[160:163], a[56:59], a[144:147], v[112:115]\n\t"
"v_cvt_pk_bf16_f32 v198, v200, v204\n\t"
"v_cvt_pk_bf16_f32 v195, v195, v199\n\t"
"v_mfma_f32_16x16x32_bf16 v[160:163], a[60:63], a[160:163], v[160:163]\n\t"
"s_waitcnt vmcnt(26) lgkmcnt(7)\n\t"
"ds_write_addtid_b32 v197 offset:2048\n\t"
"v_mfma_f32_16x16x32_bf16 v[176:179], a[64:67], a[144:147], v[128:131]\n\t"
"ds_write_addtid_b32 v198 offset:256\n\t"
"v_mfma_f32_16x16x32_bf16 v[176:179], a[68:71], a[160:163], v[176:179]\n\t"
"s_waitcnt lgkmcnt(7)\n\t"
"ds_read_b128 a[156:159], v224 offset:20032\n\t"
"v_cvt_pk_bf16_f32 v196, v192, v196\n\t"
"v_mfma_f32_4x4x4_16b_bf16 v[208:211], a[210:211], a[184:185], 0\n\t"
"ds_read_b64_tr_b16 a[200:201], v248 offset:2304\n\t"
"v_mfma_f32_4x4x4_16b_bf16 v[212:215], a[210:211], a[186:187], 0\n\t"
"ds_write_addtid_b32 v196 offset:0\n\t"
"v_mfma_f32_4x4x4_16b_bf16 v[216:219], a[210:211], a[188:189], 0\n\t"
"v_mfma_f32_4x4x4_16b_bf16 v[220:223], a[210:211], a[190:191], 0\n\t"
"v_cvt_pk_bf16_f32 v192, v202, v206\n\t"
"v_mfma_f32_16x16x32_bf16 v[180:183], a[64:67], a[148:151], v[132:135]\n\t"
"ds_read_b128 a[172:175], v224 offset:24128\n\t"
"s_nop 1\n\t"
"v_cvt_pk_bf16_f32 v212, v208, v212\n\t"
"v_mfma_f32_16x16x32_bf16 v[180:183], a[68:71], a[164:167], v[180:183]\n\t"
"ds_read_b64_tr_b16 a[202:203], v249 offset:2304\n\t"
"v_cvt_pk_bf16_f32 v199, v201, v205\n\t"
"v_mfma_f32_16x16x32_bf16 v[164:167], a[56:59], a[148:151], v[116:119]\n\t"
"ds_write_addtid_b32 v212 offset:8224\n\t"
"v_cvt_pk_bf16_f32 v213, v209, v213\n\t"
"v_mfma_f32_16x16x32_bf16 v[164:167], a[60:63], a[164:167], v[164:167]\n\t"
"v_cvt_pk_bf16_f32 v193, v203, v207\n\t"
"v_cvt_pk_bf16_f32 v211, v211, v215\n\t"
"v_mfma_f32_16x16x32_bf16 v[148:151], a[48:51], a[148:151], v[100:103]\n\t"
"ds_write_addtid_b32 v199 offset:2304\n\t"
"v_cvt_pk_bf16_f32 v210, v210, v214\n\t"
"v_mfma_f32_16x16x32_bf16 v[148:151], a[52:55], a[164:167], v[148:151]\n\t"
"s_waitcnt lgkmcnt(6)\n\t"
"ds_read_b128 a[160:163], v224 offset:20544\n\t"
"v_cvt_pk_bf16_f32 v215, v217, v221\n\t"
"v_mfma_f32_16x16x32_bf16 v[152:155], a[48:51], a[152:155], v[104:107]\n\t"
"ds_read_b128 a[144:147], v224 offset:16448\n\t"
"v_cvt_pk_bf16_f32 v214, v216, v220\n\t"
"v_mfma_f32_16x16x32_bf16 v[152:155], a[52:55], a[168:171], v[152:155]\n\t"
"ds_read_b128 a[164:167], v224 offset:21056\n\t"
"v_mfma_f32_4x4x4_16b_bf16 v[200:203], a[210:211], a[192:193], 0\n\t"
"v_mfma_f32_4x4x4_16b_bf16 v[204:207], a[210:211], a[194:195], 0\n\t"
"v_cvt_pk_bf16_f32 v208, v218, v222\n\t"
"v_mfma_f32_4x4x4_16b_bf16 v[196:199], a[210:211], a[196:197], 0\n\t"
"v_cvt_pk_bf16_f32 v209, v219, v223\n\t"
"v_mfma_f32_4x4x4_16b_bf16 v[216:219], a[210:211], a[198:199], 0\n\t"
"ds_read_b64_tr_b16 a[204:205], v248 offset:2816\n\t"
"v_mfma_f32_16x16x32_bf16 v[168:171], a[56:59], a[152:155], v[120:123]\n\t"
"ds_write_addtid_b32 v215 offset:10528\n\t"
"s_nop 0\n\t"
"v_cvt_pk_bf16_f32 v203, v203, v207\n\t"
"v_mfma_f32_16x16x32_bf16 v[168:171], a[60:63], a[168:171], v[168:171]\n\t"
"ds_read_b64_tr_b16 a[206:207], v249 offset:2816\n\t"
"v_cvt_pk_bf16_f32 v204, v200, v204\n\t"
"v_mfma_f32_16x16x32_bf16 v[184:187], a[64:67], a[152:155], v[136:139]\n\t"
"ds_read_b128 a[148:151], v224 offset:16960\n\t"
"v_cvt_pk_bf16_f32 v202, v202, v206\n\t"
"v_mfma_f32_16x16x32_bf16 v[184:187], a[68:71], a[168:171], v[184:187]\n\t"
"s_waitcnt lgkmcnt(6)\n\t"
"ds_write_addtid_b32 v204 offset:4096\n\t"
"v_cvt_pk_bf16_f32 v205, v201, v205\n\t"
"v_mfma_f32_16x16x32_bf16 v[188:191], a[64:67], a[156:159], v[140:143]\n\t"
"v_cvt_pk_bf16_f32 v201, v199, v219\n\t"
"v_cvt_pk_bf16_f32 v207, v197, v217\n\t"
"v_mfma_f32_16x16x32_bf16 v[188:191], a[68:71], a[172:175], v[188:191]\n\t"
"ds_write_addtid_b32 v213 offset:10272\n\t"
"v_cvt_pk_bf16_f32 v200, v198, v218\n\t"
"v_mfma_f32_16x16x32_bf16 v[172:175], a[56:59], a[156:159], v[124:127]\n\t"
"ds_write_addtid_b32 v207 offset:6400\n\t"
"v_cvt_pk_bf16_f32 v206, v196, v216\n\t"
"v_mfma_f32_16x16x32_bf16 v[172:175], a[60:63], a[172:175], v[172:175]\n\t"
"s_waitcnt lgkmcnt(4)\n\t"
"ds_write_addtid_b32 v214 offset:8480\n\t"
"v_mfma_f32_4x4x4_16b_bf16 v[220:223], a[210:211], a[200:201], 0\n\t"
"ds_write_addtid_b32 v205 offset:6144\n\t"
"v_mfma_f32_4x4x4_16b_bf16 v[212:215], a[210:211], a[202:203], 0\n\t"
"ds_write_addtid_b32 v206 offset:4352\n\t"
"v_mfma_f32_4x4x4_16b_bf16 v[196:199], a[210:211], a[204:205], 0\n\t"
"v_mfma_f32_4x4x4_16b_bf16 v[216:219], a[210:211], a[206:207], 0\n\t"
"v_mfma_f32_16x16x32_bf16 v[156:159], a[48:51], a[156:159], v[108:111]\n\t"
"ds_read_b128 a[152:155], v224 offset:17472\n\t"
"s_nop 2\n\t"
"v_cvt_pk_bf16_f32 v222, v222, v214\n\t"
"v_mfma_f32_16x16x32_bf16 v[156:159], a[52:55], a[172:175], v[156:159]\n\t"
"s_waitcnt vmcnt(24) lgkmcnt(6)\n\t"
"ds_read_b128 a[168:171], v224 offset:21568\n\t"
"v_cvt_pk_bf16_f32 v223, v223, v215\n\t"
"v_mfma_f32_16x16x32_bf16 v[96:99], a[72:75], a[144:147], v[96:99]\n\t"
"v_cvt_pk_bf16_f32 v212, v220, v212\n\t"
"v_cvt_pk_bf16_f32 v213, v221, v213\n\t"
"v_mfma_f32_16x16x32_bf16 v[96:99], a[76:79], a[160:163], v[96:99]\n\t"
"s_waitcnt vmcnt(22) lgkmcnt(6)\n\t"
"buffer_load_dwordx4 a[8:11], v252, s[8:11], s49 offen offset:2048 sc1\n\t"
"v_mfma_f32_16x16x32_bf16 v[112:115], a[80:83], a[144:147], v[112:115]\n\t"
"ds_write_addtid_b32 v212 offset:12320\n\t"
"v_cvt_pk_bf16_f32 v215, v197, v217\n\t"
"v_mfma_f32_16x16x32_bf16 v[112:115], a[84:87], a[160:163], v[112:115]\n\t"
"s_waitcnt vmcnt(21) lgkmcnt(5)\n\t"
"ds_write_addtid_b32 v213 offset:14368\n\t"
"v_cvt_pk_bf16_f32 v214, v196, v216\n\t"
"v_mfma_f32_16x16x32_bf16 v[128:131], a[88:91], a[144:147], v[128:131]\n\t"
"s_add_u32 s25, s25, 3145728\n\t"
"ds_write_addtid_b32 v214 offset:12576\n\t"
"ds_write_addtid_b32 v215 offset:14624\n\t"
"v_mfma_f32_16x16x32_bf16 v[128:131], a[92:95], a[160:163], v[128:131]\n\t"
"buffer_load_dwordx4 a[0:3], v252, s[8:11], s49 offen offset:0 sc1\n\t"
"v_mfma_f32_16x16x32_bf16 v[132:135], a[88:91], a[148:151], v[132:135]\n\t"
"ds_read_b128 a[172:175], v224 offset:22080\n\t"
"v_cvt_pk_bf16_f32 v221, v199, v219\n\t"
"v_mfma_f32_16x16x32_bf16 v[132:135], a[92:95], a[164:167], v[132:135]\n\t"
"v_mfma_f32_16x16x32_bf16 v[116:119], a[80:83], a[148:151], v[116:119]\n\t"
"ds_read_b128 a[156:159], v224 offset:17984\n\t"
"v_mfma_f32_16x16x32_bf16 v[116:119], a[84:87], a[164:167], v[116:119]\n\t"
"buffer_load_dwordx4 a[4:7], v252, s[8:11], s49 offen offset:1024 sc1\n\t"
"v_mfma_f32_16x16x32_bf16 v[100:103], a[72:75], a[148:151], v[100:103]\n\t"
"v_cvt_pk_bf16_f32 v220, v198, v218\n\t"
"v_mfma_f32_16x16x32_bf16 v[100:103], a[76:79], a[164:167], v[100:103]\n\t"
"s_waitcnt lgkmcnt(0)\n\t"
"s_barrier\n\t"
"v_mfma_f32_16x16x32_bf16 v[104:107], a[72:75], a[152:155], v[104:107]\n\t"
"s_nop 0\n\t"
"ds_read_b128 a[144:147], v224 offset:2048\n\t"
"ds_read_b128 a[148:151], v224 offset:2560\n\t"
"v_mfma_f32_16x16x32_bf16 v[104:107], a[76:79], a[168:171], v[104:107]\n\t"
"buffer_load_dwordx4 a[40:43], v235, s[8:11], s49 offen offset:2048 sc1\n\t"
"v_mfma_f32_16x16x32_bf16 v[120:123], a[80:83], a[152:155], v[120:123]\n\t"
"ds_read_b128 a[160:163], v224 offset:6144\n\t"
"v_mfma_f32_16x16x32_bf16 v[120:123], a[84:87], a[168:171], v[120:123]\n\t"
"ds_read_b128 a[164:167], v224 offset:6656\n\t"
"v_mfma_f32_16x16x32_bf16 v[136:139], a[88:91], a[152:155], v[136:139]\n\t"
"ds_write_addtid_b32 v221 offset:31072\n\t"
"v_accvgpr_read_b32 v248, a208\n\t"
"v_mfma_f32_16x16x32_bf16 v[136:139], a[92:95], a[168:171], v[136:139]\n\t"
"buffer_load_dwordx4 a[44:47], v235, s[8:11], s49 offen offset:3072 sc1\n\t"
"v_mfma_f32_16x16x32_bf16 v[140:143], a[88:91], a[156:159], v[140:143]\n\t"
"ds_read_b128 a[152:155], v224 offset:3072\n\t"
"v_xor_b32 v226, 64, v226\n\t"
"v_mfma_f32_16x16x32_bf16 v[140:143], a[92:95], a[172:175], v[140:143]\n\t"
"buffer_load_dwordx4 a[36:39], v235, s[8:11], s49 offen offset:1024 sc1\n\t"
"v_mfma_f32_16x16x32_bf16 v[124:127], a[80:83], a[156:159], v[124:127]\n\t"
"ds_read_b128 a[168:171], v224 offset:7168\n\t"
"v_mfma_f32_16x16x32_bf16 v[124:127], a[84:87], a[172:175], v[124:127]\n\t"
"buffer_load_dwordx4 a[32:35], v235, s[8:11], s49 offen offset:0 sc1\n\t"
"v_mfma_f32_16x16x32_bf16 v[108:111], a[72:75], a[156:159], v[108:111]\n\t"
"ds_read_b128 a[156:159], v224 offset:3584\n\t"
"v_mfma_f32_16x16x32_bf16 v[108:111], a[76:79], a[172:175], v[108:111]\n\t"
"s_waitcnt vmcnt(22) lgkmcnt(5)\n\t"
"ds_write_addtid_b32 v209 offset:26976\n\t"
"v_add_u32 v248, 32896, v248\n\t"
"v_mfma_f32_16x16x32_bf16 v[48:51], a[96:99], a[144:147], v[48:51]\n\t"
"ds_write_addtid_b32 v195 offset:18496\n\t"
"v_mfma_f32_16x16x32_bf16 v[48:51], a[100:103], a[160:163], v[48:51]\n\t"
"buffer_load_dwordx4 a[24:27], v252, s[8:11], s48 offen offset:2048 sc1\n\t"
"v_mfma_f32_16x16x32_bf16 v[64:67], a[104:107], a[144:147], v[64:67]\n\t"
"ds_read_b128 a[172:175], v224 offset:7680\n\t"
"v_mfma_f32_16x16x32_bf16 v[64:67], a[108:111], a[160:163], v[64:67]\n\t"
"s_waitcnt vmcnt(22) lgkmcnt(6)\n\t"
"ds_write_addtid_b32 v211 offset:26720\n\t"
"v_xor_b32 v227, 64, v227\n\t"
"v_mfma_f32_16x16x32_bf16 v[80:83], a[112:115], a[144:147], v[80:83]\n\t"
"ds_read_b128 a[144:147], v224 offset:0\n\t"
"v_mfma_f32_16x16x32_bf16 v[80:83], a[116:119], a[160:163], v[80:83]\n\t"
"buffer_load_dwordx4 a[28:31], v252, s[8:11], s48 offen offset:3072 sc1\n\t"
"v_mfma_f32_16x16x32_bf16 v[84:87], a[112:115], a[148:151], v[84:87]\n\t"
"ds_write_addtid_b32 v210 offset:24672\n\t"
"v_mfma_f32_16x16x32_bf16 v[84:87], a[116:119], a[164:167], v[84:87]\n\t"
"ds_write_addtid_b32 v208 offset:24928\n\t"
"v_mfma_f32_16x16x32_bf16 v[68:71], a[104:107], a[148:151], v[68:71]\n\t"
"ds_read_b128 a[160:163], v224 offset:4096\n\t"
"v_mfma_f32_16x16x32_bf16 v[68:71], a[108:111], a[164:167], v[68:71]\n\t"
"buffer_load_dwordx4 a[64:67], v235, s[8:11], s77 offen offset:0 sc1\n\t"
"v_mfma_f32_16x16x32_bf16 v[52:55], a[96:99], a[148:151], v[52:55]\n\t"
"ds_read_b128 a[148:151], v224 offset:512\n\t"
"v_mfma_f32_16x16x32_bf16 v[52:55], a[100:103], a[164:167], v[52:55]\n\t"
"s_waitcnt lgkmcnt(7)\n\t"
"ds_write_addtid_b32 v220 offset:29024\n\t"
"v_mfma_f32_16x16x32_bf16 v[56:59], a[96:99], a[152:155], v[56:59]\n\t"
"ds_read_b128 a[164:167], v224 offset:4608\n\t"
"v_mfma_f32_16x16x32_bf16 v[56:59], a[100:103], a[168:171], v[56:59]\n\t"
"buffer_load_dwordx4 a[68:71], v235, s[8:11], s77 offen offset:1024 sc1\n\t"
"v_mfma_f32_16x16x32_bf16 v[72:75], a[104:107], a[152:155], v[72:75]\n\t"
"ds_write_addtid_b32 v201 offset:22848\n\t"
"v_mfma_f32_16x16x32_bf16 v[72:75], a[108:111], a[168:171], v[72:75]\n\t"
"v_mfma_f32_16x16x32_bf16 v[88:91], a[112:115], a[152:155], v[88:91]\n\t"
"ds_write_addtid_b32 v222 offset:28768\n\t"
"v_mfma_f32_16x16x32_bf16 v[88:91], a[116:119], a[168:171], v[88:91]\n\t"
"s_waitcnt lgkmcnt(6)\n\t"
"buffer_load_dwordx4 a[60:63], v252, s[8:11], s76 offen offset:3072 sc1\n\t"
"v_mfma_f32_16x16x32_bf16 v[92:95], a[112:115], a[156:159], v[92:95]\n\t"
"ds_write_addtid_b32 v193 offset:18752\n\t"
"v_mfma_f32_16x16x32_bf16 v[92:95], a[116:119], a[172:175], v[92:95]\n\t"
"buffer_load_dwordx4 a[56:59], v252, s[8:11], s76 offen offset:2048 sc1\n\t"
"v_mfma_f32_16x16x32_bf16 v[76:79], a[104:107], a[156:159], v[76:79]\n\t"
"ds_write_addtid_b32 v202 offset:20544\n\t"
"v_mfma_f32_16x16x32_bf16 v[76:79], a[108:111], a[172:175], v[76:79]\n\t"
"ds_write_addtid_b32 v223 offset:30816\n\t"
"v_mfma_f32_16x16x32_bf16 v[60:63], a[96:99], a[156:159], v[60:63]\n\t"
"ds_write_addtid_b32 v192 offset:16704\n\t"
"v_mfma_f32_16x16x32_bf16 v[60:63], a[100:103], a[172:175], v[60:63]\n\t"
"s_waitcnt vmcnt(21) lgkmcnt(6)\n\t"
"buffer_load_dwordx4 a[48:51], v252, s[8:11], s76 offen offset:0 sc1\n\t"
"v_mfma_f32_16x16x32_bf16 v[0:3], a[120:123], a[144:147], v[0:3]\n\t"
"ds_read_b128 a[156:159], v224 offset:1536\n\t"
"v_mfma_f32_16x16x32_bf16 v[0:3], a[124:127], a[160:163], v[0:3]\n\t"
"s_add_u32 s22, s22, 3145728\n\t"
"ds_write_addtid_b32 v203 offset:22592\n\t"
"ds_write_addtid_b32 v200 offset:20800\n\t"
"v_mfma_f32_16x16x32_bf16 v[16:19], a[128:131], a[144:147], v[16:19]\n\t"
"ds_read_b128 a[152:155], v224 offset:1024\n\t"
"v_xor_b32 v249, 32, v248\n\t"
"v_mfma_f32_16x16x32_bf16 v[16:19], a[132:135], a[160:163], v[16:19]\n\t"
"buffer_load_dwordx4 a[52:55], v252, s[8:11], s76 offen offset:1024 sc1\n\t"
"v_mfma_f32_16x16x32_bf16 v[32:35], a[136:139], a[144:147], v[32:35]\n\t"
"ds_write_addtid_b32 v194 offset:16448\n\t"
"v_mfma_f32_16x16x32_bf16 v[32:35], a[140:143], a[160:163], v[32:35]\n\t"
"s_cmp_eq_u32 s21, 1\n\t"
"s_cselect_b32 s22, 2.0, s22\n\t"
"ds_read_b128 a[172:175], v224 offset:5632\n\t"
"v_mfma_f32_16x16x32_bf16 v[36:39], a[136:139], a[148:151], v[36:39]\n\t"
"ds_read_b128 a[248:251], v254 offset:18432\n\t"
"v_xor_b32 v250, 64, v248\n\t"
"v_mfma_f32_16x16x32_bf16 v[36:39], a[140:143], a[164:167], v[36:39]\n\t"
"buffer_load_dwordx4 a[88:91], v235, s[8:11], s76 offen offset:2048 sc1\n\t"
"v_mfma_f32_16x16x32_bf16 v[20:23], a[128:131], a[148:151], v[20:23]\n\t"
"ds_read_b128 a[168:171], v224 offset:5120\n\t"
"v_xor_b32 v251, 96, v248\n\t"
"v_mfma_f32_16x16x32_bf16 v[20:23], a[132:135], a[164:167], v[20:23]\n\t"
"v_mfma_f32_16x16x32_bf16 v[4:7], a[120:123], a[148:151], v[4:7]\n\t"
"buffer_load_dwordx4 a[92:95], v235, s[8:11], s76 offen offset:3072 sc1\n\t"
"v_mfma_f32_16x16x32_bf16 v[4:7], a[124:127], a[164:167], v[4:7]\n\t"
"s_waitcnt vmcnt(18) lgkmcnt(5)\n\t"
"s_waitcnt lgkmcnt(0)\n\t"
"s_barrier\n\t"
"v_mfma_f32_16x16x32_bf16 v[8:11], a[120:123], a[152:155], v[8:11]\n\t"
"s_nop 0\n\t"
"ds_read_b128 a[144:147], v224 offset:18496\n\t"
"ds_read_b128 a[148:151], v224 offset:19008\n\t"
"v_mfma_f32_16x16x32_bf16 v[8:11], a[124:127], a[168:171], v[8:11]\n\t"
"buffer_load_dwordx4 a[84:87], v235, s[8:11], s76 offen offset:1024 sc1\n\t"
"v_mfma_f32_16x16x32_bf16 v[24:27], a[128:131], a[152:155], v[24:27]\n\t"
"s_nop 0\n\t"
"ds_read_b128 a[160:163], v224 offset:22592\n\t"
"ds_read_b64_tr_b16 a[176:177], v248 offset:0\n\t"
"v_mfma_f32_16x16x32_bf16 v[24:27], a[132:135], a[168:171], v[24:27]\n\t"
"s_nop 0\n\t"
"ds_read_b128 a[164:167], v224 offset:23104\n\t"
"ds_read_b64_tr_b16 a[178:179], v249 offset:0\n\t"
"v_mfma_f32_16x16x32_bf16 v[40:43], a[136:139], a[152:155], v[40:43]\n\t"
"s_nop 0\n\t"
"ds_read_b64_tr_b16 a[180:181], v248 offset:512\n\t"
"ds_read_b64_tr_b16 a[182:183], v249 offset:512\n\t"
"v_mfma_f32_16x16x32_bf16 v[40:43], a[140:143], a[168:171], v[40:43]\n\t"
"buffer_load_dwordx4 a[80:83], v235, s[8:11], s76 offen offset:0 sc1\n\t"
"v_mfma_f32_16x16x32_bf16 v[44:47], a[136:139], a[156:159], v[44:47]\n\t"
"s_nop 0\n\t"
"ds_read_b128 a[168:171], v224 offset:23616\n\t"
"ds_read_b64_tr_b16 a[184:185], v250 offset:2304\n\t"
"v_mfma_f32_16x16x32_bf16 v[44:47], a[140:143], a[172:175], v[44:47]\n\t"
"buffer_load_dwordx4 a[72:75], v252, s[8:11], s75 offen offset:2048 sc1\n\t"
"v_mfma_f32_16x16x32_bf16 v[28:31], a[128:131], a[156:159], v[28:31]\n\t"
"s_nop 0\n\t"
"ds_read_b128 a[152:155], v224 offset:19520\n\t"
"ds_read_b64_tr_b16 a[186:187], v251 offset:2304\n\t"
"v_mfma_f32_16x16x32_bf16 v[28:31], a[132:135], a[172:175], v[28:31]\n\t"
"s_waitcnt lgkmcnt(4)\n\t"
"ds_read_b64_tr_b16 a[188:189], v250 offset:2816\n\t"
"v_mfma_f32_4x4x4_16b_bf16 v[192:195], a[210:211], a[176:177], 0\n\t"
"ds_read_b64_tr_b16 a[190:191], v251 offset:2816\n\t"
"v_mfma_f32_4x4x4_16b_bf16 v[196:199], a[210:211], a[178:179], 0\n\t"
"ds_read_b64_tr_b16 a[192:193], v250 offset:0\n\t"
"v_mfma_f32_4x4x4_16b_bf16 v[200:203], a[210:211], a[180:181], 0\n\t"
"ds_read_b64_tr_b16 a[194:195], v251 offset:0\n\t"
"v_mfma_f32_4x4x4_16b_bf16 v[204:207], a[210:211], a[182:183], 0\n\t"
"buffer_load_dwordx4 a[76:79], v252, s[8:11], s75 offen offset:3072 sc1\n\t"
"v_mfma_f32_16x16x32_bf16 v[12:15], a[120:123], a[156:159], v[12:15]\n\t"
"ds_read_b64_tr_b16 a[196:197], v250 offset:512\n\t"
"s_nop 0\n\t"
"v_cvt_pk_bf16_f32 v195, v195, v199\n\t"
"v_mfma_f32_16x16x32_bf16 v[12:15], a[124:127], a[172:175], v[12:15]\n\t"
"s_waitcnt vmcnt(18)\n\t"
"ds_read_b64_tr_b16 a[198:199], v251 offset:512\n\t"
"v_cvt_pk_bf16_f32 v197, v193, v197\n\t"
"v_mfma_f32_16x16x32_bf16 v[144:147], a[0:3], a[144:147], v[144:147]\n\t"
"ds_write_addtid_b32 v197 offset:2048\n\t"
"v_cvt_pk_bf16_f32 v194, v194, v198\n\t"
"v_mfma_f32_16x16x32_bf16 v[144:147], a[4:7], a[160:163], v[144:147]\n\t"
"buffer_load_dwordx4 a[112:115], v235, s[8:11], s80 offen offset:0 sc1\n\t"
"v_mfma_f32_16x16x32_bf16 v[160:163], a[8:11], a[144:147], v[160:163]\n\t"
"v_cvt_pk_bf16_f32 v193, v203, v207\n\t"
"v_cvt_pk_bf16_f32 v199, v201, v205\n\t"
"v_mfma_f32_16x16x32_bf16 v[160:163], a[12:15], a[160:163], v[160:163]\n\t"
"buffer_load_dwordx4 a[116:119], v235, s[8:11], s80 offen offset:1024 sc1\n\t"
"v_mfma_f32_16x16x32_bf16 v[176:179], a[16:19], a[144:147], v[176:179]\n\t"
"ds_read_b64_tr_b16 a[200:201], v248 offset:2304\n\t"
"v_cvt_pk_bf16_f32 v198, v200, v204\n\t"
"v_mfma_f32_16x16x32_bf16 v[176:179], a[20:23], a[160:163], v[176:179]\n\t"
"s_waitcnt lgkmcnt(5)\n\t"
"ds_read_b128 a[144:147], v224 offset:16448\n\t"
"v_cvt_pk_bf16_f32 v196, v192, v196\n\t"
"v_mfma_f32_4x4x4_16b_bf16 v[208:211], a[210:211], a[184:185], 0\n\t"
"ds_read_b64_tr_b16 a[202:203], v249 offset:2304\n\t"
"v_mfma_f32_4x4x4_16b_bf16 v[212:215], a[210:211], a[186:187], 0\n\t"
"ds_read_b128 a[172:175], v224 offset:24128\n\t"
"v_mfma_f32_4x4x4_16b_bf16 v[216:219], a[210:211], a[188:189], 0\n\t"
"ds_read_b128 a[156:159], v224 offset:20032\n\t"
"v_mfma_f32_4x4x4_16b_bf16 v[220:223], a[210:211], a[190:191], 0\n\t"
"buffer_load_dwordx4 a[108:111], v252, s[8:11], s79 offen offset:3072 sc1\n\t"
"v_mfma_f32_16x16x32_bf16 v[180:183], a[16:19], a[148:151], v[180:183]\n\t"
"ds_write_addtid_b32 v196 offset:0\n\t"
"s_nop 0\n\t"
"v_cvt_pk_bf16_f32 v212, v208, v212\n\t"
"v_mfma_f32_16x16x32_bf16 v[180:183], a[20:23], a[164:167], v[180:183]\n\t"
"ds_write_addtid_b32 v199 offset:2304\n\t"
"v_cvt_pk_bf16_f32 v213, v209, v213\n\t"
"v_mfma_f32_16x16x32_bf16 v[164:167], a[8:11], a[148:151], v[164:167]\n\t"
"ds_write_addtid_b32 v198 offset:256\n\t"
"v_cvt_pk_bf16_f32 v208, v218, v222\n\t"
"v_mfma_f32_16x16x32_bf16 v[164:167], a[12:15], a[164:167], v[164:167]\n\t"
"buffer_load_dwordx4 a[104:107], v252, s[8:11], s79 offen offset:2048 sc1\n\t"
"v_mfma_f32_16x16x32_bf16 v[148:151], a[0:3], a[148:151], v[148:151]\n\t"
"v_cvt_pk_bf16_f32 v211, v211, v215\n\t"
"v_cvt_pk_bf16_f32 v215, v217, v221\n\t"
"v_mfma_f32_16x16x32_bf16 v[148:151], a[4:7], a[164:167], v[148:151]\n\t"
"ds_read_b128 a[160:163], v224 offset:20544\n\t"
"v_cvt_pk_bf16_f32 v209, v219, v223\n\t"
"v_mfma_f32_16x16x32_bf16 v[152:155], a[0:3], a[152:155], v[152:155]\n\t"
"ds_read_b128 a[148:151], v224 offset:16960\n\t"
"v_cvt_pk_bf16_f32 v192, v202, v206\n\t"
"v_mfma_f32_16x16x32_bf16 v[152:155], a[4:7], a[168:171], v[152:155]\n\t"
"s_waitcnt lgkmcnt(6)\n\t"
"buffer_load_dwordx4 a[96:99], v252, s[8:11], s79 offen offset:0 sc1\n\t"
"v_mfma_f32_4x4x4_16b_bf16 v[200:203], a[210:211], a[192:193], 0\n\t"
"v_cvt_pk_bf16_f32 v210, v210, v214\n\t"
"v_mfma_f32_4x4x4_16b_bf16 v[204:207], a[210:211], a[194:195], 0\n\t"
"v_cvt_pk_bf16_f32 v214, v216, v220\n\t"
"v_mfma_f32_4x4x4_16b_bf16 v[196:199], a[210:211], a[196:197], 0\n\t"
"ds_read_b64_tr_b16 a[204:205], v248 offset:2816\n\t"
"v_mfma_f32_4x4x4_16b_bf16 v[216:219], a[210:211], a[198:199], 0\n\t"
"buffer_load_dwordx4 a[100:103], v252, s[8:11], s79 offen offset:1024 sc1\n\t"
"v_mfma_f32_16x16x32_bf16 v[168:171], a[8:11], a[152:155], v[168:171]\n\t"
"ds_write_addtid_b32 v212 offset:8224\n\t"
"s_nop 0\n\t"
"v_cvt_pk_bf16_f32 v204, v200, v204\n\t"
"v_mfma_f32_16x16x32_bf16 v[168:171], a[12:15], a[168:171], v[168:171]\n\t"
"ds_read_b64_tr_b16 a[206:207], v249 offset:2816\n\t"
"v_cvt_pk_bf16_f32 v205, v201, v205\n\t"
"v_mfma_f32_16x16x32_bf16 v[184:187], a[16:19], a[152:155], v[184:187]\n\t"
"ds_write_addtid_b32 v204 offset:4096\n\t"
"v_cvt_pk_bf16_f32 v202, v202, v206\n\t"
"v_mfma_f32_16x16x32_bf16 v[184:187], a[20:23], a[168:171], v[184:187]\n\t"
"s_waitcnt lgkmcnt(5)\n\t"
"buffer_load_dwordx4 a[136:139], v235, s[8:11], s79 offen offset:2048 sc1\n\t"
"v_mfma_f32_16x16x32_bf16 v[188:191], a[16:19], a[156:159], v[188:191]\n\t"
"ds_read_b128 a[152:155], v224 offset:17472\n\t"
"v_cvt_pk_bf16_f32 v200, v198, v218\n\t"
"v_mfma_f32_16x16x32_bf16 v[188:191], a[20:23], a[172:175], v[188:191]\n\t"
"ds_read_b128 a[164:167], v224 offset:21056\n\t"
"v_cvt_pk_bf16_f32 v203, v203, v207\n\t"
"v_mfma_f32_16x16x32_bf16 v[172:175], a[8:11], a[156:159], v[172:175]\n\t"
"ds_write_addtid_b32 v215 offset:10528\n\t"
"v_cvt_pk_bf16_f32 v207, v197, v217\n\t"
"v_mfma_f32_16x16x32_bf16 v[172:175], a[12:15], a[172:175], v[172:175]\n\t"
"s_waitcnt lgkmcnt(4)\n\t"
"ds_write_addtid_b32 v214 offset:8480\n\t"
"v_cvt_pk_bf16_f32 v206, v196, v216\n\t"
"v_mfma_f32_4x4x4_16b_bf16 v[220:223], a[210:211], a[200:201], 0\n\t"
"ds_write_addtid_b32 v213 offset:10272\n\t"
"v_mfma_f32_4x4x4_16b_bf16 v[212:215], a[210:211], a[202:203], 0\n\t"
"v_cvt_pk_bf16_f32 v201, v199, v219\n\t"
"v_mfma_f32_4x4x4_16b_bf16 v[196:199], a[210:211], a[204:205], 0\n\t"
"ds_write_addtid_b32 v207 offset:6400\n\t"
"v_mfma_f32_4x4x4_16b_bf16 v[216:219], a[210:211], a[206:207], 0\n\t"
"buffer_load_dwordx4 a[140:143], v235, s[8:11], s79 offen offset:3072 sc1\n\t"
"v_mfma_f32_16x16x32_bf16 v[156:159], a[0:3], a[156:159], v[156:159]\n\t"
"ds_read_b128 a[156:159], v224 offset:17984\n\t"
"s_nop 0\n\t"
"v_cvt_pk_bf16_f32 v213, v221, v213\n\t"
"v_mfma_f32_16x16x32_bf16 v[156:159], a[4:7], a[172:175], v[156:159]\n\t"
"s_waitcnt vmcnt(20) lgkmcnt(7)\n\t"
"buffer_load_dwordx4 a[132:135], v235, s[8:11], s79 offen offset:1024 sc1\n\t"
"v_mfma_f32_16x16x32_bf16 v[96:99], a[24:27], a[144:147], v[96:99]\n\t"
"ds_write_addtid_b32 v206 offset:4352\n\t"
"v_cvt_pk_bf16_f32 v223, v223, v215\n\t"
"v_mfma_f32_16x16x32_bf16 v[96:99], a[28:31], a[160:163], v[96:99]\n\t"
"ds_read_b128 a[168:171], v224 offset:21568\n\t"
"v_cvt_pk_bf16_f32 v222, v222, v214\n\t"
"v_mfma_f32_16x16x32_bf16 v[112:115], a[32:35], a[144:147], v[112:115]\n\t"
"ds_write_addtid_b32 v213 offset:14368\n\t"
"v_cvt_pk_bf16_f32 v212, v220, v212\n\t"
"v_mfma_f32_16x16x32_bf16 v[112:115], a[36:39], a[160:163], v[112:115]\n\t"
"ds_write_addtid_b32 v205 offset:6144\n\t"
"v_cvt_pk_bf16_f32 v215, v197, v217\n\t"
"v_mfma_f32_16x16x32_bf16 v[128:131], a[40:43], a[144:147], v[128:131]\n\t"
"ds_write_addtid_b32 v212 offset:12320\n\t"
"v_cvt_pk_bf16_f32 v214, v196, v216\n\t"
"v_mfma_f32_16x16x32_bf16 v[128:131], a[44:47], a[160:163], v[128:131]\n\t"
"s_waitcnt lgkmcnt(7)\n\t"
"ds_write_addtid_b32 v214 offset:12576\n\t"
"ds_write_addtid_b32 v215 offset:14624\n\t"
"v_mfma_f32_16x16x32_bf16 v[132:135], a[40:43], a[148:151], v[132:135]\n\t"
"ds_read_b128 a[172:175], v224 offset:22080\n\t"
"v_mfma_f32_16x16x32_bf16 v[132:135], a[44:47], a[164:167], v[132:135]\n\t"
"buffer_load_dwordx4 a[128:131], v235, s[8:11], s79 offen offset:0 sc1\n\t"
"v_mfma_f32_16x16x32_bf16 v[116:119], a[32:35], a[148:151], v[116:119]\n\t"
"ds_read_b128 a[244:247], v255 offset:41472\n\t"
"v_cvt_pk_bf16_f32 v220, v198, v218\n\t"
"v_mfma_f32_16x16x32_bf16 v[116:119], a[36:39], a[164:167], v[116:119]\n\t"
"buffer_load_dwordx4 a[120:123], v252, s[8:11], s78 offen offset:2048 sc1\n\t"
"v_mfma_f32_16x16x32_bf16 v[100:103], a[24:27], a[148:151], v[100:103]\n\t"
"v_cvt_pk_bf16_f32 v221, v199, v219\n\t"
"v_mfma_f32_16x16x32_bf16 v[100:103], a[28:31], a[164:167], v[100:103]\n\t"
"buffer_load_dwordx4 a[124:127], v252, s[8:11], s78 offen offset:3072 sc1\n\t"
"s_waitcnt lgkmcnt(0)\n\t"
"s_barrier\n\t"
"v_mfma_f32_16x16x32_bf16 v[104:107], a[24:27], a[152:155], v[104:107]\n\t"
"ds_read_b128 a[144:147], v224 offset:2048\n\t"
"v_add_u32 v248, 32896, v225\n\t"
"v_mfma_f32_16x16x32_bf16 v[104:107], a[28:31], a[168:171], v[104:107]\n\t"
"buffer_load_dwordx4 a[16:19], v235, s[8:11], s74 offen offset:0 sc1\n\t"
"v_mfma_f32_16x16x32_bf16 v[120:123], a[32:35], a[152:155], v[120:123]\n\t"
"s_nop 0\n\t"
"ds_read_b128 a[160:163], v224 offset:6144\n\t"
"ds_read_b128 a[148:151], v224 offset:2560\n\t"
"v_mfma_f32_16x16x32_bf16 v[120:123], a[36:39], a[168:171], v[120:123]\n\t"
"ds_read_b128 a[164:167], v224 offset:6656\n\t"
"v_xor_b32 v249, 32, v248\n\t"
"v_mfma_f32_16x16x32_bf16 v[136:139], a[40:43], a[152:155], v[136:139]\n\t"
"ds_write_addtid_b32 v209 offset:26976\n\t"
"v_xor_b32 v250, 64, v248\n\t"
"v_mfma_f32_16x16x32_bf16 v[136:139], a[44:47], a[168:171], v[136:139]\n\t"
"buffer_load_dwordx4 a[20:23], v235, s[8:11], s74 offen offset:1024 sc1\n\t"
"v_mfma_f32_16x16x32_bf16 v[140:143], a[40:43], a[156:159], v[140:143]\n\t"
"ds_write_addtid_b32 v194 offset:16448\n\t"
"v_xor_b32 v251, 96, v248\n\t"
"v_mfma_f32_16x16x32_bf16 v[140:143], a[44:47], a[172:175], v[140:143]\n\t"
"buffer_load_dwordx4 a[12:15], v252, s[8:11], s72 offen offset:3072 sc1\n\t"
"v_mfma_f32_16x16x32_bf16 v[124:127], a[32:35], a[156:159], v[124:127]\n\t"
"ds_read_b128 a[152:155], v224 offset:3072\n\t"
"v_mfma_f32_16x16x32_bf16 v[124:127], a[36:39], a[172:175], v[124:127]\n\t"
"s_nop 0\n\t"
"ds_read_b128 a[168:171], v224 offset:7168\n\t"
"ds_read_b64_tr_b16 a[176:177], v248 offset:0\n\t"
"v_mfma_f32_16x16x32_bf16 v[108:111], a[24:27], a[156:159], v[108:111]\n\t"
"s_nop 0\n\t"
"ds_read_b128 a[156:159], v224 offset:3584\n\t"
"ds_read_b64_tr_b16 a[178:179], v250 offset:2304\n\t"
"v_mfma_f32_16x16x32_bf16 v[108:111], a[28:31], a[172:175], v[108:111]\n\t"
"s_waitcnt vmcnt(21) lgkmcnt(9)\n\t"
"buffer_load_dwordx4 a[8:11], v252, s[8:11], s72 offen offset:2048 sc1\n\t"
"v_mfma_f32_16x16x32_bf16 v[48:51], a[48:51], a[144:147], v[48:51]\n\t"
"s_nop 0\n\t"
"ds_read_b128 a[172:175], v224 offset:7680\n\t"
"ds_write_addtid_b32 v220 offset:29024\n\t"
"v_mfma_f32_16x16x32_bf16 v[48:51], a[52:55], a[160:163], v[48:51]\n\t"
"buffer_load_dwordx4 a[0:3], v252, s[8:11], s72 offen offset:0 sc1\n\t"
"v_mfma_f32_16x16x32_bf16 v[64:67], a[56:59], a[144:147], v[64:67]\n\t"
"ds_write_addtid_b32 v202 offset:20544\n\t"
"v_mfma_f32_16x16x32_bf16 v[64:67], a[60:63], a[160:163], v[64:67]\n\t"
"s_nop 0\n\t"
"ds_write_addtid_b32 v203 offset:22592\n\t"
"ds_write_addtid_b32 v201 offset:22848\n\t"
"v_mfma_f32_16x16x32_bf16 v[80:83], a[64:67], a[144:147], v[80:83]\n\t"
"ds_write_addtid_b32 v221 offset:31072\n\t"
"v_mfma_f32_16x16x32_bf16 v[80:83], a[68:71], a[160:163], v[80:83]\n\t"
"s_waitcnt lgkmcnt(11)\n\t"
"buffer_load_dwordx4 a[4:7], v252, s[8:11], s72 offen offset:1024 sc1\n\t"
"v_mfma_f32_16x16x32_bf16 v[84:87], a[64:67], a[148:151], v[84:87]\n\t"
"ds_read_b128 a[160:163], v224 offset:4096\n\t"
"v_mfma_f32_16x16x32_bf16 v[84:87], a[68:71], a[164:167], v[84:87]\n\t"
"ds_write_addtid_b32 v211 offset:26720\n\t"
"v_mfma_f32_16x16x32_bf16 v[68:71], a[56:59], a[148:151], v[68:71]\n\t"
"s_nop 0\n\t"
"ds_read_b128 a[144:147], v224 offset:0\n\t"
"ds_write_addtid_b32 v192 offset:16704\n\t"
"v_mfma_f32_16x16x32_bf16 v[68:71], a[60:63], a[164:167], v[68:71]\n\t"
"buffer_load_dwordx4 a[40:43], v235, s[8:11], s72 offen offset:2048 sc1\n\t"
"v_mfma_f32_16x16x32_bf16 v[52:55], a[48:51], a[148:151], v[52:55]\n\t"
"ds_read_b128 a[148:151], v224 offset:512\n\t"
"v_mfma_f32_16x16x32_bf16 v[52:55], a[52:55], a[164:167], v[52:55]\n\t"
"s_waitcnt lgkmcnt(9)\n\t"
"ds_read_b64_tr_b16 a[184:185], v249 offset:0\n\t"
"ds_read_b64_tr_b16 a[186:187], v251 offset:2304\n\t"
"v_mfma_f32_16x16x32_bf16 v[56:59], a[48:51], a[152:155], v[56:59]\n\t"
"ds_read_b64_tr_b16 a[180:181], v248 offset:4608\n\t"
"v_mfma_f32_16x16x32_bf16 v[56:59], a[52:55], a[168:171], v[56:59]\n\t"
"buffer_load_dwordx4 a[44:47], v235, s[8:11], s72 offen offset:3072 sc1\n\t"
"v_mfma_f32_16x16x32_bf16 v[72:75], a[56:59], a[152:155], v[72:75]\n\t"
"s_nop 0\n\t"
"ds_read_b128 a[164:167], v224 offset:4608\n\t"
"ds_read_b64_tr_b16 a[188:189], v249 offset:4608\n\t"
"v_mfma_f32_16x16x32_bf16 v[72:75], a[60:63], a[168:171], v[72:75]\n\t"
"ds_read_b64_tr_b16 a[192:193], v250 offset:0\n\t"
"v_mfma_f32_16x16x32_bf16 v[88:91], a[64:67], a[152:155], v[88:91]\n\t"
"ds_write_addtid_b32 v193 offset:18752\n\t"
"v_mfma_f32_16x16x32_bf16 v[88:91], a[68:71], a[168:171], v[88:91]\n\t"
"buffer_load_dwordx4 a[36:39], v235, s[8:11], s72 offen offset:1024 sc1\n\t"
"v_mfma_f32_16x16x32_bf16 v[92:95], a[64:67], a[156:159], v[92:95]\n\t"
"ds_write_addtid_b32 v222 offset:28768\n\t"
"v_mfma_f32_16x16x32_bf16 v[92:95], a[68:71], a[172:175], v[92:95]\n\t"
"ds_write_addtid_b32 v200 offset:20800\n\t"
"v_mfma_f32_16x16x32_bf16 v[76:79], a[56:59], a[156:159], v[76:79]\n\t"
"ds_read_b64_tr_b16 a[194:195], v248 offset:2304\n\t"
"v_mfma_f32_16x16x32_bf16 v[76:79], a[60:63], a[172:175], v[76:79]\n\t"
"buffer_load_dwordx4 a[32:35], v235, s[8:11], s72 offen offset:0 sc1\n\t"
"v_mfma_f32_16x16x32_bf16 v[60:63], a[48:51], a[156:159], v[60:63]\n\t"
"s_nop 0\n\t"
"ds_write_addtid_b32 v223 offset:30816\n\t"
"ds_read_b128 a[240:243], v255 offset:0\n\t"
"v_mfma_f32_16x16x32_bf16 v[60:63], a[52:55], a[172:175], v[60:63]\n\t"
"s_waitcnt vmcnt(22) lgkmcnt(9)\n\t"
"ds_read_b128 a[156:159], v224 offset:1536\n\t"
"v_mfma_f32_16x16x32_bf16 v[0:3], a[72:75], a[144:147], v[0:3]\n\t"
"ds_read_b128 a[172:175], v224 offset:5632\n\t"
"v_mfma_f32_16x16x32_bf16 v[0:3], a[76:79], a[160:163], v[0:3]\n\t"
"buffer_load_dwordx4 a[24:27], v252, s[8:11], s69 offen offset:2048 sc1\n\t"
"v_mfma_f32_16x16x32_bf16 v[16:19], a[80:83], a[144:147], v[16:19]\n\t"
"s_nop 0\n\t"
"ds_write_addtid_b32 v210 offset:24672\n\t"
"ds_write_addtid_b32 v208 offset:24928\n\t"
"v_mfma_f32_16x16x32_bf16 v[16:19], a[84:87], a[160:163], v[16:19]\n\t"
"s_nop 0\n\t"
"ds_read_b64_tr_b16 a[200:201], v251 offset:0\n\t"
"ds_read_b64_tr_b16 a[202:203], v249 offset:2304\n\t"
"v_mfma_f32_16x16x32_bf16 v[32:35], a[88:91], a[144:147], v[32:35]\n\t"
"s_nop 0\n\t"
"ds_read_b128 a[168:171], v224 offset:5120\n\t"
"ds_write_addtid_b32 v195 offset:18496\n\t"
"v_mfma_f32_16x16x32_bf16 v[32:35], a[92:95], a[160:163], v[32:35]\n\t"
"s_waitcnt lgkmcnt(11)\n\t"
"buffer_load_dwordx4 a[28:31], v252, s[8:11], s69 offen offset:3072 sc1\n\t"
"v_mfma_f32_16x16x32_bf16 v[36:39], a[88:91], a[148:151], v[36:39]\n\t"
"ds_read_b64_tr_b16 a[196:197], v250 offset:4608\n\t"
"v_mfma_f32_16x16x32_bf16 v[36:39], a[92:95], a[164:167], v[36:39]\n\t"
"ds_read_b64_tr_b16 a[204:205], v251 offset:4608\n\t"
"v_mfma_f32_16x16x32_bf16 v[20:23], a[80:83], a[148:151], v[20:23]\n\t"
"ds_read_b128 a[152:155], v224 offset:1024\n\t"
"v_mfma_f32_16x16x32_bf16 v[20:23], a[84:87], a[164:167], v[20:23]\n\t"
"buffer_load_dwordx4 a[64:67], v235, s[8:11], s83 offen offset:0 sc1\n\t"
"v_mfma_f32_16x16x32_bf16 v[4:7], a[72:75], a[148:151], v[4:7]\n\t"
"v_mfma_f32_16x16x32_bf16 v[4:7], a[76:79], a[164:167], v[4:7]\n\t"
"buffer_load_dwordx4 a[68:71], v235, s[8:11], s83 offen offset:1024 sc1\n\t"
"s_waitcnt lgkmcnt(0)\n\t"
"s_barrier\n\t"
"v_mfma_f32_16x16x32_bf16 v[8:11], a[72:75], a[152:155], v[8:11]\n\t"
"ds_read_b128 a[144:147], v224 offset:18496\n\t"
"v_mfma_f32_16x16x32_bf16 v[8:11], a[76:79], a[168:171], v[8:11]\n\t"
"buffer_load_dwordx4 a[60:63], v252, s[8:11], s82 offen offset:3072 sc1\n\t"
"v_mfma_f32_16x16x32_bf16 v[24:27], a[80:83], a[152:155], v[24:27]\n\t"
"s_nop 0\n\t"
"ds_read_b128 a[160:163], v224 offset:22592\n\t"
"ds_read_b128 a[148:151], v224 offset:19008\n\t"
"v_mfma_f32_16x16x32_bf16 v[24:27], a[84:87], a[168:171], v[24:27]\n\t"
"ds_read_b128 a[164:167], v224 offset:23104\n\t"
"v_mfma_f32_16x16x32_bf16 v[40:43], a[88:91], a[152:155], v[40:43]\n\t"
"ds_read_b128 a[152:155], v224 offset:19520\n\t"
"v_mfma_f32_16x16x32_bf16 v[40:43], a[92:95], a[168:171], v[40:43]\n\t"
"buffer_load_dwordx4 a[56:59], v252, s[8:11], s82 offen offset:2048 sc1\n\t"
"v_mfma_f32_16x16x32_bf16 v[192:195], a[212:215], a[176:179], 0\n\t"
"v_mfma_f32_16x16x32_bf16 v[44:47], a[88:91], a[156:159], v[44:47]\n\t"
"ds_read_b64_tr_b16 a[176:177], v248 offset:1152\n\t"
"v_mfma_f32_16x16x32_bf16 v[44:47], a[92:95], a[172:175], v[44:47]\n\t"
"buffer_load_dwordx4 a[48:51], v252, s[8:11], s82 offen offset:0 sc1\n\t"
"v_mfma_f32_16x16x32_bf16 v[196:199], a[212:215], a[184:187], 0\n\t"
"ds_read_b128 a[168:171], v224 offset:23616\n\t"
"v_mfma_f32_16x16x32_bf16 v[28:31], a[80:83], a[156:159], v[28:31]\n\t"
"ds_read_b64_tr_b16 a[184:185], v249 offset:1152\n\t"
"v_mfma_f32_16x16x32_bf16 v[28:31], a[84:87], a[172:175], v[28:31]\n\t"
"buffer_load_dwordx4 a[52:55], v252, s[8:11], s82 offen offset:1024 sc1\n\t"
"v_mfma_f32_16x16x32_bf16 v[12:15], a[72:75], a[156:159], v[12:15]\n\t"
"ds_read_b128 a[156:159], v224 offset:20032\n\t"
"v_mfma_f32_16x16x32_bf16 v[12:15], a[76:79], a[172:175], v[12:15]\n\t"
"ds_read_b128 a[172:175], v224 offset:24128\n\t"
"v_cvt_pk_bf16_f32 v194, v194, v198\n\t"
"v_mfma_f32_16x16x32_bf16 v[200:203], a[212:215], a[178:181], 0\n\t"
"s_waitcnt vmcnt(24) lgkmcnt(5)\n\t"
"ds_read_b64_tr_b16 a[178:179], v250 offset:3456\n\t"
"v_mfma_f32_16x16x32_bf16 v[144:147], a[96:99], a[144:147], v[144:147]\n\t"
"ds_read_b64_tr_b16 a[180:181], v248 offset:5760\n\t"
"v_mfma_f32_16x16x32_bf16 v[144:147], a[100:103], a[160:163], v[144:147]\n\t"
"buffer_load_dwordx4 a[88:91], v235, s[8:11], s82 offen offset:2048 sc1\n\t"
"v_mfma_f32_16x16x32_bf16 v[204:207], a[212:215], a[186:189], 0\n\t"
"ds_write_addtid_b32 v194 offset:2048\n\t"
"v_cvt_pk_bf16_f32 v193, v193, v197\n\t"
"v_mfma_f32_16x16x32_bf16 v[160:163], a[104:107], a[144:147], v[160:163]\n\t"
"ds_read_b64_tr_b16 a[186:187], v251 offset:3456\n\t"
"v_mfma_f32_16x16x32_bf16 v[160:163], a[108:111], a[160:163], v[160:163]\n\t"
"buffer_load_dwordx4 a[92:95], v235, s[8:11], s82 offen offset:3072 sc1\n\t"
"v_mfma_f32_16x16x32_bf16 v[176:179], a[112:115], a[144:147], v[176:179]\n\t"
"ds_read_b64_tr_b16 a[188:189], v249 offset:5760\n\t"
"v_cvt_pk_bf16_f32 v195, v195, v199\n\t"
"v_mfma_f32_16x16x32_bf16 v[176:179], a[116:119], a[160:163], v[176:179]\n\t"
"ds_write_addtid_b32 v193 offset:256\n\t"
"v_cvt_pk_bf16_f32 v192, v192, v196\n\t"
"v_mfma_f32_16x16x32_bf16 v[208:211], a[212:215], a[192:195], 0\n\t"
"ds_write_addtid_b32 v195 offset:2304\n\t"
"v_cvt_pk_bf16_f32 v203, v203, v207\n\t"
"v_mfma_f32_16x16x32_bf16 v[180:183], a[112:115], a[148:151], v[180:183]\n\t"
"ds_write_addtid_b32 v192 offset:0\n\t"
"v_mfma_f32_16x16x32_bf16 v[180:183], a[116:119], a[164:167], v[180:183]\n\t"
"buffer_load_dwordx4 a[84:87], v235, s[8:11], s82 offen offset:1024 sc1\n\t"
"v_mfma_f32_16x16x32_bf16 v[164:167], a[104:107], a[148:151], v[164:167]\n\t"
"ds_read_b128 a[160:163], v224 offset:20544\n\t"
"v_cvt_pk_bf16_f32 v201, v201, v205\n\t"
"v_mfma_f32_16x16x32_bf16 v[164:167], a[108:111], a[164:167], v[164:167]\n\t"
"buffer_load_dwordx4 a[80:83], v235, s[8:11], s82 offen offset:0 sc1\n\t"
"v_mfma_f32_16x16x32_bf16 v[212:215], a[212:215], a[200:203], 0\n\t"
"s_nop 0\n\t"
"ds_read_b128 a[144:147], v224 offset:16448\n\t"
"ds_write_addtid_b32 v201 offset:8480\n\t"
"v_mfma_f32_16x16x32_bf16 v[148:151], a[96:99], a[148:151], v[148:151]\n\t"
"ds_read_b128 a[148:151], v224 offset:16960\n\t"
"v_mfma_f32_16x16x32_bf16 v[148:151], a[100:103], a[164:167], v[148:151]\n\t"
"ds_read_b64_tr_b16 a[192:193], v250 offset:1152\n\t"
"v_cvt_pk_bf16_f32 v202, v202, v206\n\t"
"v_mfma_f32_16x16x32_bf16 v[216:219], a[212:215], a[194:197], 0\n\t"
"s_waitcnt lgkmcnt(8)\n\t"
"buffer_load_dwordx4 a[72:75], v252, s[8:11], s81 offen offset:2048 sc1\n\t"
"v_mfma_f32_16x16x32_bf16 v[152:155], a[96:99], a[152:155], v[152:155]\n\t"
"s_nop 0\n\t"
"ds_write_addtid_b32 v202 offset:10272\n\t"
"ds_read_b64_tr_b16 a[200:201], v251 offset:1152\n\t"
"v_mfma_f32_16x16x32_bf16 v[152:155], a[100:103], a[168:171], v[152:155]\n\t"
"ds_read_b64_tr_b16 a[194:195], v248 offset:3456\n\t"
"v_cvt_pk_bf16_f32 v200, v200, v204\n\t"
"v_mfma_f32_16x16x32_bf16 v[168:171], a[104:107], a[152:155], v[168:171]\n\t"
"ds_read_b128 a[164:167], v224 offset:21056\n\t"
"v_cvt_pk_bf16_f32 v210, v210, v214\n\t"
"v_mfma_f32_16x16x32_bf16 v[168:171], a[108:111], a[168:171], v[168:171]\n\t"
"buffer_load_dwordx4 a[76:79], v252, s[8:11], s81 offen offset:3072 sc1\n\t"
"v_mfma_f32_16x16x32_bf16 v[220:223], a[212:215], a[202:205], 0\n\t"
"ds_write_addtid_b32 v200 offset:8224\n\t"
"v_cvt_pk_bf16_f32 v211, v211, v215\n\t"
"v_mfma_f32_16x16x32_bf16 v[184:187], a[112:115], a[152:155], v[184:187]\n\t"
"ds_write_addtid_b32 v203 offset:10528\n\t"
"v_cvt_pk_bf16_f32 v209, v209, v213\n\t"
"v_mfma_f32_16x16x32_bf16 v[184:187], a[116:119], a[168:171], v[184:187]\n\t"
"ds_read_b128 a[168:171], v224 offset:21568\n\t"
"v_cvt_pk_bf16_f32 v208, v208, v212\n\t"
"v_mfma_f32_16x16x32_bf16 v[188:191], a[112:115], a[156:159], v[188:191]\n\t"
"s_nop 0\n\t"
"ds_write_addtid_b32 v208 offset:4096\n\t"
"ds_write_addtid_b32 v210 offset:6144\n\t"
"v_mfma_f32_16x16x32_bf16 v[188:191], a[116:119], a[172:175], v[188:191]\n\t"
"buffer_load_dwordx4 a[112:115], v235, s[8:11], s56 offen offset:0 sc1\n\t"
"v_mfma_f32_16x16x32_bf16 v[172:175], a[104:107], a[156:159], v[172:175]\n\t"
"ds_write_addtid_b32 v211 offset:6400\n\t"
"v_cvt_pk_bf16_f32 v217, v217, v221\n\t"
"v_mfma_f32_16x16x32_bf16 v[172:175], a[108:111], a[172:175], v[172:175]\n\t"
"ds_write_addtid_b32 v217 offset:12576\n\t"
"v_cvt_pk_bf16_f32 v216, v216, v220\n\t"
"v_mfma_f32_16x16x32_bf16 v[156:159], a[96:99], a[156:159], v[156:159]\n\t"
"ds_read_b64_tr_b16 a[196:197], v250 offset:5760\n\t"
"v_cvt_pk_bf16_f32 v219, v219, v223\n\t"
"v_mfma_f32_16x16x32_bf16 v[156:159], a[100:103], a[172:175], v[156:159]\n\t"
"s_waitcnt vmcnt(25) lgkmcnt(8)\n\t"
"buffer_load_dwordx4 a[116:119], v235, s[8:11], s56 offen offset:1024 sc1\n\t"
"v_mfma_f32_16x16x32_bf16 v[96:99], a[120:123], a[144:147], v[96:99]\n\t"
"ds_write_addtid_b32 v209 offset:4352\n\t"
"v_cvt_pk_bf16_f32 v218, v218, v222\n\t"
"v_mfma_f32_16x16x32_bf16 v[96:99], a[124:127], a[160:163], v[96:99]\n\t"
"buffer_load_dwordx4 a[108:111], v252, s[8:11], s55 offen offset:3072 sc1\n\t"
"v_mfma_f32_16x16x32_bf16 v[112:115], a[128:131], a[144:147], v[112:115]\n\t"
"s_nop 0\n\t"
"ds_write_addtid_b32 v216 offset:12320\n\t"
"ds_read_b64_tr_b16 a[202:203], v249 offset:3456\n\t"
"v_mfma_f32_16x16x32_bf16 v[112:115], a[132:135], a[160:163], v[112:115]\n\t"
"buffer_load_dwordx4 a[104:107], v252, s[8:11], s55 offen offset:2048 sc1\n\t"
"v_pk_fma_f32 v[48:49], v[144:145], s[90:91], v[48:49]\n\t"
"v_pk_fma_f32 v[50:51], v[146:147], s[90:91], v[50:51]\n\t"
"v_pk_add_f32 v[144:145], v[48:49], v[144:145] neg_lo:[0,1] neg_hi:[0,1]\n\t"
"v_pk_add_f32 v[146:147], v[50:51], v[146:147] neg_lo:[0,1] neg_hi:[0,1]\n\t"
"v_pk_fma_f32 v[64:65], v[160:161], s[90:91], v[64:65]\n\t"
"v_pk_fma_f32 v[66:67], v[162:163], s[90:91], v[66:67]\n\t"
"v_pk_add_f32 v[160:161], v[64:65], v[160:161] neg_lo:[0,1] neg_hi:[0,1]\n\t"
"v_pk_add_f32 v[162:163], v[66:67], v[162:163] neg_lo:[0,1] neg_hi:[0,1]\n\t"
"v_pk_fma_f32 v[80:81], v[176:177], s[90:91], v[80:81]\n\t"
"v_pk_fma_f32 v[82:83], v[178:179], s[90:91], v[82:83]\n\t"
"v_pk_add_f32 v[176:177], v[80:81], v[176:177] neg_lo:[0,1] neg_hi:[0,1]\n\t"
"v_pk_add_f32 v[178:179], v[82:83], v[178:179] neg_lo:[0,1] neg_hi:[0,1]\n\t"
"v_pk_fma_f32 v[84:85], v[180:181], s[90:91], v[84:85]\n\t"
"v_pk_fma_f32 v[86:87], v[182:183], s[90:91], v[86:87]\n\t"
"v_pk_add_f32 v[180:181], v[84:85], v[180:181] neg_lo:[0,1] neg_hi:[0,1]\n\t"
"v_pk_add_f32 v[182:183], v[86:87], v[182:183] neg_lo:[0,1] neg_hi:[0,1]\n\t"
"v_pk_fma_f32 v[68:69], v[164:165], s[90:91], v[68:69]\n\t"
"v_pk_fma_f32 v[70:71], v[166:167], s[90:91], v[70:71]\n\t"
"v_pk_add_f32 v[164:165], v[68:69], v[164:165] neg_lo:[0,1] neg_hi:[0,1]\n\t"
"v_pk_add_f32 v[166:167], v[70:71], v[166:167] neg_lo:[0,1] neg_hi:[0,1]\n\t"
"v_pk_fma_f32 v[52:53], v[148:149], s[90:91], v[52:53]\n\t"
"v_pk_fma_f32 v[54:55], v[150:151], s[90:91], v[54:55]\n\t"
"v_pk_add_f32 v[148:149], v[52:53], v[148:149] neg_lo:[0,1] neg_hi:[0,1]\n\t"
"v_pk_add_f32 v[150:151], v[54:55], v[150:151] neg_lo:[0,1] neg_hi:[0,1]\n\t"
"v_pk_fma_f32 v[56:57], v[152:153], s[90:91], v[56:57]\n\t"
"v_pk_fma_f32 v[58:59], v[154:155], s[90:91], v[58:59]\n\t"
"v_pk_add_f32 v[152:153], v[56:57], v[152:153] neg_lo:[0,1] neg_hi:[0,1]\n\t"
"v_pk_add_f32 v[154:155], v[58:59], v[154:155] neg_lo:[0,1] neg_hi:[0,1]\n\t"
"v_pk_fma_f32 v[72:73], v[168:169], s[90:91], v[72:73]\n\t"
"v_pk_fma_f32 v[74:75], v[170:171], s[90:91], v[74:75]\n\t"
"v_pk_add_f32 v[168:169], v[72:73], v[168:169] neg_lo:[0,1] neg_hi:[0,1]\n\t"
"v_pk_add_f32 v[170:171], v[74:75], v[170:171] neg_lo:[0,1] neg_hi:[0,1]\n\t"
"v_pk_fma_f32 v[88:89], v[184:185], s[90:91], v[88:89]\n\t"
"v_pk_fma_f32 v[90:91], v[186:187], s[90:91], v[90:91]\n\t"
"v_pk_add_f32 v[184:185], v[88:89], v[184:185] neg_lo:[0,1] neg_hi:[0,1]\n\t"
"v_pk_add_f32 v[186:187], v[90:91], v[186:187] neg_lo:[0,1] neg_hi:[0,1]\n\t"
"v_pk_fma_f32 v[92:93], v[188:189], s[90:91], v[92:93]\n\t"
"v_pk_fma_f32 v[94:95], v[190:191], s[90:91], v[94:95]\n\t"
"v_pk_add_f32 v[188:189], v[92:93], v[188:189] neg_lo:[0,1] neg_hi:[0,1]\n\t"
"v_pk_add_f32 v[190:191], v[94:95], v[190:191] neg_lo:[0,1] neg_hi:[0,1]\n\t"
"v_pk_fma_f32 v[76:77], v[172:173], s[90:91], v[76:77]\n\t"
"v_pk_fma_f32 v[78:79], v[174:175], s[90:91], v[78:79]\n\t"
"v_pk_add_f32 v[172:173], v[76:77], v[172:173] neg_lo:[0,1] neg_hi:[0,1]\n\t"
"v_pk_add_f32 v[174:175], v[78:79], v[174:175] neg_lo:[0,1] neg_hi:[0,1]\n\t"
"v_pk_fma_f32 v[60:61], v[156:157], s[90:91], v[60:61]\n\t"
"v_pk_fma_f32 v[62:63], v[158:159], s[90:91], v[62:63]\n\t"
"v_pk_add_f32 v[156:157], v[60:61], v[156:157] neg_lo:[0,1] neg_hi:[0,1]\n\t"
"v_pk_add_f32 v[158:159], v[62:63], v[158:159] neg_lo:[0,1] neg_hi:[0,1]\n\t"
"v_mfma_f32_16x16x32_bf16 v[128:131], a[136:139], a[144:147], v[128:131]\n\t"
"s_nop 0\n\t"
"ds_write_addtid_b32 v218 offset:14368\n\t"
"ds_write_addtid_b32 v219 offset:14624\n\t"
"v_mfma_f32_16x16x32_bf16 v[128:131], a[140:143], a[160:163], v[128:131]\n\t"
"buffer_load_dwordx4 a[96:99], v252, s[8:11], s55 offen offset:0 sc1\n\t"
"v_mfma_f32_16x16x32_bf16 v[132:135], a[136:139], a[148:151], v[132:135]\n\t"
"s_nop 0\n\t"
"ds_read_b128 a[156:159], v224 offset:17984\n\t"
"ds_read_b64_tr_b16 a[204:205], v251 offset:5760\n\t"
"v_mfma_f32_16x16x32_bf16 v[132:135], a[140:143], a[164:167], v[132:135]\n\t"
"buffer_load_dwordx4 a[100:103], v252, s[8:11], s55 offen offset:1024 sc1\n\t"
"v_mfma_f32_16x16x32_bf16 v[116:119], a[128:131], a[148:151], v[116:119]\n\t"
"s_nop 0\n\t"
"ds_read_b128 a[152:155], v224 offset:17472\n\t"
"ds_read_b128 a[172:175], v224 offset:22080\n\t"
"v_mfma_f32_16x16x32_bf16 v[116:119], a[132:135], a[164:167], v[116:119]\n\t"
"v_mfma_f32_16x16x32_bf16 v[100:103], a[120:123], a[148:151], v[100:103]\n\t"
"v_mfma_f32_16x16x32_bf16 v[100:103], a[124:127], a[164:167], v[100:103]\n\t"
"s_waitcnt lgkmcnt(0)\n\t"
"s_barrier\n\t"
"v_mfma_f32_16x16x32_bf16 v[104:107], a[120:123], a[152:155], v[104:107]\n\t"
"s_add_u32 m0, s26, 34944\n\t"
"ds_read_b128 a[144:147], v224 offset:2048\n\t"
"ds_read_b128 a[160:163], v224 offset:6144\n\t"
"v_mfma_f32_16x16x32_bf16 v[104:107], a[124:127], a[168:171], v[104:107]\n\t"
"buffer_load_dwordx4 v236, s[12:15], s22 offen offset:256 nt lds\n\t"
"v_mfma_f32_16x16x32_bf16 v[120:123], a[128:131], a[152:155], v[120:123]\n\t"
"ds_read_b128 a[148:151], v224 offset:2560\n\t"
"v_add_u32 v248, 80896, v225\n\t"
"v_mfma_f32_16x16x32_bf16 v[120:123], a[132:135], a[168:171], v[120:123]\n\t"
"ds_read_b128 a[164:167], v224 offset:6656\n\t"
"v_xor_b32 v249, 32, v248\n\t"
"v_mfma_f32_16x16x32_bf16 v[136:139], a[136:139], a[152:155], v[136:139]\n\t"
"v_xor_b32 v250, 64, v248\n\t"
"v_mfma_f32_16x16x32_bf16 v[136:139], a[140:143], a[168:171], v[136:139]\n\t"
"ds_read_b128 a[228:231], v254 offset:39168\n\t"
"v_mfma_f32_16x16x32_bf16 v[192:195], a[212:215], a[176:179], 0\n\t"
"ds_read_b128 a[152:155], v224 offset:3072\n\t"
"v_xor_b32 v251, 96, v248\n\t"
"v_mfma_f32_16x16x32_bf16 v[140:143], a[136:139], a[156:159], v[140:143]\n\t"
"v_mfma_f32_16x16x32_bf16 v[140:143], a[140:143], a[172:175], v[140:143]\n\t"
"buffer_load_dwordx4 v237, s[12:15], s22 offen offset:1280 nt lds\n\t"
"v_mfma_f32_16x16x32_bf16 v[196:199], a[212:215], a[184:187], 0\n\t"
"s_lshl_b32 m0, s73, 9\n\t"
"ds_read_b128 a[168:171], v224 offset:7168\n\t"
"v_mfma_f32_16x16x32_bf16 v[124:127], a[128:131], a[156:159], v[124:127]\n\t"
"ds_read_b64_tr_b16 a[176:177], v248 offset:41472\n\t"
"v_mfma_f32_16x16x32_bf16 v[124:127], a[132:135], a[172:175], v[124:127]\n\t"
"ds_read_b64_tr_b16 a[184:185], v249 offset:41472\n\t"
"v_mfma_f32_16x16x32_bf16 v[108:111], a[120:123], a[156:159], v[108:111]\n\t"
"ds_read_b128 a[156:159], v224 offset:3584\n\t"
"v_mfma_f32_16x16x32_bf16 v[108:111], a[124:127], a[172:175], v[108:111]\n\t"
"ds_read_b128 a[172:175], v224 offset:7680\n\t"
"v_mfma_f32_16x16x32_bf16 v[200:203], a[212:215], a[178:181], 0\n\t"
"s_waitcnt vmcnt(26) lgkmcnt(7)\n\t"
"ds_read_b64_tr_b16 a[178:179], v250 offset:43776\n\t"
"v_cvt_pk_bf16_f32 v194, v194, v198\n\t"
"v_mfma_f32_16x16x32_bf16 v[48:51], a[0:3], a[144:147], v[48:51]\n\t"
"ds_write_addtid_b32 v194 offset:18496\n\t"
"s_add_u32 m0, s26, 34944\n\t"
"v_mfma_f32_16x16x32_bf16 v[48:51], a[4:7], a[160:163], v[48:51]\n\t"
"buffer_load_dwordx4 v238, s[12:15], s22 offen offset:2304 nt lds\n\t"
"v_mfma_f32_16x16x32_bf16 v[204:207], a[212:215], a[186:189], 0\n\t"
"s_waitcnt lgkmcnt(7)\n\t"
"ds_write_b128 v254, a[228:231] offset:0\n\t"
"v_pk_fma_f32 v[0:1], v[96:97], s[90:91], v[0:1]\n\t"
"v_pk_fma_f32 v[2:3], v[98:99], s[90:91], v[2:3]\n\t"
"v_pk_add_f32 v[96:97], v[0:1], v[96:97] neg_lo:[0,1] neg_hi:[0,1]\n\t"
"v_pk_add_f32 v[98:99], v[2:3], v[98:99] neg_lo:[0,1] neg_hi:[0,1]\n\t"
"v_pk_fma_f32 v[16:17], v[112:113], s[90:91], v[16:17]\n\t"
"v_pk_fma_f32 v[18:19], v[114:115], s[90:91], v[18:19]\n\t"
"v_pk_add_f32 v[112:113], v[16:17], v[112:113] neg_lo:[0,1] neg_hi:[0,1]\n\t"
"v_pk_add_f32 v[114:115], v[18:19], v[114:115] neg_lo:[0,1] neg_hi:[0,1]\n\t"
"v_pk_fma_f32 v[32:33], v[128:129], s[90:91], v[32:33]\n\t"
"v_pk_fma_f32 v[34:35], v[130:131], s[90:91], v[34:35]\n\t"
"v_pk_add_f32 v[128:129], v[32:33], v[128:129] neg_lo:[0,1] neg_hi:[0,1]\n\t"
"v_pk_add_f32 v[130:131], v[34:35], v[130:131] neg_lo:[0,1] neg_hi:[0,1]\n\t"
"v_pk_fma_f32 v[36:37], v[132:133], s[90:91], v[36:37]\n\t"
"v_pk_fma_f32 v[38:39], v[134:135], s[90:91], v[38:39]\n\t"
"v_pk_add_f32 v[132:133], v[36:37], v[132:133] neg_lo:[0,1] neg_hi:[0,1]\n\t"
"v_pk_add_f32 v[134:135], v[38:39], v[134:135] neg_lo:[0,1] neg_hi:[0,1]\n\t"
"v_pk_fma_f32 v[20:21], v[116:117], s[90:91], v[20:21]\n\t"
"v_pk_fma_f32 v[22:23], v[118:119], s[90:91], v[22:23]\n\t"
"v_pk_add_f32 v[116:117], v[20:21], v[116:117] neg_lo:[0,1] neg_hi:[0,1]\n\t"
"v_pk_add_f32 v[118:119], v[22:23], v[118:119] neg_lo:[0,1] neg_hi:[0,1]\n\t"
"v_pk_fma_f32 v[4:5], v[100:101], s[90:91], v[4:5]\n\t"
"v_pk_fma_f32 v[6:7], v[102:103], s[90:91], v[6:7]\n\t"
"v_pk_add_f32 v[100:101], v[4:5], v[100:101] neg_lo:[0,1] neg_hi:[0,1]\n\t"
"v_pk_add_f32 v[102:103], v[6:7], v[102:103] neg_lo:[0,1] neg_hi:[0,1]\n\t"
"v_pk_fma_f32 v[8:9], v[104:105], s[90:91], v[8:9]\n\t"
"v_pk_fma_f32 v[10:11], v[106:107], s[90:91], v[10:11]\n\t"
"v_pk_add_f32 v[104:105], v[8:9], v[104:105] neg_lo:[0,1] neg_hi:[0,1]\n\t"
"v_pk_add_f32 v[106:107], v[10:11], v[106:107] neg_lo:[0,1] neg_hi:[0,1]\n\t"
"v_pk_fma_f32 v[24:25], v[120:121], s[90:91], v[24:25]\n\t"
"v_pk_fma_f32 v[26:27], v[122:123], s[90:91], v[26:27]\n\t"
"v_pk_add_f32 v[120:121], v[24:25], v[120:121] neg_lo:[0,1] neg_hi:[0,1]\n\t"
"v_pk_add_f32 v[122:123], v[26:27], v[122:123] neg_lo:[0,1] neg_hi:[0,1]\n\t"
"v_pk_fma_f32 v[40:41], v[136:137], s[90:91], v[40:41]\n\t"
"v_pk_fma_f32 v[42:43], v[138:139], s[90:91], v[42:43]\n\t"
"v_pk_add_f32 v[136:137], v[40:41], v[136:137] neg_lo:[0,1] neg_hi:[0,1]\n\t"
"v_pk_add_f32 v[138:139], v[42:43], v[138:139] neg_lo:[0,1] neg_hi:[0,1]\n\t"
"v_pk_fma_f32 v[44:45], v[140:141], s[90:91], v[44:45]\n\t"
"v_pk_fma_f32 v[46:47], v[142:143], s[90:91], v[46:47]\n\t"
"v_pk_add_f32 v[140:141], v[44:45], v[140:141] neg_lo:[0,1] neg_hi:[0,1]\n\t"
"v_pk_add_f32 v[142:143], v[46:47], v[142:143] neg_lo:[0,1] neg_hi:[0,1]\n\t"
"v_pk_fma_f32 v[28:29], v[124:125], s[90:91], v[28:29]\n\t"
"v_pk_fma_f32 v[30:31], v[126:127], s[90:91], v[30:31]\n\t"
"v_pk_add_f32 v[124:125], v[28:29], v[124:125] neg_lo:[0,1] neg_hi:[0,1]\n\t"
"v_pk_add_f32 v[126:127], v[30:31], v[126:127] neg_lo:[0,1] neg_hi:[0,1]\n\t"
"v_pk_fma_f32 v[12:13], v[108:109], s[90:91], v[12:13]\n\t"
"v_pk_fma_f32 v[14:15], v[110:111], s[90:91], v[14:15]\n\t"
"v_pk_add_f32 v[108:109], v[12:13], v[108:109] neg_lo:[0,1] neg_hi:[0,1]\n\t"
"v_pk_add_f32 v[110:111], v[14:15], v[110:111] neg_lo:[0,1] neg_hi:[0,1]\n\t"
"v_mfma_f32_16x16x32_bf16 v[64:67], a[8:11], a[144:147], v[64:67]\n\t"
"v_cvt_pk_bf16_f32 v195, v195, v199\n\t"
"v_mfma_f32_16x16x32_bf16 v[64:67], a[12:15], a[160:163], v[64:67]\n\t"
"buffer_load_dwordx4 v239, s[12:15], s22 offen offset:3328 nt lds\n\t"
"v_mfma_f32_16x16x32_bf16 v[80:83], a[16:19], a[144:147], v[80:83]\n\t"
"s_lshl_b32 m0, s73, 9\n\t"
"v_cvt_pk_bf16_f32 v192, v192, v196\n\t"
"v_mfma_f32_16x16x32_bf16 v[80:83], a[20:23], a[160:163], v[80:83]\n\t"
"ds_read_b128 a[144:147], v224 offset:0\n\t"
"v_cvt_pk_bf16_f32 v193, v193, v197\n\t"
"v_mfma_f32_16x16x32_bf16 v[208:211], a[212:215], a[192:195], 0\n\t"
"ds_write_addtid_b32 v195 offset:18752\n\t"
"v_cvt_pk_bf16_f32 v203, v203, v207\n\t"
"v_mfma_f32_16x16x32_bf16 v[84:87], a[16:19], a[148:151], v[84:87]\n\t"
"ds_read_b64_tr_b16 a[180:181], v248 offset:46080\n\t"
"v_mfma_f32_16x16x32_bf16 v[84:87], a[20:23], a[164:167], v[84:87]\n\t"
"ds_write_addtid_b32 v193 offset:16704\n\t"
"v_cvt_pk_bf16_f32 v201, v201, v205\n\t"
"v_mfma_f32_16x16x32_bf16 v[68:71], a[8:11], a[148:151], v[68:71]\n\t"
"ds_write_addtid_b32 v192 offset:16448\n\t"
"s_add_u32 m0, s26, 34944\n\t"
"v_mfma_f32_16x16x32_bf16 v[68:71], a[12:15], a[164:167], v[68:71]\n\t"
"s_mov_b64 exec, s[36:37]\n\t"
"buffer_load_dwordx4 v240, s[12:15], s22 offen offset:3840 nt lds\n\t"
"s_mov_b64 exec, -1\n\t"
"v_mfma_f32_16x16x32_bf16 v[212:215], a[212:215], a[200:203], 0\n\t"
"s_lshl_b32 m0, s73, 9\n\t"
"ds_read_b64_tr_b16 a[186:187], v251 offset:43776\n\t"
"v_mfma_f32_16x16x32_bf16 v[52:55], a[0:3], a[148:151], v[52:55]\n\t"
"ds_read_b128 a[148:151], v224 offset:512\n\t"
"v_mfma_f32_16x16x32_bf16 v[52:55], a[4:7], a[164:167], v[52:55]\n\t"
"ds_read_b128 a[160:163], v224 offset:4096\n\t"
"v_mfma_f32_16x16x32_bf16 v[216:219], a[212:215], a[194:197], 0\n\t"
"s_waitcnt lgkmcnt(8)\n\t"
"ds_write_addtid_b32 v201 offset:24928\n\t"
"v_cvt_pk_bf16_f32 v202, v202, v206\n\t"
"v_mfma_f32_16x16x32_bf16 v[56:59], a[0:3], a[152:155], v[56:59]\n\t"
"ds_write_addtid_b32 v203 offset:26976\n\t"
"v_cvt_pk_bf16_f32 v210, v210, v214\n\t"
"v_mfma_f32_16x16x32_bf16 v[56:59], a[4:7], a[168:171], v[56:59]\n\t"
"ds_read_b128 a[164:167], v224 offset:4608\n\t"
"v_cvt_pk_bf16_f32 v200, v200, v204\n\t"
"v_mfma_f32_16x16x32_bf16 v[72:75], a[8:11], a[152:155], v[72:75]\n\t"
"ds_write_addtid_b32 v202 offset:26720\n\t"
"v_cvt_pk_bf16_f32 v209, v209, v213\n\t"
"v_mfma_f32_16x16x32_bf16 v[72:75], a[12:15], a[168:171], v[72:75]\n\t"
"ds_write_addtid_b32 v200 offset:24672\n\t"
"v_mfma_f32_16x16x32_bf16 v[220:223], a[212:215], a[202:205], 0\n\t"
"ds_write_addtid_b32 v209 offset:20800\n\t"
"v_mfma_f32_16x16x32_bf16 v[88:91], a[16:19], a[152:155], v[88:91]\n\t"
"ds_read_b64_tr_b16 a[188:189], v249 offset:46080\n\t"
"v_cvt_pk_bf16_f32 v208, v208, v212\n\t"
"v_mfma_f32_16x16x32_bf16 v[88:91], a[20:23], a[168:171], v[88:91]\n\t"
"buffer_load_dwordx4 a[136:139], v235, s[8:11], s55 offen offset:2048 sc1\n\t"
"v_mfma_f32_16x16x32_bf16 v[92:95], a[16:19], a[156:159], v[92:95]\n\t"
"ds_write_addtid_b32 v210 offset:22592\n\t"
"v_mfma_f32_16x16x32_bf16 v[92:95], a[20:23], a[172:175], v[92:95]\n\t"
"ds_read_b128 a[168:171], v224 offset:5120\n\t"
"v_cvt_pk_bf16_f32 v211, v211, v215\n\t"
"v_mfma_f32_16x16x32_bf16 v[76:79], a[8:11], a[156:159], v[76:79]\n\t"
"ds_read_b64_tr_b16 a[192:193], v250 offset:41472\n\t"
"v_cvt_pk_bf16_f32 v218, v218, v222\n\t"
"v_mfma_f32_16x16x32_bf16 v[76:79], a[12:15], a[172:175], v[76:79]\n\t"
"ds_read_b64_tr_b16 a[200:201], v251 offset:41472\n\t"
"v_mfma_f32_16x16x32_bf16 v[60:63], a[0:3], a[156:159], v[60:63]\n\t"
"ds_write_addtid_b32 v218 offset:30816\n\t"
"v_mfma_f32_16x16x32_bf16 v[60:63], a[4:7], a[172:175], v[60:63]\n\t"
"s_waitcnt vmcnt(24) lgkmcnt(8)\n\t"
"ds_write_addtid_b32 v211 offset:22848\n\t"
"v_cvt_pk_bf16_f32 v217, v217, v221\n\t"
"v_mfma_f32_16x16x32_bf16 v[0:3], a[24:27], a[144:147], v[0:3]\n\t"
"s_nop 0\n\t"
"ds_write_addtid_b32 v208 offset:20544\n\t"
"ds_write_addtid_b32 v217 offset:29024\n\t"
"v_mfma_f32_16x16x32_bf16 v[0:3], a[28:31], a[160:163], v[0:3]\n\t"
"s_nop 0\n\t"
"ds_read_b64_tr_b16 a[194:195], v248 offset:43776\n\t"
"ds_read_b64_tr_b16 a[196:197], v250 offset:46080\n\t"
"v_mfma_f32_16x16x32_bf16 v[16:19], a[32:35], a[144:147], v[16:19]\n\t"
"ds_read_b64_tr_b16 a[202:203], v249 offset:43776\n\t"
"v_cvt_pk_bf16_f32 v216, v216, v220\n\t"
"v_mfma_f32_16x16x32_bf16 v[16:19], a[36:39], a[160:163], v[16:19]\n\t"
"ds_write_addtid_b32 v216 offset:28768\n\t"
"v_cvt_pk_bf16_f32 v219, v219, v223\n\t"
"v_mfma_f32_16x16x32_bf16 v[32:35], a[40:43], a[144:147], v[32:35]\n\t"
"ds_write_addtid_b32 v219 offset:31072\n\t"
"v_mfma_f32_16x16x32_bf16 v[32:35], a[44:47], a[160:163], v[32:35]\n\t"
"ds_read_b64_tr_b16 a[204:205], v251 offset:46080\n\t"
"v_mfma_f32_16x16x32_bf16 v[36:39], a[40:43], a[148:151], v[36:39]\n\t"
"ds_read_b128 a[156:159], v224 offset:1536\n\t"
"v_mfma_f32_16x16x32_bf16 v[36:39], a[44:47], a[164:167], v[36:39]\n\t"
"ds_read_b128 a[172:175], v224 offset:5632\n\t"
"v_mfma_f32_16x16x32_bf16 v[20:23], a[32:35], a[148:151], v[20:23]\n\t"
"ds_read_b128 a[152:155], v224 offset:1024\n\t"
"v_mfma_f32_16x16x32_bf16 v[20:23], a[36:39], a[164:167], v[20:23]\n\t"
"v_mfma_f32_16x16x32_bf16 v[4:7], a[24:27], a[148:151], v[4:7]\n\t"
"v_mfma_f32_16x16x32_bf16 v[4:7], a[28:31], a[164:167], v[4:7]\n\t"
"s_waitcnt lgkmcnt(0)\n\t"
"s_barrier\n\t"
"v_mfma_f32_16x16x32_bf16 v[8:11], a[24:27], a[152:155], v[8:11]\n\t"
"ds_read_b128 a[144:147], v224 offset:18496\n\t"
"v_mfma_f32_16x16x32_bf16 v[8:11], a[28:31], a[168:171], v[8:11]\n\t"
"ds_read_b128 a[160:163], v224 offset:22592\n\t"
"v_mfma_f32_16x16x32_bf16 v[24:27], a[32:35], a[152:155], v[24:27]\n\t"
"ds_read_b128 a[148:151], v224 offset:19008\n\t"
"v_mfma_f32_16x16x32_bf16 v[24:27], a[36:39], a[168:171], v[24:27]\n\t"
"ds_read_b128 a[164:167], v224 offset:23104\n\t"
"v_mfma_f32_16x16x32_bf16 v[40:43], a[40:43], a[152:155], v[40:43]\n\t"
"v_mfma_f32_16x16x32_bf16 v[40:43], a[44:47], a[168:171], v[40:43]\n\t"
"v_mfma_f32_16x16x32_bf16 v[192:195], a[212:215], a[176:179], 0\n\t"
"ds_read_b128 a[168:171], v224 offset:23616\n\t"
"v_mfma_f32_16x16x32_bf16 v[44:47], a[40:43], a[156:159], v[44:47]\n\t"
"v_mfma_f32_16x16x32_bf16 v[44:47], a[44:47], a[172:175], v[44:47]\n\t"
"ds_read_b64_tr_b16 a[176:177], v248 offset:0\n\t"
"v_mfma_f32_16x16x32_bf16 v[196:199], a[212:215], a[184:187], 0\n\t"
"ds_read_b64_tr_b16 a[184:185], v249 offset:0\n\t"
"v_mfma_f32_16x16x32_bf16 v[28:31], a[32:35], a[156:159], v[28:31]\n\t"
"ds_read_b128 a[152:155], v224 offset:19520\n\t"
"v_mfma_f32_16x16x32_bf16 v[28:31], a[36:39], a[172:175], v[28:31]\n\t"
"v_mfma_f32_16x16x32_bf16 v[12:15], a[24:27], a[156:159], v[12:15]\n\t"
"ds_read_b128 a[156:159], v224 offset:20032\n\t"
"v_mfma_f32_16x16x32_bf16 v[12:15], a[28:31], a[172:175], v[12:15]\n\t"
"buffer_load_dwordx4 a[140:143], v235, s[8:11], s55 offen offset:3072 sc1\n\t"
"v_mfma_f32_16x16x32_bf16 v[200:203], a[212:215], a[178:181], 0\n\t"
"s_waitcnt vmcnt(19) lgkmcnt(5)\n\t"
"ds_read_b128 a[172:175], v224 offset:24128\n\t"
"v_cvt_pk_bf16_f32 v193, v193, v197\n\t"
"v_mfma_f32_16x16x32_bf16 v[144:147], a[48:51], a[144:147], v[144:147]\n\t"
"ds_write_addtid_b32 v193 offset:256\n\t"
"v_cvt_pk_bf16_f32 v195, v195, v199\n\t"
"v_mfma_f32_16x16x32_bf16 v[144:147], a[52:55], a[160:163], v[144:147]\n\t"
"buffer_load_dwordx4 a[132:135], v235, s[8:11], s55 offen offset:1024 sc1\n\t"
"v_mfma_f32_16x16x32_bf16 v[204:207], a[212:215], a[186:189], 0\n\t"
"ds_read_b64_tr_b16 a[178:179], v250 offset:2304\n\t"
"v_cvt_pk_bf16_f32 v192, v192, v196\n\t"
"v_mfma_f32_16x16x32_bf16 v[160:163], a[56:59], a[144:147], v[160:163]\n\t"
"ds_write_addtid_b32 v192 offset:0\n\t"
"v_mfma_f32_16x16x32_bf16 v[160:163], a[60:63], a[160:163], v[160:163]\n\t"
"buffer_load_dwordx4 a[128:131], v235, s[8:11], s55 offen offset:0 sc1\n\t"
"v_mfma_f32_16x16x32_bf16 v[176:179], a[64:67], a[144:147], v[176:179]\n\t"
"ds_read_b64_tr_b16 a[180:181], v248 offset:4608\n\t"
"v_mfma_f32_16x16x32_bf16 v[176:179], a[68:71], a[160:163], v[176:179]\n\t"
"ds_read_b64_tr_b16 a[186:187], v251 offset:2304\n\t"
"v_mfma_f32_16x16x32_bf16 v[208:211], a[212:215], a[192:195], 0\n\t"
"ds_read_b128 a[144:147], v224 offset:16448\n\t"
"v_cvt_pk_bf16_f32 v194, v194, v198\n\t"
"v_mfma_f32_16x16x32_bf16 v[180:183], a[64:67], a[148:151], v[180:183]\n\t"
"ds_read_b128 a[160:163], v224 offset:20544\n\t"
"v_cvt_pk_bf16_f32 v202, v202, v206\n\t"
"v_mfma_f32_16x16x32_bf16 v[180:183], a[68:71], a[164:167], v[180:183]\n\t"
"buffer_load_dwordx4 a[120:123], v252, s[8:11], s54 offen offset:2048 sc1\n\t"
"v_mfma_f32_16x16x32_bf16 v[164:167], a[56:59], a[148:151], v[164:167]\n\t"
"ds_read_b64_tr_b16 a[188:189], v249 offset:4608\n\t"
"v_cvt_pk_bf16_f32 v200, v200, v204\n\t"
"v_mfma_f32_16x16x32_bf16 v[164:167], a[60:63], a[164:167], v[164:167]\n\t"
"ds_write_addtid_b32 v195 offset:2304\n\t"
"v_cvt_pk_bf16_f32 v201, v201, v205\n\t"
"v_mfma_f32_16x16x32_bf16 v[212:215], a[212:215], a[200:203], 0\n\t"
"buffer_load_dwordx4 a[124:127], v252, s[8:11], s54 offen offset:3072 sc1\n\t"
"v_mfma_f32_16x16x32_bf16 v[148:151], a[48:51], a[148:151], v[148:151]\n\t"
"s_nop 0\n\t"
"ds_write_addtid_b32 v194 offset:2048\n\t"
"ds_write_addtid_b32 v202 offset:10272\n\t"
"v_mfma_f32_16x16x32_bf16 v[148:151], a[52:55], a[164:167], v[148:151]\n\t"
"ds_read_b128 a[164:167], v224 offset:21056\n\t"
"v_cvt_pk_bf16_f32 v203, v203, v207\n\t"
"v_mfma_f32_16x16x32_bf16 v[216:219], a[212:215], a[194:197], 0\n\t"
"s_waitcnt lgkmcnt(8)\n\t"
"ds_write_addtid_b32 v203 offset:10528\n\t"
"v_mfma_f32_16x16x32_bf16 v[152:155], a[48:51], a[152:155], v[152:155]\n\t"
"ds_write_addtid_b32 v200 offset:8224\n\t"
"v_cvt_pk_bf16_f32 v211, v211, v215\n\t"
"v_mfma_f32_16x16x32_bf16 v[152:155], a[52:55], a[168:171], v[152:155]\n\t"
"buffer_load_dwordx4 a[16:19], v235, s[8:11], s41 offen offset:0 sc1\n\t"
"v_mfma_f32_16x16x32_bf16 v[168:171], a[56:59], a[152:155], v[168:171]\n\t"
"ds_write_addtid_b32 v201 offset:8480\n\t"
"v_cvt_pk_bf16_f32 v208, v208, v212\n\t"
"v_mfma_f32_16x16x32_bf16 v[168:171], a[60:63], a[168:171], v[168:171]\n\t"
"buffer_load_dwordx4 a[20:23], v235, s[8:11], s41 offen offset:1024 sc1\n\t"
"v_mfma_f32_16x16x32_bf16 v[220:223], a[212:215], a[202:205], 0\n\t"
"s_nop 0\n\t"
"ds_read_b128 a[148:151], v224 offset:16960\n\t"
"ds_read_b64_tr_b16 a[192:193], v250 offset:0\n\t"
"v_mfma_f32_16x16x32_bf16 v[184:187], a[64:67], a[152:155], v[184:187]\n\t"
"ds_read_b64_tr_b16 a[200:201], v251 offset:0\n\t"
"v_cvt_pk_bf16_f32 v209, v209, v213\n\t"
"v_mfma_f32_16x16x32_bf16 v[184:187], a[68:71], a[168:171], v[184:187]\n\t"
"s_nop 0\n\t"
"ds_read_b128 a[168:171], v224 offset:21568\n\t"
"ds_write_addtid_b32 v208 offset:4096\n\t"
"v_mfma_f32_16x16x32_bf16 v[188:191], a[64:67], a[156:159], v[188:191]\n\t"
"ds_write_addtid_b32 v211 offset:6400\n\t"
"v_cvt_pk_bf16_f32 v210, v210, v214\n\t"
"v_mfma_f32_16x16x32_bf16 v[188:191], a[68:71], a[172:175], v[188:191]\n\t"
"buffer_load_dwordx4 a[12:15], v252, s[8:11], s40 offen offset:3072 sc1\n\t"
"v_mfma_f32_16x16x32_bf16 v[172:175], a[56:59], a[156:159], v[172:175]\n\t"
"ds_read_b128 a[152:155], v224 offset:17472\n\t"
"v_cvt_pk_bf16_f32 v216, v216, v220\n\t"
"v_mfma_f32_16x16x32_bf16 v[172:175], a[60:63], a[172:175], v[172:175]\n\t"
"ds_read_b64_tr_b16 a[194:195], v248 offset:2304\n\t"
"v_cvt_pk_bf16_f32 v218, v218, v222\n\t"
"v_mfma_f32_16x16x32_bf16 v[156:159], a[48:51], a[156:159], v[156:159]\n\t"
"s_nop 0\n\t"
"ds_write_addtid_b32 v209 offset:4352\n\t"
"ds_write_addtid_b32 v216 offset:12320\n\t"
"v_mfma_f32_16x16x32_bf16 v[156:159], a[52:55], a[172:175], v[156:159]\n\t"
"s_waitcnt vmcnt(20) lgkmcnt(10)\n\t"
"buffer_load_dwordx4 a[8:11], v252, s[8:11], s40 offen offset:2048 sc1\n\t"
"v_mfma_f32_16x16x32_bf16 v[96:99], a[72:75], a[144:147], v[96:99]\n\t"
"ds_write_addtid_b32 v210 offset:6144\n\t"
"v_cvt_pk_bf16_f32 v217, v217, v221\n\t"
"v_mfma_f32_16x16x32_bf16 v[96:99], a[76:79], a[160:163], v[96:99]\n\t"
"ds_read_b64_tr_b16 a[196:197], v250 offset:4608\n\t"
"v_cvt_pk_bf16_f32 v219, v219, v223\n\t"
"v_mfma_f32_16x16x32_bf16 v[112:115], a[80:83], a[144:147], v[112:115]\n\t"
"s_nop 0\n\t"
"ds_read_b64_tr_b16 a[202:203], v249 offset:2304\n\t"
"ds_read_b64_tr_b16 a[204:205], v251 offset:4608\n\t"
"v_mfma_f32_16x16x32_bf16 v[112:115], a[84:87], a[160:163], v[112:115]\n\t"
"buffer_load_dwordx4 a[0:3], v252, s[8:11], s40 offen offset:0 sc1\n\t"
"v_mfma_f32_16x16x32_bf16 v[128:131], a[88:91], a[144:147], v[128:131]\n\t"
"s_nop 0\n\t"
"ds_write_addtid_b32 v218 offset:14368\n\t"
"ds_write_addtid_b32 v219 offset:14624\n\t"
"v_mfma_f32_16x16x32_bf16 v[128:131], a[92:95], a[160:163], v[128:131]\n\t"
"s_waitcnt lgkmcnt(10)\n\t"
"ds_write_addtid_b32 v217 offset:12576\n\t"
"v_mfma_f32_16x16x32_bf16 v[132:135], a[88:91], a[148:151], v[132:135]\n\t"
"ds_read_b128 a[172:175], v224 offset:22080\n\t"
"v_mfma_f32_16x16x32_bf16 v[132:135], a[92:95], a[164:167], v[132:135]\n\t"
"buffer_load_dwordx4 a[4:7], v252, s[8:11], s40 offen offset:1024 sc1\n\t"
"v_mfma_f32_16x16x32_bf16 v[116:119], a[80:83], a[148:151], v[116:119]\n\t"
"ds_read_b128 a[156:159], v224 offset:17984\n\t"
"v_mfma_f32_16x16x32_bf16 v[116:119], a[84:87], a[164:167], v[116:119]\n\t"
"buffer_load_dwordx4 a[40:43], v235, s[8:11], s40 offen offset:2048 sc1\n\t"
"v_mfma_f32_16x16x32_bf16 v[100:103], a[72:75], a[148:151], v[100:103]\n\t"
"v_mfma_f32_16x16x32_bf16 v[100:103], a[76:79], a[164:167], v[100:103]\n\t"
"s_waitcnt lgkmcnt(0)\n\t"
"s_barrier\n\t"
"v_mfma_f32_16x16x32_bf16 v[104:107], a[72:75], a[152:155], v[104:107]\n\t"
"s_nop 0\n\t"
"ds_read_b128 a[144:147], v224 offset:2048\n\t"
"ds_read_b128 a[160:163], v224 offset:6144\n\t"
"v_mfma_f32_16x16x32_bf16 v[104:107], a[76:79], a[168:171], v[104:107]\n\t"
"buffer_load_dwordx4 a[44:47], v235, s[8:11], s40 offen offset:3072 sc1\n\t"
"v_mfma_f32_16x16x32_bf16 v[120:123], a[80:83], a[152:155], v[120:123]\n\t"
"ds_read_b128 a[148:151], v224 offset:2560\n\t"
"v_mfma_f32_16x16x32_bf16 v[120:123], a[84:87], a[168:171], v[120:123]\n\t"
"ds_read_b128 a[164:167], v224 offset:6656\n\t"
"v_mfma_f32_16x16x32_bf16 v[136:139], a[88:91], a[152:155], v[136:139]\n\t"
"v_mfma_f32_16x16x32_bf16 v[136:139], a[92:95], a[168:171], v[136:139]\n\t"
"buffer_load_dwordx4 a[36:39], v235, s[8:11], s40 offen offset:1024 sc1\n\t"
"v_mfma_f32_16x16x32_bf16 v[192:195], a[212:215], a[176:179], 0\n\t"
"ds_read_b128 a[152:155], v224 offset:3072\n\t"
"v_mfma_f32_16x16x32_bf16 v[140:143], a[88:91], a[156:159], v[140:143]\n\t"
"ds_read_b128 a[168:171], v224 offset:7168\n\t"
"v_mfma_f32_16x16x32_bf16 v[140:143], a[92:95], a[172:175], v[140:143]\n\t"
"buffer_load_dwordx4 a[32:35], v235, s[8:11], s40 offen offset:0 sc1\n\t"
"v_mfma_f32_16x16x32_bf16 v[196:199], a[212:215], a[184:187], 0\n\t"
"ds_read_b64_tr_b16 a[176:177], v248 offset:42624\n\t"
"v_mfma_f32_16x16x32_bf16 v[124:127], a[80:83], a[156:159], v[124:127]\n\t"
"ds_read_b64_tr_b16 a[184:185], v249 offset:42624\n\t"
"v_mfma_f32_16x16x32_bf16 v[124:127], a[84:87], a[172:175], v[124:127]\n\t"
"buffer_load_dwordx4 a[24:27], v252, s[8:11], 0 offen offset:2048 sc1\n\t"
"v_mfma_f32_16x16x32_bf16 v[108:111], a[72:75], a[156:159], v[108:111]\n\t"
"ds_read_b128 a[156:159], v224 offset:3584\n\t"
"v_mfma_f32_16x16x32_bf16 v[108:111], a[76:79], a[172:175], v[108:111]\n\t"
"buffer_load_dwordx4 a[28:31], v252, s[8:11], 0 offen offset:3072 sc1\n\t"
"v_mfma_f32_16x16x32_bf16 v[200:203], a[212:215], a[178:181], 0\n\t"
"s_waitcnt vmcnt(23) lgkmcnt(5)\n\t"
"ds_read_b128 a[172:175], v224 offset:7680\n\t"
"v_cvt_pk_bf16_f32 v195, v195, v199\n\t"
"v_mfma_f32_16x16x32_bf16 v[48:51], a[96:99], a[144:147], v[48:51]\n\t"
"ds_read_b64_tr_b16 a[178:179], v250 offset:44928\n\t"
"v_cvt_pk_bf16_f32 v193, v193, v197\n\t"
"v_mfma_f32_16x16x32_bf16 v[48:51], a[100:103], a[160:163], v[48:51]\n\t"
"ds_read_b64_tr_b16 a[180:181], v248 offset:47232\n\t"
"v_mfma_f32_16x16x32_bf16 v[204:207], a[212:215], a[186:189], 0\n\t"
"buffer_load_dwordx4 a[64:67], v235, s[8:11], s68 offen offset:0 sc1\n\t"
"v_mfma_f32_16x16x32_bf16 v[64:67], a[104:107], a[144:147], v[64:67]\n\t"
"ds_read_b64_tr_b16 a[186:187], v251 offset:44928\n\t"
"v_cvt_pk_bf16_f32 v192, v192, v196\n\t"
"v_mfma_f32_16x16x32_bf16 v[64:67], a[108:111], a[160:163], v[64:67]\n\t"
"ds_read_b64_tr_b16 a[188:189], v249 offset:47232\n\t"
"v_mfma_f32_16x16x32_bf16 v[80:83], a[112:115], a[144:147], v[80:83]\n\t"
"ds_write_addtid_b32 v192 offset:16448\n\t"
"v_cvt_pk_bf16_f32 v194, v194, v198\n\t"
"v_mfma_f32_16x16x32_bf16 v[80:83], a[116:119], a[160:163], v[80:83]\n\t"
"buffer_load_dwordx4 a[68:71], v235, s[8:11], s68 offen offset:1024 sc1\n\t"
"v_mfma_f32_16x16x32_bf16 v[208:211], a[212:215], a[192:195], 0\n\t"
"ds_read_b128 a[160:163], v224 offset:4096\n\t"
"v_cvt_pk_bf16_f32 v201, v201, v205\n\t"
"v_mfma_f32_16x16x32_bf16 v[84:87], a[112:115], a[148:151], v[84:87]\n\t"
"ds_write_addtid_b32 v195 offset:18752\n\t"
"v_cvt_pk_bf16_f32 v203, v203, v207\n\t"
"v_mfma_f32_16x16x32_bf16 v[84:87], a[116:119], a[164:167], v[84:87]\n\t"
"buffer_load_dwordx4 a[60:63], v252, s[8:11], s67 offen offset:3072 sc1\n\t"
"v_mfma_f32_16x16x32_bf16 v[68:71], a[104:107], a[148:151], v[68:71]\n\t"
"ds_write_addtid_b32 v201 offset:24928\n\t"
"v_cvt_pk_bf16_f32 v202, v202, v206\n\t"
"v_mfma_f32_16x16x32_bf16 v[68:71], a[108:111], a[164:167], v[68:71]\n\t"
"ds_read_b128 a[144:147], v224 offset:0\n\t"
"v_cvt_pk_bf16_f32 v200, v200, v204\n\t"
"v_mfma_f32_16x16x32_bf16 v[212:215], a[212:215], a[200:203], 0\n\t"
"ds_write_addtid_b32 v193 offset:16704\n\t"
"v_mfma_f32_16x16x32_bf16 v[52:55], a[96:99], a[148:151], v[52:55]\n\t"
"s_nop 0\n\t"
"ds_write_addtid_b32 v194 offset:18496\n\t"
"ds_write_addtid_b32 v203 offset:26976\n\t"
"v_mfma_f32_16x16x32_bf16 v[52:55], a[100:103], a[164:167], v[52:55]\n\t"
"buffer_load_dwordx4 a[56:59], v252, s[8:11], s67 offen offset:2048 sc1\n\t"
"v_mfma_f32_16x16x32_bf16 v[216:219], a[212:215], a[194:197], 0\n\t"
"s_waitcnt lgkmcnt(7)\n\t"
"ds_read_b128 a[164:167], v224 offset:4608\n\t"
"ds_write_addtid_b32 v202 offset:26720\n\t"
"v_mfma_f32_16x16x32_bf16 v[56:59], a[96:99], a[152:155], v[56:59]\n\t"
"ds_read_b128 a[148:151], v224 offset:512\n\t"
"v_cvt_pk_bf16_f32 v211, v211, v215\n\t"
"v_mfma_f32_16x16x32_bf16 v[56:59], a[100:103], a[168:171], v[56:59]\n\t"
"buffer_load_dwordx4 a[48:51], v252, s[8:11], s67 offen offset:0 sc1\n\t"
"v_mfma_f32_16x16x32_bf16 v[72:75], a[104:107], a[152:155], v[72:75]\n\t"
"ds_read_b64_tr_b16 a[192:193], v250 offset:42624\n\t"
"v_cvt_pk_bf16_f32 v210, v210, v214\n\t"
"v_mfma_f32_16x16x32_bf16 v[72:75], a[108:111], a[168:171], v[72:75]\n\t"
"ds_write_addtid_b32 v200 offset:24672\n\t"
"v_mfma_f32_16x16x32_bf16 v[220:223], a[212:215], a[202:205], 0\n\t"
"ds_read_b64_tr_b16 a[200:201], v251 offset:42624\n\t"
"v_cvt_pk_bf16_f32 v209, v209, v213\n\t"
"v_mfma_f32_16x16x32_bf16 v[88:91], a[112:115], a[152:155], v[88:91]\n\t"
"ds_write_addtid_b32 v209 offset:20800\n\t"
"v_cvt_pk_bf16_f32 v208, v208, v212\n\t"
"v_mfma_f32_16x16x32_bf16 v[88:91], a[116:119], a[168:171], v[88:91]\n\t"
"buffer_load_dwordx4 a[52:55], v252, s[8:11], s67 offen offset:1024 sc1\n\t"
"v_mfma_f32_16x16x32_bf16 v[92:95], a[112:115], a[156:159], v[92:95]\n\t"
"ds_write_addtid_b32 v210 offset:22592\n\t"
"v_mfma_f32_16x16x32_bf16 v[92:95], a[116:119], a[172:175], v[92:95]\n\t"
"buffer_load_dwordx4 a[88:91], v235, s[8:11], s67 offen offset:2048 sc1\n\t"
"v_mfma_f32_16x16x32_bf16 v[76:79], a[104:107], a[156:159], v[76:79]\n\t"
"ds_read_b64_tr_b16 a[194:195], v248 offset:44928\n\t"
"v_mfma_f32_16x16x32_bf16 v[76:79], a[108:111], a[172:175], v[76:79]\n\t"
"ds_write_addtid_b32 v208 offset:20544\n\t"
"v_cvt_pk_bf16_f32 v217, v217, v221\n\t"
"v_mfma_f32_16x16x32_bf16 v[60:63], a[96:99], a[156:159], v[60:63]\n\t"
"ds_write_addtid_b32 v217 offset:29024\n\t"
"v_cvt_pk_bf16_f32 v216, v216, v220\n\t"
"v_mfma_f32_16x16x32_bf16 v[60:63], a[100:103], a[172:175], v[60:63]\n\t"
"s_waitcnt vmcnt(19) lgkmcnt(7)\n\t"
"buffer_load_dwordx4 a[92:95], v235, s[8:11], s67 offen offset:3072 sc1\n\t"
"v_mfma_f32_16x16x32_bf16 v[0:3], a[120:123], a[144:147], v[0:3]\n\t"
"ds_write_addtid_b32 v216 offset:28768\n\t"
"v_cvt_pk_bf16_f32 v219, v219, v223\n\t"
"v_mfma_f32_16x16x32_bf16 v[0:3], a[124:127], a[160:163], v[0:3]\n\t"
"ds_read_b128 a[156:159], v224 offset:1536\n\t"
"v_cvt_pk_bf16_f32 v218, v218, v222\n\t"
"v_mfma_f32_16x16x32_bf16 v[16:19], a[128:131], a[144:147], v[16:19]\n\t"
"s_nop 0\n\t"
"ds_write_addtid_b32 v211 offset:22848\n\t"
"ds_write_addtid_b32 v218 offset:30816\n\t"
"v_mfma_f32_16x16x32_bf16 v[16:19], a[132:135], a[160:163], v[16:19]\n\t"
"buffer_load_dwordx4 a[84:87], v235, s[8:11], s67 offen offset:1024 sc1\n\t"
"v_mfma_f32_16x16x32_bf16 v[32:35], a[136:139], a[144:147], v[32:35]\n\t"
"ds_read_b128 a[168:171], v224 offset:5120\n\t"
"v_mfma_f32_16x16x32_bf16 v[32:35], a[140:143], a[160:163], v[32:35]\n\t"
"s_nop 0\n\t"
"ds_write_addtid_b32 v219 offset:31072\n\t"
"ds_read_b64_tr_b16 a[196:197], v250 offset:47232\n\t"
"v_mfma_f32_16x16x32_bf16 v[36:39], a[136:139], a[148:151], v[36:39]\n\t"
"s_nop 0\n\t"
"ds_read_b128 a[172:175], v224 offset:5632\n\t"
"ds_read_b64_tr_b16 a[202:203], v249 offset:44928\n\t"
"v_mfma_f32_16x16x32_bf16 v[36:39], a[140:143], a[164:167], v[36:39]\n\t"
"ds_read_b64_tr_b16 a[204:205], v251 offset:47232\n\t"
"v_mfma_f32_16x16x32_bf16 v[20:23], a[128:131], a[148:151], v[20:23]\n\t"
"ds_read_b128 a[152:155], v224 offset:1024\n\t"
"v_mfma_f32_16x16x32_bf16 v[20:23], a[132:135], a[164:167], v[20:23]\n\t"
"buffer_load_dwordx4 a[80:83], v235, s[8:11], s67 offen offset:0 sc1\n\t"
"v_mfma_f32_16x16x32_bf16 v[4:7], a[120:123], a[148:151], v[4:7]\n\t"
"v_mfma_f32_16x16x32_bf16 v[4:7], a[124:127], a[164:167], v[4:7]\n\t"
"buffer_load_dwordx4 a[72:75], v252, s[8:11], s66 offen offset:2048 sc1\n\t"
"s_waitcnt lgkmcnt(0)\n\t"
"s_barrier\n\t"
"v_mfma_f32_16x16x32_bf16 v[8:11], a[120:123], a[152:155], v[8:11]\n\t"
"ds_read_b128 a[160:163], v224 offset:22592\n\t"
"v_mfma_f32_16x16x32_bf16 v[8:11], a[124:127], a[168:171], v[8:11]\n\t"
"ds_read_b128 a[144:147], v224 offset:18496\n\t"
"v_mfma_f32_16x16x32_bf16 v[24:27], a[128:131], a[152:155], v[24:27]\n\t"
"ds_read_b128 a[148:151], v224 offset:19008\n\t"
"v_mfma_f32_16x16x32_bf16 v[24:27], a[132:135], a[168:171], v[24:27]\n\t"
"ds_read_b128 a[164:167], v224 offset:23104\n\t"
"v_mfma_f32_16x16x32_bf16 v[40:43], a[136:139], a[152:155], v[40:43]\n\t"
"ds_read_b128 a[228:231], v255 offset:62208\n\t"
"v_mfma_f32_16x16x32_bf16 v[40:43], a[140:143], a[168:171], v[40:43]\n\t"
"buffer_load_dwordx4 a[76:79], v252, s[8:11], s66 offen offset:3072 sc1\n\t"
"v_mfma_f32_16x16x32_bf16 v[192:195], a[212:215], a[176:179], 0\n\t"
"ds_read_b128 a[168:171], v224 offset:23616\n\t"
"v_mfma_f32_16x16x32_bf16 v[44:47], a[136:139], a[156:159], v[44:47]\n\t"
"ds_read_b64_tr_b16 a[176:177], v248 offset:1152\n\t"
"v_mfma_f32_16x16x32_bf16 v[44:47], a[140:143], a[172:175], v[44:47]\n\t"
"buffer_load_dwordx4 a[112:115], v235, s[8:11], s53 offen offset:0 sc1\n\t"
"v_mfma_f32_16x16x32_bf16 v[196:199], a[212:215], a[184:187], 0\n\t"
"ds_read_b64_tr_b16 a[184:185], v249 offset:1152\n\t"
"v_mfma_f32_16x16x32_bf16 v[28:31], a[128:131], a[156:159], v[28:31]\n\t"
"ds_read_b128 a[152:155], v224 offset:19520\n\t"
"v_mfma_f32_16x16x32_bf16 v[28:31], a[132:135], a[172:175], v[28:31]\n\t"
"buffer_load_dwordx4 a[116:119], v235, s[8:11], s53 offen offset:1024 sc1\n\t"
"v_mfma_f32_16x16x32_bf16 v[12:15], a[120:123], a[156:159], v[12:15]\n\t"
"ds_read_b128 a[156:159], v224 offset:20032\n\t"
"v_mfma_f32_16x16x32_bf16 v[12:15], a[124:127], a[172:175], v[12:15]\n\t"
"buffer_load_dwordx4 a[108:111], v252, s[8:11], s52 offen offset:3072 sc1\n\t"
"v_mfma_f32_16x16x32_bf16 v[200:203], a[212:215], a[178:181], 0\n\t"
"s_waitcnt vmcnt(21) lgkmcnt(5)\n\t"
"ds_write_b128 v255, a[228:231] offset:23040\n\t"
"v_mfma_f32_16x16x32_bf16 v[48:51], a[0:3], a[144:147], v[48:51]\n\t"
"v_cvt_pk_bf16_f32 v192, v192, v196\n\t"
"v_cvt_pk_bf16_f32 v193, v193, v197\n\t"
"v_mfma_f32_16x16x32_bf16 v[48:51], a[4:7], a[160:163], v[48:51]\n\t"
"buffer_load_dwordx4 a[104:107], v252, s[8:11], s52 offen offset:2048 sc1\n\t"
"v_mfma_f32_16x16x32_bf16 v[204:207], a[212:215], a[186:189], 0\n\t"
"v_cvt_pk_bf16_f32 v194, v194, v198\n\t"
"v_cvt_pk_bf16_f32 v195, v195, v199\n\t"
"v_mfma_f32_16x16x32_bf16 v[64:67], a[8:11], a[144:147], v[64:67]\n\t"
"s_nop 0\n\t"
"ds_read_b128 a[172:175], v224 offset:24128\n\t"
"ds_read_b64_tr_b16 a[178:179], v250 offset:3456\n\t"
"v_mfma_f32_16x16x32_bf16 v[64:67], a[12:15], a[160:163], v[64:67]\n\t"
"buffer_load_dwordx4 a[96:99], v252, s[8:11], s52 offen offset:0 sc1\n\t"
"v_mfma_f32_16x16x32_bf16 v[80:83], a[16:19], a[144:147], v[80:83]\n\t"
"ds_write_addtid_b32 v195 offset:2304\n\t"
"v_mfma_f32_16x16x32_bf16 v[80:83], a[20:23], a[160:163], v[80:83]\n\t"
"s_nop 0\n\t"
"ds_write_addtid_b32 v193 offset:256\n\t"
"ds_read_b64_tr_b16 a[180:181], v248 offset:5760\n\t"
"v_mfma_f32_16x16x32_bf16 v[208:211], a[212:215], a[192:195], 0\n\t"
"buffer_load_dwordx4 a[100:103], v252, s[8:11], s52 offen offset:1024 sc1\n\t"
"v_mfma_f32_16x16x32_bf16 v[84:87], a[16:19], a[148:151], v[84:87]\n\t"
"ds_write_addtid_b32 v194 offset:2048\n\t"
"v_cvt_pk_bf16_f32 v202, v202, v206\n\t"
"v_mfma_f32_16x16x32_bf16 v[84:87], a[20:23], a[164:167], v[84:87]\n\t"
"ds_read_b128 a[160:163], v224 offset:20544\n\t"
"v_mfma_f32_16x16x32_bf16 v[68:71], a[8:11], a[148:151], v[68:71]\n\t"
"s_nop 0\n\t"
"ds_read_b128 a[144:147], v224 offset:16448\n\t"
"ds_write_addtid_b32 v192 offset:0\n\t"
"v_mfma_f32_16x16x32_bf16 v[68:71], a[12:15], a[164:167], v[68:71]\n\t"
"buffer_load_dwordx4 a[136:139], v235, s[8:11], s52 offen offset:2048 sc1\n\t"
"v_mfma_f32_16x16x32_bf16 v[212:215], a[212:215], a[200:203], 0\n\t"
"ds_read_b64_tr_b16 a[186:187], v251 offset:3456\n\t"
"v_cvt_pk_bf16_f32 v203, v203, v207\n\t"
"v_mfma_f32_16x16x32_bf16 v[52:55], a[0:3], a[148:151], v[52:55]\n\t"
"ds_write_addtid_b32 v202 offset:10272\n\t"
"v_cvt_pk_bf16_f32 v200, v200, v204\n\t"
"v_mfma_f32_16x16x32_bf16 v[52:55], a[4:7], a[164:167], v[52:55]\n\t"
"buffer_load_dwordx4 a[140:143], v235, s[8:11], s52 offen offset:3072 sc1\n\t"
"v_mfma_f32_16x16x32_bf16 v[216:219], a[212:215], a[194:197], 0\n\t"
"s_waitcnt lgkmcnt(8)\n\t"
"ds_read_b128 a[164:167], v224 offset:21056\n\t"
"ds_write_addtid_b32 v203 offset:10528\n\t"
"v_mfma_f32_16x16x32_bf16 v[56:59], a[0:3], a[152:155], v[56:59]\n\t"
"ds_write_addtid_b32 v200 offset:8224\n\t"
"v_cvt_pk_bf16_f32 v209, v209, v213\n\t"
"v_mfma_f32_16x16x32_bf16 v[56:59], a[4:7], a[168:171], v[56:59]\n\t"
"s_nop 0\n\t"
"ds_read_b64_tr_b16 a[188:189], v249 offset:5760\n\t"
"ds_read_b64_tr_b16 a[192:193], v250 offset:1152\n\t"
"v_mfma_f32_16x16x32_bf16 v[72:75], a[8:11], a[152:155], v[72:75]\n\t"
"ds_read_b128 a[148:151], v224 offset:16960\n\t"
"v_cvt_pk_bf16_f32 v201, v201, v205\n\t"
"v_mfma_f32_16x16x32_bf16 v[72:75], a[12:15], a[168:171], v[72:75]\n\t"
"buffer_load_dwordx4 a[132:135], v235, s[8:11], s52 offen offset:1024 sc1\n\t"
"v_mfma_f32_16x16x32_bf16 v[220:223], a[212:215], a[202:205], 0\n\t"
"ds_read_b64_tr_b16 a[200:201], v251 offset:1152\n\t"
"v_cvt_pk_bf16_f32 v208, v208, v212\n\t"
"v_mfma_f32_16x16x32_bf16 v[88:91], a[16:19], a[152:155], v[88:91]\n\t"
"ds_write_addtid_b32 v201 offset:8480\n\t"
"v_cvt_pk_bf16_f32 v211, v211, v215\n\t"
"v_mfma_f32_16x16x32_bf16 v[88:91], a[20:23], a[168:171], v[88:91]\n\t"
"buffer_load_dwordx4 a[128:131], v235, s[8:11], s52 offen offset:0 sc1\n\t"
"v_mfma_f32_16x16x32_bf16 v[92:95], a[16:19], a[156:159], v[92:95]\n\t"
"ds_write_addtid_b32 v211 offset:6400\n\t"
"v_mfma_f32_16x16x32_bf16 v[92:95], a[20:23], a[172:175], v[92:95]\n\t"
"ds_read_b64_tr_b16 a[194:195], v248 offset:3456\n\t"
"v_cvt_pk_bf16_f32 v218, v218, v222\n\t"
"v_mfma_f32_16x16x32_bf16 v[76:79], a[8:11], a[156:159], v[76:79]\n\t"
"ds_write_addtid_b32 v209 offset:4352\n\t"
"v_cvt_pk_bf16_f32 v216, v216, v220\n\t"
"v_mfma_f32_16x16x32_bf16 v[76:79], a[12:15], a[172:175], v[76:79]\n\t"
"buffer_load_dwordx4 a[120:123], v252, s[8:11], s51 offen offset:2048 sc1\n\t"
"v_mfma_f32_16x16x32_bf16 v[60:63], a[0:3], a[156:159], v[60:63]\n\t"
"ds_write_addtid_b32 v208 offset:4096\n\t"
"v_cvt_pk_bf16_f32 v210, v210, v214\n\t"
"v_mfma_f32_16x16x32_bf16 v[60:63], a[4:7], a[172:175], v[60:63]\n\t"
"s_waitcnt vmcnt(23) lgkmcnt(6)\n\t"
"ds_write_addtid_b32 v216 offset:12320\n\t"
"ds_write_addtid_b32 v218 offset:14368\n\t"
"v_mfma_f32_16x16x32_bf16 v[0:3], a[24:27], a[144:147], v[0:3]\n\t"
"ds_write_addtid_b32 v210 offset:6144\n\t"
"v_cvt_pk_bf16_f32 v217, v217, v221\n\t"
"v_mfma_f32_16x16x32_bf16 v[0:3], a[28:31], a[160:163], v[0:3]\n\t"
"buffer_load_dwordx4 a[124:127], v252, s[8:11], s51 offen offset:3072 sc1\n\t"
"v_mfma_f32_16x16x32_bf16 v[16:19], a[32:35], a[144:147], v[16:19]\n\t"
"ds_write_addtid_b32 v217 offset:12576\n\t"
"v_cvt_pk_bf16_f32 v219, v219, v223\n\t"
"v_mfma_f32_16x16x32_bf16 v[16:19], a[36:39], a[160:163], v[16:19]\n\t"
"ds_write_addtid_b32 v219 offset:14624\n\t"
"v_mfma_f32_16x16x32_bf16 v[32:35], a[40:43], a[144:147], v[32:35]\n\t"
"ds_read_b128 a[152:155], v224 offset:17472\n\t"
"v_mfma_f32_16x16x32_bf16 v[32:35], a[44:47], a[160:163], v[32:35]\n\t"
"buffer_load_dwordx4 a[16:19], v252, s[84:87], s69 offen offset:0 sc1\n\t"
"v_mfma_f32_16x16x32_bf16 v[36:39], a[40:43], a[148:151], v[36:39]\n\t"
"s_nop 0\n\t"
"ds_read_b64_tr_b16 a[196:197], v250 offset:5760\n\t"
"ds_read_b64_tr_b16 a[202:203], v249 offset:3456\n\t"
"v_mfma_f32_16x16x32_bf16 v[36:39], a[44:47], a[164:167], v[36:39]\n\t"
"s_nop 0\n\t"
"ds_read_b128 a[172:175], v224 offset:22080\n\t"
"ds_read_b64_tr_b16 a[204:205], v251 offset:5760\n\t"
"v_mfma_f32_16x16x32_bf16 v[20:23], a[32:35], a[148:151], v[20:23]\n\t"
"s_nop 0\n\t"
"ds_read_b128 a[168:171], v224 offset:21568\n\t"
"ds_read_b128 a[156:159], v224 offset:17984\n\t"
"v_mfma_f32_16x16x32_bf16 v[20:23], a[36:39], a[164:167], v[20:23]\n\t"
"buffer_load_dwordx4 a[20:23], v252, s[84:87], s69 offen offset:1024 sc1\n\t"
"v_mfma_f32_16x16x32_bf16 v[4:7], a[24:27], a[148:151], v[4:7]\n\t"
"v_mfma_f32_16x16x32_bf16 v[4:7], a[28:31], a[164:167], v[4:7]\n\t"
"s_waitcnt lgkmcnt(0)\n\t"
"s_barrier\n\t"
"v_mfma_f32_16x16x32_bf16 v[8:11], a[24:27], a[152:155], v[8:11]\n\t"
"s_add_u32 m0, s26, 124544\n\t"
"ds_read_b128 a[148:151], v224 offset:2560\n\t"
"v_add_u32 v248, 32896, v227\n\t"
"v_mfma_f32_16x16x32_bf16 v[8:11], a[28:31], a[168:171], v[8:11]\n\t"
"ds_read_b128 a[144:147], v224 offset:2048\n\t"
"v_mfma_f32_16x16x32_bf16 v[24:27], a[32:35], a[152:155], v[24:27]\n\t"
"ds_read_b128 a[160:163], v224 offset:6144\n\t"
"v_xor_b32 v249, 32, v248\n\t"
"v_mfma_f32_16x16x32_bf16 v[24:27], a[36:39], a[168:171], v[24:27]\n\t"
"ds_read_b128 a[164:167], v224 offset:6656\n\t"
"v_xor_b32 v250, 64, v248\n\t"
"v_mfma_f32_16x16x32_bf16 v[40:43], a[40:43], a[152:155], v[40:43]\n\t"
"buffer_load_dwordx4 v236, s[12:15], s22 offen offset:128 nt lds\n\t"
"v_mfma_f32_16x16x32_bf16 v[40:43], a[44:47], a[168:171], v[40:43]\n\t"
"ds_read_b128 a[152:155], v224 offset:3072\n\t"
"v_xor_b32 v251, 96, v248\n\t"
"v_mfma_f32_16x16x32_bf16 v[192:195], a[212:215], a[176:179], 0\n\t"
"s_nop 0\n\t"
"ds_read_b64_tr_b16 a[182:183], v249 offset:21248\n\t"
"ds_read_b64_tr_b16 a[190:191], v251 offset:21248\n\t"
"v_mfma_f32_16x16x32_bf16 v[44:47], a[40:43], a[156:159], v[44:47]\n\t"
"ds_read_b128 a[168:171], v224 offset:7168\n\t"
"v_mfma_f32_16x16x32_bf16 v[44:47], a[44:47], a[172:175], v[44:47]\n\t"
"buffer_load_dwordx4 v237, s[12:15], s22 offen offset:1152 nt lds\n\t"
"v_mfma_f32_16x16x32_bf16 v[196:199], a[212:215], a[184:187], 0\n\t"
"ds_read_b64_tr_b16 a[198:199], v249 offset:22400\n\t"
"v_mfma_f32_16x16x32_bf16 v[28:31], a[32:35], a[156:159], v[28:31]\n\t"
"ds_read_b64_tr_b16 a[206:207], v251 offset:22400\n\t"
"v_mfma_f32_16x16x32_bf16 v[28:31], a[36:39], a[172:175], v[28:31]\n\t"
"s_nop 0\n\t"
"ds_read_b64_tr_b16 a[176:177], v248 offset:20736\n\t"
"ds_read_b64_tr_b16 a[184:185], v250 offset:20736\n\t"
"v_mfma_f32_16x16x32_bf16 v[12:15], a[24:27], a[156:159], v[12:15]\n\t"
"ds_read_b128 a[156:159], v224 offset:3584\n\t"
"v_mfma_f32_16x16x32_bf16 v[12:15], a[28:31], a[172:175], v[12:15]\n\t"
"buffer_load_dwordx4 v238, s[12:15], s22 offen offset:2176 nt lds\n\t"
"v_mfma_f32_16x16x32_bf16 v[200:203], a[212:215], a[178:181], 0\n\t"
"s_waitcnt vmcnt(23) lgkmcnt(9)\n\t"
"ds_read_b128 a[172:175], v224 offset:7680\n\t"
"v_cvt_pk_bf16_f32 v193, v193, v197\n\t"
"v_mfma_f32_16x16x32_bf16 v[144:147], a[48:51], a[144:147], v[144:147]\n\t"
"s_lshl_b32 m0, s73, 9\n\t"
"v_cvt_pk_bf16_f32 v192, v192, v196\n\t"
"v_cvt_pk_bf16_f32 v194, v194, v198\n\t"
"v_mfma_f32_16x16x32_bf16 v[144:147], a[52:55], a[160:163], v[144:147]\n\t"
"ds_read_b64_tr_b16 a[178:179], v248 offset:21248\n\t"
"v_cvt_pk_bf16_f32 v195, v195, v199\n\t"
"v_mfma_f32_16x16x32_bf16 v[204:207], a[212:215], a[186:189], 0\n\t"
"s_nop 0\n\t"
"ds_write_addtid_b32 v192 offset:16448\n\t"
"ds_write_addtid_b32 v193 offset:16704\n\t"
"v_pk_add_f32 v[0:1], v[0:1], v[48:49]\n\t"
"v_pk_add_f32 v[2:3], v[2:3], v[50:51]\n\t"
"v_pk_fma_f32 v[48:49], v[48:49], s[38:39], v[0:1]\n\t"
"v_pk_fma_f32 v[50:51], v[50:51], s[38:39], v[2:3]\n\t"
"v_pk_add_f32 v[16:17], v[16:17], v[64:65]\n\t"
"v_pk_add_f32 v[18:19], v[18:19], v[66:67]\n\t"
"v_pk_fma_f32 v[64:65], v[64:65], s[38:39], v[16:17]\n\t"
"v_pk_fma_f32 v[66:67], v[66:67], s[38:39], v[18:19]\n\t"
"v_pk_add_f32 v[32:33], v[32:33], v[80:81]\n\t"
"v_pk_add_f32 v[34:35], v[34:35], v[82:83]\n\t"
"v_pk_fma_f32 v[80:81], v[80:81], s[38:39], v[32:33]\n\t"
"v_pk_fma_f32 v[82:83], v[82:83], s[38:39], v[34:35]\n\t"
"v_pk_add_f32 v[36:37], v[36:37], v[84:85]\n\t"
"v_pk_add_f32 v[38:39], v[38:39], v[86:87]\n\t"
"v_pk_fma_f32 v[84:85], v[84:85], s[38:39], v[36:37]\n\t"
"v_pk_fma_f32 v[86:87], v[86:87], s[38:39], v[38:39]\n\t"
"v_pk_add_f32 v[20:21], v[20:21], v[68:69]\n\t"
"v_pk_add_f32 v[22:23], v[22:23], v[70:71]\n\t"
"v_pk_fma_f32 v[68:69], v[68:69], s[38:39], v[20:21]\n\t"
"v_pk_fma_f32 v[70:71], v[70:71], s[38:39], v[22:23]\n\t"
"v_pk_add_f32 v[4:5], v[4:5], v[52:53]\n\t"
"v_pk_add_f32 v[6:7], v[6:7], v[54:55]\n\t"
"v_pk_fma_f32 v[52:53], v[52:53], s[38:39], v[4:5]\n\t"
"v_pk_fma_f32 v[54:55], v[54:55], s[38:39], v[6:7]\n\t"
"v_pk_add_f32 v[8:9], v[8:9], v[56:57]\n\t"
"v_pk_add_f32 v[10:11], v[10:11], v[58:59]\n\t"
"v_pk_fma_f32 v[56:57], v[56:57], s[38:39], v[8:9]\n\t"
"v_pk_fma_f32 v[58:59], v[58:59], s[38:39], v[10:11]\n\t"
"v_pk_add_f32 v[24:25], v[24:25], v[72:73]\n\t"
"v_pk_add_f32 v[26:27], v[26:27], v[74:75]\n\t"
"v_pk_fma_f32 v[72:73], v[72:73], s[38:39], v[24:25]\n\t"
"v_pk_fma_f32 v[74:75], v[74:75], s[38:39], v[26:27]\n\t"
"v_pk_add_f32 v[40:41], v[40:41], v[88:89]\n\t"
"v_pk_add_f32 v[42:43], v[42:43], v[90:91]\n\t"
"v_pk_fma_f32 v[88:89], v[88:89], s[38:39], v[40:41]\n\t"
"v_pk_fma_f32 v[90:91], v[90:91], s[38:39], v[42:43]\n\t"
"v_pk_add_f32 v[44:45], v[44:45], v[92:93]\n\t"
"v_pk_add_f32 v[46:47], v[46:47], v[94:95]\n\t"
"v_pk_fma_f32 v[92:93], v[92:93], s[38:39], v[44:45]\n\t"
"v_pk_fma_f32 v[94:95], v[94:95], s[38:39], v[46:47]\n\t"
"v_pk_add_f32 v[28:29], v[28:29], v[76:77]\n\t"
"v_pk_add_f32 v[30:31], v[30:31], v[78:79]\n\t"
"v_pk_fma_f32 v[76:77], v[76:77], s[38:39], v[28:29]\n\t"
"v_pk_fma_f32 v[78:79], v[78:79], s[38:39], v[30:31]\n\t"
"v_pk_add_f32 v[12:13], v[12:13], v[60:61]\n\t"
"v_pk_add_f32 v[14:15], v[14:15], v[62:63]\n\t"
"v_pk_fma_f32 v[60:61], v[60:61], s[38:39], v[12:13]\n\t"
"v_pk_fma_f32 v[62:63], v[62:63], s[38:39], v[14:15]\n\t"
"v_mfma_f32_16x16x32_bf16 v[160:163], a[56:59], a[144:147], v[160:163]\n\t"
"ds_write_addtid_b32 v194 offset:18496\n\t"
"v_cvt_pk_bf16_f32 v0, v0, v1\n\t"
"v_mfma_f32_16x16x32_bf16 v[160:163], a[60:63], a[160:163], v[160:163]\n\t"
"ds_write_addtid_b32 v195 offset:18752\n\t"
"s_add_u32 m0, s26, 124544\n\t"
"s_nop 0\n\t"
"buffer_load_dwordx4 v239, s[12:15], s22 offen offset:3200 nt lds\n\t"
"v_cvt_pk_bf16_f32 v4, v4, v5\n\t"
"v_cvt_pk_bf16_f32 v5, v6, v7\n\t"
"v_mfma_f32_16x16x32_bf16 v[176:179], a[64:67], a[144:147], v[176:179]\n\t"
"v_cvt_pk_bf16_f32 v1, v2, v3\n\t"
"v_cvt_pk_bf16_f32 v20, v20, v21\n\t"
"v_mfma_f32_16x16x32_bf16 v[176:179], a[68:71], a[160:163], v[176:179]\n\t"
"ds_read_b128 a[144:147], v224 offset:0\n\t"
"ds_write2st64_b64 v228, v[0:1], v[4:5] offset0:39 offset1:52\n\t"
"v_mfma_f32_16x16x32_bf16 v[208:211], a[212:215], a[192:195], 0\n\t"
"v_cvt_pk_bf16_f32 v21, v22, v23\n\t"
"v_cvt_pk_bf16_f32 v32, v32, v33\n\t"
"v_mfma_f32_16x16x32_bf16 v[180:183], a[64:67], a[148:151], v[180:183]\n\t"
"v_cvt_pk_bf16_f32 v16, v16, v17\n\t"
"v_cvt_pk_bf16_f32 v17, v18, v19\n\t"
"v_mfma_f32_16x16x32_bf16 v[180:183], a[68:71], a[164:167], v[180:183]\n\t"
"ds_write2st64_b64 v246, v[16:17], v[20:21] offset0:39 offset1:52\n\t"
"v_mfma_f32_16x16x32_bf16 v[164:167], a[56:59], a[148:151], v[164:167]\n\t"
"v_cvt_pk_bf16_f32 v201, v201, v205\n\t"
"v_cvt_pk_bf16_f32 v36, v36, v37\n\t"
"v_mfma_f32_16x16x32_bf16 v[164:167], a[60:63], a[164:167], v[164:167]\n\t"
"s_mov_b64 exec, s[36:37]\n\t"
"buffer_load_dwordx4 v240, s[12:15], s22 offen offset:3712 nt lds\n\t"
"s_mov_b64 exec, -1\n\t"
"v_mfma_f32_16x16x32_bf16 v[212:215], a[212:215], a[200:203], 0\n\t"
"s_lshl_b32 m0, s73, 9\n\t"
"v_cvt_pk_bf16_f32 v200, v200, v204\n\t"
"v_cvt_pk_bf16_f32 v37, v38, v39\n\t"
"v_mfma_f32_16x16x32_bf16 v[148:151], a[48:51], a[148:151], v[148:151]\n\t"
"v_cvt_pk_bf16_f32 v33, v34, v35\n\t"
"v_cvt_pk_bf16_f32 v12, v12, v13\n\t"
"v_mfma_f32_16x16x32_bf16 v[148:151], a[52:55], a[164:167], v[148:151]\n\t"
"ds_read_b128 a[160:163], v224 offset:4096\n\t"
"ds_write2st64_b64 v247, v[32:33], v[36:37] offset0:39 offset1:52\n\t"
"v_mfma_f32_16x16x32_bf16 v[216:219], a[212:215], a[194:197], 0\n\t"
"s_waitcnt lgkmcnt(5)\n\t"
"v_cvt_pk_bf16_f32 v203, v203, v207\n\t"
"v_cvt_pk_bf16_f32 v24, v24, v25\n\t"
"v_mfma_f32_16x16x32_bf16 v[152:155], a[48:51], a[152:155], v[152:155]\n\t"
"v_cvt_pk_bf16_f32 v202, v202, v206\n\t"
"v_cvt_pk_bf16_f32 v8, v8, v9\n\t"
"v_mfma_f32_16x16x32_bf16 v[152:155], a[52:55], a[168:171], v[152:155]\n\t"
"s_nop 0\n\t"
"ds_read_b128 a[148:151], v224 offset:512\n\t"
"ds_read_b128 a[164:167], v224 offset:4608\n\t"
"ds_write_addtid_b32 v200 offset:24672\n\t"
"ds_write_addtid_b32 v201 offset:24928\n\t"
"ds_write_addtid_b32 v202 offset:26720\n\t"
"ds_write_addtid_b32 v203 offset:26976\n\t"
"v_mfma_f32_16x16x32_bf16 v[168:171], a[56:59], a[152:155], v[168:171]\n\t"
"v_cvt_pk_bf16_f32 v9, v10, v11\n\t"
"v_cvt_pk_bf16_f32 v13, v14, v15\n\t"
"v_mfma_f32_16x16x32_bf16 v[168:171], a[60:63], a[168:171], v[168:171]\n\t"
"ds_read_b64_tr_b16 a[180:181], v249 offset:20736\n\t"
"ds_write2st64_b64 v228, v[8:9], v[12:13] offset0:65 offset1:78\n\t"
"v_mfma_f32_16x16x32_bf16 v[220:223], a[212:215], a[202:205], 0\n\t"
"v_cvt_pk_bf16_f32 v28, v28, v29\n\t"
"v_cvt_pk_bf16_f32 v29, v30, v31\n\t"
"v_mfma_f32_16x16x32_bf16 v[184:187], a[64:67], a[152:155], v[184:187]\n\t"
"v_cvt_pk_bf16_f32 v209, v209, v213\n\t"
"v_cvt_pk_bf16_f32 v25, v26, v27\n\t"
"v_mfma_f32_16x16x32_bf16 v[184:187], a[68:71], a[168:171], v[184:187]\n\t"
"ds_write2st64_b64 v246, v[24:25], v[28:29] offset0:65 offset1:78\n\t"
"v_mfma_f32_16x16x32_bf16 v[188:191], a[64:67], a[156:159], v[188:191]\n\t"
"v_cvt_pk_bf16_f32 v40, v40, v41\n\t"
"v_cvt_pk_bf16_f32 v44, v44, v45\n\t"
"v_mfma_f32_16x16x32_bf16 v[188:191], a[68:71], a[172:175], v[188:191]\n\t"
"v_cvt_pk_bf16_f32 v216, v216, v220\n\t"
"v_cvt_pk_bf16_f32 v41, v42, v43\n\t"
"v_mfma_f32_16x16x32_bf16 v[172:175], a[56:59], a[156:159], v[172:175]\n\t"
"v_cvt_pk_bf16_f32 v211, v211, v215\n\t"
"v_cvt_pk_bf16_f32 v45, v46, v47\n\t"
"v_mfma_f32_16x16x32_bf16 v[172:175], a[60:63], a[172:175], v[172:175]\n\t"
"ds_read_b64_tr_b16 a[186:187], v250 offset:21248\n\t"
"ds_write2st64_b64 v247, v[40:41], v[44:45] offset0:65 offset1:78\n\t"
"v_mfma_f32_16x16x32_bf16 v[156:159], a[48:51], a[156:159], v[156:159]\n\t"
"v_cvt_pk_bf16_f32 v210, v210, v214\n\t"
"v_cvt_pk_bf16_f32 v218, v218, v222\n\t"
"v_mfma_f32_16x16x32_bf16 v[156:159], a[52:55], a[172:175], v[156:159]\n\t"
"s_waitcnt vmcnt(19) lgkmcnt(5)\n\t"
"v_cvt_pk_bf16_f32 v219, v219, v223\n\t"
"v_mfma_f32_16x16x32_bf16 v[96:99], a[72:75], a[144:147], v[96:99]\n\t"
"v_cvt_pk_bf16_f32 v208, v208, v212\n\t"
"v_cvt_pk_bf16_f32 v217, v217, v221\n\t"
"v_mfma_f32_16x16x32_bf16 v[96:99], a[76:79], a[160:163], v[96:99]\n\t"
"s_nop 0\n\t"
"ds_write_addtid_b32 v209 offset:20800\n\t"
"ds_write_addtid_b32 v210 offset:22592\n\t"
"ds_write_addtid_b32 v211 offset:22848\n\t"
"v_mfma_f32_16x16x32_bf16 v[112:115], a[80:83], a[144:147], v[112:115]\n\t"
"s_nop 0\n\t"
"ds_write_addtid_b32 v208 offset:20544\n\t"
"ds_write_addtid_b32 v218 offset:30816\n\t"
"v_mfma_f32_16x16x32_bf16 v[112:115], a[84:87], a[160:163], v[112:115]\n\t"
"s_nop 0\n\t"
"ds_read_b64_tr_b16 a[188:189], v251 offset:20736\n\t"
"ds_read_b64_tr_b16 a[192:193], v248 offset:21888\n\t"
"v_mfma_f32_16x16x32_bf16 v[128:131], a[88:91], a[144:147], v[128:131]\n\t"
"s_nop 0\n\t"
"ds_read_b128 a[152:155], v224 offset:1024\n\t"
"ds_write_addtid_b32 v219 offset:31072\n\t"
"v_mfma_f32_16x16x32_bf16 v[128:131], a[92:95], a[160:163], v[128:131]\n\t"
"s_nop 0\n\t"
"ds_read_b128 a[172:175], v224 offset:5632\n\t"
"ds_write_addtid_b32 v216 offset:28768\n\t"
"ds_write_addtid_b32 v217 offset:29024\n\t"
"v_mfma_f32_16x16x32_bf16 v[132:135], a[88:91], a[148:151], v[132:135]\n\t"
"s_nop 0\n\t"
"ds_read_b64_tr_b16 a[200:201], v250 offset:21888\n\t"
"ds_read_b64_tr_b16 a[194:195], v248 offset:22400\n\t"
"v_mfma_f32_16x16x32_bf16 v[132:135], a[92:95], a[164:167], v[132:135]\n\t"
"s_nop 0\n\t"
"ds_read_b64_tr_b16 a[196:197], v249 offset:21888\n\t"
"ds_read_b64_tr_b16 a[202:203], v250 offset:22400\n\t"
"ds_read_b64_tr_b16 a[204:205], v251 offset:21888\n\t"
"v_mfma_f32_16x16x32_bf16 v[116:119], a[80:83], a[148:151], v[116:119]\n\t"
"s_nop 0\n\t"
"ds_read_b128 a[168:171], v224 offset:5120\n\t"
"ds_read_b128 a[156:159], v224 offset:1536\n\t"
"v_mfma_f32_16x16x32_bf16 v[116:119], a[84:87], a[164:167], v[116:119]\n\t"
"buffer_load_dwordx4 a[12:15], v235, s[84:87], s69 offen offset:3072 sc1\n\t"
"v_mfma_f32_16x16x32_bf16 v[100:103], a[72:75], a[148:151], v[100:103]\n\t"
"v_mfma_f32_16x16x32_bf16 v[100:103], a[76:79], a[164:167], v[100:103]\n\t"
"s_waitcnt lgkmcnt(0)\n\t"
"s_barrier\n\t"
"s_cmp_eq_u32 s21, 1\n\t"
"s_cbranch_scc1 Ltail_%=\n\t"
"v_mfma_f32_16x16x32_bf16 v[104:107], a[72:75], a[152:155], v[104:107]\n\t"
"ds_read_b128 a[144:147], v224 offset:18496\n\t"
"v_mfma_f32_16x16x32_bf16 v[104:107], a[76:79], a[168:171], v[104:107]\n\t"
"ds_read_b128 a[148:151], v224 offset:19008\n\t"
"v_mfma_f32_16x16x32_bf16 v[120:123], a[80:83], a[152:155], v[120:123]\n\t"
"ds_read_b128 a[160:163], v224 offset:22592\n\t"
"v_mfma_f32_16x16x32_bf16 v[120:123], a[84:87], a[168:171], v[120:123]\n\t"
"ds_read_b128 a[164:167], v224 offset:23104\n\t"
"v_mfma_f32_16x16x32_bf16 v[136:139], a[88:91], a[152:155], v[136:139]\n\t"
"ds_read_b128 a[152:155], v224 offset:19520\n\t"
"v_mfma_f32_16x16x32_bf16 v[136:139], a[92:95], a[168:171], v[136:139]\n\t"
"ds_read_b128 v[8:11], v229 offset:19968\n\t"
"v_mfma_f32_16x16x32_bf16 v[192:195], a[224:227], a[176:179], 0\n\t"
"ds_read_b64_tr_b16 a[176:177], v248 offset:0\n\t"
"v_mfma_f32_16x16x32_bf16 v[140:143], a[88:91], a[156:159], v[140:143]\n\t"
"ds_read_b128 a[168:171], v224 offset:23616\n\t"
"v_mfma_f32_16x16x32_bf16 v[140:143], a[92:95], a[172:175], v[140:143]\n\t"
"ds_read_b64_tr_b16 a[178:179], v248 offset:512\n\t"
"v_mfma_f32_16x16x32_bf16 v[196:199], a[224:227], a[180:183], 0\n\t"
"ds_read_b64_tr_b16 a[180:181], v249 offset:0\n\t"
"v_mfma_f32_16x16x32_bf16 v[124:127], a[80:83], a[156:159], v[124:127]\n\t"
"ds_read_b64_tr_b16 a[182:183], v249 offset:512\n\t"
"v_mfma_f32_16x16x32_bf16 v[124:127], a[84:87], a[172:175], v[124:127]\n\t"
"s_waitcnt lgkmcnt(5)\n\t"
"buffer_store_dwordx4 v[8:11], v232, s[16:19], s24 offen nt\n\t"
"v_mfma_f32_16x16x32_bf16 v[108:111], a[72:75], a[156:159], v[108:111]\n\t"
"ds_read_b128 a[156:159], v224 offset:20032\n\t"
"v_mfma_f32_16x16x32_bf16 v[108:111], a[76:79], a[172:175], v[108:111]\n\t"
"ds_read_b128 a[172:175], v224 offset:24128\n\t"
"v_cvt_pk_bf16_f32 v192, v192, v196\n\t"
"v_mfma_f32_16x16x32_bf16 v[200:203], a[224:227], a[184:187], 0\n\t"
"s_waitcnt vmcnt(15)\n\t"
"ds_read_b128 v[24:27], v230 offset:19968\n\t"
"v_cvt_pk_bf16_f32 v195, v195, v199\n\t"
"v_mfma_f32_16x16x32_bf16 v[144:147], a[96:99], a[144:147], v[144:147]\n\t"
"s_nop 0\n\t"
"ds_read_b64_tr_b16 a[184:185], v250 offset:0\n\t"
"ds_read_b64_tr_b16 a[186:187], v250 offset:512\n\t"
"v_mfma_f32_16x16x32_bf16 v[144:147], a[100:103], a[160:163], v[144:147]\n\t"
"ds_write_addtid_b32 v195 offset:8480\n\t"
"v_cvt_pk_bf16_f32 v194, v194, v198\n\t"
"v_mfma_f32_16x16x32_bf16 v[204:207], a[224:227], a[188:191], 0\n\t"
"ds_write_addtid_b32 v192 offset:0\n\t"
"v_mfma_f32_16x16x32_bf16 v[160:163], a[104:107], a[144:147], v[160:163]\n\t"
"ds_write_addtid_b32 v194 offset:8224\n\t"
"v_cvt_pk_bf16_f32 v193, v193, v197\n\t"
"v_mfma_f32_16x16x32_bf16 v[160:163], a[108:111], a[160:163], v[160:163]\n\t"
"ds_read_b64_tr_b16 a[188:189], v251 offset:0\n\t"
"v_mfma_f32_16x16x32_bf16 v[176:179], a[112:115], a[144:147], v[176:179]\n\t"
"s_nop 0\n\t"
"ds_read_b128 a[144:147], v224 offset:16448\n\t"
"ds_read_b64_tr_b16 a[190:191], v251 offset:512\n\t"
"v_mfma_f32_16x16x32_bf16 v[176:179], a[116:119], a[160:163], v[176:179]\n\t"
"ds_write_addtid_b32 v193 offset:256\n\t"
"v_cvt_pk_bf16_f32 v203, v203, v207\n\t"
"v_mfma_f32_16x16x32_bf16 v[208:211], a[224:227], a[192:195], 0\n\t"
"ds_write_addtid_b32 v203 offset:12576\n\t"
"v_cvt_pk_bf16_f32 v202, v202, v206\n\t"
"v_mfma_f32_16x16x32_bf16 v[180:183], a[112:115], a[148:151], v[180:183]\n\t"
"ds_read_b64_tr_b16 a[192:193], v248 offset:1152\n\t"
"v_cvt_pk_bf16_f32 v200, v200, v204\n\t"
"v_mfma_f32_16x16x32_bf16 v[180:183], a[116:119], a[164:167], v[180:183]\n\t"
"buffer_load_dwordx4 a[8:11], v235, s[84:87], s69 offen offset:2048 sc1\n\t"
"v_mfma_f32_16x16x32_bf16 v[164:167], a[104:107], a[148:151], v[164:167]\n\t"
"ds_write_addtid_b32 v202 offset:12320\n\t"
"v_cvt_pk_bf16_f32 v201, v201, v205\n\t"
"v_mfma_f32_16x16x32_bf16 v[164:167], a[108:111], a[164:167], v[164:167]\n\t"
"buffer_load_dwordx4 a[0:3], v235, s[84:87], s69 offen offset:0 sc1\n\t"
"v_mfma_f32_16x16x32_bf16 v[212:215], a[224:227], a[196:199], 0\n\t"
"s_nop 0\n\t"
"ds_write_addtid_b32 v200 offset:4096\n\t"
"ds_read_b64_tr_b16 a[194:195], v248 offset:1664\n\t"
"v_mfma_f32_16x16x32_bf16 v[148:151], a[96:99], a[148:151], v[148:151]\n\t"
"s_nop 0\n\t"
"ds_read_b128 a[160:163], v224 offset:20544\n\t"
"ds_read_b128 a[148:151], v224 offset:16960\n\t"
"v_mfma_f32_16x16x32_bf16 v[148:151], a[100:103], a[164:167], v[148:151]\n\t"
"s_waitcnt lgkmcnt(10)\n\t"
"buffer_store_dwordx4 v[24:27], v233, s[16:19], s24 offen nt\n\t"
"v_mfma_f32_16x16x32_bf16 v[216:219], a[224:227], a[200:203], 0\n\t"
"s_nop 0\n\t"
"ds_read_b128 a[164:167], v224 offset:21056\n\t"
"ds_read_b128 v[28:31], v230 offset:33280\n\t"
"v_mfma_f32_16x16x32_bf16 v[152:155], a[96:99], a[152:155], v[152:155]\n\t"
"ds_read_b128 v[40:43], v231 offset:19968\n\t"
"v_cvt_pk_bf16_f32 v209, v209, v213\n\t"
"v_mfma_f32_16x16x32_bf16 v[152:155], a[100:103], a[168:171], v[152:155]\n\t"
"buffer_load_dwordx4 a[4:7], v235, s[84:87], s69 offen offset:1024 sc1\n\t"
"v_mfma_f32_16x16x32_bf16 v[168:171], a[104:107], a[152:155], v[168:171]\n\t"
"s_nop 0\n\t"
"ds_write_addtid_b32 v201 offset:4352\n\t"
"ds_read_b64_tr_b16 a[196:197], v249 offset:1152\n\t"
"v_mfma_f32_16x16x32_bf16 v[168:171], a[108:111], a[168:171], v[168:171]\n\t"
"buffer_load_dwordx4 a[40:43], v252, s[84:87], s81 offen offset:0 sc1\n\t"
"v_mfma_f32_16x16x32_bf16 v[220:223], a[224:227], a[204:207], 0\n\t"
"ds_write_addtid_b32 v209 offset:2304\n\t"
"v_cvt_pk_bf16_f32 v210, v210, v214\n\t"
"v_mfma_f32_16x16x32_bf16 v[184:187], a[112:115], a[152:155], v[184:187]\n\t"
"ds_read_b128 v[44:47], v231 offset:33280\n\t"
"v_cvt_pk_bf16_f32 v208, v208, v212\n\t"
"v_mfma_f32_16x16x32_bf16 v[184:187], a[116:119], a[168:171], v[184:187]\n\t"
"buffer_load_dwordx4 a[44:47], v252, s[84:87], s81 offen offset:1024 sc1\n\t"
"v_mfma_f32_16x16x32_bf16 v[188:191], a[112:115], a[156:159], v[188:191]\n\t"
"ds_write_addtid_b32 v210 offset:10272\n\t"
"v_cvt_pk_bf16_f32 v211, v211, v215\n\t"
"v_mfma_f32_16x16x32_bf16 v[188:191], a[116:119], a[172:175], v[188:191]\n\t"
"ds_read_b64_tr_b16 a[198:199], v249 offset:1664\n\t"
"v_cvt_pk_bf16_f32 v219, v219, v223\n\t"
"v_mfma_f32_16x16x32_bf16 v[172:175], a[104:107], a[156:159], v[172:175]\n\t"
"ds_write_addtid_b32 v219 offset:14624\n\t"
"v_cvt_pk_bf16_f32 v218, v218, v222\n\t"
"v_mfma_f32_16x16x32_bf16 v[172:175], a[108:111], a[172:175], v[172:175]\n\t"
"s_waitcnt lgkmcnt(7)\n\t"
"buffer_store_dwordx4 v[40:43], v234, s[16:19], s24 offen nt\n\t"
"v_mfma_f32_16x16x32_bf16 v[156:159], a[96:99], a[156:159], v[156:159]\n\t"
"ds_read_b128 v[12:15], v229 offset:33280\n\t"
"v_cvt_pk_bf16_f32 v217, v217, v221\n\t"
"v_mfma_f32_16x16x32_bf16 v[156:159], a[100:103], a[172:175], v[156:159]\n\t"
"s_waitcnt vmcnt(16) lgkmcnt(6)\n\t"
"ds_write_addtid_b32 v211 offset:10528\n\t"
"ds_write_addtid_b32 v217 offset:6400\n\t"
"v_mfma_f32_16x16x32_bf16 v[96:99], a[120:123], a[144:147], v[96:99]\n\t"
"s_nop 0\n\t"
"ds_write_addtid_b32 v208 offset:2048\n\t"
"ds_write_addtid_b32 v218 offset:14368\n\t"
"v_mfma_f32_16x16x32_bf16 v[96:99], a[124:127], a[160:163], v[96:99]\n\t"
"buffer_load_dwordx4 a[36:39], v235, s[84:87], s81 offen offset:3072 sc1\n\t"
"v_mfma_f32_16x16x32_bf16 v[112:115], a[128:131], a[144:147], v[112:115]\n\t"
"ds_read_b64_tr_b16 a[200:201], v250 offset:1152\n\t"
"v_cvt_pk_bf16_f32 v216, v216, v220\n\t"
"v_mfma_f32_16x16x32_bf16 v[112:115], a[132:135], a[160:163], v[112:115]\n\t"
"buffer_load_dwordx4 a[32:35], v235, s[84:87], s81 offen offset:2048 sc1\n\t"
"v_mfma_f32_16x16x32_bf16 v[128:131], a[136:139], a[144:147], v[128:131]\n\t"
"s_nop 0\n\t"
"ds_read_b128 a[168:171], v224 offset:21568\n\t"
"ds_write_addtid_b32 v216 offset:6144\n\t"
"v_mfma_f32_16x16x32_bf16 v[128:131], a[140:143], a[160:163], v[128:131]\n\t"
"buffer_load_dwordx4 a[24:27], v235, s[84:87], s81 offen offset:0 sc1\n\t"
"v_mfma_f32_16x16x32_bf16 v[132:135], a[136:139], a[148:151], v[132:135]\n\t"
"s_nop 0\n\t"
"ds_read_b128 a[152:155], v224 offset:17472\n\t"
"ds_read_b64_tr_b16 a[202:203], v250 offset:1664\n\t"
"v_mfma_f32_16x16x32_bf16 v[132:135], a[140:143], a[164:167], v[132:135]\n\t"
"s_nop 0\n\t"
"ds_read_b64_tr_b16 a[204:205], v251 offset:1152\n\t"
"ds_read_b64_tr_b16 a[206:207], v251 offset:1664\n\t"
"v_mfma_f32_16x16x32_bf16 v[116:119], a[128:131], a[148:151], v[116:119]\n\t"
"s_nop 0\n\t"
"ds_read_b128 a[156:159], v224 offset:17984\n\t"
"ds_read_b128 a[172:175], v224 offset:22080\n\t"
"v_mfma_f32_16x16x32_bf16 v[116:119], a[132:135], a[164:167], v[116:119]\n\t"
"buffer_load_dwordx4 a[28:31], v235, s[84:87], s81 offen offset:1024 sc1\n\t"
"v_mfma_f32_16x16x32_bf16 v[100:103], a[120:123], a[148:151], v[100:103]\n\t"
"v_mfma_f32_16x16x32_bf16 v[100:103], a[124:127], a[164:167], v[100:103]\n\t"
"s_waitcnt lgkmcnt(9)\n\t"
"buffer_store_dwordx4 v[12:15], v232, s[16:19], s27 offen nt\n\t"
"s_waitcnt lgkmcnt(0)\n\t"
"s_barrier\n\t"
"v_mfma_f32_16x16x32_bf16 v[104:107], a[120:123], a[152:155], v[104:107]\n\t"
"ds_read_b128 a[144:147], v224 offset:0\n\t"
"v_add_u32 v248, 101632, v227\n\t"
"v_mfma_f32_16x16x32_bf16 v[104:107], a[124:127], a[168:171], v[104:107]\n\t"
"buffer_load_dwordx4 a[64:67], v252, s[84:87], s54 offen offset:0 sc1\n\t"
"v_mfma_f32_16x16x32_bf16 v[120:123], a[128:131], a[152:155], v[120:123]\n\t"
"s_nop 0\n\t"
"ds_read_b128 a[160:163], v224 offset:4096\n\t"
"ds_read_b128 a[148:151], v224 offset:512\n\t"
"v_mfma_f32_16x16x32_bf16 v[120:123], a[132:135], a[168:171], v[120:123]\n\t"
"ds_read_b128 a[164:167], v224 offset:4608\n\t"
"v_xor_b32 v249, 32, v248\n\t"
"v_mfma_f32_16x16x32_bf16 v[136:139], a[136:139], a[152:155], v[136:139]\n\t"
"ds_read_b128 a[152:155], v224 offset:1024\n\t"
"v_xor_b32 v250, 64, v248\n\t"
"v_mfma_f32_16x16x32_bf16 v[136:139], a[140:143], a[168:171], v[136:139]\n\t"
"buffer_load_dwordx4 a[68:71], v252, s[84:87], s54 offen offset:1024 sc1\n\t"
"v_mfma_f32_16x16x32_bf16 v[192:195], a[216:219], a[176:179], 0\n\t"
"ds_read_b128 a[168:171], v224 offset:5120\n\t"
"v_xor_b32 v251, 96, v248\n\t"
"v_mfma_f32_16x16x32_bf16 v[140:143], a[136:139], a[156:159], v[140:143]\n\t"
"ds_read_b64_tr_b16 a[176:177], v248 offset:41472\n\t"
"v_mfma_f32_16x16x32_bf16 v[140:143], a[140:143], a[172:175], v[140:143]\n\t"
"buffer_load_dwordx4 a[60:63], v235, s[84:87], s54 offen offset:3072 sc1\n\t"
"v_mfma_f32_16x16x32_bf16 v[196:199], a[216:219], a[180:183], 0\n\t"
"ds_read_b64_tr_b16 a[178:179], v248 offset:41984\n\t"
"v_mfma_f32_16x16x32_bf16 v[124:127], a[128:131], a[156:159], v[124:127]\n\t"
"ds_read_b64_tr_b16 a[180:181], v249 offset:41472\n\t"
"v_mfma_f32_16x16x32_bf16 v[124:127], a[132:135], a[172:175], v[124:127]\n\t"
"buffer_load_dwordx4 a[56:59], v235, s[84:87], s54 offen offset:2048 sc1\n\t"
"v_mfma_f32_16x16x32_bf16 v[108:111], a[120:123], a[156:159], v[108:111]\n\t"
"s_nop 0\n\t"
"ds_read_b128 a[156:159], v224 offset:1536\n\t"
"ds_read_b64_tr_b16 a[182:183], v249 offset:41984\n\t"
"v_mfma_f32_16x16x32_bf16 v[108:111], a[124:127], a[172:175], v[108:111]\n\t"
"ds_read_b128 a[172:175], v224 offset:5632\n\t"
"v_cvt_pk_bf16_f32 v194, v194, v198\n\t"
"v_mfma_f32_16x16x32_bf16 v[200:203], a[216:219], a[184:187], 0\n\t"
"s_waitcnt vmcnt(12) lgkmcnt(7)\n\t"
"buffer_load_dwordx4 a[48:51], v235, s[84:87], s54 offen offset:0 sc1\n\t"
"buffer_load_dwordx4 a[52:55], v235, s[84:87], s54 offen offset:1024 sc1\n\t"
"v_mfma_f32_16x16x32_bf16 v[48:51], a[0:3], a[144:147], v[48:51]\n\t"
"ds_read_b64_tr_b16 a[184:185], v250 offset:41472\n\t"
"v_cvt_pk_bf16_f32 v192, v192, v196\n\t"
"v_mfma_f32_16x16x32_bf16 v[48:51], a[4:7], a[160:163], v[48:51]\n\t"
"ds_write_addtid_b32 v192 offset:16448\n\t"
"v_cvt_pk_bf16_f32 v193, v193, v197\n\t"
"v_mfma_f32_16x16x32_bf16 v[204:207], a[216:219], a[188:191], 0\n\t"
"ds_write_addtid_b32 v193 offset:16704\n\t"
"buffer_store_dwordx4 v[28:31], v233, s[16:19], s27 offen nt\n\t"
"v_pk_add_f32 v[96:97], v[96:97], v[144:145]\n\t"
"v_pk_add_f32 v[98:99], v[98:99], v[146:147]\n\t"
"v_pk_fma_f32 v[144:145], v[144:145], s[38:39], v[96:97]\n\t"
"v_pk_fma_f32 v[146:147], v[146:147], s[38:39], v[98:99]\n\t"
"v_pk_add_f32 v[112:113], v[112:113], v[160:161]\n\t"
"v_pk_add_f32 v[114:115], v[114:115], v[162:163]\n\t"
"v_pk_fma_f32 v[160:161], v[160:161], s[38:39], v[112:113]\n\t"
"v_pk_fma_f32 v[162:163], v[162:163], s[38:39], v[114:115]\n\t"
"v_pk_add_f32 v[128:129], v[128:129], v[176:177]\n\t"
"v_pk_add_f32 v[130:131], v[130:131], v[178:179]\n\t"
"v_pk_fma_f32 v[176:177], v[176:177], s[38:39], v[128:129]\n\t"
"v_pk_fma_f32 v[178:179], v[178:179], s[38:39], v[130:131]\n\t"
"v_pk_add_f32 v[132:133], v[132:133], v[180:181]\n\t"
"v_pk_add_f32 v[134:135], v[134:135], v[182:183]\n\t"
"v_pk_fma_f32 v[180:181], v[180:181], s[38:39], v[132:133]\n\t"
"v_pk_fma_f32 v[182:183], v[182:183], s[38:39], v[134:135]\n\t"
"v_pk_add_f32 v[116:117], v[116:117], v[164:165]\n\t"
"v_pk_add_f32 v[118:119], v[118:119], v[166:167]\n\t"
"v_pk_fma_f32 v[164:165], v[164:165], s[38:39], v[116:117]\n\t"
"v_pk_fma_f32 v[166:167], v[166:167], s[38:39], v[118:119]\n\t"
"v_pk_add_f32 v[100:101], v[100:101], v[148:149]\n\t"
"v_pk_add_f32 v[102:103], v[102:103], v[150:151]\n\t"
"v_pk_fma_f32 v[148:149], v[148:149], s[38:39], v[100:101]\n\t"
"v_pk_fma_f32 v[150:151], v[150:151], s[38:39], v[102:103]\n\t"
"v_pk_add_f32 v[104:105], v[104:105], v[152:153]\n\t"
"v_pk_add_f32 v[106:107], v[106:107], v[154:155]\n\t"
"v_pk_fma_f32 v[152:153], v[152:153], s[38:39], v[104:105]\n\t"
"v_pk_fma_f32 v[154:155], v[154:155], s[38:39], v[106:107]\n\t"
"v_pk_add_f32 v[120:121], v[120:121], v[168:169]\n\t"
"v_pk_add_f32 v[122:123], v[122:123], v[170:171]\n\t"
"v_pk_fma_f32 v[168:169], v[168:169], s[38:39], v[120:121]\n\t"
"v_pk_fma_f32 v[170:171], v[170:171], s[38:39], v[122:123]\n\t"
"v_pk_add_f32 v[136:137], v[136:137], v[184:185]\n\t"
"v_pk_add_f32 v[138:139], v[138:139], v[186:187]\n\t"
"v_pk_fma_f32 v[184:185], v[184:185], s[38:39], v[136:137]\n\t"
"v_pk_fma_f32 v[186:187], v[186:187], s[38:39], v[138:139]\n\t"
"v_pk_add_f32 v[140:141], v[140:141], v[188:189]\n\t"
"v_pk_add_f32 v[142:143], v[142:143], v[190:191]\n\t"
"v_pk_fma_f32 v[188:189], v[188:189], s[38:39], v[140:141]\n\t"
"v_pk_fma_f32 v[190:191], v[190:191], s[38:39], v[142:143]\n\t"
"v_pk_add_f32 v[124:125], v[124:125], v[172:173]\n\t"
"v_pk_add_f32 v[126:127], v[126:127], v[174:175]\n\t"
"v_pk_fma_f32 v[172:173], v[172:173], s[38:39], v[124:125]\n\t"
"v_pk_fma_f32 v[174:175], v[174:175], s[38:39], v[126:127]\n\t"
"v_pk_add_f32 v[108:109], v[108:109], v[156:157]\n\t"
"v_pk_add_f32 v[110:111], v[110:111], v[158:159]\n\t"
"v_pk_fma_f32 v[156:157], v[156:157], s[38:39], v[108:109]\n\t"
"v_pk_fma_f32 v[158:159], v[158:159], s[38:39], v[110:111]\n\t"
"v_cvt_pk_bf16_f32 v96, v96, v97\n\t"
"v_cvt_pk_bf16_f32 v100, v100, v101\n\t"
"v_cvt_pk_bf16_f32 v132, v132, v133\n\t"
"v_mfma_f32_16x16x32_bf16 v[64:67], a[8:11], a[144:147], v[64:67]\n\t"
"ds_write_addtid_b32 v194 offset:24672\n\t"
"v_cvt_pk_bf16_f32 v140, v140, v141\n\t"
"v_mfma_f32_16x16x32_bf16 v[64:67], a[12:15], a[160:163], v[64:67]\n\t"
"ds_read_b64_tr_b16 a[186:187], v250 offset:41984\n\t"
"v_cvt_pk_bf16_f32 v97, v98, v99\n\t"
"v_cvt_pk_bf16_f32 v101, v102, v103\n\t"
"v_cvt_pk_bf16_f32 v120, v120, v121\n\t"
"v_mfma_f32_16x16x32_bf16 v[80:83], a[16:19], a[144:147], v[80:83]\n\t"
"ds_read_b128 a[144:147], v224 offset:2048\n\t"
"v_cvt_pk_bf16_f32 v128, v128, v129\n\t"
"v_mfma_f32_16x16x32_bf16 v[80:83], a[20:23], a[160:163], v[80:83]\n\t"
"buffer_load_dwordx4 a[88:91], v252, s[84:87], s66 offen offset:0 sc1\n\t"
"buffer_load_dwordx4 a[92:95], v252, s[84:87], s66 offen offset:1024 sc1\n\t"
"buffer_load_dwordx4 a[84:87], v235, s[84:87], s66 offen offset:3072 sc1\n\t"
"buffer_load_dwordx4 a[80:83], v235, s[84:87], s66 offen offset:2048 sc1\n\t"
"buffer_load_dwordx4 a[72:75], v235, s[84:87], s66 offen offset:0 sc1\n\t"
"v_cvt_pk_bf16_f32 v195, v195, v199\n\t"
"v_cvt_pk_bf16_f32 v116, v116, v117\n\t"
"v_mfma_f32_16x16x32_bf16 v[208:211], a[216:219], a[192:195], 0\n\t"
"ds_read_b128 a[160:163], v224 offset:6144\n\t"
"ds_write2st64_b64 v228, v[96:97], v[100:101] offset0:39 offset1:52\n\t"
"v_mfma_f32_16x16x32_bf16 v[84:87], a[16:19], a[148:151], v[84:87]\n\t"
"v_cvt_pk_bf16_f32 v112, v112, v113\n\t"
"v_cvt_pk_bf16_f32 v133, v134, v135\n\t"
"v_mfma_f32_16x16x32_bf16 v[84:87], a[20:23], a[164:167], v[84:87]\n\t"
"v_cvt_pk_bf16_f32 v113, v114, v115\n\t"
"v_cvt_pk_bf16_f32 v117, v118, v119\n\t"
"v_mfma_f32_16x16x32_bf16 v[68:71], a[8:11], a[148:151], v[68:71]\n\t"
"v_cvt_pk_bf16_f32 v108, v108, v109\n\t"
"v_cvt_pk_bf16_f32 v121, v122, v123\n\t"
"v_mfma_f32_16x16x32_bf16 v[68:71], a[12:15], a[164:167], v[68:71]\n\t"
"ds_write_addtid_b32 v195 offset:24928\n\t"
"buffer_load_dwordx4 a[76:79], v235, s[84:87], s66 offen offset:1024 sc1\n\t"
"ds_write2st64_b64 v246, v[112:113], v[116:117] offset0:39 offset1:52\n\t"
"v_mfma_f32_16x16x32_bf16 v[212:215], a[216:219], a[196:199], 0\n\t"
"v_cvt_pk_bf16_f32 v200, v200, v204\n\t"
"v_cvt_pk_bf16_f32 v203, v203, v207\n\t"
"v_mfma_f32_16x16x32_bf16 v[52:55], a[0:3], a[148:151], v[52:55]\n\t"
"v_cvt_pk_bf16_f32 v129, v130, v131\n\t"
"v_cvt_pk_bf16_f32 v109, v110, v111\n\t"
"v_mfma_f32_16x16x32_bf16 v[52:55], a[4:7], a[164:167], v[52:55]\n\t"
"buffer_store_dwordx4 v[44:47], v234, s[16:19], s27 offen nt\n\t"
"v_cvt_pk_bf16_f32 v201, v201, v205\n\t"
"v_mfma_f32_16x16x32_bf16 v[216:219], a[216:219], a[200:203], 0\n\t"
"s_waitcnt lgkmcnt(4)\n\t"
"ds_read_b128 a[148:151], v224 offset:2560\n\t"
"ds_read_b128 a[164:167], v224 offset:6656\n\t"
"ds_write_addtid_b32 v200 offset:20544\n\t"
"ds_write_addtid_b32 v201 offset:20800\n\t"
"ds_write_addtid_b32 v203 offset:29024\n\t"
"ds_read_b64_tr_b16 a[188:189], v251 offset:41472\n\t"
"ds_write2st64_b64 v247, v[128:129], v[132:133] offset0:39 offset1:52\n\t"
"v_mfma_f32_16x16x32_bf16 v[56:59], a[0:3], a[152:155], v[56:59]\n\t"
"v_cvt_pk_bf16_f32 v104, v104, v105\n\t"
"v_cvt_pk_bf16_f32 v141, v142, v143\n\t"
"v_mfma_f32_16x16x32_bf16 v[56:59], a[4:7], a[168:171], v[56:59]\n\t"
"buffer_load_dwordx4 a[112:115], v252, s[84:87], 0 offen offset:0 sc1\n\t"
"v_mfma_f32_16x16x32_bf16 v[72:75], a[8:11], a[152:155], v[72:75]\n\t"
"v_cvt_pk_bf16_f32 v202, v202, v206\n\t"
"v_cvt_pk_bf16_f32 v105, v106, v107\n\t"
"v_mfma_f32_16x16x32_bf16 v[72:75], a[12:15], a[168:171], v[72:75]\n\t"
"ds_write_addtid_b32 v202 offset:28768\n\t"
"ds_write2st64_b64 v228, v[104:105], v[108:109] offset0:65 offset1:78\n\t"
"v_mfma_f32_16x16x32_bf16 v[220:223], a[216:219], a[204:207], 0\n\t"
"buffer_load_dwordx4 a[116:119], v252, s[84:87], 0 offen offset:1024 sc1\n\t"
"v_cvt_pk_bf16_f32 v124, v124, v125\n\t"
"v_mfma_f32_16x16x32_bf16 v[88:91], a[16:19], a[152:155], v[88:91]\n\t"
"v_cvt_pk_bf16_f32 v211, v211, v215\n\t"
"v_cvt_pk_bf16_f32 v125, v126, v127\n\t"
"v_mfma_f32_16x16x32_bf16 v[88:91], a[20:23], a[168:171], v[88:91]\n\t"
"ds_write2st64_b64 v246, v[120:121], v[124:125] offset0:65 offset1:78\n\t"
"v_mfma_f32_16x16x32_bf16 v[92:95], a[16:19], a[156:159], v[92:95]\n\t"
"v_cvt_pk_bf16_f32 v208, v208, v212\n\t"
"v_cvt_pk_bf16_f32 v136, v136, v137\n\t"
"v_mfma_f32_16x16x32_bf16 v[92:95], a[20:23], a[172:175], v[92:95]\n\t"
"buffer_load_dwordx4 a[108:111], v235, s[84:87], 0 offen offset:3072 sc1\n\t"
"v_mfma_f32_16x16x32_bf16 v[76:79], a[8:11], a[156:159], v[76:79]\n\t"
"v_cvt_pk_bf16_f32 v210, v210, v214\n\t"
"v_cvt_pk_bf16_f32 v137, v138, v139\n\t"
"v_mfma_f32_16x16x32_bf16 v[76:79], a[12:15], a[172:175], v[76:79]\n\t"
"ds_write_addtid_b32 v208 offset:18496\n\t"
"ds_write2st64_b64 v247, v[136:137], v[140:141] offset0:65 offset1:78\n\t"
"v_mfma_f32_16x16x32_bf16 v[60:63], a[0:3], a[156:159], v[60:63]\n\t"
"v_cvt_pk_bf16_f32 v216, v216, v220\n\t"
"v_cvt_pk_bf16_f32 v218, v218, v222\n\t"
"v_mfma_f32_16x16x32_bf16 v[60:63], a[4:7], a[172:175], v[60:63]\n\t"
"s_waitcnt vmcnt(18) lgkmcnt(5)\n\t"
"buffer_load_dwordx4 a[104:107], v235, s[84:87], 0 offen offset:2048 sc1\n\t"
"v_mfma_f32_16x16x32_bf16 v[144:147], a[24:27], a[144:147], v[144:147]\n\t"
"v_cvt_pk_bf16_f32 v209, v209, v213\n\t"
"v_cvt_pk_bf16_f32 v217, v217, v221\n\t"
"v_mfma_f32_16x16x32_bf16 v[144:147], a[28:31], a[160:163], v[144:147]\n\t"
"s_nop 0\n\t"
"ds_read_b128 a[152:155], v224 offset:3072\n\t"
"ds_read_b128 a[156:159], v224 offset:3584\n\t"
"ds_write_addtid_b32 v209 offset:18752\n\t"
"ds_write_addtid_b32 v216 offset:22592\n\t"
"ds_read_b64_tr_b16 a[190:191], v251 offset:41984\n\t"
"ds_read_b64_tr_b16 a[192:193], v248 offset:42624\n\t"
"ds_read_b64_tr_b16 a[194:195], v248 offset:43136\n\t"
"v_mfma_f32_16x16x32_bf16 v[160:163], a[32:35], a[144:147], v[160:163]\n\t"
"s_nop 0\n\t"
"ds_write_addtid_b32 v210 offset:26720\n\t"
"ds_write_addtid_b32 v211 offset:26976\n\t"
"v_mfma_f32_16x16x32_bf16 v[160:163], a[36:39], a[160:163], v[160:163]\n\t"
"ds_read_b64_tr_b16 a[196:197], v249 offset:42624\n\t"
"buffer_load_dwordx4 a[96:99], v235, s[84:87], 0 offen offset:0 sc1\n\t"
"v_mfma_f32_16x16x32_bf16 v[176:179], a[40:43], a[144:147], v[176:179]\n\t"
"ds_read_b64_tr_b16 a[198:199], v249 offset:43136\n\t"
"v_cvt_pk_bf16_f32 v219, v219, v223\n\t"
"v_mfma_f32_16x16x32_bf16 v[176:179], a[44:47], a[160:163], v[176:179]\n\t"
"s_nop 0\n\t"
"ds_write_addtid_b32 v217 offset:22848\n\t"
"ds_write_addtid_b32 v218 offset:30816\n\t"
"ds_write_addtid_b32 v219 offset:31072\n\t"
"ds_read_b64_tr_b16 a[200:201], v250 offset:42624\n\t"
"ds_read_b64_tr_b16 a[202:203], v250 offset:43136\n\t"
"v_mfma_f32_16x16x32_bf16 v[180:183], a[40:43], a[148:151], v[180:183]\n\t"
"s_nop 0\n\t"
"ds_read_b64_tr_b16 a[204:205], v251 offset:42624\n\t"
"ds_read_b64_tr_b16 a[206:207], v251 offset:43136\n\t"
"v_mfma_f32_16x16x32_bf16 v[180:183], a[44:47], a[164:167], v[180:183]\n\t"
"buffer_load_dwordx4 a[100:103], v235, s[84:87], 0 offen offset:1024 sc1\n\t"
"v_mfma_f32_16x16x32_bf16 v[164:167], a[32:35], a[148:151], v[164:167]\n\t"
"s_nop 0\n\t"
"ds_read_b128 a[168:171], v224 offset:7168\n\t"
"ds_read_b128 a[172:175], v224 offset:7680\n\t"
"v_mfma_f32_16x16x32_bf16 v[164:167], a[36:39], a[164:167], v[164:167]\n\t"
"buffer_load_dwordx4 a[136:139], v252, s[84:87], s51 offen offset:0 sc1\n\t"
"v_mfma_f32_16x16x32_bf16 v[148:151], a[24:27], a[148:151], v[148:151]\n\t"
"v_mfma_f32_16x16x32_bf16 v[148:151], a[28:31], a[164:167], v[148:151]\n\t"
"s_waitcnt lgkmcnt(0)\n\t"
"s_barrier\n\t"
"v_mfma_f32_16x16x32_bf16 v[152:155], a[24:27], a[152:155], v[152:155]\n\t"
"s_nop 0\n\t"
"ds_read_b128 a[144:147], v224 offset:16448\n\t"
"ds_read_b128 a[148:151], v224 offset:16960\n\t"
"v_mfma_f32_16x16x32_bf16 v[152:155], a[28:31], a[168:171], v[152:155]\n\t"
"buffer_load_dwordx4 a[140:143], v252, s[84:87], s51 offen offset:1024 sc1\n\t"
"v_mfma_f32_16x16x32_bf16 v[168:171], a[32:35], a[152:155], v[168:171]\n\t"
"s_nop 0\n\t"
"ds_read_b128 a[160:163], v224 offset:20544\n\t"
"ds_read_b128 a[164:167], v224 offset:21056\n\t"
"v_mfma_f32_16x16x32_bf16 v[168:171], a[36:39], a[168:171], v[168:171]\n\t"
"buffer_load_dwordx4 a[132:135], v235, s[84:87], s51 offen offset:3072 sc1\n\t"
"v_mfma_f32_16x16x32_bf16 v[184:187], a[40:43], a[152:155], v[184:187]\n\t"
"ds_read_b128 v[104:107], v229 offset:19968\n\t"
"v_mfma_f32_16x16x32_bf16 v[184:187], a[44:47], a[168:171], v[184:187]\n\t"
"buffer_load_dwordx4 a[128:131], v235, s[84:87], s51 offen offset:2048 sc1\n\t"
"v_mfma_f32_16x16x32_bf16 v[192:195], a[224:227], a[176:179], 0\n\t"
"ds_read_b64_tr_b16 a[176:177], v248 offset:20736\n\t"
"v_mfma_f32_16x16x32_bf16 v[188:191], a[40:43], a[156:159], v[188:191]\n\t"
"ds_read_b128 a[168:171], v224 offset:21568\n\t"
"v_mfma_f32_16x16x32_bf16 v[188:191], a[44:47], a[172:175], v[188:191]\n\t"
"ds_read_b64_tr_b16 a[178:179], v248 offset:21248\n\t"
"v_mfma_f32_16x16x32_bf16 v[196:199], a[224:227], a[180:183], 0\n\t"
"buffer_load_dwordx4 a[120:123], v235, s[84:87], s51 offen offset:0 sc1\n\t"
"v_mfma_f32_16x16x32_bf16 v[172:175], a[32:35], a[156:159], v[172:175]\n\t"
"ds_read_b128 v[120:123], v230 offset:19968\n\t"
"v_mfma_f32_16x16x32_bf16 v[172:175], a[36:39], a[172:175], v[172:175]\n\t"
"s_nop 0\n\t"
"ds_read_b128 a[152:155], v224 offset:17472\n\t"
"ds_read_b64_tr_b16 a[180:181], v249 offset:20736\n\t"
"v_mfma_f32_16x16x32_bf16 v[156:159], a[24:27], a[156:159], v[156:159]\n\t"
"ds_read_b64_tr_b16 a[182:183], v249 offset:21248\n\t"
"v_mfma_f32_16x16x32_bf16 v[156:159], a[28:31], a[172:175], v[156:159]\n\t"
"s_waitcnt lgkmcnt(7)\n\t"
"buffer_store_dwordx4 v[104:107], v232, s[16:19], s20 offen nt\n\t"
"v_mfma_f32_16x16x32_bf16 v[200:203], a[224:227], a[184:187], 0\n\t"
"ds_read_b128 a[172:175], v224 offset:22080\n\t"
"v_cvt_pk_bf16_f32 v195, v195, v199\n\t"
"v_mfma_f32_16x16x32_bf16 v[0:3], a[0:3], a[144:147], 0\n\t"
"ds_read_b64_tr_b16 a[184:185], v250 offset:20736\n\t"
"v_mfma_f32_16x16x32_bf16 v[0:3], a[4:7], a[160:163], v[0:3]\n\t"
"buffer_load_dwordx4 a[124:127], v235, s[84:87], s51 offen offset:1024 sc1\n\t"
"v_mfma_f32_16x16x32_bf16 v[204:207], a[224:227], a[188:191], 0\n\t"
"ds_read_b64_tr_b16 a[186:187], v250 offset:21248\n\t"
"v_cvt_pk_bf16_f32 v193, v193, v197\n\t"
"v_mfma_f32_16x16x32_bf16 v[16:19], a[8:11], a[144:147], 0\n\t"
"ds_read_b64_tr_b16 a[188:189], v251 offset:20736\n\t"
"v_cvt_pk_bf16_f32 v192, v192, v196\n\t"
"v_mfma_f32_16x16x32_bf16 v[16:19], a[12:15], a[160:163], v[16:19]\n\t"
"ds_write_addtid_b32 v195 offset:8480\n\t"
"v_cvt_pk_bf16_f32 v194, v194, v198\n\t"
"v_mfma_f32_16x16x32_bf16 v[32:35], a[16:19], a[144:147], 0\n\t"
"s_nop 0\n\t"
"ds_read_b128 a[156:159], v224 offset:17984\n\t"
"ds_read_b128 a[144:147], v224 offset:18496\n\t"
"v_mfma_f32_16x16x32_bf16 v[32:35], a[20:23], a[160:163], v[32:35]\n\t"
"s_waitcnt lgkmcnt(8)\n\t"
"buffer_store_dwordx4 v[120:123], v233, s[16:19], s20 offen nt\n\t"
"v_mfma_f32_16x16x32_bf16 v[208:211], a[224:227], a[192:195], 0\n\t"
"ds_read_b128 v[136:139], v231 offset:19968\n\t"
"v_cvt_pk_bf16_f32 v203, v203, v207\n\t"
"v_mfma_f32_16x16x32_bf16 v[36:39], a[16:19], a[148:151], 0\n\t"
"ds_read_b128 a[160:163], v224 offset:22592\n\t"
"v_cvt_pk_bf16_f32 v201, v201, v205\n\t"
"v_mfma_f32_16x16x32_bf16 v[36:39], a[20:23], a[164:167], v[36:39]\n\t"
"ds_read_b64_tr_b16 a[190:191], v251 offset:21248\n\t"
"v_cvt_pk_bf16_f32 v200, v200, v204\n\t"
"v_mfma_f32_16x16x32_bf16 v[20:23], a[8:11], a[148:151], 0\n\t"
"s_nop 0\n\t"
"ds_write_addtid_b32 v192 offset:0\n\t"
"ds_read_b64_tr_b16 a[192:193], v248 offset:21888\n\t"
"v_mfma_f32_16x16x32_bf16 v[20:23], a[12:15], a[164:167], v[20:23]\n\t"
"s_nop 0\n\t"
"ds_write_addtid_b32 v194 offset:8224\n\t"
"ds_read_b64_tr_b16 a[194:195], v248 offset:22400\n\t"
"v_mfma_f32_16x16x32_bf16 v[212:215], a[224:227], a[196:199], 0\n\t"
"s_nop 0\n\t"
"ds_write_addtid_b32 v193 offset:256\n\t"
"ds_write_addtid_b32 v203 offset:12576\n\t"
"v_mfma_f32_16x16x32_bf16 v[4:7], a[0:3], a[148:151], 0\n\t"
"s_nop 0\n\t"
"ds_write_addtid_b32 v200 offset:4096\n\t"
"ds_read_b128 a[232:235], v230 offset:33280\n\t"
"v_mfma_f32_16x16x32_bf16 v[4:7], a[4:7], a[164:167], v[4:7]\n\t"
"ds_write_addtid_b32 v201 offset:4352\n\t"
"v_mfma_f32_16x16x32_bf16 v[216:219], a[224:227], a[200:203], 0\n\t"
"ds_read_b64_tr_b16 a[196:197], v249 offset:21888\n\t"
"v_cvt_pk_bf16_f32 v202, v202, v206\n\t"
"v_mfma_f32_16x16x32_bf16 v[8:11], a[0:3], a[152:155], 0\n\t"
"ds_read_b128 a[148:151], v224 offset:19008\n\t"
"v_cvt_pk_bf16_f32 v210, v210, v214\n\t"
"v_mfma_f32_16x16x32_bf16 v[8:11], a[4:7], a[168:171], v[8:11]\n\t"
"s_waitcnt lgkmcnt(13)\n\t"
"buffer_store_dwordx4 v[136:139], v234, s[16:19], s20 offen nt\n\t"
"v_mfma_f32_16x16x32_bf16 v[24:27], a[8:11], a[152:155], 0\n\t"
"ds_read_b128 v[108:111], v229 offset:33280\n\t"
"v_cvt_pk_bf16_f32 v209, v209, v213\n\t"
"v_mfma_f32_16x16x32_bf16 v[24:27], a[12:15], a[168:171], v[24:27]\n\t"
"s_nop 0\n\t"
"ds_read_b128 a[164:167], v224 offset:23104\n\t"
"ds_write_addtid_b32 v202 offset:12320\n\t"
"v_mfma_f32_16x16x32_bf16 v[220:223], a[224:227], a[204:207], 0\n\t"
"ds_read_b64_tr_b16 a[198:199], v249 offset:22400\n\t"
"v_cvt_pk_bf16_f32 v211, v211, v215\n\t"
"v_mfma_f32_16x16x32_bf16 v[40:43], a[16:19], a[152:155], 0\n\t"
"ds_write_addtid_b32 v211 offset:10528\n\t"
"v_cvt_pk_bf16_f32 v208, v208, v212\n\t"
"v_mfma_f32_16x16x32_bf16 v[40:43], a[20:23], a[168:171], v[40:43]\n\t"
"s_nop 0\n\t"
"ds_write_addtid_b32 v208 offset:2048\n\t"
"ds_write_addtid_b32 v210 offset:10272\n\t"
"v_mfma_f32_16x16x32_bf16 v[44:47], a[16:19], a[156:159], 0\n\t"
"s_nop 0\n\t"
"ds_read_b64_tr_b16 a[200:201], v250 offset:21888\n\t"
"ds_read_b64_tr_b16 a[202:203], v250 offset:22400\n\t"
"v_mfma_f32_16x16x32_bf16 v[44:47], a[20:23], a[172:175], v[44:47]\n\t"
"buffer_load_dwordx4 a[16:19], v252, s[84:87], s57 offen offset:0 sc1\n\t"
"v_mfma_f32_16x16x32_bf16 v[28:31], a[8:11], a[156:159], 0\n\t"
"ds_read_b128 a[236:239], v231 offset:33280\n\t"
"v_cvt_pk_bf16_f32 v218, v218, v222\n\t"
"v_mfma_f32_16x16x32_bf16 v[28:31], a[12:15], a[172:175], v[28:31]\n\t"
"buffer_load_dwordx4 a[20:23], v252, s[84:87], s57 offen offset:1024 sc1\n\t"
"v_mfma_f32_16x16x32_bf16 v[12:15], a[0:3], a[156:159], 0\n\t"
"ds_write_addtid_b32 v209 offset:2304\n\t"
"v_cvt_pk_bf16_f32 v217, v217, v221\n\t"
"v_mfma_f32_16x16x32_bf16 v[12:15], a[4:7], a[172:175], v[12:15]\n\t"
"s_waitcnt lgkmcnt(10)\n\t"
"buffer_load_dwordx4 a[12:15], v235, s[84:87], s57 offen offset:3072 sc1\n\t"
"v_mfma_f32_16x16x32_bf16 v[96:99], a[24:27], a[144:147], 0\n\t"
"ds_write_addtid_b32 v218 offset:14368\n\t"
"v_cvt_pk_bf16_f32 v216, v216, v220\n\t"
"v_mfma_f32_16x16x32_bf16 v[96:99], a[28:31], a[160:163], v[96:99]\n\t"
"buffer_load_dwordx4 a[8:11], v235, s[84:87], s57 offen offset:2048 sc1\n\t"
"v_mfma_f32_16x16x32_bf16 v[112:115], a[32:35], a[144:147], 0\n\t"
"ds_write_addtid_b32 v216 offset:6144\n\t"
"v_cvt_pk_bf16_f32 v219, v219, v223\n\t"
"v_mfma_f32_16x16x32_bf16 v[112:115], a[36:39], a[160:163], v[112:115]\n\t"
"buffer_load_dwordx4 a[0:3], v235, s[84:87], s57 offen offset:0 sc1\n\t"
"v_mfma_f32_16x16x32_bf16 v[128:131], a[40:43], a[144:147], 0\n\t"
"s_nop 0\n\t"
"ds_write_addtid_b32 v217 offset:6400\n\t"
"ds_write_addtid_b32 v219 offset:14624\n\t"
"v_mfma_f32_16x16x32_bf16 v[128:131], a[44:47], a[160:163], v[128:131]\n\t"
"s_waitcnt lgkmcnt(6)\n\t"
"buffer_load_dwordx4 a[4:7], v235, s[84:87], s57 offen offset:1024 sc1\n\t"
"v_mfma_f32_16x16x32_bf16 v[132:135], a[40:43], a[148:151], 0\n\t"
"s_nop 0\n\t"
"ds_read_b128 a[156:159], v224 offset:20032\n\t"
"ds_read_b64_tr_b16 a[204:205], v251 offset:21888\n\t"
"v_mfma_f32_16x16x32_bf16 v[132:135], a[44:47], a[164:167], v[132:135]\n\t"
"s_nop 0\n\t"
"ds_read_b128 a[152:155], v224 offset:19520\n\t"
"ds_read_b64_tr_b16 a[206:207], v251 offset:22400\n\t"
"v_mfma_f32_16x16x32_bf16 v[116:119], a[32:35], a[148:151], 0\n\t"
"s_nop 0\n\t"
"ds_read_b128 a[168:171], v224 offset:23616\n\t"
"ds_read_b128 a[172:175], v224 offset:24128\n\t"
"v_mfma_f32_16x16x32_bf16 v[116:119], a[36:39], a[164:167], v[116:119]\n\t"
"buffer_store_dwordx4 v[108:111], v232, s[16:19], s25 offen nt\n\t"
"v_mfma_f32_16x16x32_bf16 v[100:103], a[24:27], a[148:151], 0\n\t"
"v_mfma_f32_16x16x32_bf16 v[100:103], a[28:31], a[164:167], v[100:103]\n\t"
"s_waitcnt lgkmcnt(0)\n\t"
"s_barrier\n\t"
"v_mfma_f32_16x16x32_bf16 v[104:107], a[24:27], a[152:155], 0\n\t"
"s_add_u32 m0, s26, 83200\n\t"
"ds_read_b128 a[144:147], v224 offset:0\n\t"
"ds_read_b128 a[160:163], v224 offset:4096\n\t"
"v_mfma_f32_16x16x32_bf16 v[104:107], a[28:31], a[168:171], v[104:107]\n\t"
"buffer_load_dwordx4 v236, s[12:15], s22 offen offset:0 nt lds\n\t"
"v_mfma_f32_16x16x32_bf16 v[120:123], a[32:35], a[152:155], 0\n\t"
"ds_read_b128 a[148:151], v224 offset:512\n\t"
"v_mfma_f32_16x16x32_bf16 v[120:123], a[36:39], a[168:171], v[120:123]\n\t"
"ds_read_b128 a[164:167], v224 offset:4608\n\t"
"v_mfma_f32_16x16x32_bf16 v[136:139], a[40:43], a[152:155], 0\n\t"
"ds_read_b128 a[228:231], v255 offset:20736\n\t"
"v_mfma_f32_16x16x32_bf16 v[136:139], a[44:47], a[168:171], v[136:139]\n\t"
"ds_read_b128 a[152:155], v224 offset:1024\n\t"
"v_mfma_f32_16x16x32_bf16 v[192:195], a[216:219], a[176:179], 0\n\t"
"ds_read_b64_tr_b16 a[176:177], v248 offset:0\n\t"
"v_mfma_f32_16x16x32_bf16 v[140:143], a[40:43], a[156:159], 0\n\t"
"ds_read_b64_tr_b16 a[178:179], v248 offset:512\n\t"
"v_mfma_f32_16x16x32_bf16 v[140:143], a[44:47], a[172:175], v[140:143]\n\t"
"buffer_load_dwordx4 v237, s[12:15], s22 offen offset:1024 nt lds\n\t"
"v_mfma_f32_16x16x32_bf16 v[196:199], a[216:219], a[180:183], 0\n\t"
"ds_read_b64_tr_b16 a[180:181], v249 offset:0\n\t"
"v_mfma_f32_16x16x32_bf16 v[124:127], a[32:35], a[156:159], 0\n\t"
"ds_read_b64_tr_b16 a[182:183], v249 offset:512\n\t"
"v_mfma_f32_16x16x32_bf16 v[124:127], a[36:39], a[172:175], v[124:127]\n\t"
"ds_read_b128 a[168:171], v224 offset:5120\n\t"
"v_mfma_f32_16x16x32_bf16 v[108:111], a[24:27], a[156:159], 0\n\t"
"ds_read_b128 a[156:159], v224 offset:1536\n\t"
"v_mfma_f32_16x16x32_bf16 v[108:111], a[28:31], a[172:175], v[108:111]\n\t"
"buffer_load_dwordx4 v238, s[12:15], s22 offen offset:2048 nt lds\n\t"
"v_mfma_f32_16x16x32_bf16 v[200:203], a[216:219], a[184:187], 0\n\t"
"s_waitcnt vmcnt(33) lgkmcnt(7)\n\t"
"ds_read_b64_tr_b16 a[184:185], v250 offset:0\n\t"
"v_cvt_pk_bf16_f32 v194, v194, v198\n\t"
"v_mfma_f32_16x16x32_bf16 v[48:51], a[48:51], a[144:147], v[48:51]\n\t"
"s_lshl_b32 m0, s73, 9\n\t"
"ds_read_b64_tr_b16 a[186:187], v250 offset:512\n\t"
"v_mfma_f32_16x16x32_bf16 v[48:51], a[52:55], a[160:163], v[48:51]\n\t"
"ds_read_b128 a[172:175], v224 offset:5632\n\t"
"v_cvt_pk_bf16_f32 v193, v193, v197\n\t"
"v_mfma_f32_16x16x32_bf16 v[204:207], a[216:219], a[188:191], 0\n\t"
"ds_read_b64_tr_b16 a[188:189], v251 offset:0\n\t"
"v_mfma_f32_16x16x32_bf16 v[64:67], a[56:59], a[144:147], v[64:67]\n\t"
"ds_write_addtid_b32 v193 offset:16704\n\t"
"v_cvt_pk_bf16_f32 v195, v195, v199\n\t"
"s_add_u32 m0, s26, 83200\n\t"
"v_mfma_f32_16x16x32_bf16 v[64:67], a[60:63], a[160:163], v[64:67]\n\t"
"buffer_load_dwordx4 v239, s[12:15], s22 offen offset:3072 nt lds\n\t"
"s_lshl_b32 m0, s73, 9\n\t"
"v_mfma_f32_16x16x32_bf16 v[80:83], a[64:67], a[144:147], v[80:83]\n\t"
"ds_write_addtid_b32 v194 offset:24672\n\t"
"v_mfma_f32_16x16x32_bf16 v[80:83], a[68:71], a[160:163], v[80:83]\n\t"
"ds_read_b128 a[160:163], v224 offset:6144\n\t"
"v_cvt_pk_bf16_f32 v192, v192, v196\n\t"
"v_mfma_f32_16x16x32_bf16 v[208:211], a[216:219], a[192:195], 0\n\t"
"ds_read_b64_tr_b16 a[190:191], v251 offset:512\n\t"
"v_mfma_f32_16x16x32_bf16 v[84:87], a[64:67], a[148:151], v[84:87]\n\t"
"ds_write_addtid_b32 v195 offset:24928\n\t"
"v_cvt_pk_bf16_f32 v200, v200, v204\n\t"
"v_mfma_f32_16x16x32_bf16 v[84:87], a[68:71], a[164:167], v[84:87]\n\t"
"s_nop 0\n\t"
"ds_read_b128 a[144:147], v224 offset:2048\n\t"
"ds_write_addtid_b32 v192 offset:16448\n\t"
"v_mfma_f32_16x16x32_bf16 v[68:71], a[56:59], a[148:151], v[68:71]\n\t"
"ds_write_addtid_b32 v200 offset:20544\n\t"
"v_cvt_pk_bf16_f32 v201, v201, v205\n\t"
"s_add_u32 m0, s26, 83200\n\t"
"v_mfma_f32_16x16x32_bf16 v[68:71], a[60:63], a[164:167], v[68:71]\n\t"
"s_mov_b64 exec, s[36:37]\n\t"
"buffer_load_dwordx4 v240, s[12:15], s22 offen offset:3584 nt lds\n\t"
"s_mov_b64 exec, -1\n\t"
"v_mfma_f32_16x16x32_bf16 v[212:215], a[216:219], a[196:199], 0\n\t"
"s_lshl_b32 m0, s73, 9\n\t"
"ds_read_b64_tr_b16 a[192:193], v248 offset:1152\n\t"
"v_mfma_f32_16x16x32_bf16 v[52:55], a[48:51], a[148:151], v[52:55]\n\t"
"ds_read_b64_tr_b16 a[194:195], v248 offset:1664\n\t"
"v_cvt_pk_bf16_f32 v203, v203, v207\n\t"
"v_mfma_f32_16x16x32_bf16 v[52:55], a[52:55], a[164:167], v[52:55]\n\t"
"ds_read_b64_tr_b16 a[196:197], v249 offset:1152\n\t"
"v_mfma_f32_16x16x32_bf16 v[216:219], a[216:219], a[200:203], 0\n\t"
"s_waitcnt lgkmcnt(9)\n\t"
"ds_read_b128 a[164:167], v224 offset:6656\n\t"
"v_cvt_pk_bf16_f32 v202, v202, v206\n\t"
"v_mfma_f32_16x16x32_bf16 v[56:59], a[48:51], a[152:155], v[56:59]\n\t"
"ds_read_b64_tr_b16 a[198:199], v249 offset:1664\n\t"
"v_cvt_pk_bf16_f32 v209, v209, v213\n\t"
"v_mfma_f32_16x16x32_bf16 v[56:59], a[52:55], a[168:171], v[56:59]\n\t"
"ds_read_b128 a[148:151], v224 offset:2560\n\t"
"v_mfma_f32_16x16x32_bf16 v[72:75], a[56:59], a[152:155], v[72:75]\n\t"
"ds_read_b64_tr_b16 a[200:201], v250 offset:1152\n\t"
"v_cvt_pk_bf16_f32 v211, v211, v215\n\t"
"v_mfma_f32_16x16x32_bf16 v[72:75], a[60:63], a[168:171], v[72:75]\n\t"
"ds_write_addtid_b32 v203 offset:29024\n\t"
"v_cvt_pk_bf16_f32 v210, v210, v214\n\t"
"v_mfma_f32_16x16x32_bf16 v[220:223], a[216:219], a[204:207], 0\n\t"
"ds_write_addtid_b32 v202 offset:28768\n\t"
"v_cvt_pk_bf16_f32 v208, v208, v212\n\t"
"v_mfma_f32_16x16x32_bf16 v[88:91], a[64:67], a[152:155], v[88:91]\n\t"
"s_nop 0\n\t"
"ds_write_addtid_b32 v201 offset:20800\n\t"
"ds_write_addtid_b32 v211 offset:26976\n\t"
"v_mfma_f32_16x16x32_bf16 v[88:91], a[68:71], a[168:171], v[88:91]\n\t"
"ds_write_addtid_b32 v208 offset:18496\n\t"
"v_mfma_f32_16x16x32_bf16 v[92:95], a[64:67], a[156:159], v[92:95]\n\t"
"ds_read_b64_tr_b16 a[202:203], v250 offset:1664\n\t"
"v_mfma_f32_16x16x32_bf16 v[92:95], a[68:71], a[172:175], v[92:95]\n\t"
"ds_write_addtid_b32 v210 offset:26720\n\t"
"v_mfma_f32_16x16x32_bf16 v[76:79], a[56:59], a[156:159], v[76:79]\n\t"
"ds_read_b64_tr_b16 a[204:205], v251 offset:1152\n\t"
"v_cvt_pk_bf16_f32 v216, v216, v220\n\t"
"v_mfma_f32_16x16x32_bf16 v[76:79], a[60:63], a[172:175], v[76:79]\n\t"
"ds_read_b128 a[168:171], v224 offset:7168\n\t"
"v_cvt_pk_bf16_f32 v218, v218, v222\n\t"
"v_mfma_f32_16x16x32_bf16 v[60:63], a[48:51], a[156:159], v[60:63]\n\t"
"ds_write_addtid_b32 v209 offset:18752\n\t"
"v_cvt_pk_bf16_f32 v217, v217, v221\n\t"
"v_mfma_f32_16x16x32_bf16 v[60:63], a[52:55], a[172:175], v[60:63]\n\t"
"s_waitcnt vmcnt(28) lgkmcnt(10)\n\t"
"ds_read_b128 a[156:159], v224 offset:3584\n\t"
"v_mfma_f32_16x16x32_bf16 v[144:147], a[72:75], a[144:147], v[144:147]\n\t"
"ds_write_addtid_b32 v216 offset:22592\n\t"
"v_mfma_f32_16x16x32_bf16 v[144:147], a[76:79], a[160:163], v[144:147]\n\t"
"ds_write_addtid_b32 v217 offset:22848\n\t"
"v_mfma_f32_16x16x32_bf16 v[160:163], a[80:83], a[144:147], v[160:163]\n\t"
"ds_read_b64_tr_b16 a[206:207], v251 offset:1664\n\t"
"v_cvt_pk_bf16_f32 v219, v219, v223\n\t"
"v_mfma_f32_16x16x32_bf16 v[160:163], a[84:87], a[160:163], v[160:163]\n\t"
"s_nop 0\n\t"
"ds_read_b128 a[172:175], v224 offset:7680\n\t"
"ds_write_addtid_b32 v219 offset:31072\n\t"
"v_mfma_f32_16x16x32_bf16 v[176:179], a[88:91], a[144:147], v[176:179]\n\t"
"ds_write_addtid_b32 v218 offset:30816\n\t"
"v_mfma_f32_16x16x32_bf16 v[176:179], a[92:95], a[160:163], v[176:179]\n\t"
"ds_read_b128 a[152:155], v224 offset:3072\n\t"
"v_mfma_f32_16x16x32_bf16 v[180:183], a[88:91], a[148:151], v[180:183]\n\t"
"v_mfma_f32_16x16x32_bf16 v[180:183], a[92:95], a[164:167], v[180:183]\n\t"
"ds_write_b128 v254, a[228:231] offset:48000\n\t"
"v_mfma_f32_16x16x32_bf16 v[164:167], a[80:83], a[148:151], v[164:167]\n\t"
"v_mfma_f32_16x16x32_bf16 v[164:167], a[84:87], a[164:167], v[164:167]\n\t"
"buffer_load_dwordx4 a[40:43], v252, s[84:87], s63 offen offset:0 sc1\n\t"
"v_mfma_f32_16x16x32_bf16 v[148:151], a[72:75], a[148:151], v[148:151]\n\t"
"v_mfma_f32_16x16x32_bf16 v[148:151], a[76:79], a[164:167], v[148:151]\n\t"
"s_waitcnt lgkmcnt(0)\n\t"
"s_barrier\n\t"
"v_mfma_f32_16x16x32_bf16 v[152:155], a[72:75], a[152:155], v[152:155]\n\t"
"ds_read_b128 a[144:147], v224 offset:16448\n\t"
"v_mfma_f32_16x16x32_bf16 v[152:155], a[76:79], a[168:171], v[152:155]\n\t"
"ds_read_b128 a[148:151], v224 offset:16960\n\t"
"v_add_u32 v248, 143104, v226\n\t"
"v_mfma_f32_16x16x32_bf16 v[168:171], a[80:83], a[152:155], v[168:171]\n\t"
"ds_read_b128 a[160:163], v224 offset:20544\n\t"
"v_mfma_f32_16x16x32_bf16 v[168:171], a[84:87], a[168:171], v[168:171]\n\t"
"ds_read_b128 a[164:167], v224 offset:21056\n\t"
"v_xor_b32 v249, 32, v248\n\t"
"v_mfma_f32_16x16x32_bf16 v[184:187], a[88:91], a[152:155], v[184:187]\n\t"
"v_xor_b32 v250, 64, v248\n\t"
"v_mfma_f32_16x16x32_bf16 v[184:187], a[92:95], a[168:171], v[184:187]\n\t"
"v_swap_b32 v236, v241\n\t"
"v_xor_b32 v251, 96, v248\n\t"
"v_mfma_f32_16x16x32_bf16 v[192:195], a[224:227], a[176:179], 0\n\t"
"ds_read_b128 a[152:155], v224 offset:17472\n\t"
"v_swap_b32 v237, v242\n\t"
"v_mfma_f32_16x16x32_bf16 v[188:191], a[88:91], a[156:159], v[188:191]\n\t"
"v_swap_b32 v238, v243\n\t"
"v_mfma_f32_16x16x32_bf16 v[188:191], a[92:95], a[172:175], v[188:191]\n\t"
"ds_read_b64_tr_b16 a[176:177], v248 offset:0\n\t"
"v_swap_b32 v239, v244\n\t"
"v_mfma_f32_16x16x32_bf16 v[196:199], a[224:227], a[180:183], 0\n\t"
"ds_read_b128 a[168:171], v224 offset:21568\n\t"
"v_swap_b32 v240, v245\n\t"
"v_mfma_f32_16x16x32_bf16 v[172:175], a[80:83], a[156:159], v[172:175]\n\t"
"ds_read_b64_tr_b16 a[178:179], v248 offset:512\n\t"
"v_mfma_f32_16x16x32_bf16 v[172:175], a[84:87], a[172:175], v[172:175]\n\t"
"ds_read_b64_tr_b16 a[180:181], v248 offset:1024\n\t"
"v_mfma_f32_16x16x32_bf16 v[156:159], a[72:75], a[156:159], v[156:159]\n\t"
"ds_read_b128 a[156:159], v224 offset:17984\n\t"
"v_mfma_f32_16x16x32_bf16 v[156:159], a[76:79], a[172:175], v[156:159]\n\t"
"ds_read_b128 a[172:175], v224 offset:22080\n\t"
"v_mfma_f32_16x16x32_bf16 v[200:203], a[224:227], a[184:187], 0\n\t"
"s_waitcnt lgkmcnt(7)\n\t"
"ds_read_b64_tr_b16 a[184:185], v249 offset:0\n\t"
"v_cvt_pk_bf16_f32 v195, v195, v199\n\t"
"v_mfma_f32_16x16x32_bf16 v[0:3], a[48:51], a[144:147], v[0:3]\n\t"
"ds_write_addtid_b32 v195 offset:8480\n\t"
"v_cvt_pk_bf16_f32 v194, v194, v198\n\t"
"v_mfma_f32_16x16x32_bf16 v[0:3], a[52:55], a[160:163], v[0:3]\n\t"
"ds_read_b64_tr_b16 a[186:187], v249 offset:512\n\t"
"v_cvt_pk_bf16_f32 v192, v192, v196\n\t"
"v_mfma_f32_16x16x32_bf16 v[204:207], a[224:227], a[188:191], 0\n\t"
"ds_read_b64_tr_b16 a[188:189], v249 offset:1024\n\t"
"v_cvt_pk_bf16_f32 v193, v193, v197\n\t"
"v_mfma_f32_16x16x32_bf16 v[16:19], a[56:59], a[144:147], v[16:19]\n\t"
"ds_write_addtid_b32 v192 offset:0\n\t"
"v_add_u32 v225, v225, v253\n\t"
"v_mfma_f32_16x16x32_bf16 v[16:19], a[60:63], a[160:163], v[16:19]\n\t"
"ds_write_addtid_b32 v193 offset:256\n\t"
"v_mfma_f32_16x16x32_bf16 v[32:35], a[64:67], a[144:147], v[32:35]\n\t"
"ds_read_b128 a[144:147], v224 offset:18496\n\t"
"v_sub_u32 v253, 0, v253\n\t"
"v_mfma_f32_16x16x32_bf16 v[32:35], a[68:71], a[160:163], v[32:35]\n\t"
"ds_write_addtid_b32 v194 offset:8224\n\t"
"v_cvt_pk_bf16_f32 v203, v203, v207\n\t"
"v_mfma_f32_16x16x32_bf16 v[208:211], a[224:227], a[192:195], 0\n\t"
"ds_read_b64_tr_b16 a[192:193], v250 offset:0\n\t"
"v_cvt_pk_bf16_f32 v200, v200, v204\n\t"
"v_mfma_f32_16x16x32_bf16 v[36:39], a[64:67], a[148:151], v[36:39]\n\t"
"ds_read_b64_tr_b16 a[194:195], v250 offset:512\n\t"
"v_mfma_f32_16x16x32_bf16 v[36:39], a[68:71], a[164:167], v[36:39]\n\t"
"ds_read_b128 a[160:163], v224 offset:22592\n\t"
"v_mfma_f32_16x16x32_bf16 v[20:23], a[56:59], a[148:151], v[20:23]\n\t"
"ds_write_addtid_b32 v203 offset:12576\n\t"
"v_cvt_pk_bf16_f32 v202, v202, v206\n\t"
"v_mfma_f32_16x16x32_bf16 v[20:23], a[60:63], a[164:167], v[20:23]\n\t"
"ds_write_addtid_b32 v202 offset:12320\n\t"
"v_cvt_pk_bf16_f32 v201, v201, v205\n\t"
"v_mfma_f32_16x16x32_bf16 v[212:215], a[224:227], a[196:199], 0\n\t"
"buffer_load_dwordx4 a[44:47], v252, s[84:87], s63 offen offset:1024 sc1\n\t"
"v_mfma_f32_16x16x32_bf16 v[4:7], a[48:51], a[148:151], v[4:7]\n\t"
"ds_write_addtid_b32 v201 offset:4352\n\t"
"v_mfma_f32_16x16x32_bf16 v[4:7], a[52:55], a[164:167], v[4:7]\n\t"
"ds_read_b64_tr_b16 a[196:197], v250 offset:1024\n\t"
"v_mfma_f32_16x16x32_bf16 v[216:219], a[224:227], a[200:203], 0\n\t"
"s_waitcnt lgkmcnt(8)\n\t"
"ds_read_b128 a[164:167], v224 offset:23104\n\t"
"v_mfma_f32_16x16x32_bf16 v[8:11], a[48:51], a[152:155], v[8:11]\n\t"
"ds_read_b128 a[148:151], v224 offset:19008\n\t"
"v_cvt_pk_bf16_f32 v209, v209, v213\n\t"
"v_mfma_f32_16x16x32_bf16 v[8:11], a[52:55], a[168:171], v[8:11]\n\t"
"buffer_load_dwordx4 a[36:39], v235, s[84:87], s63 offen offset:3072 sc1\n\t"
"v_mfma_f32_16x16x32_bf16 v[24:27], a[56:59], a[152:155], v[24:27]\n\t"
"ds_write_addtid_b32 v200 offset:4096\n\t"
"v_cvt_pk_bf16_f32 v211, v211, v215\n\t"
"v_mfma_f32_16x16x32_bf16 v[24:27], a[60:63], a[168:171], v[24:27]\n\t"
"ds_read_b64_tr_b16 a[200:201], v251 offset:0\n\t"
"v_mfma_f32_16x16x32_bf16 v[220:223], a[224:227], a[204:207], 0\n\t"
"buffer_load_dwordx4 a[32:35], v235, s[84:87], s63 offen offset:2048 sc1\n\t"
"v_mfma_f32_16x16x32_bf16 v[40:43], a[64:67], a[152:155], v[40:43]\n\t"
"ds_read_b64_tr_b16 a[202:203], v251 offset:512\n\t"
"v_cvt_pk_bf16_f32 v208, v208, v212\n\t"
"v_mfma_f32_16x16x32_bf16 v[40:43], a[68:71], a[168:171], v[40:43]\n\t"
"ds_write_addtid_b32 v209 offset:2304\n\t"
"v_cvt_pk_bf16_f32 v210, v210, v214\n\t"
"v_mfma_f32_16x16x32_bf16 v[44:47], a[64:67], a[156:159], v[44:47]\n\t"
"ds_read_b64_tr_b16 a[204:205], v251 offset:1024\n\t"
"v_mfma_f32_16x16x32_bf16 v[44:47], a[68:71], a[172:175], v[44:47]\n\t"
"buffer_load_dwordx4 a[24:27], v235, s[84:87], s63 offen offset:0 sc1\n\t"
"v_mfma_f32_16x16x32_bf16 v[28:31], a[56:59], a[156:159], v[28:31]\n\t"
"ds_write_addtid_b32 v208 offset:2048\n\t"
"v_cvt_pk_bf16_f32 v219, v219, v223\n\t"
"v_mfma_f32_16x16x32_bf16 v[28:31], a[60:63], a[172:175], v[28:31]\n\t"
"ds_write_addtid_b32 v211 offset:10528\n\t"
"v_cvt_pk_bf16_f32 v218, v218, v222\n\t"
"v_mfma_f32_16x16x32_bf16 v[12:15], a[48:51], a[156:159], v[12:15]\n\t"
"ds_read_b128 a[156:159], v224 offset:20032\n\t"
"v_cvt_pk_bf16_f32 v216, v216, v220\n\t"
"v_mfma_f32_16x16x32_bf16 v[12:15], a[52:55], a[172:175], v[12:15]\n\t"
"s_waitcnt lgkmcnt(7)\n\t"
"buffer_load_dwordx4 a[28:31], v235, s[84:87], s63 offen offset:1024 sc1\n\t"
"v_mfma_f32_16x16x32_bf16 v[96:99], a[72:75], a[144:147], v[96:99]\n\t"
"ds_write_addtid_b32 v216 offset:6144\n\t"
"v_mfma_f32_16x16x32_bf16 v[96:99], a[76:79], a[160:163], v[96:99]\n\t"
"s_nop 0\n\t"
"ds_write_addtid_b32 v210 offset:10272\n\t"
"ds_write_addtid_b32 v219 offset:14624\n\t"
"v_mfma_f32_16x16x32_bf16 v[112:115], a[80:83], a[144:147], v[112:115]\n\t"
"ds_write_addtid_b32 v218 offset:14368\n\t"
"v_mfma_f32_16x16x32_bf16 v[112:115], a[84:87], a[160:163], v[112:115]\n\t"
"buffer_load_dwordx4 a[64:67], v252, s[84:87], s42 offen offset:0 sc1\n\t"
"v_mfma_f32_16x16x32_bf16 v[128:131], a[88:91], a[144:147], v[128:131]\n\t"
"ds_read_b128 a[152:155], v224 offset:19520\n\t"
"v_cvt_pk_bf16_f32 v217, v217, v221\n\t"
"v_mfma_f32_16x16x32_bf16 v[128:131], a[92:95], a[160:163], v[128:131]\n\t"
"ds_write_addtid_b32 v217 offset:6400\n\t"
"v_mfma_f32_16x16x32_bf16 v[132:135], a[88:91], a[148:151], v[132:135]\n\t"
"ds_read_b128 a[168:171], v224 offset:23616\n\t"
"v_accvgpr_mov_b32 a228, a208\n\t"
"v_mfma_f32_16x16x32_bf16 v[132:135], a[92:95], a[164:167], v[132:135]\n\t"
"buffer_load_dwordx4 a[68:71], v252, s[84:87], s42 offen offset:1024 sc1\n\t"
"v_mfma_f32_16x16x32_bf16 v[116:119], a[80:83], a[148:151], v[116:119]\n\t"
"ds_read_b128 a[172:175], v224 offset:24128\n\t"
"v_accvgpr_mov_b32 a208, a209\n\t"
"v_mfma_f32_16x16x32_bf16 v[116:119], a[84:87], a[164:167], v[116:119]\n\t"
"v_accvgpr_mov_b32 a209, a228\n\t"
"v_mfma_f32_16x16x32_bf16 v[100:103], a[72:75], a[148:151], v[100:103]\n\t"
"buffer_load_dwordx4 a[60:63], v235, s[84:87], s42 offen offset:3072 sc1\n\t"
"v_mfma_f32_16x16x32_bf16 v[100:103], a[76:79], a[164:167], v[100:103]\n\t"
"s_waitcnt vmcnt(9) lgkmcnt(5)\n\t"
"s_waitcnt lgkmcnt(0)\n\t"
"s_barrier\n\t"
"v_mfma_f32_16x16x32_bf16 v[104:107], a[72:75], a[152:155], v[104:107]\n\t"
"s_add_u32 m0, s26, 145280\n\t"
"ds_read_b128 a[144:147], v224 offset:0\n\t"
"ds_read_b128 a[160:163], v224 offset:4096\n\t"
"v_mfma_f32_16x16x32_bf16 v[104:107], a[76:79], a[168:171], v[104:107]\n\t"
"buffer_load_dwordx4 v236, s[12:15], s22 offen offset:128 nt lds\n\t"
"v_mfma_f32_16x16x32_bf16 v[120:123], a[80:83], a[152:155], v[120:123]\n\t"
"ds_read_b128 a[148:151], v224 offset:512\n\t"
"v_add_u32 v248, 80896, v227\n\t"
"v_mfma_f32_16x16x32_bf16 v[120:123], a[84:87], a[168:171], v[120:123]\n\t"
"ds_read_b128 a[164:167], v224 offset:4608\n\t"
"v_xor_b32 v249, 32, v248\n\t"
"v_mfma_f32_16x16x32_bf16 v[136:139], a[88:91], a[152:155], v[136:139]\n\t"
"ds_read_b128 a[152:155], v224 offset:1024\n\t"
"v_xor_b32 v250, 64, v248\n\t"
"v_mfma_f32_16x16x32_bf16 v[136:139], a[92:95], a[168:171], v[136:139]\n\t"
"buffer_store_dwordx4 a[232:235], v233, s[16:19], s25 offen nt\n\t"
"v_mfma_f32_16x16x32_bf16 v[192:195], a[220:223], a[176:179], 0\n\t"
"ds_read_b128 a[168:171], v224 offset:5120\n\t"
"v_xor_b32 v251, 96, v248\n\t"
"v_mfma_f32_16x16x32_bf16 v[140:143], a[88:91], a[156:159], v[140:143]\n\t"
"ds_read_b64_tr_b16 a[182:183], v249 offset:512\n\t"
"v_mfma_f32_16x16x32_bf16 v[140:143], a[92:95], a[172:175], v[140:143]\n\t"
"ds_read_b64_tr_b16 a[190:191], v251 offset:512\n\t"
"v_mfma_f32_16x16x32_bf16 v[196:199], a[220:223], a[184:187], 0\n\t"
"ds_read_b64_tr_b16 a[198:199], v249 offset:1664\n\t"
"v_mfma_f32_16x16x32_bf16 v[124:127], a[80:83], a[156:159], v[124:127]\n\t"
"buffer_load_dwordx4 v237, s[12:15], s22 offen offset:1152 nt lds\n\t"
"v_mfma_f32_16x16x32_bf16 v[124:127], a[84:87], a[172:175], v[124:127]\n\t"
"ds_write_b128 v255, a[244:247] offset:43776\n\t"
"v_mfma_f32_16x16x32_bf16 v[108:111], a[72:75], a[156:159], v[108:111]\n\t"
"v_mfma_f32_16x16x32_bf16 v[108:111], a[76:79], a[172:175], v[108:111]\n\t"
"buffer_load_dwordx4 v238, s[12:15], s22 offen offset:2176 nt lds\n\t"
"v_mfma_f32_16x16x32_bf16 v[200:203], a[220:223], a[178:181], 0\n\t"
"s_waitcnt lgkmcnt(5)\n\t"
"v_cvt_pk_bf16_f32 v192, v192, v196\n\t"
"v_cvt_pk_bf16_f32 v195, v195, v199\n\t"
"v_mfma_f32_16x16x32_bf16 v[48:51], a[96:99], a[144:147], v[48:51]\n\t"
"ds_read_b128 a[172:175], v224 offset:5632\n\t"
"v_cvt_pk_bf16_f32 v194, v194, v198\n\t"
"v_mfma_f32_16x16x32_bf16 v[48:51], a[100:103], a[160:163], v[48:51]\n\t"
"ds_read_b64_tr_b16 a[206:207], v251 offset:1664\n\t"
"v_mfma_f32_16x16x32_bf16 v[204:207], a[220:223], a[186:189], 0\n\t"
"ds_read_b128 a[156:159], v224 offset:1536\n\t"
"v_cvt_pk_bf16_f32 v193, v193, v197\n\t"
"v_mfma_f32_16x16x32_bf16 v[64:67], a[104:107], a[144:147], v[64:67]\n\t"
"ds_read_b64_tr_b16 a[176:177], v248 offset:0\n\t"
"v_mfma_f32_16x16x32_bf16 v[64:67], a[108:111], a[160:163], v[64:67]\n\t"
"buffer_load_dwordx4 v239, s[12:15], s22 offen offset:3200 nt lds\n\t"
"s_lshl_b32 m0, s73, 9\n\t"
"v_mfma_f32_16x16x32_bf16 v[80:83], a[112:115], a[144:147], v[80:83]\n\t"
"ds_write_addtid_b32 v194 offset:18496\n\t"
"v_mfma_f32_16x16x32_bf16 v[80:83], a[116:119], a[160:163], v[80:83]\n\t"
"ds_read_b64_tr_b16 a[184:185], v250 offset:0\n\t"
"v_cvt_pk_bf16_f32 v201, v201, v205\n\t"
"v_mfma_f32_16x16x32_bf16 v[208:211], a[220:223], a[192:195], 0\n\t"
"buffer_store_dwordx4 a[236:239], v234, s[16:19], s25 offen nt\n\t"
"v_mfma_f32_16x16x32_bf16 v[84:87], a[112:115], a[148:151], v[84:87]\n\t"
"ds_read_b128 a[160:163], v224 offset:6144\n\t"
"v_cvt_pk_bf16_f32 v202, v202, v206\n\t"
"v_mfma_f32_16x16x32_bf16 v[84:87], a[116:119], a[164:167], v[84:87]\n\t"
"ds_write_addtid_b32 v193 offset:24672\n\t"
"v_mfma_f32_16x16x32_bf16 v[68:71], a[104:107], a[148:151], v[68:71]\n\t"
"ds_write_addtid_b32 v192 offset:16448\n\t"
"s_add_u32 m0, s26, 145280\n\t"
"v_mfma_f32_16x16x32_bf16 v[68:71], a[108:111], a[164:167], v[68:71]\n\t"
"s_mov_b64 exec, s[36:37]\n\t"
"buffer_load_dwordx4 v240, s[12:15], s22 offen offset:3712 nt lds\n\t"
"s_mov_b64 exec, -1\n\t"
"s_lshl_b32 m0, s73, 9\n\t"
"v_mfma_f32_16x16x32_bf16 v[212:215], a[220:223], a[200:203], 0\n\t"
"ds_write_addtid_b32 v195 offset:26720\n\t"
"v_cvt_pk_bf16_f32 v203, v203, v207\n\t"
"v_mfma_f32_16x16x32_bf16 v[52:55], a[96:99], a[148:151], v[52:55]\n\t"
"ds_read_b128 a[144:147], v224 offset:2048\n\t"
"v_cvt_pk_bf16_f32 v200, v200, v204\n\t"
"v_mfma_f32_16x16x32_bf16 v[52:55], a[100:103], a[164:167], v[52:55]\n\t"
"ds_read_b128 a[164:167], v224 offset:6656\n\t"
"v_mfma_f32_16x16x32_bf16 v[216:219], a[220:223], a[194:197], 0\n\t"
"s_waitcnt lgkmcnt(7)\n\t"
"ds_write_addtid_b32 v201 offset:24928\n\t"
"v_mfma_f32_16x16x32_bf16 v[56:59], a[96:99], a[152:155], v[56:59]\n\t"
"ds_write_addtid_b32 v203 offset:26976\n\t"
"v_cvt_pk_bf16_f32 v209, v209, v213\n\t"
"v_mfma_f32_16x16x32_bf16 v[56:59], a[100:103], a[168:171], v[56:59]\n\t"
"ds_write_addtid_b32 v202 offset:18752\n\t"
"v_cvt_pk_bf16_f32 v210, v210, v214\n\t"
"v_mfma_f32_16x16x32_bf16 v[72:75], a[104:107], a[152:155], v[72:75]\n\t"
"ds_read_b128 a[148:151], v224 offset:2560\n\t"
"v_cvt_pk_bf16_f32 v211, v211, v215\n\t"
"v_mfma_f32_16x16x32_bf16 v[72:75], a[108:111], a[168:171], v[72:75]\n\t"
"ds_write_addtid_b32 v200 offset:16704\n\t"
"v_mfma_f32_16x16x32_bf16 v[220:223], a[220:223], a[202:205], 0\n\t"
"ds_write_addtid_b32 v211 offset:30816\n\t"
"v_cvt_pk_bf16_f32 v208, v208, v212\n\t"
"v_mfma_f32_16x16x32_bf16 v[88:91], a[112:115], a[152:155], v[88:91]\n\t"
"s_nop 0\n\t"
"ds_write_addtid_b32 v208 offset:20544\n\t"
"ds_read_b64_tr_b16 a[178:179], v248 offset:512\n\t"
"v_mfma_f32_16x16x32_bf16 v[88:91], a[116:119], a[168:171], v[88:91]\n\t"
"ds_read_b64_tr_b16 a[180:181], v249 offset:0\n\t"
"v_mfma_f32_16x16x32_bf16 v[92:95], a[112:115], a[156:159], v[92:95]\n\t"
"s_nop 0\n\t"
"ds_read_b64_tr_b16 a[186:187], v250 offset:512\n\t"
"ds_read_b64_tr_b16 a[188:189], v251 offset:0\n\t"
"v_mfma_f32_16x16x32_bf16 v[92:95], a[116:119], a[172:175], v[92:95]\n\t"
"ds_read_b64_tr_b16 a[192:193], v248 offset:1152\n\t"
"v_cvt_pk_bf16_f32 v217, v217, v221\n\t"
"v_mfma_f32_16x16x32_bf16 v[76:79], a[104:107], a[156:159], v[76:79]\n\t"
"s_nop 0\n\t"
"ds_read_b128 a[152:155], v224 offset:3072\n\t"
"ds_read_b64_tr_b16 a[200:201], v250 offset:1152\n\t"
"v_mfma_f32_16x16x32_bf16 v[76:79], a[108:111], a[172:175], v[76:79]\n\t"
"s_nop 0\n\t"
"ds_read_b128 a[168:171], v224 offset:7168\n\t"
"ds_read_b64_tr_b16 a[194:195], v248 offset:1664\n\t"
"v_mfma_f32_16x16x32_bf16 v[60:63], a[96:99], a[156:159], v[60:63]\n\t"
"ds_read_b128 a[156:159], v224 offset:3584\n\t"
"v_cvt_pk_bf16_f32 v219, v219, v223\n\t"
"v_mfma_f32_16x16x32_bf16 v[60:63], a[100:103], a[172:175], v[60:63]\n\t"
"s_waitcnt lgkmcnt(13)\n\t"
"ds_write_addtid_b32 v219 offset:31072\n\t"
"v_mfma_f32_16x16x32_bf16 v[144:147], a[120:123], a[144:147], v[144:147]\n\t"
"ds_write_addtid_b32 v209 offset:28768\n\t"
"v_cvt_pk_bf16_f32 v216, v216, v220\n\t"
"v_mfma_f32_16x16x32_bf16 v[144:147], a[124:127], a[160:163], v[144:147]\n\t"
"ds_write_addtid_b32 v216 offset:20800\n\t"
"v_mfma_f32_16x16x32_bf16 v[160:163], a[128:131], a[144:147], v[160:163]\n\t"
"ds_write_addtid_b32 v210 offset:22592\n\t"
"v_mfma_f32_16x16x32_bf16 v[160:163], a[132:135], a[160:163], v[160:163]\n\t"
"ds_read_b64_tr_b16 a[196:197], v249 offset:1152\n\t"
"v_cvt_pk_bf16_f32 v218, v218, v222\n\t"
"v_mfma_f32_16x16x32_bf16 v[176:179], a[136:139], a[144:147], v[176:179]\n\t"
"ds_write_addtid_b32 v217 offset:29024\n\t"
"v_mfma_f32_16x16x32_bf16 v[176:179], a[140:143], a[160:163], v[176:179]\n\t"
"ds_write_addtid_b32 v218 offset:22848\n\t"
"v_mfma_f32_16x16x32_bf16 v[180:183], a[136:139], a[148:151], v[180:183]\n\t"
"ds_read_b64_tr_b16 a[202:203], v250 offset:1664\n\t"
"v_mfma_f32_16x16x32_bf16 v[180:183], a[140:143], a[164:167], v[180:183]\n\t"
"ds_read_b64_tr_b16 a[204:205], v251 offset:1152\n\t"
"v_mfma_f32_16x16x32_bf16 v[164:167], a[128:131], a[148:151], v[164:167]\n\t"
"ds_read_b128 a[172:175], v224 offset:7680\n\t"
"v_mfma_f32_16x16x32_bf16 v[164:167], a[132:135], a[164:167], v[164:167]\n\t"
"v_mfma_f32_16x16x32_bf16 v[148:151], a[120:123], a[148:151], v[148:151]\n\t"
"v_mfma_f32_16x16x32_bf16 v[148:151], a[124:127], a[164:167], v[148:151]\n\t"
"s_waitcnt lgkmcnt(0)\n\t"
"s_barrier\n\t"
"v_mfma_f32_16x16x32_bf16 v[152:155], a[120:123], a[152:155], v[152:155]\n\t"
"ds_read_b128 a[160:163], v224 offset:20544\n\t"
"v_mfma_f32_16x16x32_bf16 v[152:155], a[124:127], a[168:171], v[152:155]\n\t"
"ds_read_b128 a[144:147], v224 offset:16448\n\t"
"v_mfma_f32_16x16x32_bf16 v[168:171], a[128:131], a[152:155], v[168:171]\n\t"
"ds_read_b128 a[148:151], v224 offset:16960\n\t"
"v_add_u32 v248, 53632, v226\n\t"
"v_mfma_f32_16x16x32_bf16 v[168:171], a[132:135], a[168:171], v[168:171]\n\t"
"ds_read_b128 a[164:167], v224 offset:21056\n\t"
"v_mfma_f32_16x16x32_bf16 v[184:187], a[136:139], a[152:155], v[184:187]\n\t"
"s_xor_b32 s24, s24, 384\n\t"
"v_mfma_f32_16x16x32_bf16 v[184:187], a[140:143], a[168:171], v[184:187]\n\t"
"v_xor_b32 v249, 32, v248\n\t"
"v_xor_b32 v250, 64, v248\n\t"
"v_mfma_f32_16x16x32_bf16 v[192:195], a[216:219], a[176:179], 0\n\t"
"v_xor_b32 v251, 96, v248\n\t"
"v_mfma_f32_16x16x32_bf16 v[188:191], a[136:139], a[156:159], v[188:191]\n\t"
"ds_read_b128 a[168:171], v224 offset:21568\n\t"
"v_mfma_f32_16x16x32_bf16 v[188:191], a[140:143], a[172:175], v[188:191]\n\t"
"ds_read_b128 a[152:155], v224 offset:17472\n\t"
"v_mfma_f32_16x16x32_bf16 v[196:199], a[216:219], a[180:183], 0\n\t"
"ds_read_b64_tr_b16 a[176:177], v248 offset:48000\n\t"
"v_mfma_f32_16x16x32_bf16 v[172:175], a[128:131], a[156:159], v[172:175]\n\t"
"ds_read_b64_tr_b16 a[178:179], v248 offset:48512\n\t"
"v_mfma_f32_16x16x32_bf16 v[172:175], a[132:135], a[172:175], v[172:175]\n\t"
"ds_read_b64_tr_b16 a[180:181], v248 offset:49024\n\t"
"v_mfma_f32_16x16x32_bf16 v[156:159], a[120:123], a[156:159], v[156:159]\n\t"
"ds_read_b128 a[156:159], v224 offset:17984\n\t"
"v_mfma_f32_16x16x32_bf16 v[156:159], a[124:127], a[172:175], v[156:159]\n\t"
"v_cvt_pk_bf16_f32 v193, v193, v197\n\t"
"v_cvt_pk_bf16_f32 v194, v194, v198\n\t"
"v_mfma_f32_16x16x32_bf16 v[200:203], a[216:219], a[184:187], 0\n\t"
"s_waitcnt lgkmcnt(6)\n\t"
"ds_read_b64_tr_b16 a[184:185], v249 offset:48000\n\t"
"v_pk_add_f32 v[144:145], v[48:49], v[144:145] neg_lo:[0,1] neg_hi:[0,1]\n\t"
"v_pk_add_f32 v[146:147], v[50:51], v[146:147] neg_lo:[0,1] neg_hi:[0,1]\n\t"
"v_pk_fma_f32 v[48:49], v[144:145], s[90:91], v[48:49] neg_lo:[1,0,0] neg_hi:[1,0,0]\n\t"
"v_pk_fma_f32 v[50:51], v[146:147], s[90:91], v[50:51] neg_lo:[1,0,0] neg_hi:[1,0,0]\n\t"
"v_pk_add_f32 v[160:161], v[64:65], v[160:161] neg_lo:[0,1] neg_hi:[0,1]\n\t"
"v_pk_add_f32 v[162:163], v[66:67], v[162:163] neg_lo:[0,1] neg_hi:[0,1]\n\t"
"v_pk_fma_f32 v[64:65], v[160:161], s[90:91], v[64:65] neg_lo:[1,0,0] neg_hi:[1,0,0]\n\t"
"v_pk_fma_f32 v[66:67], v[162:163], s[90:91], v[66:67] neg_lo:[1,0,0] neg_hi:[1,0,0]\n\t"
"v_pk_add_f32 v[176:177], v[80:81], v[176:177] neg_lo:[0,1] neg_hi:[0,1]\n\t"
"v_pk_add_f32 v[178:179], v[82:83], v[178:179] neg_lo:[0,1] neg_hi:[0,1]\n\t"
"v_pk_fma_f32 v[80:81], v[176:177], s[90:91], v[80:81] neg_lo:[1,0,0] neg_hi:[1,0,0]\n\t"
"v_pk_fma_f32 v[82:83], v[178:179], s[90:91], v[82:83] neg_lo:[1,0,0] neg_hi:[1,0,0]\n\t"
"v_pk_add_f32 v[180:181], v[84:85], v[180:181] neg_lo:[0,1] neg_hi:[0,1]\n\t"
"v_pk_add_f32 v[182:183], v[86:87], v[182:183] neg_lo:[0,1] neg_hi:[0,1]\n\t"
"v_pk_fma_f32 v[84:85], v[180:181], s[90:91], v[84:85] neg_lo:[1,0,0] neg_hi:[1,0,0]\n\t"
"v_pk_fma_f32 v[86:87], v[182:183], s[90:91], v[86:87] neg_lo:[1,0,0] neg_hi:[1,0,0]\n\t"
"v_pk_add_f32 v[164:165], v[68:69], v[164:165] neg_lo:[0,1] neg_hi:[0,1]\n\t"
"v_pk_add_f32 v[166:167], v[70:71], v[166:167] neg_lo:[0,1] neg_hi:[0,1]\n\t"
"v_pk_fma_f32 v[68:69], v[164:165], s[90:91], v[68:69] neg_lo:[1,0,0] neg_hi:[1,0,0]\n\t"
"v_pk_fma_f32 v[70:71], v[166:167], s[90:91], v[70:71] neg_lo:[1,0,0] neg_hi:[1,0,0]\n\t"
"v_pk_add_f32 v[148:149], v[52:53], v[148:149] neg_lo:[0,1] neg_hi:[0,1]\n\t"
"v_pk_add_f32 v[150:151], v[54:55], v[150:151] neg_lo:[0,1] neg_hi:[0,1]\n\t"
"v_pk_fma_f32 v[52:53], v[148:149], s[90:91], v[52:53] neg_lo:[1,0,0] neg_hi:[1,0,0]\n\t"
"v_pk_fma_f32 v[54:55], v[150:151], s[90:91], v[54:55] neg_lo:[1,0,0] neg_hi:[1,0,0]\n\t"
"v_pk_add_f32 v[152:153], v[56:57], v[152:153] neg_lo:[0,1] neg_hi:[0,1]\n\t"
"v_pk_add_f32 v[154:155], v[58:59], v[154:155] neg_lo:[0,1] neg_hi:[0,1]\n\t"
"v_pk_fma_f32 v[56:57], v[152:153], s[90:91], v[56:57] neg_lo:[1,0,0] neg_hi:[1,0,0]\n\t"
"v_pk_fma_f32 v[58:59], v[154:155], s[90:91], v[58:59] neg_lo:[1,0,0] neg_hi:[1,0,0]\n\t"
"v_pk_add_f32 v[168:169], v[72:73], v[168:169] neg_lo:[0,1] neg_hi:[0,1]\n\t"
"v_pk_add_f32 v[170:171], v[74:75], v[170:171] neg_lo:[0,1] neg_hi:[0,1]\n\t"
"v_pk_fma_f32 v[72:73], v[168:169], s[90:91], v[72:73] neg_lo:[1,0,0] neg_hi:[1,0,0]\n\t"
"v_pk_fma_f32 v[74:75], v[170:171], s[90:91], v[74:75] neg_lo:[1,0,0] neg_hi:[1,0,0]\n\t"
"v_pk_add_f32 v[184:185], v[88:89], v[184:185] neg_lo:[0,1] neg_hi:[0,1]\n\t"
"v_pk_add_f32 v[186:187], v[90:91], v[186:187] neg_lo:[0,1] neg_hi:[0,1]\n\t"
"v_pk_fma_f32 v[88:89], v[184:185], s[90:91], v[88:89] neg_lo:[1,0,0] neg_hi:[1,0,0]\n\t"
"v_pk_fma_f32 v[90:91], v[186:187], s[90:91], v[90:91] neg_lo:[1,0,0] neg_hi:[1,0,0]\n\t"
"v_pk_add_f32 v[188:189], v[92:93], v[188:189] neg_lo:[0,1] neg_hi:[0,1]\n\t"
"v_pk_add_f32 v[190:191], v[94:95], v[190:191] neg_lo:[0,1] neg_hi:[0,1]\n\t"
"v_pk_fma_f32 v[92:93], v[188:189], s[90:91], v[92:93] neg_lo:[1,0,0] neg_hi:[1,0,0]\n\t"
"v_pk_fma_f32 v[94:95], v[190:191], s[90:91], v[94:95] neg_lo:[1,0,0] neg_hi:[1,0,0]\n\t"
"v_pk_add_f32 v[172:173], v[76:77], v[172:173] neg_lo:[0,1] neg_hi:[0,1]\n\t"
"v_pk_add_f32 v[174:175], v[78:79], v[174:175] neg_lo:[0,1] neg_hi:[0,1]\n\t"
"v_pk_fma_f32 v[76:77], v[172:173], s[90:91], v[76:77] neg_lo:[1,0,0] neg_hi:[1,0,0]\n\t"
"v_pk_fma_f32 v[78:79], v[174:175], s[90:91], v[78:79] neg_lo:[1,0,0] neg_hi:[1,0,0]\n\t"
"v_pk_add_f32 v[156:157], v[60:61], v[156:157] neg_lo:[0,1] neg_hi:[0,1]\n\t"
"v_pk_add_f32 v[158:159], v[62:63], v[158:159] neg_lo:[0,1] neg_hi:[0,1]\n\t"
"v_pk_fma_f32 v[60:61], v[156:157], s[90:91], v[60:61] neg_lo:[1,0,0] neg_hi:[1,0,0]\n\t"
"v_pk_fma_f32 v[62:63], v[158:159], s[90:91], v[62:63] neg_lo:[1,0,0] neg_hi:[1,0,0]\n\t"
"v_mfma_f32_16x16x32_bf16 v[48:51], a[0:3], a[144:147], v[48:51]\n\t"
"ds_read_b64_tr_b16 a[186:187], v249 offset:48512\n\t"
"v_cvt_pk_bf16_f32 v192, v192, v196\n\t"
"v_mfma_f32_16x16x32_bf16 v[48:51], a[4:7], a[160:163], v[48:51]\n\t"
"ds_write_addtid_b32 v192 offset:0\n\t"
"v_mfma_f32_16x16x32_bf16 v[204:207], a[216:219], a[188:191], 0\n\t"
"ds_read_b64_tr_b16 a[188:189], v249 offset:49024\n\t"
"v_mfma_f32_16x16x32_bf16 v[64:67], a[8:11], a[144:147], v[64:67]\n\t"
"ds_write_addtid_b32 v194 offset:8224\n\t"
"v_mfma_f32_16x16x32_bf16 v[64:67], a[12:15], a[160:163], v[64:67]\n\t"
"ds_read_b128 a[172:175], v224 offset:22080\n\t"
"v_cvt_pk_bf16_f32 v195, v195, v199\n\t"
"v_mfma_f32_16x16x32_bf16 v[80:83], a[16:19], a[144:147], v[80:83]\n\t"
"ds_write_addtid_b32 v195 offset:8480\n\t"
"v_mfma_f32_16x16x32_bf16 v[80:83], a[20:23], a[160:163], v[80:83]\n\t"
"ds_read_b128 a[144:147], v224 offset:18496\n\t"
"v_cvt_pk_bf16_f32 v200, v200, v204\n\t"
"v_mfma_f32_16x16x32_bf16 v[208:211], a[216:219], a[192:195], 0\n\t"
"ds_read_b64_tr_b16 a[192:193], v250 offset:48000\n\t"
"v_cvt_pk_bf16_f32 v201, v201, v205\n\t"
"v_mfma_f32_16x16x32_bf16 v[84:87], a[16:19], a[148:151], v[84:87]\n\t"
"ds_write_addtid_b32 v201 offset:4352\n\t"
"v_cvt_pk_bf16_f32 v202, v202, v206\n\t"
"v_mfma_f32_16x16x32_bf16 v[84:87], a[20:23], a[164:167], v[84:87]\n\t"
"buffer_load_dwordx4 a[56:59], v235, s[84:87], s42 offen offset:2048 sc1\n\t"
"v_mfma_f32_16x16x32_bf16 v[68:71], a[8:11], a[148:151], v[68:71]\n\t"
"s_xor_b32 s20, s20, 384\n\t"
"ds_write_addtid_b32 v200 offset:4096\n\t"
"ds_read_b64_tr_b16 a[194:195], v250 offset:48512\n\t"
"v_mfma_f32_16x16x32_bf16 v[68:71], a[12:15], a[164:167], v[68:71]\n\t"
"ds_read_b128 a[160:163], v224 offset:22592\n\t"
"v_mfma_f32_16x16x32_bf16 v[212:215], a[216:219], a[196:199], 0\n\t"
"ds_write_addtid_b32 v193 offset:256\n\t"
"v_mfma_f32_16x16x32_bf16 v[52:55], a[0:3], a[148:151], v[52:55]\n\t"
"ds_read_b64_tr_b16 a[196:197], v250 offset:49024\n\t"
"v_cvt_pk_bf16_f32 v203, v203, v207\n\t"
"v_mfma_f32_16x16x32_bf16 v[52:55], a[4:7], a[164:167], v[52:55]\n\t"
"buffer_load_dwordx4 a[48:51], v235, s[84:87], s42 offen offset:0 sc1\n\t"
"v_mfma_f32_16x16x32_bf16 v[216:219], a[216:219], a[200:203], 0\n\t"
"s_waitcnt lgkmcnt(8)\n\t"
"ds_read_b128 a[148:151], v224 offset:19008\n\t"
"ds_read_b128 a[164:167], v224 offset:23104\n\t"
"s_xor_b32 s27, s27, 384\n\t"
"v_mfma_f32_16x16x32_bf16 v[56:59], a[0:3], a[152:155], v[56:59]\n\t"
"ds_write_addtid_b32 v203 offset:12576\n\t"
"v_cvt_pk_bf16_f32 v208, v208, v212\n\t"
"v_mfma_f32_16x16x32_bf16 v[56:59], a[4:7], a[168:171], v[56:59]\n\t"
"buffer_load_dwordx4 a[52:55], v235, s[84:87], s42 offen offset:1024 sc1\n\t"
"v_mfma_f32_16x16x32_bf16 v[72:75], a[8:11], a[152:155], v[72:75]\n\t"
"ds_write_addtid_b32 v208 offset:2048\n\t"
"v_cvt_pk_bf16_f32 v211, v211, v215\n\t"
"v_mfma_f32_16x16x32_bf16 v[72:75], a[12:15], a[168:171], v[72:75]\n\t"
"ds_write_addtid_b32 v202 offset:12320\n\t"
"v_mfma_f32_16x16x32_bf16 v[220:223], a[216:219], a[204:207], 0\n\t"
"buffer_load_dwordx4 a[88:91], v252, s[84:87], s48 offen offset:0 sc1\n\t"
"v_mfma_f32_16x16x32_bf16 v[88:91], a[16:19], a[152:155], v[88:91]\n\t"
"ds_read_b128 a[152:155], v224 offset:19520\n\t"
"v_cvt_pk_bf16_f32 v209, v209, v213\n\t"
"v_mfma_f32_16x16x32_bf16 v[88:91], a[20:23], a[168:171], v[88:91]\n\t"
"ds_read_b64_tr_b16 a[200:201], v251 offset:48000\n\t"
"v_cvt_pk_bf16_f32 v210, v210, v214\n\t"
"v_mfma_f32_16x16x32_bf16 v[92:95], a[16:19], a[156:159], v[92:95]\n\t"
"ds_write_addtid_b32 v210 offset:10272\n\t"
"v_mfma_f32_16x16x32_bf16 v[92:95], a[20:23], a[172:175], v[92:95]\n\t"
"buffer_load_dwordx4 a[92:95], v252, s[84:87], s48 offen offset:1024 sc1\n\t"
"v_mfma_f32_16x16x32_bf16 v[76:79], a[8:11], a[156:159], v[76:79]\n\t"
"ds_read_b64_tr_b16 a[202:203], v251 offset:48512\n\t"
"v_mfma_f32_16x16x32_bf16 v[76:79], a[12:15], a[172:175], v[76:79]\n\t"
"ds_read_b128 a[168:171], v224 offset:23616\n\t"
"v_cvt_pk_bf16_f32 v217, v217, v221\n\t"
"v_mfma_f32_16x16x32_bf16 v[60:63], a[0:3], a[156:159], v[60:63]\n\t"
"ds_read_b64_tr_b16 a[204:205], v251 offset:49024\n\t"
"v_cvt_pk_bf16_f32 v219, v219, v223\n\t"
"v_mfma_f32_16x16x32_bf16 v[60:63], a[4:7], a[172:175], v[60:63]\n\t"
"s_waitcnt vmcnt(15) lgkmcnt(7)\n\t"
"buffer_load_dwordx4 a[84:87], v235, s[84:87], s48 offen offset:3072 sc1\n\t"
"v_mfma_f32_16x16x32_bf16 v[144:147], a[24:27], a[144:147], v[144:147]\n\t"
"ds_write_addtid_b32 v211 offset:10528\n\t"
"v_cvt_pk_bf16_f32 v218, v218, v222\n\t"
"v_mfma_f32_16x16x32_bf16 v[144:147], a[28:31], a[160:163], v[144:147]\n\t"
"ds_write_addtid_b32 v218 offset:14368\n\t"
"v_mfma_f32_16x16x32_bf16 v[160:163], a[32:35], a[144:147], v[160:163]\n\t"
"ds_write_addtid_b32 v209 offset:2304\n\t"
"v_cvt_pk_bf16_f32 v216, v216, v220\n\t"
"v_mfma_f32_16x16x32_bf16 v[160:163], a[36:39], a[160:163], v[160:163]\n\t"
"buffer_load_dwordx4 a[80:83], v235, s[84:87], s48 offen offset:2048 sc1\n\t"
"v_mfma_f32_16x16x32_bf16 v[176:179], a[40:43], a[144:147], v[176:179]\n\t"
"s_xor_b32 s25, s25, 384\n\t"
"ds_write_addtid_b32 v216 offset:6144\n\t"
"ds_write_addtid_b32 v217 offset:6400\n\t"
"v_mfma_f32_16x16x32_bf16 v[176:179], a[44:47], a[160:163], v[176:179]\n\t"
"ds_write_addtid_b32 v219 offset:14624\n\t"
"v_mfma_f32_16x16x32_bf16 v[180:183], a[40:43], a[148:151], v[180:183]\n\t"
"ds_read_b128 a[156:159], v224 offset:20032\n\t"
"v_mfma_f32_16x16x32_bf16 v[180:183], a[44:47], a[164:167], v[180:183]\n\t"
"buffer_load_dwordx4 a[72:75], v235, s[84:87], s48 offen offset:0 sc1\n\t"
"v_mfma_f32_16x16x32_bf16 v[164:167], a[32:35], a[148:151], v[164:167]\n\t"
"ds_read_b128 a[172:175], v224 offset:24128\n\t"
"v_mfma_f32_16x16x32_bf16 v[164:167], a[36:39], a[164:167], v[164:167]\n\t"
"v_mfma_f32_16x16x32_bf16 v[148:151], a[24:27], a[148:151], v[148:151]\n\t"
"buffer_load_dwordx4 a[76:79], v235, s[84:87], s48 offen offset:1024 sc1\n\t"
"v_mfma_f32_16x16x32_bf16 v[148:151], a[28:31], a[164:167], v[148:151]\n\t"
"s_waitcnt lgkmcnt(0)\n\t"
"s_barrier\n\t"
"v_mfma_f32_16x16x32_bf16 v[152:155], a[24:27], a[152:155], v[152:155]\n\t"
"ds_read_b128 a[144:147], v224 offset:0\n\t"
"v_mfma_f32_16x16x32_bf16 v[152:155], a[28:31], a[168:171], v[152:155]\n\t"
"ds_read_b128 a[148:151], v224 offset:512\n\t"
"v_mfma_f32_16x16x32_bf16 v[168:171], a[32:35], a[152:155], v[168:171]\n\t"
"ds_read_b128 a[160:163], v224 offset:4096\n\t"
"v_mfma_f32_16x16x32_bf16 v[168:171], a[36:39], a[168:171], v[168:171]\n\t"
"ds_read_b128 a[164:167], v224 offset:4608\n\t"
"v_mfma_f32_16x16x32_bf16 v[184:187], a[40:43], a[152:155], v[184:187]\n\t"
"v_mfma_f32_16x16x32_bf16 v[184:187], a[44:47], a[168:171], v[184:187]\n\t"
"ds_write_b128 v255, a[240:243] offset:2304\n\t"
"v_mfma_f32_16x16x32_bf16 v[192:195], a[220:223], a[176:179], 0\n\t"
"v_mfma_f32_16x16x32_bf16 v[188:191], a[40:43], a[156:159], v[188:191]\n\t"
"v_mfma_f32_16x16x32_bf16 v[188:191], a[44:47], a[172:175], v[188:191]\n\t"
"v_mfma_f32_16x16x32_bf16 v[196:199], a[220:223], a[184:187], 0\n\t"
"ds_read_b128 a[152:155], v224 offset:1024\n\t"
"v_mfma_f32_16x16x32_bf16 v[172:175], a[32:35], a[156:159], v[172:175]\n\t"
"ds_read_b64_tr_b16 a[176:177], v248 offset:0\n\t"
"v_mfma_f32_16x16x32_bf16 v[172:175], a[36:39], a[172:175], v[172:175]\n\t"
"ds_read_b128 a[168:171], v224 offset:5120\n\t"
"v_mfma_f32_16x16x32_bf16 v[156:159], a[24:27], a[156:159], v[156:159]\n\t"
"ds_read_b64_tr_b16 a[184:185], v249 offset:0\n\t"
"v_mfma_f32_16x16x32_bf16 v[156:159], a[28:31], a[172:175], v[156:159]\n\t"
"ds_read_b128 a[156:159], v224 offset:1536\n\t"
"v_cvt_pk_bf16_f32 v193, v193, v197\n\t"
"v_mfma_f32_16x16x32_bf16 v[200:203], a[220:223], a[178:181], 0\n\t"
"s_waitcnt lgkmcnt(6)\n\t"
"ds_read_b128 a[172:175], v224 offset:5632\n\t"
"v_mfma_f32_16x16x32_bf16 v[0:3], a[96:99], a[144:147], v[0:3]\n\t"
"ds_read_b64_tr_b16 a[178:179], v248 offset:512\n\t"
"v_mfma_f32_16x16x32_bf16 v[0:3], a[100:103], a[160:163], v[0:3]\n\t"
"ds_read_b64_tr_b16 a[180:181], v248 offset:1024\n\t"
"v_cvt_pk_bf16_f32 v192, v192, v196\n\t"
"v_mfma_f32_16x16x32_bf16 v[204:207], a[220:223], a[186:189], 0\n\t"
"ds_read_b64_tr_b16 a[186:187], v249 offset:512\n\t"
"v_mfma_f32_16x16x32_bf16 v[16:19], a[104:107], a[144:147], v[16:19]\n\t"
"ds_read_b64_tr_b16 a[188:189], v249 offset:1024\n\t"
"v_mfma_f32_16x16x32_bf16 v[16:19], a[108:111], a[160:163], v[16:19]\n\t"
"ds_write_addtid_b32 v193 offset:24672\n\t"
"v_cvt_pk_bf16_f32 v194, v194, v198\n\t"
"v_mfma_f32_16x16x32_bf16 v[32:35], a[112:115], a[144:147], v[32:35]\n\t"
"ds_write_addtid_b32 v192 offset:16448\n\t"
"v_mfma_f32_16x16x32_bf16 v[32:35], a[116:119], a[160:163], v[32:35]\n\t"
"ds_write_addtid_b32 v194 offset:18496\n\t"
"v_cvt_pk_bf16_f32 v200, v200, v204\n\t"
"v_mfma_f32_16x16x32_bf16 v[208:211], a[220:223], a[192:195], 0\n\t"
"ds_read_b128 a[144:147], v224 offset:2048\n\t"
"v_mfma_f32_16x16x32_bf16 v[36:39], a[112:115], a[148:151], v[36:39]\n\t"
"ds_write_addtid_b32 v200 offset:16704\n\t"
"v_cvt_pk_bf16_f32 v203, v203, v207\n\t"
"v_mfma_f32_16x16x32_bf16 v[36:39], a[116:119], a[164:167], v[36:39]\n\t"
"ds_read_b64_tr_b16 a[192:193], v250 offset:0\n\t"
"v_mfma_f32_16x16x32_bf16 v[20:23], a[104:107], a[148:151], v[20:23]\n\t"
"ds_read_b128 a[160:163], v224 offset:6144\n\t"
"v_cvt_pk_bf16_f32 v195, v195, v199\n\t"
"v_mfma_f32_16x16x32_bf16 v[20:23], a[108:111], a[164:167], v[20:23]\n\t"
"ds_write_addtid_b32 v203 offset:26976\n\t"
"v_cvt_pk_bf16_f32 v202, v202, v206\n\t"
"v_mfma_f32_16x16x32_bf16 v[212:215], a[220:223], a[200:203], 0\n\t"
"ds_write_addtid_b32 v195 offset:26720\n\t"
"v_mfma_f32_16x16x32_bf16 v[4:7], a[96:99], a[148:151], v[4:7]\n\t"
"ds_write_addtid_b32 v202 offset:18752\n\t"
"v_cvt_pk_bf16_f32 v201, v201, v205\n\t"
"v_mfma_f32_16x16x32_bf16 v[4:7], a[100:103], a[164:167], v[4:7]\n\t"
"ds_read_b64_tr_b16 a[200:201], v251 offset:0\n\t"
"v_mfma_f32_16x16x32_bf16 v[216:219], a[220:223], a[194:197], 0\n\t"
"s_waitcnt lgkmcnt(9)\n\t"
"ds_read_b128 a[148:151], v224 offset:2560\n\t"
"v_mfma_f32_16x16x32_bf16 v[8:11], a[96:99], a[152:155], v[8:11]\n\t"
"ds_write_addtid_b32 v201 offset:24928\n\t"
"v_mfma_f32_16x16x32_bf16 v[8:11], a[100:103], a[168:171], v[8:11]\n\t"
"ds_read_b64_tr_b16 a[194:195], v250 offset:512\n\t"
"v_mfma_f32_16x16x32_bf16 v[24:27], a[104:107], a[152:155], v[24:27]\n\t"
"ds_read_b64_tr_b16 a[196:197], v250 offset:1024\n\t"
"v_cvt_pk_bf16_f32 v209, v209, v213\n\t"
"v_mfma_f32_16x16x32_bf16 v[24:27], a[108:111], a[168:171], v[24:27]\n\t"
"ds_read_b128 a[164:167], v224 offset:6656\n\t"
"v_cvt_pk_bf16_f32 v208, v208, v212\n\t"
"v_mfma_f32_16x16x32_bf16 v[220:223], a[220:223], a[202:205], 0\n\t"
"ds_read_b64_tr_b16 a[202:203], v251 offset:512\n\t"
"v_mfma_f32_16x16x32_bf16 v[40:43], a[112:115], a[152:155], v[40:43]\n\t"
"ds_write_addtid_b32 v208 offset:20544\n\t"
"v_mfma_f32_16x16x32_bf16 v[40:43], a[116:119], a[168:171], v[40:43]\n\t"
"ds_read_b64_tr_b16 a[204:205], v251 offset:1024\n\t"
"v_cvt_pk_bf16_f32 v210, v210, v214\n\t"
"v_mfma_f32_16x16x32_bf16 v[44:47], a[112:115], a[156:159], v[44:47]\n\t"
"ds_write_addtid_b32 v209 offset:28768\n\t"
"v_cvt_pk_bf16_f32 v211, v211, v215\n\t"
"v_mfma_f32_16x16x32_bf16 v[44:47], a[116:119], a[172:175], v[44:47]\n\t"
"buffer_load_dwordx4 a[112:115], v252, s[84:87], s75 offen offset:0 sc1\n\t"
"v_mfma_f32_16x16x32_bf16 v[28:31], a[104:107], a[156:159], v[28:31]\n\t"
"ds_write_addtid_b32 v211 offset:30816\n\t"
"v_cvt_pk_bf16_f32 v219, v219, v223\n\t"
"v_mfma_f32_16x16x32_bf16 v[28:31], a[108:111], a[172:175], v[28:31]\n\t"
"buffer_load_dwordx4 a[116:119], v252, s[84:87], s75 offen offset:1024 sc1\n\t"
"v_mfma_f32_16x16x32_bf16 v[12:15], a[96:99], a[156:159], v[12:15]\n\t"
"ds_write_addtid_b32 v210 offset:22592\n\t"
"v_cvt_pk_bf16_f32 v216, v216, v220\n\t"
"v_mfma_f32_16x16x32_bf16 v[12:15], a[100:103], a[172:175], v[12:15]\n\t"
"s_waitcnt lgkmcnt(7)\n\t"
"buffer_load_dwordx4 a[108:111], v235, s[84:87], s75 offen offset:3072 sc1\n\t"
"v_mfma_f32_16x16x32_bf16 v[96:99], a[120:123], a[144:147], v[96:99]\n\t"
"ds_write_addtid_b32 v219 offset:31072\n\t"
"v_cvt_pk_bf16_f32 v218, v218, v222\n\t"
"v_mfma_f32_16x16x32_bf16 v[96:99], a[124:127], a[160:163], v[96:99]\n\t"
"buffer_load_dwordx4 a[104:107], v235, s[84:87], s75 offen offset:2048 sc1\n\t"
"v_mfma_f32_16x16x32_bf16 v[112:115], a[128:131], a[144:147], v[112:115]\n\t"
"ds_write_addtid_b32 v218 offset:22848\n\t"
"v_cvt_pk_bf16_f32 v217, v217, v221\n\t"
"v_mfma_f32_16x16x32_bf16 v[112:115], a[132:135], a[160:163], v[112:115]\n\t"
"buffer_load_dwordx4 a[96:99], v235, s[84:87], s75 offen offset:0 sc1\n\t"
"v_mfma_f32_16x16x32_bf16 v[128:131], a[136:139], a[144:147], v[128:131]\n\t"
"s_nop 0\n\t"
"ds_write_addtid_b32 v216 offset:20800\n\t"
"ds_write_addtid_b32 v217 offset:29024\n\t"
"v_mfma_f32_16x16x32_bf16 v[128:131], a[140:143], a[160:163], v[128:131]\n\t"
"s_waitcnt lgkmcnt(6)\n\t"
"buffer_load_dwordx4 a[100:103], v235, s[84:87], s75 offen offset:1024 sc1\n\t"
"v_mfma_f32_16x16x32_bf16 v[132:135], a[136:139], a[148:151], v[132:135]\n\t"
"ds_read_b128 a[156:159], v224 offset:3584\n\t"
"v_mfma_f32_16x16x32_bf16 v[132:135], a[140:143], a[164:167], v[132:135]\n\t"
"ds_read_b128 a[172:175], v224 offset:7680\n\t"
"v_mfma_f32_16x16x32_bf16 v[116:119], a[128:131], a[148:151], v[116:119]\n\t"
"s_nop 0\n\t"
"ds_read_b128 a[152:155], v224 offset:3072\n\t"
"ds_read_b128 a[168:171], v224 offset:7168\n\t"
"v_mfma_f32_16x16x32_bf16 v[116:119], a[132:135], a[164:167], v[116:119]\n\t"
"v_mfma_f32_16x16x32_bf16 v[100:103], a[120:123], a[148:151], v[100:103]\n\t"
"v_mfma_f32_16x16x32_bf16 v[100:103], a[124:127], a[164:167], v[100:103]\n\t"
"s_waitcnt lgkmcnt(0)\n\t"
"s_barrier\n\t"
"v_mfma_f32_16x16x32_bf16 v[104:107], a[120:123], a[152:155], v[104:107]\n\t"
"ds_read_b128 a[148:151], v224 offset:16960\n\t"
"v_mfma_f32_16x16x32_bf16 v[104:107], a[124:127], a[168:171], v[104:107]\n\t"
"ds_read_b128 a[144:147], v224 offset:16448\n\t"
"v_add_u32 v248, 80896, v226\n\t"
"v_mfma_f32_16x16x32_bf16 v[120:123], a[128:131], a[152:155], v[120:123]\n\t"
"ds_read_b128 a[160:163], v224 offset:20544\n\t"
"v_xor_b32 v249, 32, v248\n\t"
"v_mfma_f32_16x16x32_bf16 v[120:123], a[132:135], a[168:171], v[120:123]\n\t"
"ds_read_b128 a[164:167], v224 offset:21056\n\t"
"v_mfma_f32_16x16x32_bf16 v[136:139], a[136:139], a[152:155], v[136:139]\n\t"
"v_xor_b32 v250, 64, v248\n\t"
"v_xor_b32 v251, 96, v248\n\t"
"v_mfma_f32_16x16x32_bf16 v[136:139], a[140:143], a[168:171], v[136:139]\n\t"
"v_mfma_f32_16x16x32_bf16 v[192:195], a[220:223], a[176:179], 0\n\t"
"ds_read_b128 a[168:171], v224 offset:21568\n\t"
"v_mfma_f32_16x16x32_bf16 v[140:143], a[136:139], a[156:159], v[140:143]\n\t"
"buffer_load_dwordx4 a[136:139], v252, s[84:87], s78 offen offset:0 sc1\n\t"
"v_mfma_f32_16x16x32_bf16 v[140:143], a[140:143], a[172:175], v[140:143]\n\t"
"ds_read_b128 a[152:155], v224 offset:17472\n\t"
"v_mfma_f32_16x16x32_bf16 v[196:199], a[220:223], a[184:187], 0\n\t"
"buffer_load_dwordx4 a[140:143], v252, s[84:87], s78 offen offset:1024 sc1\n\t"
"v_mfma_f32_16x16x32_bf16 v[124:127], a[128:131], a[156:159], v[124:127]\n\t"
"v_mfma_f32_16x16x32_bf16 v[124:127], a[132:135], a[172:175], v[124:127]\n\t"
"buffer_load_dwordx4 a[132:135], v235, s[84:87], s78 offen offset:3072 sc1\n\t"
"v_mfma_f32_16x16x32_bf16 v[108:111], a[120:123], a[156:159], v[108:111]\n\t"
"ds_read_b64_tr_b16 a[176:177], v248 offset:41472\n\t"
"v_mfma_f32_16x16x32_bf16 v[108:111], a[124:127], a[172:175], v[108:111]\n\t"
"buffer_load_dwordx4 a[128:131], v235, s[84:87], s78 offen offset:2048 sc1\n\t"
"v_mfma_f32_16x16x32_bf16 v[200:203], a[220:223], a[178:181], 0\n\t"
"s_waitcnt vmcnt(16) lgkmcnt(3)\n\t"
"ds_read_b64_tr_b16 a[184:185], v249 offset:41472\n\t"
"v_cvt_pk_bf16_f32 v193, v193, v197\n\t"
"v_mfma_f32_16x16x32_bf16 v[48:51], a[48:51], a[144:147], v[48:51]\n\t"
"ds_write_addtid_b32 v193 offset:8224\n\t"
"v_mfma_f32_16x16x32_bf16 v[48:51], a[52:55], a[160:163], v[48:51]\n\t"
"buffer_load_dwordx4 a[120:123], v235, s[84:87], s78 offen offset:0 sc1\n\t"
"v_mfma_f32_16x16x32_bf16 v[204:207], a[220:223], a[186:189], 0\n\t"
"ds_read_b128 a[156:159], v224 offset:17984\n\t"
"v_pk_add_f32 v[96:97], v[0:1], v[96:97] neg_lo:[0,1] neg_hi:[0,1]\n\t"
"v_pk_add_f32 v[98:99], v[2:3], v[98:99] neg_lo:[0,1] neg_hi:[0,1]\n\t"
"v_pk_fma_f32 v[0:1], v[96:97], s[90:91], v[0:1] neg_lo:[1,0,0] neg_hi:[1,0,0]\n\t"
"v_pk_fma_f32 v[2:3], v[98:99], s[90:91], v[2:3] neg_lo:[1,0,0] neg_hi:[1,0,0]\n\t"
"v_pk_add_f32 v[112:113], v[16:17], v[112:113] neg_lo:[0,1] neg_hi:[0,1]\n\t"
"v_pk_add_f32 v[114:115], v[18:19], v[114:115] neg_lo:[0,1] neg_hi:[0,1]\n\t"
"v_pk_fma_f32 v[16:17], v[112:113], s[90:91], v[16:17] neg_lo:[1,0,0] neg_hi:[1,0,0]\n\t"
"v_pk_fma_f32 v[18:19], v[114:115], s[90:91], v[18:19] neg_lo:[1,0,0] neg_hi:[1,0,0]\n\t"
"v_pk_add_f32 v[128:129], v[32:33], v[128:129] neg_lo:[0,1] neg_hi:[0,1]\n\t"
"v_pk_add_f32 v[130:131], v[34:35], v[130:131] neg_lo:[0,1] neg_hi:[0,1]\n\t"
"v_pk_fma_f32 v[32:33], v[128:129], s[90:91], v[32:33] neg_lo:[1,0,0] neg_hi:[1,0,0]\n\t"
"v_pk_fma_f32 v[34:35], v[130:131], s[90:91], v[34:35] neg_lo:[1,0,0] neg_hi:[1,0,0]\n\t"
"v_pk_add_f32 v[132:133], v[36:37], v[132:133] neg_lo:[0,1] neg_hi:[0,1]\n\t"
"v_pk_add_f32 v[134:135], v[38:39], v[134:135] neg_lo:[0,1] neg_hi:[0,1]\n\t"
"v_pk_fma_f32 v[36:37], v[132:133], s[90:91], v[36:37] neg_lo:[1,0,0] neg_hi:[1,0,0]\n\t"
"v_pk_fma_f32 v[38:39], v[134:135], s[90:91], v[38:39] neg_lo:[1,0,0] neg_hi:[1,0,0]\n\t"
"v_pk_add_f32 v[116:117], v[20:21], v[116:117] neg_lo:[0,1] neg_hi:[0,1]\n\t"
"v_pk_add_f32 v[118:119], v[22:23], v[118:119] neg_lo:[0,1] neg_hi:[0,1]\n\t"
"v_pk_fma_f32 v[20:21], v[116:117], s[90:91], v[20:21] neg_lo:[1,0,0] neg_hi:[1,0,0]\n\t"
"v_pk_fma_f32 v[22:23], v[118:119], s[90:91], v[22:23] neg_lo:[1,0,0] neg_hi:[1,0,0]\n\t"
"v_pk_add_f32 v[100:101], v[4:5], v[100:101] neg_lo:[0,1] neg_hi:[0,1]\n\t"
"v_pk_add_f32 v[102:103], v[6:7], v[102:103] neg_lo:[0,1] neg_hi:[0,1]\n\t"
"v_pk_fma_f32 v[4:5], v[100:101], s[90:91], v[4:5] neg_lo:[1,0,0] neg_hi:[1,0,0]\n\t"
"v_pk_fma_f32 v[6:7], v[102:103], s[90:91], v[6:7] neg_lo:[1,0,0] neg_hi:[1,0,0]\n\t"
"v_pk_add_f32 v[104:105], v[8:9], v[104:105] neg_lo:[0,1] neg_hi:[0,1]\n\t"
"v_pk_add_f32 v[106:107], v[10:11], v[106:107] neg_lo:[0,1] neg_hi:[0,1]\n\t"
"v_pk_fma_f32 v[8:9], v[104:105], s[90:91], v[8:9] neg_lo:[1,0,0] neg_hi:[1,0,0]\n\t"
"v_pk_fma_f32 v[10:11], v[106:107], s[90:91], v[10:11] neg_lo:[1,0,0] neg_hi:[1,0,0]\n\t"
"v_pk_add_f32 v[120:121], v[24:25], v[120:121] neg_lo:[0,1] neg_hi:[0,1]\n\t"
"v_pk_add_f32 v[122:123], v[26:27], v[122:123] neg_lo:[0,1] neg_hi:[0,1]\n\t"
"v_pk_fma_f32 v[24:25], v[120:121], s[90:91], v[24:25] neg_lo:[1,0,0] neg_hi:[1,0,0]\n\t"
"v_pk_fma_f32 v[26:27], v[122:123], s[90:91], v[26:27] neg_lo:[1,0,0] neg_hi:[1,0,0]\n\t"
"v_pk_add_f32 v[136:137], v[40:41], v[136:137] neg_lo:[0,1] neg_hi:[0,1]\n\t"
"v_pk_add_f32 v[138:139], v[42:43], v[138:139] neg_lo:[0,1] neg_hi:[0,1]\n\t"
"v_pk_fma_f32 v[40:41], v[136:137], s[90:91], v[40:41] neg_lo:[1,0,0] neg_hi:[1,0,0]\n\t"
"v_pk_fma_f32 v[42:43], v[138:139], s[90:91], v[42:43] neg_lo:[1,0,0] neg_hi:[1,0,0]\n\t"
"v_pk_add_f32 v[140:141], v[44:45], v[140:141] neg_lo:[0,1] neg_hi:[0,1]\n\t"
"v_pk_add_f32 v[142:143], v[46:47], v[142:143] neg_lo:[0,1] neg_hi:[0,1]\n\t"
"v_pk_fma_f32 v[44:45], v[140:141], s[90:91], v[44:45] neg_lo:[1,0,0] neg_hi:[1,0,0]\n\t"
"v_pk_fma_f32 v[46:47], v[142:143], s[90:91], v[46:47] neg_lo:[1,0,0] neg_hi:[1,0,0]\n\t"
"v_pk_add_f32 v[124:125], v[28:29], v[124:125] neg_lo:[0,1] neg_hi:[0,1]\n\t"
"v_pk_add_f32 v[126:127], v[30:31], v[126:127] neg_lo:[0,1] neg_hi:[0,1]\n\t"
"v_pk_fma_f32 v[28:29], v[124:125], s[90:91], v[28:29] neg_lo:[1,0,0] neg_hi:[1,0,0]\n\t"
"v_pk_fma_f32 v[30:31], v[126:127], s[90:91], v[30:31] neg_lo:[1,0,0] neg_hi:[1,0,0]\n\t"
"v_pk_add_f32 v[108:109], v[12:13], v[108:109] neg_lo:[0,1] neg_hi:[0,1]\n\t"
"v_pk_add_f32 v[110:111], v[14:15], v[110:111] neg_lo:[0,1] neg_hi:[0,1]\n\t"
"v_pk_fma_f32 v[12:13], v[108:109], s[90:91], v[12:13] neg_lo:[1,0,0] neg_hi:[1,0,0]\n\t"
"v_pk_fma_f32 v[14:15], v[110:111], s[90:91], v[14:15] neg_lo:[1,0,0] neg_hi:[1,0,0]\n\t"
"v_mfma_f32_16x16x32_bf16 v[64:67], a[56:59], a[144:147], v[64:67]\n\t"
"ds_read_b64_tr_b16 a[178:179], v248 offset:41984\n\t"
"v_cvt_pk_bf16_f32 v194, v194, v198\n\t"
"v_mfma_f32_16x16x32_bf16 v[64:67], a[60:63], a[160:163], v[64:67]\n\t"
"buffer_load_dwordx4 a[124:127], v235, s[84:87], s78 offen offset:1024 sc1\n\t"
"v_mfma_f32_16x16x32_bf16 v[80:83], a[64:67], a[144:147], v[80:83]\n\t"
"ds_read_b128 a[172:175], v224 offset:22080\n\t"
"v_cvt_pk_bf16_f32 v195, v195, v199\n\t"
"v_mfma_f32_16x16x32_bf16 v[80:83], a[68:71], a[160:163], v[80:83]\n\t"
"ds_write_addtid_b32 v195 offset:10272\n\t"
"v_cvt_pk_bf16_f32 v201, v201, v205\n\t"
"v_mfma_f32_16x16x32_bf16 v[208:211], a[220:223], a[192:195], 0\n\t"
"ds_read_b64_tr_b16 a[180:181], v248 offset:42496\n\t"
"v_cvt_pk_bf16_f32 v192, v192, v196\n\t"
"v_mfma_f32_16x16x32_bf16 v[84:87], a[64:67], a[148:151], v[84:87]\n\t"
"ds_read_b64_tr_b16 a[186:187], v249 offset:41984\n\t"
"v_mfma_f32_16x16x32_bf16 v[84:87], a[68:71], a[164:167], v[84:87]\n\t"
"ds_read_b128 a[144:147], v224 offset:18496\n\t"
"v_cvt_pk_bf16_f32 v203, v203, v207\n\t"
"v_mfma_f32_16x16x32_bf16 v[68:71], a[56:59], a[148:151], v[68:71]\n\t"
"ds_read_b64_tr_b16 a[188:189], v249 offset:42496\n\t"
"v_cvt_pk_bf16_f32 v200, v200, v204\n\t"
"v_mfma_f32_16x16x32_bf16 v[68:71], a[60:63], a[164:167], v[68:71]\n\t"
"ds_write_addtid_b32 v192 offset:0\n\t"
"v_mfma_f32_16x16x32_bf16 v[212:215], a[220:223], a[200:203], 0\n\t"
"ds_write_addtid_b32 v194 offset:2048\n\t"
"v_mfma_f32_16x16x32_bf16 v[52:55], a[48:51], a[148:151], v[52:55]\n\t"
"ds_read_b128 a[160:163], v224 offset:22592\n\t"
"v_mfma_f32_16x16x32_bf16 v[52:55], a[52:55], a[164:167], v[52:55]\n\t"
"ds_read_b64_tr_b16 a[192:193], v250 offset:41472\n\t"
"v_mfma_f32_16x16x32_bf16 v[216:219], a[220:223], a[194:197], 0\n\t"
"s_waitcnt lgkmcnt(9)\n\t"
"ds_read_b128 a[148:151], v224 offset:19008\n\t"
"v_mfma_f32_16x16x32_bf16 v[56:59], a[48:51], a[152:155], v[56:59]\n\t"
"ds_write_addtid_b32 v201 offset:8480\n\t"
"v_cvt_pk_bf16_f32 v202, v202, v206\n\t"
"v_mfma_f32_16x16x32_bf16 v[56:59], a[52:55], a[168:171], v[56:59]\n\t"
"ds_read_b64_tr_b16 a[200:201], v251 offset:41472\n\t"
"v_cvt_pk_bf16_f32 v208, v208, v212\n\t"
"v_mfma_f32_16x16x32_bf16 v[72:75], a[56:59], a[152:155], v[72:75]\n\t"
"ds_write_addtid_b32 v202 offset:2304\n\t"
"v_mfma_f32_16x16x32_bf16 v[72:75], a[60:63], a[168:171], v[72:75]\n\t"
"ds_read_b128 a[164:167], v224 offset:23104\n\t"
"v_cvt_pk_bf16_f32 v210, v210, v214\n\t"
"v_mfma_f32_16x16x32_bf16 v[220:223], a[220:223], a[202:205], 0\n\t"
"ds_write_addtid_b32 v203 offset:10528\n\t"
"v_cvt_pk_bf16_f32 v209, v209, v213\n\t"
"v_mfma_f32_16x16x32_bf16 v[88:91], a[64:67], a[152:155], v[88:91]\n\t"
"ds_write_addtid_b32 v200 offset:256\n\t"
"v_mfma_f32_16x16x32_bf16 v[88:91], a[68:71], a[168:171], v[88:91]\n\t"
"ds_read_b64_tr_b16 a[194:195], v250 offset:41984\n\t"
"v_mfma_f32_16x16x32_bf16 v[92:95], a[64:67], a[156:159], v[92:95]\n\t"
"ds_write_addtid_b32 v208 offset:4096\n\t"
"v_mfma_f32_16x16x32_bf16 v[92:95], a[68:71], a[172:175], v[92:95]\n\t"
"ds_read_b64_tr_b16 a[196:197], v250 offset:42496\n\t"
"v_cvt_pk_bf16_f32 v211, v211, v215\n\t"
"v_mfma_f32_16x16x32_bf16 v[76:79], a[56:59], a[156:159], v[76:79]\n\t"
"ds_write_addtid_b32 v210 offset:6144\n\t"
"v_cvt_pk_bf16_f32 v219, v219, v223\n\t"
"v_mfma_f32_16x16x32_bf16 v[76:79], a[60:63], a[172:175], v[76:79]\n\t"
"ds_write_addtid_b32 v211 offset:14368\n\t"
"v_mfma_f32_16x16x32_bf16 v[60:63], a[48:51], a[156:159], v[60:63]\n\t"
"ds_read_b128 a[156:159], v224 offset:20032\n\t"
"v_mfma_f32_16x16x32_bf16 v[60:63], a[52:55], a[172:175], v[60:63]\n\t"
"s_waitcnt vmcnt(12) lgkmcnt(9)\n\t"
"ds_write_addtid_b32 v219 offset:14624\n\t"
"v_cvt_pk_bf16_f32 v218, v218, v222\n\t"
"v_mfma_f32_16x16x32_bf16 v[144:147], a[72:75], a[144:147], v[144:147]\n\t"
"ds_read_b64_tr_b16 a[202:203], v251 offset:41984\n\t"
"v_cvt_pk_bf16_f32 v216, v216, v220\n\t"
"v_mfma_f32_16x16x32_bf16 v[144:147], a[76:79], a[160:163], v[144:147]\n\t"
"ds_write_addtid_b32 v216 offset:4352\n\t"
"v_mfma_f32_16x16x32_bf16 v[160:163], a[80:83], a[144:147], v[160:163]\n\t"
"ds_write_addtid_b32 v209 offset:12320\n\t"
"v_mfma_f32_16x16x32_bf16 v[160:163], a[84:87], a[160:163], v[160:163]\n\t"
"ds_write_addtid_b32 v218 offset:6400\n\t"
"v_cvt_pk_bf16_f32 v217, v217, v221\n\t"
"v_mfma_f32_16x16x32_bf16 v[176:179], a[88:91], a[144:147], v[176:179]\n\t"
"ds_read_b128 a[168:171], v224 offset:23616\n\t"
"v_mfma_f32_16x16x32_bf16 v[176:179], a[92:95], a[160:163], v[176:179]\n\t"
"s_waitcnt lgkmcnt(9)\n\t"
"ds_write_addtid_b32 v217 offset:12576\n\t"
"v_mfma_f32_16x16x32_bf16 v[180:183], a[88:91], a[148:151], v[180:183]\n\t"
"ds_read_b128 a[152:155], v224 offset:19520\n\t"
"v_mfma_f32_16x16x32_bf16 v[180:183], a[92:95], a[164:167], v[180:183]\n\t"
"ds_read_b64_tr_b16 a[204:205], v251 offset:42496\n\t"
"v_mfma_f32_16x16x32_bf16 v[164:167], a[80:83], a[148:151], v[164:167]\n\t"
"ds_read_b128 a[172:175], v224 offset:24128\n\t"
"v_mfma_f32_16x16x32_bf16 v[164:167], a[84:87], a[164:167], v[164:167]\n\t"
"v_mfma_f32_16x16x32_bf16 v[148:151], a[72:75], a[148:151], v[148:151]\n\t"
"v_mfma_f32_16x16x32_bf16 v[148:151], a[76:79], a[164:167], v[148:151]\n\t"
"s_waitcnt lgkmcnt(0)\n\t"
"s_barrier\n\t"
"v_mfma_f32_16x16x32_bf16 v[152:155], a[72:75], a[152:155], v[152:155]\n\t"
"s_add_u32 m0, s26, 103936\n\t"
"ds_read_b128 a[148:151], v224 offset:512\n\t"
"v_mfma_f32_16x16x32_bf16 v[152:155], a[76:79], a[168:171], v[152:155]\n\t"
"ds_read_b128 a[144:147], v224 offset:0\n\t"
"v_mfma_f32_16x16x32_bf16 v[168:171], a[80:83], a[152:155], v[168:171]\n\t"
"ds_read_b128 a[160:163], v224 offset:4096\n\t"
"v_mfma_f32_16x16x32_bf16 v[168:171], a[84:87], a[168:171], v[168:171]\n\t"
"ds_read_b128 a[164:167], v224 offset:4608\n\t"
"v_mfma_f32_16x16x32_bf16 v[184:187], a[88:91], a[152:155], v[184:187]\n\t"
"buffer_load_dwordx4 v236, s[12:15], s22 offen offset:0 nt lds\n\t"
"v_mfma_f32_16x16x32_bf16 v[184:187], a[92:95], a[168:171], v[184:187]\n\t"
"v_mfma_f32_16x16x32_bf16 v[192:195], a[252:255], a[176:179], 0\n\t"
"v_mfma_f32_16x16x32_bf16 v[188:191], a[88:91], a[156:159], v[188:191]\n\t"
"v_mfma_f32_16x16x32_bf16 v[188:191], a[92:95], a[172:175], v[188:191]\n\t"
"buffer_load_dwordx4 v237, s[12:15], s22 offen offset:1024 nt lds\n\t"
"v_mfma_f32_16x16x32_bf16 v[196:199], a[252:255], a[184:187], 0\n\t"
"ds_read_b64_tr_b16 a[176:177], v248 offset:0\n\t"
"v_mfma_f32_16x16x32_bf16 v[172:175], a[80:83], a[156:159], v[172:175]\n\t"
"ds_read_b128 a[152:155], v224 offset:1024\n\t"
"v_mfma_f32_16x16x32_bf16 v[172:175], a[84:87], a[172:175], v[172:175]\n\t"
"ds_read_b64_tr_b16 a[184:185], v249 offset:0\n\t"
"v_mfma_f32_16x16x32_bf16 v[156:159], a[72:75], a[156:159], v[156:159]\n\t"
"ds_read_b128 a[156:159], v224 offset:1536\n\t"
"v_mfma_f32_16x16x32_bf16 v[156:159], a[76:79], a[172:175], v[156:159]\n\t"
"buffer_load_dwordx4 v238, s[12:15], s22 offen offset:2048 nt lds\n\t"
"v_mfma_f32_16x16x32_bf16 v[200:203], a[252:255], a[178:181], 0\n\t"
"s_waitcnt vmcnt(9) lgkmcnt(4)\n\t"
"ds_read_b128 a[168:171], v224 offset:5120\n\t"
"v_cvt_pk_bf16_f32 v195, v195, v199\n\t"
"v_mfma_f32_16x16x32_bf16 v[48:51], a[96:99], a[144:147], v[48:51]\n\t"
"s_lshl_b32 m0, s73, 9\n\t"
"ds_read_b64_tr_b16 a[178:179], v248 offset:512\n\t"
"v_mfma_f32_16x16x32_bf16 v[48:51], a[100:103], a[160:163], v[48:51]\n\t"
"ds_read_b64_tr_b16 a[180:181], v248 offset:1024\n\t"
"v_cvt_pk_bf16_f32 v194, v194, v198\n\t"
"v_mfma_f32_16x16x32_bf16 v[204:207], a[252:255], a[186:189], 0\n\t"
"v_cvt_pk_bf16_f32 v193, v193, v197\n\t"
"v_mfma_f32_16x16x32_bf16 v[64:67], a[104:107], a[144:147], v[64:67]\n\t"
"ds_write_addtid_b32 v194 offset:18496\n\t"
"s_add_u32 m0, s26, 103936\n\t"
"v_mfma_f32_16x16x32_bf16 v[64:67], a[108:111], a[160:163], v[64:67]\n\t"
"buffer_load_dwordx4 v239, s[12:15], s22 offen offset:3072 nt lds\n\t"
"v_mfma_f32_16x16x32_bf16 v[80:83], a[112:115], a[144:147], v[80:83]\n\t"
"s_lshl_b32 m0, s73, 9\n\t"
"ds_read_b64_tr_b16 a[186:187], v249 offset:512\n\t"
"v_mfma_f32_16x16x32_bf16 v[80:83], a[116:119], a[160:163], v[80:83]\n\t"
"ds_read_b128 a[172:175], v224 offset:5632\n\t"
"v_cvt_pk_bf16_f32 v192, v192, v196\n\t"
"v_mfma_f32_16x16x32_bf16 v[208:211], a[252:255], a[192:195], 0\n\t"
"ds_read_b128 a[144:147], v224 offset:2048\n\t"
"v_cvt_pk_bf16_f32 v201, v201, v205\n\t"
"v_mfma_f32_16x16x32_bf16 v[84:87], a[112:115], a[148:151], v[84:87]\n\t"
"ds_write_addtid_b32 v201 offset:24928\n\t"
"v_cvt_pk_bf16_f32 v203, v203, v207\n\t"
"v_mfma_f32_16x16x32_bf16 v[84:87], a[116:119], a[164:167], v[84:87]\n\t"
"ds_write_addtid_b32 v193 offset:24672\n\t"
"v_cvt_pk_bf16_f32 v200, v200, v204\n\t"
"v_mfma_f32_16x16x32_bf16 v[68:71], a[104:107], a[148:151], v[68:71]\n\t"
"s_add_u32 m0, s26, 103936\n\t"
"ds_read_b128 a[160:163], v224 offset:6144\n\t"
"v_cvt_pk_bf16_f32 v202, v202, v206\n\t"
"v_mfma_f32_16x16x32_bf16 v[68:71], a[108:111], a[164:167], v[68:71]\n\t"
"s_mov_b64 exec, s[36:37]\n\t"
"buffer_load_dwordx4 v240, s[12:15], s22 offen offset:3584 nt lds\n\t"
"s_mov_b64 exec, -1\n\t"
"s_lshl_b32 m0, s73, 9\n\t"
"v_mfma_f32_16x16x32_bf16 v[212:215], a[252:255], a[200:203], 0\n\t"
"ds_write_addtid_b32 v195 offset:26720\n\t"
"v_mfma_f32_16x16x32_bf16 v[52:55], a[96:99], a[148:151], v[52:55]\n\t"
"ds_write_addtid_b32 v192 offset:16448\n\t"
"v_mfma_f32_16x16x32_bf16 v[52:55], a[100:103], a[164:167], v[52:55]\n\t"
"ds_read_b128 a[148:151], v224 offset:2560\n\t"
"v_mfma_f32_16x16x32_bf16 v[216:219], a[252:255], a[194:197], 0\n\t"
"s_waitcnt lgkmcnt(8)\n\t"
"ds_read_b64_tr_b16 a[188:189], v249 offset:1024\n\t"
"v_mfma_f32_16x16x32_bf16 v[56:59], a[96:99], a[152:155], v[56:59]\n\t"
"ds_write_addtid_b32 v203 offset:26976\n\t"
"v_cvt_pk_bf16_f32 v210, v210, v214\n\t"
"v_mfma_f32_16x16x32_bf16 v[56:59], a[100:103], a[168:171], v[56:59]\n\t"
"ds_read_b128 a[164:167], v224 offset:6656\n\t"
"v_mfma_f32_16x16x32_bf16 v[72:75], a[104:107], a[152:155], v[72:75]\n\t"
"ds_read_b64_tr_b16 a[192:193], v250 offset:0\n\t"
"v_mfma_f32_16x16x32_bf16 v[72:75], a[108:111], a[168:171], v[72:75]\n\t"
"ds_write_addtid_b32 v202 offset:18752\n\t"
"v_mfma_f32_16x16x32_bf16 v[220:223], a[252:255], a[202:205], 0\n\t"
"ds_read_b64_tr_b16 a[200:201], v251 offset:0\n\t"
"v_mfma_f32_16x16x32_bf16 v[88:91], a[112:115], a[152:155], v[88:91]\n\t"
"ds_write_addtid_b32 v200 offset:16704\n\t"
"v_mfma_f32_16x16x32_bf16 v[88:91], a[116:119], a[168:171], v[88:91]\n\t"
"s_waitcnt lgkmcnt(9)\n\t"
"ds_write_addtid_b32 v210 offset:22592\n\t"
"v_cvt_pk_bf16_f32 v211, v211, v215\n\t"
"v_mfma_f32_16x16x32_bf16 v[92:95], a[112:115], a[156:159], v[92:95]\n\t"
"ds_read_b128 a[152:155], v224 offset:3072\n\t"
"v_cvt_pk_bf16_f32 v209, v209, v213\n\t"
"v_mfma_f32_16x16x32_bf16 v[92:95], a[116:119], a[172:175], v[92:95]\n\t"
"ds_read_b64_tr_b16 a[194:195], v250 offset:512\n\t"
"v_mfma_f32_16x16x32_bf16 v[76:79], a[104:107], a[156:159], v[76:79]\n\t"
"ds_write_addtid_b32 v211 offset:30816\n\t"
"v_mfma_f32_16x16x32_bf16 v[76:79], a[108:111], a[172:175], v[76:79]\n\t"
"ds_read_b64_tr_b16 a[196:197], v250 offset:1024\n\t"
"v_cvt_pk_bf16_f32 v217, v217, v221\n\t"
"v_mfma_f32_16x16x32_bf16 v[60:63], a[96:99], a[156:159], v[60:63]\n\t"
"ds_write_addtid_b32 v217 offset:29024\n\t"
"v_cvt_pk_bf16_f32 v208, v208, v212\n\t"
"v_mfma_f32_16x16x32_bf16 v[60:63], a[100:103], a[172:175], v[60:63]\n\t"
"s_waitcnt vmcnt(5) lgkmcnt(9)\n\t"
"ds_write_addtid_b32 v208 offset:20544\n\t"
"v_cvt_pk_bf16_f32 v218, v218, v222\n\t"
"v_mfma_f32_16x16x32_bf16 v[144:147], a[120:123], a[144:147], v[144:147]\n\t"
"ds_read_b64_tr_b16 a[202:203], v251 offset:512\n\t"
"v_cvt_pk_bf16_f32 v219, v219, v223\n\t"
"v_mfma_f32_16x16x32_bf16 v[144:147], a[124:127], a[160:163], v[144:147]\n\t"
"ds_write_addtid_b32 v209 offset:28768\n\t"
"v_mfma_f32_16x16x32_bf16 v[160:163], a[128:131], a[144:147], v[160:163]\n\t"
"ds_read_b128 a[172:175], v224 offset:7680\n\t"
"v_cvt_pk_bf16_f32 v216, v216, v220\n\t"
"v_mfma_f32_16x16x32_bf16 v[160:163], a[132:135], a[160:163], v[160:163]\n\t"
"ds_write_addtid_b32 v219 offset:31072\n\t"
"v_mfma_f32_16x16x32_bf16 v[176:179], a[136:139], a[144:147], v[176:179]\n\t"
"ds_write_addtid_b32 v218 offset:22848\n\t"
"v_mfma_f32_16x16x32_bf16 v[176:179], a[140:143], a[160:163], v[176:179]\n\t"
"ds_write_addtid_b32 v216 offset:20800\n\t"
"v_mfma_f32_16x16x32_bf16 v[180:183], a[136:139], a[148:151], v[180:183]\n\t"
"ds_read_b64_tr_b16 a[204:205], v251 offset:1024\n\t"
"v_mfma_f32_16x16x32_bf16 v[180:183], a[140:143], a[164:167], v[180:183]\n\t"
"ds_read_b128 a[156:159], v224 offset:3584\n\t"
"v_mfma_f32_16x16x32_bf16 v[164:167], a[128:131], a[148:151], v[164:167]\n\t"
"ds_read_b128 a[168:171], v224 offset:7168\n\t"
"v_mfma_f32_16x16x32_bf16 v[164:167], a[132:135], a[164:167], v[164:167]\n\t"
"v_mfma_f32_16x16x32_bf16 v[148:151], a[120:123], a[148:151], v[148:151]\n\t"
"v_mfma_f32_16x16x32_bf16 v[148:151], a[124:127], a[164:167], v[148:151]\n\t"
"s_waitcnt lgkmcnt(0)\n\t"
"s_barrier\n\t"
"v_mfma_f32_16x16x32_bf16 v[152:155], a[120:123], a[152:155], v[152:155]\n\t"
"ds_read_b128 a[148:151], v224 offset:16960\n\t"
"v_add_u32 v248, 32896, v226\n\t"
"v_mfma_f32_16x16x32_bf16 v[152:155], a[124:127], a[168:171], v[152:155]\n\t"
"ds_read_b128 a[144:147], v224 offset:16448\n\t"
"v_xor_b32 v249, 32, v248\n\t"
"v_mfma_f32_16x16x32_bf16 v[168:171], a[128:131], a[152:155], v[168:171]\n\t"
"ds_read_b128 a[160:163], v224 offset:20544\n\t"
"v_mfma_f32_16x16x32_bf16 v[168:171], a[132:135], a[168:171], v[168:171]\n\t"
"ds_read_b128 a[164:167], v224 offset:21056\n\t"
"v_xor_b32 v250, 64, v248\n\t"
"v_mfma_f32_16x16x32_bf16 v[184:187], a[136:139], a[152:155], v[184:187]\n\t"
"v_mfma_f32_16x16x32_bf16 v[184:187], a[140:143], a[168:171], v[184:187]\n\t"
"v_xor_b32 v251, 96, v248\n\t"
"v_mfma_f32_16x16x32_bf16 v[192:195], a[252:255], a[176:179], 0\n\t"
"v_mfma_f32_16x16x32_bf16 v[188:191], a[136:139], a[156:159], v[188:191]\n\t"
"v_mfma_f32_16x16x32_bf16 v[188:191], a[140:143], a[172:175], v[188:191]\n\t"
"ds_read_b128 a[168:171], v224 offset:21568\n\t"
"v_mfma_f32_16x16x32_bf16 v[196:199], a[252:255], a[184:187], 0\n\t"
"ds_read_b64_tr_b16 a[176:177], v248 offset:0\n\t"
"v_mfma_f32_16x16x32_bf16 v[172:175], a[128:131], a[156:159], v[172:175]\n\t"
"ds_read_b128 a[152:155], v224 offset:17472\n\t"
"v_mfma_f32_16x16x32_bf16 v[172:175], a[132:135], a[172:175], v[172:175]\n\t"
"ds_read_b64_tr_b16 a[184:185], v249 offset:0\n\t"
"v_mfma_f32_16x16x32_bf16 v[156:159], a[120:123], a[156:159], v[156:159]\n\t"
"ds_read_b128 a[156:159], v224 offset:17984\n\t"
"v_mfma_f32_16x16x32_bf16 v[156:159], a[124:127], a[172:175], v[156:159]\n\t"
"ds_read_b128 a[172:175], v224 offset:22080\n\t"
"v_cvt_pk_bf16_f32 v192, v192, v196\n\t"
"v_mfma_f32_16x16x32_bf16 v[200:203], a[252:255], a[178:181], 0\n\t"
"s_waitcnt lgkmcnt(6)\n\t"
"ds_read_b64_tr_b16 a[178:179], v248 offset:512\n\t"
"v_cvt_pk_bf16_f32 v193, v193, v197\n\t"
"v_mfma_f32_16x16x32_bf16 v[0:3], a[0:3], a[144:147], v[0:3]\n\t"
"ds_read_b64_tr_b16 a[180:181], v248 offset:1024\n\t"
"v_cvt_pk_bf16_f32 v194, v194, v198\n\t"
"v_mfma_f32_16x16x32_bf16 v[0:3], a[4:7], a[160:163], v[0:3]\n\t"
"ds_write_addtid_b32 v194 offset:2048\n\t"
"v_cvt_pk_bf16_f32 v195, v195, v199\n\t"
"v_mfma_f32_16x16x32_bf16 v[204:207], a[252:255], a[186:189], 0\n\t"
"s_nop 0\n\t"
"ds_write_addtid_b32 v192 offset:0\n\t"
"ds_read_b64_tr_b16 a[186:187], v249 offset:512\n\t"
"v_pk_fma_f32 v[48:49], v[144:145], s[90:91], v[48:49]\n\t"
"v_pk_fma_f32 v[50:51], v[146:147], s[90:91], v[50:51]\n\t"
"v_pk_add_f32 v[144:145], v[48:49], v[144:145] neg_lo:[0,1] neg_hi:[0,1]\n\t"
"v_pk_add_f32 v[146:147], v[50:51], v[146:147] neg_lo:[0,1] neg_hi:[0,1]\n\t"
"v_pk_fma_f32 v[64:65], v[160:161], s[90:91], v[64:65]\n\t"
"v_pk_fma_f32 v[66:67], v[162:163], s[90:91], v[66:67]\n\t"
"v_pk_add_f32 v[160:161], v[64:65], v[160:161] neg_lo:[0,1] neg_hi:[0,1]\n\t"
"v_pk_add_f32 v[162:163], v[66:67], v[162:163] neg_lo:[0,1] neg_hi:[0,1]\n\t"
"v_pk_fma_f32 v[80:81], v[176:177], s[90:91], v[80:81]\n\t"
"v_pk_fma_f32 v[82:83], v[178:179], s[90:91], v[82:83]\n\t"
"v_pk_add_f32 v[176:177], v[80:81], v[176:177] neg_lo:[0,1] neg_hi:[0,1]\n\t"
"v_pk_add_f32 v[178:179], v[82:83], v[178:179] neg_lo:[0,1] neg_hi:[0,1]\n\t"
"v_pk_fma_f32 v[84:85], v[180:181], s[90:91], v[84:85]\n\t"
"v_pk_fma_f32 v[86:87], v[182:183], s[90:91], v[86:87]\n\t"
"v_pk_add_f32 v[180:181], v[84:85], v[180:181] neg_lo:[0,1] neg_hi:[0,1]\n\t"
"v_pk_add_f32 v[182:183], v[86:87], v[182:183] neg_lo:[0,1] neg_hi:[0,1]\n\t"
"v_pk_fma_f32 v[68:69], v[164:165], s[90:91], v[68:69]\n\t"
"v_pk_fma_f32 v[70:71], v[166:167], s[90:91], v[70:71]\n\t"
"v_pk_add_f32 v[164:165], v[68:69], v[164:165] neg_lo:[0,1] neg_hi:[0,1]\n\t"
"v_pk_add_f32 v[166:167], v[70:71], v[166:167] neg_lo:[0,1] neg_hi:[0,1]\n\t"
"v_pk_fma_f32 v[52:53], v[148:149], s[90:91], v[52:53]\n\t"
"v_pk_fma_f32 v[54:55], v[150:151], s[90:91], v[54:55]\n\t"
"v_pk_add_f32 v[148:149], v[52:53], v[148:149] neg_lo:[0,1] neg_hi:[0,1]\n\t"
"v_pk_add_f32 v[150:151], v[54:55], v[150:151] neg_lo:[0,1] neg_hi:[0,1]\n\t"
"v_pk_fma_f32 v[56:57], v[152:153], s[90:91], v[56:57]\n\t"
"v_pk_fma_f32 v[58:59], v[154:155], s[90:91], v[58:59]\n\t"
"v_pk_add_f32 v[152:153], v[56:57], v[152:153] neg_lo:[0,1] neg_hi:[0,1]\n\t"
"v_pk_add_f32 v[154:155], v[58:59], v[154:155] neg_lo:[0,1] neg_hi:[0,1]\n\t"
"v_pk_fma_f32 v[72:73], v[168:169], s[90:91], v[72:73]\n\t"
"v_pk_fma_f32 v[74:75], v[170:171], s[90:91], v[74:75]\n\t"
"v_pk_add_f32 v[168:169], v[72:73], v[168:169] neg_lo:[0,1] neg_hi:[0,1]\n\t"
"v_pk_add_f32 v[170:171], v[74:75], v[170:171] neg_lo:[0,1] neg_hi:[0,1]\n\t"
"v_pk_fma_f32 v[88:89], v[184:185], s[90:91], v[88:89]\n\t"
"v_pk_fma_f32 v[90:91], v[186:187], s[90:91], v[90:91]\n\t"
"v_pk_add_f32 v[184:185], v[88:89], v[184:185] neg_lo:[0,1] neg_hi:[0,1]\n\t"
"v_pk_add_f32 v[186:187], v[90:91], v[186:187] neg_lo:[0,1] neg_hi:[0,1]\n\t"
"v_pk_fma_f32 v[92:93], v[188:189], s[90:91], v[92:93]\n\t"
"v_pk_fma_f32 v[94:95], v[190:191], s[90:91], v[94:95]\n\t"
"v_pk_add_f32 v[188:189], v[92:93], v[188:189] neg_lo:[0,1] neg_hi:[0,1]\n\t"
"v_pk_add_f32 v[190:191], v[94:95], v[190:191] neg_lo:[0,1] neg_hi:[0,1]\n\t"
"v_pk_fma_f32 v[76:77], v[172:173], s[90:91], v[76:77]\n\t"
"v_pk_fma_f32 v[78:79], v[174:175], s[90:91], v[78:79]\n\t"
"v_pk_add_f32 v[172:173], v[76:77], v[172:173] neg_lo:[0,1] neg_hi:[0,1]\n\t"
"v_pk_add_f32 v[174:175], v[78:79], v[174:175] neg_lo:[0,1] neg_hi:[0,1]\n\t"
"v_pk_fma_f32 v[60:61], v[156:157], s[90:91], v[60:61]\n\t"
"v_pk_fma_f32 v[62:63], v[158:159], s[90:91], v[62:63]\n\t"
"v_pk_add_f32 v[156:157], v[60:61], v[156:157] neg_lo:[0,1] neg_hi:[0,1]\n\t"
"v_pk_add_f32 v[158:159], v[62:63], v[158:159] neg_lo:[0,1] neg_hi:[0,1]\n\t"
"v_cvt_pk_bf16_f32 v48, v48, v49\n\t"
"v_mfma_f32_16x16x32_bf16 v[16:19], a[8:11], a[144:147], v[16:19]\n\t"
"ds_write_addtid_b32 v195 offset:10272\n\t"
"v_cvt_pk_bf16_f32 v68, v68, v69\n\t"
"v_mfma_f32_16x16x32_bf16 v[16:19], a[12:15], a[160:163], v[16:19]\n\t"
"ds_write_addtid_b32 v193 offset:8224\n\t"
"v_cvt_pk_bf16_f32 v49, v50, v51\n\t"
"v_cvt_pk_bf16_f32 v52, v52, v53\n\t"
"v_cvt_pk_bf16_f32 v64, v64, v65\n\t"
"v_mfma_f32_16x16x32_bf16 v[32:35], a[16:19], a[144:147], v[32:35]\n\t"
"ds_read_b64_tr_b16 a[188:189], v249 offset:1024\n\t"
"v_cvt_pk_bf16_f32 v53, v54, v55\n\t"
"v_mfma_f32_16x16x32_bf16 v[32:35], a[20:23], a[160:163], v[32:35]\n\t"
"v_cvt_pk_bf16_f32 v200, v200, v204\n\t"
"v_cvt_pk_bf16_f32 v65, v66, v67\n\t"
"v_mfma_f32_16x16x32_bf16 v[208:211], a[252:255], a[192:195], 0\n\t"
"ds_read_b128 a[160:163], v224 offset:22592\n\t"
"ds_write2st64_b64 v228, v[48:49], v[52:53] offset0:0 offset1:13\n\t"
"v_mfma_f32_16x16x32_bf16 v[36:39], a[16:19], a[148:151], v[36:39]\n\t"
"v_cvt_pk_bf16_f32 v84, v84, v85\n\t"
"v_cvt_pk_bf16_f32 v92, v92, v93\n\t"
"v_mfma_f32_16x16x32_bf16 v[36:39], a[20:23], a[164:167], v[36:39]\n\t"
"v_cvt_pk_bf16_f32 v56, v56, v57\n\t"
"v_cvt_pk_bf16_f32 v60, v60, v61\n\t"
"v_mfma_f32_16x16x32_bf16 v[20:23], a[8:11], a[148:151], v[20:23]\n\t"
"v_cvt_pk_bf16_f32 v69, v70, v71\n\t"
"v_cvt_pk_bf16_f32 v93, v94, v95\n\t"
"v_mfma_f32_16x16x32_bf16 v[20:23], a[12:15], a[164:167], v[20:23]\n\t"
"ds_read_b128 a[144:147], v224 offset:18496\n\t"
"ds_write2st64_b64 v246, v[64:65], v[68:69] offset0:0 offset1:13\n\t"
"v_mfma_f32_16x16x32_bf16 v[212:215], a[252:255], a[200:203], 0\n\t"
"v_cvt_pk_bf16_f32 v201, v201, v205\n\t"
"v_cvt_pk_bf16_f32 v80, v80, v81\n\t"
"v_cvt_pk_bf16_f32 v76, v76, v77\n\t"
"v_mfma_f32_16x16x32_bf16 v[4:7], a[0:3], a[148:151], v[4:7]\n\t"
"v_cvt_pk_bf16_f32 v81, v82, v83\n\t"
"v_cvt_pk_bf16_f32 v85, v86, v87\n\t"
"v_mfma_f32_16x16x32_bf16 v[4:7], a[4:7], a[164:167], v[4:7]\n\t"
"ds_write2st64_b64 v247, v[80:81], v[84:85] offset0:0 offset1:13\n\t"
"v_mfma_f32_16x16x32_bf16 v[216:219], a[252:255], a[194:197], 0\n\t"
"s_waitcnt lgkmcnt(5)\n\t"
"v_cvt_pk_bf16_f32 v57, v58, v59\n\t"
"v_cvt_pk_bf16_f32 v77, v78, v79\n\t"
"v_mfma_f32_16x16x32_bf16 v[8:11], a[0:3], a[152:155], v[8:11]\n\t"
"v_cvt_pk_bf16_f32 v209, v209, v213\n\t"
"v_cvt_pk_bf16_f32 v61, v62, v63\n\t"
"v_mfma_f32_16x16x32_bf16 v[8:11], a[4:7], a[168:171], v[8:11]\n\t"
"ds_write2st64_b64 v228, v[56:57], v[60:61] offset0:26 offset1:39\n\t"
"v_cvt_pk_bf16_f32 v203, v203, v207\n\t"
"v_mfma_f32_16x16x32_bf16 v[24:27], a[8:11], a[152:155], v[24:27]\n\t"
"v_cvt_pk_bf16_f32 v202, v202, v206\n\t"
"v_cvt_pk_bf16_f32 v211, v211, v215\n\t"
"v_mfma_f32_16x16x32_bf16 v[24:27], a[12:15], a[168:171], v[24:27]\n\t"
"v_cvt_pk_bf16_f32 v208, v208, v212\n\t"
"v_cvt_pk_bf16_f32 v210, v210, v214\n\t"
"v_mfma_f32_16x16x32_bf16 v[220:223], a[252:255], a[202:205], 0\n\t"
"s_nop 0\n\t"
"ds_read_b128 a[148:151], v224 offset:19008\n\t"
"ds_read_b128 a[164:167], v224 offset:23104\n\t"
"ds_write_addtid_b32 v200 offset:256\n\t"
"ds_write_addtid_b32 v201 offset:8480\n\t"
"ds_write_addtid_b32 v202 offset:2304\n\t"
"ds_write_addtid_b32 v203 offset:10528\n\t"
"ds_write_addtid_b32 v208 offset:4096\n\t"
"ds_write_addtid_b32 v209 offset:12320\n\t"
"ds_write_addtid_b32 v211 offset:14368\n\t"
"ds_read_b64_tr_b16 a[192:193], v250 offset:0\n\t"
"ds_read_b64_tr_b16 a[200:201], v251 offset:0\n\t"
"v_mfma_f32_16x16x32_bf16 v[40:43], a[16:19], a[152:155], v[40:43]\n\t"
"v_cvt_pk_bf16_f32 v72, v72, v73\n\t"
"v_cvt_pk_bf16_f32 v73, v74, v75\n\t"
"v_mfma_f32_16x16x32_bf16 v[40:43], a[20:23], a[168:171], v[40:43]\n\t"
"ds_write2st64_b64 v246, v[72:73], v[76:77] offset0:26 offset1:39\n\t"
"v_mfma_f32_16x16x32_bf16 v[44:47], a[16:19], a[156:159], v[44:47]\n\t"
"buffer_load_dwordx4 a[16:19], v235, s[8:11], s59 offen offset:0 sc1\n\t"
"v_mfma_f32_16x16x32_bf16 v[44:47], a[20:23], a[172:175], v[44:47]\n\t"
"v_cvt_pk_bf16_f32 v88, v88, v89\n\t"
"v_cvt_pk_bf16_f32 v89, v90, v91\n\t"
"v_mfma_f32_16x16x32_bf16 v[28:31], a[8:11], a[156:159], v[28:31]\n\t"
"buffer_load_dwordx4 a[20:23], v235, s[8:11], s59 offen offset:1024 sc1\n\t"
"v_mfma_f32_16x16x32_bf16 v[28:31], a[12:15], a[172:175], v[28:31]\n\t"
"ds_write_addtid_b32 v210 offset:6144\n\t"
"ds_write2st64_b64 v247, v[88:89], v[92:93] offset0:26 offset1:39\n\t"
"v_mfma_f32_16x16x32_bf16 v[12:15], a[0:3], a[156:159], v[12:15]\n\t"
"buffer_load_dwordx4 a[12:15], v252, s[8:11], s58 offen offset:3072 sc1\n\t"
"v_mfma_f32_16x16x32_bf16 v[12:15], a[4:7], a[172:175], v[12:15]\n\t"
"s_waitcnt lgkmcnt(14)\n\t"
"v_cvt_pk_bf16_f32 v216, v216, v220\n\t"
"v_cvt_pk_bf16_f32 v218, v218, v222\n\t"
"v_mfma_f32_16x16x32_bf16 v[96:99], a[24:27], a[144:147], v[96:99]\n\t"
"buffer_load_dwordx4 a[8:11], v252, s[8:11], s58 offen offset:2048 sc1\n\t"
"v_mfma_f32_16x16x32_bf16 v[96:99], a[28:31], a[160:163], v[96:99]\n\t"
"ds_write_addtid_b32 v216 offset:4352\n\t"
"v_cvt_pk_bf16_f32 v217, v217, v221\n\t"
"v_mfma_f32_16x16x32_bf16 v[112:115], a[32:35], a[144:147], v[112:115]\n\t"
"ds_write_addtid_b32 v218 offset:6400\n\t"
"v_cvt_pk_bf16_f32 v219, v219, v223\n\t"
"v_mfma_f32_16x16x32_bf16 v[112:115], a[36:39], a[160:163], v[112:115]\n\t"
"ds_write_addtid_b32 v217 offset:12576\n\t"
"buffer_load_dwordx4 a[0:3], v252, s[8:11], s58 offen offset:0 sc1\n\t"
"v_mfma_f32_16x16x32_bf16 v[128:131], a[40:43], a[144:147], v[128:131]\n\t"
"s_nop 0\n\t"
"ds_write_addtid_b32 v219 offset:14624\n\t"
"ds_read_b64_tr_b16 a[194:195], v250 offset:512\n\t"
"v_mfma_f32_16x16x32_bf16 v[128:131], a[44:47], a[160:163], v[128:131]\n\t"
"s_waitcnt lgkmcnt(7)\n\t"
"buffer_load_dwordx4 a[4:7], v252, s[8:11], s58 offen offset:1024 sc1\n\t"
"v_mfma_f32_16x16x32_bf16 v[132:135], a[40:43], a[148:151], v[132:135]\n\t"
"s_nop 0\n\t"
"ds_read_b128 a[168:171], v224 offset:23616\n\t"
"ds_read_b64_tr_b16 a[196:197], v250 offset:1024\n\t"
"v_mfma_f32_16x16x32_bf16 v[132:135], a[44:47], a[164:167], v[132:135]\n\t"
"s_nop 0\n\t"
"ds_read_b128 a[156:159], v224 offset:20032\n\t"
"ds_read_b64_tr_b16 a[202:203], v251 offset:512\n\t"
"ds_read_b64_tr_b16 a[204:205], v251 offset:1024\n\t"
"v_mfma_f32_16x16x32_bf16 v[116:119], a[32:35], a[148:151], v[116:119]\n\t"
"s_nop 0\n\t"
"ds_read_b128 a[152:155], v224 offset:19520\n\t"
"ds_read_b128 a[172:175], v224 offset:24128\n\t"
"v_mfma_f32_16x16x32_bf16 v[116:119], a[36:39], a[164:167], v[116:119]\n\t"
"v_mfma_f32_16x16x32_bf16 v[100:103], a[24:27], a[148:151], v[100:103]\n\t"
"v_mfma_f32_16x16x32_bf16 v[100:103], a[28:31], a[164:167], v[100:103]\n\t"
"s_waitcnt lgkmcnt(0)\n\t"
"s_barrier\n\t"
"v_mfma_f32_16x16x32_bf16 v[104:107], a[24:27], a[152:155], v[104:107]\n\t"
"ds_read_b128 a[144:147], v224 offset:0\n\t"
"v_cvt_pk_bf16_f32 v176, v176, v177\n\t"
"v_mfma_f32_16x16x32_bf16 v[104:107], a[28:31], a[168:171], v[104:107]\n\t"
"s_nop 0\n\t"
"ds_read_b128 a[160:163], v224 offset:4096\n\t"
"ds_read_b128 a[148:151], v224 offset:512\n\t"
"ds_read_b128 v[56:59], v229 offset:0\n\t"
"ds_read_b128 v[72:75], v230 offset:0\n\t"
"ds_read_b128 v[88:91], v231 offset:0\n\t"
"ds_read_b128 v[60:63], v229 offset:13312\n\t"
"v_mfma_f32_16x16x32_bf16 v[120:123], a[32:35], a[152:155], v[120:123]\n\t"
"ds_read_b128 a[164:167], v224 offset:4608\n\t"
"v_cvt_pk_bf16_f32 v164, v164, v165\n\t"
"v_mfma_f32_16x16x32_bf16 v[120:123], a[36:39], a[168:171], v[120:123]\n\t"
"ds_read_b128 v[76:79], v230 offset:13312\n\t"
"v_cvt_pk_bf16_f32 v144, v144, v145\n\t"
"v_mfma_f32_16x16x32_bf16 v[136:139], a[40:43], a[152:155], v[136:139]\n\t"
"ds_read_b128 v[92:95], v231 offset:13312\n\t"
"v_cvt_pk_bf16_f32 v180, v180, v181\n\t"
"v_mfma_f32_16x16x32_bf16 v[136:139], a[44:47], a[168:171], v[136:139]\n\t"
"v_cvt_pk_bf16_f32 v145, v146, v147\n\t"
"v_cvt_pk_bf16_f32 v152, v152, v153\n\t"
"v_mfma_f32_16x16x32_bf16 v[192:195], a[252:255], a[176:179], 0\n\t"
"v_cvt_pk_bf16_f32 v181, v182, v183\n\t"
"v_cvt_pk_bf16_f32 v188, v188, v189\n\t"
"v_mfma_f32_16x16x32_bf16 v[140:143], a[40:43], a[156:159], v[140:143]\n\t"
"ds_read_b128 a[152:155], v224 offset:1024\n\t"
"v_cvt_pk_bf16_f32 v177, v178, v179\n\t"
"v_mfma_f32_16x16x32_bf16 v[140:143], a[44:47], a[172:175], v[140:143]\n\t"
"buffer_load_dwordx4 a[40:43], v235, s[8:11], s58 offen offset:2048 sc1\n\t"
"buffer_load_dwordx4 a[44:47], v235, s[8:11], s58 offen offset:3072 sc1\n\t"
"v_mfma_f32_16x16x32_bf16 v[196:199], a[252:255], a[184:187], 0\n\t"
"s_waitcnt lgkmcnt(1)\n\t"
"s_barrier\n\t"
"v_mfma_f32_16x16x32_bf16 v[124:127], a[32:35], a[156:159], v[124:127]\n\t"
"ds_read_b128 a[168:171], v224 offset:5120\n\t"
"v_cvt_pk_bf16_f32 v148, v148, v149\n\t"
"v_mfma_f32_16x16x32_bf16 v[124:127], a[36:39], a[172:175], v[124:127]\n\t"
"buffer_store_dwordx4 v[56:59], v232, s[16:19], s24 offen nt\n\t"
"v_mfma_f32_16x16x32_bf16 v[108:111], a[24:27], a[156:159], v[108:111]\n\t"
"v_cvt_pk_bf16_f32 v149, v150, v151\n\t"
"v_cvt_pk_bf16_f32 v165, v166, v167\n\t"
"v_mfma_f32_16x16x32_bf16 v[108:111], a[28:31], a[172:175], v[108:111]\n\t"
"ds_read_b128 a[172:175], v224 offset:5632\n\t"
"ds_write2st64_b64 v228, v[144:145], v[148:149] offset0:0 offset1:13\n\t"
"v_mfma_f32_16x16x32_bf16 v[200:203], a[252:255], a[178:181], 0\n\t"
"buffer_load_dwordx4 a[36:39], v235, s[8:11], s58 offen offset:1024 sc1\n\t"
"buffer_load_dwordx4 a[32:35], v235, s[8:11], s58 offen offset:0 sc1\n\t"
"v_mfma_f32_16x16x32_bf16 v[0:3], a[48:51], a[144:147], v[0:3]\n\t"
"v_cvt_pk_bf16_f32 v156, v156, v157\n\t"
"v_cvt_pk_bf16_f32 v184, v184, v185\n\t"
"v_mfma_f32_16x16x32_bf16 v[0:3], a[52:55], a[160:163], v[0:3]\n\t"
"buffer_load_dwordx4 a[24:27], v252, s[8:11], s57 offen offset:2048 sc1\n\t"
"v_mfma_f32_16x16x32_bf16 v[204:207], a[252:255], a[186:189], 0\n\t"
"ds_read_b128 a[156:159], v224 offset:1536\n\t"
"ds_write2st64_b64 v247, v[176:177], v[180:181] offset0:0 offset1:13\n\t"
"v_mfma_f32_16x16x32_bf16 v[16:19], a[56:59], a[144:147], v[16:19]\n\t"
"v_cvt_pk_bf16_f32 v194, v194, v198\n\t"
"v_cvt_pk_bf16_f32 v160, v160, v161\n\t"
"v_mfma_f32_16x16x32_bf16 v[16:19], a[60:63], a[160:163], v[16:19]\n\t"
"buffer_load_dwordx4 a[28:31], v252, s[8:11], s57 offen offset:3072 sc1\n\t"
"v_cvt_pk_bf16_f32 v161, v162, v163\n\t"
"v_mfma_f32_16x16x32_bf16 v[32:35], a[64:67], a[144:147], v[32:35]\n\t"
"v_cvt_pk_bf16_f32 v193, v193, v197\n\t"
"v_cvt_pk_bf16_f32 v195, v195, v199\n\t"
"v_mfma_f32_16x16x32_bf16 v[32:35], a[68:71], a[160:163], v[32:35]\n\t"
"ds_write2st64_b64 v246, v[160:161], v[164:165] offset0:0 offset1:13\n\t"
"v_mfma_f32_16x16x32_bf16 v[208:211], a[252:255], a[192:195], 0\n\t"
"buffer_store_dwordx4 v[72:75], v233, s[16:19], s24 offen nt\n\t"
"v_mfma_f32_16x16x32_bf16 v[36:39], a[64:67], a[148:151], v[36:39]\n\t"
"v_cvt_pk_bf16_f32 v201, v201, v205\n\t"
"v_cvt_pk_bf16_f32 v153, v154, v155\n\t"
"v_mfma_f32_16x16x32_bf16 v[36:39], a[68:71], a[164:167], v[36:39]\n\t"
"v_cvt_pk_bf16_f32 v192, v192, v196\n\t"
"v_cvt_pk_bf16_f32 v202, v202, v206\n\t"
"v_mfma_f32_16x16x32_bf16 v[20:23], a[56:59], a[148:151], v[20:23]\n\t"
"ds_write_addtid_b32 v194 offset:18496\n\t"
"v_cvt_pk_bf16_f32 v200, v200, v204\n\t"
"v_mfma_f32_16x16x32_bf16 v[20:23], a[60:63], a[164:167], v[20:23]\n\t"
"ds_read_b128 a[144:147], v224 offset:2048\n\t"
"buffer_store_dwordx4 v[88:91], v234, s[16:19], s24 offen nt\n\t"
"v_mfma_f32_16x16x32_bf16 v[212:215], a[252:255], a[200:203], 0\n\t"
"s_nop 0\n\t"
"ds_read_b128 a[160:163], v224 offset:6144\n\t"
"ds_write_addtid_b32 v192 offset:16448\n\t"
"ds_write_addtid_b32 v193 offset:24672\n\t"
"ds_write_addtid_b32 v195 offset:26720\n\t"
"ds_write_addtid_b32 v200 offset:16704\n\t"
"ds_write_addtid_b32 v202 offset:18752\n\t"
"v_mfma_f32_16x16x32_bf16 v[4:7], a[48:51], a[148:151], v[4:7]\n\t"
"ds_write_addtid_b32 v201 offset:24928\n\t"
"v_cvt_pk_bf16_f32 v172, v172, v173\n\t"
"v_mfma_f32_16x16x32_bf16 v[4:7], a[52:55], a[164:167], v[4:7]\n\t"
"ds_read_b128 a[148:151], v224 offset:2560\n\t"
"v_cvt_pk_bf16_f32 v173, v174, v175\n\t"
"v_mfma_f32_16x16x32_bf16 v[216:219], a[252:255], a[194:197], 0\n\t"
"s_waitcnt lgkmcnt(11)\n\t"
"ds_read_b128 a[164:167], v224 offset:6656\n\t"
"v_cvt_pk_bf16_f32 v157, v158, v159\n\t"
"v_mfma_f32_16x16x32_bf16 v[8:11], a[48:51], a[152:155], v[8:11]\n\t"
"v_cvt_pk_bf16_f32 v203, v203, v207\n\t"
"v_cvt_pk_bf16_f32 v168, v168, v169\n\t"
"v_mfma_f32_16x16x32_bf16 v[8:11], a[52:55], a[168:171], v[8:11]\n\t"
"ds_write_addtid_b32 v203 offset:26976\n\t"
"ds_write2st64_b64 v228, v[152:153], v[156:157] offset0:26 offset1:39\n\t"
"v_mfma_f32_16x16x32_bf16 v[24:27], a[56:59], a[152:155], v[24:27]\n\t"
"v_cvt_pk_bf16_f32 v169, v170, v171\n\t"
"v_cvt_pk_bf16_f32 v189, v190, v191\n\t"
"v_mfma_f32_16x16x32_bf16 v[24:27], a[60:63], a[168:171], v[24:27]\n\t"
"buffer_store_dwordx4 v[60:63], v232, s[16:19], s27 offen nt\n\t"
"v_mfma_f32_16x16x32_bf16 v[220:223], a[252:255], a[202:205], 0\n\t"
"ds_write2st64_b64 v246, v[168:169], v[172:173] offset0:26 offset1:39\n\t"
"v_mfma_f32_16x16x32_bf16 v[40:43], a[64:67], a[152:155], v[40:43]\n\t"
"v_cvt_pk_bf16_f32 v208, v208, v212\n\t"
"v_accvgpr_read_b32 v248, a208\n\t"
"v_mfma_f32_16x16x32_bf16 v[40:43], a[68:71], a[168:171], v[40:43]\n\t"
"v_cvt_pk_bf16_f32 v211, v211, v215\n\t"
"v_cvt_pk_bf16_f32 v185, v186, v187\n\t"
"v_mfma_f32_16x16x32_bf16 v[44:47], a[64:67], a[156:159], v[44:47]\n\t"
"buffer_load_dwordx4 a[64:67], v235, s[8:11], s65 offen offset:0 sc1\n\t"
"v_mfma_f32_16x16x32_bf16 v[44:47], a[68:71], a[172:175], v[44:47]\n\t"
"ds_write_addtid_b32 v208 offset:20544\n\t"
"ds_write2st64_b64 v247, v[184:185], v[188:189] offset0:26 offset1:39\n\t"
"v_mfma_f32_16x16x32_bf16 v[28:31], a[56:59], a[156:159], v[28:31]\n\t"
"v_cvt_pk_bf16_f32 v210, v210, v214\n\t"
"v_cvt_pk_bf16_f32 v217, v217, v221\n\t"
"v_mfma_f32_16x16x32_bf16 v[28:31], a[60:63], a[172:175], v[28:31]\n\t"
"buffer_store_dwordx4 v[76:79], v233, s[16:19], s27 offen nt\n\t"
"v_mfma_f32_16x16x32_bf16 v[12:15], a[48:51], a[156:159], v[12:15]\n\t"
"v_cvt_pk_bf16_f32 v209, v209, v213\n\t"
"v_cvt_pk_bf16_f32 v219, v219, v223\n\t"
"v_mfma_f32_16x16x32_bf16 v[12:15], a[52:55], a[172:175], v[12:15]\n\t"
"s_waitcnt lgkmcnt(3)\n\t"
"ds_write_addtid_b32 v211 offset:30816\n\t"
"v_cvt_pk_bf16_f32 v216, v216, v220\n\t"
"v_mfma_f32_16x16x32_bf16 v[96:99], a[72:75], a[144:147], v[96:99]\n\t"
"s_nop 0\n\t"
"ds_write_addtid_b32 v210 offset:22592\n\t"
"ds_write_addtid_b32 v217 offset:29024\n\t"
"v_mfma_f32_16x16x32_bf16 v[96:99], a[76:79], a[160:163], v[96:99]\n\t"
"buffer_load_dwordx4 a[68:71], v235, s[8:11], s65 offen offset:1024 sc1\n\t"
"buffer_load_dwordx4 a[60:63], v252, s[8:11], s64 offen offset:3072 sc1\n\t"
"buffer_load_dwordx4 a[56:59], v252, s[8:11], s64 offen offset:2048 sc1\n\t"
"v_mfma_f32_16x16x32_bf16 v[112:115], a[80:83], a[144:147], v[112:115]\n\t"
"ds_write_addtid_b32 v209 offset:28768\n\t"
"v_add_u32 v248, 80896, v248\n\t"
"v_mfma_f32_16x16x32_bf16 v[112:115], a[84:87], a[160:163], v[112:115]\n\t"
"buffer_load_dwordx4 a[48:51], v252, s[8:11], s64 offen offset:0 sc1\n\t"
"v_mfma_f32_16x16x32_bf16 v[128:131], a[88:91], a[144:147], v[128:131]\n\t"
"ds_write_addtid_b32 v216 offset:20800\n\t"
"v_cvt_pk_bf16_f32 v218, v218, v222\n\t"
"v_mfma_f32_16x16x32_bf16 v[128:131], a[92:95], a[160:163], v[128:131]\n\t"
"s_nop 0\n\t"
"ds_read_b128 a[172:175], v224 offset:7680\n\t"
"ds_write_addtid_b32 v218 offset:22848\n\t"
"ds_write_addtid_b32 v219 offset:31072\n\t"
"v_mfma_f32_16x16x32_bf16 v[132:135], a[88:91], a[148:151], v[132:135]\n\t"
"buffer_load_dwordx4 a[52:55], v252, s[8:11], s64 offen offset:1024 sc1\n\t"
"v_mfma_f32_16x16x32_bf16 v[132:135], a[92:95], a[164:167], v[132:135]\n\t"
"ds_read_b128 a[168:171], v224 offset:7168\n\t"
"v_xor_b32 v249, 32, v248\n\t"
"v_mfma_f32_16x16x32_bf16 v[116:119], a[80:83], a[148:151], v[116:119]\n\t"
"s_nop 0\n\t"
"ds_read_b128 a[152:155], v224 offset:3072\n\t"
"ds_read_b128 a[156:159], v224 offset:3584\n\t"
"v_mfma_f32_16x16x32_bf16 v[116:119], a[84:87], a[164:167], v[116:119]\n\t"
"buffer_store_dwordx4 v[92:95], v234, s[16:19], s27 offen nt\n\t"
"v_mfma_f32_16x16x32_bf16 v[100:103], a[72:75], a[148:151], v[100:103]\n\t"
"v_xor_b32 v250, 64, v248\n\t"
"v_xor_b32 v251, 96, v248\n\t"
"v_mfma_f32_16x16x32_bf16 v[100:103], a[76:79], a[164:167], v[100:103]\n\t"
"s_waitcnt lgkmcnt(0)\n\t"
"s_barrier\n\t"
"v_mfma_f32_16x16x32_bf16 v[104:107], a[72:75], a[152:155], v[104:107]\n\t"
"s_nop 0\n\t"
"ds_read_b128 a[144:147], v224 offset:16448\n\t"
"ds_read_b64_tr_b16 a[176:177], v248 offset:41472\n\t"
"v_mfma_f32_16x16x32_bf16 v[104:107], a[76:79], a[168:171], v[104:107]\n\t"
"s_nop 0\n\t"
"ds_read_b128 a[160:163], v224 offset:20544\n\t"
"ds_read_b64_tr_b16 a[178:179], v249 offset:41472\n\t"
"v_mfma_f32_16x16x32_bf16 v[120:123], a[80:83], a[152:155], v[120:123]\n\t"
"s_nop 0\n\t"
"ds_read_b128 a[148:151], v224 offset:16960\n\t"
"ds_read_b64_tr_b16 a[180:181], v248 offset:41984\n\t"
"v_mfma_f32_16x16x32_bf16 v[120:123], a[84:87], a[168:171], v[120:123]\n\t"
"s_nop 0\n\t"
"ds_read_b128 a[164:167], v224 offset:21056\n\t"
"ds_read_b64_tr_b16 a[182:183], v249 offset:41984\n\t"
"v_mfma_f32_16x16x32_bf16 v[136:139], a[88:91], a[152:155], v[136:139]\n\t"
"s_nop 0\n\t"
"ds_read_b64_tr_b16 a[184:185], v250 offset:43776\n\t"
"ds_read_b128 v[152:155], v229 offset:0\n\t"
"v_mfma_f32_16x16x32_bf16 v[136:139], a[92:95], a[168:171], v[136:139]\n\t"
"s_nop 0\n\t"
"ds_read_b128 a[152:155], v224 offset:17472\n\t"
"ds_read_b64_tr_b16 a[186:187], v251 offset:43776\n\t"
"v_mfma_f32_16x16x32_bf16 v[140:143], a[88:91], a[156:159], v[140:143]\n\t"
"s_nop 0\n\t"
"ds_read_b128 a[168:171], v224 offset:21568\n\t"
"ds_read_b64_tr_b16 a[188:189], v250 offset:44288\n\t"
"v_mfma_f32_16x16x32_bf16 v[140:143], a[92:95], a[172:175], v[140:143]\n\t"
"buffer_load_dwordx4 a[88:91], v235, s[8:11], s64 offen offset:2048 sc1\n\t"
"buffer_load_dwordx4 a[92:95], v235, s[8:11], s64 offen offset:3072 sc1\n\t"
"v_mfma_f32_16x16x32_bf16 v[124:127], a[80:83], a[156:159], v[124:127]\n\t"
"s_nop 0\n\t"
"ds_read_b64_tr_b16 a[190:191], v251 offset:44288\n\t"
"ds_read_b64_tr_b16 a[192:193], v250 offset:41472\n\t"
"v_mfma_f32_16x16x32_bf16 v[124:127], a[84:87], a[172:175], v[124:127]\n\t"
"s_waitcnt lgkmcnt(8)\n\t"
"buffer_load_dwordx4 a[84:87], v235, s[8:11], s64 offen offset:1024 sc1\n\t"
"v_mfma_f32_4x4x4_16b_bf16 v[192:195], a[210:211], a[176:177], 0\n\t"
"ds_read_b128 v[168:171], v230 offset:0\n\t"
"v_mfma_f32_4x4x4_16b_bf16 v[196:199], a[210:211], a[178:179], 0\n\t"
"ds_read_b64_tr_b16 a[194:195], v251 offset:41472\n\t"
"v_mfma_f32_4x4x4_16b_bf16 v[200:203], a[210:211], a[180:181], 0\n\t"
"s_waitcnt lgkmcnt(8)\n\t"
"buffer_store_dwordx4 v[152:155], v232, s[16:19], s20 offen nt\n\t"
"v_mfma_f32_4x4x4_16b_bf16 v[204:207], a[210:211], a[182:183], 0\n\t"
"s_nop 0\n\t"
"ds_read_b64_tr_b16 a[196:197], v250 offset:41984\n\t"
"ds_read_b128 v[184:187], v231 offset:0\n\t"
"v_mfma_f32_16x16x32_bf16 v[108:111], a[72:75], a[156:159], v[108:111]\n\t"
"ds_read_b64_tr_b16 a[198:199], v251 offset:41984\n\t"
"v_cvt_pk_bf16_f32 v196, v192, v196\n\t"
"v_mfma_f32_16x16x32_bf16 v[108:111], a[76:79], a[172:175], v[108:111]\n\t"
"buffer_load_dwordx4 a[80:83], v235, s[8:11], s64 offen offset:0 sc1\n\t"
"v_mfma_f32_16x16x32_bf16 v[0:3], a[96:99], a[144:147], v[0:3]\n\t"
"ds_read_b64_tr_b16 a[200:201], v248 offset:43776\n\t"
"v_cvt_pk_bf16_f32 v195, v195, v199\n\t"
"v_mfma_f32_16x16x32_bf16 v[0:3], a[100:103], a[160:163], v[0:3]\n\t"
"ds_read_b128 a[172:175], v224 offset:22080\n\t"
"buffer_load_dwordx4 a[72:75], v252, s[8:11], s63 offen offset:2048 sc1\n\t"
"v_mfma_f32_16x16x32_bf16 v[16:19], a[104:107], a[144:147], v[16:19]\n\t"
"ds_read_b128 a[156:159], v224 offset:17984\n\t"
"v_cvt_pk_bf16_f32 v197, v193, v197\n\t"
"v_mfma_f32_16x16x32_bf16 v[16:19], a[108:111], a[160:163], v[16:19]\n\t"
"buffer_load_dwordx4 a[76:79], v252, s[8:11], s63 offen offset:3072 sc1\n\t"
"v_mfma_f32_16x16x32_bf16 v[32:35], a[112:115], a[144:147], v[32:35]\n\t"
"ds_read_b64_tr_b16 a[202:203], v249 offset:43776\n\t"
"v_cvt_pk_bf16_f32 v199, v201, v205\n\t"
"v_mfma_f32_16x16x32_bf16 v[32:35], a[116:119], a[160:163], v[32:35]\n\t"
"s_waitcnt lgkmcnt(7)\n\t"
"ds_write_addtid_b32 v199 offset:2304\n\t"
"v_cvt_pk_bf16_f32 v194, v194, v198\n\t"
"v_mfma_f32_4x4x4_16b_bf16 v[208:211], a[210:211], a[184:185], 0\n\t"
"buffer_store_dwordx4 v[168:171], v233, s[16:19], s20 offen nt\n\t"
"v_mfma_f32_4x4x4_16b_bf16 v[212:215], a[210:211], a[186:187], 0\n\t"
"ds_read_b128 v[156:159], v229 offset:13312\n\t"
"v_mfma_f32_4x4x4_16b_bf16 v[216:219], a[210:211], a[188:189], 0\n\t"
"v_cvt_pk_bf16_f32 v193, v203, v207\n\t"
"v_mfma_f32_4x4x4_16b_bf16 v[220:223], a[210:211], a[190:191], 0\n\t"
"s_waitcnt lgkmcnt(6)\n\t"
"buffer_store_dwordx4 v[184:187], v234, s[16:19], s20 offen nt\n\t"
"v_mfma_f32_16x16x32_bf16 v[36:39], a[112:115], a[148:151], v[36:39]\n\t"
"s_nop 0\n\t"
"v_cvt_pk_bf16_f32 v212, v208, v212\n\t"
"v_cvt_pk_bf16_f32 v213, v209, v213\n\t"
"v_mfma_f32_16x16x32_bf16 v[36:39], a[116:119], a[164:167], v[36:39]\n\t"
"ds_read_b128 v[172:175], v230 offset:13312\n\t"
"v_cvt_pk_bf16_f32 v210, v210, v214\n\t"
"v_mfma_f32_16x16x32_bf16 v[20:23], a[104:107], a[148:151], v[20:23]\n\t"
"ds_write_addtid_b32 v197 offset:2048\n\t"
"v_cvt_pk_bf16_f32 v208, v218, v222\n\t"
"v_mfma_f32_16x16x32_bf16 v[20:23], a[108:111], a[164:167], v[20:23]\n\t"
"ds_read_b64_tr_b16 a[204:205], v248 offset:44288\n\t"
"v_cvt_pk_bf16_f32 v214, v216, v220\n\t"
"v_mfma_f32_16x16x32_bf16 v[4:7], a[96:99], a[148:151], v[4:7]\n\t"
"ds_read_b128 a[148:151], v224 offset:19008\n\t"
"v_cvt_pk_bf16_f32 v192, v202, v206\n\t"
"v_mfma_f32_16x16x32_bf16 v[4:7], a[100:103], a[164:167], v[4:7]\n\t"
"ds_write_addtid_b32 v213 offset:10272\n\t"
"s_waitcnt lgkmcnt(5)\n\t"
"buffer_store_dwordx4 v[156:159], v232, s[16:19], s25 offen nt\n\t"
"v_mfma_f32_16x16x32_bf16 v[8:11], a[96:99], a[152:155], v[8:11]\n\t"
"ds_read_b128 a[160:163], v224 offset:22592\n\t"
"v_cvt_pk_bf16_f32 v211, v211, v215\n\t"
"v_mfma_f32_16x16x32_bf16 v[8:11], a[100:103], a[168:171], v[8:11]\n\t"
"ds_read_b128 v[188:191], v231 offset:13312\n\t"
"v_cvt_pk_bf16_f32 v198, v200, v204\n\t"
"v_mfma_f32_4x4x4_16b_bf16 v[200:203], a[210:211], a[192:193], 0\n\t"
"ds_write_addtid_b32 v198 offset:256\n\t"
"v_mfma_f32_4x4x4_16b_bf16 v[204:207], a[210:211], a[194:195], 0\n\t"
"ds_write_addtid_b32 v196 offset:0\n\t"
"v_mfma_f32_4x4x4_16b_bf16 v[196:199], a[210:211], a[196:197], 0\n\t"
"ds_read_b128 a[164:167], v224 offset:23104\n\t"
"v_cvt_pk_bf16_f32 v209, v219, v223\n\t"
"v_cvt_pk_bf16_f32 v215, v217, v221\n\t"
"v_mfma_f32_4x4x4_16b_bf16 v[216:219], a[210:211], a[198:199], 0\n\t"
"s_nop 0\n\t"
"ds_read_b128 a[144:147], v224 offset:18496\n\t"
"ds_write_addtid_b32 v214 offset:8480\n\t"
"v_mfma_f32_16x16x32_bf16 v[24:27], a[104:107], a[152:155], v[24:27]\n\t"
"ds_write_addtid_b32 v212 offset:8224\n\t"
"v_cvt_pk_bf16_f32 v202, v202, v206\n\t"
"v_mfma_f32_16x16x32_bf16 v[24:27], a[108:111], a[168:171], v[24:27]\n\t"
"s_waitcnt lgkmcnt(10)\n\t"
"buffer_store_dwordx4 v[172:175], v233, s[16:19], s25 offen nt\n\t"
"v_mfma_f32_16x16x32_bf16 v[40:43], a[112:115], a[152:155], v[40:43]\n\t"
"ds_read_b64_tr_b16 a[206:207], v249 offset:44288\n\t"
"v_cvt_pk_bf16_f32 v203, v203, v207\n\t"
"v_mfma_f32_16x16x32_bf16 v[40:43], a[116:119], a[168:171], v[40:43]\n\t"
"v_cvt_pk_bf16_f32 v205, v201, v205\n\t"
"v_cvt_pk_bf16_f32 v207, v197, v217\n\t"
"v_mfma_f32_16x16x32_bf16 v[44:47], a[112:115], a[156:159], v[44:47]\n\t"
"ds_write_addtid_b32 v215 offset:10528\n\t"
"v_cvt_pk_bf16_f32 v201, v199, v219\n\t"
"v_mfma_f32_16x16x32_bf16 v[44:47], a[116:119], a[172:175], v[44:47]\n\t"
"buffer_load_dwordx4 a[112:115], v235, s[8:11], s47 offen offset:0 sc1\n\t"
"buffer_load_dwordx4 a[116:119], v235, s[8:11], s47 offen offset:1024 sc1\n\t"
"v_mfma_f32_16x16x32_bf16 v[28:31], a[104:107], a[156:159], v[28:31]\n\t"
"v_cvt_pk_bf16_f32 v204, v200, v204\n\t"
"v_cvt_pk_bf16_f32 v206, v196, v216\n\t"
"v_mfma_f32_16x16x32_bf16 v[28:31], a[108:111], a[172:175], v[28:31]\n\t"
"s_waitcnt lgkmcnt(1)\n\t"
"ds_write_addtid_b32 v207 offset:6400\n\t"
"v_cvt_pk_bf16_f32 v200, v198, v218\n\t"
"v_mfma_f32_4x4x4_16b_bf16 v[220:223], a[210:211], a[200:201], 0\n\t"
"buffer_store_dwordx4 v[188:191], v234, s[16:19], s25 offen nt\n\t"
"v_mfma_f32_4x4x4_16b_bf16 v[212:215], a[210:211], a[202:203], 0\n\t"
"ds_write_addtid_b32 v206 offset:4352\n\t"
"v_mfma_f32_4x4x4_16b_bf16 v[196:199], a[210:211], a[204:205], 0\n\t"
"ds_write_addtid_b32 v204 offset:4096\n\t"
"v_mfma_f32_4x4x4_16b_bf16 v[216:219], a[210:211], a[206:207], 0\n\t"
"buffer_load_dwordx4 a[108:111], v252, s[8:11], s46 offen offset:3072 sc1\n\t"
"v_mfma_f32_16x16x32_bf16 v[12:15], a[96:99], a[156:159], v[12:15]\n\t"
"ds_write_addtid_b32 v205 offset:6144\n\t"
"s_nop 0\n\t"
"v_cvt_pk_bf16_f32 v223, v223, v215\n\t"
"v_mfma_f32_16x16x32_bf16 v[12:15], a[100:103], a[172:175], v[12:15]\n\t"
"buffer_load_dwordx4 a[104:107], v252, s[8:11], s46 offen offset:2048 sc1\n\t"
"v_mfma_f32_16x16x32_bf16 v[96:99], a[120:123], a[144:147], v[96:99]\n\t"
"v_cvt_pk_bf16_f32 v222, v222, v214\n\t"
"v_cvt_pk_bf16_f32 v215, v197, v217\n\t"
"v_mfma_f32_16x16x32_bf16 v[96:99], a[124:127], a[160:163], v[96:99]\n\t"
"buffer_load_dwordx4 a[96:99], v252, s[8:11], s46 offen offset:0 sc1\n\t"
"v_mfma_f32_16x16x32_bf16 v[112:115], a[128:131], a[144:147], v[112:115]\n\t"
"v_cvt_pk_bf16_f32 v212, v220, v212\n\t"
"v_cvt_pk_bf16_f32 v213, v221, v213\n\t"
"v_mfma_f32_16x16x32_bf16 v[112:115], a[132:135], a[160:163], v[112:115]\n\t"
"ds_write_addtid_b32 v215 offset:14624\n\t"
"v_cvt_pk_bf16_f32 v214, v196, v216\n\t"
"v_mfma_f32_16x16x32_bf16 v[128:131], a[136:139], a[144:147], v[128:131]\n\t"
"s_nop 0\n\t"
"ds_write_addtid_b32 v212 offset:12320\n\t"
"ds_write_addtid_b32 v213 offset:14368\n\t"
"v_mfma_f32_16x16x32_bf16 v[128:131], a[140:143], a[160:163], v[128:131]\n\t"
"ds_write_addtid_b32 v214 offset:12576\n\t"
"buffer_load_dwordx4 a[100:103], v252, s[8:11], s46 offen offset:1024 sc1\n\t"
"v_mfma_f32_16x16x32_bf16 v[132:135], a[136:139], a[148:151], v[132:135]\n\t"
"ds_read_b128 a[172:175], v224 offset:24128\n\t"
"v_mfma_f32_16x16x32_bf16 v[132:135], a[140:143], a[164:167], v[132:135]\n\t"
"s_nop 0\n\t"
"ds_read_b128 a[168:171], v224 offset:23616\n\t"
"ds_read_b128 a[156:159], v224 offset:20032\n\t"
"v_mfma_f32_16x16x32_bf16 v[116:119], a[128:131], a[148:151], v[116:119]\n\t"
"ds_read_b128 a[152:155], v224 offset:19520\n\t"
"v_mfma_f32_16x16x32_bf16 v[116:119], a[132:135], a[164:167], v[116:119]\n\t"
"v_cvt_pk_bf16_f32 v221, v199, v219\n\t"
"v_mfma_f32_16x16x32_bf16 v[100:103], a[120:123], a[148:151], v[100:103]\n\t"
"v_cvt_pk_bf16_f32 v220, v198, v218\n\t"
"v_mfma_f32_16x16x32_bf16 v[100:103], a[124:127], a[164:167], v[100:103]\n\t"
"s_waitcnt lgkmcnt(0)\n\t"
"s_barrier\n\t"
"s_add_u32 s71, s71, 1\n\t"
"v_mov_b32 v215, s71\n\t"
"s_mov_b64 exec, 1\n\t"
"buffer_store_dword v215, off, s[8:11], s70 sc1\n\t"
"s_mov_b64 exec, -1\n\t"
"s_sub_u32 s21, s21, 1\n\t"
"s_xor_b64 s[84:85], s[84:85], s[88:89]\n\t"
"s_branch Lunit_%=\n\t"
"Lfb_%=:\n\t"
"v_mbcnt_lo_u32_b32 v162, -1, 0\n\t"
"v_mbcnt_hi_u32_b32 v162, -1, v162\n\t"
"v_bfe_u32 v161, v162, 1, 2\n\t"
"v_lshlrev_b32 v163, 4, v162\n\t"
"v_and_or_b32 v161, v163, 16, v161\n\t"
"v_mul_u32_u24 v161, 0xc0, v161\n\t"
"v_lshrrev_b32 v163, 5, v162\n\t"
"v_mul_u32_u24 v163, 12, v163\n\t"
"v_bfe_u32 v164, v162, 3, 2\n\t"
"v_add3_u32 v161, v161, v163, v164\n\t"
"v_lshlrev_b32 v161, 1, v161\n\t"
"v_lshlrev_b32 v162, 1, v162\n\t"
"v_mov_b32 v152, 0\n\t"
"v_mov_b32 v153, 0\n\t"
"v_mov_b32 v154, 0\n\t"
"v_mov_b32 v155, 0\n\t"
"v_mov_b32 v156, 0\n\t"
"v_mov_b32 v157, 0\n\t"
"v_mov_b32 v158, 0\n\t"
"v_mov_b32 v159, 0\n\t"
"v_mov_b32 v160, 0\n\t"
"s_mov_b32 s67, 0\n\t"
"Lfb0_%=:\n\t"
"s_mov_b32 s69, 0\n\t"
"Lfb1_%=:\n\t"
"s_lshr_b32 s60, s69, 5\n\t"
"s_mul_i32 s56, s60, 12288\n\t"
"s_bfe_u32 s61, s69, 0x20001\n\t"
"s_mul_i32 s61, s61, 1536\n\t"
"s_add_u32 s56, s56, s61\n\t"
"s_bitcmp1_b32 s69, 0\n\t"
"s_cselect_b32 s61, 48, 0\n\t"
"s_add_u32 s56, s56, s61\n\t"
"s_bfe_u32 s62, s69, 0x20003\n\t"
"s_mul_i32 s61, s62, 96\n\t"
"s_add_u32 s56, s56, s61\n\t"
"s_lshl3_add_u32 s56, s67, s56\n\t"
"s_lshr_b32 s60, s69, 5\n\t"
"s_bfe_u32 s62, s69, 0x20003\n\t"
"s_and_b32 s61, s60, 1\n\t"
"s_lshl1_add_u32 s61, s67, s61\n\t"
"s_mul_i32 s62, s62, 288\n\t"
"s_add_u32 s61, s61, s62\n\t"
"s_lshr_b32 s60, s60, 1\n\t"
"s_mul_i32 s62, s60, 96\n\t"
"s_add_u32 s61, s61, s62\n\t"
"s_lshl_b32 s57, s61, 10\n\t"
"s_and_b32 s61, s69, 7\n\t"
"s_lshl_b32 s61, s61, 7\n\t"
"s_add_u32 s57, s57, s61\n\t"
"s_add_u32 s60, s56, 0\n\t"
"buffer_load_short_d16_hi v152, v161, s[52:55], s60 offen\n\t"
"s_add_u32 s60, s56, 73728\n\t"
"buffer_load_short_d16_hi v153, v161, s[52:55], s60 offen\n\t"
"s_add_u32 s60, s56, 147456\n\t"
"buffer_load_short_d16_hi v154, v161, s[52:55], s60 offen\n\t"
"s_add_u32 s60, s56, 221184\n\t"
"buffer_load_short_d16_hi v155, v161, s[52:55], s60 offen\n\t"
"s_add_u32 s60, s56, 294912\n\t"
"buffer_load_short_d16_hi v156, v161, s[52:55], s60 offen\n\t"
"s_add_u32 s60, s56, 368640\n\t"
"buffer_load_short_d16_hi v157, v161, s[52:55], s60 offen\n\t"
"s_add_u32 s60, s56, 442368\n\t"
"buffer_load_short_d16_hi v158, v161, s[52:55], s60 offen\n\t"
"s_add_u32 s60, s56, 516096\n\t"
"buffer_load_short_d16_hi v159, v161, s[52:55], s60 offen\n\t"
"s_add_u32 s60, s56, 589824\n\t"
"buffer_load_short_d16_hi v160, v161, s[52:55], s60 offen\n\t"
"s_waitcnt vmcnt(2)\n\t"
"v_add_f32 v168, v152, v158\n\t"
"s_waitcnt vmcnt(1)\n\t"
"v_add_f32 v169, v153, v159\n\t"
"s_waitcnt vmcnt(0)\n\t"
"v_add_f32 v170, v154, v160\n\t"
"v_add_f32 v171, v168, v155\n\t"
"v_sub_f32 v174, v168, v155\n\t"
"v_add_f32 v172, v169, v156\n\t"
"v_sub_f32 v175, v169, v156\n\t"
"v_add_f32 v173, v170, v157\n\t"
"v_sub_f32 v176, v170, v157\n\t"
"v_add_f32 v177, v152, v154\n\t"
"v_add_f32 v178, v171, v173\n\t"
"v_add_f32 v179, v174, v176\n\t"
"v_add_f32 v180, v158, v160\n\t"
"v_add_f32 v182, v177, v153\n\t"
"v_sub_f32 v183, v177, v153\n\t"
"v_add_f32 v184, v178, v172\n\t"
"v_sub_f32 v185, v178, v172\n\t"
"v_add_f32 v186, v179, v175\n\t"
"v_sub_f32 v187, v179, v175\n\t"
"v_add_f32 v188, v180, v159\n\t"
"v_sub_f32 v189, v180, v159\n\t"
"v_mul_f32 v191, 0.5, v182\n\t"
"v_mul_f32 v192, 0.5, v183\n\t"
"v_mul_f32 v194, 0.5, v171\n\t"
"v_mul_f32 v195, 0x3e800000, v184\n\t"
"v_mul_f32 v196, 0x3e800000, v185\n\t"
"v_mul_f32 v197, -0.5, v173\n\t"
"v_mul_f32 v199, 0.5, v186\n\t"
"v_mul_f32 v200, 0.5, v187\n\t"
"v_mul_f32 v203, -0.5, v188\n\t"
"v_mul_f32 v204, -0.5, v189\n\t"
"v_cvt_pk_bf16_f32 v206, v152, v191\n\t"
"v_cvt_pk_bf16_f32 v207, v192, -v154\n\t"
"v_cvt_pk_bf16_f32 v208, v194, v195\n\t"
"v_cvt_pk_bf16_f32 v209, v196, v197\n\t"
"v_cvt_pk_bf16_f32 v210, v174, v199\n\t"
"v_cvt_pk_bf16_f32 v211, v200, -v176\n\t"
"v_cvt_pk_bf16_f32 v212, -v158, v203\n\t"
"v_cvt_pk_bf16_f32 v213, v204, v160\n\t"
"s_add_u32 s60, s57, 0\n\t"
"buffer_store_short v206, v162, s[8:11], s60 offen sc1\n\t"
"s_add_u32 s60, s57, 6144\n\t"
"buffer_store_short_d16_hi v206, v162, s[8:11], s60 offen sc1\n\t"
"s_add_u32 s60, s57, 12288\n\t"
"buffer_store_short v207, v162, s[8:11], s60 offen sc1\n\t"
"s_add_u32 s60, s57, 18432\n\t"
"buffer_store_short_d16_hi v207, v162, s[8:11], s60 offen sc1\n\t"
"s_add_u32 s60, s57, 24576\n\t"
"buffer_store_short v208, v162, s[8:11], s60 offen sc1\n\t"
"s_add_u32 s60, s57, 30720\n\t"
"buffer_store_short_d16_hi v208, v162, s[8:11], s60 offen sc1\n\t"
"s_add_u32 s60, s57, 36864\n\t"
"buffer_store_short v209, v162, s[8:11], s60 offen sc1\n\t"
"s_add_u32 s60, s57, 43008\n\t"
"buffer_store_short_d16_hi v209, v162, s[8:11], s60 offen sc1\n\t"
"s_add_u32 s60, s57, 49152\n\t"
"buffer_store_short v210, v162, s[8:11], s60 offen sc1\n\t"
"s_add_u32 s60, s57, 55296\n\t"
"buffer_store_short_d16_hi v210, v162, s[8:11], s60 offen sc1\n\t"
"s_add_u32 s60, s57, 61440\n\t"
"buffer_store_short v211, v162, s[8:11], s60 offen sc1\n\t"
"s_add_u32 s60, s57, 67584\n\t"
"buffer_store_short_d16_hi v211, v162, s[8:11], s60 offen sc1\n\t"
"s_add_u32 s60, s57, 73728\n\t"
"buffer_store_short v212, v162, s[8:11], s60 offen sc1\n\t"
"s_add_u32 s60, s57, 79872\n\t"
"buffer_store_short_d16_hi v212, v162, s[8:11], s60 offen sc1\n\t"
"s_add_u32 s60, s57, 86016\n\t"
"buffer_store_short v213, v162, s[8:11], s60 offen sc1\n\t"
"s_add_u32 s60, s57, 92160\n\t"
"buffer_store_short_d16_hi v213, v162, s[8:11], s60 offen sc1\n\t"
"s_waitcnt vmcnt(0)\n\t"
"s_add_u32 s69, s69, 1\n\t"
"s_cmp_lt_u32 s69, 192\n\t"
"s_cbranch_scc1 Lfb1_%=\n\t"
"s_add_u32 s67, s67, 1\n\t"
"s_cmp_lt_u32 s67, 3\n\t"
"s_cbranch_scc1 Lfb0_%=\n\t"
"s_branch Lgo_%=\n\t"
"Ltail_%=:\n\t"
"v_mfma_f32_16x16x32_bf16 v[104:107], a[72:75], a[152:155], v[104:107]\n\t"
"s_nop 0\n\t"
"ds_read_b128 a[144:147], v224 offset:18496\n\t"
"ds_read_b128 a[160:163], v224 offset:22592\n\t"
"v_mfma_f32_16x16x32_bf16 v[104:107], a[76:79], a[168:171], v[104:107]\n\t"
"buffer_load_dwordx4 a[8:11], v235, s[84:87], s69 offen offset:2048 sc1\n\t"
"buffer_load_dwordx4 a[0:3], v235, s[84:87], s69 offen offset:0 sc1\n\t"
"v_mfma_f32_16x16x32_bf16 v[120:123], a[80:83], a[152:155], v[120:123]\n\t"
"ds_read_b128 a[148:151], v224 offset:19008\n\t"
"v_add_u32 v248, 101632, v226\n\t"
"v_mfma_f32_16x16x32_bf16 v[120:123], a[84:87], a[168:171], v[120:123]\n\t"
"ds_read_b128 a[164:167], v224 offset:23104\n\t"
"v_xor_b32 v249, 32, v248\n\t"
"v_mfma_f32_16x16x32_bf16 v[136:139], a[88:91], a[152:155], v[136:139]\n\t"
"buffer_load_dwordx4 a[4:7], v235, s[84:87], s69 offen offset:1024 sc1\n\t"
"v_mfma_f32_16x16x32_bf16 v[136:139], a[92:95], a[168:171], v[136:139]\n\t"
"ds_read_b128 v[8:11], v229 offset:19968\n\t"
"v_xor_b32 v250, 64, v248\n\t"
"v_mfma_f32_16x16x32_bf16 v[192:195], a[224:227], a[176:179], 0\n\t"
"buffer_load_dwordx4 a[40:43], v252, s[84:87], s81 offen offset:0 sc1\n\t"
"buffer_load_dwordx4 a[44:47], v252, s[84:87], s81 offen offset:1024 sc1\n\t"
"v_mfma_f32_16x16x32_bf16 v[140:143], a[88:91], a[156:159], v[140:143]\n\t"
"ds_read_b128 v[24:27], v230 offset:19968\n\t"
"v_xor_b32 v251, 96, v248\n\t"
"v_mfma_f32_16x16x32_bf16 v[140:143], a[92:95], a[172:175], v[140:143]\n\t"
"buffer_load_dwordx4 a[36:39], v235, s[84:87], s81 offen offset:3072 sc1\n\t"
"v_mfma_f32_16x16x32_bf16 v[196:199], a[224:227], a[180:183], 0\n\t"
"s_nop 0\n\t"
"ds_read_b128 a[168:171], v224 offset:23616\n\t"
"ds_read_b64_tr_b16 a[176:177], v248 offset:41472\n\t"
"v_mfma_f32_16x16x32_bf16 v[124:127], a[80:83], a[156:159], v[124:127]\n\t"
"s_nop 0\n\t"
"ds_read_b128 a[152:155], v224 offset:19520\n\t"
"ds_read_b64_tr_b16 a[178:179], v248 offset:41984\n\t"
"v_mfma_f32_16x16x32_bf16 v[124:127], a[84:87], a[172:175], v[124:127]\n\t"
"buffer_load_dwordx4 a[32:35], v235, s[84:87], s81 offen offset:2048 sc1\n\t"
"v_mfma_f32_16x16x32_bf16 v[108:111], a[72:75], a[156:159], v[108:111]\n\t"
"s_nop 0\n\t"
"ds_read_b128 a[156:159], v224 offset:20032\n\t"
"ds_read_b128 v[40:43], v231 offset:19968\n\t"
"v_mfma_f32_16x16x32_bf16 v[108:111], a[76:79], a[172:175], v[108:111]\n\t"
"buffer_load_dwordx4 a[24:27], v235, s[84:87], s81 offen offset:0 sc1\n\t"
"v_mfma_f32_16x16x32_bf16 v[200:203], a[224:227], a[184:187], 0\n\t"
"s_waitcnt vmcnt(22) lgkmcnt(8)\n\t"
"ds_read_b64_tr_b16 a[180:181], v248 offset:42496\n\t"
"v_cvt_pk_bf16_f32 v195, v195, v199\n\t"
"v_mfma_f32_16x16x32_bf16 v[144:147], a[96:99], a[144:147], v[144:147]\n\t"
"buffer_load_dwordx4 a[28:31], v235, s[84:87], s81 offen offset:1024 sc1\n\t"
"v_mfma_f32_16x16x32_bf16 v[144:147], a[100:103], a[160:163], v[144:147]\n\t"
"ds_read_b128 a[172:175], v224 offset:24128\n\t"
"v_cvt_pk_bf16_f32 v194, v194, v198\n\t"
"v_mfma_f32_16x16x32_bf16 v[204:207], a[224:227], a[188:191], 0\n\t"
"s_waitcnt lgkmcnt(8)\n\t"
"buffer_store_dwordx4 v[8:11], v232, s[16:19], s24 offen nt\n\t"
"v_mfma_f32_16x16x32_bf16 v[160:163], a[104:107], a[144:147], v[160:163]\n\t"
"ds_read_b64_tr_b16 a[184:185], v249 offset:41472\n\t"
"v_cvt_pk_bf16_f32 v192, v192, v196\n\t"
"v_mfma_f32_16x16x32_bf16 v[160:163], a[108:111], a[160:163], v[160:163]\n\t"
"s_nop 0\n\t"
"ds_write_addtid_b32 v192 offset:0\n\t"
"ds_read_b64_tr_b16 a[186:187], v249 offset:41984\n\t"
"ds_read_b64_tr_b16 a[188:189], v249 offset:42496\n\t"
"v_mfma_f32_16x16x32_bf16 v[176:179], a[112:115], a[144:147], v[176:179]\n\t"
"ds_write_addtid_b32 v194 offset:8224\n\t"
"v_cvt_pk_bf16_f32 v193, v193, v197\n\t"
"v_mfma_f32_16x16x32_bf16 v[176:179], a[116:119], a[160:163], v[176:179]\n\t"
"buffer_store_dwordx4 v[24:27], v233, s[16:19], s24 offen nt\n\t"
"v_mfma_f32_16x16x32_bf16 v[208:211], a[224:227], a[192:195], 0\n\t"
"s_nop 0\n\t"
"ds_read_b128 a[144:147], v224 offset:16448\n\t"
"ds_write_addtid_b32 v193 offset:256\n\t"
"v_mfma_f32_16x16x32_bf16 v[180:183], a[112:115], a[148:151], v[180:183]\n\t"
"s_nop 0\n\t"
"ds_read_b64_tr_b16 a[192:193], v250 offset:41472\n\t"
"ds_read_b128 v[12:15], v229 offset:33280\n\t"
"v_mfma_f32_16x16x32_bf16 v[180:183], a[116:119], a[164:167], v[180:183]\n\t"
"buffer_load_dwordx4 a[64:67], v252, s[84:87], s57 offen offset:0 sc1\n\t"
"buffer_load_dwordx4 a[68:71], v252, s[84:87], s57 offen offset:1024 sc1\n\t"
"v_mfma_f32_16x16x32_bf16 v[164:167], a[104:107], a[148:151], v[164:167]\n\t"
"ds_read_b128 a[160:163], v224 offset:20544\n\t"
"v_cvt_pk_bf16_f32 v202, v202, v206\n\t"
"v_mfma_f32_16x16x32_bf16 v[164:167], a[108:111], a[164:167], v[164:167]\n\t"
"ds_write_addtid_b32 v195 offset:8480\n\t"
"buffer_load_dwordx4 a[60:63], v235, s[84:87], s57 offen offset:3072 sc1\n\t"
"v_mfma_f32_16x16x32_bf16 v[212:215], a[224:227], a[196:199], 0\n\t"
"ds_write_addtid_b32 v202 offset:12320\n\t"
"v_cvt_pk_bf16_f32 v201, v201, v205\n\t"
"v_mfma_f32_16x16x32_bf16 v[148:151], a[96:99], a[148:151], v[148:151]\n\t"
"ds_write_addtid_b32 v201 offset:4352\n\t"
"v_cvt_pk_bf16_f32 v200, v200, v204\n\t"
"v_mfma_f32_16x16x32_bf16 v[148:151], a[100:103], a[164:167], v[148:151]\n\t"
"s_waitcnt lgkmcnt(9)\n\t"
"buffer_store_dwordx4 v[40:43], v234, s[16:19], s24 offen nt\n\t"
"v_mfma_f32_16x16x32_bf16 v[216:219], a[224:227], a[200:203], 0\n\t"
"ds_read_b128 a[164:167], v224 offset:21056\n\t"
"v_cvt_pk_bf16_f32 v203, v203, v207\n\t"
"v_mfma_f32_16x16x32_bf16 v[152:155], a[96:99], a[152:155], v[152:155]\n\t"
"ds_read_b128 v[28:31], v230 offset:33280\n\t"
"v_cvt_pk_bf16_f32 v208, v208, v212\n\t"
"v_mfma_f32_16x16x32_bf16 v[152:155], a[100:103], a[168:171], v[152:155]\n\t"
"s_waitcnt lgkmcnt(6)\n\t"
"buffer_store_dwordx4 v[12:15], v232, s[16:19], s27 offen nt\n\t"
"v_mfma_f32_16x16x32_bf16 v[168:171], a[104:107], a[152:155], v[168:171]\n\t"
"s_nop 0\n\t"
"ds_write_addtid_b32 v200 offset:4096\n\t"
"ds_read_b64_tr_b16 a[194:195], v250 offset:41984\n\t"
"v_mfma_f32_16x16x32_bf16 v[168:171], a[108:111], a[168:171], v[168:171]\n\t"
"ds_read_b128 a[148:151], v224 offset:16960\n\t"
"v_cvt_pk_bf16_f32 v210, v210, v214\n\t"
"v_mfma_f32_16x16x32_bf16 v[220:223], a[224:227], a[204:207], 0\n\t"
"ds_read_b128 v[44:47], v231 offset:33280\n\t"
"buffer_load_dwordx4 a[56:59], v235, s[84:87], s57 offen offset:2048 sc1\n\t"
"v_mfma_f32_16x16x32_bf16 v[184:187], a[112:115], a[152:155], v[184:187]\n\t"
"ds_write_addtid_b32 v203 offset:12576\n\t"
"v_cvt_pk_bf16_f32 v209, v209, v213\n\t"
"v_mfma_f32_16x16x32_bf16 v[184:187], a[116:119], a[168:171], v[184:187]\n\t"
"ds_write_addtid_b32 v208 offset:2048\n\t"
"buffer_load_dwordx4 a[48:51], v235, s[84:87], s57 offen offset:0 sc1\n\t"
"v_mfma_f32_16x16x32_bf16 v[188:191], a[112:115], a[156:159], v[188:191]\n\t"
"s_nop 0\n\t"
"ds_write_addtid_b32 v209 offset:2304\n\t"
"ds_read_b64_tr_b16 a[196:197], v250 offset:42496\n\t"
"v_mfma_f32_16x16x32_bf16 v[188:191], a[116:119], a[172:175], v[188:191]\n\t"
"buffer_load_dwordx4 a[52:55], v235, s[84:87], s57 offen offset:1024 sc1\n\t"
"v_mfma_f32_16x16x32_bf16 v[172:175], a[104:107], a[156:159], v[172:175]\n\t"
"ds_write_addtid_b32 v210 offset:10272\n\t"
"v_cvt_pk_bf16_f32 v216, v216, v220\n\t"
"v_mfma_f32_16x16x32_bf16 v[172:175], a[108:111], a[172:175], v[172:175]\n\t"
"ds_read_b64_tr_b16 a[200:201], v251 offset:41472\n\t"
"v_cvt_pk_bf16_f32 v211, v211, v215\n\t"
"v_mfma_f32_16x16x32_bf16 v[156:159], a[96:99], a[156:159], v[156:159]\n\t"
"ds_write_addtid_b32 v211 offset:10528\n\t"
"v_cvt_pk_bf16_f32 v218, v218, v222\n\t"
"v_mfma_f32_16x16x32_bf16 v[156:159], a[100:103], a[172:175], v[156:159]\n\t"
"s_waitcnt vmcnt(27) lgkmcnt(9)\n\t"
"ds_write_addtid_b32 v216 offset:6144\n\t"
"buffer_store_dwordx4 v[28:31], v233, s[16:19], s27 offen nt\n\t"
"v_mfma_f32_16x16x32_bf16 v[96:99], a[120:123], a[144:147], v[96:99]\n\t"
"ds_read_b128 a[172:175], v224 offset:22080\n\t"
"v_cvt_pk_bf16_f32 v217, v217, v221\n\t"
"v_mfma_f32_16x16x32_bf16 v[96:99], a[124:127], a[160:163], v[96:99]\n\t"
"ds_write_addtid_b32 v218 offset:14368\n\t"
"v_cvt_pk_bf16_f32 v219, v219, v223\n\t"
"v_mfma_f32_16x16x32_bf16 v[112:115], a[128:131], a[144:147], v[112:115]\n\t"
"buffer_load_dwordx4 a[88:91], v252, s[84:87], s63 offen offset:0 sc1\n\t"
"v_mfma_f32_16x16x32_bf16 v[112:115], a[132:135], a[160:163], v[112:115]\n\t"
"s_nop 0\n\t"
"ds_write_addtid_b32 v217 offset:6400\n\t"
"ds_write_addtid_b32 v219 offset:14624\n\t"
"ds_read_b64_tr_b16 a[202:203], v251 offset:41984\n\t"
"v_mfma_f32_16x16x32_bf16 v[128:131], a[136:139], a[144:147], v[128:131]\n\t"
"buffer_load_dwordx4 a[92:95], v252, s[84:87], s63 offen offset:1024 sc1\n\t"
"v_mfma_f32_16x16x32_bf16 v[128:131], a[140:143], a[160:163], v[128:131]\n\t"
"s_waitcnt lgkmcnt(9)\n\t"
"ds_read_b128 a[168:171], v224 offset:21568\n\t"
"ds_read_b64_tr_b16 a[204:205], v251 offset:42496\n\t"
"v_mfma_f32_16x16x32_bf16 v[132:135], a[136:139], a[148:151], v[132:135]\n\t"
"buffer_load_dwordx4 a[84:87], v235, s[84:87], s63 offen offset:3072 sc1\n\t"
"v_mfma_f32_16x16x32_bf16 v[132:135], a[140:143], a[164:167], v[132:135]\n\t"
"s_nop 0\n\t"
"ds_read_b128 a[152:155], v224 offset:17472\n\t"
"ds_read_b128 a[156:159], v224 offset:17984\n\t"
"v_mfma_f32_16x16x32_bf16 v[116:119], a[128:131], a[148:151], v[116:119]\n\t"
"buffer_load_dwordx4 a[80:83], v235, s[84:87], s63 offen offset:2048 sc1\n\t"
"v_mfma_f32_16x16x32_bf16 v[116:119], a[132:135], a[164:167], v[116:119]\n\t"
"buffer_store_dwordx4 v[44:47], v234, s[16:19], s27 offen nt\n\t"
"v_mfma_f32_16x16x32_bf16 v[100:103], a[120:123], a[148:151], v[100:103]\n\t"
"v_mfma_f32_16x16x32_bf16 v[100:103], a[124:127], a[164:167], v[100:103]\n\t"
"s_waitcnt vmcnt(26) lgkmcnt(7)\n\t"
"s_waitcnt lgkmcnt(0)\n\t"
"s_barrier\n\t"
"v_mfma_f32_16x16x32_bf16 v[104:107], a[120:123], a[152:155], v[104:107]\n\t"
"s_nop 0\n\t"
"ds_read_b128 a[144:147], v224 offset:0\n\t"
"ds_read_b128 a[148:151], v224 offset:512\n\t"
"v_mfma_f32_16x16x32_bf16 v[104:107], a[124:127], a[168:171], v[104:107]\n\t"
"buffer_load_dwordx4 a[72:75], v235, s[84:87], s63 offen offset:0 sc1\n\t"
"v_mfma_f32_16x16x32_bf16 v[120:123], a[128:131], a[152:155], v[120:123]\n\t"
"ds_read_b128 a[160:163], v224 offset:4096\n\t"
"v_mfma_f32_16x16x32_bf16 v[120:123], a[132:135], a[168:171], v[120:123]\n\t"
"ds_read_b128 a[164:167], v224 offset:4608\n\t"
"v_mfma_f32_16x16x32_bf16 v[136:139], a[136:139], a[152:155], v[136:139]\n\t"
"v_mfma_f32_16x16x32_bf16 v[136:139], a[140:143], a[168:171], v[136:139]\n\t"
"buffer_load_dwordx4 a[76:79], v235, s[84:87], s63 offen offset:1024 sc1\n\t"
"v_mfma_f32_16x16x32_bf16 v[192:195], a[220:223], a[176:179], 0\n\t"
"ds_read_b128 a[152:155], v224 offset:1024\n\t"
"v_mfma_f32_16x16x32_bf16 v[140:143], a[136:139], a[156:159], v[140:143]\n\t"
"ds_read_b64_tr_b16 a[176:177], v248 offset:0\n\t"
"v_mfma_f32_16x16x32_bf16 v[140:143], a[140:143], a[172:175], v[140:143]\n\t"
"buffer_load_dwordx4 a[112:115], v252, s[84:87], s42 offen offset:0 sc1\n\t"
"v_mfma_f32_16x16x32_bf16 v[196:199], a[220:223], a[184:187], 0\n\t"
"ds_read_b64_tr_b16 a[184:185], v249 offset:0\n\t"
"v_mfma_f32_16x16x32_bf16 v[124:127], a[128:131], a[156:159], v[124:127]\n\t"
"ds_read_b128 a[168:171], v224 offset:5120\n\t"
"v_mfma_f32_16x16x32_bf16 v[124:127], a[132:135], a[172:175], v[124:127]\n\t"
"v_mfma_f32_16x16x32_bf16 v[108:111], a[120:123], a[156:159], v[108:111]\n\t"
"buffer_load_dwordx4 a[116:119], v252, s[84:87], s42 offen offset:1024 sc1\n\t"
"v_mfma_f32_16x16x32_bf16 v[108:111], a[124:127], a[172:175], v[108:111]\n\t"
"s_nop 0\n\t"
"v_cvt_pk_bf16_f32 v193, v193, v197\n\t"
"v_cvt_pk_bf16_f32 v195, v195, v199\n\t"
"v_mfma_f32_16x16x32_bf16 v[200:203], a[220:223], a[178:181], 0\n\t"
"s_waitcnt vmcnt(26) lgkmcnt(4)\n\t"
"buffer_load_dwordx4 a[108:111], v235, s[84:87], s42 offen offset:3072 sc1\n\t"
"buffer_load_dwordx4 a[104:107], v235, s[84:87], s42 offen offset:2048 sc1\n\t"
"v_mfma_f32_16x16x32_bf16 v[48:51], a[0:3], a[144:147], v[48:51]\n\t"
"ds_read_b128 a[172:175], v224 offset:5632\n\t"
"v_cvt_pk_bf16_f32 v194, v194, v198\n\t"
"v_mfma_f32_16x16x32_bf16 v[48:51], a[4:7], a[160:163], v[48:51]\n\t"
"ds_write_addtid_b32 v195 offset:26720\n\t"
"v_cvt_pk_bf16_f32 v192, v192, v196\n\t"
"v_mfma_f32_16x16x32_bf16 v[204:207], a[220:223], a[186:189], 0\n\t"
"ds_write_addtid_b32 v193 offset:24672\n\t"
"buffer_load_dwordx4 a[96:99], v235, s[84:87], s42 offen offset:0 sc1\n\t"
"buffer_load_dwordx4 a[100:103], v235, s[84:87], s42 offen offset:1024 sc1\n\t"
"buffer_load_dwordx4 a[136:139], v252, s[84:87], s48 offen offset:0 sc1\n\t"
"v_pk_add_f32 v[96:97], v[96:97], v[144:145]\n\t"
"v_pk_add_f32 v[98:99], v[98:99], v[146:147]\n\t"
"v_pk_fma_f32 v[144:145], v[144:145], s[38:39], v[96:97]\n\t"
"v_pk_fma_f32 v[146:147], v[146:147], s[38:39], v[98:99]\n\t"
"v_pk_add_f32 v[112:113], v[112:113], v[160:161]\n\t"
"v_pk_add_f32 v[114:115], v[114:115], v[162:163]\n\t"
"v_pk_fma_f32 v[160:161], v[160:161], s[38:39], v[112:113]\n\t"
"v_pk_fma_f32 v[162:163], v[162:163], s[38:39], v[114:115]\n\t"
"v_pk_add_f32 v[128:129], v[128:129], v[176:177]\n\t"
"v_pk_add_f32 v[130:131], v[130:131], v[178:179]\n\t"
"v_pk_fma_f32 v[176:177], v[176:177], s[38:39], v[128:129]\n\t"
"v_pk_fma_f32 v[178:179], v[178:179], s[38:39], v[130:131]\n\t"
"v_pk_add_f32 v[132:133], v[132:133], v[180:181]\n\t"
"v_pk_add_f32 v[134:135], v[134:135], v[182:183]\n\t"
"v_pk_fma_f32 v[180:181], v[180:181], s[38:39], v[132:133]\n\t"
"v_pk_fma_f32 v[182:183], v[182:183], s[38:39], v[134:135]\n\t"
"v_pk_add_f32 v[116:117], v[116:117], v[164:165]\n\t"
"v_pk_add_f32 v[118:119], v[118:119], v[166:167]\n\t"
"v_pk_fma_f32 v[164:165], v[164:165], s[38:39], v[116:117]\n\t"
"v_pk_fma_f32 v[166:167], v[166:167], s[38:39], v[118:119]\n\t"
"v_pk_add_f32 v[100:101], v[100:101], v[148:149]\n\t"
"v_pk_add_f32 v[102:103], v[102:103], v[150:151]\n\t"
"v_pk_fma_f32 v[148:149], v[148:149], s[38:39], v[100:101]\n\t"
"v_pk_fma_f32 v[150:151], v[150:151], s[38:39], v[102:103]\n\t"
"v_pk_add_f32 v[104:105], v[104:105], v[152:153]\n\t"
"v_pk_add_f32 v[106:107], v[106:107], v[154:155]\n\t"
"v_pk_fma_f32 v[152:153], v[152:153], s[38:39], v[104:105]\n\t"
"v_pk_fma_f32 v[154:155], v[154:155], s[38:39], v[106:107]\n\t"
"v_pk_add_f32 v[120:121], v[120:121], v[168:169]\n\t"
"v_pk_add_f32 v[122:123], v[122:123], v[170:171]\n\t"
"v_pk_fma_f32 v[168:169], v[168:169], s[38:39], v[120:121]\n\t"
"v_pk_fma_f32 v[170:171], v[170:171], s[38:39], v[122:123]\n\t"
"v_pk_add_f32 v[136:137], v[136:137], v[184:185]\n\t"
"v_pk_add_f32 v[138:139], v[138:139], v[186:187]\n\t"
"v_pk_fma_f32 v[184:185], v[184:185], s[38:39], v[136:137]\n\t"
"v_pk_fma_f32 v[186:187], v[186:187], s[38:39], v[138:139]\n\t"
"v_pk_add_f32 v[140:141], v[140:141], v[188:189]\n\t"
"v_pk_add_f32 v[142:143], v[142:143], v[190:191]\n\t"
"v_pk_fma_f32 v[188:189], v[188:189], s[38:39], v[140:141]\n\t"
"v_pk_fma_f32 v[190:191], v[190:191], s[38:39], v[142:143]\n\t"
"v_pk_add_f32 v[124:125], v[124:125], v[172:173]\n\t"
"v_pk_add_f32 v[126:127], v[126:127], v[174:175]\n\t"
"v_pk_fma_f32 v[172:173], v[172:173], s[38:39], v[124:125]\n\t"
"v_pk_fma_f32 v[174:175], v[174:175], s[38:39], v[126:127]\n\t"
"v_pk_add_f32 v[108:109], v[108:109], v[156:157]\n\t"
"v_pk_add_f32 v[110:111], v[110:111], v[158:159]\n\t"
"v_pk_fma_f32 v[156:157], v[156:157], s[38:39], v[108:109]\n\t"
"v_pk_fma_f32 v[158:159], v[158:159], s[38:39], v[110:111]\n\t"
"v_cvt_pk_bf16_f32 v100, v100, v101\n\t"
"v_cvt_pk_bf16_f32 v112, v112, v113\n\t"
"v_cvt_pk_bf16_f32 v128, v128, v129\n\t"
"v_mfma_f32_16x16x32_bf16 v[64:67], a[8:11], a[144:147], v[64:67]\n\t"
"ds_read_b128 a[156:159], v224 offset:1536\n\t"
"v_cvt_pk_bf16_f32 v96, v96, v97\n\t"
"v_mfma_f32_16x16x32_bf16 v[64:67], a[12:15], a[160:163], v[64:67]\n\t"
"buffer_load_dwordx4 a[140:143], v252, s[84:87], s48 offen offset:1024 sc1\n\t"
"v_cvt_pk_bf16_f32 v101, v102, v103\n\t"
"v_cvt_pk_bf16_f32 v116, v116, v117\n\t"
"v_mfma_f32_16x16x32_bf16 v[80:83], a[16:19], a[144:147], v[80:83]\n\t"
"ds_read_b64_tr_b16 a[178:179], v248 offset:512\n\t"
"v_cvt_pk_bf16_f32 v97, v98, v99\n\t"
"v_mfma_f32_16x16x32_bf16 v[80:83], a[20:23], a[160:163], v[80:83]\n\t"
"v_cvt_pk_bf16_f32 v203, v203, v207\n\t"
"v_cvt_pk_bf16_f32 v120, v120, v121\n\t"
"v_mfma_f32_16x16x32_bf16 v[208:211], a[220:223], a[192:195], 0\n\t"
"s_nop 0\n\t"
"ds_read_b128 a[160:163], v224 offset:6144\n\t"
"ds_write_addtid_b32 v192 offset:16448\n\t"
"ds_write_addtid_b32 v194 offset:18496\n\t"
"ds_read_b64_tr_b16 a[180:181], v248 offset:1024\n\t"
"ds_read_b64_tr_b16 a[186:187], v249 offset:512\n\t"
"ds_read_b64_tr_b16 a[188:189], v249 offset:1024\n\t"
"buffer_load_dwordx4 a[132:135], v235, s[84:87], s48 offen offset:3072 sc1\n\t"
"ds_write2st64_b64 v228, v[96:97], v[100:101] offset0:39 offset1:52\n\t"
"v_mfma_f32_16x16x32_bf16 v[84:87], a[16:19], a[148:151], v[84:87]\n\t"
"v_cvt_pk_bf16_f32 v113, v114, v115\n\t"
"v_cvt_pk_bf16_f32 v117, v118, v119\n\t"
"v_mfma_f32_16x16x32_bf16 v[84:87], a[20:23], a[164:167], v[84:87]\n\t"
"v_cvt_pk_bf16_f32 v132, v132, v133\n\t"
"v_cvt_pk_bf16_f32 v108, v108, v109\n\t"
"v_mfma_f32_16x16x32_bf16 v[68:71], a[8:11], a[148:151], v[68:71]\n\t"
"v_cvt_pk_bf16_f32 v129, v130, v131\n\t"
"v_cvt_pk_bf16_f32 v109, v110, v111\n\t"
"v_mfma_f32_16x16x32_bf16 v[68:71], a[12:15], a[164:167], v[68:71]\n\t"
"ds_read_b128 a[144:147], v224 offset:2048\n\t"
"buffer_load_dwordx4 a[128:131], v235, s[84:87], s48 offen offset:2048 sc1\n\t"
"ds_write2st64_b64 v246, v[112:113], v[116:117] offset0:39 offset1:52\n\t"
"v_cvt_pk_bf16_f32 v104, v104, v105\n\t"
"v_mfma_f32_16x16x32_bf16 v[212:215], a[220:223], a[200:203], 0\n\t"
"v_cvt_pk_bf16_f32 v202, v202, v206\n\t"
"v_cvt_pk_bf16_f32 v124, v124, v125\n\t"
"v_mfma_f32_16x16x32_bf16 v[52:55], a[0:3], a[148:151], v[52:55]\n\t"
"v_cvt_pk_bf16_f32 v201, v201, v205\n\t"
"v_cvt_pk_bf16_f32 v133, v134, v135\n\t"
"v_mfma_f32_16x16x32_bf16 v[52:55], a[4:7], a[164:167], v[52:55]\n\t"
"ds_write2st64_b64 v247, v[128:129], v[132:133] offset0:39 offset1:52\n\t"
"v_mfma_f32_16x16x32_bf16 v[216:219], a[220:223], a[194:197], 0\n\t"
"s_waitcnt lgkmcnt(10)\n\t"
"buffer_load_dwordx4 a[120:123], v235, s[84:87], s48 offen offset:0 sc1\n\t"
"v_mfma_f32_16x16x32_bf16 v[56:59], a[0:3], a[152:155], v[56:59]\n\t"
"v_cvt_pk_bf16_f32 v105, v106, v107\n\t"
"v_cvt_pk_bf16_f32 v140, v140, v141\n\t"
"v_mfma_f32_16x16x32_bf16 v[56:59], a[4:7], a[168:171], v[56:59]\n\t"
"ds_write2st64_b64 v228, v[104:105], v[108:109] offset0:65 offset1:78\n\t"
"v_mfma_f32_16x16x32_bf16 v[72:75], a[8:11], a[152:155], v[72:75]\n\t"
"v_cvt_pk_bf16_f32 v208, v208, v212\n\t"
"v_cvt_pk_bf16_f32 v121, v122, v123\n\t"
"v_mfma_f32_16x16x32_bf16 v[72:75], a[12:15], a[168:171], v[72:75]\n\t"
"buffer_load_dwordx4 a[124:127], v235, s[84:87], s48 offen offset:1024 sc1\n\t"
"v_cvt_pk_bf16_f32 v200, v200, v204\n\t"
"v_mfma_f32_16x16x32_bf16 v[220:223], a[220:223], a[202:205], 0\n\t"
"s_nop 0\n\t"
"ds_read_b128 a[148:151], v224 offset:2560\n\t"
"ds_read_b128 a[164:167], v224 offset:6656\n\t"
"ds_write_addtid_b32 v200 offset:16704\n\t"
"ds_write_addtid_b32 v201 offset:24928\n\t"
"ds_write_addtid_b32 v202 offset:18752\n\t"
"ds_write_addtid_b32 v203 offset:26976\n\t"
"ds_read_b64_tr_b16 a[192:193], v250 offset:0\n\t"
"v_mfma_f32_16x16x32_bf16 v[88:91], a[16:19], a[152:155], v[88:91]\n\t"
"v_cvt_pk_bf16_f32 v210, v210, v214\n\t"
"v_cvt_pk_bf16_f32 v125, v126, v127\n\t"
"v_mfma_f32_16x16x32_bf16 v[88:91], a[20:23], a[168:171], v[88:91]\n\t"
"ds_read_b64_tr_b16 a[200:201], v251 offset:0\n\t"
"ds_write2st64_b64 v246, v[120:121], v[124:125] offset0:65 offset1:78\n\t"
"v_mfma_f32_16x16x32_bf16 v[92:95], a[16:19], a[156:159], v[92:95]\n\t"
"v_cvt_pk_bf16_f32 v209, v209, v213\n\t"
"v_cvt_pk_bf16_f32 v136, v136, v137\n\t"
"v_mfma_f32_16x16x32_bf16 v[92:95], a[20:23], a[172:175], v[92:95]\n\t"
"buffer_load_dwordx4 a[16:19], v252, s[84:87], s75 offen offset:0 sc1\n\t"
"v_mfma_f32_16x16x32_bf16 v[76:79], a[8:11], a[156:159], v[76:79]\n\t"
"v_cvt_pk_bf16_f32 v137, v138, v139\n\t"
"v_cvt_pk_bf16_f32 v141, v142, v143\n\t"
"v_mfma_f32_16x16x32_bf16 v[76:79], a[12:15], a[172:175], v[76:79]\n\t"
"ds_write2st64_b64 v247, v[136:137], v[140:141] offset0:65 offset1:78\n\t"
"v_mfma_f32_16x16x32_bf16 v[60:63], a[0:3], a[156:159], v[60:63]\n\t"
"v_cvt_pk_bf16_f32 v211, v211, v215\n\t"
"v_cvt_pk_bf16_f32 v217, v217, v221\n\t"
"v_mfma_f32_16x16x32_bf16 v[60:63], a[4:7], a[172:175], v[60:63]\n\t"
"s_waitcnt vmcnt(31) lgkmcnt(10)\n\t"
"buffer_load_dwordx4 a[20:23], v252, s[84:87], s75 offen offset:1024 sc1\n\t"
"v_mfma_f32_16x16x32_bf16 v[144:147], a[24:27], a[144:147], v[144:147]\n\t"
"v_cvt_pk_bf16_f32 v216, v216, v220\n\t"
"v_cvt_pk_bf16_f32 v219, v219, v223\n\t"
"v_mfma_f32_16x16x32_bf16 v[144:147], a[28:31], a[160:163], v[144:147]\n\t"
"s_nop 0\n\t"
"ds_write_addtid_b32 v208 offset:20544\n\t"
"ds_write_addtid_b32 v210 offset:22592\n\t"
"ds_write_addtid_b32 v211 offset:30816\n\t"
"ds_write_addtid_b32 v217 offset:29024\n\t"
"ds_write_addtid_b32 v219 offset:31072\n\t"
"v_mfma_f32_16x16x32_bf16 v[160:163], a[32:35], a[144:147], v[160:163]\n\t"
"ds_write_addtid_b32 v209 offset:28768\n\t"
"v_cvt_pk_bf16_f32 v218, v218, v222\n\t"
"v_mfma_f32_16x16x32_bf16 v[160:163], a[36:39], a[160:163], v[160:163]\n\t"
"ds_write_addtid_b32 v216 offset:20800\n\t"
"buffer_load_dwordx4 a[12:15], v235, s[84:87], s75 offen offset:3072 sc1\n\t"
"v_mfma_f32_16x16x32_bf16 v[176:179], a[40:43], a[144:147], v[176:179]\n\t"
"s_nop 0\n\t"
"ds_read_b128 a[172:175], v224 offset:7680\n\t"
"ds_read_b64_tr_b16 a[194:195], v250 offset:512\n\t"
"v_mfma_f32_16x16x32_bf16 v[176:179], a[44:47], a[160:163], v[176:179]\n\t"
"s_waitcnt lgkmcnt(10)\n\t"
"ds_write_addtid_b32 v218 offset:22848\n\t"
"buffer_load_dwordx4 a[8:11], v235, s[84:87], s75 offen offset:2048 sc1\n\t"
"v_mfma_f32_16x16x32_bf16 v[180:183], a[40:43], a[148:151], v[180:183]\n\t"
"s_nop 0\n\t"
"ds_read_b64_tr_b16 a[196:197], v250 offset:1024\n\t"
"ds_read_b64_tr_b16 a[202:203], v251 offset:512\n\t"
"v_mfma_f32_16x16x32_bf16 v[180:183], a[44:47], a[164:167], v[180:183]\n\t"
"s_nop 0\n\t"
"ds_read_b128 a[156:159], v224 offset:3584\n\t"
"ds_read_b64_tr_b16 a[204:205], v251 offset:1024\n\t"
"v_mfma_f32_16x16x32_bf16 v[164:167], a[32:35], a[148:151], v[164:167]\n\t"
"s_nop 0\n\t"
"ds_read_b128 a[152:155], v224 offset:3072\n\t"
"ds_read_b128 a[168:171], v224 offset:7168\n\t"
"v_mfma_f32_16x16x32_bf16 v[164:167], a[36:39], a[164:167], v[164:167]\n\t"
"buffer_load_dwordx4 a[0:3], v235, s[84:87], s75 offen offset:0 sc1\n\t"
"v_mfma_f32_16x16x32_bf16 v[148:151], a[24:27], a[148:151], v[148:151]\n\t"
"v_mfma_f32_16x16x32_bf16 v[148:151], a[28:31], a[164:167], v[148:151]\n\t"
"s_waitcnt lgkmcnt(0)\n\t"
"s_barrier\n\t"
"v_mfma_f32_16x16x32_bf16 v[152:155], a[24:27], a[152:155], v[152:155]\n\t"
"ds_read_b128 a[144:147], v224 offset:16448\n\t"
"v_add_u32 v248, 53632, v226\n\t"
"v_mfma_f32_16x16x32_bf16 v[152:155], a[28:31], a[168:171], v[152:155]\n\t"
"buffer_load_dwordx4 a[4:7], v235, s[84:87], s75 offen offset:1024 sc1\n\t"
"v_mfma_f32_16x16x32_bf16 v[168:171], a[32:35], a[152:155], v[168:171]\n\t"
"s_nop 0\n\t"
"ds_read_b128 a[160:163], v224 offset:20544\n\t"
"ds_read_b128 a[148:151], v224 offset:16960\n\t"
"v_mfma_f32_16x16x32_bf16 v[168:171], a[36:39], a[168:171], v[168:171]\n\t"
"ds_read_b128 a[164:167], v224 offset:21056\n\t"
"v_xor_b32 v249, 32, v248\n\t"
"v_mfma_f32_16x16x32_bf16 v[184:187], a[40:43], a[152:155], v[184:187]\n\t"
"ds_read_b128 v[104:107], v229 offset:19968\n\t"
"v_xor_b32 v250, 64, v248\n\t"
"v_mfma_f32_16x16x32_bf16 v[184:187], a[44:47], a[168:171], v[184:187]\n\t"
"s_nop 0\n\t"
"ds_read_b128 a[168:171], v224 offset:21568\n\t"
"ds_read_b128 v[120:123], v230 offset:19968\n\t"
"v_mfma_f32_16x16x32_bf16 v[192:195], a[220:223], a[176:179], 0\n\t"
"ds_read_b128 a[152:155], v224 offset:17472\n\t"
"v_xor_b32 v251, 96, v248\n\t"
"v_mfma_f32_16x16x32_bf16 v[188:191], a[40:43], a[156:159], v[188:191]\n\t"
"buffer_load_dwordx4 a[40:43], v252, s[84:87], s78 offen offset:0 sc1\n\t"
"v_mfma_f32_16x16x32_bf16 v[188:191], a[44:47], a[172:175], v[188:191]\n\t"
"ds_read_b64_tr_b16 a[176:177], v248 offset:0\n\t"
"v_mfma_f32_16x16x32_bf16 v[196:199], a[220:223], a[184:187], 0\n\t"
"buffer_load_dwordx4 a[44:47], v252, s[84:87], s78 offen offset:1024 sc1\n\t"
"v_mfma_f32_16x16x32_bf16 v[172:175], a[32:35], a[156:159], v[172:175]\n\t"
"ds_read_b64_tr_b16 a[184:185], v249 offset:0\n\t"
"v_mfma_f32_16x16x32_bf16 v[172:175], a[36:39], a[172:175], v[172:175]\n\t"
"s_waitcnt lgkmcnt(5)\n\t"
"buffer_store_dwordx4 v[104:107], v232, s[16:19], s20 offen nt\n\t"
"v_mfma_f32_16x16x32_bf16 v[156:159], a[24:27], a[156:159], v[156:159]\n\t"
"s_nop 0\n\t"
"ds_read_b128 a[156:159], v224 offset:17984\n\t"
"ds_read_b128 v[136:139], v231 offset:19968\n\t"
"v_mfma_f32_16x16x32_bf16 v[156:159], a[28:31], a[172:175], v[156:159]\n\t"
"ds_read_b128 a[172:175], v224 offset:22080\n\t"
"v_cvt_pk_bf16_f32 v194, v194, v198\n\t"
"v_mfma_f32_16x16x32_bf16 v[200:203], a[220:223], a[178:181], 0\n\t"
"s_waitcnt vmcnt(29)\n\t"
"buffer_load_dwordx4 a[36:39], v235, s[84:87], s78 offen offset:3072 sc1\n\t"
"v_pk_add_f32 v[144:145], v[48:49], v[144:145] neg_lo:[0,1] neg_hi:[0,1]\n\t"
"v_pk_add_f32 v[146:147], v[50:51], v[146:147] neg_lo:[0,1] neg_hi:[0,1]\n\t"
"v_pk_fma_f32 v[48:49], v[144:145], s[90:91], v[48:49] neg_lo:[1,0,0] neg_hi:[1,0,0]\n\t"
"v_pk_fma_f32 v[50:51], v[146:147], s[90:91], v[50:51] neg_lo:[1,0,0] neg_hi:[1,0,0]\n\t"
"v_pk_add_f32 v[160:161], v[64:65], v[160:161] neg_lo:[0,1] neg_hi:[0,1]\n\t"
"v_pk_add_f32 v[162:163], v[66:67], v[162:163] neg_lo:[0,1] neg_hi:[0,1]\n\t"
"v_pk_fma_f32 v[64:65], v[160:161], s[90:91], v[64:65] neg_lo:[1,0,0] neg_hi:[1,0,0]\n\t"
"v_pk_fma_f32 v[66:67], v[162:163], s[90:91], v[66:67] neg_lo:[1,0,0] neg_hi:[1,0,0]\n\t"
"v_pk_add_f32 v[176:177], v[80:81], v[176:177] neg_lo:[0,1] neg_hi:[0,1]\n\t"
"v_pk_add_f32 v[178:179], v[82:83], v[178:179] neg_lo:[0,1] neg_hi:[0,1]\n\t"
"v_pk_fma_f32 v[80:81], v[176:177], s[90:91], v[80:81] neg_lo:[1,0,0] neg_hi:[1,0,0]\n\t"
"v_pk_fma_f32 v[82:83], v[178:179], s[90:91], v[82:83] neg_lo:[1,0,0] neg_hi:[1,0,0]\n\t"
"v_pk_add_f32 v[180:181], v[84:85], v[180:181] neg_lo:[0,1] neg_hi:[0,1]\n\t"
"v_pk_add_f32 v[182:183], v[86:87], v[182:183] neg_lo:[0,1] neg_hi:[0,1]\n\t"
"v_pk_fma_f32 v[84:85], v[180:181], s[90:91], v[84:85] neg_lo:[1,0,0] neg_hi:[1,0,0]\n\t"
"v_pk_fma_f32 v[86:87], v[182:183], s[90:91], v[86:87] neg_lo:[1,0,0] neg_hi:[1,0,0]\n\t"
"v_pk_add_f32 v[164:165], v[68:69], v[164:165] neg_lo:[0,1] neg_hi:[0,1]\n\t"
"v_pk_add_f32 v[166:167], v[70:71], v[166:167] neg_lo:[0,1] neg_hi:[0,1]\n\t"
"v_pk_fma_f32 v[68:69], v[164:165], s[90:91], v[68:69] neg_lo:[1,0,0] neg_hi:[1,0,0]\n\t"
"v_pk_fma_f32 v[70:71], v[166:167], s[90:91], v[70:71] neg_lo:[1,0,0] neg_hi:[1,0,0]\n\t"
"v_pk_add_f32 v[148:149], v[52:53], v[148:149] neg_lo:[0,1] neg_hi:[0,1]\n\t"
"v_pk_add_f32 v[150:151], v[54:55], v[150:151] neg_lo:[0,1] neg_hi:[0,1]\n\t"
"v_pk_fma_f32 v[52:53], v[148:149], s[90:91], v[52:53] neg_lo:[1,0,0] neg_hi:[1,0,0]\n\t"
"v_pk_fma_f32 v[54:55], v[150:151], s[90:91], v[54:55] neg_lo:[1,0,0] neg_hi:[1,0,0]\n\t"
"v_pk_add_f32 v[152:153], v[56:57], v[152:153] neg_lo:[0,1] neg_hi:[0,1]\n\t"
"v_pk_add_f32 v[154:155], v[58:59], v[154:155] neg_lo:[0,1] neg_hi:[0,1]\n\t"
"v_pk_fma_f32 v[56:57], v[152:153], s[90:91], v[56:57] neg_lo:[1,0,0] neg_hi:[1,0,0]\n\t"
"v_pk_fma_f32 v[58:59], v[154:155], s[90:91], v[58:59] neg_lo:[1,0,0] neg_hi:[1,0,0]\n\t"
"v_pk_add_f32 v[168:169], v[72:73], v[168:169] neg_lo:[0,1] neg_hi:[0,1]\n\t"
"v_pk_add_f32 v[170:171], v[74:75], v[170:171] neg_lo:[0,1] neg_hi:[0,1]\n\t"
"v_pk_fma_f32 v[72:73], v[168:169], s[90:91], v[72:73] neg_lo:[1,0,0] neg_hi:[1,0,0]\n\t"
"v_pk_fma_f32 v[74:75], v[170:171], s[90:91], v[74:75] neg_lo:[1,0,0] neg_hi:[1,0,0]\n\t"
"v_pk_add_f32 v[184:185], v[88:89], v[184:185] neg_lo:[0,1] neg_hi:[0,1]\n\t"
"v_pk_add_f32 v[186:187], v[90:91], v[186:187] neg_lo:[0,1] neg_hi:[0,1]\n\t"
"v_pk_fma_f32 v[88:89], v[184:185], s[90:91], v[88:89] neg_lo:[1,0,0] neg_hi:[1,0,0]\n\t"
"v_pk_fma_f32 v[90:91], v[186:187], s[90:91], v[90:91] neg_lo:[1,0,0] neg_hi:[1,0,0]\n\t"
"v_pk_add_f32 v[188:189], v[92:93], v[188:189] neg_lo:[0,1] neg_hi:[0,1]\n\t"
"v_pk_add_f32 v[190:191], v[94:95], v[190:191] neg_lo:[0,1] neg_hi:[0,1]\n\t"
"v_pk_fma_f32 v[92:93], v[188:189], s[90:91], v[92:93] neg_lo:[1,0,0] neg_hi:[1,0,0]\n\t"
"v_pk_fma_f32 v[94:95], v[190:191], s[90:91], v[94:95] neg_lo:[1,0,0] neg_hi:[1,0,0]\n\t"
"v_pk_add_f32 v[172:173], v[76:77], v[172:173] neg_lo:[0,1] neg_hi:[0,1]\n\t"
"v_pk_add_f32 v[174:175], v[78:79], v[174:175] neg_lo:[0,1] neg_hi:[0,1]\n\t"
"v_pk_fma_f32 v[76:77], v[172:173], s[90:91], v[76:77] neg_lo:[1,0,0] neg_hi:[1,0,0]\n\t"
"v_pk_fma_f32 v[78:79], v[174:175], s[90:91], v[78:79] neg_lo:[1,0,0] neg_hi:[1,0,0]\n\t"
"v_pk_add_f32 v[156:157], v[60:61], v[156:157] neg_lo:[0,1] neg_hi:[0,1]\n\t"
"v_pk_add_f32 v[158:159], v[62:63], v[158:159] neg_lo:[0,1] neg_hi:[0,1]\n\t"
"v_pk_fma_f32 v[60:61], v[156:157], s[90:91], v[60:61] neg_lo:[1,0,0] neg_hi:[1,0,0]\n\t"
"v_pk_fma_f32 v[62:63], v[158:159], s[90:91], v[62:63] neg_lo:[1,0,0] neg_hi:[1,0,0]\n\t"
"v_mfma_f32_16x16x32_bf16 v[48:51], a[48:51], a[144:147], v[48:51]\n\t"
"ds_read_b64_tr_b16 a[178:179], v248 offset:512\n\t"
"v_cvt_pk_bf16_f32 v192, v192, v196\n\t"
"v_mfma_f32_16x16x32_bf16 v[48:51], a[52:55], a[160:163], v[48:51]\n\t"
"buffer_load_dwordx4 a[32:35], v235, s[84:87], s78 offen offset:2048 sc1\n\t"
"v_mfma_f32_16x16x32_bf16 v[204:207], a[220:223], a[186:189], 0\n\t"
"ds_read_b64_tr_b16 a[180:181], v248 offset:1024\n\t"
"v_cvt_pk_bf16_f32 v193, v193, v197\n\t"
"v_mfma_f32_16x16x32_bf16 v[64:67], a[56:59], a[144:147], v[64:67]\n\t"
"s_nop 0\n\t"
"ds_read_b64_tr_b16 a[186:187], v249 offset:512\n\t"
"ds_read_b64_tr_b16 a[188:189], v249 offset:1024\n\t"
"v_mfma_f32_16x16x32_bf16 v[64:67], a[60:63], a[160:163], v[64:67]\n\t"
"buffer_load_dwordx4 a[24:27], v235, s[84:87], s78 offen offset:0 sc1\n\t"
"v_mfma_f32_16x16x32_bf16 v[80:83], a[64:67], a[144:147], v[80:83]\n\t"
"ds_write_addtid_b32 v193 offset:8224\n\t"
"v_cvt_pk_bf16_f32 v195, v195, v199\n\t"
"v_mfma_f32_16x16x32_bf16 v[80:83], a[68:71], a[160:163], v[80:83]\n\t"
"buffer_load_dwordx4 a[28:31], v235, s[84:87], s78 offen offset:1024 sc1\n\t"
"v_mfma_f32_16x16x32_bf16 v[208:211], a[220:223], a[192:195], 0\n\t"
"ds_write_addtid_b32 v195 offset:10272\n\t"
"v_mfma_f32_16x16x32_bf16 v[84:87], a[64:67], a[148:151], v[84:87]\n\t"
"ds_read_b128 v[108:111], v229 offset:33280\n\t"
"v_cvt_pk_bf16_f32 v203, v203, v207\n\t"
"v_mfma_f32_16x16x32_bf16 v[84:87], a[68:71], a[164:167], v[84:87]\n\t"
"s_waitcnt lgkmcnt(7)\n\t"
"buffer_store_dwordx4 v[120:123], v233, s[16:19], s20 offen nt\n\t"
"v_mfma_f32_16x16x32_bf16 v[68:71], a[56:59], a[148:151], v[68:71]\n\t"
"ds_read_b128 a[144:147], v224 offset:18496\n\t"
"v_cvt_pk_bf16_f32 v201, v201, v205\n\t"
"v_mfma_f32_16x16x32_bf16 v[68:71], a[60:63], a[164:167], v[68:71]\n\t"
"s_nop 0\n\t"
"ds_read_b128 a[160:163], v224 offset:22592\n\t"
"ds_write_addtid_b32 v192 offset:0\n\t"
"v_mfma_f32_16x16x32_bf16 v[212:215], a[220:223], a[200:203], 0\n\t"
"buffer_store_dwordx4 v[136:139], v234, s[16:19], s20 offen nt\n\t"
"v_mfma_f32_16x16x32_bf16 v[52:55], a[48:51], a[148:151], v[52:55]\n\t"
"ds_write_addtid_b32 v194 offset:2048\n\t"
"v_cvt_pk_bf16_f32 v200, v200, v204\n\t"
"v_mfma_f32_16x16x32_bf16 v[52:55], a[52:55], a[164:167], v[52:55]\n\t"
"s_nop 0\n\t"
"ds_write_addtid_b32 v203 offset:10528\n\t"
"ds_read_b128 a[232:235], v230 offset:33280\n\t"
"v_mfma_f32_16x16x32_bf16 v[216:219], a[220:223], a[194:197], 0\n\t"
"s_nop 0\n\t"
"ds_read_b128 a[148:151], v224 offset:19008\n\t"
"ds_read_b64_tr_b16 a[192:193], v250 offset:0\n\t"
"v_mfma_f32_16x16x32_bf16 v[56:59], a[48:51], a[152:155], v[56:59]\n\t"
"ds_read_b128 a[164:167], v224 offset:23104\n\t"
"v_cvt_pk_bf16_f32 v210, v210, v214\n\t"
"v_mfma_f32_16x16x32_bf16 v[56:59], a[52:55], a[168:171], v[56:59]\n\t"
"s_waitcnt lgkmcnt(9)\n\t"
"buffer_store_dwordx4 v[108:111], v232, s[16:19], s25 offen nt\n\t"
"v_mfma_f32_16x16x32_bf16 v[72:75], a[56:59], a[152:155], v[72:75]\n\t"
"ds_read_b64_tr_b16 a[200:201], v251 offset:0\n\t"
"v_cvt_pk_bf16_f32 v211, v211, v215\n\t"
"v_mfma_f32_16x16x32_bf16 v[72:75], a[60:63], a[168:171], v[72:75]\n\t"
"ds_write_addtid_b32 v211 offset:14368\n\t"
"v_cvt_pk_bf16_f32 v209, v209, v213\n\t"
"v_mfma_f32_16x16x32_bf16 v[220:223], a[220:223], a[202:205], 0\n\t"
"ds_write_addtid_b32 v200 offset:256\n\t"
"v_cvt_pk_bf16_f32 v202, v202, v206\n\t"
"v_mfma_f32_16x16x32_bf16 v[88:91], a[64:67], a[152:155], v[88:91]\n\t"
"s_nop 0\n\t"
"ds_write_addtid_b32 v201 offset:8480\n\t"
"ds_write_addtid_b32 v202 offset:2304\n\t"
"v_mfma_f32_16x16x32_bf16 v[88:91], a[68:71], a[168:171], v[88:91]\n\t"
"ds_read_b128 a[236:239], v231 offset:33280\n\t"
"v_cvt_pk_bf16_f32 v208, v208, v212\n\t"
"v_mfma_f32_16x16x32_bf16 v[92:95], a[64:67], a[156:159], v[92:95]\n\t"
"buffer_load_dwordx4 a[64:67], v252, s[84:87], s54 offen offset:0 sc1\n\t"
"v_mfma_f32_16x16x32_bf16 v[92:95], a[68:71], a[172:175], v[92:95]\n\t"
"ds_write_addtid_b32 v208 offset:4096\n\t"
"v_cvt_pk_bf16_f32 v219, v219, v223\n\t"
"v_mfma_f32_16x16x32_bf16 v[76:79], a[56:59], a[156:159], v[76:79]\n\t"
"ds_write_addtid_b32 v210 offset:6144\n\t"
"v_cvt_pk_bf16_f32 v217, v217, v221\n\t"
"v_mfma_f32_16x16x32_bf16 v[76:79], a[60:63], a[172:175], v[76:79]\n\t"
"s_waitcnt lgkmcnt(8)\n\t"
"buffer_store_dwordx4 a[232:235], v233, s[16:19], s25 offen nt\n\t"
"v_mfma_f32_16x16x32_bf16 v[60:63], a[48:51], a[156:159], v[60:63]\n\t"
"ds_read_b64_tr_b16 a[194:195], v250 offset:512\n\t"
"v_cvt_pk_bf16_f32 v216, v216, v220\n\t"
"v_mfma_f32_16x16x32_bf16 v[60:63], a[52:55], a[172:175], v[60:63]\n\t"
"s_waitcnt vmcnt(30) lgkmcnt(8)\n\t"
"ds_write_addtid_b32 v217 offset:12576\n\t"
"v_cvt_pk_bf16_f32 v218, v218, v222\n\t"
"v_mfma_f32_16x16x32_bf16 v[144:147], a[72:75], a[144:147], v[144:147]\n\t"
"s_nop 0\n\t"
"ds_write_addtid_b32 v216 offset:4352\n\t"
"ds_write_addtid_b32 v218 offset:6400\n\t"
"v_mfma_f32_16x16x32_bf16 v[144:147], a[76:79], a[160:163], v[144:147]\n\t"
"buffer_load_dwordx4 a[68:71], v252, s[84:87], s54 offen offset:1024 sc1\n\t"
"buffer_load_dwordx4 a[60:63], v235, s[84:87], s54 offen offset:3072 sc1\n\t"
"buffer_load_dwordx4 a[56:59], v235, s[84:87], s54 offen offset:2048 sc1\n\t"
"v_mfma_f32_16x16x32_bf16 v[160:163], a[80:83], a[144:147], v[160:163]\n\t"
"s_nop 0\n\t"
"ds_write_addtid_b32 v209 offset:12320\n\t"
"ds_read_b64_tr_b16 a[196:197], v250 offset:1024\n\t"
"v_mfma_f32_16x16x32_bf16 v[160:163], a[84:87], a[160:163], v[160:163]\n\t"
"buffer_load_dwordx4 a[48:51], v235, s[84:87], s54 offen offset:0 sc1\n\t"
"v_mfma_f32_16x16x32_bf16 v[176:179], a[88:91], a[144:147], v[176:179]\n\t"
"s_nop 0\n\t"
"ds_read_b128 a[156:159], v224 offset:20032\n\t"
"ds_write_addtid_b32 v219 offset:14624\n\t"
"v_mfma_f32_16x16x32_bf16 v[176:179], a[92:95], a[160:163], v[176:179]\n\t"
"buffer_load_dwordx4 a[52:55], v235, s[84:87], s54 offen offset:1024 sc1\n\t"
"v_mfma_f32_16x16x32_bf16 v[180:183], a[88:91], a[148:151], v[180:183]\n\t"
"ds_read_b64_tr_b16 a[202:203], v251 offset:512\n\t"
"v_mfma_f32_16x16x32_bf16 v[180:183], a[92:95], a[164:167], v[180:183]\n\t"
"s_nop 0\n\t"
"ds_read_b128 a[172:175], v224 offset:24128\n\t"
"ds_read_b64_tr_b16 a[204:205], v251 offset:1024\n\t"
"v_mfma_f32_16x16x32_bf16 v[164:167], a[80:83], a[148:151], v[164:167]\n\t"
"s_nop 0\n\t"
"ds_read_b128 a[152:155], v224 offset:19520\n\t"
"ds_read_b128 a[168:171], v224 offset:23616\n\t"
"v_mfma_f32_16x16x32_bf16 v[164:167], a[84:87], a[164:167], v[164:167]\n\t"
"s_waitcnt lgkmcnt(11)\n\t"
"buffer_store_dwordx4 a[236:239], v234, s[16:19], s25 offen nt\n\t"
"v_mfma_f32_16x16x32_bf16 v[148:151], a[72:75], a[148:151], v[148:151]\n\t"
"v_mfma_f32_16x16x32_bf16 v[148:151], a[76:79], a[164:167], v[148:151]\n\t"
"s_waitcnt lgkmcnt(0)\n\t"
"s_barrier\n\t"
"v_mfma_f32_16x16x32_bf16 v[152:155], a[72:75], a[152:155], v[152:155]\n\t"
"ds_read_b128 a[144:147], v224 offset:0\n\t"
"v_add_u32 v248, 101632, v227\n\t"
"v_mfma_f32_16x16x32_bf16 v[152:155], a[76:79], a[168:171], v[152:155]\n\t"
"ds_read_b128 a[148:151], v224 offset:512\n\t"
"v_xor_b32 v249, 32, v248\n\t"
"v_mfma_f32_16x16x32_bf16 v[168:171], a[80:83], a[152:155], v[168:171]\n\t"
"ds_read_b128 a[160:163], v224 offset:4096\n\t"
"v_xor_b32 v250, 64, v248\n\t"
"v_mfma_f32_16x16x32_bf16 v[168:171], a[84:87], a[168:171], v[168:171]\n\t"
"ds_read_b128 a[164:167], v224 offset:4608\n\t"
"v_xor_b32 v251, 96, v248\n\t"
"v_mfma_f32_16x16x32_bf16 v[184:187], a[88:91], a[152:155], v[184:187]\n\t"
"s_nop 0\n\t"
"ds_read_b64_tr_b16 a[182:183], v249 offset:41984\n\t"
"ds_read_b64_tr_b16 a[190:191], v251 offset:41984\n\t"
"v_mfma_f32_16x16x32_bf16 v[184:187], a[92:95], a[168:171], v[184:187]\n\t"
"ds_read_b128 a[152:155], v224 offset:1024\n\t"
"v_mfma_f32_16x16x32_bf16 v[192:195], a[220:223], a[176:179], 0\n\t"
"ds_read_b64_tr_b16 a[198:199], v249 offset:512\n\t"
"v_mfma_f32_16x16x32_bf16 v[188:191], a[88:91], a[156:159], v[188:191]\n\t"
"buffer_load_dwordx4 a[88:91], v252, s[84:87], 0 offen offset:0 sc1\n\t"
"v_mfma_f32_16x16x32_bf16 v[188:191], a[92:95], a[172:175], v[188:191]\n\t"
"ds_read_b128 a[168:171], v224 offset:5120\n\t"
"v_mfma_f32_16x16x32_bf16 v[196:199], a[220:223], a[184:187], 0\n\t"
"buffer_load_dwordx4 a[92:95], v252, s[84:87], 0 offen offset:1024 sc1\n\t"
"v_mfma_f32_16x16x32_bf16 v[172:175], a[80:83], a[156:159], v[172:175]\n\t"
"ds_read_b64_tr_b16 a[206:207], v251 offset:512\n\t"
"v_mfma_f32_16x16x32_bf16 v[172:175], a[84:87], a[172:175], v[172:175]\n\t"
"buffer_load_dwordx4 a[84:87], v235, s[84:87], 0 offen offset:3072 sc1\n\t"
"v_mfma_f32_16x16x32_bf16 v[156:159], a[72:75], a[156:159], v[156:159]\n\t"
"ds_read_b128 a[156:159], v224 offset:1536\n\t"
"v_mfma_f32_16x16x32_bf16 v[156:159], a[76:79], a[172:175], v[156:159]\n\t"
"buffer_load_dwordx4 a[80:83], v235, s[84:87], 0 offen offset:2048 sc1\n\t"
"v_mfma_f32_16x16x32_bf16 v[200:203], a[220:223], a[178:181], 0\n\t"
"s_waitcnt vmcnt(34) lgkmcnt(5)\n\t"
"ds_read_b128 a[172:175], v224 offset:5632\n\t"
"v_mfma_f32_16x16x32_bf16 v[48:51], a[96:99], a[144:147], v[48:51]\n\t"
"ds_read_b64_tr_b16 a[176:177], v248 offset:41472\n\t"
"v_mfma_f32_16x16x32_bf16 v[48:51], a[100:103], a[160:163], v[48:51]\n\t"
"buffer_load_dwordx4 a[72:75], v235, s[84:87], 0 offen offset:0 sc1\n\t"
"v_mfma_f32_16x16x32_bf16 v[204:207], a[220:223], a[186:189], 0\n\t"
"ds_read_b64_tr_b16 a[184:185], v250 offset:41472\n\t"
"v_cvt_pk_bf16_f32 v195, v195, v199\n\t"
"v_mfma_f32_16x16x32_bf16 v[64:67], a[104:107], a[144:147], v[64:67]\n\t"
"ds_read_b64_tr_b16 a[178:179], v248 offset:41984\n\t"
"v_cvt_pk_bf16_f32 v193, v193, v197\n\t"
"v_mfma_f32_16x16x32_bf16 v[64:67], a[108:111], a[160:163], v[64:67]\n\t"
"buffer_load_dwordx4 a[76:79], v235, s[84:87], 0 offen offset:1024 sc1\n\t"
"v_mfma_f32_16x16x32_bf16 v[80:83], a[112:115], a[144:147], v[80:83]\n\t"
"ds_read_b64_tr_b16 a[180:181], v249 offset:41472\n\t"
"v_cvt_pk_bf16_f32 v192, v192, v196\n\t"
"v_mfma_f32_16x16x32_bf16 v[80:83], a[116:119], a[160:163], v[80:83]\n\t"
"ds_read_b64_tr_b16 a[186:187], v250 offset:41984\n\t"
"v_cvt_pk_bf16_f32 v194, v194, v198\n\t"
"v_mfma_f32_16x16x32_bf16 v[208:211], a[220:223], a[192:195], 0\n\t"
"ds_write_addtid_b32 v194 offset:18496\n\t"
"v_cvt_pk_bf16_f32 v200, v200, v204\n\t"
"v_mfma_f32_16x16x32_bf16 v[84:87], a[112:115], a[148:151], v[84:87]\n\t"
"ds_write_addtid_b32 v195 offset:26720\n\t"
"v_mfma_f32_16x16x32_bf16 v[84:87], a[116:119], a[164:167], v[84:87]\n\t"
"ds_read_b128 a[144:147], v224 offset:2048\n\t"
"v_mfma_f32_16x16x32_bf16 v[68:71], a[104:107], a[148:151], v[68:71]\n\t"
"ds_read_b64_tr_b16 a[188:189], v251 offset:41472\n\t"
"v_cvt_pk_bf16_f32 v202, v202, v206\n\t"
"v_mfma_f32_16x16x32_bf16 v[68:71], a[108:111], a[164:167], v[68:71]\n\t"
"s_nop 0\n\t"
"ds_write_addtid_b32 v193 offset:24672\n\t"
"ds_read_b64_tr_b16 a[192:193], v248 offset:0\n\t"
"v_mfma_f32_16x16x32_bf16 v[212:215], a[220:223], a[200:203], 0\n\t"
"ds_write_addtid_b32 v192 offset:16448\n\t"
"v_mfma_f32_16x16x32_bf16 v[52:55], a[96:99], a[148:151], v[52:55]\n\t"
"s_nop 0\n\t"
"ds_read_b128 a[148:151], v224 offset:2560\n\t"
"ds_write_addtid_b32 v202 offset:18752\n\t"
"v_mfma_f32_16x16x32_bf16 v[52:55], a[100:103], a[164:167], v[52:55]\n\t"
"ds_read_b128 a[160:163], v224 offset:6144\n\t"
"v_mfma_f32_16x16x32_bf16 v[216:219], a[220:223], a[194:197], 0\n\t"
"s_waitcnt lgkmcnt(11)\n\t"
"ds_write_addtid_b32 v200 offset:16704\n\t"
"v_cvt_pk_bf16_f32 v203, v203, v207\n\t"
"v_mfma_f32_16x16x32_bf16 v[56:59], a[96:99], a[152:155], v[56:59]\n\t"
"ds_read_b64_tr_b16 a[200:201], v250 offset:0\n\t"
"v_cvt_pk_bf16_f32 v211, v211, v215\n\t"
"v_mfma_f32_16x16x32_bf16 v[56:59], a[100:103], a[168:171], v[56:59]\n\t"
"ds_read_b128 a[164:167], v224 offset:6656\n\t"
"v_cvt_pk_bf16_f32 v208, v208, v212\n\t"
"v_mfma_f32_16x16x32_bf16 v[72:75], a[104:107], a[152:155], v[72:75]\n\t"
"ds_read_b64_tr_b16 a[194:195], v248 offset:512\n\t"
"v_cvt_pk_bf16_f32 v201, v201, v205\n\t"
"v_mfma_f32_16x16x32_bf16 v[72:75], a[108:111], a[168:171], v[72:75]\n\t"
"s_nop 0\n\t"
"ds_write_addtid_b32 v203 offset:26976\n\t"
"ds_read_b64_tr_b16 a[196:197], v249 offset:0\n\t"
"v_mfma_f32_16x16x32_bf16 v[220:223], a[220:223], a[202:205], 0\n\t"
"ds_read_b64_tr_b16 a[202:203], v250 offset:512\n\t"
"v_cvt_pk_bf16_f32 v210, v210, v214\n\t"
"v_mfma_f32_16x16x32_bf16 v[88:91], a[112:115], a[152:155], v[88:91]\n\t"
"ds_write_addtid_b32 v201 offset:24928\n\t"
"v_cvt_pk_bf16_f32 v209, v209, v213\n\t"
"v_mfma_f32_16x16x32_bf16 v[88:91], a[116:119], a[168:171], v[88:91]\n\t"
"ds_write_addtid_b32 v209 offset:28768\n\t"
"v_mfma_f32_16x16x32_bf16 v[92:95], a[112:115], a[156:159], v[92:95]\n\t"
"s_nop 0\n\t"
"ds_read_b128 a[152:155], v224 offset:3072\n\t"
"ds_write_addtid_b32 v210 offset:22592\n\t"
"v_mfma_f32_16x16x32_bf16 v[92:95], a[116:119], a[172:175], v[92:95]\n\t"
"buffer_load_dwordx4 a[112:115], v252, s[84:87], s66 offen offset:0 sc1\n\t"
"v_mfma_f32_16x16x32_bf16 v[76:79], a[104:107], a[156:159], v[76:79]\n\t"
"ds_write_addtid_b32 v208 offset:20544\n\t"
"v_cvt_pk_bf16_f32 v217, v217, v221\n\t"
"v_mfma_f32_16x16x32_bf16 v[76:79], a[108:111], a[172:175], v[76:79]\n\t"
"buffer_load_dwordx4 a[116:119], v252, s[84:87], s66 offen offset:1024 sc1\n\t"
"v_mfma_f32_16x16x32_bf16 v[60:63], a[96:99], a[156:159], v[60:63]\n\t"
"ds_write_addtid_b32 v217 offset:29024\n\t"
"v_cvt_pk_bf16_f32 v218, v218, v222\n\t"
"v_mfma_f32_16x16x32_bf16 v[60:63], a[100:103], a[172:175], v[60:63]\n\t"
"s_waitcnt vmcnt(32) lgkmcnt(9)\n\t"
"buffer_load_dwordx4 a[108:111], v235, s[84:87], s66 offen offset:3072 sc1\n\t"
"v_mfma_f32_16x16x32_bf16 v[144:147], a[120:123], a[144:147], v[144:147]\n\t"
"ds_write_addtid_b32 v218 offset:22848\n\t"
"v_cvt_pk_bf16_f32 v219, v219, v223\n\t"
"v_mfma_f32_16x16x32_bf16 v[144:147], a[124:127], a[160:163], v[144:147]\n\t"
"buffer_load_dwordx4 a[104:107], v235, s[84:87], s66 offen offset:2048 sc1\n\t"
"v_mfma_f32_16x16x32_bf16 v[160:163], a[128:131], a[144:147], v[160:163]\n\t"
"ds_write_addtid_b32 v211 offset:30816\n\t"
"v_cvt_pk_bf16_f32 v216, v216, v220\n\t"
"v_mfma_f32_16x16x32_bf16 v[160:163], a[132:135], a[160:163], v[160:163]\n\t"
"buffer_load_dwordx4 a[96:99], v235, s[84:87], s66 offen offset:0 sc1\n\t"
"v_mfma_f32_16x16x32_bf16 v[176:179], a[136:139], a[144:147], v[176:179]\n\t"
"s_nop 0\n\t"
"ds_write_addtid_b32 v216 offset:20800\n\t"
"ds_write_addtid_b32 v219 offset:31072\n\t"
"v_mfma_f32_16x16x32_bf16 v[176:179], a[140:143], a[160:163], v[176:179]\n\t"
"buffer_load_dwordx4 a[100:103], v235, s[84:87], s66 offen offset:1024 sc1\n\t"
"v_mfma_f32_16x16x32_bf16 v[180:183], a[136:139], a[148:151], v[180:183]\n\t"
"s_nop 0\n\t"
"ds_read_b128 a[172:175], v224 offset:7680\n\t"
"ds_read_b64_tr_b16 a[204:205], v251 offset:0\n\t"
"v_mfma_f32_16x16x32_bf16 v[180:183], a[140:143], a[164:167], v[180:183]\n\t"
"ds_read_b128 a[156:159], v224 offset:3584\n\t"
"v_mfma_f32_16x16x32_bf16 v[164:167], a[128:131], a[148:151], v[164:167]\n\t"
"ds_read_b128 a[168:171], v224 offset:7168\n\t"
"v_mfma_f32_16x16x32_bf16 v[164:167], a[132:135], a[164:167], v[164:167]\n\t"
"v_mfma_f32_16x16x32_bf16 v[148:151], a[120:123], a[148:151], v[148:151]\n\t"
"v_mfma_f32_16x16x32_bf16 v[148:151], a[124:127], a[164:167], v[148:151]\n\t"
"s_waitcnt lgkmcnt(0)\n\t"
"s_barrier\n\t"
"v_mfma_f32_16x16x32_bf16 v[152:155], a[120:123], a[152:155], v[152:155]\n\t"
"ds_read_b128 a[160:163], v224 offset:20544\n\t"
"v_mfma_f32_16x16x32_bf16 v[152:155], a[124:127], a[168:171], v[152:155]\n\t"
"ds_read_b128 a[144:147], v224 offset:16448\n\t"
"v_mfma_f32_16x16x32_bf16 v[168:171], a[128:131], a[152:155], v[168:171]\n\t"
"ds_read_b128 a[148:151], v224 offset:16960\n\t"
"v_mfma_f32_16x16x32_bf16 v[168:171], a[132:135], a[168:171], v[168:171]\n\t"
"ds_read_b128 a[164:167], v224 offset:21056\n\t"
"v_mfma_f32_16x16x32_bf16 v[184:187], a[136:139], a[152:155], v[184:187]\n\t"
"v_mfma_f32_16x16x32_bf16 v[184:187], a[140:143], a[168:171], v[184:187]\n\t"
"ds_read_b128 a[168:171], v224 offset:21568\n\t"
"v_mfma_f32_16x16x32_bf16 v[192:195], a[224:227], a[176:179], 0\n\t"
"ds_read_b64_tr_b16 a[176:177], v248 offset:42624\n\t"
"v_mfma_f32_16x16x32_bf16 v[188:191], a[136:139], a[156:159], v[188:191]\n\t"
"buffer_load_dwordx4 a[136:139], v252, s[84:87], s51 offen offset:0 sc1\n\t"
"v_mfma_f32_16x16x32_bf16 v[188:191], a[140:143], a[172:175], v[188:191]\n\t"
"v_mfma_f32_16x16x32_bf16 v[196:199], a[224:227], a[180:183], 0\n\t"
"buffer_load_dwordx4 a[140:143], v252, s[84:87], s51 offen offset:1024 sc1\n\t"
"v_mfma_f32_16x16x32_bf16 v[172:175], a[128:131], a[156:159], v[172:175]\n\t"
"ds_read_b128 a[152:155], v224 offset:17472\n\t"
"v_mfma_f32_16x16x32_bf16 v[172:175], a[132:135], a[172:175], v[172:175]\n\t"
"buffer_load_dwordx4 a[132:135], v235, s[84:87], s51 offen offset:3072 sc1\n\t"
"v_mfma_f32_16x16x32_bf16 v[156:159], a[120:123], a[156:159], v[156:159]\n\t"
"ds_read_b64_tr_b16 a[178:179], v248 offset:43136\n\t"
"v_mfma_f32_16x16x32_bf16 v[156:159], a[124:127], a[172:175], v[156:159]\n\t"
"buffer_load_dwordx4 a[128:131], v235, s[84:87], s51 offen offset:2048 sc1\n\t"
"v_mfma_f32_16x16x32_bf16 v[200:203], a[224:227], a[184:187], 0\n\t"
"s_waitcnt vmcnt(34) lgkmcnt(4)\n\t"
"ds_read_b64_tr_b16 a[180:181], v249 offset:42624\n\t"
"v_cvt_pk_bf16_f32 v194, v194, v198\n\t"
"v_mfma_f32_16x16x32_bf16 v[48:51], a[0:3], a[144:147], v[48:51]\n\t"
"ds_read_b128 a[156:159], v224 offset:17984\n\t"
"v_cvt_pk_bf16_f32 v193, v193, v197\n\t"
"v_mfma_f32_16x16x32_bf16 v[48:51], a[4:7], a[160:163], v[48:51]\n\t"
"buffer_load_dwordx4 a[120:123], v235, s[84:87], s51 offen offset:0 sc1\n\t"
"v_mfma_f32_16x16x32_bf16 v[204:207], a[224:227], a[188:191], 0\n\t"
"ds_read_b128 a[172:175], v224 offset:22080\n\t"
"v_cvt_pk_bf16_f32 v192, v192, v196\n\t"
"v_mfma_f32_16x16x32_bf16 v[64:67], a[8:11], a[144:147], v[64:67]\n\t"
"s_xor_b32 s24, s24, 384\n\t"
"ds_write_addtid_b32 v194 offset:8224\n\t"
"ds_read_b64_tr_b16 a[182:183], v249 offset:43136\n\t"
"v_mfma_f32_16x16x32_bf16 v[64:67], a[12:15], a[160:163], v[64:67]\n\t"
"buffer_load_dwordx4 a[124:127], v235, s[84:87], s51 offen offset:1024 sc1\n\t"
"v_mfma_f32_16x16x32_bf16 v[80:83], a[16:19], a[144:147], v[80:83]\n\t"
"ds_read_b64_tr_b16 a[184:185], v250 offset:42624\n\t"
"v_mfma_f32_16x16x32_bf16 v[80:83], a[20:23], a[160:163], v[80:83]\n\t"
"s_xor_b32 s20, s20, 384\n\t"
"ds_read_b128 a[144:147], v224 offset:18496\n\t"
"ds_read_b64_tr_b16 a[186:187], v250 offset:43136\n\t"
"v_mfma_f32_16x16x32_bf16 v[208:211], a[224:227], a[192:195], 0\n\t"
"ds_read_b64_tr_b16 a[188:189], v251 offset:42624\n\t"
"v_mfma_f32_16x16x32_bf16 v[84:87], a[16:19], a[148:151], v[84:87]\n\t"
"ds_write_addtid_b32 v193 offset:256\n\t"
"v_cvt_pk_bf16_f32 v195, v195, v199\n\t"
"v_mfma_f32_16x16x32_bf16 v[84:87], a[20:23], a[164:167], v[84:87]\n\t"
"ds_write_addtid_b32 v195 offset:8480\n\t"
"v_cvt_pk_bf16_f32 v202, v202, v206\n\t"
"v_mfma_f32_16x16x32_bf16 v[68:71], a[8:11], a[148:151], v[68:71]\n\t"
"ds_write_addtid_b32 v192 offset:0\n\t"
"v_cvt_pk_bf16_f32 v200, v200, v204\n\t"
"v_mfma_f32_16x16x32_bf16 v[68:71], a[12:15], a[164:167], v[68:71]\n\t"
"ds_read_b64_tr_b16 a[190:191], v251 offset:43136\n\t"
"v_cvt_pk_bf16_f32 v203, v203, v207\n\t"
"v_mfma_f32_16x16x32_bf16 v[212:215], a[224:227], a[196:199], 0\n\t"
"ds_read_b128 a[160:163], v224 offset:22592\n\t"
"v_mfma_f32_16x16x32_bf16 v[52:55], a[0:3], a[148:151], v[52:55]\n\t"
"ds_read_b128 a[148:151], v224 offset:19008\n\t"
"v_mfma_f32_16x16x32_bf16 v[52:55], a[4:7], a[164:167], v[52:55]\n\t"
"ds_write_addtid_b32 v202 offset:12320\n\t"
"v_mfma_f32_16x16x32_bf16 v[216:219], a[224:227], a[200:203], 0\n\t"
"s_waitcnt lgkmcnt(10)\n\t"
"ds_write_addtid_b32 v203 offset:12576\n\t"
"v_mfma_f32_16x16x32_bf16 v[56:59], a[0:3], a[152:155], v[56:59]\n\t"
"ds_read_b64_tr_b16 a[192:193], v248 offset:1152\n\t"
"v_cvt_pk_bf16_f32 v201, v201, v205\n\t"
"v_mfma_f32_16x16x32_bf16 v[56:59], a[4:7], a[168:171], v[56:59]\n\t"
"ds_read_b64_tr_b16 a[194:195], v248 offset:1664\n\t"
"v_cvt_pk_bf16_f32 v208, v208, v212\n\t"
"v_mfma_f32_16x16x32_bf16 v[72:75], a[8:11], a[152:155], v[72:75]\n\t"
"ds_write_addtid_b32 v200 offset:4096\n\t"
"v_cvt_pk_bf16_f32 v211, v211, v215\n\t"
"v_mfma_f32_16x16x32_bf16 v[72:75], a[12:15], a[168:171], v[72:75]\n\t"
"ds_read_b64_tr_b16 a[196:197], v249 offset:1152\n\t"
"v_mfma_f32_16x16x32_bf16 v[220:223], a[224:227], a[204:207], 0\n\t"
"ds_read_b128 a[164:167], v224 offset:23104\n\t"
"v_cvt_pk_bf16_f32 v209, v209, v213\n\t"
"v_mfma_f32_16x16x32_bf16 v[88:91], a[16:19], a[152:155], v[88:91]\n\t"
"ds_write_addtid_b32 v201 offset:4352\n\t"
"v_mfma_f32_16x16x32_bf16 v[88:91], a[20:23], a[168:171], v[88:91]\n\t"
"ds_read_b64_tr_b16 a[198:199], v249 offset:1664\n\t"
"v_mfma_f32_16x16x32_bf16 v[92:95], a[16:19], a[156:159], v[92:95]\n\t"
"ds_write_addtid_b32 v211 offset:10528\n\t"
"v_mfma_f32_16x16x32_bf16 v[92:95], a[20:23], a[172:175], v[92:95]\n\t"
"ds_read_b64_tr_b16 a[200:201], v250 offset:1152\n\t"
"v_cvt_pk_bf16_f32 v216, v216, v220\n\t"
"v_mfma_f32_16x16x32_bf16 v[76:79], a[8:11], a[156:159], v[76:79]\n\t"
"v_cvt_pk_bf16_f32 v218, v218, v222\n\t"
"v_cvt_pk_bf16_f32 v219, v219, v223\n\t"
"v_mfma_f32_16x16x32_bf16 v[76:79], a[12:15], a[172:175], v[76:79]\n\t"
"ds_write_addtid_b32 v218 offset:14368\n\t"
"v_mfma_f32_16x16x32_bf16 v[60:63], a[0:3], a[156:159], v[60:63]\n\t"
"ds_write_addtid_b32 v208 offset:2048\n\t"
"v_cvt_pk_bf16_f32 v210, v210, v214\n\t"
"v_mfma_f32_16x16x32_bf16 v[60:63], a[4:7], a[172:175], v[60:63]\n\t"
"s_waitcnt vmcnt(29) lgkmcnt(8)\n\t"
"ds_write_addtid_b32 v210 offset:10272\n\t"
"ds_read_b64_tr_b16 a[202:203], v250 offset:1664\n\t"
"s_xor_b32 s27, s27, 384\n\t"
"v_mfma_f32_16x16x32_bf16 v[144:147], a[24:27], a[144:147], v[144:147]\n\t"
"ds_write_addtid_b32 v209 offset:2304\n\t"
"v_mfma_f32_16x16x32_bf16 v[144:147], a[28:31], a[160:163], v[144:147]\n\t"
"ds_read_b64_tr_b16 a[204:205], v251 offset:1152\n\t"
"v_mfma_f32_16x16x32_bf16 v[160:163], a[32:35], a[144:147], v[160:163]\n\t"
"ds_read_b64_tr_b16 a[206:207], v251 offset:1664\n\t"
"v_mfma_f32_16x16x32_bf16 v[160:163], a[36:39], a[160:163], v[160:163]\n\t"
"ds_write_addtid_b32 v219 offset:14624\n\t"
"v_cvt_pk_bf16_f32 v217, v217, v221\n\t"
"v_mfma_f32_16x16x32_bf16 v[176:179], a[40:43], a[144:147], v[176:179]\n\t"
"ds_write_addtid_b32 v217 offset:6400\n\t"
"v_mfma_f32_16x16x32_bf16 v[176:179], a[44:47], a[160:163], v[176:179]\n\t"
"s_waitcnt lgkmcnt(9)\n\t"
"ds_write_addtid_b32 v216 offset:6144\n\t"
"v_mfma_f32_16x16x32_bf16 v[180:183], a[40:43], a[148:151], v[180:183]\n\t"
"s_xor_b32 s25, s25, 384\n\t"
"ds_read_b128 a[152:155], v224 offset:19520\n\t"
"ds_read_b128 a[168:171], v224 offset:23616\n\t"
"v_mfma_f32_16x16x32_bf16 v[180:183], a[44:47], a[164:167], v[180:183]\n\t"
"ds_read_b128 a[172:175], v224 offset:24128\n\t"
"v_mfma_f32_16x16x32_bf16 v[164:167], a[32:35], a[148:151], v[164:167]\n\t"
"ds_read_b128 a[156:159], v224 offset:20032\n\t"
"v_mfma_f32_16x16x32_bf16 v[164:167], a[36:39], a[164:167], v[164:167]\n\t"
"v_mfma_f32_16x16x32_bf16 v[148:151], a[24:27], a[148:151], v[148:151]\n\t"
"v_mfma_f32_16x16x32_bf16 v[148:151], a[28:31], a[164:167], v[148:151]\n\t"
"s_waitcnt lgkmcnt(0)\n\t"
"s_barrier\n\t"
"v_mfma_f32_16x16x32_bf16 v[152:155], a[24:27], a[152:155], v[152:155]\n\t"
"ds_read_b128 a[160:163], v224 offset:4096\n\t"
"v_mfma_f32_16x16x32_bf16 v[152:155], a[28:31], a[168:171], v[152:155]\n\t"
"ds_read_b128 a[144:147], v224 offset:0\n\t"
"v_mfma_f32_16x16x32_bf16 v[168:171], a[32:35], a[152:155], v[168:171]\n\t"
"ds_read_b128 a[148:151], v224 offset:512\n\t"
"v_mfma_f32_16x16x32_bf16 v[168:171], a[36:39], a[168:171], v[168:171]\n\t"
"ds_read_b128 a[164:167], v224 offset:4608\n\t"
"v_mfma_f32_16x16x32_bf16 v[184:187], a[40:43], a[152:155], v[184:187]\n\t"
"v_mfma_f32_16x16x32_bf16 v[184:187], a[44:47], a[168:171], v[184:187]\n\t"
"v_mfma_f32_16x16x32_bf16 v[192:195], a[224:227], a[176:179], 0\n\t"
"ds_read_b128 a[152:155], v224 offset:2048\n\t"
"v_mfma_f32_16x16x32_bf16 v[188:191], a[40:43], a[156:159], v[188:191]\n\t"
"v_mfma_f32_16x16x32_bf16 v[188:191], a[44:47], a[172:175], v[188:191]\n\t"
"v_mfma_f32_16x16x32_bf16 v[196:199], a[224:227], a[180:183], 0\n\t"
"v_mfma_f32_16x16x32_bf16 v[172:175], a[32:35], a[156:159], v[172:175]\n\t"
"ds_read_b128 a[168:171], v224 offset:6144\n\t"
"v_mfma_f32_16x16x32_bf16 v[172:175], a[36:39], a[172:175], v[172:175]\n\t"
"v_mfma_f32_16x16x32_bf16 v[156:159], a[24:27], a[156:159], v[156:159]\n\t"
"v_mfma_f32_16x16x32_bf16 v[156:159], a[28:31], a[172:175], v[156:159]\n\t"
"s_nop 2\n\t"
"v_cvt_pk_bf16_f32 v194, v194, v198\n\t"
"v_cvt_pk_bf16_f32 v195, v195, v199\n\t"
"v_mfma_f32_16x16x32_bf16 v[200:203], a[224:227], a[184:187], 0\n\t"
"s_waitcnt vmcnt(19) lgkmcnt(2)\n\t"
"v_pk_fma_f32 v[48:49], v[144:145], s[90:91], v[48:49]\n\t"
"v_pk_fma_f32 v[50:51], v[146:147], s[90:91], v[50:51]\n\t"
"v_pk_add_f32 v[144:145], v[48:49], v[144:145] neg_lo:[0,1] neg_hi:[0,1]\n\t"
"v_pk_add_f32 v[146:147], v[50:51], v[146:147] neg_lo:[0,1] neg_hi:[0,1]\n\t"
"v_pk_fma_f32 v[64:65], v[160:161], s[90:91], v[64:65]\n\t"
"v_pk_fma_f32 v[66:67], v[162:163], s[90:91], v[66:67]\n\t"
"v_pk_add_f32 v[160:161], v[64:65], v[160:161] neg_lo:[0,1] neg_hi:[0,1]\n\t"
"v_pk_add_f32 v[162:163], v[66:67], v[162:163] neg_lo:[0,1] neg_hi:[0,1]\n\t"
"v_pk_fma_f32 v[80:81], v[176:177], s[90:91], v[80:81]\n\t"
"v_pk_fma_f32 v[82:83], v[178:179], s[90:91], v[82:83]\n\t"
"v_pk_add_f32 v[176:177], v[80:81], v[176:177] neg_lo:[0,1] neg_hi:[0,1]\n\t"
"v_pk_add_f32 v[178:179], v[82:83], v[178:179] neg_lo:[0,1] neg_hi:[0,1]\n\t"
"v_pk_fma_f32 v[84:85], v[180:181], s[90:91], v[84:85]\n\t"
"v_pk_fma_f32 v[86:87], v[182:183], s[90:91], v[86:87]\n\t"
"v_pk_add_f32 v[180:181], v[84:85], v[180:181] neg_lo:[0,1] neg_hi:[0,1]\n\t"
"v_pk_add_f32 v[182:183], v[86:87], v[182:183] neg_lo:[0,1] neg_hi:[0,1]\n\t"
"v_pk_fma_f32 v[68:69], v[164:165], s[90:91], v[68:69]\n\t"
"v_pk_fma_f32 v[70:71], v[166:167], s[90:91], v[70:71]\n\t"
"v_pk_add_f32 v[164:165], v[68:69], v[164:165] neg_lo:[0,1] neg_hi:[0,1]\n\t"
"v_pk_add_f32 v[166:167], v[70:71], v[166:167] neg_lo:[0,1] neg_hi:[0,1]\n\t"
"v_pk_fma_f32 v[52:53], v[148:149], s[90:91], v[52:53]\n\t"
"v_pk_fma_f32 v[54:55], v[150:151], s[90:91], v[54:55]\n\t"
"v_pk_add_f32 v[148:149], v[52:53], v[148:149] neg_lo:[0,1] neg_hi:[0,1]\n\t"
"v_pk_add_f32 v[150:151], v[54:55], v[150:151] neg_lo:[0,1] neg_hi:[0,1]\n\t"
"v_pk_fma_f32 v[56:57], v[152:153], s[90:91], v[56:57]\n\t"
"v_pk_fma_f32 v[58:59], v[154:155], s[90:91], v[58:59]\n\t"
"v_pk_add_f32 v[152:153], v[56:57], v[152:153] neg_lo:[0,1] neg_hi:[0,1]\n\t"
"v_pk_add_f32 v[154:155], v[58:59], v[154:155] neg_lo:[0,1] neg_hi:[0,1]\n\t"
"v_pk_fma_f32 v[72:73], v[168:169], s[90:91], v[72:73]\n\t"
"v_pk_fma_f32 v[74:75], v[170:171], s[90:91], v[74:75]\n\t"
"v_pk_add_f32 v[168:169], v[72:73], v[168:169] neg_lo:[0,1] neg_hi:[0,1]\n\t"
"v_pk_add_f32 v[170:171], v[74:75], v[170:171] neg_lo:[0,1] neg_hi:[0,1]\n\t"
"v_pk_fma_f32 v[88:89], v[184:185], s[90:91], v[88:89]\n\t"
"v_pk_fma_f32 v[90:91], v[186:187], s[90:91], v[90:91]\n\t"
"v_pk_add_f32 v[184:185], v[88:89], v[184:185] neg_lo:[0,1] neg_hi:[0,1]\n\t"
"v_pk_add_f32 v[186:187], v[90:91], v[186:187] neg_lo:[0,1] neg_hi:[0,1]\n\t"
"v_pk_fma_f32 v[92:93], v[188:189], s[90:91], v[92:93]\n\t"
"v_pk_fma_f32 v[94:95], v[190:191], s[90:91], v[94:95]\n\t"
"v_pk_add_f32 v[188:189], v[92:93], v[188:189] neg_lo:[0,1] neg_hi:[0,1]\n\t"
"v_pk_add_f32 v[190:191], v[94:95], v[190:191] neg_lo:[0,1] neg_hi:[0,1]\n\t"
"v_pk_fma_f32 v[76:77], v[172:173], s[90:91], v[76:77]\n\t"
"v_pk_fma_f32 v[78:79], v[174:175], s[90:91], v[78:79]\n\t"
"v_pk_add_f32 v[172:173], v[76:77], v[172:173] neg_lo:[0,1] neg_hi:[0,1]\n\t"
"v_pk_add_f32 v[174:175], v[78:79], v[174:175] neg_lo:[0,1] neg_hi:[0,1]\n\t"
"v_pk_fma_f32 v[60:61], v[156:157], s[90:91], v[60:61]\n\t"
"v_pk_fma_f32 v[62:63], v[158:159], s[90:91], v[62:63]\n\t"
"v_pk_add_f32 v[156:157], v[60:61], v[156:157] neg_lo:[0,1] neg_hi:[0,1]\n\t"
"v_pk_add_f32 v[158:159], v[62:63], v[158:159] neg_lo:[0,1] neg_hi:[0,1]\n\t"
"v_mfma_f32_16x16x32_bf16 v[48:51], a[48:51], a[144:147], v[48:51]\n\t"
"ds_write_addtid_b32 v195 offset:24928\n\t"
"v_cvt_pk_bf16_f32 v192, v192, v196\n\t"
"v_mfma_f32_16x16x32_bf16 v[48:51], a[52:55], a[160:163], v[48:51]\n\t"
"v_mfma_f32_16x16x32_bf16 v[204:207], a[224:227], a[188:191], 0\n\t"
"ds_write_addtid_b32 v192 offset:16448\n\t"
"v_cvt_pk_bf16_f32 v193, v193, v197\n\t"
"v_mfma_f32_16x16x32_bf16 v[64:67], a[56:59], a[144:147], v[64:67]\n\t"
"ds_read_b128 a[156:159], v224 offset:2560\n\t"
"v_mfma_f32_16x16x32_bf16 v[64:67], a[60:63], a[160:163], v[64:67]\n\t"
"ds_read_b128 a[172:175], v224 offset:6656\n\t"
"v_mfma_f32_16x16x32_bf16 v[80:83], a[64:67], a[144:147], v[80:83]\n\t"
"v_mfma_f32_16x16x32_bf16 v[80:83], a[68:71], a[160:163], v[80:83]\n\t"
"ds_write_addtid_b32 v193 offset:16704\n\t"
"v_mfma_f32_16x16x32_bf16 v[208:211], a[224:227], a[192:195], 0\n\t"
"ds_read_b128 a[144:147], v224 offset:1024\n\t"
"v_cvt_pk_bf16_f32 v200, v200, v204\n\t"
"v_mfma_f32_16x16x32_bf16 v[84:87], a[64:67], a[148:151], v[84:87]\n\t"
"ds_read_b128 a[160:163], v224 offset:5120\n\t"
"v_cvt_pk_bf16_f32 v202, v202, v206\n\t"
"v_mfma_f32_16x16x32_bf16 v[84:87], a[68:71], a[164:167], v[84:87]\n\t"
"ds_write_addtid_b32 v194 offset:24672\n\t"
"v_mfma_f32_16x16x32_bf16 v[68:71], a[56:59], a[148:151], v[68:71]\n\t"
"ds_write_addtid_b32 v202 offset:28768\n\t"
"v_mfma_f32_16x16x32_bf16 v[68:71], a[60:63], a[164:167], v[68:71]\n\t"
"v_mfma_f32_16x16x32_bf16 v[212:215], a[224:227], a[196:199], 0\n\t"
"ds_write_addtid_b32 v200 offset:20544\n\t"
"v_cvt_pk_bf16_f32 v203, v203, v207\n\t"
"v_mfma_f32_16x16x32_bf16 v[52:55], a[48:51], a[148:151], v[52:55]\n\t"
"ds_read_b128 a[148:151], v224 offset:1536\n\t"
"v_mfma_f32_16x16x32_bf16 v[52:55], a[52:55], a[164:167], v[52:55]\n\t"
"ds_write_addtid_b32 v203 offset:29024\n\t"
"v_cvt_pk_bf16_f32 v201, v201, v205\n\t"
"v_mfma_f32_16x16x32_bf16 v[216:219], a[224:227], a[200:203], 0\n\t"
"s_waitcnt vmcnt(12) lgkmcnt(8)\n\t"
"ds_read_b128 a[164:167], v224 offset:5632\n\t"
"v_mfma_f32_16x16x32_bf16 v[48:51], a[72:75], a[152:155], v[48:51]\n\t"
"v_cvt_pk_bf16_f32 v208, v208, v212\n\t"
"v_cvt_pk_bf16_f32 v209, v209, v213\n\t"
"v_mfma_f32_16x16x32_bf16 v[48:51], a[76:79], a[168:171], v[48:51]\n\t"
"ds_write_addtid_b32 v201 offset:20800\n\t"
"v_cvt_pk_bf16_f32 v210, v210, v214\n\t"
"v_mfma_f32_16x16x32_bf16 v[64:67], a[80:83], a[152:155], v[64:67]\n\t"
"ds_write_addtid_b32 v210 offset:26720\n\t"
"v_cvt_pk_bf16_f32 v211, v211, v215\n\t"
"v_mfma_f32_16x16x32_bf16 v[64:67], a[84:87], a[168:171], v[64:67]\n\t"
"ds_write_addtid_b32 v208 offset:18496\n\t"
"v_mfma_f32_16x16x32_bf16 v[220:223], a[224:227], a[204:207], 0\n\t"
"ds_write_addtid_b32 v209 offset:18752\n\t"
"v_mfma_f32_16x16x32_bf16 v[80:83], a[88:91], a[152:155], v[80:83]\n\t"
"ds_read_b128 a[152:155], v224 offset:3072\n\t"
"v_cvt_pk_bf16_f32 v48, v48, v49\n\t"
"v_mfma_f32_16x16x32_bf16 v[80:83], a[92:95], a[168:171], v[80:83]\n\t"
"ds_write_addtid_b32 v211 offset:26976\n\t"
"v_cvt_pk_bf16_f32 v64, v64, v65\n\t"
"v_mfma_f32_16x16x32_bf16 v[84:87], a[88:91], a[156:159], v[84:87]\n\t"
"ds_read_b128 a[168:171], v224 offset:7168\n\t"
"v_cvt_pk_bf16_f32 v49, v50, v51\n\t"
"v_mfma_f32_16x16x32_bf16 v[84:87], a[92:95], a[172:175], v[84:87]\n\t"
"v_cvt_pk_bf16_f32 v216, v216, v220\n\t"
"v_cvt_pk_bf16_f32 v217, v217, v221\n\t"
"v_mfma_f32_16x16x32_bf16 v[68:71], a[80:83], a[156:159], v[68:71]\n\t"
"ds_write_addtid_b32 v216 offset:22592\n\t"
"v_cvt_pk_bf16_f32 v218, v218, v222\n\t"
"v_mfma_f32_16x16x32_bf16 v[68:71], a[84:87], a[172:175], v[68:71]\n\t"
"ds_write_addtid_b32 v217 offset:22848\n\t"
"v_cvt_pk_bf16_f32 v80, v80, v81\n\t"
"v_mfma_f32_16x16x32_bf16 v[52:55], a[72:75], a[156:159], v[52:55]\n\t"
"s_nop 0\n\t"
"ds_read_b128 a[156:159], v224 offset:3584\n\t"
"ds_write_addtid_b32 v218 offset:30816\n\t"
"v_mfma_f32_16x16x32_bf16 v[52:55], a[76:79], a[172:175], v[52:55]\n\t"
"s_waitcnt lgkmcnt(9)\n\t"
"ds_read_b128 a[172:175], v224 offset:7680\n\t"
"v_cvt_pk_bf16_f32 v84, v84, v85\n\t"
"v_mfma_f32_16x16x32_bf16 v[56:59], a[48:51], a[144:147], v[56:59]\n\t"
"v_cvt_pk_bf16_f32 v81, v82, v83\n\t"
"v_cvt_pk_bf16_f32 v85, v86, v87\n\t"
"v_mfma_f32_16x16x32_bf16 v[56:59], a[52:55], a[160:163], v[56:59]\n\t"
"ds_write2st64_b64 v247, v[80:81], v[84:85] offset0:0 offset1:13\n\t"
"v_mfma_f32_16x16x32_bf16 v[72:75], a[56:59], a[144:147], v[72:75]\n\t"
"v_cvt_pk_bf16_f32 v68, v68, v69\n\t"
"v_cvt_pk_bf16_f32 v69, v70, v71\n\t"
"v_mfma_f32_16x16x32_bf16 v[72:75], a[60:63], a[160:163], v[72:75]\n\t"
"v_cvt_pk_bf16_f32 v52, v52, v53\n\t"
"v_cvt_pk_bf16_f32 v65, v66, v67\n\t"
"v_mfma_f32_16x16x32_bf16 v[88:91], a[64:67], a[144:147], v[88:91]\n\t"
"v_cvt_pk_bf16_f32 v219, v219, v223\n\t"
"v_cvt_pk_bf16_f32 v53, v54, v55\n\t"
"v_mfma_f32_16x16x32_bf16 v[88:91], a[68:71], a[160:163], v[88:91]\n\t"
"ds_write_addtid_b32 v219 offset:31072\n\t"
"ds_write2st64_b64 v246, v[64:65], v[68:69] offset0:0 offset1:13\n\t"
"v_mfma_f32_16x16x32_bf16 v[92:95], a[64:67], a[148:151], v[92:95]\n\t"
"v_mfma_f32_16x16x32_bf16 v[92:95], a[68:71], a[164:167], v[92:95]\n\t"
"ds_write2st64_b64 v228, v[48:49], v[52:53] offset0:0 offset1:13\n\t"
"v_mfma_f32_16x16x32_bf16 v[76:79], a[56:59], a[148:151], v[76:79]\n\t"
"v_mfma_f32_16x16x32_bf16 v[76:79], a[60:63], a[164:167], v[76:79]\n\t"
"v_mfma_f32_16x16x32_bf16 v[60:63], a[48:51], a[148:151], v[60:63]\n\t"
"v_mfma_f32_16x16x32_bf16 v[60:63], a[52:55], a[164:167], v[60:63]\n\t"
"s_waitcnt lgkmcnt(0)\n\t"
"s_barrier\n\t"
"v_mfma_f32_16x16x32_bf16 v[56:59], a[72:75], a[152:155], v[56:59]\n\t"
"s_nop 0\n\t"
"ds_read_b128 a[144:147], v224 offset:16448\n\t"
"ds_read_b128 a[148:151], v224 offset:16960\n\t"
"v_mfma_f32_16x16x32_bf16 v[56:59], a[76:79], a[168:171], v[56:59]\n\t"
"ds_read_b128 a[0:3], v229 offset:0\n\t"
"v_mfma_f32_16x16x32_bf16 v[72:75], a[80:83], a[152:155], v[72:75]\n\t"
"ds_read_b128 a[160:163], v224 offset:20544\n\t"
"v_mfma_f32_16x16x32_bf16 v[72:75], a[84:87], a[168:171], v[72:75]\n\t"
"ds_read_b128 a[164:167], v224 offset:21056\n\t"
"v_mfma_f32_16x16x32_bf16 v[88:91], a[88:91], a[152:155], v[88:91]\n\t"
"ds_read_b128 a[4:7], v230 offset:0\n\t"
"v_mfma_f32_16x16x32_bf16 v[88:91], a[92:95], a[168:171], v[88:91]\n\t"
"ds_read_b128 a[8:11], v231 offset:0\n\t"
"v_mfma_f32_16x16x32_bf16 v[92:95], a[88:91], a[156:159], v[92:95]\n\t"
"ds_read_b128 a[168:171], v224 offset:22592\n\t"
"v_cvt_pk_bf16_f32 v56, v56, v57\n\t"
"v_mfma_f32_16x16x32_bf16 v[92:95], a[92:95], a[172:175], v[92:95]\n\t"
"s_waitcnt lgkmcnt(5)\n\t"
"buffer_store_dwordx4 a[0:3], v232, s[16:19], s24 offen nt\n\t"
"v_mfma_f32_16x16x32_bf16 v[76:79], a[80:83], a[156:159], v[76:79]\n\t"
"ds_read_b128 a[152:155], v224 offset:18496\n\t"
"v_cvt_pk_bf16_f32 v57, v58, v59\n\t"
"v_mfma_f32_16x16x32_bf16 v[76:79], a[84:87], a[172:175], v[76:79]\n\t"
"v_mfma_f32_16x16x32_bf16 v[60:63], a[72:75], a[156:159], v[60:63]\n\t"
"ds_read_b128 a[156:159], v224 offset:19008\n\t"
"v_cvt_pk_bf16_f32 v88, v88, v89\n\t"
"v_mfma_f32_16x16x32_bf16 v[60:63], a[76:79], a[172:175], v[60:63]\n\t"
"s_waitcnt vmcnt(7) lgkmcnt(6)\n\t"
"s_waitcnt lgkmcnt(4)\n\t"
"buffer_store_dwordx4 a[4:7], v233, s[16:19], s24 offen nt\n\t"
"v_mfma_f32_16x16x32_bf16 v[144:147], a[96:99], a[144:147], v[144:147]\n\t"
"ds_read_b128 a[172:175], v224 offset:23104\n\t"
"v_cvt_pk_bf16_f32 v72, v72, v73\n\t"
"v_mfma_f32_16x16x32_bf16 v[144:147], a[100:103], a[160:163], v[144:147]\n\t"
"v_cvt_pk_bf16_f32 v76, v76, v77\n\t"
"v_cvt_pk_bf16_f32 v92, v92, v93\n\t"
"v_mfma_f32_16x16x32_bf16 v[160:163], a[104:107], a[144:147], v[160:163]\n\t"
"v_cvt_pk_bf16_f32 v73, v74, v75\n\t"
"v_cvt_pk_bf16_f32 v77, v78, v79\n\t"
"v_mfma_f32_16x16x32_bf16 v[160:163], a[108:111], a[160:163], v[160:163]\n\t"
"ds_write2st64_b64 v246, v[72:73], v[76:77] offset0:26 offset1:39\n\t"
"v_mfma_f32_16x16x32_bf16 v[176:179], a[112:115], a[144:147], v[176:179]\n\t"
"v_cvt_pk_bf16_f32 v89, v90, v91\n\t"
"v_cvt_pk_bf16_f32 v93, v94, v95\n\t"
"v_mfma_f32_16x16x32_bf16 v[176:179], a[116:119], a[160:163], v[176:179]\n\t"
"s_waitcnt lgkmcnt(4)\n\t"
"buffer_store_dwordx4 a[8:11], v234, s[16:19], s24 offen nt\n\t"
"v_mfma_f32_16x16x32_bf16 v[180:183], a[112:115], a[148:151], v[180:183]\n\t"
"v_cvt_pk_bf16_f32 v60, v60, v61\n\t"
"v_mfma_f32_16x16x32_bf16 v[180:183], a[116:119], a[164:167], v[180:183]\n\t"
"ds_write2st64_b64 v247, v[88:89], v[92:93] offset0:26 offset1:39\n\t"
"v_mfma_f32_16x16x32_bf16 v[164:167], a[104:107], a[148:151], v[164:167]\n\t"
"v_cvt_pk_bf16_f32 v61, v62, v63\n\t"
"v_mfma_f32_16x16x32_bf16 v[164:167], a[108:111], a[164:167], v[164:167]\n\t"
"s_nop 0\n\t"
"ds_read_b128 a[144:147], v224 offset:17472\n\t"
"ds_read_b128 a[160:163], v224 offset:21568\n\t"
"v_mfma_f32_16x16x32_bf16 v[148:151], a[96:99], a[148:151], v[148:151]\n\t"
"v_mfma_f32_16x16x32_bf16 v[148:151], a[100:103], a[164:167], v[148:151]\n\t"
"s_waitcnt vmcnt(3) lgkmcnt(4)\n\t"
"ds_write2st64_b64 v228, v[56:57], v[60:61] offset0:26 offset1:39\n\t"
"v_mfma_f32_16x16x32_bf16 v[144:147], a[120:123], a[152:155], v[144:147]\n\t"
"v_mfma_f32_16x16x32_bf16 v[144:147], a[124:127], a[168:171], v[144:147]\n\t"
"s_nop 0\n\t"
"ds_read_b128 a[148:151], v224 offset:17984\n\t"
"ds_read_b128 a[164:167], v224 offset:22080\n\t"
"v_mfma_f32_16x16x32_bf16 v[160:163], a[128:131], a[152:155], v[160:163]\n\t"
"v_mfma_f32_16x16x32_bf16 v[160:163], a[132:135], a[168:171], v[160:163]\n\t"
"s_waitcnt lgkmcnt(2)\n\t"
"s_barrier\n\t"
"v_mfma_f32_16x16x32_bf16 v[176:179], a[136:139], a[152:155], v[176:179]\n\t"
"ds_read_b128 a[12:15], v229 offset:13312\n\t"
"v_mfma_f32_16x16x32_bf16 v[176:179], a[140:143], a[168:171], v[176:179]\n\t"
"ds_read_b128 a[168:171], v224 offset:23616\n\t"
"v_cvt_pk_bf16_f32 v144, v144, v145\n\t"
"v_mfma_f32_16x16x32_bf16 v[180:183], a[136:139], a[156:159], v[180:183]\n\t"
"ds_read_b128 a[16:19], v230 offset:13312\n\t"
"v_cvt_pk_bf16_f32 v160, v160, v161\n\t"
"v_mfma_f32_16x16x32_bf16 v[180:183], a[140:143], a[172:175], v[180:183]\n\t"
"ds_read_b128 a[152:155], v224 offset:19520\n\t"
"v_cvt_pk_bf16_f32 v145, v146, v147\n\t"
"v_mfma_f32_16x16x32_bf16 v[164:167], a[128:131], a[156:159], v[164:167]\n\t"
"v_cvt_pk_bf16_f32 v176, v176, v177\n\t"
"v_cvt_pk_bf16_f32 v177, v178, v179\n\t"
"v_mfma_f32_16x16x32_bf16 v[164:167], a[132:135], a[172:175], v[164:167]\n\t"
"ds_read_b128 a[20:23], v231 offset:13312\n\t"
"v_mfma_f32_16x16x32_bf16 v[148:151], a[120:123], a[156:159], v[148:151]\n\t"
"ds_read_b128 a[156:159], v224 offset:20032\n\t"
"v_cvt_pk_bf16_f32 v180, v180, v181\n\t"
"v_mfma_f32_16x16x32_bf16 v[148:151], a[124:127], a[172:175], v[148:151]\n\t"
"ds_read_b128 a[172:175], v224 offset:24128\n\t"
"s_waitcnt lgkmcnt(6)\n\t"
"buffer_store_dwordx4 a[12:15], v232, s[16:19], s27 offen nt\n\t"
"v_mfma_f32_16x16x32_bf16 v[152:155], a[96:99], a[144:147], v[152:155]\n\t"
"v_cvt_pk_bf16_f32 v161, v162, v163\n\t"
"v_cvt_pk_bf16_f32 v181, v182, v183\n\t"
"v_mfma_f32_16x16x32_bf16 v[152:155], a[100:103], a[160:163], v[152:155]\n\t"
"ds_write2st64_b64 v247, v[176:177], v[180:181] offset0:52 offset1:65\n\t"
"v_mfma_f32_16x16x32_bf16 v[168:171], a[104:107], a[144:147], v[168:171]\n\t"
"v_cvt_pk_bf16_f32 v148, v148, v149\n\t"
"v_cvt_pk_bf16_f32 v164, v164, v165\n\t"
"v_mfma_f32_16x16x32_bf16 v[168:171], a[108:111], a[160:163], v[168:171]\n\t"
"s_waitcnt lgkmcnt(5)\n\t"
"buffer_store_dwordx4 a[16:19], v233, s[16:19], s27 offen nt\n\t"
"v_mfma_f32_16x16x32_bf16 v[184:187], a[112:115], a[144:147], v[184:187]\n\t"
"v_cvt_pk_bf16_f32 v149, v150, v151\n\t"
"v_cvt_pk_bf16_f32 v165, v166, v167\n\t"
"v_mfma_f32_16x16x32_bf16 v[184:187], a[116:119], a[160:163], v[184:187]\n\t"
"ds_write2st64_b64 v228, v[144:145], v[148:149] offset0:52 offset1:65\n\t"
"v_mfma_f32_16x16x32_bf16 v[188:191], a[112:115], a[148:151], v[188:191]\n\t"
"v_mfma_f32_16x16x32_bf16 v[188:191], a[116:119], a[164:167], v[188:191]\n\t"
"ds_write2st64_b64 v246, v[160:161], v[164:165] offset0:52 offset1:65\n\t"
"v_mfma_f32_16x16x32_bf16 v[172:175], a[104:107], a[148:151], v[172:175]\n\t"
"v_mfma_f32_16x16x32_bf16 v[172:175], a[108:111], a[164:167], v[172:175]\n\t"
"s_waitcnt lgkmcnt(3)\n\t"
"buffer_store_dwordx4 a[20:23], v234, s[16:19], s27 offen nt\n\t"
"v_mfma_f32_16x16x32_bf16 v[156:159], a[96:99], a[148:151], v[156:159]\n\t"
"v_mfma_f32_16x16x32_bf16 v[156:159], a[100:103], a[164:167], v[156:159]\n\t"
"s_waitcnt lgkmcnt(0)\n\t"
"s_barrier\n\t"
"v_mfma_f32_16x16x32_bf16 v[152:155], a[120:123], a[152:155], v[152:155]\n\t"
"v_mfma_f32_16x16x32_bf16 v[152:155], a[124:127], a[168:171], v[152:155]\n\t"
"ds_read_b128 a[28:31], v230 offset:26624\n\t"
"v_mfma_f32_16x16x32_bf16 v[168:171], a[128:131], a[152:155], v[168:171]\n\t"
"v_mfma_f32_16x16x32_bf16 v[168:171], a[132:135], a[168:171], v[168:171]\n\t"
"ds_read_b128 a[24:27], v229 offset:26624\n\t"
"v_mfma_f32_16x16x32_bf16 v[184:187], a[136:139], a[152:155], v[184:187]\n\t"
"ds_read_b128 a[32:35], v231 offset:26624\n\t"
"v_mfma_f32_16x16x32_bf16 v[184:187], a[140:143], a[168:171], v[184:187]\n\t"
"s_nop 0\n\t"
"v_cvt_pk_bf16_f32 v152, v152, v153\n\t"
"v_cvt_pk_bf16_f32 v153, v154, v155\n\t"
"v_mfma_f32_16x16x32_bf16 v[188:191], a[136:139], a[156:159], v[188:191]\n\t"
"v_cvt_pk_bf16_f32 v168, v168, v169\n\t"
"v_cvt_pk_bf16_f32 v169, v170, v171\n\t"
"v_mfma_f32_16x16x32_bf16 v[188:191], a[140:143], a[172:175], v[188:191]\n\t"
"s_waitcnt lgkmcnt(2)\n\t"
"buffer_store_dwordx4 a[28:31], v233, s[16:19], s20 offen nt\n\t"
"v_mfma_f32_16x16x32_bf16 v[172:175], a[128:131], a[156:159], v[172:175]\n\t"
"v_cvt_pk_bf16_f32 v184, v184, v185\n\t"
"v_cvt_pk_bf16_f32 v185, v186, v187\n\t"
"v_mfma_f32_16x16x32_bf16 v[172:175], a[132:135], a[172:175], v[172:175]\n\t"
"s_waitcnt lgkmcnt(1)\n\t"
"buffer_store_dwordx4 a[24:27], v232, s[16:19], s20 offen nt\n\t"
"v_mfma_f32_16x16x32_bf16 v[156:159], a[120:123], a[156:159], v[156:159]\n\t"
"v_cvt_pk_bf16_f32 v188, v188, v189\n\t"
"v_cvt_pk_bf16_f32 v189, v190, v191\n\t"
"v_mfma_f32_16x16x32_bf16 v[156:159], a[124:127], a[172:175], v[156:159]\n\t"
"s_waitcnt lgkmcnt(0)\n\t"
"buffer_store_dwordx4 a[32:35], v234, s[16:19], s20 offen nt\n\t"
"s_nop 5\n\t"
"v_cvt_pk_bf16_f32 v156, v156, v157\n\t"
"v_cvt_pk_bf16_f32 v157, v158, v159\n\t"
"ds_write2st64_b64 v228, v[152:153], v[156:157] offset0:78 offset1:91\n\t"
"v_cvt_pk_bf16_f32 v172, v172, v173\n\t"
"v_cvt_pk_bf16_f32 v173, v174, v175\n\t"
"ds_write2st64_b64 v246, v[168:169], v[172:173] offset0:78 offset1:91\n\t"
"ds_write2st64_b64 v247, v[184:185], v[188:189] offset0:78 offset1:91\n\t"
"s_waitcnt lgkmcnt(0)\n\t"
"s_barrier\n\t"
"ds_read_b128 a[36:39], v229 offset:39936\n\t"
"ds_read_b128 a[40:43], v230 offset:39936\n\t"
"ds_read_b128 a[44:47], v231 offset:39936\n\t"
"s_waitcnt lgkmcnt(2)\n\t"
"buffer_store_dwordx4 a[36:39], v232, s[16:19], s25 offen nt\n\t"
"s_waitcnt lgkmcnt(1)\n\t"
"buffer_store_dwordx4 a[40:43], v233, s[16:19], s25 offen nt\n\t"
"s_waitcnt lgkmcnt(0)\n\t"
"buffer_store_dwordx4 a[44:47], v234, s[16:19], s25 offen nt\n\t"
"s_waitcnt vmcnt(0)\n\t"
"Lwino_end:\n\t"
  : "+{v205}"(tid)
  : "s"(__builtin_amdgcn_kernarg_segment_ptr()), "s"(blockIdx.x), "s"((unsigned)(size_t)(__attribute__((address_space(3))) char*)smem)
  : "v0", "v1", "v2", "v3", "v4", "v5", "v6", "v7", "v8", "v9", "v10", "v11", "v12", "v13", "v14", "v15", "v16", "v17", "v18", "v19", "v20", "v21", "v22", "v23", "v24", "v25", "v26", "v27", "v28", "v29", "v30", "v31", "v32", "v33", "v34", "v35", "v36", "v37", "v38", "v39", "v40", "v41", "v42", "v43", "v44", "v45", "v46", "v47", "v48", "v49", "v50", "v51", "v52", "v53", "v54", "v55", "v56", "v57", "v58", "v59", "v60", "v61", "v62", "v63", "v64", "v65", "v66", "v67", "v68", "v69", "v70", "v71", "v72", "v73", "v74", "v75", "v76", "v77", "v78", "v79", "v80", "v81", "v82", "v83", "v84", "v85", "v86", "v87", "v88", "v89", "v90", "v91", "v92", "v93", "v94", "v95", "v96", "v97", "v98", "v99", "v100", "v101", "v102", "v103", "v104", "v105", "v106", "v107", "v108", "v109", "v110", "v111", "v112", "v113", "v114", "v115", "v116", "v117", "v118", "v119", "v120", "v121", "v122", "v123", "v124", "v125", "v126", "v127", "v128", "v129", "v130", "v131", "v132", "v133", "v134", "v135", "v136", "v137", "v138", "v139", "v140", "v141", "v142", "v143", "v144", "v145", "v146", "v147", "v148", "v149", "v150", "v151", "v152", "v153", "v154", "v155", "v156", "v157", "v158", "v159", "v160", "v161", "v162", "v163", "v164", "v165", "v166", "v167", "v168", "v169", "v170", "v171", "v172", "v173", "v174", "v175", "v176", "v177", "v178", "v179", "v180", "v181", "v182", "v183", "v184", "v185", "v186", "v187", "v188", "v189", "v190", "v191", "v192", "v193", "v194", "v195", "v196", "v197", "v198", "v199", "v200", "v201", "v202", "v203", "v204", "v206", "v207", "v208", "v209", "v210", "v211", "v212", "v213", "v214", "v215", "v216", "v217", "v218", "v219", "v220", "v221", "v222", "v223", "v224", "v225", "v226", "v227", "v228", "v229", "v230", "v231", "v232", "v233", "v234", "v235", "v236", "v237", "v238", "v239", "v240", "v241", "v242", "v243", "v244", "v245", "v246", "v247", "v248", "v249", "v250", "v251", "v252", "v253", "v254", "v255", "a0", "a1", "a2", "a3", "a4", "a5", "a6", "a7", "a8", "a9", "a10", "a11", "a12", "a13", "a14", "a15", "a16", "a17", "a18", "a19", "a20", "a21", "a22", "a23", "a24", "a25", "a26", "a27", "a28", "a29", "a30", "a31", "a32", "a33", "a34", "a35", "a36", "a37", "a38", "a39", "a40", "a41", "a42", "a43", "a44", "a45", "a46", "a47", "a48", "a49", "a50", "a51", "a52", "a53", "a54", "a55", "a56", "a57", "a58", "a59", "a60", "a61", "a62", "a63", "a64", "a65", "a66", "a67", "a68", "a69", "a70", "a71", "a72", "a73", "a74", "a75", "a76", "a77", "a78", "a79", "a80", "a81", "a82", "a83", "a84", "a85", "a86", "a87", "a88", "a89", "a90", "a91", "a92", "a93", "a94", "a95", "a96", "a97", "a98", "a99", "a100", "a101", "a102", "a103", "a104", "a105", "a106", "a107", "a108", "a109", "a110", "a111", "a112", "a113", "a114", "a115", "a116", "a117", "a118", "a119", "a120", "a121", "a122", "a123", "a124", "a125", "a126", "a127", "a128", "a129", "a130", "a131", "a132", "a133", "a134", "a135", "a136", "a137", "a138", "a139", "a140", "a141", "a142", "a143", "a144", "a145", "a146", "a147", "a148", "a149", "a150", "a151", "a152", "a153", "a154", "a155", "a156", "a157", "a158", "a159", "a160", "a161", "a162", "a163", "a164", "a165", "a166", "a167", "a168", "a169", "a170", "a171", "a172", "a173", "a174", "a175", "a176", "a177", "a178", "a179", "a180", "a181", "a182", "a183", "a184", "a185", "a186", "a187", "a188", "a189", "a190", "a191", "a192", "a193", "a194", "a195", "a196", "a197", "a198", "a199", "a200", "a201", "a202", "a203", "a204", "a205", "a206", "a207", "a208", "a209", "a210", "a211", "a212", "a213", "a214", "a215", "a216", "a217", "a218", "a219", "a220", "a221", "a222", "a223", "a224", "a225", "a226", "a227", "a228", "a229", "a230", "a231", "a232", "a233", "a234", "a235", "a236", "a237", "a238", "a239", "a240", "a241", "a242", "a243", "a244", "a245", "a246", "a247", "a248", "a249", "a250", "a251", "a252", "a253", "a254", "a255", "s6", "s7", "s8", "s9", "s10", "s11", "s12", "s13", "s14", "s15", "s16", "s17", "s18", "s19", "s20", "s21", "s22", "s23", "s24", "s25", "s26", "s27", "s28", "s29", "s30", "s31", "s32", "s33", "s34", "s35", "s36", "s37", "s38", "s39", "s40", "s41", "s42", "s43", "s44", "s45", "s46", "s47", "s48", "s49", "s50", "s51", "s52", "s53", "s54", "s55", "s56", "s57", "s58", "s59", "s60", "s61", "s62", "s63", "s64", "s65", "s66", "s67", "s68", "s69", "s70", "s71", "s72", "s73", "s74", "s75", "s76", "s77", "s78", "s79", "s80", "s81", "s82", "s83", "s84", "s85", "s86", "s87", "s88", "s89", "s90", "s91", "scc", "vcc", "memory");
}

extern "C" void kernel_launch(void* const* d_in, const int* in_sizes, int n_in, void* d_out, int out_size, void* d_ws,
                              size_t ws_size, hipStream_t stream) {
  wino_main_kernel<<<256, 256, 0, stream>>>((const char*)d_in[0], (char*)d_ws, (char*)d_out, (const char*)d_in[1]);
}
```

```cpp
#include <hip/hip_runtime.h>
#include <hip/hip_bf16.h>
#include <stdint.h>

#define U_BYTES 1179648

__global__ __launch_bounds__(256, 1) void wino_main_kernel(const char* __restrict__ x, char* __restrict__ Uq, char* __restrict__ out, const char* __restrict__ wt) {
  __shared__ __attribute__((aligned(1024))) char smem[163840];
  unsigned tid = threadIdx.x;
  asm volatile(
"s_load_dwordx8 s[76:83], %1, 0x0\n\t"
"s_mov_b32 s66, %2\n\t"
"s_getpc_b64 s[6:7]\n\t"
"Lwino_pc:\n\t"
"s_bfe_u32 s42, %2, 0x50003\n\t"
"s_lshl_b32 s42, s42, 12\n\t"
"v_lshl_add_u32 v203, v205, 4, s42\n\t"
"v_min_u32 v203, (Lwino_end-Lwino_pc-16)&-16, v203\n\t"
"global_load_dwordx4 v[196:199], v203, s[6:7]\n\t"
"v_and_b32 v204, 63, v205\n\t"
"v_readfirstlane_b32 s73, v205\n\t"
"v_lshrrev_b32 v120, 3, v204\n\t"
"v_and_b32 v121, 7, v204\n\t"
"s_lshr_b32 s73, s73, 6\n\t"
"s_lshr_b32 s56, s66, 5\n\t"
"s_mul_i32 s56, s56, 12288\n\t"
"s_bfe_u32 s61, s66, 0x20001\n\t"
"s_mul_i32 s61, s61, 1536\n\t"
"s_add_u32 s56, s56, s61\n\t"
"s_bitcmp1_b32 s66, 0\n\t"
"s_cselect_b32 s61, 48, 0\n\t"
"s_add_u32 s56, s56, s61\n\t"
"s_bfe_u32 s62, s66, 0x20003\n\t"
"s_mul_i32 s61, s62, 96\n\t"
"s_add_u32 s56, s56, s61\n\t"
"s_cmp_lt_u32 s66, 192\n\t"
"s_cselect_b32 s56, s56, 2.0\n\t"
"v_bfe_u32 v42, v204, 3, 2\n\t"
"v_lshlrev_b32 v43, 2, v204\n\t"
"v_and_or_b32 v42, v43, 16, v42\n\t"
"v_mul_u32_u24 v42, 0x180, v42\n\t"
"v_lshrrev_b32 v44, 5, v204\n\t"
"v_mul_u32_u24 v43, 0x12000, v44\n\t"
"v_and_b32 v45, 3, v204\n\t"
"v_min_u32 v41, 2, v45\n\t"
"v_lshl_add_u32 v41, v41, 4, v42\n\t"
"v_add_u32 v41, v41, v43\n\t"
"v_lshlrev_b32 v43, 4, v204\n\t"
"v_bfe_u32 v45, v204, 1, 2\n\t"
"v_and_or_b32 v45, v43, 16, v45\n\t"
"v_mul_u32_u24 v45, 0x180, v45\n\t"
"v_lshl_add_u32 v44, v44, 3, v120\n\t"
"v_lshl_add_u32 v45, v44, 1, v45\n\t"
"v_mov_b32 v40, 0\n\t"
"s_mul_i32 s61, s73, 147456\n\t"
"s_add_u32 s61, s61, s56\n\t"
"s_lshl3_add_u32 s62, s73, s56\n\t"
"s_add_u32 s62, s62, 589824\n\t"
"s_lshl_b32 s64, s73, 10\n\t"
"s_and_b32 s60, s66, 7\n\t"
"s_lshr_b32 s65, s66, 3\n\t"
"s_mul_i32 s24, s60, 12582912\n\t"
"s_mul_i32 s63, s65, 6144\n\t"
"s_add_u32 s24, s24, s63\n\t"
"s_add_u32 s22, s24, 196224\n\t"
"s_mul_i32 s63, s73, 786432\n\t"
"s_mul_i32 s26, s73, 4608\n\t"
"s_mov_b32 s48, 189696\n\t"
"s_cmp_eq_u32 s65, 0\n\t"
"s_cselect_b64 s[40:41], 0xff, 0\n\t"
"s_cselect_b64 s[46:47], -1, 0\n\t"
"v_mul_u32_u24 v122, 0x300, v120\n\t"
"v_xor_b32 v123, 4, v121\n\t"
"v_add_u32 v122, s63, v122\n\t"
"v_and_b32 v124, 2, v120\n\t"
"v_xor_b32 v124, v124, v123\n\t"
"v_lshl_add_u32 v128, v124, 4, v122\n\t"
"v_cndmask_b32 v241, v128, 2.0, s[40:41]\n\t"
"v_add_u32 v122, 194816, v122\n\t"
"v_add_u32 v124, -1, v120\n\t"
"v_ashrrev_i32 v125, 31, v124\n\t"
"v_sub_u32 v124, v124, v125\n\t"
"v_and_b32 v124, 2, v124\n\t"
"v_xor_b32 v124, v124, v123\n\t"
"v_mad_i32_i24 v125, v125, s48, v122\n\t"
"v_lshl_add_u32 v129, v124, 4, v125\n\t"
"s_lshl_b64 s[40:41], s[40:41], 8\n\t"
"v_cndmask_b32 v242, v129, 2.0, s[40:41]\n\t"
"v_add_u32 v122, 194816, v122\n\t"
"v_add_u32 v124, -2, v120\n\t"
"v_ashrrev_i32 v125, 31, v124\n\t"
"v_sub_u32 v124, v124, v125\n\t"
"v_and_b32 v124, 2, v124\n\t"
"v_and_or_b32 v124, v125, 4, v124\n\t"
"v_xor_b32 v124, v124, v121\n\t"
"v_mad_i32_i24 v125, v125, s48, v122\n\t"
"v_lshl_add_u32 v130, v124, 4, v125\n\t"
"s_lshl_b64 s[40:41], s[40:41], 8\n\t"
"v_cndmask_b32 v243, v130, 2.0, s[40:41]\n\t"
"v_add_u32 v122, 194816, v122\n\t"
"v_add_u32 v124, -3, v120\n\t"
"v_ashrrev_i32 v125, 31, v124\n\t"
"v_sub_u32 v124, v124, v125\n\t"
"v_and_b32 v124, 2, v124\n\t"
"v_xor_b32 v124, v124, v121\n\t"
"v_mad_i32_i24 v125, v125, s48, v122\n\t"
"v_lshl_add_u32 v131, v124, 4, v125\n\t"
"s_lshl_b64 s[40:41], s[40:41], 8\n\t"
"v_cndmask_b32 v244, v131, 2.0, s[40:41]\n\t"
"v_add_u32 v124, 5, v120\n\t"
"v_and_b32 v124, 2, v124\n\t"
"v_xor_b32 v124, v124, v121\n\t"
"v_add_u32 v122, 2560, v122\n\t"
"v_lshl_add_u32 v245, v124, 4, v122\n\t"
"s_mov_b32 s37, -1\n\t"
"s_mov_b32 s36, 0\n\t"
"s_mul_i32 s67, s73, 48\n\t"
"s_add_u32 s67, s67, 118784\n\t"
"s_mov_b32 s10, 0x200000\n\t"
"s_mov_b32 s11, 0x20000\n\t"
"v_min_u32 v189, 47, v204\n\t"
"v_add_u32 v189, s67, v189\n\t"
"v_mul_u32_u24 v189, 16, v189\n\t"
"s_mov_b32 s54, 663552\n\t"
"s_mov_b32 s55, 0x20000\n\t"
"s_mov_b32 s14, 100663296\n\t"
"s_mov_b32 s15, 0x20000\n\t"
"s_add_u32 m0, s64, 0\n\t"
"s_waitcnt lgkmcnt(0)\n\t"
"s_mov_b32 s52, s82\n\t"
"s_and_b32 s53, s83, 0xffff\n\t"
"buffer_load_dwordx4 v41, s[52:55], s61 offen lds\n\t"
"buffer_load_short_d16_hi v40, v45, s[52:55], s62 offen\n\t"
"s_mov_b32 s8, s78\n\t"
"s_and_b32 s9, s79, 0xffff\n\t"
"buffer_load_dword v188, v189, s[8:11], 0 offen sc1\n\t"
"s_mov_b32 s12, s76\n\t"
"s_and_b32 s13, s77, 0xffff\n\t"
"s_add_u32 m0, s26, 83200\n\t"
"s_nop 0\n\t"
"buffer_load_dwordx4 v241, s[12:15], s22 offen offset:0 nt lds\n\t"
"buffer_load_dwordx4 v242, s[12:15], s22 offen offset:1024 nt lds\n\t"
"buffer_load_dwordx4 v243, s[12:15], s22 offen offset:2048 nt lds\n\t"
"buffer_load_dwordx4 v244, s[12:15], s22 offen offset:3072 nt lds\n\t"
"s_mov_b64 exec, s[36:37]\n\t"
"buffer_load_dwordx4 v245, s[12:15], s22 offen offset:3584 nt lds\n\t"
"s_mov_b64 exec, -1\n\t"
"s_mul_i32 s49, s73, 36\n\t"
"s_mul_i32 s43, s73, 576\n\t"
"s_sub_u32 s42, s22, 203520\n\t"
"s_mov_b32 s44, -1\n\t"
"s_mov_b32 s45, 15\n\t"
"v_min_u32 v124, 35, v204\n\t"
"v_add_u32 v124, s49, v124\n\t"
"v_lshrrev_b32 v119, 3, v124\n\t"
"v_and_b32 v124, 7, v124\n\t"
"v_mul_u32_u24 v127, 0x300, v119\n\t"
"v_cmp_gt_u32 vcc, 9, v119\n\t"
"v_add_u32 v119, -9, v119\n\t"
"v_ashrrev_i32 v125, 31, v119\n\t"
"v_add_u32 v127, s42, v127\n\t"
"v_mad_i32_i24 v127, v125, s48, v127\n\t"
"v_and_b32 v125, 9, v125\n\t"
"v_add_u32 v119, v119, v125\n\t"
"v_and_b32 v125, 2, v119\n\t"
"v_xor_b32 v124, v124, v125\n\t"
"v_lshl_add_u32 v133, v124, 4, v127\n\t"
"s_cmp_eq_u32 s60, 0\n\t"
"s_cselect_b64 s[50:51], vcc, 0\n\t"
"v_cmp_eq_u32 vcc, 0, v119\n\t"
"v_cndmask_b32 v246, v133, 2.0, s[50:51]\n\t"
"s_and_b64 vcc, vcc, s[46:47]\n\t"
"v_cndmask_b32 v246, v246, 2.0, vcc\n\t"
"s_add_u32 m0, s43, 80896\n\t"
"s_mov_b64 exec, s[44:45]\n\t"
"buffer_load_dwordx4 v246, s[12:15], 0 offen offset:0 nt lds\n\t"
"s_mov_b64 exec, -1\n\t"
"s_mul_i32 s70, s66, 16\n\t"
"s_add_u32 s70, s70, 1900544\n\t"
"s_lshl_b32 s61, s73, 8\n\t"
"s_or_b32 s61, s61, s66\n\t"
"s_cmp_lt_u32 s61, 192\n\t"
"s_cselect_b32 s70, s70, 2.0\n\t"
"s_lshr_b32 s61, s66, 3\n\t"
"s_lshl2_add_u32 s61, s61, s73\n\t"
"s_lshl_b32 s60, s61, 8\n\t"
"s_cmp_lt_u32 s61, 96\n\t"
"s_cselect_b32 s60, s60, 2.0\n\t"
"v_lshrrev_b32 v118, 1, v204\n\t"
"v_mul_u32_u24 v118, 220928, v118\n\t"
"v_lshl_add_u32 v118, v204, 7, v118\n\t"
"s_mov_b64 exec, 63\n\t"
"buffer_load_dword v118, v118, s[52:55], s60 offen\n\t"
"s_mov_b64 exec, -1\n\t"
"buffer_load_dword v187, off, s[8:11], s70 sc1\n\t"
"v_add_u32 v107, 0, v204\n\t"
"v_mul_u32_u24 v104, 0xab, v107\n\t"
"v_lshrrev_b32 v104, 10, v104\n\t"
"v_mul_u32_u24 v104, 0x120, v104\n\t"
"v_lshl_add_u32 v104, v107, 4, v104\n\t"
"v_add_u32 v107, 64, v204\n\t"
"v_mul_u32_u24 v105, 0xab, v107\n\t"
"v_lshrrev_b32 v105, 10, v105\n\t"
"v_mul_u32_u24 v105, 0x120, v105\n\t"
"v_lshl_add_u32 v105, v107, 4, v105\n\t"
"v_add_u32 v107, 128, v204\n\t"
"v_mul_u32_u24 v106, 0xab, v107\n\t"
"v_lshrrev_b32 v106, 10, v106\n\t"
"v_mul_u32_u24 v106, 0x120, v106\n\t"
"v_lshl_add_u32 v106, v107, 4, v106\n\t"
"s_mul_i32 s74, s73, 41472\n\t"
"s_mul_i32 s72, s73, 96\n\t"
"s_add_u32 s74, s74, 60160\n\t"
"s_cmp_eq_u32 s73, 0\n\t"
"s_cselect_b32 s74, 53632, s74\n\t"
"s_add_u32 s75, s74, 12288\n\t"
"s_cmp_eq_u32 s73, 3\n\t"
"s_cselect_b32 s74, 16448, s74\n\t"
"s_cselect_b32 s75, 74368, s75\n\t"
"s_lshr_b32 s60, s66, 5\n\t"
"s_bfe_u32 s62, s66, 0x20003\n\t"
"s_and_b32 s61, s60, 1\n\t"
"s_lshl1_add_u32 s61, s73, s61\n\t"
"s_mul_i32 s62, s62, 288\n\t"
"s_add_u32 s61, s61, s62\n\t"
"s_lshr_b32 s60, s60, 1\n\t"
"s_mul_i32 s62, s60, 96\n\t"
"s_add_u32 s61, s61, s62\n\t"
"s_lshl_b32 s57, s61, 10\n\t"
"s_and_b32 s61, s66, 7\n\t"
"s_lshl_b32 s61, s61, 7\n\t"
"s_add_u32 s57, s57, s61\n\t"
"s_cmp_lt_u32 s73, 3\n\t"
"s_cselect_b32 s67, s66, 0xff\n\t"
"s_cmp_lt_u32 s67, 192\n\t"
"s_cselect_b32 s57, s57, 2.0\n\t"
"s_lshl_b32 s60, s73, 3\n\t"
"s_add_u32 s60, s60, 0\n\t"
"s_lshl_b32 s61, s73, 11\n\t"
"s_add_u32 s61, s61, 4096\n\t"
"v_lshl_add_u32 v46, v121, 6, s60\n\t"
"v_lshl_add_u32 v46, v44, 1, v46\n\t"
"v_lshl_add_u32 v42, v204, 1, s61\n\t"
"v_lshl_add_u32 v43, v204, 4, s61\n\t"
"v_mul_u32_u24 v44, 6144, v120\n\t"
"v_lshl_add_u32 v44, v121, 4, v44\n\t"
"v_mov_b32 v32, 0\n\t"
"v_mov_b32 v33, 0\n\t"
"v_mov_b32 v34, 0\n\t"
"v_mov_b32 v35, 0\n\t"
"v_mov_b32 v36, 0\n\t"
"v_mov_b32 v37, 0\n\t"
"v_mov_b32 v38, 0\n\t"
"v_mov_b32 v39, 0\n\t"
"s_waitcnt vmcnt(9)\n\t"
"s_barrier\n\t"
".p2align 3\n\t"
"ds_read_u16_d16_hi v32, v46 offset:0\n\t"
"ds_read_u16_d16_hi v33, v46 offset:512\n\t"
"ds_read_u16_d16_hi v34, v46 offset:1024\n\t"
"ds_read_u16_d16_hi v35, v46 offset:1536\n\t"
"ds_read_u16_d16_hi v36, v46 offset:2048\n\t"
"ds_read_u16_d16_hi v37, v46 offset:2560\n\t"
"ds_read_u16_d16_hi v38, v46 offset:3072\n\t"
"ds_read_u16_d16_hi v39, v46 offset:3584\n\t"
"s_waitcnt lgkmcnt(1)\n\t"
"v_add_f32 v48, v32, v38\n\t"
"s_waitcnt lgkmcnt(0)\n\t"
"v_add_f32 v49, v33, v39\n\t"
"v_add_f32 v50, v34, v40\n\t"
"v_add_f32 v51, v48, v35\n\t"
"v_sub_f32 v54, v48, v35\n\t"
"v_add_f32 v52, v49, v36\n\t"
"v_sub_f32 v55, v49, v36\n\t"
"v_add_f32 v53, v50, v37\n\t"
"v_sub_f32 v56, v50, v37\n\t"
"v_add_f32 v57, v32, v34\n\t"
"v_add_f32 v58, v51, v53\n\t"
"v_add_f32 v59, v54, v56\n\t"
"v_add_f32 v60, v38, v40\n\t"
"v_add_f32 v62, v57, v33\n\t"
"v_sub_f32 v63, v57, v33\n\t"
"v_add_f32 v64, v58, v52\n\t"
"v_sub_f32 v65, v58, v52\n\t"
"v_add_f32 v66, v59, v55\n\t"
"v_sub_f32 v67, v59, v55\n\t"
"v_add_f32 v68, v60, v39\n\t"
"v_sub_f32 v69, v60, v39\n\t"
"v_mul_f32 v71, 0.5, v62\n\t"
"v_mul_f32 v72, 0.5, v63\n\t"
"v_mul_f32 v74, 0.5, v51\n\t"
"v_mul_f32 v75, 0x3e800000, v64\n\t"
"v_mul_f32 v76, 0x3e800000, v65\n\t"
"v_mul_f32 v77, -0.5, v53\n\t"
"v_mul_f32 v79, 0.5, v66\n\t"
"v_mul_f32 v80, 0.5, v67\n\t"
"v_mul_f32 v83, -0.5, v68\n\t"
"v_mul_f32 v84, -0.5, v69\n\t"
".p2align 3\n\t"
"v_cvt_pk_bf16_f32 v86, v32, v71\n\t"
"v_cvt_pk_bf16_f32 v87, v72, -v34\n\t"
"v_cvt_pk_bf16_f32 v88, v74, v75\n\t"
"v_cvt_pk_bf16_f32 v89, v76, v77\n\t"
"v_cvt_pk_bf16_f32 v90, v54, v79\n\t"
"v_cvt_pk_bf16_f32 v91, v80, -v56\n\t"
"v_cvt_pk_bf16_f32 v92, -v38, v83\n\t"
"v_cvt_pk_bf16_f32 v93, v84, v40\n\t"
"ds_write_b16 v42, v86 offset:0\n\t"
"ds_write_b16_d16_hi v42, v86 offset:128\n\t"
"ds_write_b16 v42, v87 offset:256\n\t"
"ds_write_b16_d16_hi v42, v87 offset:384\n\t"
"ds_write_b16 v42, v88 offset:512\n\t"
"ds_write_b16_d16_hi v42, v88 offset:640\n\t"
"ds_write_b16 v42, v89 offset:768\n\t"
"ds_write_b16_d16_hi v42, v89 offset:896\n\t"
"ds_write_b16 v42, v90 offset:1024\n\t"
"ds_write_b16_d16_hi v42, v90 offset:1152\n\t"
"ds_write_b16 v42, v91 offset:1280\n\t"
"ds_write_b16_d16_hi v42, v91 offset:1408\n\t"
"ds_write_b16 v42, v92 offset:1536\n\t"
"ds_write_b16_d16_hi v42, v92 offset:1664\n\t"
"ds_write_b16 v42, v93 offset:1792\n\t"
"ds_write_b16_d16_hi v42, v93 offset:1920\n\t"
"ds_read_b128 v[96:99], v43 offset:0\n\t"
"ds_read_b128 v[100:103], v43 offset:1024\n\t"
"s_add_u32 s60, s57, 0\n\t"
"s_waitcnt lgkmcnt(1)\n\t"
"buffer_store_dwordx4 v[96:99], v44, s[8:11], s60 offen sc1\n\t"
"s_add_u32 s60, s57, 49152\n\t"
"s_waitcnt lgkmcnt(0)\n\t"
"buffer_store_dwordx4 v[100:103], v44, s[8:11], s60 offen sc1\n\t"
"v_lshrrev_b32 v123, 4, v204\n\t"
"v_bfe_u32 v124, v204, 2, 2\n\t"
"v_and_b32 v125, 3, v204\n\t"
"v_lshlrev_b32 v125, 3, v125\n\t"
"v_and_or_b32 v138, v204, 48, v124\n\t"
"v_lshrrev_b32 v139, 1, v125\n\t"
"v_or_b32 v138, v138, v139\n\t"
"s_add_u32 s63, s26, 1152\n\t"
"v_and_b32 v126, 48, v204\n\t"
"v_lshl_add_u32 v126, v121, 6, v126\n\t"
"v_and_b32 v127, 8, v204\n\t"
"v_mul_u32_u24 v134, 1028, v127\n\t"
"v_add_u32 v224, v134, v126\n\t"
"s_add_u32 m0, s74, 0\n\t"
"s_add_u32 s27, s72, 0\n\t"
"buffer_load_dwordx4 v104, s[52:55], s27 offen lds\n\t"
"v_lshl_add_u32 v127, v127, 2, v125\n\t"
"v_lshl_add_u32 v127, v124, 7, v127\n\t"
"v_add_u32 v126, 16, v204\n\t"
"v_and_b32 v126, 32, v126\n\t"
"v_lshl_add_u32 v127, v126, 1, v127\n\t"
"v_mul_u32_u24 v126, 1152, v123\n\t"
"v_add3_u32 v226, v126, v127, s63\n\t"
"v_min_u32 v126, 2, v124\n\t"
"v_cmp_gt_u32 vcc, 48, v204\n\t"
"v_mov_b32 v135, 0x3f80bf80\n\t"
"v_lshlrev_b32 v137, 4, v138\n\t"
"v_cndmask_b32 v126, v126, v123, vcc\n\t"
"v_cndmask_b32 v127, 4, v124, vcc\n\t"
"s_add_u32 m0, s74, 1024\n\t"
"s_add_u32 s27, s72, 0\n\t"
"buffer_load_dwordx4 v105, s[52:55], s27 offen lds\n\t"
"v_and_b32 v134, 1, v126\n\t"
"v_lshl_add_u32 v134, v134, 6, v125\n\t"
"v_lshl_add_u32 v134, v127, 7, v134\n\t"
"v_and_b32 v127, 2, v127\n\t"
"v_lshl_add_u32 v134, v127, 4, v134\n\t"
"v_mul_u32_u24 v126, 2304, v126\n\t"
"v_add3_u32 v227, v126, v134, s26\n\t"
"v_and_b32 v126, 24, v138\n\t"
"v_cmp_eq_u32 vcc, 8, v126\n\t"
"v_mov_b32 v134, 0xbf803f80\n\t"
"v_lshrrev_b32 v126, 3, v138\n\t"
"v_cndmask_b32 v134, v134, v135, vcc\n\t"
"s_add_u32 m0, s74, 2048\n\t"
"s_add_u32 s27, s72, 0\n\t"
"buffer_load_dwordx4 v106, s[52:55], s27 offen lds\n\t"
"v_xor_b32 v126, v126, v138\n\t"
"v_and_b32 v126, 4, v126\n\t"
"v_cmp_eq_u32 vcc, 0, v126\n\t"
"v_mov_b32 v135, 0\n\t"
"v_and_b32 v126, 3, v138\n\t"
"v_cndmask_b32 v134, 0, v134, vcc\n\t"
"v_cmp_eq_u32 vcc, 3, v126\n\t"
"v_lshrrev_b32 v136, 16, v134\n\t"
"v_lshlrev_b64 v[134:135], v137, v[134:135]\n\t"
"v_cndmask_b32 v136, 0, v136, vcc\n\t"
"v_accvgpr_write_b32 a220, v134\n\t"
"v_accvgpr_write_b32 a221, v135\n\t"
"v_accvgpr_write_b32 a222, v136\n\t"
"s_add_u32 m0, s74, 3072\n\t"
"s_add_u32 s27, s72, 12288\n\t"
"buffer_load_dwordx4 v104, s[52:55], s27 offen lds\n\t"
"v_accvgpr_write_b32 a223, 0\n\t"
"s_mov_b32 s48, 384\n\t"
"s_cmp_eq_u32 s65, 31\n\t"
"s_cselect_b64 s[40:41], 0xff, 0\n\t"
"s_cselect_b64 s[46:47], -1, 0\n\t"
"v_xad_u32 v236, v128, 64, s48\n\t"
"v_xad_u32 v237, v129, 64, s48\n\t"
"v_cndmask_b32 v237, v237, 2.0, s[40:41]\n\t"
"s_lshl_b64 s[40:41], s[40:41], 8\n\t"
"v_xad_u32 v238, v130, 64, s48\n\t"
"v_cndmask_b32 v238, v238, 2.0, s[40:41]\n\t"
"s_lshl_b64 s[40:41], s[40:41], 8\n\t"
"v_xad_u32 v239, v131, 64, s48\n\t"
"s_add_u32 m0, s74, 4096\n\t"
"s_add_u32 s27, s72, 12288\n\t"
"buffer_load_dwordx4 v105, s[52:55], s27 offen lds\n\t"
"v_cndmask_b32 v239, v239, 2.0, s[40:41]\n\t"
"s_lshl_b64 s[40:41], s[40:41], 40\n\t"
"v_xad_u32 v240, v245, 64, s48\n\t"
"v_cndmask_b32 v240, v240, 2.0, s[40:41]\n\t"
"v_cmp_eq_u32 vcc, 8, v119\n\t"
"v_xad_u32 v247, v133, 64, s48\n\t"
"s_and_b64 vcc, vcc, s[46:47]\n\t"
"v_cndmask_b32 v247, v247, 2.0, s[50:51]\n\t"
"v_cndmask_b32 v247, v247, 2.0, vcc\n\t"
"v_and_b32 v139, 7, v138\n\t"
"v_bfe_u32 v126, v138, 3, 1\n\t"
"v_lshrrev_b32 v127, 4, v138\n\t"
"s_add_u32 m0, s74, 5120\n\t"
"s_add_u32 s27, s72, 12288\n\t"
"buffer_load_dwordx4 v106, s[52:55], s27 offen lds\n\t"
"v_sub_u32 v127, v127, v126\n\t"
"v_mov_b32 v134, 0xbf803f80\n\t"
"v_mov_b32 v135, 0x3f80bf80\n\t"
"v_cmp_eq_u32 vcc, 0, v127\n\t"
"v_add_u32 v126, 4, v126\n\t"
"s_nop 0\n\t"
"v_cndmask_b32 v134, 0, v134, vcc\n\t"
"v_cmp_eq_u32 vcc, 1, v127\n\t"
"s_nop 1\n\t"
"v_cndmask_b32 v134, v134, v135, vcc\n\t"
"v_cmp_eq_u32 vcc, 7, v139\n\t"
"s_nop 1\n\t"
"v_cndmask_b32 v135, 0, v135, vcc\n\t"
"v_cmp_gt_u32 vcc, 48, v138\n\t"
"s_nop 1\n\t"
"v_cndmask_b32 v134, v135, v134, vcc\n\t"
"v_mov_b32 v135, 0\n\t"
"s_add_u32 m0, s74, 6144\n\t"
"s_add_u32 s27, s72, 442368\n\t"
"buffer_load_dwordx4 v104, s[52:55], s27 offen lds\n\t"
"v_cndmask_b32 v136, v126, v139, vcc\n\t"
"v_and_b32 v137, 1, v136\n\t"
"v_lshlrev_b32 v137, 4, v137\n\t"
"v_lshrrev_b32 v136, 1, v136\n\t"
"v_lshlrev_b64 v[134:135], v137, v[134:135]\n\t"
"v_cmp_eq_u32 vcc, 0, v136\n\t"
"s_nop 1\n\t"
"v_cndmask_b32 v140, 0, v134, vcc\n\t"
"v_accvgpr_write_b32 a224, v140\n\t"
"v_cndmask_b32 v141, 0, v135, vcc\n\t"
"v_cmp_eq_u32 vcc, 1, v136\n\t"
"v_and_b32 v140, 0xff7fff7f, v140\n\t"
"v_xor_b32 v148, 0x80008000, v140\n\t"
"v_cndmask_b32 v141, v141, v134, vcc\n\t"
"s_add_u32 m0, s74, 7168\n\t"
"s_add_u32 s27, s72, 442368\n\t"
"buffer_load_dwordx4 v105, s[52:55], s27 offen lds\n\t"
"v_cndmask_b32 v142, 0, v135, vcc\n\t"
"v_cmp_eq_u32 vcc, 2, v136\n\t"
"v_accvgpr_write_b32 a225, v141\n\t"
"v_accvgpr_write_b32 a216, v140\n\t"
"v_cndmask_b32 v142, v142, v134, vcc\n\t"
"v_cndmask_b32 v143, 0, v135, vcc\n\t"
"v_cmp_eq_u32 vcc, 3, v136\n\t"
"v_accvgpr_write_b32 a226, v142\n\t"
"v_and_b32 v141, 0xff7fff7f, v141\n\t"
"v_cndmask_b32 v143, v143, v134, vcc\n\t"
"v_accvgpr_write_b32 a227, v143\n\t"
"v_xor_b32 v149, 0x80008000, v141\n\t"
"s_add_u32 m0, s74, 8192\n\t"
"s_add_u32 s27, s72, 442368\n\t"
"buffer_load_dwordx4 v106, s[52:55], s27 offen lds\n\t"
"v_accvgpr_write_b32 a217, v141\n\t"
"v_and_b32 v142, 0xff7fff7f, v142\n\t"
"v_xor_b32 v150, 0x80008000, v142\n\t"
"v_accvgpr_write_b32 a218, v142\n\t"
"v_and_b32 v143, 0xff7fff7f, v143\n\t"
"v_xor_b32 v151, 0x80008000, v143\n\t"
"v_accvgpr_write_b32 a219, v143\n\t"
"s_mov_b32 s68, 3000\n\t"
"s_mul_i32 s61, s73, 294912\n\t"
"v_lshl_add_u32 v235, v204, 4, s61\n\t"
"v_and_b32 v107, 51, v204\n\t"
"v_mul_u32_u24 v107, 24, v107\n\t"
"v_and_b32 v47, 8, v204\n\t"
"s_add_u32 m0, s74, 9216\n\t"
"s_add_u32 s27, s72, 454656\n\t"
"buffer_load_dwordx4 v104, s[52:55], s27 offen lds\n\t"
"v_mad_u32_u24 v107, v47, 12, v107\n\t"
"v_and_b32 v47, 4, v204\n\t"
"v_mul_u32_u24 v47, 0x180, v47\n\t"
"v_add_u32 v107, v47, v107\n\t"
"v_add_u32 v47, s75, v107\n\t"
"v_add_u32 v107, s74, v107\n\t"
"v_lshrrev_b32 v112, 4, v204\n\t"
"v_bfe_u32 v113, v204, 2, 2\n\t"
"v_cmp_eq_u32 vcc, v112, v113\n\t"
"v_lshlrev_b32 v112, 4, v204\n\t"
"v_mov_b32 v113, 0x3e80\n\t"
"v_lshlrev_b32 v113, v112, v113\n\t"
"v_cndmask_b32 v113, 0, v113, vcc\n\t"
"s_add_u32 m0, s74, 10240\n\t"
"s_add_u32 s27, s72, 454656\n\t"
"buffer_load_dwordx4 v105, s[52:55], s27 offen lds\n\t"
"v_and_b32 v112, 2, v204\n\t"
"v_cmp_eq_u32 vcc, 0, v112\n\t"
"v_mov_b32 v108, 0\n\t"
"v_mov_b32 v109, 0\n\t"
"v_cndmask_b32 v110, 0, v113, vcc\n\t"
"v_cndmask_b32 v111, v113, 0, vcc\n\t"
"v_xor_b32 v114, 0x80008000, v110\n\t"
"v_xor_b32 v115, 0x80008000, v111\n\t"
"v_mov_b32 v112, 0\n\t"
"v_mov_b32 v113, 0\n\t"
"v_mov_b32 v116, 0\n\t"
"v_mov_b32 v117, 0\n\t"
"s_add_u32 m0, s74, 11264\n\t"
"s_add_u32 s27, s72, 454656\n\t"
"buffer_load_dwordx4 v106, s[52:55], s27 offen lds\n\t"
"s_waitcnt vmcnt(16)\n\t"
"s_waitcnt vmcnt(12)\n\t"
"v_readfirstlane_b32 s71, v187\n\t"
"s_barrier\n\t"
"s_add_u32 s71, s71, 1\n\t"
"v_mov_b32 v215, s71\n\t"
"s_mov_b64 exec, 1\n\t"
"buffer_store_dword v215, off, s[8:11], s70 sc1\n\t"
"s_mov_b64 exec, -1\n\t"
"v_add_u32 v248, 80896, v226\n\t"
"v_xor_b32 v249, 32, v248\n\t"
"v_xor_b32 v250, 64, v248\n\t"
"v_xor_b32 v251, 96, v248\n\t"
"ds_read_b64_tr_b16 a[176:177], v248 offset:0\n\t"
"ds_read_b64_tr_b16 a[178:179], v248 offset:512\n\t"
"ds_read_b64_tr_b16 a[184:185], v249 offset:0\n\t"
"ds_read_b64_tr_b16 a[186:187], v249 offset:512\n\t"
"s_add_u32 m0, s75, 0\n\t"
"s_add_u32 s27, s72, 221184\n\t"
"buffer_load_dwordx4 v104, s[52:55], s27 offen lds\n\t"
"ds_read_b64_tr_b16 a[180:181], v248 offset:1024\n\t"
"ds_read_b64_tr_b16 a[188:189], v249 offset:1024\n\t"
"s_add_u32 m0, s75, 1024\n\t"
"s_add_u32 s27, s72, 221184\n\t"
"buffer_load_dwordx4 v105, s[52:55], s27 offen lds\n\t"
"ds_read_b64_tr_b16 a[192:193], v250 offset:0\n\t"
"ds_read_b64_tr_b16 a[194:195], v250 offset:512\n\t"
"ds_read_b64_tr_b16 a[200:201], v251 offset:0\n\t"
"ds_read_b64_tr_b16 a[202:203], v251 offset:512\n\t"
"s_add_u32 m0, s75, 2048\n\t"
"s_add_u32 s27, s72, 221184\n\t"
"buffer_load_dwordx4 v106, s[52:55], s27 offen lds\n\t"
"ds_read_b64_tr_b16 a[196:197], v250 offset:1024\n\t"
"ds_read_b64_tr_b16 a[204:205], v251 offset:1024\n\t"
"s_add_u32 m0, s75, 3072\n\t"
"s_add_u32 s27, s72, 233472\n\t"
"buffer_load_dwordx4 v104, s[52:55], s27 offen lds\n\t"
"s_waitcnt lgkmcnt(10)\n\t"
"v_mfma_f32_16x16x32_bf16 v[0:3], a[220:223], a[176:179], 0\n\t"
"s_waitcnt lgkmcnt(8)\n\t"
"v_mfma_f32_16x16x32_bf16 v[4:7], a[220:223], a[184:187], 0\n\t"
"s_waitcnt lgkmcnt(7)\n\t"
"v_mfma_f32_16x16x32_bf16 v[8:11], a[220:223], a[178:181], 0\n\t"
"s_waitcnt lgkmcnt(6)\n\t"
"v_mfma_f32_16x16x32_bf16 v[12:15], a[220:223], a[186:189], 0\n\t"
"s_add_u32 m0, s75, 4096\n\t"
"s_add_u32 s27, s72, 233472\n\t"
"buffer_load_dwordx4 v105, s[52:55], s27 offen lds\n\t"
"s_add_u32 m0, s75, 5120\n\t"
"s_add_u32 s27, s72, 233472\n\t"
"buffer_load_dwordx4 v106, s[52:55], s27 offen lds\n\t"
"s_waitcnt lgkmcnt(4)\n\t"
"v_mfma_f32_16x16x32_bf16 v[16:19], a[220:223], a[192:195], 0\n\t"
"s_waitcnt lgkmcnt(2)\n\t"
"v_mfma_f32_16x16x32_bf16 v[20:23], a[220:223], a[200:203], 0\n\t"
"s_waitcnt lgkmcnt(1)\n\t"
"v_mfma_f32_16x16x32_bf16 v[24:27], a[220:223], a[194:197], 0\n\t"
"s_waitcnt lgkmcnt(0)\n\t"
"v_mfma_f32_16x16x32_bf16 v[28:31], a[220:223], a[202:205], 0\n\t"
"s_add_u32 m0, s26, 124544\n\t"
"s_nop 0\n\t"
"buffer_load_dwordx4 v241, s[12:15], s22 offen offset:128 nt lds\n\t"
"buffer_load_dwordx4 v242, s[12:15], s22 offen offset:1152 nt lds\n\t"
"v_cvt_pk_bf16_f32 v0, v0, v4\n\t"
"v_cvt_pk_bf16_f32 v1, v1, v5\n\t"
"v_cvt_pk_bf16_f32 v2, v2, v6\n\t"
"v_cvt_pk_bf16_f32 v3, v3, v7\n\t"
"s_lshl_b32 m0, s73, 9\n\t"
"s_nop 0\n\t"
".p2align 3\n\t"
"ds_write_addtid_b32 v0 offset:0\n\t"
"ds_write_addtid_b32 v1 offset:8224\n\t"
"ds_write_addtid_b32 v2 offset:2048\n\t"
"ds_write_addtid_b32 v3 offset:10272\n\t"
"v_cvt_pk_bf16_f32 v8, v8, v12\n\t"
"v_cvt_pk_bf16_f32 v9, v9, v13\n\t"
"v_cvt_pk_bf16_f32 v10, v10, v14\n\t"
"v_cvt_pk_bf16_f32 v11, v11, v15\n\t"
"ds_write_addtid_b32 v8 offset:256\n\t"
"ds_write_addtid_b32 v9 offset:8480\n\t"
"ds_write_addtid_b32 v10 offset:2304\n\t"
"ds_write_addtid_b32 v11 offset:10528\n\t"
"v_cvt_pk_bf16_f32 v16, v16, v20\n\t"
"v_cvt_pk_bf16_f32 v17, v17, v21\n\t"
"v_cvt_pk_bf16_f32 v18, v18, v22\n\t"
"v_cvt_pk_bf16_f32 v19, v19, v23\n\t"
"ds_write_addtid_b32 v16 offset:4096\n\t"
"ds_write_addtid_b32 v17 offset:12320\n\t"
"ds_write_addtid_b32 v18 offset:6144\n\t"
"ds_write_addtid_b32 v19 offset:14368\n\t"
"v_cvt_pk_bf16_f32 v24, v24, v28\n\t"
"v_cvt_pk_bf16_f32 v25, v25, v29\n\t"
"v_cvt_pk_bf16_f32 v26, v26, v30\n\t"
"v_cvt_pk_bf16_f32 v27, v27, v31\n\t"
"ds_write_addtid_b32 v24 offset:4352\n\t"
"ds_write_addtid_b32 v25 offset:12576\n\t"
"ds_write_addtid_b32 v26 offset:6400\n\t"
"ds_write_addtid_b32 v27 offset:14624\n\t"
"v_add_u32 v248, 80896, v227\n\t"
"v_xor_b32 v249, 32, v248\n\t"
"v_xor_b32 v250, 64, v248\n\t"
"v_xor_b32 v251, 96, v248\n\t"
"ds_read_b64_tr_b16 a[176:177], v248 offset:0\n\t"
"ds_read_b64_tr_b16 a[178:179], v248 offset:512\n\t"
"ds_read_b64_tr_b16 a[180:181], v249 offset:0\n\t"
"ds_read_b64_tr_b16 a[182:183], v249 offset:512\n\t"
"s_add_u32 m0, s26, 124544\n\t"
"s_nop 0\n\t"
".p2align 3\n\t"
"buffer_load_dwordx4 v243, s[12:15], s22 offen offset:2176 nt lds\n\t"
"ds_read_b64_tr_b16 a[184:185], v250 offset:0\n\t"
"ds_read_b64_tr_b16 a[186:187], v250 offset:512\n\t"
"ds_read_b64_tr_b16 a[188:189], v251 offset:0\n\t"
"ds_read_b64_tr_b16 a[190:191], v251 offset:512\n\t"
"buffer_load_dwordx4 v244, s[12:15], s22 offen offset:3200 nt lds\n\t"
"ds_read_b64_tr_b16 a[192:193], v248 offset:1152\n\t"
"ds_read_b64_tr_b16 a[194:195], v248 offset:1664\n\t"
"ds_read_b64_tr_b16 a[196:197], v249 offset:1152\n\t"
"ds_read_b64_tr_b16 a[198:199], v249 offset:1664\n\t"
"s_mov_b64 exec, s[36:37]\n\t"
"buffer_load_dwordx4 v245, s[12:15], s22 offen offset:3712 nt lds\n\t"
"s_mov_b64 exec, -1\n\t"
"ds_read_b64_tr_b16 a[200:201], v250 offset:1152\n\t"
"ds_read_b64_tr_b16 a[202:203], v250 offset:1664\n\t"
"ds_read_b64_tr_b16 a[204:205], v251 offset:1152\n\t"
"ds_read_b64_tr_b16 a[206:207], v251 offset:1664\n\t"
"s_add_u32 m0, s43, 122240\n\t"
"s_mov_b64 exec, s[44:45]\n\t"
"buffer_load_dwordx4 v246, s[12:15], 0 offen offset:128 nt lds\n\t"
"s_mov_b64 exec, -1\n\t"
"s_waitcnt vmcnt(19)\n\t"
".p2align 3\n\t"
"ds_read_b64_tr_b16 a[48:49], v107 offset:0\n\t"
"ds_read_b64_tr_b16 a[50:51], v107 offset:192\n\t"
"ds_read_b64_tr_b16 a[52:53], v107 offset:3072\n\t"
"ds_read_b64_tr_b16 a[54:55], v107 offset:3264\n\t"
"ds_read_b64_tr_b16 a[56:57], v107 offset:8\n\t"
"ds_read_b64_tr_b16 a[58:59], v107 offset:200\n\t"
"ds_read_b64_tr_b16 a[60:61], v107 offset:3080\n\t"
"ds_read_b64_tr_b16 a[62:63], v107 offset:3272\n\t"
"ds_read_b64_tr_b16 a[64:65], v107 offset:16\n\t"
"ds_read_b64_tr_b16 a[66:67], v107 offset:208\n\t"
"ds_read_b64_tr_b16 a[68:69], v107 offset:3088\n\t"
"ds_read_b64_tr_b16 a[70:71], v107 offset:3280\n\t"
"s_waitcnt vmcnt(13)\n\t"
".p2align 3\n\t"
"ds_read_b64_tr_b16 a[72:73], v107 offset:6144\n\t"
"ds_read_b64_tr_b16 a[74:75], v107 offset:6336\n\t"
"ds_read_b64_tr_b16 a[76:77], v107 offset:9216\n\t"
"ds_read_b64_tr_b16 a[78:79], v107 offset:9408\n\t"
"ds_read_b64_tr_b16 a[80:81], v107 offset:6152\n\t"
"ds_read_b64_tr_b16 a[82:83], v107 offset:6344\n\t"
"ds_read_b64_tr_b16 a[84:85], v107 offset:9224\n\t"
"ds_read_b64_tr_b16 a[86:87], v107 offset:9416\n\t"
"ds_read_b64_tr_b16 a[88:89], v107 offset:6160\n\t"
"ds_read_b64_tr_b16 a[90:91], v107 offset:6352\n\t"
"ds_read_b64_tr_b16 a[92:93], v107 offset:9232\n\t"
"ds_read_b64_tr_b16 a[94:95], v107 offset:9424\n\t"
"s_waitcnt lgkmcnt(15)\n\t"
"v_mfma_f32_16x16x32_bf16 v[0:3], v[110:113], a[48:51], 0\n\t"
"s_waitcnt lgkmcnt(10)\n\t"
"v_mfma_f32_16x16x32_bf16 v[0:3], v[110:113], a[72:75], v[0:3]\n\t"
"v_mfma_f32_16x16x32_bf16 v[8:11], v[108:111], a[48:51], 0\n\t"
"v_mfma_f32_16x16x32_bf16 v[8:11], v[108:111], a[72:75], v[8:11]\n\t"
"s_add_u32 m0, s26, 34944\n\t"
"s_nop 0\n\t"
"buffer_load_dwordx4 v241, s[12:15], s22 offen offset:256 nt lds\n\t"
"v_mfma_f32_16x16x32_bf16 v[16:19], v[110:113], a[52:55], 0\n\t"
"s_waitcnt lgkmcnt(8)\n\t"
"v_mfma_f32_16x16x32_bf16 v[16:19], v[110:113], a[76:79], v[16:19]\n\t"
"v_mfma_f32_16x16x32_bf16 v[24:27], v[108:111], a[52:55], 0\n\t"
"v_mfma_f32_16x16x32_bf16 v[24:27], v[108:111], a[76:79], v[24:27]\n\t"
"buffer_load_dwordx4 v242, s[12:15], s22 offen offset:1280 nt lds\n\t"
"s_waitcnt vmcnt(8)\n\t"
".p2align 3\n\t"
"ds_read_b64_tr_b16 a[0:1], v47 offset:0\n\t"
"ds_read_b64_tr_b16 a[2:3], v47 offset:192\n\t"
"ds_read_b64_tr_b16 a[4:5], v47 offset:3072\n\t"
"ds_read_b64_tr_b16 a[6:7], v47 offset:3264\n\t"
"ds_read_b64_tr_b16 a[8:9], v47 offset:8\n\t"
"ds_read_b64_tr_b16 a[10:11], v47 offset:200\n\t"
"ds_read_b64_tr_b16 a[12:13], v47 offset:3080\n\t"
"ds_read_b64_tr_b16 a[14:15], v47 offset:3272\n\t"
"ds_read_b64_tr_b16 a[16:17], v47 offset:16\n\t"
"ds_read_b64_tr_b16 a[18:19], v47 offset:208\n\t"
"ds_read_b64_tr_b16 a[20:21], v47 offset:3088\n\t"
"ds_read_b64_tr_b16 a[22:23], v47 offset:3280\n\t"
"v_mfma_f32_16x16x32_bf16 v[32:35], v[110:113], a[56:59], 0\n\t"
"s_waitcnt lgkmcnt(15)\n\t"
"v_mfma_f32_16x16x32_bf16 v[32:35], v[110:113], a[80:83], v[32:35]\n\t"
"v_mfma_f32_16x16x32_bf16 v[96:99], v[108:111], a[56:59], 0\n\t"
"v_mfma_f32_16x16x32_bf16 v[96:99], v[108:111], a[80:83], v[96:99]\n\t"
"buffer_load_dwordx4 v243, s[12:15], s22 offen offset:2304 nt lds\n\t"
"v_mfma_f32_16x16x32_bf16 v[120:123], v[110:113], a[60:63], 0\n\t"
"v_mfma_f32_16x16x32_bf16 v[120:123], v[110:113], a[84:87], v[120:123]\n\t"
"v_mfma_f32_16x16x32_bf16 v[128:131], v[108:111], a[60:63], 0\n\t"
"v_mfma_f32_16x16x32_bf16 v[128:131], v[108:111], a[84:87], v[128:131]\n\t"
"buffer_load_dwordx4 v244, s[12:15], s22 offen offset:3328 nt lds\n\t"
"v_mfma_f32_16x16x32_bf16 v[136:139], v[110:113], a[64:67], 0\n\t"
"s_waitcnt lgkmcnt(14)\n\t"
"v_mfma_f32_16x16x32_bf16 v[136:139], v[110:113], a[88:91], v[136:139]\n\t"
"v_mfma_f32_16x16x32_bf16 v[152:155], v[108:111], a[64:67], 0\n\t"
"v_mfma_f32_16x16x32_bf16 v[152:155], v[108:111], a[88:91], v[152:155]\n\t"
"s_mov_b64 exec, s[36:37]\n\t"
"buffer_load_dwordx4 v245, s[12:15], s22 offen offset:3840 nt lds\n\t"
"s_mov_b64 exec, -1\n\t"
"v_mfma_f32_16x16x32_bf16 v[160:163], v[110:113], a[68:71], 0\n\t"
"s_waitcnt lgkmcnt(12)\n\t"
"v_mfma_f32_16x16x32_bf16 v[160:163], v[110:113], a[92:95], v[160:163]\n\t"
"v_mfma_f32_16x16x32_bf16 v[168:171], v[108:111], a[68:71], 0\n\t"
"v_mfma_f32_16x16x32_bf16 v[168:171], v[108:111], a[92:95], v[168:171]\n\t"
"s_add_u32 m0, s43, 32640\n\t"
"s_mov_b64 exec, s[44:45]\n\t"
"buffer_load_dwordx4 v246, s[12:15], 0 offen offset:256 nt lds\n\t"
"s_mov_b64 exec, -1\n\t"
"s_waitcnt lgkmcnt(10)\n\t"
"v_mfma_f32_16x16x32_bf16 v[4:7], v[110:113], a[0:3], v[0:3]\n\t"
"v_mfma_f32_16x16x32_bf16 v[0:3], v[114:117], a[0:3], v[0:3]\n\t"
"v_mfma_f32_16x16x32_bf16 v[12:15], v[108:111], a[0:3], v[8:11]\n\t"
"v_mfma_f32_16x16x32_bf16 v[8:11], v[112:115], a[0:3], v[8:11]\n\t"
"s_waitcnt lgkmcnt(8)\n\t"
"v_mfma_f32_16x16x32_bf16 v[20:23], v[110:113], a[4:7], v[16:19]\n\t"
"v_mfma_f32_16x16x32_bf16 v[16:19], v[114:117], a[4:7], v[16:19]\n\t"
"v_mfma_f32_16x16x32_bf16 v[28:31], v[108:111], a[4:7], v[24:27]\n\t"
"s_nop 0\n\t"
"v_cvt_pk_bf16_f32 v48, v4, v5\n\t"
"v_cvt_pk_bf16_f32 v49, v6, v7\n\t"
"v_mfma_f32_16x16x32_bf16 v[24:27], v[112:115], a[4:7], v[24:27]\n\t"
"v_cvt_pk_bf16_f32 v72, v0, v1\n\t"
"v_cvt_pk_bf16_f32 v73, v2, v3\n\t"
"s_waitcnt lgkmcnt(6)\n\t"
"v_mfma_f32_16x16x32_bf16 v[36:39], v[110:113], a[8:11], v[32:35]\n\t"
"v_cvt_pk_bf16_f32 v50, v12, v13\n\t"
"v_cvt_pk_bf16_f32 v51, v14, v15\n\t"
"v_mfma_f32_16x16x32_bf16 v[32:35], v[114:117], a[8:11], v[32:35]\n\t"
"v_cvt_pk_bf16_f32 v74, v8, v9\n\t"
"v_cvt_pk_bf16_f32 v75, v10, v11\n\t"
"v_mfma_f32_16x16x32_bf16 v[100:103], v[108:111], a[8:11], v[96:99]\n\t"
"v_cvt_pk_bf16_f32 v52, v20, v21\n\t"
"v_cvt_pk_bf16_f32 v53, v22, v23\n\t"
"v_mfma_f32_16x16x32_bf16 v[96:99], v[112:115], a[8:11], v[96:99]\n\t"
"v_cvt_pk_bf16_f32 v76, v16, v17\n\t"
"v_cvt_pk_bf16_f32 v77, v18, v19\n\t"
"s_waitcnt lgkmcnt(4)\n\t"
"v_mfma_f32_16x16x32_bf16 v[124:127], v[110:113], a[12:15], v[120:123]\n\t"
"v_cvt_pk_bf16_f32 v54, v28, v29\n\t"
"v_cvt_pk_bf16_f32 v55, v30, v31\n\t"
"v_mfma_f32_16x16x32_bf16 v[120:123], v[114:117], a[12:15], v[120:123]\n\t"
"v_cvt_pk_bf16_f32 v78, v24, v25\n\t"
"v_cvt_pk_bf16_f32 v79, v26, v27\n\t"
"v_mfma_f32_16x16x32_bf16 v[132:135], v[108:111], a[12:15], v[128:131]\n\t"
"v_cvt_pk_bf16_f32 v56, v36, v37\n\t"
"v_cvt_pk_bf16_f32 v57, v38, v39\n\t"
"v_mfma_f32_16x16x32_bf16 v[128:131], v[112:115], a[12:15], v[128:131]\n\t"
"v_cvt_pk_bf16_f32 v80, v32, v33\n\t"
"v_cvt_pk_bf16_f32 v81, v34, v35\n\t"
"s_waitcnt lgkmcnt(2)\n\t"
"v_mfma_f32_16x16x32_bf16 v[140:143], v[110:113], a[16:19], v[136:139]\n\t"
"v_cvt_pk_bf16_f32 v58, v100, v101\n\t"
"v_cvt_pk_bf16_f32 v59, v102, v103\n\t"
"v_mfma_f32_16x16x32_bf16 v[136:139], v[114:117], a[16:19], v[136:139]\n\t"
"v_cvt_pk_bf16_f32 v82, v96, v97\n\t"
"v_cvt_pk_bf16_f32 v83, v98, v99\n\t"
"v_mfma_f32_16x16x32_bf16 v[156:159], v[108:111], a[16:19], v[152:155]\n\t"
"v_cvt_pk_bf16_f32 v60, v124, v125\n\t"
"v_cvt_pk_bf16_f32 v61, v126, v127\n\t"
"v_mfma_f32_16x16x32_bf16 v[152:155], v[112:115], a[16:19], v[152:155]\n\t"
"v_cvt_pk_bf16_f32 v84, v120, v121\n\t"
"v_cvt_pk_bf16_f32 v85, v122, v123\n\t"
"s_waitcnt lgkmcnt(0)\n\t"
"v_mfma_f32_16x16x32_bf16 v[164:167], v[110:113], a[20:23], v[160:163]\n\t"
"v_cvt_pk_bf16_f32 v62, v132, v133\n\t"
"v_cvt_pk_bf16_f32 v63, v134, v135\n\t"
"v_mfma_f32_16x16x32_bf16 v[160:163], v[114:117], a[20:23], v[160:163]\n\t"
"v_cvt_pk_bf16_f32 v86, v128, v129\n\t"
"v_cvt_pk_bf16_f32 v87, v130, v131\n\t"
"v_mfma_f32_16x16x32_bf16 v[172:175], v[108:111], a[20:23], v[168:171]\n\t"
"v_cvt_pk_bf16_f32 v64, v140, v141\n\t"
"v_cvt_pk_bf16_f32 v65, v142, v143\n\t"
"v_mfma_f32_16x16x32_bf16 v[168:171], v[112:115], a[20:23], v[168:171]\n\t"
".p2align 3\n\t"
"v_cvt_pk_bf16_f32 v88, v136, v137\n\t"
"v_cvt_pk_bf16_f32 v89, v138, v139\n\t"
"v_cvt_pk_bf16_f32 v66, v156, v157\n\t"
"v_cvt_pk_bf16_f32 v67, v158, v159\n\t"
"v_cvt_pk_bf16_f32 v90, v152, v153\n\t"
"v_cvt_pk_bf16_f32 v91, v154, v155\n\t"
"v_cvt_pk_bf16_f32 v68, v164, v165\n\t"
"v_cvt_pk_bf16_f32 v69, v166, v167\n\t"
"v_cvt_pk_bf16_f32 v92, v160, v161\n\t"
"v_cvt_pk_bf16_f32 v93, v162, v163\n\t"
"v_cvt_pk_bf16_f32 v70, v172, v173\n\t"
"v_cvt_pk_bf16_f32 v71, v174, v175\n\t"
"v_cvt_pk_bf16_f32 v94, v168, v169\n\t"
"v_cvt_pk_bf16_f32 v95, v170, v171\n\t"
"s_barrier\n\t"
"v_mfma_f32_16x16x32_bf16 v[192:195], a[216:219], a[176:179], 0\n\t"
"s_add_u32 m0, s43, 142976\n\t"
"s_mov_b64 exec, s[44:45]\n\t"
"buffer_load_dwordx4 v247, s[12:15], 0 offen offset:128 nt lds\n\t"
"s_mov_b64 exec, -1\n\t"
"v_mfma_f32_16x16x32_bf16 v[196:199], a[216:219], a[180:183], 0\n\t"
"s_add_u32 m0, s26, 145280\n\t"
"ds_read_b128 a[144:147], v224 offset:0\n\t"
"ds_read_b128 a[168:171], v224 offset:5120\n\t"
"ds_read_b128 a[156:159], v224 offset:1536\n\t"
"v_mfma_f32_16x16x32_bf16 v[200:203], a[216:219], a[184:187], 0\n\t"
"s_mov_b32 s16, s80\n\t"
"ds_read_b128 a[160:163], v224 offset:4096\n\t"
"ds_read_b128 a[148:151], v224 offset:512\n\t"
"v_mfma_f32_16x16x32_bf16 v[204:207], a[216:219], a[188:191], 0\n\t"
"s_and_b32 s17, s81, 0xffff\n\t"
"ds_read_b128 a[164:167], v224 offset:4608\n\t"
"ds_read_b128 a[152:155], v224 offset:1024\n\t"
"v_mfma_f32_16x16x32_bf16 v[208:211], v[148:151], a[192:195], 0\n\t"
"s_mov_b32 s18, 100663296\n\t"
"ds_read_b128 a[172:175], v224 offset:5632\n\t"
"v_add_u32 v252, 4096, v235\n\t"
"v_mfma_f32_16x16x32_bf16 v[212:215], v[148:151], a[196:199], 0\n\t"
"s_waitcnt lgkmcnt(4)\n\t"
"s_mov_b32 s19, 0x20000\n\t"
"s_mov_b32 s21, 4\n\t"
"s_mov_b32 s38, -2.0\n\t"
"v_mfma_f32_16x16x32_bf16 v[96:99], v[48:51], a[144:147], 0\n\t"
"s_mov_b32 s39, -2.0\n\t"
"v_mbcnt_lo_u32_b32 v176, -1, 0\n\t"
"v_mbcnt_hi_u32_b32 v176, -1, v176\n\t"
"v_mfma_f32_16x16x32_bf16 v[96:99], v[52:55], a[160:163], v[96:99]\n\t"
"buffer_load_dwordx4 v236, s[12:15], s22 offen offset:128 nt lds\n\t"
"v_bfe_u32 v156, v176, 2, 2\n\t"
"v_and_b32 v157, 3, v176\n\t"
"v_lshlrev_b32 v157, 3, v157\n\t"
"v_and_or_b32 v170, v176, 48, v156\n\t"
"v_lshrrev_b32 v171, 1, v157\n\t"
"v_or_b32 v170, v170, v171\n\t"
"v_and_b32 v158, 24, v170\n\t"
"v_cmp_eq_u32 vcc, 8, v158\n\t"
"v_mov_b32 v166, 0x3f80\n\t"
"v_mov_b32 v167, 0xbf80\n\t"
"v_cndmask_b32 v168, v167, v166, vcc\n\t"
"v_cndmask_b32 v166, v166, v167, vcc\n\t"
"v_cvt_pk_bf16_f32 v195, v195, v199\n\t"
"v_mfma_f32_16x16x32_bf16 v[112:115], v[56:59], a[144:147], 0\n\t"
"s_lshl_b32 m0, s73, 9\n\t"
"v_lshrrev_b32 v158, 3, v170\n\t"
"v_cvt_pk_bf16_f32 v192, v192, v196\n\t"
"v_mfma_f32_16x16x32_bf16 v[112:115], v[60:63], a[160:163], v[112:115]\n\t"
"ds_write_addtid_b32 v192 offset:16448\n\t"
"v_xor_b32 v158, v158, v170\n\t"
"v_and_b32 v158, 4, v158\n\t"
"v_cmp_eq_u32 vcc, 0, v158\n\t"
"v_mov_b32 v167, 0\n\t"
"v_mov_b32 v169, 0\n\t"
"v_cndmask_b32 v166, 0, v166, vcc\n\t"
"v_cndmask_b32 v168, 0, v168, vcc\n\t"
"v_cvt_pk_bf16_f32 v201, v201, v205\n\t"
"v_mfma_f32_16x16x32_bf16 v[128:131], v[64:67], a[144:147], 0\n\t"
"ds_read_b128 a[144:147], v224 offset:2048\n\t"
"v_lshlrev_b32 v158, 4, v170\n\t"
"v_mfma_f32_16x16x32_bf16 v[128:131], v[68:71], a[160:163], v[128:131]\n\t"
"s_waitcnt lgkmcnt(4)\n\t"
"ds_write_addtid_b32 v195 offset:24928\n\t"
"v_lshlrev_b64 v[166:167], v158, v[166:167]\n\t"
"v_lshlrev_b64 v[168:169], v158, v[168:169]\n\t"
"v_cvt_pk_bf16_f32 v194, v194, v198\n\t"
"v_cvt_pk_bf16_f32 v200, v200, v204\n\t"
"v_cvt_pk_bf16_f32 v210, v210, v214\n\t"
"v_mfma_f32_16x16x32_bf16 v[132:135], v[64:67], a[148:151], 0\n\t"
"ds_write_addtid_b32 v194 offset:24672\n\t"
"v_accvgpr_write_b32 a212, v166\n\t"
"v_mfma_f32_16x16x32_bf16 v[132:135], v[68:71], a[164:167], v[132:135]\n\t"
"ds_read_b128 a[160:163], v224 offset:6144\n\t"
"v_accvgpr_write_b32 a213, v167\n\t"
"v_accvgpr_write_b32 a214, v168\n\t"
"v_cvt_pk_bf16_f32 v209, v209, v213\n\t"
"v_cvt_pk_bf16_f32 v211, v211, v215\n\t"
"v_mfma_f32_16x16x32_bf16 v[116:119], v[56:59], a[148:151], 0\n\t"
"ds_write_addtid_b32 v209 offset:18752\n\t"
"v_cvt_pk_bf16_f32 v203, v203, v207\n\t"
"v_mfma_f32_16x16x32_bf16 v[116:119], v[60:63], a[164:167], v[116:119]\n\t"
"ds_write_addtid_b32 v203 offset:29024\n\t"
"buffer_load_dword v190, v189, s[8:11], 0 offen sc1\n\t"
"v_accvgpr_write_b32 a215, v169\n\t"
"s_waitcnt vmcnt(9)\n\t"
"s_barrier\n\t"
"v_mfma_f32_16x16x32_bf16 v[216:219], v[148:151], a[200:203], 0\n\t"
"ds_write_addtid_b32 v211 offset:26976\n\t"
"v_lshlrev_b32 v158, 15, v176\n\t"
"v_cvt_pk_bf16_f32 v208, v208, v212\n\t"
"v_mfma_f32_16x16x32_bf16 v[100:103], v[48:51], a[148:151], 0\n\t"
"s_add_u32 m0, s26, 145280\n\t"
"ds_read_b64_tr_b16 a[176:177], v248 offset:41472\n\t"
"v_and_b32 v158, 0x8000, v158\n\t"
"v_mfma_f32_16x16x32_bf16 v[100:103], v[52:55], a[164:167], v[100:103]\n\t"
"s_waitcnt lgkmcnt(10)\n\t"
"ds_read_b64_tr_b16 a[178:179], v248 offset:41984\n\t"
"v_or_b32 v167, 0x3f803f80, v158\n\t"
"v_cvt_pk_bf16_f32 v202, v202, v206\n\t"
"v_mfma_f32_16x16x32_bf16 v[104:107], v[48:51], a[152:155], 0\n\t"
"ds_read_b128 a[148:151], v224 offset:2560\n\t"
"v_and_b32 v158, 2, v176\n\t"
"v_mfma_f32_16x16x32_bf16 v[104:107], v[52:55], a[168:171], v[104:107]\n\t"
"ds_read_b128 a[164:167], v224 offset:6656\n\t"
"buffer_load_dwordx4 v237, s[12:15], s22 offen offset:1152 nt lds\n\t"
"v_mfma_f32_16x16x32_bf16 v[220:223], v[148:151], a[204:207], 0\n\t"
"s_lshl_b32 m0, s73, 9\n\t"
"ds_read_b64_tr_b16 a[180:181], v249 offset:41472\n\t"
"v_mul_u32_u24 v158, 0x4000, v158\n\t"
"v_mfma_f32_16x16x32_bf16 v[120:123], v[56:59], a[152:155], 0\n\t"
"ds_read_b64_tr_b16 a[182:183], v249 offset:41984\n\t"
"v_lshl_or_b32 v158, v158, 16, v158\n\t"
"v_mfma_f32_16x16x32_bf16 v[120:123], v[60:63], a[168:171], v[120:123]\n\t"
"ds_write_addtid_b32 v210 offset:26720\n\t"
"v_xor_b32 v166, v167, v158\n\t"
"v_mfma_f32_16x16x32_bf16 v[136:139], v[64:67], a[152:155], 0\n\t"
"ds_read_b64_tr_b16 a[184:185], v250 offset:41472\n\t"
"v_accvgpr_write_b32 a210, v166\n\t"
"v_mfma_f32_16x16x32_bf16 v[136:139], v[68:71], a[168:171], v[136:139]\n\t"
"s_waitcnt lgkmcnt(15)\n\t"
"ds_read_b64_tr_b16 a[186:187], v250 offset:41984\n\t"
"v_cvt_pk_bf16_f32 v193, v193, v197\n\t"
"v_mfma_f32_16x16x32_bf16 v[140:143], v[64:67], a[156:159], 0\n\t"
"ds_read_b64_tr_b16 a[188:189], v251 offset:41472\n\t"
"v_accvgpr_write_b32 a211, v167\n\t"
"v_mfma_f32_16x16x32_bf16 v[140:143], v[68:71], a[172:175], v[140:143]\n\t"
"s_add_u32 s61, s26, 21888\n\t"
"ds_read_b128 a[168:171], v224 offset:7168\n\t"
"ds_write_addtid_b32 v201 offset:20800\n\t"
"ds_write_addtid_b32 v202 offset:28768\n\t"
"ds_read_b64_tr_b16 a[190:191], v251 offset:41984\n\t"
"ds_read_b64_tr_b16 a[192:193], v248 offset:42624\n\t"
"ds_read_b64_tr_b16 a[194:195], v248 offset:43136\n\t"
"ds_read_b64_tr_b16 a[196:197], v249 offset:42624\n\t"
"v_lshrrev_b32 v155, 4, v176\n\t"
"v_and_b32 v158, 1, v156\n\t"
"v_cvt_pk_bf16_f32 v217, v217, v221\n\t"
"v_mfma_f32_16x16x32_bf16 v[124:127], v[56:59], a[156:159], 0\n\t"
"ds_write_addtid_b32 v208 offset:18496\n\t"
"v_lshrrev_b32 v159, 1, v156\n\t"
"v_mfma_f32_16x16x32_bf16 v[124:127], v[60:63], a[172:175], v[124:127]\n\t"
"ds_read_b128 a[152:155], v224 offset:3072\n\t"
"v_cvt_pk_bf16_f32 v216, v216, v220\n\t"
"v_cvt_pk_bf16_f32 v218, v218, v222\n\t"
"v_cvt_pk_bf16_f32 v219, v219, v223\n\t"
"v_mfma_f32_16x16x32_bf16 v[108:111], v[48:51], a[156:159], 0\n\t"
"ds_write_addtid_b32 v217 offset:22848\n\t"
"v_mul_u32_u24 v166, 1152, v159\n\t"
"v_mfma_f32_16x16x32_bf16 v[108:111], v[52:55], a[172:175], v[108:111]\n\t"
"s_waitcnt lgkmcnt(15)\n\t"
"ds_write_addtid_b32 v193 offset:16704\n\t"
"ds_write_addtid_b32 v216 offset:22592\n\t"
"ds_write_addtid_b32 v219 offset:31072\n\t"
"ds_read_b64_tr_b16 a[198:199], v249 offset:43136\n\t"
"buffer_load_dword v191, v189, s[8:11], 0 offen sc1\n\t"
"v_mul_u32_u24 v167, 20736, v158\n\t"
"v_mfma_f32_16x16x32_bf16 v[0:3], v[72:75], a[144:147], v[96:99]\n\t"
"ds_read_b128 a[172:175], v224 offset:7680\n\t"
"v_add3_u32 v166, v166, v157, s61\n\t"
"v_mfma_f32_16x16x32_bf16 v[0:3], v[76:79], a[160:163], v[0:3]\n\t"
"ds_read_b64_tr_b16 a[200:201], v250 offset:42624\n\t"
"v_sub_u32 v166, v166, v167\n\t"
"v_mfma_f32_16x16x32_bf16 v[16:19], v[80:83], a[144:147], v[112:115]\n\t"
"ds_write_addtid_b32 v218 offset:30816\n\t"
"v_xor_b32 v159, v159, v158\n\t"
"v_mfma_f32_16x16x32_bf16 v[16:19], v[84:87], a[160:163], v[16:19]\n\t"
"ds_write_addtid_b32 v200 offset:20544\n\t"
"v_lshl_add_u32 v166, v159, 6, v166\n\t"
"v_mfma_f32_16x16x32_bf16 v[32:35], v[88:91], a[144:147], v[128:131]\n\t"
"v_xad_u32 v167, v158, 0, v155\n\t"
"v_lshl_add_u32 v168, v167, 7, v166\n\t"
"v_mfma_f32_16x16x32_bf16 v[32:35], v[92:95], a[160:163], v[32:35]\n\t"
"ds_read_b128 a[156:159], v224 offset:3584\n\t"
"v_and_b32 v167, 2, v167\n\t"
"v_lshl_add_u32 v168, v167, 4, v168\n\t"
"v_mfma_f32_16x16x32_bf16 v[36:39], v[88:91], a[148:151], v[132:135]\n\t"
"ds_read_b64_tr_b16 a[202:203], v250 offset:43136\n\t"
"v_xor_b32 v168, 64, v168\n\t"
"v_mfma_f32_16x16x32_bf16 v[36:39], v[92:95], a[164:167], v[36:39]\n\t"
"ds_read_b64_tr_b16 a[204:205], v251 offset:42624\n\t"
"v_accvgpr_write_b32 a208, v168\n\t"
"v_mfma_f32_16x16x32_bf16 v[20:23], v[80:83], a[148:151], v[116:119]\n\t"
"ds_read_b64_tr_b16 a[206:207], v251 offset:43136\n\t"
"v_xad_u32 v167, v158, 1, v155\n\t"
"v_mfma_f32_16x16x32_bf16 v[20:23], v[84:87], a[164:167], v[20:23]\n\t"
"v_lshl_add_u32 v168, v167, 7, v166\n\t"
"v_and_b32 v167, 2, v167\n\t"
"v_mfma_f32_16x16x32_bf16 v[4:7], v[72:75], a[148:151], v[100:103]\n\t"
"v_lshl_add_u32 v168, v167, 4, v168\n\t"
"v_accvgpr_write_b32 a209, v168\n\t"
"v_mfma_f32_16x16x32_bf16 v[4:7], v[76:79], a[164:167], v[4:7]\n\t"
"s_waitcnt lgkmcnt(3)\n\t"
"s_waitcnt vmcnt(2)\n\t"
"v_cmp_eq_u32 vcc, v190, v188\n\t"
"s_nop 4\n\t"
"s_cmp_eq_u64 vcc, 0\n\t"
"s_cbranch_scc1 Lgo_%=\n\t"
"s_sub_u32 s68, s68, 1\n\t"
"s_waitcnt vmcnt(0)\n\t"
"Lchk_%=:\n\t"
"v_cmp_eq_u32 vcc, v191, v188\n\t"
"s_nop 4\n\t"
"s_cmp_eq_u64 vcc, 0\n\t"
"s_cbranch_scc1 Lgo_%=\n\t"
"s_sub_u32 s68, s68, 1\n\t"
"s_cmp_eq_u32 s68, 0\n\t"
"s_cbranch_scc1 Lfb_%=\n\t"
"buffer_load_dword v191, v189, s[8:11], 0 offen sc1\n\t"
"s_waitcnt vmcnt(0)\n\t"
"s_branch Lchk_%=\n\t"
"Lgo_%=:\n\t"
"s_barrier\n\t"
"v_mfma_f32_16x16x32_bf16 v[8:11], v[72:75], a[152:155], v[104:107]\n\t"
"ds_read_b128 a[144:147], v224 offset:16448\n\t"
"v_add_u32 v248, 32896, v227\n\t"
"v_mfma_f32_16x16x32_bf16 v[8:11], v[76:79], a[168:171], v[8:11]\n\t"
"ds_read_b128 a[160:163], v224 offset:20544\n\t"
"s_mov_b32 s54, 98304\n\t"
"buffer_load_dwordx4 a[96:99], v235, s[8:11], s54 offen offset:0 sc1\n\t"
"buffer_load_dwordx4 a[100:103], v235, s[8:11], s54 offen offset:1024 sc1\n\t"
"v_mfma_f32_16x16x32_bf16 v[24:27], v[80:83], a[152:155], v[120:123]\n\t"
"ds_read_b128 a[148:151], v224 offset:16960\n\t"
"v_mbcnt_lo_u32_b32 v176, -1, 0\n\t"
"v_mfma_f32_16x16x32_bf16 v[24:27], v[84:87], a[168:171], v[24:27]\n\t"
"ds_read_b128 a[164:167], v224 offset:21056\n\t"
"buffer_load_dwordx4 a[104:107], v235, s[8:11], s54 offen offset:2048 sc1\n\t"
"buffer_load_dwordx4 a[108:111], v235, s[8:11], s54 offen offset:3072 sc1\n\t"
"v_mbcnt_hi_u32_b32 v176, -1, v176\n\t"
"v_mfma_f32_16x16x32_bf16 v[40:43], v[88:91], a[152:155], v[136:139]\n\t"
"v_and_b32 v154, 15, v176\n\t"
"v_lshrrev_b32 v155, 4, v176\n\t"
"v_mfma_f32_16x16x32_bf16 v[40:43], v[92:95], a[168:171], v[40:43]\n\t"
"buffer_load_dwordx4 a[112:115], v252, s[8:11], s54 offen offset:0 sc1\n\t"
"buffer_load_dwordx4 a[116:119], v252, s[8:11], s54 offen offset:1024 sc1\n\t"
"v_bfe_u32 v156, v176, 2, 2\n\t"
"v_mfma_f32_16x16x32_bf16 v[44:47], v[88:91], a[156:159], v[140:143]\n\t"
"v_and_b32 v157, 3, v176\n\t"
"v_lshlrev_b32 v157, 3, v157\n\t"
"v_mfma_f32_16x16x32_bf16 v[44:47], v[92:95], a[172:175], v[44:47]\n\t"
"s_add_u32 s60, s43, 32896\n\t"
"ds_read_b128 a[168:171], v224 offset:21568\n\t"
"v_xor_b32 v249, 32, v248\n\t"
"v_mfma_f32_16x16x32_bf16 v[28:31], v[80:83], a[156:159], v[124:127]\n\t"
"s_add_u32 s61, s26, 20800\n\t"
"v_xor_b32 v250, 64, v248\n\t"
"v_xor_b32 v251, 96, v248\n\t"
"v_mfma_f32_16x16x32_bf16 v[28:31], v[84:87], a[172:175], v[28:31]\n\t"
"s_mov_b32 s66, 172032\n\t"
"buffer_load_dwordx4 a[120:123], v235, s[8:11], s66 offen offset:0 sc1\n\t"
"v_mfma_f32_16x16x32_bf16 v[12:15], v[72:75], a[156:159], v[108:111]\n\t"
"s_mul_i32 s62, s73, 96\n\t"
"ds_read_b128 a[152:155], v224 offset:17472\n\t"
"ds_read_b128 a[156:159], v224 offset:17984\n\t"
"v_mfma_f32_16x16x32_bf16 v[12:15], v[76:79], a[172:175], v[12:15]\n\t"
"s_add_u32 s62, s62, 54400\n\t"
"v_min_u32 v158, 35, v176\n\t"
"v_lshl_add_u32 v254, v158, 4, s60\n\t"
"v_add_u32 v255, 66432, v254\n\t"
"v_and_b32 v158, 1, v155\n\t"
"v_add_u32 v159, v156, v158\n\t"
"v_lshl_add_u32 v166, v159, 7, v157\n\t"
"v_and_b32 v159, 2, v159\n\t"
"v_mfma_f32_16x16x32_bf16 v[192:195], a[216:219], a[176:179], 0\n\t"
"v_lshl_add_u32 v166, v159, 4, v166\n\t"
"v_xad_u32 v167, v158, 1, v156\n\t"
"v_lshl_add_u32 v168, v167, 7, v157\n\t"
"v_and_b32 v167, 2, v167\n\t"
"v_lshl_add_u32 v168, v167, 4, v168\n\t"
"v_mfma_f32_16x16x32_bf16 v[196:199], a[216:219], a[180:183], 0\n\t"
"v_sub_u32 v168, v168, v166\n\t"
"v_mul_u32_u24 v159, 20800, v158\n\t"
"v_sub_u32 v166, v166, v159\n\t"
"v_mfma_f32_16x16x32_bf16 v[200:203], a[216:219], a[184:187], 0\n\t"
"ds_read_b128 a[172:175], v224 offset:22080\n\t"
"v_lshl_add_u32 v168, v158, 7, v168\n\t"
"v_mfma_f32_16x16x32_bf16 v[204:207], a[216:219], a[188:191], 0\n\t"
".p2align 3\n\t"
"buffer_load_dwordx4 a[124:127], v235, s[8:11], s66 offen offset:1024 sc1\n\t"
"v_pk_fma_f32 v[96:97], v[96:97], s[38:39], v[0:1] neg_lo:[1,0,1] neg_hi:[1,0,1]\n\t"
"v_pk_fma_f32 v[98:99], v[98:99], s[38:39], v[2:3] neg_lo:[1,0,1] neg_hi:[1,0,1]\n\t"
"v_pk_fma_f32 v[112:113], v[112:113], s[38:39], v[16:17] neg_lo:[1,0,1] neg_hi:[1,0,1]\n\t"
"v_pk_fma_f32 v[114:115], v[114:115], s[38:39], v[18:19] neg_lo:[1,0,1] neg_hi:[1,0,1]\n\t"
"v_pk_fma_f32 v[128:129], v[128:129], s[38:39], v[32:33] neg_lo:[1,0,1] neg_hi:[1,0,1]\n\t"
"v_pk_fma_f32 v[130:131], v[130:131], s[38:39], v[34:35] neg_lo:[1,0,1] neg_hi:[1,0,1]\n\t"
"v_pk_fma_f32 v[132:133], v[132:133], s[38:39], v[36:37] neg_lo:[1,0,1] neg_hi:[1,0,1]\n\t"
"v_pk_fma_f32 v[134:135], v[134:135], s[38:39], v[38:39] neg_lo:[1,0,1] neg_hi:[1,0,1]\n\t"
"v_pk_fma_f32 v[116:117], v[116:117], s[38:39], v[20:21] neg_lo:[1,0,1] neg_hi:[1,0,1]\n\t"
"v_pk_fma_f32 v[118:119], v[118:119], s[38:39], v[22:23] neg_lo:[1,0,1] neg_hi:[1,0,1]\n\t"
"v_pk_fma_f32 v[100:101], v[100:101], s[38:39], v[4:5] neg_lo:[1,0,1] neg_hi:[1,0,1]\n\t"
"v_pk_fma_f32 v[102:103], v[102:103], s[38:39], v[6:7] neg_lo:[1,0,1] neg_hi:[1,0,1]\n\t"
"buffer_load_dwordx4 a[128:131], v235, s[8:11], s66 offen offset:2048 sc1\n\t"
"v_pk_fma_f32 v[104:105], v[104:105], s[38:39], v[8:9] neg_lo:[1,0,1] neg_hi:[1,0,1]\n\t"
"v_pk_fma_f32 v[106:107], v[106:107], s[38:39], v[10:11] neg_lo:[1,0,1] neg_hi:[1,0,1]\n\t"
"v_pk_fma_f32 v[120:121], v[120:121], s[38:39], v[24:25] neg_lo:[1,0,1] neg_hi:[1,0,1]\n\t"
"v_pk_fma_f32 v[122:123], v[122:123], s[38:39], v[26:27] neg_lo:[1,0,1] neg_hi:[1,0,1]\n\t"
"v_pk_fma_f32 v[136:137], v[136:137], s[38:39], v[40:41] neg_lo:[1,0,1] neg_hi:[1,0,1]\n\t"
"v_pk_fma_f32 v[138:139], v[138:139], s[38:39], v[42:43] neg_lo:[1,0,1] neg_hi:[1,0,1]\n\t"
"v_pk_fma_f32 v[140:141], v[140:141], s[38:39], v[44:45] neg_lo:[1,0,1] neg_hi:[1,0,1]\n\t"
"v_pk_fma_f32 v[142:143], v[142:143], s[38:39], v[46:47] neg_lo:[1,0,1] neg_hi:[1,0,1]\n\t"
"v_pk_fma_f32 v[124:125], v[124:125], s[38:39], v[28:29] neg_lo:[1,0,1] neg_hi:[1,0,1]\n\t"
"v_pk_fma_f32 v[126:127], v[126:127], s[38:39], v[30:31] neg_lo:[1,0,1] neg_hi:[1,0,1]\n\t"
"v_pk_fma_f32 v[108:109], v[108:109], s[38:39], v[12:13] neg_lo:[1,0,1] neg_hi:[1,0,1]\n\t"
"v_pk_fma_f32 v[110:111], v[110:111], s[38:39], v[14:15] neg_lo:[1,0,1] neg_hi:[1,0,1]\n\t"
"v_mfma_f32_16x16x32_bf16 v[208:211], a[216:219], a[192:195], 0\n\t"
"v_subrev_u32 v253, 64, v168\n\t"
"v_and_b32 v159, 32, v176\n\t"
"v_mfma_f32_16x16x32_bf16 v[212:215], a[216:219], a[196:199], 0\n\t"
"s_waitcnt lgkmcnt(10)\n\t"
"buffer_load_dwordx4 a[132:135], v235, s[8:11], s66 offen offset:3072 sc1\n\t"
"v_lshlrev_b32 v159, 4, v159\n\t"
"v_mfma_f32_16x16x32_bf16 v[216:219], a[216:219], a[200:203], 0\n\t"
"s_waitcnt lgkmcnt(8)\n\t"
"v_add3_u32 v225, v166, v159, s61\n\t"
"v_mul_u32_u24 v158, 400, v154\n\t"
"v_mfma_f32_16x16x32_bf16 v[220:223], a[216:219], a[204:207], 0\n\t"
"s_waitcnt lgkmcnt(6)\n\t"
"v_mul_u32_u24 v159, 24, v155\n\t"
"v_cvt_pk_bf16_f32 v193, v193, v197\n\t"
"v_cvt_pk_bf16_f32 v194, v194, v198\n\t"
"v_mfma_f32_16x16x32_bf16 v[0:3], a[48:51], a[144:147], v[0:3]\n\t"
"v_cvt_pk_bf16_f32 v192, v192, v196\n\t"
"v_cvt_pk_bf16_f32 v195, v195, v199\n\t"
"v_mfma_f32_16x16x32_bf16 v[0:3], a[52:55], a[160:163], v[0:3]\n\t"
"ds_write_addtid_b32 v193 offset:256\n\t"
"buffer_load_dwordx4 a[136:139], v252, s[8:11], s66 offen offset:0 sc1\n\t"
"v_cvt_pk_bf16_f32 v200, v200, v204\n\t"
"v_mfma_f32_16x16x32_bf16 v[16:19], a[56:59], a[144:147], v[16:19]\n\t"
"ds_write_addtid_b32 v194 offset:8224\n\t"
"v_cvt_pk_bf16_f32 v202, v202, v206\n\t"
"v_mfma_f32_16x16x32_bf16 v[16:19], a[60:63], a[160:163], v[16:19]\n\t"
"v_add3_u32 v228, v158, v159, s62\n\t"
"v_cvt_pk_bf16_f32 v203, v203, v207\n\t"
"v_cvt_pk_bf16_f32 v209, v209, v213\n\t"
"v_cvt_pk_bf16_f32 v210, v210, v214\n\t"
"v_cvt_pk_bf16_f32 v211, v211, v215\n\t"
"v_mfma_f32_16x16x32_bf16 v[32:35], a[64:67], a[144:147], v[32:35]\n\t"
"ds_read_b128 a[144:147], v224 offset:18496\n\t"
"v_cvt_pk_bf16_f32 v208, v208, v212\n\t"
"v_mfma_f32_16x16x32_bf16 v[32:35], a[68:71], a[160:163], v[32:35]\n\t"
"s_waitcnt lgkmcnt(7)\n\t"
"ds_write_addtid_b32 v208 offset:2048\n\t"
"buffer_load_dwordx4 a[140:143], v252, s[8:11], s66 offen offset:1024 sc1\n\t"
"v_cvt_pk_bf16_f32 v216, v216, v220\n\t"
"v_mfma_f32_16x16x32_bf16 v[36:39], a[64:67], a[148:151], v[36:39]\n\t"
"s_mul_i32 s60, s73, 3200\n\t"
"ds_read_b128 a[160:163], v224 offset:22592\n\t"
"v_cvt_pk_bf16_f32 v217, v217, v221\n\t"
"v_mfma_f32_16x16x32_bf16 v[36:39], a[68:71], a[164:167], v[36:39]\n\t"
"ds_write_addtid_b32 v210 offset:10272\n\t"
"s_mov_b32 s69, 196608\n\t"
"buffer_load_dwordx4 a[0:3], v235, s[8:11], s69 offen offset:0 sc1\n\t"
"v_cvt_pk_bf16_f32 v201, v201, v205\n\t"
"s_barrier\n\t"
"v_mfma_f32_16x16x32_bf16 v[20:23], a[56:59], a[148:151], v[20:23]\n\t"
"s_add_u32 s60, s60, 54400\n\t"
"ds_write_addtid_b32 v216 offset:6144\n\t"
"ds_read_b64_tr_b16 a[176:177], v248 offset:0\n\t"
"v_mfma_f32_16x16x32_bf16 v[20:23], a[60:63], a[164:167], v[20:23]\n\t"
"s_and_b32 s61, s73, 2\n\t"
"ds_read_b64_tr_b16 a[178:179], v248 offset:512\n\t"
"ds_read_b64_tr_b16 a[180:181], v249 offset:0\n\t"
"ds_read_b64_tr_b16 a[182:183], v249 offset:512\n\t"
"ds_read_b64_tr_b16 a[184:185], v250 offset:0\n\t"
"v_mfma_f32_16x16x32_bf16 v[4:7], a[48:51], a[148:151], v[4:7]\n\t"
"ds_write_addtid_b32 v195 offset:8480\n\t"
"v_cvt_pk_bf16_f32 v219, v219, v223\n\t"
"v_mfma_f32_16x16x32_bf16 v[4:7], a[52:55], a[164:167], v[4:7]\n\t"
"s_waitcnt lgkmcnt(15)\n\t"
"ds_write_addtid_b32 v203 offset:12576\n\t"
"ds_write_addtid_b32 v211 offset:10528\n\t"
"ds_write_addtid_b32 v219 offset:14624\n\t"
"ds_read_b64_tr_b16 a[186:187], v250 offset:512\n\t"
"ds_read_b64_tr_b16 a[188:189], v251 offset:0\n\t"
"s_lshl_b32 s61, s61, 7\n\t"
"v_mfma_f32_16x16x32_bf16 v[8:11], a[48:51], a[152:155], v[8:11]\n\t"
"s_add_u32 s60, s60, s61\n\t"
"ds_read_b64_tr_b16 a[190:191], v251 offset:512\n\t"
"ds_read_b64_tr_b16 a[192:193], v248 offset:1152\n\t"
"v_mfma_f32_16x16x32_bf16 v[8:11], a[52:55], a[168:171], v[8:11]\n\t"
"s_mul_i32 s61, s73, 393216\n\t"
".p2align 3\n\t"
"ds_read_b128 a[148:151], v224 offset:19008\n\t"
"ds_read_b128 a[164:167], v224 offset:23104\n\t"
"ds_write_addtid_b32 v192 offset:0\n\t"
"ds_write_addtid_b32 v200 offset:4096\n\t"
"ds_write_addtid_b32 v201 offset:4352\n\t"
"ds_write_addtid_b32 v202 offset:12320\n\t"
"ds_write_addtid_b32 v209 offset:2304\n\t"
"ds_write_addtid_b32 v217 offset:6400\n\t"
"ds_read_b64_tr_b16 a[194:195], v248 offset:1664\n\t"
"ds_read_b64_tr_b16 a[196:197], v249 offset:1152\n\t"
"v_add_u32 v158, 0, v176\n\t"
"v_mul_u32_u24 v159, 0xab, v158\n\t"
"v_mfma_f32_16x16x32_bf16 v[24:27], a[56:59], a[152:155], v[24:27]\n\t"
"v_lshrrev_b32 v159, 12, v159\n\t"
"v_cvt_pk_bf16_f32 v218, v218, v222\n\t"
"v_mfma_f32_16x16x32_bf16 v[24:27], a[60:63], a[168:171], v[24:27]\n\t"
"buffer_load_dwordx4 a[4:7], v235, s[8:11], s69 offen offset:1024 sc1\n\t"
"buffer_load_dwordx4 a[8:11], v235, s[8:11], s69 offen offset:2048 sc1\n\t"
"v_add_lshl_u32 v166, v158, v159, 4\n\t"
"v_mfma_f32_16x16x32_bf16 v[40:43], a[64:67], a[152:155], v[40:43]\n\t"
"ds_read_b64_tr_b16 a[198:199], v249 offset:1664\n\t"
"v_add_u32 v229, s60, v166\n\t"
"v_mfma_f32_16x16x32_bf16 v[40:43], a[68:71], a[168:171], v[40:43]\n\t"
"s_waitcnt lgkmcnt(15)\n\t"
"ds_write_addtid_b32 v218 offset:14368\n\t"
"buffer_load_dwordx4 a[12:15], v235, s[8:11], s69 offen offset:3072 sc1\n\t"
"v_mfma_f32_16x16x32_bf16 v[44:47], a[64:67], a[156:159], v[44:47]\n\t"
"v_mad_u32_u24 v158, v159, 24, v158\n\t"
"v_lshl_add_u32 v232, v158, 4, s61\n\t"
"v_mfma_f32_16x16x32_bf16 v[44:47], a[68:71], a[172:175], v[44:47]\n\t"
"ds_read_b64_tr_b16 a[200:201], v250 offset:1152\n\t"
"v_add_u32 v158, 64, v176\n\t"
"v_mfma_f32_16x16x32_bf16 v[28:31], a[56:59], a[156:159], v[28:31]\n\t"
"ds_read_b64_tr_b16 a[202:203], v250 offset:1664\n\t"
"v_mul_u32_u24 v159, 0xab, v158\n\t"
"v_mfma_f32_16x16x32_bf16 v[28:31], a[60:63], a[172:175], v[28:31]\n\t"
"buffer_load_dwordx4 a[16:19], v252, s[8:11], s69 offen offset:0 sc1\n\t"
"v_mfma_f32_16x16x32_bf16 v[12:15], a[48:51], a[156:159], v[12:15]\n\t"
"ds_read_b128 a[156:159], v224 offset:20032\n\t"
"v_lshrrev_b32 v159, 12, v159\n\t"
"v_mfma_f32_16x16x32_bf16 v[12:15], a[52:55], a[172:175], v[12:15]\n\t"
"ds_read_b128 a[172:175], v224 offset:24128\n\t"
"v_add_lshl_u32 v166, v158, v159, 4\n\t"
"v_mfma_f32_16x16x32_bf16 v[96:99], a[72:75], a[144:147], v[96:99]\n\t"
"ds_read_b64_tr_b16 a[204:205], v251 offset:1152\n\t"
"v_add_u32 v230, s60, v166\n\t"
"v_mfma_f32_16x16x32_bf16 v[96:99], a[76:79], a[160:163], v[96:99]\n\t"
"buffer_load_dwordx4 a[20:23], v252, s[8:11], s69 offen offset:1024 sc1\n\t"
"v_mfma_f32_16x16x32_bf16 v[112:115], a[80:83], a[144:147], v[112:115]\n\t"
"v_mad_u32_u24 v158, v159, 24, v158\n\t"
"v_lshl_add_u32 v233, v158, 4, s61\n\t"
"v_mfma_f32_16x16x32_bf16 v[112:115], a[84:87], a[160:163], v[112:115]\n\t"
"s_mov_b32 s81, 270336\n\t"
"buffer_load_dwordx4 a[24:27], v235, s[8:11], s81 offen offset:0 sc1\n\t"
"v_mfma_f32_16x16x32_bf16 v[128:131], a[88:91], a[144:147], v[128:131]\n\t"
"ds_read_b128 a[168:171], v224 offset:23616\n\t"
"v_add_u32 v158, 128, v176\n\t"
"v_mfma_f32_16x16x32_bf16 v[128:131], a[92:95], a[160:163], v[128:131]\n\t"
"s_waitcnt lgkmcnt(15)\n\t"
"ds_read_b64_tr_b16 a[206:207], v251 offset:1664\n\t"
"v_mul_u32_u24 v159, 0xab, v158\n\t"
"v_mfma_f32_16x16x32_bf16 v[132:135], a[88:91], a[148:151], v[132:135]\n\t"
"ds_read_b128 a[152:155], v224 offset:19520\n\t"
"v_lshrrev_b32 v159, 12, v159\n\t"
"v_mfma_f32_16x16x32_bf16 v[132:135], a[92:95], a[164:167], v[132:135]\n\t"
"buffer_load_dwordx4 a[28:31], v235, s[8:11], s81 offen offset:1024 sc1\n\t"
"v_mfma_f32_16x16x32_bf16 v[116:119], a[80:83], a[148:151], v[116:119]\n\t"
"v_add_lshl_u32 v166, v158, v159, 4\n\t"
"v_add_u32 v231, s60, v166\n\t"
"v_mfma_f32_16x16x32_bf16 v[116:119], a[84:87], a[164:167], v[116:119]\n\t"
"buffer_load_dwordx4 a[32:35], v235, s[8:11], s81 offen offset:2048 sc1\n\t"
"v_mfma_f32_16x16x32_bf16 v[100:103], a[72:75], a[148:151], v[100:103]\n\t"
"v_mad_u32_u24 v158, v159, 24, v158\n\t"
"v_lshl_add_u32 v234, v158, 4, s61\n\t"
"v_mfma_f32_16x16x32_bf16 v[100:103], a[76:79], a[164:167], v[100:103]\n\t"
"s_waitcnt lgkmcnt(0)\n\t"
"s_add_u32 s71, s71, 1\n\t"
"v_mov_b32 v215, s71\n\t"
"s_mov_b64 exec, 1\n\t"
"buffer_store_dword v215, off, s[8:11], s70 sc1\n\t"
"s_mov_b64 exec, -1\n\t"
"s_barrier\n\t"
"v_mfma_f32_16x16x32_bf16 v[104:107], a[72:75], a[152:155], v[104:107]\n\t"
"s_nop 0\n\t"
"ds_read_b128 a[144:147], v224 offset:0\n\t"
"ds_read_b128 a[160:163], v224 offset:4096\n\t"
"v_mfma_f32_16x16x32_bf16 v[104:107], a[76:79], a[168:171], v[104:107]\n\t"
"buffer_load_dwordx4 a[36:39], v235, s[8:11], s81 offen offset:3072 sc1\n\t"
"v_mfma_f32_16x16x32_bf16 v[120:123], a[80:83], a[152:155], v[120:123]\n\t"
"ds_read_b128 a[148:151], v224 offset:512\n\t"
"v_accvgpr_read_b32 v172, a220\n\t"
"v_mfma_f32_16x16x32_bf16 v[120:123], a[84:87], a[168:171], v[120:123]\n\t"
"ds_read_b128 a[164:167], v224 offset:4608\n\t"
"v_and_b32 v172, 0xff7fff7f, v172\n\t"
"v_mfma_f32_16x16x32_bf16 v[136:139], a[88:91], a[152:155], v[136:139]\n\t"
"ds_read_b128 a[152:155], v224 offset:1024\n\t"
"v_accvgpr_write_b32 a252, v172\n\t"
"v_mfma_f32_16x16x32_bf16 v[136:139], a[92:95], a[168:171], v[136:139]\n\t"
"buffer_load_dwordx4 a[40:43], v252, s[8:11], s81 offen offset:0 sc1\n\t"
"v_mfma_f32_16x16x32_bf16 v[140:143], a[88:91], a[156:159], v[140:143]\n\t"
"v_add_u32 v248, 122368, v226\n\t"
"v_xor_b32 v249, 32, v248\n\t"
"v_mfma_f32_16x16x32_bf16 v[140:143], a[92:95], a[172:175], v[140:143]\n\t"
"buffer_load_dwordx4 a[44:47], v252, s[8:11], s81 offen offset:1024 sc1\n\t"
"v_accvgpr_read_b32 v173, a221\n\t"
"v_mfma_f32_16x16x32_bf16 v[124:127], a[80:83], a[156:159], v[124:127]\n\t"
"s_mov_b32 s40, 8192\n\t"
"v_and_b32 v173, 0xff7fff7f, v173\n\t"
"v_xor_b32 v250, 64, v248\n\t"
"v_mfma_f32_16x16x32_bf16 v[124:127], a[84:87], a[172:175], v[124:127]\n\t"
"v_accvgpr_write_b32 a253, v173\n\t"
"v_xor_b32 v251, 96, v248\n\t"
"v_mfma_f32_16x16x32_bf16 v[108:111], a[72:75], a[156:159], v[108:111]\n\t"
"ds_read_b128 a[168:171], v224 offset:5120\n\t"
"v_accvgpr_read_b32 v174, a222\n\t"
"v_mfma_f32_16x16x32_bf16 v[108:111], a[76:79], a[172:175], v[108:111]\n\t"
"s_mov_b32 s57, 122880\n\t"
"buffer_load_dwordx4 a[48:51], v235, s[8:11], s57 offen offset:0 sc1\n\t"
"v_mfma_f32_16x16x32_bf16 v[192:195], a[216:219], a[176:179], 0\n\t"
"s_mov_b32 s41, 16384\n\t"
"ds_read_b64_tr_b16 a[176:177], v248 offset:0\n\t"
"ds_read_b64_tr_b16 a[178:179], v248 offset:512\n\t"
"v_mfma_f32_16x16x32_bf16 v[196:199], a[216:219], a[180:183], 0\n\t"
"ds_read_b64_tr_b16 a[180:181], v248 offset:1024\n\t"
"v_and_b32 v174, 0xff7fff7f, v174\n\t"
"v_mfma_f32_16x16x32_bf16 v[200:203], a[216:219], a[184:187], 0\n\t"
"buffer_load_dwordx4 a[52:55], v235, s[8:11], s57 offen offset:1024 sc1\n\t"
"v_mfma_f32_16x16x32_bf16 v[204:207], a[216:219], a[188:191], 0\n\t"
"ds_read_b64_tr_b16 a[184:185], v249 offset:0\n\t"
"v_accvgpr_write_b32 a254, v174\n\t"
"v_mfma_f32_16x16x32_bf16 v[208:211], a[216:219], a[192:195], 0\n\t"
"s_mov_b32 s42, 24576\n\t"
"ds_read_b128 a[172:175], v224 offset:5632\n\t"
"v_accvgpr_read_b32 v175, a223\n\t"
"v_mfma_f32_16x16x32_bf16 v[212:215], a[216:219], a[196:199], 0\n\t"
"buffer_load_dwordx4 a[56:59], v235, s[8:11], s57 offen offset:2048 sc1\n\t"
"v_mfma_f32_16x16x32_bf16 v[216:219], a[216:219], a[200:203], 0\n\t"
"v_and_b32 v175, 0xff7fff7f, v175\n\t"
"v_accvgpr_write_b32 a255, v175\n\t"
"v_mfma_f32_16x16x32_bf16 v[220:223], a[216:219], a[204:207], 0\n\t"
"s_waitcnt vmcnt(26) lgkmcnt(9)\n\t"
"ds_read_b64_tr_b16 a[186:187], v249 offset:512\n\t"
"v_cvt_pk_bf16_f32 v192, v192, v196\n\t"
"v_mfma_f32_16x16x32_bf16 v[0:3], a[96:99], a[144:147], v[0:3]\n\t"
"s_mov_b32 s90, 0.5\n\t"
"ds_read_b128 a[156:159], v224 offset:1536\n\t"
"ds_read_b64_tr_b16 a[188:189], v249 offset:1024\n\t"
"v_mfma_f32_16x16x32_bf16 v[0:3], a[100:103], a[160:163], v[0:3]\n\t"
"s_waitcnt vmcnt(24)\n\t"
"buffer_load_dwordx4 a[60:63], v235, s[8:11], s57 offen offset:3072 sc1\n\t"
"v_mfma_f32_16x16x32_bf16 v[16:19], a[104:107], a[144:147], v[16:19]\n\t"
"v_cvt_pk_bf16_f32 v201, v201, v205\n\t"
"v_cvt_pk_bf16_f32 v203, v203, v207\n\t"
"v_mfma_f32_16x16x32_bf16 v[16:19], a[108:111], a[160:163], v[16:19]\n\t"
"s_waitcnt vmcnt(23)\n\t"
"v_cvt_pk_bf16_f32 v195, v195, v199\n\t"
"v_cvt_pk_bf16_f32 v200, v200, v204\n\t"
"s_mov_b32 s91, 0.5\n\t"
"v_mfma_f32_16x16x32_bf16 v[32:35], a[112:115], a[144:147], v[32:35]\n\t"
"ds_read_b128 a[144:147], v224 offset:2048\n\t"
"v_cvt_pk_bf16_f32 v211, v211, v215\n\t"
"v_mfma_f32_16x16x32_bf16 v[32:35], a[116:119], a[160:163], v[32:35]\n\t"
"s_waitcnt lgkmcnt(11)\n\t"
"buffer_load_dwordx4 a[64:67], v252, s[8:11], s57 offen offset:0 sc1\n\t"
"v_mfma_f32_16x16x32_bf16 v[36:39], a[112:115], a[148:151], v[36:39]\n\t"
"s_add_u32 s84, s8, 18432\n\t"
"s_addc_u32 s85, s9, 0\n\t"
"ds_read_b128 a[160:163], v224 offset:6144\n\t"
"v_mfma_f32_16x16x32_bf16 v[36:39], a[116:119], a[164:167], v[36:39]\n\t"
"buffer_load_dwordx4 a[68:71], v252, s[8:11], s57 offen offset:1024 sc1\n\t"
"v_mfma_f32_16x16x32_bf16 v[20:23], a[104:107], a[148:151], v[20:23]\n\t"
"ds_write_addtid_b32 v192 offset:16448\n\t"
"v_cvt_pk_bf16_f32 v193, v193, v197\n\t"
"v_mfma_f32_16x16x32_bf16 v[20:23], a[108:111], a[164:167], v[20:23]\n\t"
"s_mov_b64 s[86:87], s[10:11]\n\t"
"ds_read_b64_tr_b16 a[192:193], v250 offset:0\n\t"
"v_cvt_pk_bf16_f32 v194, v194, v198\n\t"
"v_mfma_f32_16x16x32_bf16 v[4:7], a[96:99], a[148:151], v[4:7]\n\t"
"ds_read_b128 a[148:151], v224 offset:2560\n\t"
"v_cvt_pk_bf16_f32 v210, v210, v214\n\t"
"v_mfma_f32_16x16x32_bf16 v[4:7], a[100:103], a[164:167], v[4:7]\n\t"
"s_waitcnt lgkmcnt(13)\n\t"
"s_mov_b32 s63, 147456\n\t"
"buffer_load_dwordx4 a[72:75], v235, s[8:11], s63 offen offset:0 sc1\n\t"
"v_mfma_f32_16x16x32_bf16 v[8:11], a[96:99], a[152:155], v[8:11]\n\t"
"ds_read_b128 a[164:167], v224 offset:6656\n\t"
"v_cvt_pk_bf16_f32 v202, v202, v206\n\t"
"v_mfma_f32_16x16x32_bf16 v[8:11], a[100:103], a[168:171], v[8:11]\n\t"
"buffer_load_dwordx4 a[76:79], v235, s[8:11], s63 offen offset:1024 sc1\n\t"
"v_mfma_f32_16x16x32_bf16 v[24:27], a[104:107], a[152:155], v[24:27]\n\t"
"ds_write_addtid_b32 v203 offset:29024\n\t"
"v_cvt_pk_bf16_f32 v217, v217, v221\n\t"
"v_mfma_f32_16x16x32_bf16 v[24:27], a[108:111], a[168:171], v[24:27]\n\t"
"s_xor_b64 s[88:89], s[84:85], s[8:9]\n\t"
"ds_write_addtid_b32 v211 offset:26976\n\t"
"ds_write_addtid_b32 v217 offset:22848\n\t"
"v_mfma_f32_16x16x32_bf16 v[40:43], a[112:115], a[152:155], v[40:43]\n\t"
"ds_write_addtid_b32 v200 offset:20544\n\t"
"v_cvt_pk_bf16_f32 v209, v209, v213\n\t"
"v_mfma_f32_16x16x32_bf16 v[40:43], a[116:119], a[168:171], v[40:43]\n\t"
"s_waitcnt lgkmcnt(11)\n\t"
"buffer_load_dwordx4 a[80:83], v235, s[8:11], s63 offen offset:2048 sc1\n\t"
"v_mfma_f32_16x16x32_bf16 v[44:47], a[112:115], a[156:159], v[44:47]\n\t"
"s_mov_b32 s47, 40960\n\t"
"ds_write_addtid_b32 v195 offset:24928\n\t"
"ds_write_addtid_b32 v210 offset:26720\n\t"
"v_mfma_f32_16x16x32_bf16 v[44:47], a[116:119], a[172:175], v[44:47]\n\t"
"s_add_u32 s25, s24, 0xffeb0000\n\t"
"ds_write_addtid_b32 v194 offset:24672\n\t"
"ds_read_b64_tr_b16 a[194:195], v250 offset:512\n\t"
"v_mfma_f32_16x16x32_bf16 v[28:31], a[104:107], a[156:159], v[28:31]\n\t"
"s_mov_b32 s49, 57344\n\t"
"ds_write_addtid_b32 v202 offset:28768\n\t"
"v_cvt_pk_bf16_f32 v208, v208, v212\n\t"
"v_mfma_f32_16x16x32_bf16 v[28:31], a[108:111], a[172:175], v[28:31]\n\t"
"buffer_load_dwordx4 a[84:87], v235, s[8:11], s63 offen offset:3072 sc1\n\t"
"v_mfma_f32_16x16x32_bf16 v[12:15], a[96:99], a[156:159], v[12:15]\n\t"
"s_add_u32 s27, s24, 0xffe80000\n\t"
"ds_read_b128 a[156:159], v224 offset:3584\n\t"
"ds_write_addtid_b32 v193 offset:16704\n\t"
"v_mfma_f32_16x16x32_bf16 v[12:15], a[100:103], a[172:175], v[12:15]\n\t"
"s_waitcnt vmcnt(27) lgkmcnt(15)\n\t"
"ds_write_addtid_b32 v208 offset:18496\n\t"
"v_cvt_pk_bf16_f32 v216, v216, v220\n\t"
"v_mfma_f32_16x16x32_bf16 v[96:99], a[120:123], a[144:147], v[96:99]\n\t"
"ds_write_addtid_b32 v201 offset:20800\n\t"
"v_cvt_pk_bf16_f32 v219, v219, v223\n\t"
"v_mfma_f32_16x16x32_bf16 v[96:99], a[124:127], a[160:163], v[96:99]\n\t"
"s_waitcnt vmcnt(25)\n\t"
"ds_write_addtid_b32 v219 offset:31072\n\t"
"ds_read_b64_tr_b16 a[196:197], v250 offset:1024\n\t"
"s_mov_b32 s48, 49152\n\t"
"v_mfma_f32_16x16x32_bf16 v[112:115], a[128:131], a[144:147], v[112:115]\n\t"
"ds_write_addtid_b32 v216 offset:22592\n\t"
"v_cvt_pk_bf16_f32 v218, v218, v222\n\t"
"v_mfma_f32_16x16x32_bf16 v[112:115], a[132:135], a[160:163], v[112:115]\n\t"
"s_waitcnt vmcnt(23)\n\t"
"ds_write_addtid_b32 v209 offset:18752\n\t"
"ds_write_addtid_b32 v218 offset:30816\n\t"
"s_mov_b32 s50, 65536\n\t"
"v_mfma_f32_16x16x32_bf16 v[128:131], a[136:139], a[144:147], v[128:131]\n\t"
"s_add_u32 m0, s26, 145280\n\t"
"ds_read_b128 a[168:171], v224 offset:7168\n\t"
"ds_read_b64_tr_b16 a[200:201], v251 offset:0\n\t"
"s_add_u32 s20, s24, 0xffd30000\n\t"
"v_mfma_f32_16x16x32_bf16 v[128:131], a[140:143], a[160:163], v[128:131]\n\t"
"s_waitcnt lgkmcnt(15)\n\t"
"buffer_load_dwordx4 a[88:91], v252, s[8:11], s63 offen offset:0 sc1\n\t"
"v_mfma_f32_16x16x32_bf16 v[132:135], a[136:139], a[148:151], v[132:135]\n\t"
"s_add_u32 s24, s24, 0xffd00000\n\t"
"ds_read_b128 a[152:155], v224 offset:3072\n\t"
"ds_read_b128 a[172:175], v224 offset:7680\n\t"
"v_mfma_f32_16x16x32_bf16 v[132:135], a[140:143], a[164:167], v[132:135]\n\t"
"buffer_load_dwordx4 a[92:95], v252, s[8:11], s63 offen offset:1024 sc1\n\t"
"v_mfma_f32_16x16x32_bf16 v[116:119], a[128:131], a[148:151], v[116:119]\n\t"
"s_mov_b32 s46, 32768\n\t"
"ds_read_b64_tr_b16 a[202:203], v251 offset:512\n\t"
"ds_read_b64_tr_b16 a[204:205], v251 offset:1024\n\t"
"v_mfma_f32_16x16x32_bf16 v[116:119], a[132:135], a[164:167], v[116:119]\n\t"
"buffer_load_dwordx4 v238, s[12:15], s22 offen offset:2176 nt lds\n\t"
"v_mfma_f32_16x16x32_bf16 v[100:103], a[120:123], a[148:151], v[100:103]\n\t"
"s_lshl_b32 m0, s73, 9\n\t"
"v_mfma_f32_16x16x32_bf16 v[100:103], a[124:127], a[164:167], v[100:103]\n\t"
"s_waitcnt lgkmcnt(2)\n\t"
"s_barrier\n\t"
"v_mfma_f32_16x16x32_bf16 v[104:107], a[120:123], a[152:155], v[104:107]\n\t"
"s_add_u32 m0, s26, 145280\n\t"
"ds_read_b128 a[144:147], v224 offset:16448\n\t"
"v_mfma_f32_16x16x32_bf16 v[104:107], a[124:127], a[168:171], v[104:107]\n\t"
"ds_read_b128 a[160:163], v224 offset:20544\n\t"
"v_add_u32 v248, 32896, v226\n\t"
"v_mfma_f32_16x16x32_bf16 v[120:123], a[128:131], a[152:155], v[120:123]\n\t"
"ds_read_b128 a[148:151], v224 offset:16960\n\t"
"v_xor_b32 v249, 32, v248\n\t"
"v_mfma_f32_16x16x32_bf16 v[120:123], a[132:135], a[168:171], v[120:123]\n\t"
"ds_read_b128 a[164:167], v224 offset:21056\n\t"
"v_xor_b32 v250, 64, v248\n\t"
"v_mfma_f32_16x16x32_bf16 v[136:139], a[136:139], a[152:155], v[136:139]\n\t"
"v_mfma_f32_16x16x32_bf16 v[136:139], a[140:143], a[168:171], v[136:139]\n\t"
"ds_read_b128 a[168:171], v224 offset:21568\n\t"
"v_xor_b32 v251, 96, v248\n\t"
"v_mfma_f32_16x16x32_bf16 v[140:143], a[136:139], a[156:159], v[140:143]\n\t"
"v_mfma_f32_16x16x32_bf16 v[140:143], a[140:143], a[172:175], v[140:143]\n\t"
"buffer_load_dwordx4 v239, s[12:15], s22 offen offset:3200 nt lds\n\t"
"v_mfma_f32_16x16x32_bf16 v[124:127], a[128:131], a[156:159], v[124:127]\n\t"
"v_mfma_f32_16x16x32_bf16 v[124:127], a[132:135], a[172:175], v[124:127]\n\t"
"ds_read_b128 a[152:155], v224 offset:17472\n\t"
"v_mfma_f32_16x16x32_bf16 v[108:111], a[120:123], a[156:159], v[108:111]\n\t"
"ds_read_b128 a[156:159], v224 offset:17984\n\t"
"v_mfma_f32_16x16x32_bf16 v[108:111], a[124:127], a[172:175], v[108:111]\n\t"
"ds_read_b128 a[172:175], v224 offset:22080\n\t"
"v_mfma_f32_16x16x32_bf16 v[192:195], a[252:255], a[176:179], 0\n\t"
"ds_read_b64_tr_b16 a[176:177], v248 offset:0\n\t"
"v_mfma_f32_16x16x32_bf16 v[196:199], a[252:255], a[184:187], 0\n\t"
"ds_read_b64_tr_b16 a[184:185], v249 offset:0\n\t"
"v_mfma_f32_16x16x32_bf16 v[200:203], a[252:255], a[178:181], 0\n\t"
"s_mov_b32 s51, 73728\n\t"
"ds_read_b64_tr_b16 a[178:179], v248 offset:512\n\t"
"ds_read_b64_tr_b16 a[180:181], v248 offset:1024\n\t"
"v_mfma_f32_16x16x32_bf16 v[204:207], a[252:255], a[186:189], 0\n\t"
"ds_read_b64_tr_b16 a[186:187], v249 offset:512\n\t"
"v_mfma_f32_16x16x32_bf16 v[208:211], a[252:255], a[192:195], 0\n\t"
"s_waitcnt lgkmcnt(14)\n\t"
"ds_read_b64_tr_b16 a[188:189], v249 offset:1024\n\t"
"s_mov_b32 s52, 81920\n\t"
"v_mfma_f32_16x16x32_bf16 v[212:215], a[252:255], a[200:203], 0\n\t"
"s_mov_b32 s53, 90112\n\t"
"ds_read_b64_tr_b16 a[192:193], v250 offset:0\n\t"
"ds_read_b64_tr_b16 a[200:201], v251 offset:0\n\t"
"v_mfma_f32_16x16x32_bf16 v[216:219], a[252:255], a[194:197], 0\n\t"
"s_waitcnt lgkmcnt(15)\n\t"
"ds_read_b64_tr_b16 a[194:195], v250 offset:512\n\t"
"v_mfma_f32_16x16x32_bf16 v[220:223], a[252:255], a[202:205], 0\n\t"
"s_waitcnt vmcnt(25) lgkmcnt(15)\n\t"
"s_mov_b64 exec, s[36:37]\n\t"
"buffer_load_dwordx4 v240, s[12:15], s22 offen offset:3712 nt lds\n\t"
"s_mov_b64 exec, -1\n\t"
"v_mfma_f32_16x16x32_bf16 v[0:3], a[0:3], a[144:147], v[0:3]\n\t"
"s_lshl_b32 m0, s73, 9\n\t"
"ds_read_b64_tr_b16 a[196:197], v250 offset:1024\n\t"
"v_cvt_pk_bf16_f32 v195, v195, v199\n\t"
"v_mfma_f32_16x16x32_bf16 v[0:3], a[4:7], a[160:163], v[0:3]\n\t"
"s_waitcnt vmcnt(24)\n\t"
"ds_write_addtid_b32 v195 offset:10272\n\t"
"v_cvt_pk_bf16_f32 v200, v200, v204\n\t"
"v_mfma_f32_16x16x32_bf16 v[16:19], a[8:11], a[144:147], v[16:19]\n\t"
"ds_write_addtid_b32 v200 offset:256\n\t"
"v_cvt_pk_bf16_f32 v193, v193, v197\n\t"
"v_mfma_f32_16x16x32_bf16 v[16:19], a[12:15], a[160:163], v[16:19]\n\t"
"s_waitcnt vmcnt(22)\n\t"
"ds_read_b64_tr_b16 a[202:203], v251 offset:512\n\t"
"v_cvt_pk_bf16_f32 v201, v201, v205\n\t"
"v_mfma_f32_16x16x32_bf16 v[32:35], a[16:19], a[144:147], v[32:35]\n\t"
"v_cvt_pk_bf16_f32 v210, v210, v214\n\t"
"v_cvt_pk_bf16_f32 v211, v211, v215\n\t"
"v_mfma_f32_16x16x32_bf16 v[32:35], a[20:23], a[160:163], v[32:35]\n\t"
"s_waitcnt lgkmcnt(15)\n\t"
"ds_read_b128 a[144:147], v224 offset:18496\n\t"
"v_cvt_pk_bf16_f32 v202, v202, v206\n\t"
"v_mfma_f32_16x16x32_bf16 v[36:39], a[16:19], a[148:151], v[36:39]\n\t"
"ds_read_b128 a[160:163], v224 offset:22592\n\t"
"v_cvt_pk_bf16_f32 v218, v218, v222\n\t"
"v_mfma_f32_16x16x32_bf16 v[36:39], a[20:23], a[164:167], v[36:39]\n\t"
"s_mov_b32 s75, 221184\n\t"
"buffer_load_dwordx4 a[96:99], v235, s[8:11], s75 offen offset:0 sc1\n\t"
"v_mfma_f32_16x16x32_bf16 v[20:23], a[8:11], a[148:151], v[20:23]\n\t"
"ds_write_addtid_b32 v211 offset:14368\n\t"
"v_cvt_pk_bf16_f32 v217, v217, v221\n\t"
"v_mfma_f32_16x16x32_bf16 v[20:23], a[12:15], a[164:167], v[20:23]\n\t"
"buffer_load_dwordx4 a[100:103], v235, s[8:11], s75 offen offset:1024 sc1\n\t"
"v_mfma_f32_16x16x32_bf16 v[4:7], a[0:3], a[148:151], v[4:7]\n\t"
"ds_read_b128 a[148:151], v224 offset:19008\n\t"
"v_cvt_pk_bf16_f32 v219, v219, v223\n\t"
"v_mfma_f32_16x16x32_bf16 v[4:7], a[4:7], a[164:167], v[4:7]\n\t"
"ds_read_b128 a[164:167], v224 offset:23104\n\t"
"v_cvt_pk_bf16_f32 v216, v216, v220\n\t"
"v_mfma_f32_16x16x32_bf16 v[8:11], a[0:3], a[152:155], v[8:11]\n\t"
"s_mov_b32 s56, 114688\n\t"
"ds_write_addtid_b32 v217 offset:12576\n\t"
"ds_write_addtid_b32 v218 offset:6400\n\t"
"v_mfma_f32_16x16x32_bf16 v[8:11], a[4:7], a[168:171], v[8:11]\n\t"
"buffer_load_dwordx4 a[104:107], v235, s[8:11], s75 offen offset:2048 sc1\n\t"
"v_mfma_f32_16x16x32_bf16 v[24:27], a[8:11], a[152:155], v[24:27]\n\t"
"ds_write_addtid_b32 v210 offset:6144\n\t"
"v_cvt_pk_bf16_f32 v208, v208, v212\n\t"
"v_mfma_f32_16x16x32_bf16 v[24:27], a[12:15], a[168:171], v[24:27]\n\t"
"ds_write_addtid_b32 v193 offset:8224\n\t"
"v_cvt_pk_bf16_f32 v194, v194, v198\n\t"
"v_mfma_f32_16x16x32_bf16 v[40:43], a[16:19], a[152:155], v[40:43]\n\t"
"ds_write_addtid_b32 v208 offset:4096\n\t"
"v_cvt_pk_bf16_f32 v192, v192, v196\n\t"
"v_mfma_f32_16x16x32_bf16 v[40:43], a[20:23], a[168:171], v[40:43]\n\t"
"s_waitcnt lgkmcnt(15)\n\t"
"buffer_load_dwordx4 a[108:111], v235, s[8:11], s75 offen offset:3072 sc1\n\t"
"v_mfma_f32_16x16x32_bf16 v[44:47], a[16:19], a[156:159], v[44:47]\n\t"
"ds_write_addtid_b32 v202 offset:2304\n\t"
"v_mfma_f32_16x16x32_bf16 v[44:47], a[20:23], a[172:175], v[44:47]\n\t"
"buffer_load_dwordx4 a[112:115], v252, s[8:11], s75 offen offset:0 sc1\n\t"
"v_mfma_f32_16x16x32_bf16 v[28:31], a[8:11], a[156:159], v[28:31]\n\t"
"ds_write_addtid_b32 v192 offset:0\n\t"
"v_mfma_f32_16x16x32_bf16 v[28:31], a[12:15], a[172:175], v[28:31]\n\t"
"ds_write_addtid_b32 v219 offset:14624\n\t"
"v_cvt_pk_bf16_f32 v209, v209, v213\n\t"
"v_mfma_f32_16x16x32_bf16 v[12:15], a[0:3], a[156:159], v[12:15]\n\t"
"ds_write_addtid_b32 v216 offset:4352\n\t"
"v_cvt_pk_bf16_f32 v203, v203, v207\n\t"
"v_mfma_f32_16x16x32_bf16 v[12:15], a[4:7], a[172:175], v[12:15]\n\t"
"s_waitcnt vmcnt(25) lgkmcnt(12)\n\t"
"buffer_load_dwordx4 a[116:119], v252, s[8:11], s75 offen offset:1024 sc1\n\t"
"v_mfma_f32_16x16x32_bf16 v[96:99], a[24:27], a[144:147], v[96:99]\n\t"
"s_mov_b32 s58, 131072\n\t"
"ds_write_addtid_b32 v194 offset:2048\n\t"
"ds_write_addtid_b32 v209 offset:12320\n\t"
"v_mfma_f32_16x16x32_bf16 v[96:99], a[28:31], a[160:163], v[96:99]\n\t"
"s_waitcnt vmcnt(23)\n\t"
"ds_write_addtid_b32 v201 offset:8480\n\t"
"ds_write_addtid_b32 v203 offset:10528\n\t"
"s_mov_b32 s55, 106496\n\t"
"v_mfma_f32_16x16x32_bf16 v[112:115], a[32:35], a[144:147], v[112:115]\n\t"
"s_mov_b32 s59, 139264\n\t"
"ds_read_b128 a[168:171], v224 offset:23616\n\t"
"ds_read_b128 a[156:159], v224 offset:20032\n\t"
"v_mfma_f32_16x16x32_bf16 v[112:115], a[36:39], a[160:163], v[112:115]\n\t"
"s_waitcnt vmcnt(21)\n\t"
"s_mov_b32 s78, 245760\n\t"
"buffer_load_dwordx4 a[120:123], v235, s[8:11], s78 offen offset:0 sc1\n\t"
"v_mfma_f32_16x16x32_bf16 v[128:131], a[40:43], a[144:147], v[128:131]\n\t"
"ds_read_b128 a[152:155], v224 offset:19520\n\t"
"v_mfma_f32_16x16x32_bf16 v[128:131], a[44:47], a[160:163], v[128:131]\n\t"
"s_waitcnt lgkmcnt(15)\n\t"
"buffer_load_dwordx4 a[124:127], v235, s[8:11], s78 offen offset:1024 sc1\n\t"
"v_mfma_f32_16x16x32_bf16 v[132:135], a[40:43], a[148:151], v[132:135]\n\t"
"ds_read_b128 a[172:175], v224 offset:24128\n\t"
"v_mfma_f32_16x16x32_bf16 v[132:135], a[44:47], a[164:167], v[132:135]\n\t"
"v_mfma_f32_16x16x32_bf16 v[116:119], a[32:35], a[148:151], v[116:119]\n\t"
"ds_read_b64_tr_b16 a[204:205], v251 offset:1024\n\t"
"v_mfma_f32_16x16x32_bf16 v[116:119], a[36:39], a[164:167], v[116:119]\n\t"
"buffer_load_dwordx4 a[128:131], v235, s[8:11], s78 offen offset:2048 sc1\n\t"
"v_mfma_f32_16x16x32_bf16 v[100:103], a[24:27], a[148:151], v[100:103]\n\t"
"v_mfma_f32_16x16x32_bf16 v[100:103], a[28:31], a[164:167], v[100:103]\n\t"
"s_waitcnt lgkmcnt(1)\n\t"
"s_add_u32 s71, s71, 1\n\t"
"v_mov_b32 v215, s71\n\t"
"s_mov_b64 exec, 1\n\t"
"buffer_store_dword v215, off, s[8:11], s70 sc1\n\t"
"s_mov_b64 exec, -1\n\t"
"s_barrier\n\t"
"v_mfma_f32_16x16x32_bf16 v[104:107], a[24:27], a[152:155], v[104:107]\n\t"
"ds_read_b128 a[148:151], v224 offset:512\n\t"
"v_accvgpr_read_b32 v248, a208\n\t"
"v_mfma_f32_16x16x32_bf16 v[104:107], a[28:31], a[168:171], v[104:107]\n\t"
"ds_read_b128 a[144:147], v224 offset:0\n\t"
"v_add_u32 v248, 80896, v248\n\t"
"v_mfma_f32_16x16x32_bf16 v[120:123], a[32:35], a[152:155], v[120:123]\n\t"
"ds_read_b128 a[160:163], v224 offset:4096\n\t"
"v_xor_b32 v249, 32, v248\n\t"
"v_mfma_f32_16x16x32_bf16 v[120:123], a[36:39], a[168:171], v[120:123]\n\t"
"ds_read_b128 a[164:167], v224 offset:4608\n\t"
"v_xor_b32 v250, 64, v248\n\t"
"v_mfma_f32_16x16x32_bf16 v[136:139], a[40:43], a[152:155], v[136:139]\n\t"
"s_mov_b32 s64, 155648\n\t"
"v_xor_b32 v251, 96, v248\n\t"
"v_mfma_f32_16x16x32_bf16 v[136:139], a[44:47], a[168:171], v[136:139]\n\t"
"buffer_load_dwordx4 a[132:135], v235, s[8:11], s78 offen offset:3072 sc1\n\t"
"v_mfma_f32_16x16x32_bf16 v[140:143], a[40:43], a[156:159], v[140:143]\n\t"
"ds_read_b128 a[152:155], v224 offset:1024\n\t"
"v_mfma_f32_16x16x32_bf16 v[140:143], a[44:47], a[172:175], v[140:143]\n\t"
"buffer_load_dwordx4 a[136:139], v252, s[8:11], s78 offen offset:0 sc1\n\t"
"v_mfma_f32_16x16x32_bf16 v[124:127], a[32:35], a[156:159], v[124:127]\n\t"
"ds_read_b128 a[168:171], v224 offset:5120\n\t"
"v_mfma_f32_16x16x32_bf16 v[124:127], a[36:39], a[172:175], v[124:127]\n\t"
"buffer_load_dwordx4 a[140:143], v252, s[8:11], s78 offen offset:1024 sc1\n\t"
"v_mfma_f32_16x16x32_bf16 v[108:111], a[24:27], a[156:159], v[108:111]\n\t"
"ds_read_b128 a[156:159], v224 offset:1536\n\t"
"v_mfma_f32_16x16x32_bf16 v[108:111], a[28:31], a[172:175], v[108:111]\n\t"
"buffer_load_dwordx4 a[248:251], v247, s[12:15], 0 offen offset:256 nt\n\t"
"v_mfma_f32_16x16x32_bf16 v[192:195], a[252:255], a[176:179], 0\n\t"
"v_mfma_f32_16x16x32_bf16 v[196:199], a[252:255], a[184:187], 0\n\t"
"s_mov_b32 s65, 163840\n\t"
"ds_read_b128 a[172:175], v224 offset:5632\n\t"
"v_mfma_f32_16x16x32_bf16 v[200:203], a[252:255], a[178:181], 0\n\t"
"v_mfma_f32_16x16x32_bf16 v[204:207], a[252:255], a[186:189], 0\n\t"
".p2align 3\n\t"
"v_pk_add_f32 v[96:97], v[0:1], v[96:97] neg_lo:[0,1] neg_hi:[0,1]\n\t"
"v_pk_add_f32 v[98:99], v[2:3], v[98:99] neg_lo:[0,1] neg_hi:[0,1]\n\t"
"v_pk_fma_f32 v[0:1], v[96:97], s[90:91], v[0:1] neg_lo:[1,0,0] neg_hi:[1,0,0]\n\t"
"v_pk_fma_f32 v[2:3], v[98:99], s[90:91], v[2:3] neg_lo:[1,0,0] neg_hi:[1,0,0]\n\t"
"v_pk_add_f32 v[112:113], v[16:17], v[112:113] neg_lo:[0,1] neg_hi:[0,1]\n\t"
"v_pk_add_f32 v[114:115], v[18:19], v[114:115] neg_lo:[0,1] neg_hi:[0,1]\n\t"
"v_pk_fma_f32 v[16:17], v[112:113], s[90:91], v[16:17] neg_lo:[1,0,0] neg_hi:[1,0,0]\n\t"
"v_pk_fma_f32 v[18:19], v[114:115], s[90:91], v[18:19] neg_lo:[1,0,0] neg_hi:[1,0,0]\n\t"
"v_pk_add_f32 v[128:129], v[32:33], v[128:129] neg_lo:[0,1] neg_hi:[0,1]\n\t"
"v_pk_add_f32 v[130:131], v[34:35], v[130:131] neg_lo:[0,1] neg_hi:[0,1]\n\t"
"v_pk_fma_f32 v[32:33], v[128:129], s[90:91], v[32:33] neg_lo:[1,0,0] neg_hi:[1,0,0]\n\t"
"v_pk_fma_f32 v[34:35], v[130:131], s[90:91], v[34:35] neg_lo:[1,0,0] neg_hi:[1,0,0]\n\t"
"v_pk_add_f32 v[132:133], v[36:37], v[132:133] neg_lo:[0,1] neg_hi:[0,1]\n\t"
"v_pk_add_f32 v[134:135], v[38:39], v[134:135] neg_lo:[0,1] neg_hi:[0,1]\n\t"
"v_pk_fma_f32 v[36:37], v[132:133], s[90:91], v[36:37] neg_lo:[1,0,0] neg_hi:[1,0,0]\n\t"
"v_pk_fma_f32 v[38:39], v[134:135], s[90:91], v[38:39] neg_lo:[1,0,0] neg_hi:[1,0,0]\n\t"
"v_pk_add_f32 v[116:117], v[20:21], v[116:117] neg_lo:[0,1] neg_hi:[0,1]\n\t"
"v_pk_add_f32 v[118:119], v[22:23], v[118:119] neg_lo:[0,1] neg_hi:[0,1]\n\t"
"v_pk_fma_f32 v[20:21], v[116:117], s[90:91], v[20:21] neg_lo:[1,0,0] neg_hi:[1,0,0]\n\t"
"v_pk_fma_f32 v[22:23], v[118:119], s[90:91], v[22:23] neg_lo:[1,0,0] neg_hi:[1,0,0]\n\t"
"v_pk_add_f32 v[100:101], v[4:5], v[100:101] neg_lo:[0,1] neg_hi:[0,1]\n\t"
"v_pk_add_f32 v[102:103], v[6:7], v[102:103] neg_lo:[0,1] neg_hi:[0,1]\n\t"
"v_pk_fma_f32 v[4:5], v[100:101], s[90:91], v[4:5] neg_lo:[1,0,0] neg_hi:[1,0,0]\n\t"
"v_pk_fma_f32 v[6:7], v[102:103], s[90:91], v[6:7] neg_lo:[1,0,0] neg_hi:[1,0,0]\n\t"
"v_pk_add_f32 v[104:105], v[8:9], v[104:105] neg_lo:[0,1] neg_hi:[0,1]\n\t"
"v_pk_add_f32 v[106:107], v[10:11], v[106:107] neg_lo:[0,1] neg_hi:[0,1]\n\t"
"v_pk_fma_f32 v[8:9], v[104:105], s[90:91], v[8:9] neg_lo:[1,0,0] neg_hi:[1,0,0]\n\t"
"v_pk_fma_f32 v[10:11], v[106:107], s[90:91], v[10:11] neg_lo:[1,0,0] neg_hi:[1,0,0]\n\t"
"v_pk_add_f32 v[120:121], v[24:25], v[120:121] neg_lo:[0,1] neg_hi:[0,1]\n\t"
"v_pk_add_f32 v[122:123], v[26:27], v[122:123] neg_lo:[0,1] neg_hi:[0,1]\n\t"
"v_pk_fma_f32 v[24:25], v[120:121], s[90:91], v[24:25] neg_lo:[1,0,0] neg_hi:[1,0,0]\n\t"
"v_pk_fma_f32 v[26:27], v[122:123], s[90:91], v[26:27] neg_lo:[1,0,0] neg_hi:[1,0,0]\n\t"
"v_pk_add_f32 v[136:137], v[40:41], v[136:137] neg_lo:[0,1] neg_hi:[0,1]\n\t"
"v_pk_add_f32 v[138:139], v[42:43], v[138:139] neg_lo:[0,1] neg_hi:[0,1]\n\t"
"v_pk_fma_f32 v[40:41], v[136:137], s[90:91], v[40:41] neg_lo:[1,0,0] neg_hi:[1,0,0]\n\t"
"v_pk_fma_f32 v[42:43], v[138:139], s[90:91], v[42:43] neg_lo:[1,0,0] neg_hi:[1,0,0]\n\t"
"v_pk_add_f32 v[140:141], v[44:45], v[140:141] neg_lo:[0,1] neg_hi:[0,1]\n\t"
"v_pk_add_f32 v[142:143], v[46:47], v[142:143] neg_lo:[0,1] neg_hi:[0,1]\n\t"
"v_pk_fma_f32 v[44:45], v[140:141], s[90:91], v[44:45] neg_lo:[1,0,0] neg_hi:[1,0,0]\n\t"
"v_pk_fma_f32 v[46:47], v[142:143], s[90:91], v[46:47] neg_lo:[1,0,0] neg_hi:[1,0,0]\n\t"
"v_pk_add_f32 v[124:125], v[28:29], v[124:125] neg_lo:[0,1] neg_hi:[0,1]\n\t"
"v_pk_add_f32 v[126:127], v[30:31], v[126:127] neg_lo:[0,1] neg_hi:[0,1]\n\t"
"v_pk_fma_f32 v[28:29], v[124:125], s[90:91], v[28:29] neg_lo:[1,0,0] neg_hi:[1,0,0]\n\t"
"v_pk_fma_f32 v[30:31], v[126:127], s[90:91], v[30:31] neg_lo:[1,0,0] neg_hi:[1,0,0]\n\t"
"v_pk_add_f32 v[108:109], v[12:13], v[108:109] neg_lo:[0,1] neg_hi:[0,1]\n\t"
"v_pk_add_f32 v[110:111], v[14:15], v[110:111] neg_lo:[0,1] neg_hi:[0,1]\n\t"
"v_pk_fma_f32 v[12:13], v[108:109], s[90:91], v[12:13] neg_lo:[1,0,0] neg_hi:[1,0,0]\n\t"
"v_pk_fma_f32 v[14:15], v[110:111], s[90:91], v[14:15] neg_lo:[1,0,0] neg_hi:[1,0,0]\n\t"
"v_mfma_f32_16x16x32_bf16 v[208:211], a[252:255], a[192:195], 0\n\t"
"buffer_load_dwordx4 a[0:3], v252, s[8:11], s58 offen offset:0 sc1\n\t"
"v_mfma_f32_16x16x32_bf16 v[212:215], a[252:255], a[200:203], 0\n\t"
"v_mfma_f32_16x16x32_bf16 v[216:219], a[252:255], a[194:197], 0\n\t"
"s_waitcnt lgkmcnt(8)\n\t"
"v_mfma_f32_16x16x32_bf16 v[220:223], a[252:255], a[202:205], 0\n\t"
"s_waitcnt vmcnt(28) lgkmcnt(5)\n\t"
"v_cvt_pk_bf16_f32 v193, v193, v197\n\t"
"v_cvt_pk_bf16_f32 v195, v195, v199\n\t"
"v_mfma_f32_16x16x32_bf16 v[0:3], a[48:51], a[144:147], v[0:3]\n\t"
"ds_write_addtid_b32 v195 offset:26720\n\t"
"v_cvt_pk_bf16_f32 v194, v194, v198\n\t"
"v_mfma_f32_16x16x32_bf16 v[0:3], a[52:55], a[160:163], v[0:3]\n\t"
"s_waitcnt vmcnt(26)\n\t"
"buffer_load_dwordx4 a[4:7], v252, s[8:11], s58 offen offset:1024 sc1\n\t"
"v_mfma_f32_16x16x32_bf16 v[16:19], a[56:59], a[144:147], v[16:19]\n\t"
"v_cvt_pk_bf16_f32 v201, v201, v205\n\t"
"v_cvt_pk_bf16_f32 v202, v202, v206\n\t"
"v_mfma_f32_16x16x32_bf16 v[16:19], a[60:63], a[160:163], v[16:19]\n\t"
"s_waitcnt vmcnt(25)\n\t"
"buffer_load_dwordx4 a[8:11], v252, s[8:11], s58 offen offset:2048 sc1\n\t"
"v_mfma_f32_16x16x32_bf16 v[32:35], a[64:67], a[144:147], v[32:35]\n\t"
"ds_read_b128 a[144:147], v224 offset:2048\n\t"
"v_cvt_pk_bf16_f32 v210, v210, v214\n\t"
"v_mfma_f32_16x16x32_bf16 v[32:35], a[68:71], a[160:163], v[32:35]\n\t"
"s_waitcnt lgkmcnt(6)\n\t"
"ds_read_b128 a[160:163], v224 offset:6144\n\t"
"ds_write_addtid_b32 v193 offset:24672\n\t"
"s_mov_b32 s67, 180224\n\t"
"v_mfma_f32_16x16x32_bf16 v[36:39], a[64:67], a[148:151], v[36:39]\n\t"
"ds_write_addtid_b32 v210 offset:22592\n\t"
"v_cvt_pk_bf16_f32 v218, v218, v222\n\t"
"v_mfma_f32_16x16x32_bf16 v[36:39], a[68:71], a[164:167], v[36:39]\n\t"
"buffer_load_dwordx4 a[12:15], v252, s[8:11], s58 offen offset:3072 sc1\n\t"
"v_mfma_f32_16x16x32_bf16 v[20:23], a[56:59], a[148:151], v[20:23]\n\t"
"v_cvt_pk_bf16_f32 v192, v192, v196\n\t"
"v_cvt_pk_bf16_f32 v211, v211, v215\n\t"
"v_mfma_f32_16x16x32_bf16 v[20:23], a[60:63], a[164:167], v[20:23]\n\t"
"s_mov_b32 s68, 188416\n\t"
"ds_write_addtid_b32 v192 offset:16448\n\t"
"ds_write_addtid_b32 v194 offset:18496\n\t"
"v_mfma_f32_16x16x32_bf16 v[4:7], a[48:51], a[148:151], v[4:7]\n\t"
"ds_read_b128 a[148:151], v224 offset:2560\n\t"
"v_cvt_pk_bf16_f32 v203, v203, v207\n\t"
"v_mfma_f32_16x16x32_bf16 v[4:7], a[52:55], a[164:167], v[4:7]\n\t"
"s_waitcnt lgkmcnt(10)\n\t"
"buffer_load_dwordx4 a[16:19], v235, s[8:11], s59 offen offset:0 sc1\n\t"
"v_mfma_f32_16x16x32_bf16 v[8:11], a[48:51], a[152:155], v[8:11]\n\t"
"ds_read_b128 a[164:167], v224 offset:6656\n\t"
"v_cvt_pk_bf16_f32 v217, v217, v221\n\t"
"v_mfma_f32_16x16x32_bf16 v[8:11], a[52:55], a[168:171], v[8:11]\n\t"
"buffer_load_dwordx4 a[20:23], v235, s[8:11], s59 offen offset:1024 sc1\n\t"
"v_mfma_f32_16x16x32_bf16 v[24:27], a[56:59], a[152:155], v[24:27]\n\t"
"ds_write_addtid_b32 v202 offset:18752\n\t"
"v_cvt_pk_bf16_f32 v208, v208, v212\n\t"
"v_mfma_f32_16x16x32_bf16 v[24:27], a[60:63], a[168:171], v[24:27]\n\t"
"ds_write_addtid_b32 v217 offset:29024\n\t"
"v_cvt_pk_bf16_f32 v216, v216, v220\n\t"
"v_mfma_f32_16x16x32_bf16 v[40:43], a[64:67], a[152:155], v[40:43]\n\t"
"ds_write_addtid_b32 v208 offset:20544\n\t"
"v_cvt_pk_bf16_f32 v219, v219, v223\n\t"
"v_mfma_f32_16x16x32_bf16 v[40:43], a[68:71], a[168:171], v[40:43]\n\t"
"s_waitcnt lgkmcnt(12)\n\t"
"buffer_load_dwordx4 a[24:27], v252, s[8:11], s57 offen offset:2048 sc1\n\t"
"v_mfma_f32_16x16x32_bf16 v[44:47], a[64:67], a[156:159], v[44:47]\n\t"
"ds_write_addtid_b32 v216 offset:20800\n\t"
"v_cvt_pk_bf16_f32 v200, v200, v204\n\t"
"v_mfma_f32_16x16x32_bf16 v[44:47], a[68:71], a[172:175], v[44:47]\n\t"
"buffer_load_dwordx4 a[28:31], v252, s[8:11], s57 offen offset:3072 sc1\n\t"
"v_mfma_f32_16x16x32_bf16 v[28:31], a[56:59], a[156:159], v[28:31]\n\t"
"ds_read_b128 a[168:171], v224 offset:7168\n\t"
"v_cvt_pk_bf16_f32 v209, v209, v213\n\t"
"v_mfma_f32_16x16x32_bf16 v[28:31], a[60:63], a[172:175], v[28:31]\n\t"
"ds_write_addtid_b32 v209 offset:28768\n\t"
"v_mfma_f32_16x16x32_bf16 v[12:15], a[48:51], a[156:159], v[12:15]\n\t"
"ds_write_addtid_b32 v201 offset:24928\n\t"
"v_mfma_f32_16x16x32_bf16 v[12:15], a[52:55], a[172:175], v[12:15]\n\t"
"s_waitcnt vmcnt(29) lgkmcnt(13)\n\t"
"buffer_load_dwordx4 a[32:35], v235, s[8:11], s58 offen offset:0 sc1\n\t"
"v_mfma_f32_16x16x32_bf16 v[96:99], a[72:75], a[144:147], v[96:99]\n\t"
"s_mov_b32 s72, 204800\n\t"
"ds_write_addtid_b32 v200 offset:16704\n\t"
"ds_write_addtid_b32 v203 offset:26976\n\t"
"v_mfma_f32_16x16x32_bf16 v[96:99], a[76:79], a[160:163], v[96:99]\n\t"
"s_waitcnt vmcnt(28)\n\t"
"ds_read_b128 a[172:175], v224 offset:7680\n\t"
"ds_write_addtid_b32 v218 offset:22848\n\t"
"s_mov_b32 s74, 212992\n\t"
"v_mfma_f32_16x16x32_bf16 v[112:115], a[80:83], a[144:147], v[112:115]\n\t"
"ds_write_addtid_b32 v211 offset:30816\n\t"
"v_mfma_f32_16x16x32_bf16 v[112:115], a[84:87], a[160:163], v[112:115]\n\t"
"s_waitcnt vmcnt(26)\n\t"
"ds_write_addtid_b32 v219 offset:31072\n\t"
"v_mfma_f32_16x16x32_bf16 v[128:131], a[88:91], a[144:147], v[128:131]\n\t"
"ds_read_b128 a[152:155], v224 offset:3072\n\t"
"v_mfma_f32_16x16x32_bf16 v[128:131], a[92:95], a[160:163], v[128:131]\n\t"
"s_waitcnt lgkmcnt(14)\n\t"
"buffer_load_dwordx4 a[36:39], v235, s[8:11], s58 offen offset:1024 sc1\n\t"
"v_mfma_f32_16x16x32_bf16 v[132:135], a[88:91], a[148:151], v[132:135]\n\t"
"ds_read_b128 a[156:159], v224 offset:3584\n\t"
"v_mfma_f32_16x16x32_bf16 v[132:135], a[92:95], a[164:167], v[132:135]\n\t"
"buffer_load_dwordx4 a[40:43], v235, s[8:11], s58 offen offset:2048 sc1\n\t"
"v_mfma_f32_16x16x32_bf16 v[116:119], a[80:83], a[148:151], v[116:119]\n\t"
"v_mfma_f32_16x16x32_bf16 v[116:119], a[84:87], a[164:167], v[116:119]\n\t"
"buffer_load_dwordx4 a[44:47], v235, s[8:11], s58 offen offset:3072 sc1\n\t"
"v_mfma_f32_16x16x32_bf16 v[100:103], a[72:75], a[148:151], v[100:103]\n\t"
"v_mfma_f32_16x16x32_bf16 v[100:103], a[76:79], a[164:167], v[100:103]\n\t"
"s_waitcnt vmcnt(26)\n\t"
"s_waitcnt lgkmcnt(0)\n\t"
"s_barrier\n\t"
"v_mfma_f32_16x16x32_bf16 v[104:107], a[72:75], a[152:155], v[104:107]\n\t"
"s_add_u32 m0, s43, 101632\n\t"
"ds_read_b128 a[144:147], v224 offset:16448\n\t"
"ds_read_b128 a[148:151], v224 offset:16960\n\t"
"v_mfma_f32_16x16x32_bf16 v[104:107], a[76:79], a[168:171], v[104:107]\n\t"
"s_mov_b64 exec, s[44:45]\n\t"
"buffer_load_dwordx4 v247, s[12:15], 0 offen offset:0 nt lds\n\t"
"s_mov_b64 exec, -1\n\t"
"v_mfma_f32_16x16x32_bf16 v[120:123], a[80:83], a[152:155], v[120:123]\n\t"
"s_add_u32 m0, s26, 103936\n\t"
"ds_read_b128 a[160:163], v224 offset:20544\n\t"
"v_mfma_f32_16x16x32_bf16 v[120:123], a[84:87], a[168:171], v[120:123]\n\t"
"ds_read_b128 a[164:167], v224 offset:21056\n\t"
"v_mfma_f32_16x16x32_bf16 v[136:139], a[88:91], a[152:155], v[136:139]\n\t"
"s_mov_b32 s76, 229376\n\t"
"ds_read_b64_tr_b16 a[176:177], v248 offset:41472\n\t"
"ds_read_b64_tr_b16 a[178:179], v249 offset:41472\n\t"
"v_mfma_f32_16x16x32_bf16 v[136:139], a[92:95], a[168:171], v[136:139]\n\t"
"ds_read_b64_tr_b16 a[180:181], v248 offset:41984\n\t"
"v_mfma_f32_16x16x32_bf16 v[140:143], a[88:91], a[156:159], v[140:143]\n\t"
"ds_read_b64_tr_b16 a[182:183], v249 offset:41984\n\t"
"v_mfma_f32_16x16x32_bf16 v[140:143], a[92:95], a[172:175], v[140:143]\n\t"
"buffer_load_dwordx4 v236, s[12:15], s22 offen offset:0 nt lds\n\t"
"v_mfma_f32_16x16x32_bf16 v[124:127], a[80:83], a[156:159], v[124:127]\n\t"
"s_lshl_b32 m0, s73, 9\n\t"
"ds_read_b64_tr_b16 a[184:185], v250 offset:43776\n\t"
"v_mfma_f32_16x16x32_bf16 v[124:127], a[84:87], a[172:175], v[124:127]\n\t"
"s_waitcnt lgkmcnt(1)\n\t"
"ds_read_b128 a[168:171], v224 offset:21568\n\t"
"v_mfma_f32_4x4x4_16b_bf16 v[192:195], a[210:211], a[176:177], 0\n\t"
"ds_read_b64_tr_b16 a[186:187], v251 offset:43776\n\t"
"v_mfma_f32_4x4x4_16b_bf16 v[196:199], a[210:211], a[178:179], 0\n\t"
"ds_read_b64_tr_b16 a[188:189], v250 offset:44288\n\t"
"v_mfma_f32_4x4x4_16b_bf16 v[200:203], a[210:211], a[180:181], 0\n\t"
"ds_read_b64_tr_b16 a[190:191], v251 offset:44288\n\t"
"v_mfma_f32_4x4x4_16b_bf16 v[204:207], a[210:211], a[182:183], 0\n\t"
"s_mov_b32 s77, 237568\n\t"
"ds_read_b64_tr_b16 a[192:193], v250 offset:41472\n\t"
"ds_read_b64_tr_b16 a[194:195], v251 offset:41472\n\t"
"v_mfma_f32_16x16x32_bf16 v[108:111], a[72:75], a[156:159], v[108:111]\n\t"
"ds_read_b64_tr_b16 a[196:197], v250 offset:41984\n\t"
"v_mfma_f32_16x16x32_bf16 v[108:111], a[76:79], a[172:175], v[108:111]\n\t"
"s_waitcnt vmcnt(26)\n\t"
"ds_read_b128 a[152:155], v224 offset:17472\n\t"
"v_cvt_pk_bf16_f32 v195, v195, v199\n\t"
"v_mfma_f32_16x16x32_bf16 v[0:3], a[96:99], a[144:147], v[0:3]\n\t"
"ds_read_b64_tr_b16 a[198:199], v251 offset:41984\n\t"
"v_cvt_pk_bf16_f32 v196, v192, v196\n\t"
"v_mfma_f32_16x16x32_bf16 v[0:3], a[100:103], a[160:163], v[0:3]\n\t"
"s_waitcnt vmcnt(24)\n\t"
"ds_read_b64_tr_b16 a[200:201], v248 offset:43776\n\t"
"v_cvt_pk_bf16_f32 v199, v201, v205\n\t"
"v_mfma_f32_16x16x32_bf16 v[16:19], a[104:107], a[144:147], v[16:19]\n\t"
"ds_write_addtid_b32 v196 offset:0\n\t"
"v_cvt_pk_bf16_f32 v197, v193, v197\n\t"
"v_mfma_f32_16x16x32_bf16 v[16:19], a[108:111], a[160:163], v[16:19]\n\t"
"s_waitcnt vmcnt(22)\n\t"
"ds_read_b128 a[172:175], v224 offset:22080\n\t"
"v_cvt_pk_bf16_f32 v194, v194, v198\n\t"
"v_mfma_f32_16x16x32_bf16 v[32:35], a[112:115], a[144:147], v[32:35]\n\t"
"ds_write_addtid_b32 v197 offset:2048\n\t"
"v_cvt_pk_bf16_f32 v198, v200, v204\n\t"
"v_mfma_f32_16x16x32_bf16 v[32:35], a[116:119], a[160:163], v[32:35]\n\t"
"s_waitcnt lgkmcnt(9)\n\t"
"ds_read_b128 a[156:159], v224 offset:17984\n\t"
"v_cvt_pk_bf16_f32 v193, v203, v207\n\t"
"v_mfma_f32_4x4x4_16b_bf16 v[208:211], a[210:211], a[184:185], 0\n\t"
"ds_write_addtid_b32 v199 offset:2304\n\t"
"v_mfma_f32_4x4x4_16b_bf16 v[212:215], a[210:211], a[186:187], 0\n\t"
"ds_read_b128 a[144:147], v224 offset:18496\n\t"
"v_mfma_f32_4x4x4_16b_bf16 v[216:219], a[210:211], a[188:189], 0\n\t"
"s_add_u32 m0, s26, 103936\n\t"
"ds_read_b64_tr_b16 a[202:203], v249 offset:43776\n\t"
"v_mfma_f32_4x4x4_16b_bf16 v[220:223], a[210:211], a[190:191], 0\n\t"
"buffer_load_dwordx4 v237, s[12:15], s22 offen offset:1024 nt lds\n\t"
"v_mfma_f32_16x16x32_bf16 v[36:39], a[112:115], a[148:151], v[36:39]\n\t"
"s_lshl_b32 m0, s73, 9\n\t"
"ds_read_b128 a[160:163], v224 offset:22592\n\t"
"v_cvt_pk_bf16_f32 v192, v202, v206\n\t"
"s_mov_b32 s79, 253952\n\t"
"v_mfma_f32_16x16x32_bf16 v[36:39], a[116:119], a[164:167], v[36:39]\n\t"
"ds_write_addtid_b32 v198 offset:256\n\t"
"v_cvt_pk_bf16_f32 v212, v208, v212\n\t"
"v_mfma_f32_16x16x32_bf16 v[20:23], a[104:107], a[148:151], v[20:23]\n\t"
"ds_read_b64_tr_b16 a[204:205], v248 offset:44288\n\t"
"v_cvt_pk_bf16_f32 v211, v211, v215\n\t"
"v_mfma_f32_16x16x32_bf16 v[20:23], a[108:111], a[164:167], v[20:23]\n\t"
"v_cvt_pk_bf16_f32 v210, v210, v214\n\t"
"v_cvt_pk_bf16_f32 v213, v209, v213\n\t"
"v_mfma_f32_16x16x32_bf16 v[4:7], a[96:99], a[148:151], v[4:7]\n\t"
"ds_write_addtid_b32 v213 offset:10272\n\t"
"v_cvt_pk_bf16_f32 v208, v218, v222\n\t"
"v_mfma_f32_16x16x32_bf16 v[4:7], a[100:103], a[164:167], v[4:7]\n\t"
"s_waitcnt lgkmcnt(13)\n\t"
"ds_read_b128 a[148:151], v224 offset:19008\n\t"
"v_cvt_pk_bf16_f32 v214, v216, v220\n\t"
"v_mfma_f32_16x16x32_bf16 v[8:11], a[96:99], a[152:155], v[8:11]\n\t"
"s_mov_b32 s80, 262144\n\t"
"ds_write_addtid_b32 v212 offset:8224\n\t"
"v_cvt_pk_bf16_f32 v209, v219, v223\n\t"
"v_mfma_f32_16x16x32_bf16 v[8:11], a[100:103], a[168:171], v[8:11]\n\t"
"s_waitcnt lgkmcnt(14)\n\t"
"ds_write_addtid_b32 v214 offset:8480\n\t"
"v_cvt_pk_bf16_f32 v215, v217, v221\n\t"
"v_mfma_f32_4x4x4_16b_bf16 v[200:203], a[210:211], a[192:193], 0\n\t"
"ds_write_addtid_b32 v215 offset:10528\n\t"
"v_mfma_f32_4x4x4_16b_bf16 v[204:207], a[210:211], a[194:195], 0\n\t"
"ds_read_b64_tr_b16 a[206:207], v249 offset:44288\n\t"
"v_mfma_f32_4x4x4_16b_bf16 v[196:199], a[210:211], a[196:197], 0\n\t"
"s_add_u32 m0, s26, 103936\n\t"
"ds_read_b128 a[164:167], v224 offset:23104\n\t"
"v_mfma_f32_4x4x4_16b_bf16 v[216:219], a[210:211], a[198:199], 0\n\t"
"buffer_load_dwordx4 v238, s[12:15], s22 offen offset:2048 nt lds\n\t"
"v_mfma_f32_16x16x32_bf16 v[24:27], a[104:107], a[152:155], v[24:27]\n\t"
"v_mfma_f32_16x16x32_bf16 v[24:27], a[108:111], a[168:171], v[24:27]\n\t"
"v_cvt_pk_bf16_f32 v202, v202, v206\n\t"
"v_cvt_pk_bf16_f32 v203, v203, v207\n\t"
"v_mfma_f32_16x16x32_bf16 v[40:43], a[112:115], a[152:155], v[40:43]\n\t"
"v_cvt_pk_bf16_f32 v204, v200, v204\n\t"
"v_cvt_pk_bf16_f32 v205, v201, v205\n\t"
"v_mfma_f32_16x16x32_bf16 v[40:43], a[116:119], a[168:171], v[40:43]\n\t"
"s_waitcnt lgkmcnt(13)\n\t"
"ds_read_b128 a[152:155], v224 offset:19520\n\t"
"v_cvt_pk_bf16_f32 v201, v199, v219\n\t"
"v_mfma_f32_16x16x32_bf16 v[44:47], a[112:115], a[156:159], v[44:47]\n\t"
"v_cvt_pk_bf16_f32 v200, v198, v218\n\t"
"v_cvt_pk_bf16_f32 v206, v196, v216\n\t"
"v_mfma_f32_16x16x32_bf16 v[44:47], a[116:119], a[172:175], v[44:47]\n\t"
"buffer_load_dwordx4 v239, s[12:15], s22 offen offset:3072 nt lds\n\t"
"s_mov_b64 exec, s[36:37]\n\t"
"buffer_load_dwordx4 v240, s[12:15], s22 offen offset:3584 nt lds\n\t"
"s_mov_b64 exec, -1\n\t"
"s_lshl_b32 m0, s73, 9\n\t"
"v_mfma_f32_16x16x32_bf16 v[28:31], a[104:107], a[156:159], v[28:31]\n\t"
"ds_write_addtid_b32 v206 offset:4352\n\t"
"v_cvt_pk_bf16_f32 v207, v197, v217\n\t"
"v_mfma_f32_16x16x32_bf16 v[28:31], a[108:111], a[172:175], v[28:31]\n\t"
"s_waitcnt lgkmcnt(3)\n\t"
"buffer_load_dwordx4 a[48:51], v252, s[8:11], s64 offen offset:0 sc1\n\t"
"buffer_load_dwordx4 a[52:55], v252, s[8:11], s64 offen offset:1024 sc1\n\t"
"v_mfma_f32_4x4x4_16b_bf16 v[220:223], a[210:211], a[200:201], 0\n\t"
"ds_write_addtid_b32 v205 offset:6144\n\t"
"v_mfma_f32_4x4x4_16b_bf16 v[212:215], a[210:211], a[202:203], 0\n\t"
"buffer_load_dwordx4 a[56:59], v252, s[8:11], s64 offen offset:2048 sc1\n\t"
"buffer_load_dwordx4 a[60:63], v252, s[8:11], s64 offen offset:3072 sc1\n\t"
"v_mfma_f32_4x4x4_16b_bf16 v[196:199], a[210:211], a[204:205], 0\n\t"
"ds_write_addtid_b32 v204 offset:4096\n\t"
"v_mfma_f32_4x4x4_16b_bf16 v[216:219], a[210:211], a[206:207], 0\n\t"
"buffer_load_dwordx4 a[64:67], v235, s[8:11], s65 offen offset:0 sc1\n\t"
"buffer_load_dwordx4 a[68:71], v235, s[8:11], s65 offen offset:1024 sc1\n\t"
"v_mfma_f32_16x16x32_bf16 v[12:15], a[96:99], a[156:159], v[12:15]\n\t"
"s_mov_b32 s83, 286720\n\t"
"ds_read_b128 a[156:159], v224 offset:20032\n\t"
"ds_write_addtid_b32 v207 offset:6400\n\t"
"v_mfma_f32_16x16x32_bf16 v[12:15], a[100:103], a[172:175], v[12:15]\n\t"
"s_waitcnt vmcnt(30)\n\t"
"buffer_load_dwordx4 a[72:75], v252, s[8:11], s63 offen offset:2048 sc1\n\t"
"buffer_load_dwordx4 a[76:79], v252, s[8:11], s63 offen offset:3072 sc1\n\t"
"v_cvt_pk_bf16_f32 v222, v222, v214\n\t"
"v_cvt_pk_bf16_f32 v223, v223, v215\n\t"
"v_mfma_f32_16x16x32_bf16 v[96:99], a[120:123], a[144:147], v[96:99]\n\t"
"v_cvt_pk_bf16_f32 v212, v220, v212\n\t"
"v_cvt_pk_bf16_f32 v213, v221, v213\n\t"
"v_mfma_f32_16x16x32_bf16 v[96:99], a[124:127], a[160:163], v[96:99]\n\t"
"s_waitcnt vmcnt(29)\n\t"
"ds_write_addtid_b32 v212 offset:12320\n\t"
"buffer_load_dwordx4 a[80:83], v235, s[8:11], s64 offen offset:0 sc1\n\t"
"buffer_load_dwordx4 a[84:87], v235, s[8:11], s64 offen offset:1024 sc1\n\t"
"v_cvt_pk_bf16_f32 v214, v196, v216\n\t"
"v_mfma_f32_16x16x32_bf16 v[112:115], a[128:131], a[144:147], v[112:115]\n\t"
"ds_write_addtid_b32 v213 offset:14368\n\t"
"v_cvt_pk_bf16_f32 v215, v197, v217\n\t"
"v_mfma_f32_16x16x32_bf16 v[112:115], a[132:135], a[160:163], v[112:115]\n\t"
"s_waitcnt vmcnt(29)\n\t"
"ds_write_addtid_b32 v214 offset:12576\n\t"
"ds_write_addtid_b32 v215 offset:14624\n\t"
"s_mov_b32 s82, 278528\n\t"
"v_mfma_f32_16x16x32_bf16 v[128:131], a[136:139], a[144:147], v[128:131]\n\t"
"ds_read_b128 a[172:175], v224 offset:24128\n\t"
"v_cvt_pk_bf16_f32 v221, v199, v219\n\t"
"v_mfma_f32_16x16x32_bf16 v[128:131], a[140:143], a[160:163], v[128:131]\n\t"
"s_waitcnt lgkmcnt(11)\n\t"
"buffer_load_dwordx4 a[88:91], v235, s[8:11], s64 offen offset:2048 sc1\n\t"
"buffer_load_dwordx4 a[92:95], v235, s[8:11], s64 offen offset:3072 sc1\n\t"
"buffer_load_dwordx4 a[96:99], v252, s[8:11], s46 offen offset:0 sc1\n\t"
"buffer_load_dwordx4 a[100:103], v252, s[8:11], s46 offen offset:1024 sc1\n\t"
"v_mfma_f32_16x16x32_bf16 v[132:135], a[136:139], a[148:151], v[132:135]\n\t"
"ds_read_b128 a[168:171], v224 offset:23616\n\t"
"v_cvt_pk_bf16_f32 v220, v198, v218\n\t"
"v_mfma_f32_16x16x32_bf16 v[132:135], a[140:143], a[164:167], v[132:135]\n\t"
"buffer_load_dwordx4 a[104:107], v252, s[8:11], s46 offen offset:2048 sc1\n\t"
"buffer_load_dwordx4 a[108:111], v252, s[8:11], s46 offen offset:3072 sc1\n\t"
"v_mfma_f32_16x16x32_bf16 v[116:119], a[128:131], a[148:151], v[116:119]\n\t"
"v_mfma_f32_16x16x32_bf16 v[116:119], a[132:135], a[164:167], v[116:119]\n\t"
"buffer_load_dwordx4 a[112:115], v235, s[8:11], s47 offen offset:0 sc1\n\t"
"v_mfma_f32_16x16x32_bf16 v[100:103], a[120:123], a[148:151], v[100:103]\n\t"
"buffer_load_dwordx4 a[116:119], v235, s[8:11], s47 offen offset:1024 sc1\n\t"
"v_mfma_f32_16x16x32_bf16 v[100:103], a[124:127], a[164:167], v[100:103]\n\t"
"v_add_u32 v246, 8, v228\n\t"
"v_add_u32 v247, 16, v228\n\t"
"s_waitcnt lgkmcnt(0)\n\t"
"s_barrier\n\t"
"Lunit_%=:\n\t"
"v_mfma_f32_16x16x32_bf16 v[104:107], a[120:123], a[152:155], v[104:107]\n\t"
"ds_read_b128 a[148:151], v224 offset:2560\n\t"
"v_mfma_f32_16x16x32_bf16 v[104:107], a[124:127], a[168:171], v[104:107]\n\t"
"ds_read_b128 a[144:147], v224 offset:2048\n\t"
"v_mfma_f32_16x16x32_bf16 v[120:123], a[128:131], a[152:155], v[120:123]\n\t"
"ds_read_b128 a[160:163], v224 offset:6144\n\t"
"v_mfma_f32_16x16x32_bf16 v[120:123], a[132:135], a[168:171], v[120:123]\n\t"
"ds_read_b128 a[164:167], v224 offset:6656\n\t"
"v_mfma_f32_16x16x32_bf16 v[136:139], a[136:139], a[152:155], v[136:139]\n\t"
"s_nop 0\n\t"
"ds_read_b128 a[152:155], v224 offset:3072\n\t"
"ds_write_addtid_b32 v209 offset:26976\n\t"
"v_mfma_f32_16x16x32_bf16 v[136:139], a[140:143], a[168:171], v[136:139]\n\t"
"ds_write_addtid_b32 v194 offset:16448\n\t"
"v_mfma_f32_16x16x32_bf16 v[140:143], a[136:139], a[156:159], v[140:143]\n\t"
"s_nop 0\n\t"
"ds_read_b128 a[168:171], v224 offset:7168\n\t"
"ds_write_addtid_b32 v208 offset:24928\n\t"
"v_mfma_f32_16x16x32_bf16 v[140:143], a[140:143], a[172:175], v[140:143]\n\t"
"buffer_load_dwordx4 a[136:139], v235, s[8:11], s46 offen offset:2048 sc1\n\t"
"v_mfma_f32_16x16x32_bf16 v[124:127], a[128:131], a[156:159], v[124:127]\n\t"
"ds_write_addtid_b32 v221 offset:31072\n\t"
"v_mfma_f32_16x16x32_bf16 v[124:127], a[132:135], a[172:175], v[124:127]\n\t"
"buffer_load_dwordx4 a[140:143], v235, s[8:11], s46 offen offset:3072 sc1\n\t"
"v_mfma_f32_16x16x32_bf16 v[108:111], a[120:123], a[156:159], v[108:111]\n\t"
"ds_read_b128 a[156:159], v224 offset:3584\n\t"
"v_mfma_f32_16x16x32_bf16 v[108:111], a[124:127], a[172:175], v[108:111]\n\t"
"s_waitcnt vmcnt(36) lgkmcnt(8)\n\t"
"buffer_load_dwordx4 a[132:135], v235, s[8:11], s46 offen offset:1024 sc1\n\t"
"v_mfma_f32_16x16x32_bf16 v[48:51], a[0:3], a[144:147], v[0:3]\n\t"
"ds_read_b128 a[172:175], v224 offset:7680\n\t"
"v_mfma_f32_16x16x32_bf16 v[48:51], a[4:7], a[160:163], v[48:51]\n\t"
"s_waitcnt vmcnt(35) lgkmcnt(8)\n\t"
"ds_write_addtid_b32 v202 offset:20544\n\t"
"v_mfma_f32_16x16x32_bf16 v[64:67], a[8:11], a[144:147], v[16:19]\n\t"
"ds_write_addtid_b32 v193 offset:18752\n\t"
"v_mfma_f32_16x16x32_bf16 v[64:67], a[12:15], a[160:163], v[64:67]\n\t"
"s_waitcnt vmcnt(33) lgkmcnt(7)\n\t"
"buffer_load_dwordx4 a[128:131], v235, s[8:11], s46 offen offset:0 sc1\n\t"
"v_mfma_f32_16x16x32_bf16 v[80:83], a[16:19], a[144:147], v[32:35]\n\t"
"ds_write_addtid_b32 v222 offset:28768\n\t"
"v_mfma_f32_16x16x32_bf16 v[80:83], a[20:23], a[160:163], v[80:83]\n\t"
"buffer_load_dwordx4 a[120:123], v252, s[8:11], s42 offen offset:2048 sc1\n\t"
"v_mfma_f32_16x16x32_bf16 v[84:87], a[16:19], a[148:151], v[36:39]\n\t"
"ds_read_b128 a[160:163], v224 offset:4096\n\t"
"v_mfma_f32_16x16x32_bf16 v[84:87], a[20:23], a[164:167], v[84:87]\n\t"
"ds_read_b128 a[144:147], v224 offset:0\n\t"
"v_mfma_f32_16x16x32_bf16 v[68:71], a[8:11], a[148:151], v[20:23]\n\t"
"ds_write_addtid_b32 v210 offset:24672\n\t"
"v_mfma_f32_16x16x32_bf16 v[68:71], a[12:15], a[164:167], v[68:71]\n\t"
"buffer_load_dwordx4 a[124:127], v252, s[8:11], s42 offen offset:3072 sc1\n\t"
"v_mfma_f32_16x16x32_bf16 v[52:55], a[0:3], a[148:151], v[4:7]\n\t"
"ds_read_b128 a[148:151], v224 offset:512\n\t"
"v_mfma_f32_16x16x32_bf16 v[52:55], a[4:7], a[164:167], v[52:55]\n\t"
"s_waitcnt lgkmcnt(6)\n\t"
"ds_read_b128 a[164:167], v224 offset:4608\n\t"
"ds_write_addtid_b32 v192 offset:16704\n\t"
"v_mfma_f32_16x16x32_bf16 v[56:59], a[0:3], a[152:155], v[8:11]\n\t"
"ds_write_addtid_b32 v211 offset:26720\n\t"
"v_mfma_f32_16x16x32_bf16 v[56:59], a[4:7], a[168:171], v[56:59]\n\t"
"ds_write_addtid_b32 v203 offset:22592\n\t"
"v_mfma_f32_16x16x32_bf16 v[72:75], a[8:11], a[152:155], v[24:27]\n\t"
"ds_write_addtid_b32 v223 offset:30816\n\t"
"v_mfma_f32_16x16x32_bf16 v[72:75], a[12:15], a[168:171], v[72:75]\n\t"
"ds_write_addtid_b32 v201 offset:22848\n\t"
"v_mfma_f32_16x16x32_bf16 v[88:91], a[16:19], a[152:155], v[40:43]\n\t"
"v_mfma_f32_16x16x32_bf16 v[88:91], a[20:23], a[168:171], v[88:91]\n\t"
"ds_write_b128 v254, a[248:251] offset:20736\n\t"
"v_mfma_f32_16x16x32_bf16 v[92:95], a[16:19], a[156:159], v[44:47]\n\t"
"v_mfma_f32_16x16x32_bf16 v[92:95], a[20:23], a[172:175], v[92:95]\n\t"
"buffer_load_dwordx4 a[16:19], v235, s[8:11], s50 offen offset:0 sc1\n\t"
"v_mfma_f32_16x16x32_bf16 v[76:79], a[8:11], a[156:159], v[28:31]\n\t"
"v_mfma_f32_16x16x32_bf16 v[76:79], a[12:15], a[172:175], v[76:79]\n\t"
"buffer_load_dwordx4 a[20:23], v235, s[8:11], s50 offen offset:1024 sc1\n\t"
"v_mfma_f32_16x16x32_bf16 v[60:63], a[0:3], a[156:159], v[12:15]\n\t"
"ds_write_addtid_b32 v195 offset:18496\n\t"
"v_mfma_f32_16x16x32_bf16 v[60:63], a[4:7], a[172:175], v[60:63]\n\t"
"s_waitcnt vmcnt(36) lgkmcnt(4)\n\t"
"buffer_load_dwordx4 a[12:15], v252, s[8:11], s49 offen offset:3072 sc1\n\t"
"v_mfma_f32_16x16x32_bf16 v[0:3], a[24:27], a[144:147], v[0:3]\n\t"
"ds_write_addtid_b32 v200 offset:20800\n\t"
"v_mfma_f32_16x16x32_bf16 v[0:3], a[28:31], a[160:163], v[0:3]\n\t"
"s_waitcnt vmcnt(35) lgkmcnt(3)\n\t"
"buffer_load_dwordx4 a[8:11], v252, s[8:11], s49 offen offset:2048 sc1\n\t"
"v_mfma_f32_16x16x32_bf16 v[16:19], a[32:35], a[144:147], v[16:19]\n\t"
"ds_read_b128 a[172:175], v224 offset:5632\n\t"
"v_mfma_f32_16x16x32_bf16 v[16:19], a[36:39], a[160:163], v[16:19]\n\t"
"s_waitcnt vmcnt(34) lgkmcnt(3)\n\t"
"ds_write_addtid_b32 v220 offset:29024\n\t"
"v_mfma_f32_16x16x32_bf16 v[32:35], a[40:43], a[144:147], v[32:35]\n\t"
"ds_read_b128 a[168:171], v224 offset:5120\n\t"
"v_mfma_f32_16x16x32_bf16 v[32:35], a[44:47], a[160:163], v[32:35]\n\t"
"ds_read_b128 a[156:159], v224 offset:1536\n\t"
"v_mfma_f32_16x16x32_bf16 v[36:39], a[40:43], a[148:151], v[36:39]\n\t"
"ds_read_b128 a[152:155], v224 offset:1024\n\t"
"v_mfma_f32_16x16x32_bf16 v[36:39], a[44:47], a[164:167], v[36:39]\n\t"
"buffer_load_dwordx4 a[0:3], v252, s[8:11], s49 offen offset:0 sc1\n\t"
"v_mfma_f32_16x16x32_bf16 v[20:23], a[32:35], a[148:151], v[20:23]\n\t"
"v_mfma_f32_16x16x32_bf16 v[20:23], a[36:39], a[164:167], v[20:23]\n\t"
"buffer_load_dwordx4 a[4:7], v252, s[8:11], s49 offen offset:1024 sc1\n\t"
"v_mfma_f32_16x16x32_bf16 v[4:7], a[24:27], a[148:151], v[4:7]\n\t"
"v_mfma_f32_16x16x32_bf16 v[4:7], a[28:31], a[164:167], v[4:7]\n\t"
"s_waitcnt vmcnt(30) lgkmcnt(5)\n\t"
"s_waitcnt lgkmcnt(0)\n\t"
"s_barrier\n\t"
"v_mfma_f32_16x16x32_bf16 v[8:11], a[24:27], a[152:155], v[8:11]\n\t"
"s_add_u32 m0, s26, 55680\n\t"
"ds_read_b128 a[144:147], v224 offset:18496\n\t"
"v_mfma_f32_16x16x32_bf16 v[8:11], a[28:31], a[168:171], v[8:11]\n\t"
"ds_read_b128 a[148:151], v224 offset:19008\n\t"
"v_mfma_f32_16x16x32_bf16 v[24:27], a[32:35], a[152:155], v[24:27]\n\t"
"s_add_u32 s24, s24, 3145728\n\t"
"ds_read_b128 a[160:163], v224 offset:22592\n\t"
"ds_read_b64_tr_b16 a[176:177], v248 offset:0\n\t"
"v_mfma_f32_16x16x32_bf16 v[24:27], a[36:39], a[168:171], v[24:27]\n\t"
"s_add_u32 s20, s20, 3145728\n\t"
"ds_read_b128 a[164:167], v224 offset:23104\n\t"
"ds_read_b64_tr_b16 a[178:179], v249 offset:0\n\t"
"v_mfma_f32_16x16x32_bf16 v[40:43], a[40:43], a[152:155], v[40:43]\n\t"
"ds_read_b64_tr_b16 a[180:181], v248 offset:512\n\t"
"v_mfma_f32_16x16x32_bf16 v[40:43], a[44:47], a[168:171], v[40:43]\n\t"
"s_add_u32 s27, s27, 3145728\n\t"
"ds_read_b128 a[168:171], v224 offset:23616\n\t"
"ds_read_b64_tr_b16 a[182:183], v249 offset:512\n\t"
"v_mfma_f32_16x16x32_bf16 v[44:47], a[40:43], a[156:159], v[44:47]\n\t"
"ds_read_b64_tr_b16 a[184:185], v250 offset:2304\n\t"
"v_mfma_f32_16x16x32_bf16 v[44:47], a[44:47], a[172:175], v[44:47]\n\t"
"buffer_load_dwordx4 v236, s[12:15], s22 offen offset:256 nt lds\n\t"
"v_mfma_f32_16x16x32_bf16 v[28:31], a[32:35], a[156:159], v[28:31]\n\t"
"ds_read_b64_tr_b16 a[186:187], v251 offset:2304\n\t"
"v_mfma_f32_16x16x32_bf16 v[28:31], a[36:39], a[172:175], v[28:31]\n\t"
"s_waitcnt lgkmcnt(2)\n\t"
"ds_read_b64_tr_b16 a[188:189], v250 offset:2816\n\t"
"v_mfma_f32_4x4x4_16b_bf16 v[192:195], a[210:211], a[176:177], 0\n\t"
"ds_read_b64_tr_b16 a[190:191], v251 offset:2816\n\t"
"v_mfma_f32_4x4x4_16b_bf16 v[196:199], a[210:211], a[178:179], 0\n\t"
"ds_read_b64_tr_b16 a[192:193], v250 offset:0\n\t"
"v_mfma_f32_4x4x4_16b_bf16 v[200:203], a[210:211], a[180:181], 0\n\t"
"ds_read_b128 a[152:155], v224 offset:19520\n\t"
"v_mfma_f32_4x4x4_16b_bf16 v[204:207], a[210:211], a[182:183], 0\n\t"
"buffer_load_dwordx4 v237, s[12:15], s22 offen offset:1280 nt lds\n\t"
"v_mfma_f32_16x16x32_bf16 v[12:15], a[24:27], a[156:159], v[12:15]\n\t"
"ds_read_b64_tr_b16 a[194:195], v251 offset:0\n\t"
"v_mfma_f32_16x16x32_bf16 v[12:15], a[28:31], a[172:175], v[12:15]\n\t"
"s_waitcnt vmcnt(30)\n\t"
"ds_read_b64_tr_b16 a[196:197], v250 offset:512\n\t"
"v_cvt_pk_bf16_f32 v195, v195, v199\n\t"
"v_mfma_f32_16x16x32_bf16 v[144:147], a[48:51], a[144:147], v[96:99]\n\t"
"ds_read_b64_tr_b16 a[198:199], v251 offset:512\n\t"
"v_cvt_pk_bf16_f32 v194, v194, v198\n\t"
"v_mfma_f32_16x16x32_bf16 v[144:147], a[52:55], a[160:163], v[144:147]\n\t"
"s_waitcnt vmcnt(28) lgkmcnt(8)\n\t"
"ds_read_b128 a[156:159], v224 offset:20032\n\t"
"v_cvt_pk_bf16_f32 v197, v193, v197\n\t"
"v_mfma_f32_16x16x32_bf16 v[160:163], a[56:59], a[144:147], v[112:115]\n\t"
"ds_read_b64_tr_b16 a[200:201], v248 offset:2304\n\t"
"v_cvt_pk_bf16_f32 v193, v203, v207\n\t"
"v_mfma_f32_16x16x32_bf16 v[160:163], a[60:63], a[160:163], v[160:163]\n\t"
"s_waitcnt vmcnt(26) lgkmcnt(8)\n\t"
"buffer_load_dwordx4 v238, s[12:15], s22 offen offset:2304 nt lds\n\t"
"v_mfma_f32_16x16x32_bf16 v[176:179], a[64:67], a[144:147], v[128:131]\n\t"
"s_lshl_b32 m0, s73, 9\n\t"
"ds_read_b128 a[172:175], v224 offset:24128\n\t"
"v_cvt_pk_bf16_f32 v196, v192, v196\n\t"
"v_mfma_f32_16x16x32_bf16 v[176:179], a[68:71], a[160:163], v[176:179]\n\t"
"s_waitcnt lgkmcnt(7)\n\t"
"ds_read_b128 a[144:147], v224 offset:16448\n\t"
"v_cvt_pk_bf16_f32 v199, v201, v205\n\t"
"v_mfma_f32_4x4x4_16b_bf16 v[208:211], a[210:211], a[184:185], 0\n\t"
"ds_write_addtid_b32 v197 offset:2048\n\t"
"v_mfma_f32_4x4x4_16b_bf16 v[212:215], a[210:211], a[186:187], 0\n\t"
"ds_write_addtid_b32 v199 offset:2304\n\t"
"v_mfma_f32_4x4x4_16b_bf16 v[216:219], a[210:211], a[188:189], 0\n\t"
"s_add_u32 m0, s26, 55680\n\t"
"ds_read_b128 a[160:163], v224 offset:20544\n\t"
"v_mfma_f32_4x4x4_16b_bf16 v[220:223], a[210:211], a[190:191], 0\n\t"
"buffer_load_dwordx4 v239, s[12:15], s22 offen offset:3328 nt lds\n\t"
"s_lshl_b32 m0, s73, 9\n\t"
"v_mfma_f32_16x16x32_bf16 v[180:183], a[64:67], a[148:151], v[132:135]\n\t"
"ds_write_addtid_b32 v196 offset:0\n\t"
"v_cvt_pk_bf16_f32 v198, v200, v204\n\t"
"v_mfma_f32_16x16x32_bf16 v[180:183], a[68:71], a[164:167], v[180:183]\n\t"
"buffer_load_dwordx4 a[40:43], v235, s[8:11], s49 offen offset:2048 sc1\n\t"
"v_mfma_f32_16x16x32_bf16 v[164:167], a[56:59], a[148:151], v[116:119]\n\t"
"v_cvt_pk_bf16_f32 v192, v202, v206\n\t"
"v_cvt_pk_bf16_f32 v213, v209, v213\n\t"
"v_mfma_f32_16x16x32_bf16 v[164:167], a[60:63], a[164:167], v[164:167]\n\t"
"v_cvt_pk_bf16_f32 v212, v208, v212\n\t"
"v_cvt_pk_bf16_f32 v211, v211, v215\n\t"
"v_mfma_f32_16x16x32_bf16 v[148:151], a[48:51], a[148:151], v[100:103]\n\t"
"ds_write_addtid_b32 v198 offset:256\n\t"
"v_cvt_pk_bf16_f32 v210, v210, v214\n\t"
"s_add_u32 m0, s26, 55680\n\t"
"v_mfma_f32_16x16x32_bf16 v[148:151], a[52:55], a[164:167], v[148:151]\n\t"
"s_waitcnt lgkmcnt(5)\n\t"
"s_mov_b64 exec, s[36:37]\n\t"
"buffer_load_dwordx4 v240, s[12:15], s22 offen offset:3840 nt lds\n\t"
"s_mov_b64 exec, -1\n\t"
"v_mfma_f32_16x16x32_bf16 v[152:155], a[48:51], a[152:155], v[104:107]\n\t"
"s_lshl_b32 m0, s73, 9\n\t"
"ds_read_b64_tr_b16 a[202:203], v249 offset:2304\n\t"
"v_cvt_pk_bf16_f32 v214, v216, v220\n\t"
"v_mfma_f32_16x16x32_bf16 v[152:155], a[52:55], a[168:171], v[152:155]\n\t"
"v_cvt_pk_bf16_f32 v208, v218, v222\n\t"
"v_cvt_pk_bf16_f32 v209, v219, v223\n\t"
"v_mfma_f32_4x4x4_16b_bf16 v[200:203], a[210:211], a[192:193], 0\n\t"
"ds_write_addtid_b32 v212 offset:8224\n\t"
"v_mfma_f32_4x4x4_16b_bf16 v[204:207], a[210:211], a[194:195], 0\n\t"
"v_cvt_pk_bf16_f32 v215, v217, v221\n\t"
"v_mfma_f32_4x4x4_16b_bf16 v[196:199], a[210:211], a[196:197], 0\n\t"
"ds_write_addtid_b32 v213 offset:10272\n\t"
"v_mfma_f32_4x4x4_16b_bf16 v[216:219], a[210:211], a[198:199], 0\n\t"
"ds_read_b64_tr_b16 a[204:205], v248 offset:2816\n\t"
"v_mfma_f32_16x16x32_bf16 v[168:171], a[56:59], a[152:155], v[120:123]\n\t"
"ds_read_b64_tr_b16 a[206:207], v249 offset:2816\n\t"
"v_mfma_f32_16x16x32_bf16 v[168:171], a[60:63], a[168:171], v[168:171]\n\t"
"ds_read_b128 a[164:167], v224 offset:21056\n\t"
"v_cvt_pk_bf16_f32 v203, v203, v207\n\t"
"v_mfma_f32_16x16x32_bf16 v[184:187], a[64:67], a[152:155], v[136:139]\n\t"
"ds_write_addtid_b32 v214 offset:8480\n\t"
"v_cvt_pk_bf16_f32 v204, v200, v204\n\t"
"v_mfma_f32_16x16x32_bf16 v[184:187], a[68:71], a[168:171], v[184:187]\n\t"
"ds_read_b128 a[148:151], v224 offset:16960\n\t"
"v_cvt_pk_bf16_f32 v207, v197, v217\n\t"
"v_mfma_f32_16x16x32_bf16 v[188:191], a[64:67], a[156:159], v[140:143]\n\t"
"ds_read_b128 a[168:171], v224 offset:21568\n\t"
"v_cvt_pk_bf16_f32 v205, v201, v205\n\t"
"v_mfma_f32_16x16x32_bf16 v[188:191], a[68:71], a[172:175], v[188:191]\n\t"
"v_cvt_pk_bf16_f32 v200, v198, v218\n\t"
"v_cvt_pk_bf16_f32 v201, v199, v219\n\t"
"v_mfma_f32_16x16x32_bf16 v[172:175], a[56:59], a[156:159], v[124:127]\n\t"
"ds_write_addtid_b32 v215 offset:10528\n\t"
"v_cvt_pk_bf16_f32 v202, v202, v206\n\t"
"v_mfma_f32_16x16x32_bf16 v[172:175], a[60:63], a[172:175], v[172:175]\n\t"
"s_waitcnt lgkmcnt(5)\n\t"
"ds_read_b128 a[152:155], v224 offset:17472\n\t"
"v_cvt_pk_bf16_f32 v206, v196, v216\n\t"
"v_mfma_f32_4x4x4_16b_bf16 v[220:223], a[210:211], a[200:201], 0\n\t"
"ds_write_addtid_b32 v206 offset:4352\n\t"
"v_mfma_f32_4x4x4_16b_bf16 v[212:215], a[210:211], a[202:203], 0\n\t"
"ds_write_addtid_b32 v205 offset:6144\n\t"
"v_mfma_f32_4x4x4_16b_bf16 v[196:199], a[210:211], a[204:205], 0\n\t"
"ds_write_addtid_b32 v207 offset:6400\n\t"
"v_mfma_f32_4x4x4_16b_bf16 v[216:219], a[210:211], a[206:207], 0\n\t"
"ds_write_addtid_b32 v204 offset:4096\n\t"
"v_mfma_f32_16x16x32_bf16 v[156:159], a[48:51], a[156:159], v[108:111]\n\t"
"ds_read_b128 a[156:159], v224 offset:17984\n\t"
"v_mfma_f32_16x16x32_bf16 v[156:159], a[52:55], a[172:175], v[156:159]\n\t"
"s_waitcnt vmcnt(28) lgkmcnt(8)\n\t"
"v_cvt_pk_bf16_f32 v222, v222, v214\n\t"
"v_cvt_pk_bf16_f32 v223, v223, v215\n\t"
"v_mfma_f32_16x16x32_bf16 v[96:99], a[72:75], a[144:147], v[96:99]\n\t"
"v_cvt_pk_bf16_f32 v212, v220, v212\n\t"
"v_cvt_pk_bf16_f32 v213, v221, v213\n\t"
"v_mfma_f32_16x16x32_bf16 v[96:99], a[76:79], a[160:163], v[96:99]\n\t"
"s_waitcnt vmcnt(26) lgkmcnt(7)\n\t"
"ds_write_addtid_b32 v212 offset:12320\n\t"
"v_cvt_pk_bf16_f32 v215, v197, v217\n\t"
"v_mfma_f32_16x16x32_bf16 v[112:115], a[80:83], a[144:147], v[112:115]\n\t"
"ds_write_addtid_b32 v215 offset:14624\n\t"
"v_cvt_pk_bf16_f32 v214, v196, v216\n\t"
"v_mfma_f32_16x16x32_bf16 v[112:115], a[84:87], a[160:163], v[112:115]\n\t"
"s_waitcnt vmcnt(24) lgkmcnt(7)\n\t"
"ds_write_addtid_b32 v214 offset:12576\n\t"
"ds_write_addtid_b32 v213 offset:14368\n\t"
"s_add_u32 s25, s25, 3145728\n\t"
"v_mfma_f32_16x16x32_bf16 v[128:131], a[88:91], a[144:147], v[128:131]\n\t"
"ds_read_b128 a[172:175], v224 offset:22080\n\t"
"v_cvt_pk_bf16_f32 v220, v198, v218\n\t"
"v_mfma_f32_16x16x32_bf16 v[128:131], a[92:95], a[160:163], v[128:131]\n\t"
"v_cvt_pk_bf16_f32 v221, v199, v219\n\t"
"v_mfma_f32_16x16x32_bf16 v[132:135], a[88:91], a[148:151], v[132:135]\n\t"
"v_mfma_f32_16x16x32_bf16 v[132:135], a[92:95], a[164:167], v[132:135]\n\t"
"v_mfma_f32_16x16x32_bf16 v[116:119], a[80:83], a[148:151], v[116:119]\n\t"
"v_mfma_f32_16x16x32_bf16 v[116:119], a[84:87], a[164:167], v[116:119]\n\t"
"v_mfma_f32_16x16x32_bf16 v[100:103], a[72:75], a[148:151], v[100:103]\n\t"
"s_waitcnt lgkmcnt(0)\n\t"
"v_mfma_f32_16x16x32_bf16 v[100:103], a[76:79], a[164:167], v[100:103]\n\t"
"buffer_load_dwordx4 a[44:47], v235, s[8:11], s49 offen offset:3072 sc1\n\t"
"s_barrier\n\t"
"v_mfma_f32_16x16x32_bf16 v[104:107], a[72:75], a[152:155], v[104:107]\n\t"
"s_nop 0\n\t"
"ds_read_b128 a[144:147], v224 offset:2048\n\t"
"ds_read_b128 a[160:163], v224 offset:6144\n\t"
"v_mfma_f32_16x16x32_bf16 v[104:107], a[76:79], a[168:171], v[104:107]\n\t"
"buffer_load_dwordx4 a[36:39], v235, s[8:11], s49 offen offset:1024 sc1\n\t"
"v_mfma_f32_16x16x32_bf16 v[120:123], a[80:83], a[152:155], v[120:123]\n\t"
"ds_read_b128 a[148:151], v224 offset:2560\n\t"
"v_accvgpr_read_b32 v248, a208\n\t"
"v_mfma_f32_16x16x32_bf16 v[120:123], a[84:87], a[168:171], v[120:123]\n\t"
"ds_read_b128 a[164:167], v224 offset:6656\n\t"
"v_add_u32 v248, 32896, v248\n\t"
"v_mfma_f32_16x16x32_bf16 v[136:139], a[88:91], a[152:155], v[136:139]\n\t"
"ds_write_addtid_b32 v194 offset:16448\n\t"
"v_xor_b32 v249, 32, v248\n\t"
"v_mfma_f32_16x16x32_bf16 v[136:139], a[92:95], a[168:171], v[136:139]\n\t"
"buffer_load_dwordx4 a[32:35], v235, s[8:11], s49 offen offset:0 sc1\n\t"
"v_mfma_f32_16x16x32_bf16 v[140:143], a[88:91], a[156:159], v[140:143]\n\t"
"ds_read_b128 a[168:171], v224 offset:7168\n\t"
"v_xor_b32 v226, 64, v226\n\t"
"v_mfma_f32_16x16x32_bf16 v[140:143], a[92:95], a[172:175], v[140:143]\n\t"
"ds_read_b128 a[152:155], v224 offset:3072\n\t"
"v_xor_b32 v227, 64, v227\n\t"
"v_mfma_f32_16x16x32_bf16 v[124:127], a[80:83], a[156:159], v[124:127]\n\t"
"ds_write_addtid_b32 v221 offset:31072\n\t"
"v_mfma_f32_16x16x32_bf16 v[124:127], a[84:87], a[172:175], v[124:127]\n\t"
"buffer_load_dwordx4 a[24:27], v252, s[8:11], s48 offen offset:2048 sc1\n\t"
"v_mfma_f32_16x16x32_bf16 v[108:111], a[72:75], a[156:159], v[108:111]\n\t"
"ds_read_b128 a[156:159], v224 offset:3584\n\t"
"v_xor_b32 v250, 64, v248\n\t"
"v_mfma_f32_16x16x32_bf16 v[108:111], a[76:79], a[172:175], v[108:111]\n\t"
"s_waitcnt vmcnt(23) lgkmcnt(7)\n\t"
"buffer_load_dwordx4 a[28:31], v252, s[8:11], s48 offen offset:3072 sc1\n\t"
"v_mfma_f32_16x16x32_bf16 v[48:51], a[96:99], a[144:147], v[48:51]\n\t"
"s_add_u32 s22, s22, 3145728\n\t"
"ds_read_b128 a[172:175], v224 offset:7680\n\t"
"ds_write_addtid_b32 v203 offset:22592\n\t"
"v_mfma_f32_16x16x32_bf16 v[48:51], a[100:103], a[160:163], v[48:51]\n\t"
"ds_write_addtid_b32 v222 offset:28768\n\t"
"v_xor_b32 v251, 96, v248\n\t"
"v_mfma_f32_16x16x32_bf16 v[64:67], a[104:107], a[144:147], v[64:67]\n\t"
"s_cmp_eq_u32 s21, 1\n\t"
"s_cselect_b32 s22, 2.0, s22\n\t"
"ds_write_addtid_b32 v211 offset:26720\n\t"
"v_mfma_f32_16x16x32_bf16 v[64:67], a[108:111], a[160:163], v[64:67]\n\t"
"s_waitcnt vmcnt(23) lgkmcnt(8)\n\t"
"buffer_load_dwordx4 a[64:67], v235, s[8:11], s77 offen offset:0 sc1\n\t"
"v_mfma_f32_16x16x32_bf16 v[80:83], a[112:115], a[144:147], v[80:83]\n\t"
"ds_read_b128 a[144:147], v224 offset:0\n\t"
"v_mfma_f32_16x16x32_bf16 v[80:83], a[116:119], a[160:163], v[80:83]\n\t"
"ds_read_b128 a[160:163], v224 offset:4096\n\t"
"v_mfma_f32_16x16x32_bf16 v[84:87], a[112:115], a[148:151], v[84:87]\n\t"
"ds_write_addtid_b32 v220 offset:29024\n\t"
"v_mfma_f32_16x16x32_bf16 v[84:87], a[116:119], a[164:167], v[84:87]\n\t"
"buffer_load_dwordx4 a[68:71], v235, s[8:11], s77 offen offset:1024 sc1\n\t"
"v_mfma_f32_16x16x32_bf16 v[68:71], a[104:107], a[148:151], v[68:71]\n\t"
"ds_write_addtid_b32 v201 offset:22848\n\t"
"v_mfma_f32_16x16x32_bf16 v[68:71], a[108:111], a[164:167], v[68:71]\n\t"
"ds_write_addtid_b32 v210 offset:24672\n\t"
"v_mfma_f32_16x16x32_bf16 v[52:55], a[96:99], a[148:151], v[52:55]\n\t"
"ds_read_b128 a[148:151], v224 offset:512\n\t"
"v_mfma_f32_16x16x32_bf16 v[52:55], a[100:103], a[164:167], v[52:55]\n\t"
"s_waitcnt lgkmcnt(7)\n\t"
"buffer_load_dwordx4 a[60:63], v252, s[8:11], s76 offen offset:3072 sc1\n\t"
"v_mfma_f32_16x16x32_bf16 v[56:59], a[96:99], a[152:155], v[56:59]\n\t"
"ds_read_b128 a[164:167], v224 offset:4608\n\t"
"v_mfma_f32_16x16x32_bf16 v[56:59], a[100:103], a[168:171], v[56:59]\n\t"
"ds_write_addtid_b32 v200 offset:20800\n\t"
"v_mfma_f32_16x16x32_bf16 v[72:75], a[104:107], a[152:155], v[72:75]\n\t"
"ds_write_addtid_b32 v192 offset:16704\n\t"
"v_mfma_f32_16x16x32_bf16 v[72:75], a[108:111], a[168:171], v[72:75]\n\t"
"buffer_load_dwordx4 a[56:59], v252, s[8:11], s76 offen offset:2048 sc1\n\t"
"v_mfma_f32_16x16x32_bf16 v[88:91], a[112:115], a[152:155], v[88:91]\n\t"
"ds_write_addtid_b32 v209 offset:26976\n\t"
"v_mfma_f32_16x16x32_bf16 v[88:91], a[116:119], a[168:171], v[88:91]\n\t"
"ds_write_addtid_b32 v208 offset:24928\n\t"
"v_mfma_f32_16x16x32_bf16 v[92:95], a[112:115], a[156:159], v[92:95]\n\t"
"ds_write_addtid_b32 v195 offset:18496\n\t"
"v_mfma_f32_16x16x32_bf16 v[92:95], a[116:119], a[172:175], v[92:95]\n\t"
"buffer_load_dwordx4 a[48:51], v252, s[8:11], s76 offen offset:0 sc1\n\t"
"v_mfma_f32_16x16x32_bf16 v[76:79], a[104:107], a[156:159], v[76:79]\n\t"
"ds_write_addtid_b32 v223 offset:30816\n\t"
"v_mfma_f32_16x16x32_bf16 v[76:79], a[108:111], a[172:175], v[76:79]\n\t"
"ds_write_addtid_b32 v202 offset:20544\n\t"
"v_mfma_f32_16x16x32_bf16 v[60:63], a[96:99], a[156:159], v[60:63]\n\t"
"ds_read_b128 a[248:251], v254 offset:18432\n\t"
"v_mfma_f32_16x16x32_bf16 v[60:63], a[100:103], a[172:175], v[60:63]\n\t"
"s_waitcnt vmcnt(22) lgkmcnt(7)\n\t"
"buffer_load_dwordx4 a[52:55], v252, s[8:11], s76 offen offset:1024 sc1\n\t"
"v_mfma_f32_16x16x32_bf16 v[0:3], a[120:123], a[144:147], v[0:3]\n\t"
"ds_read_b128 a[152:155], v224 offset:1024\n\t"
"v_mfma_f32_16x16x32_bf16 v[0:3], a[124:127], a[160:163], v[0:3]\n\t"
"ds_read_b128 a[172:175], v224 offset:5632\n\t"
"v_mfma_f32_16x16x32_bf16 v[16:19], a[128:131], a[144:147], v[16:19]\n\t"
"ds_write_addtid_b32 v193 offset:18752\n\t"
"v_mfma_f32_16x16x32_bf16 v[16:19], a[132:135], a[160:163], v[16:19]\n\t"
"buffer_load_dwordx4 a[88:91], v235, s[8:11], s76 offen offset:2048 sc1\n\t"
"v_mfma_f32_16x16x32_bf16 v[32:35], a[136:139], a[144:147], v[32:35]\n\t"
"ds_read_b128 a[156:159], v224 offset:1536\n\t"
"v_mfma_f32_16x16x32_bf16 v[32:35], a[140:143], a[160:163], v[32:35]\n\t"
"ds_read_b128 a[168:171], v224 offset:5120\n\t"
"v_mfma_f32_16x16x32_bf16 v[36:39], a[136:139], a[148:151], v[36:39]\n\t"
"v_mfma_f32_16x16x32_bf16 v[36:39], a[140:143], a[164:167], v[36:39]\n\t"
"buffer_load_dwordx4 a[92:95], v235, s[8:11], s76 offen offset:3072 sc1\n\t"
"v_mfma_f32_16x16x32_bf16 v[20:23], a[128:131], a[148:151], v[20:23]\n\t"
"v_mfma_f32_16x16x32_bf16 v[20:23], a[132:135], a[164:167], v[20:23]\n\t"
"v_mfma_f32_16x16x32_bf16 v[4:7], a[120:123], a[148:151], v[4:7]\n\t"
"v_mfma_f32_16x16x32_bf16 v[4:7], a[124:127], a[164:167], v[4:7]\n\t"
"buffer_load_dwordx4 a[84:87], v235, s[8:11], s76 offen offset:1024 sc1\n\t"
"s_waitcnt vmcnt(14) lgkmcnt(3)\n\t"
"s_waitcnt lgkmcnt(0)\n\t"
"s_barrier\n\t"
"v_mfma_f32_16x16x32_bf16 v[8:11], a[120:123], a[152:155], v[8:11]\n\t"
"s_nop 0\n\t"
"ds_read_b128 a[144:147], v224 offset:18496\n\t"
"ds_read_b128 a[160:163], v224 offset:22592\n\t"
"v_mfma_f32_16x16x32_bf16 v[8:11], a[124:127], a[168:171], v[8:11]\n\t"
"buffer_load_dwordx4 a[80:83], v235, s[8:11], s76 offen offset:0 sc1\n\t"
"v_mfma_f32_16x16x32_bf16 v[24:27], a[128:131], a[152:155], v[24:27]\n\t"
"s_nop 0\n\t"
"ds_read_b128 a[148:151], v224 offset:19008\n\t"
"ds_read_b64_tr_b16 a[176:177], v248 offset:0\n\t"
"v_mfma_f32_16x16x32_bf16 v[24:27], a[132:135], a[168:171], v[24:27]\n\t"
"s_nop 0\n\t"
"ds_read_b128 a[164:167], v224 offset:23104\n\t"
"ds_read_b64_tr_b16 a[178:179], v249 offset:0\n\t"
"v_mfma_f32_16x16x32_bf16 v[40:43], a[136:139], a[152:155], v[40:43]\n\t"
"s_nop 0\n\t"
"ds_read_b64_tr_b16 a[180:181], v248 offset:512\n\t"
"ds_read_b64_tr_b16 a[182:183], v249 offset:512\n\t"
"v_mfma_f32_16x16x32_bf16 v[40:43], a[140:143], a[168:171], v[40:43]\n\t"
"buffer_load_dwordx4 a[72:75], v252, s[8:11], s75 offen offset:2048 sc1\n\t"
"v_mfma_f32_16x16x32_bf16 v[44:47], a[136:139], a[156:159], v[44:47]\n\t"
"s_nop 0\n\t"
"ds_read_b64_tr_b16 a[184:185], v250 offset:2304\n\t"
"ds_read_b64_tr_b16 a[186:187], v251 offset:2304\n\t"
"v_mfma_f32_16x16x32_bf16 v[44:47], a[140:143], a[172:175], v[44:47]\n\t"
"s_nop 0\n\t"
"ds_read_b128 a[152:155], v224 offset:19520\n\t"
"ds_read_b128 a[168:171], v224 offset:23616\n\t"
"v_mfma_f32_16x16x32_bf16 v[28:31], a[128:131], a[156:159], v[28:31]\n\t"
"s_nop 0\n\t"
"ds_read_b64_tr_b16 a[188:189], v250 offset:2816\n\t"
"ds_read_b64_tr_b16 a[190:191], v251 offset:2816\n\t"
"v_mfma_f32_16x16x32_bf16 v[28:31], a[132:135], a[172:175], v[28:31]\n\t"
"s_waitcnt lgkmcnt(6)\n\t"
"buffer_load_dwordx4 a[76:79], v252, s[8:11], s75 offen offset:3072 sc1\n\t"
"v_mfma_f32_4x4x4_16b_bf16 v[192:195], a[210:211], a[176:177], 0\n\t"
"ds_read_b128 a[244:247], v255 offset:41472\n\t"
"v_mfma_f32_4x4x4_16b_bf16 v[196:199], a[210:211], a[178:179], 0\n\t"
"ds_read_b64_tr_b16 a[192:193], v250 offset:0\n\t"
"v_mfma_f32_4x4x4_16b_bf16 v[200:203], a[210:211], a[180:181], 0\n\t"
"ds_read_b64_tr_b16 a[194:195], v251 offset:0\n\t"
"v_mfma_f32_4x4x4_16b_bf16 v[204:207], a[210:211], a[182:183], 0\n\t"
"buffer_load_dwordx4 a[112:115], v235, s[8:11], s80 offen offset:0 sc1\n\t"
"v_mfma_f32_16x16x32_bf16 v[12:15], a[120:123], a[156:159], v[12:15]\n\t"
"s_nop 0\n\t"
"ds_read_b64_tr_b16 a[196:197], v250 offset:512\n\t"
"ds_read_b64_tr_b16 a[198:199], v251 offset:512\n\t"
"v_mfma_f32_16x16x32_bf16 v[12:15], a[124:127], a[172:175], v[12:15]\n\t"
"ds_read_b64_tr_b16 a[200:201], v248 offset:2304\n\t"
"v_cvt_pk_bf16_f32 v194, v194, v198\n\t"
"v_mfma_f32_16x16x32_bf16 v[144:147], a[0:3], a[144:147], v[144:147]\n\t"
"ds_read_b64_tr_b16 a[202:203], v249 offset:2304\n\t"
"v_cvt_pk_bf16_f32 v195, v195, v199\n\t"
"v_mfma_f32_16x16x32_bf16 v[144:147], a[4:7], a[160:163], v[144:147]\n\t"
"buffer_load_dwordx4 a[116:119], v235, s[8:11], s80 offen offset:1024 sc1\n\t"
"v_mfma_f32_16x16x32_bf16 v[160:163], a[8:11], a[144:147], v[160:163]\n\t"
"ds_read_b64_tr_b16 a[204:205], v248 offset:2816\n\t"
"v_cvt_pk_bf16_f32 v199, v201, v205\n\t"
"v_mfma_f32_16x16x32_bf16 v[160:163], a[12:15], a[160:163], v[160:163]\n\t"
"ds_read_b128 a[172:175], v224 offset:24128\n\t"
"v_cvt_pk_bf16_f32 v196, v192, v196\n\t"
"v_mfma_f32_16x16x32_bf16 v[176:179], a[16:19], a[144:147], v[176:179]\n\t"
"ds_write_addtid_b32 v199 offset:2304\n\t"
"v_cvt_pk_bf16_f32 v198, v200, v204\n\t"
"v_mfma_f32_16x16x32_bf16 v[176:179], a[20:23], a[160:163], v[176:179]\n\t"
"s_waitcnt lgkmcnt(8)\n\t"
"buffer_load_dwordx4 a[108:111], v252, s[8:11], s79 offen offset:3072 sc1\n\t"
"v_mfma_f32_4x4x4_16b_bf16 v[208:211], a[210:211], a[184:185], 0\n\t"
"ds_read_b64_tr_b16 a[206:207], v249 offset:2816\n\t"
"v_mfma_f32_4x4x4_16b_bf16 v[212:215], a[210:211], a[186:187], 0\n\t"
"ds_read_b128 a[156:159], v224 offset:20032\n\t"
"v_mfma_f32_4x4x4_16b_bf16 v[216:219], a[210:211], a[188:189], 0\n\t"
"ds_read_b128 a[160:163], v224 offset:20544\n\t"
"v_mfma_f32_4x4x4_16b_bf16 v[220:223], a[210:211], a[190:191], 0\n\t"
"v_cvt_pk_bf16_f32 v192, v202, v206\n\t"
"v_cvt_pk_bf16_f32 v197, v193, v197\n\t"
"v_mfma_f32_16x16x32_bf16 v[180:183], a[16:19], a[148:151], v[180:183]\n\t"
"ds_write_addtid_b32 v196 offset:0\n\t"
"v_cvt_pk_bf16_f32 v193, v203, v207\n\t"
"v_mfma_f32_16x16x32_bf16 v[180:183], a[20:23], a[164:167], v[180:183]\n\t"
"buffer_load_dwordx4 a[104:107], v252, s[8:11], s79 offen offset:2048 sc1\n\t"
"v_mfma_f32_16x16x32_bf16 v[164:167], a[8:11], a[148:151], v[164:167]\n\t"
"ds_write_addtid_b32 v197 offset:2048\n\t"
"v_cvt_pk_bf16_f32 v210, v210, v214\n\t"
"v_mfma_f32_16x16x32_bf16 v[164:167], a[12:15], a[164:167], v[164:167]\n\t"
"v_cvt_pk_bf16_f32 v213, v209, v213\n\t"
"v_cvt_pk_bf16_f32 v211, v211, v215\n\t"
"v_mfma_f32_16x16x32_bf16 v[148:151], a[0:3], a[148:151], v[148:151]\n\t"
"v_cvt_pk_bf16_f32 v212, v208, v212\n\t"
"v_cvt_pk_bf16_f32 v208, v218, v222\n\t"
"v_mfma_f32_16x16x32_bf16 v[148:151], a[4:7], a[164:167], v[148:151]\n\t"
"buffer_load_dwordx4 a[96:99], v252, s[8:11], s79 offen offset:0 sc1\n\t"
"v_mfma_f32_16x16x32_bf16 v[152:155], a[0:3], a[152:155], v[152:155]\n\t"
"ds_read_b128 a[144:147], v224 offset:16448\n\t"
"v_cvt_pk_bf16_f32 v215, v217, v221\n\t"
"v_mfma_f32_16x16x32_bf16 v[152:155], a[4:7], a[168:171], v[152:155]\n\t"
"s_waitcnt lgkmcnt(4)\n\t"
"v_cvt_pk_bf16_f32 v214, v216, v220\n\t"
"v_cvt_pk_bf16_f32 v209, v219, v223\n\t"
"v_mfma_f32_4x4x4_16b_bf16 v[200:203], a[210:211], a[192:193], 0\n\t"
"ds_write_addtid_b32 v198 offset:256\n\t"
"v_mfma_f32_4x4x4_16b_bf16 v[204:207], a[210:211], a[194:195], 0\n\t"
"ds_read_b128 a[148:151], v224 offset:16960\n\t"
"v_mfma_f32_4x4x4_16b_bf16 v[196:199], a[210:211], a[196:197], 0\n\t"
"ds_write_addtid_b32 v214 offset:8480\n\t"
"v_mfma_f32_4x4x4_16b_bf16 v[216:219], a[210:211], a[198:199], 0\n\t"
"buffer_load_dwordx4 a[100:103], v252, s[8:11], s79 offen offset:1024 sc1\n\t"
"v_mfma_f32_16x16x32_bf16 v[168:171], a[8:11], a[152:155], v[168:171]\n\t"
"s_nop 0\n\t"
"ds_write_addtid_b32 v213 offset:10272\n\t"
"ds_write_addtid_b32 v215 offset:10528\n\t"
"v_mfma_f32_16x16x32_bf16 v[168:171], a[12:15], a[168:171], v[168:171]\n\t"
"v_cvt_pk_bf16_f32 v204, v200, v204\n\t"
"v_cvt_pk_bf16_f32 v203, v203, v207\n\t"
"v_mfma_f32_16x16x32_bf16 v[184:187], a[16:19], a[152:155], v[184:187]\n\t"
"ds_read_b128 a[164:167], v224 offset:21056\n\t"
"v_cvt_pk_bf16_f32 v205, v201, v205\n\t"
"v_mfma_f32_16x16x32_bf16 v[184:187], a[20:23], a[168:171], v[184:187]\n\t"
"buffer_load_dwordx4 a[136:139], v235, s[8:11], s79 offen offset:2048 sc1\n\t"
"v_mfma_f32_16x16x32_bf16 v[188:191], a[16:19], a[156:159], v[188:191]\n\t"
"ds_read_b128 a[152:155], v224 offset:17472\n\t"
"v_cvt_pk_bf16_f32 v207, v197, v217\n\t"
"v_mfma_f32_16x16x32_bf16 v[188:191], a[20:23], a[172:175], v[188:191]\n\t"
"ds_write_addtid_b32 v212 offset:8224\n\t"
"v_cvt_pk_bf16_f32 v202, v202, v206\n\t"
"v_mfma_f32_16x16x32_bf16 v[172:175], a[8:11], a[156:159], v[172:175]\n\t"
"v_cvt_pk_bf16_f32 v200, v198, v218\n\t"
"v_cvt_pk_bf16_f32 v206, v196, v216\n\t"
"v_mfma_f32_16x16x32_bf16 v[172:175], a[12:15], a[172:175], v[172:175]\n\t"
"buffer_load_dwordx4 a[140:143], v235, s[8:11], s79 offen offset:3072 sc1\n\t"
"v_mfma_f32_4x4x4_16b_bf16 v[220:223], a[210:211], a[200:201], 0\n\t"
"v_cvt_pk_bf16_f32 v201, v199, v219\n\t"
"v_mfma_f32_4x4x4_16b_bf16 v[212:215], a[210:211], a[202:203], 0\n\t"
"ds_write_addtid_b32 v206 offset:4352\n\t"
"v_mfma_f32_4x4x4_16b_bf16 v[196:199], a[210:211], a[204:205], 0\n\t"
"ds_write_addtid_b32 v204 offset:4096\n\t"
"v_mfma_f32_4x4x4_16b_bf16 v[216:219], a[210:211], a[206:207], 0\n\t"
"buffer_load_dwordx4 a[132:135], v235, s[8:11], s79 offen offset:1024 sc1\n\t"
"v_mfma_f32_16x16x32_bf16 v[156:159], a[0:3], a[156:159], v[156:159]\n\t"
"s_nop 0\n\t"
"ds_write_addtid_b32 v205 offset:6144\n\t"
"ds_write_addtid_b32 v207 offset:6400\n\t"
"v_mfma_f32_16x16x32_bf16 v[156:159], a[4:7], a[172:175], v[156:159]\n\t"
"s_waitcnt vmcnt(21) lgkmcnt(6)\n\t"
"v_cvt_pk_bf16_f32 v212, v220, v212\n\t"
"v_cvt_pk_bf16_f32 v222, v222, v214\n\t"
"v_mfma_f32_16x16x32_bf16 v[96:99], a[24:27], a[144:147], v[96:99]\n\t"
"v_cvt_pk_bf16_f32 v213, v221, v213\n\t"
"v_cvt_pk_bf16_f32 v223, v223, v215\n\t"
"v_mfma_f32_16x16x32_bf16 v[96:99], a[28:31], a[160:163], v[96:99]\n\t"
"ds_write_addtid_b32 v212 offset:12320\n\t"
"v_cvt_pk_bf16_f32 v215, v197, v217\n\t"
"v_mfma_f32_16x16x32_bf16 v[112:115], a[32:35], a[144:147], v[112:115]\n\t"
"ds_write_addtid_b32 v215 offset:14624\n\t"
"v_cvt_pk_bf16_f32 v214, v196, v216\n\t"
"v_mfma_f32_16x16x32_bf16 v[112:115], a[36:39], a[160:163], v[112:115]\n\t"
"s_nop 0\n\t"
"ds_write_addtid_b32 v214 offset:12576\n\t"
"ds_write_addtid_b32 v213 offset:14368\n\t"
"v_mfma_f32_16x16x32_bf16 v[128:131], a[40:43], a[144:147], v[128:131]\n\t"
"s_nop 0\n\t"
"ds_read_b128 a[156:159], v224 offset:17984\n\t"
"ds_read_b128 a[172:175], v224 offset:22080\n\t"
"v_mfma_f32_16x16x32_bf16 v[128:131], a[44:47], a[160:163], v[128:131]\n\t"
"buffer_load_dwordx4 a[128:131], v235, s[8:11], s79 offen offset:0 sc1\n\t"
"v_mfma_f32_16x16x32_bf16 v[132:135], a[40:43], a[148:151], v[132:135]\n\t"
"ds_read_b128 a[168:171], v224 offset:21568\n\t"
"v_cvt_pk_bf16_f32 v221, v199, v219\n\t"
"v_mfma_f32_16x16x32_bf16 v[132:135], a[44:47], a[164:167], v[132:135]\n\t"
"buffer_load_dwordx4 a[120:123], v252, s[8:11], s78 offen offset:2048 sc1\n\t"
"v_mfma_f32_16x16x32_bf16 v[116:119], a[32:35], a[148:151], v[116:119]\n\t"
"v_cvt_pk_bf16_f32 v220, v198, v218\n\t"
"v_mfma_f32_16x16x32_bf16 v[116:119], a[36:39], a[164:167], v[116:119]\n\t"
"buffer_load_dwordx4 a[124:127], v252, s[8:11], s78 offen offset:3072 sc1\n\t"
"v_mfma_f32_16x16x32_bf16 v[100:103], a[24:27], a[148:151], v[100:103]\n\t"
"s_waitcnt lgkmcnt(0)\n\t"
"v_mfma_f32_16x16x32_bf16 v[100:103], a[28:31], a[164:167], v[100:103]\n\t"
"s_barrier\n\t"
"v_mfma_f32_16x16x32_bf16 v[104:107], a[24:27], a[152:155], v[104:107]\n\t"
"s_nop 0\n\t"
"ds_read_b128 a[144:147], v224 offset:2048\n\t"
"ds_read_b128 a[148:151], v224 offset:2560\n\t"
"v_mfma_f32_16x16x32_bf16 v[104:107], a[28:31], a[168:171], v[104:107]\n\t"
"buffer_load_dwordx4 a[16:19], v235, s[8:11], s74 offen offset:0 sc1\n\t"
"v_mfma_f32_16x16x32_bf16 v[120:123], a[32:35], a[152:155], v[120:123]\n\t"
"ds_read_b128 a[160:163], v224 offset:6144\n\t"
"v_add_u32 v248, 32896, v225\n\t"
"v_mfma_f32_16x16x32_bf16 v[120:123], a[36:39], a[168:171], v[120:123]\n\t"
"ds_read_b128 a[164:167], v224 offset:6656\n\t"
"v_xor_b32 v249, 32, v248\n\t"
"v_mfma_f32_16x16x32_bf16 v[136:139], a[40:43], a[152:155], v[136:139]\n\t"
"ds_read_b128 a[152:155], v224 offset:3072\n\t"
"v_xor_b32 v250, 64, v248\n\t"
"v_mfma_f32_16x16x32_bf16 v[136:139], a[44:47], a[168:171], v[136:139]\n\t"
"buffer_load_dwordx4 a[20:23], v235, s[8:11], s74 offen offset:1024 sc1\n\t"
"v_mfma_f32_16x16x32_bf16 v[140:143], a[40:43], a[156:159], v[140:143]\n\t"
"ds_read_b128 a[168:171], v224 offset:7168\n\t"
"v_xor_b32 v251, 96, v248\n\t"
"v_mfma_f32_16x16x32_bf16 v[140:143], a[44:47], a[172:175], v[140:143]\n\t"
"buffer_load_dwordx4 a[12:15], v252, s[8:11], s72 offen offset:3072 sc1\n\t"
"v_mfma_f32_16x16x32_bf16 v[124:127], a[32:35], a[156:159], v[124:127]\n\t"
"s_nop 0\n\t"
"ds_write_addtid_b32 v222 offset:28768\n\t"
"ds_read_b64_tr_b16 a[176:177], v248 offset:0\n\t"
"v_mfma_f32_16x16x32_bf16 v[124:127], a[36:39], a[172:175], v[124:127]\n\t"
"s_nop 0\n\t"
"ds_write_addtid_b32 v223 offset:30816\n\t"
"ds_read_b64_tr_b16 a[178:179], v250 offset:2304\n\t"
"v_mfma_f32_16x16x32_bf16 v[108:111], a[24:27], a[156:159], v[108:111]\n\t"
"s_nop 0\n\t"
"ds_write_addtid_b32 v195 offset:18496\n\t"
"ds_write_addtid_b32 v211 offset:26720\n\t"
"v_mfma_f32_16x16x32_bf16 v[108:111], a[28:31], a[172:175], v[108:111]\n\t"
"s_waitcnt vmcnt(21) lgkmcnt(9)\n\t"
"buffer_load_dwordx4 a[8:11], v252, s[8:11], s72 offen offset:2048 sc1\n\t"
"v_mfma_f32_16x16x32_bf16 v[48:51], a[48:51], a[144:147], v[48:51]\n\t"
"s_nop 0\n\t"
"ds_read_b128 a[156:159], v224 offset:3584\n\t"
"ds_read_b64_tr_b16 a[184:185], v249 offset:0\n\t"
"v_mfma_f32_16x16x32_bf16 v[48:51], a[52:55], a[160:163], v[48:51]\n\t"
"s_nop 0\n\t"
"ds_read_b128 a[172:175], v224 offset:7680\n\t"
"ds_write_addtid_b32 v193 offset:18752\n\t"
"v_mfma_f32_16x16x32_bf16 v[64:67], a[56:59], a[144:147], v[64:67]\n\t"
"s_nop 0\n\t"
"ds_write_addtid_b32 v202 offset:20544\n\t"
"ds_write_addtid_b32 v221 offset:31072\n\t"
"v_mfma_f32_16x16x32_bf16 v[64:67], a[60:63], a[160:163], v[64:67]\n\t"
"buffer_load_dwordx4 a[0:3], v252, s[8:11], s72 offen offset:0 sc1\n\t"
"v_mfma_f32_16x16x32_bf16 v[80:83], a[64:67], a[144:147], v[80:83]\n\t"
"s_nop 0\n\t"
"ds_read_b128 a[144:147], v224 offset:0\n\t"
"ds_write_addtid_b32 v220 offset:29024\n\t"
"v_mfma_f32_16x16x32_bf16 v[80:83], a[68:71], a[160:163], v[80:83]\n\t"
"s_waitcnt lgkmcnt(14)\n\t"
"ds_read_b128 a[240:243], v255 offset:0\n\t"
"v_mfma_f32_16x16x32_bf16 v[84:87], a[64:67], a[148:151], v[84:87]\n\t"
"s_nop 0\n\t"
"ds_read_b128 a[160:163], v224 offset:4096\n\t"
"ds_read_b64_tr_b16 a[186:187], v251 offset:2304\n\t"
"v_mfma_f32_16x16x32_bf16 v[84:87], a[68:71], a[164:167], v[84:87]\n\t"
"buffer_load_dwordx4 a[4:7], v252, s[8:11], s72 offen offset:1024 sc1\n\t"
"v_mfma_f32_16x16x32_bf16 v[68:71], a[56:59], a[148:151], v[68:71]\n\t"
"s_nop 0\n\t"
"ds_write_addtid_b32 v209 offset:26976\n\t"
"ds_read_b64_tr_b16 a[180:181], v248 offset:4608\n\t"
"v_mfma_f32_16x16x32_bf16 v[68:71], a[60:63], a[164:167], v[68:71]\n\t"
"s_nop 0\n\t"
"ds_read_b64_tr_b16 a[188:189], v249 offset:4608\n\t"
"ds_read_b64_tr_b16 a[192:193], v250 offset:0\n\t"
"v_mfma_f32_16x16x32_bf16 v[52:55], a[48:51], a[148:151], v[52:55]\n\t"
"ds_write_addtid_b32 v200 offset:20800\n\t"
"v_mfma_f32_16x16x32_bf16 v[52:55], a[52:55], a[164:167], v[52:55]\n\t"
"buffer_load_dwordx4 a[40:43], v235, s[8:11], s72 offen offset:2048 sc1\n\t"
"v_mfma_f32_16x16x32_bf16 v[56:59], a[48:51], a[152:155], v[56:59]\n\t"
"ds_read_b128 a[148:151], v224 offset:512\n\t"
"v_mfma_f32_16x16x32_bf16 v[56:59], a[52:55], a[168:171], v[56:59]\n\t"
"ds_read_b128 a[164:167], v224 offset:4608\n\t"
"v_mfma_f32_16x16x32_bf16 v[72:75], a[56:59], a[152:155], v[72:75]\n\t"
"ds_write_addtid_b32 v203 offset:22592\n\t"
"v_mfma_f32_16x16x32_bf16 v[72:75], a[60:63], a[168:171], v[72:75]\n\t"
"buffer_load_dwordx4 a[44:47], v235, s[8:11], s72 offen offset:3072 sc1\n\t"
"v_mfma_f32_16x16x32_bf16 v[88:91], a[64:67], a[152:155], v[88:91]\n\t"
"ds_write_addtid_b32 v192 offset:16704\n\t"
"v_mfma_f32_16x16x32_bf16 v[88:91], a[68:71], a[168:171], v[88:91]\n\t"
"s_waitcnt lgkmcnt(9)\n\t"
"ds_read_b64_tr_b16 a[194:195], v248 offset:2304\n\t"
"v_mfma_f32_16x16x32_bf16 v[92:95], a[64:67], a[156:159], v[92:95]\n\t"
"ds_write_addtid_b32 v201 offset:22848\n\t"
"v_mfma_f32_16x16x32_bf16 v[92:95], a[68:71], a[172:175], v[92:95]\n\t"
"buffer_load_dwordx4 a[36:39], v235, s[8:11], s72 offen offset:1024 sc1\n\t"
"v_mfma_f32_16x16x32_bf16 v[76:79], a[56:59], a[156:159], v[76:79]\n\t"
"ds_read_b128 a[168:171], v224 offset:5120\n\t"
"v_mfma_f32_16x16x32_bf16 v[76:79], a[60:63], a[172:175], v[76:79]\n\t"
"ds_read_b128 a[152:155], v224 offset:1024\n\t"
"v_mfma_f32_16x16x32_bf16 v[60:63], a[48:51], a[156:159], v[60:63]\n\t"
"ds_write_addtid_b32 v208 offset:24928\n\t"
"v_mfma_f32_16x16x32_bf16 v[60:63], a[52:55], a[172:175], v[60:63]\n\t"
"s_waitcnt vmcnt(21) lgkmcnt(7)\n\t"
"buffer_load_dwordx4 a[32:35], v235, s[8:11], s72 offen offset:0 sc1\n\t"
"v_mfma_f32_16x16x32_bf16 v[0:3], a[72:75], a[144:147], v[0:3]\n\t"
"ds_read_b64_tr_b16 a[200:201], v251 offset:0\n\t"
"v_mfma_f32_16x16x32_bf16 v[0:3], a[76:79], a[160:163], v[0:3]\n\t"
"ds_write_addtid_b32 v210 offset:24672\n\t"
"v_mfma_f32_16x16x32_bf16 v[16:19], a[80:83], a[144:147], v[16:19]\n\t"
"ds_write_addtid_b32 v194 offset:16448\n\t"
"v_mfma_f32_16x16x32_bf16 v[16:19], a[84:87], a[160:163], v[16:19]\n\t"
"buffer_load_dwordx4 a[24:27], v252, s[8:11], s69 offen offset:2048 sc1\n\t"
"v_mfma_f32_16x16x32_bf16 v[32:35], a[88:91], a[144:147], v[32:35]\n\t"
"ds_read_b128 a[156:159], v224 offset:1536\n\t"
"v_mfma_f32_16x16x32_bf16 v[32:35], a[92:95], a[160:163], v[32:35]\n\t"
"ds_read_b128 a[172:175], v224 offset:5632\n\t"
"v_mfma_f32_16x16x32_bf16 v[36:39], a[88:91], a[148:151], v[36:39]\n\t"
"ds_read_b64_tr_b16 a[202:203], v249 offset:2304\n\t"
"v_mfma_f32_16x16x32_bf16 v[36:39], a[92:95], a[164:167], v[36:39]\n\t"
"ds_read_b64_tr_b16 a[196:197], v250 offset:4608\n\t"
"v_mfma_f32_16x16x32_bf16 v[20:23], a[80:83], a[148:151], v[20:23]\n\t"
"ds_read_b64_tr_b16 a[204:205], v251 offset:4608\n\t"
"v_mfma_f32_16x16x32_bf16 v[20:23], a[84:87], a[164:167], v[20:23]\n\t"
"buffer_load_dwordx4 a[28:31], v252, s[8:11], s69 offen offset:3072 sc1\n\t"
"v_mfma_f32_16x16x32_bf16 v[4:7], a[72:75], a[148:151], v[4:7]\n\t"
"s_waitcnt lgkmcnt(3)\n\t"
"v_mfma_f32_16x16x32_bf16 v[4:7], a[76:79], a[164:167], v[4:7]\n\t"
"buffer_load_dwordx4 a[64:67], v235, s[8:11], s83 offen offset:0 sc1\n\t"
"s_barrier\n\t"
"v_mfma_f32_16x16x32_bf16 v[8:11], a[72:75], a[152:155], v[8:11]\n\t"
"ds_read_b128 a[160:163], v224 offset:22592\n\t"
"v_mfma_f32_16x16x32_bf16 v[8:11], a[76:79], a[168:171], v[8:11]\n\t"
"ds_read_b128 a[144:147], v224 offset:18496\n\t"
"v_mfma_f32_16x16x32_bf16 v[24:27], a[80:83], a[152:155], v[24:27]\n\t"
"ds_read_b128 a[148:151], v224 offset:19008\n\t"
"v_mfma_f32_16x16x32_bf16 v[24:27], a[84:87], a[168:171], v[24:27]\n\t"
"ds_read_b128 a[164:167], v224 offset:23104\n\t"
"v_mfma_f32_16x16x32_bf16 v[40:43], a[88:91], a[152:155], v[40:43]\n\t"
"ds_read_b128 a[152:155], v224 offset:19520\n\t"
"v_mfma_f32_16x16x32_bf16 v[40:43], a[92:95], a[168:171], v[40:43]\n\t"
"buffer_load_dwordx4 a[68:71], v235, s[8:11], s83 offen offset:1024 sc1\n\t"
"v_mfma_f32_16x16x32_bf16 v[44:47], a[88:91], a[156:159], v[44:47]\n\t"
"ds_read_b128 a[168:171], v224 offset:23616\n\t"
"v_mfma_f32_16x16x32_bf16 v[44:47], a[92:95], a[172:175], v[44:47]\n\t"
"buffer_load_dwordx4 a[60:63], v252, s[8:11], s82 offen offset:3072 sc1\n\t"
"v_mfma_f32_16x16x32_bf16 v[28:31], a[80:83], a[156:159], v[28:31]\n\t"
"v_mfma_f32_16x16x32_bf16 v[28:31], a[84:87], a[172:175], v[28:31]\n\t"
"buffer_load_dwordx4 a[56:59], v252, s[8:11], s82 offen offset:2048 sc1\n\t"
"v_mfma_f32_16x16x32_bf16 v[12:15], a[72:75], a[156:159], v[12:15]\n\t"
"ds_read_b128 a[156:159], v224 offset:20032\n\t"
"v_mfma_f32_16x16x32_bf16 v[12:15], a[76:79], a[172:175], v[12:15]\n\t"
"buffer_load_dwordx4 a[48:51], v252, s[8:11], s82 offen offset:0 sc1\n\t"
"v_mfma_f32_16x16x32_bf16 v[192:195], a[212:215], a[176:179], 0\n\t"
"ds_read_b64_tr_b16 a[176:177], v248 offset:1152\n\t"
"v_mfma_f32_16x16x32_bf16 v[196:199], a[212:215], a[184:187], 0\n\t"
"s_nop 0\n\t"
"ds_read_b128 a[172:175], v224 offset:24128\n\t"
"ds_read_b64_tr_b16 a[184:185], v249 offset:1152\n\t"
"v_mfma_f32_16x16x32_bf16 v[200:203], a[212:215], a[178:181], 0\n\t"
"s_nop 0\n\t"
"ds_read_b64_tr_b16 a[178:179], v250 offset:3456\n\t"
"ds_read_b64_tr_b16 a[180:181], v248 offset:5760\n\t"
"v_mfma_f32_16x16x32_bf16 v[204:207], a[212:215], a[186:189], 0\n\t"
"buffer_load_dwordx4 a[52:55], v252, s[8:11], s82 offen offset:1024 sc1\n\t"
"v_mfma_f32_16x16x32_bf16 v[208:211], a[212:215], a[192:195], 0\n\t"
"s_waitcnt lgkmcnt(6)\n\t"
"ds_read_b64_tr_b16 a[186:187], v251 offset:3456\n\t"
"ds_read_b64_tr_b16 a[188:189], v249 offset:5760\n\t"
"v_mfma_f32_16x16x32_bf16 v[212:215], a[212:215], a[200:203], 0\n\t"
"ds_read_b64_tr_b16 a[192:193], v250 offset:1152\n\t"
"v_mfma_f32_16x16x32_bf16 v[216:219], a[212:215], a[194:197], 0\n\t"
"ds_read_b64_tr_b16 a[200:201], v251 offset:1152\n\t"
"v_mfma_f32_16x16x32_bf16 v[220:223], a[212:215], a[202:205], 0\n\t"
"s_waitcnt vmcnt(24)\n\t"
"buffer_load_dwordx4 a[88:91], v235, s[8:11], s82 offen offset:2048 sc1\n\t"
"v_mfma_f32_16x16x32_bf16 v[144:147], a[96:99], a[144:147], v[144:147]\n\t"
"ds_read_b64_tr_b16 a[194:195], v248 offset:3456\n\t"
"v_cvt_pk_bf16_f32 v195, v195, v199\n\t"
"v_mfma_f32_16x16x32_bf16 v[144:147], a[100:103], a[160:163], v[144:147]\n\t"
"ds_read_b64_tr_b16 a[196:197], v250 offset:5760\n\t"
"v_cvt_pk_bf16_f32 v194, v194, v198\n\t"
"v_mfma_f32_16x16x32_bf16 v[160:163], a[104:107], a[144:147], v[160:163]\n\t"
"ds_write_addtid_b32 v195 offset:2304\n\t"
"v_cvt_pk_bf16_f32 v201, v201, v205\n\t"
"v_mfma_f32_16x16x32_bf16 v[160:163], a[108:111], a[160:163], v[160:163]\n\t"
"buffer_load_dwordx4 a[92:95], v235, s[8:11], s82 offen offset:3072 sc1\n\t"
"v_mfma_f32_16x16x32_bf16 v[176:179], a[112:115], a[144:147], v[176:179]\n\t"
"ds_read_b128 a[144:147], v224 offset:16448\n\t"
"v_cvt_pk_bf16_f32 v211, v211, v215\n\t"
"v_mfma_f32_16x16x32_bf16 v[176:179], a[116:119], a[160:163], v[176:179]\n\t"
"ds_read_b128 a[160:163], v224 offset:20544\n\t"
"v_cvt_pk_bf16_f32 v193, v193, v197\n\t"
"v_mfma_f32_16x16x32_bf16 v[180:183], a[112:115], a[148:151], v[180:183]\n\t"
"ds_write_addtid_b32 v193 offset:256\n\t"
"v_cvt_pk_bf16_f32 v202, v202, v206\n\t"
"v_mfma_f32_16x16x32_bf16 v[180:183], a[116:119], a[164:167], v[180:183]\n\t"
"buffer_load_dwordx4 a[84:87], v235, s[8:11], s82 offen offset:1024 sc1\n\t"
"v_mfma_f32_16x16x32_bf16 v[164:167], a[104:107], a[148:151], v[164:167]\n\t"
"ds_write_addtid_b32 v202 offset:10272\n\t"
"v_cvt_pk_bf16_f32 v218, v218, v222\n\t"
"v_mfma_f32_16x16x32_bf16 v[164:167], a[108:111], a[164:167], v[164:167]\n\t"
"ds_write_addtid_b32 v211 offset:6400\n\t"
"v_cvt_pk_bf16_f32 v200, v200, v204\n\t"
"v_mfma_f32_16x16x32_bf16 v[148:151], a[96:99], a[148:151], v[148:151]\n\t"
"ds_read_b128 a[148:151], v224 offset:16960\n\t"
"v_cvt_pk_bf16_f32 v209, v209, v213\n\t"
"v_mfma_f32_16x16x32_bf16 v[148:151], a[100:103], a[164:167], v[148:151]\n\t"
"buffer_load_dwordx4 a[80:83], v235, s[8:11], s82 offen offset:0 sc1\n\t"
"v_mfma_f32_16x16x32_bf16 v[152:155], a[96:99], a[152:155], v[152:155]\n\t"
"ds_read_b128 a[164:167], v224 offset:21056\n\t"
"v_cvt_pk_bf16_f32 v208, v208, v212\n\t"
"v_mfma_f32_16x16x32_bf16 v[152:155], a[100:103], a[168:171], v[152:155]\n\t"
"ds_read_b64_tr_b16 a[202:203], v249 offset:3456\n\t"
"v_cvt_pk_bf16_f32 v192, v192, v196\n\t"
"v_mfma_f32_16x16x32_bf16 v[168:171], a[104:107], a[152:155], v[168:171]\n\t"
"ds_write_addtid_b32 v218 offset:14368\n\t"
"v_cvt_pk_bf16_f32 v216, v216, v220\n\t"
"v_mfma_f32_16x16x32_bf16 v[168:171], a[108:111], a[168:171], v[168:171]\n\t"
"buffer_load_dwordx4 a[72:75], v252, s[8:11], s81 offen offset:2048 sc1\n\t"
"v_mfma_f32_16x16x32_bf16 v[184:187], a[112:115], a[152:155], v[184:187]\n\t"
"ds_write_addtid_b32 v208 offset:4096\n\t"
"v_cvt_pk_bf16_f32 v203, v203, v207\n\t"
"v_mfma_f32_16x16x32_bf16 v[184:187], a[116:119], a[168:171], v[184:187]\n\t"
"s_waitcnt lgkmcnt(7)\n\t"
"ds_write_addtid_b32 v200 offset:8224\n\t"
"v_cvt_pk_bf16_f32 v210, v210, v214\n\t"
"v_mfma_f32_16x16x32_bf16 v[188:191], a[112:115], a[156:159], v[188:191]\n\t"
"ds_read_b128 a[168:171], v224 offset:21568\n\t"
"v_cvt_pk_bf16_f32 v217, v217, v221\n\t"
"v_mfma_f32_16x16x32_bf16 v[188:191], a[116:119], a[172:175], v[188:191]\n\t"
"buffer_load_dwordx4 a[76:79], v252, s[8:11], s81 offen offset:3072 sc1\n\t"
"v_mfma_f32_16x16x32_bf16 v[172:175], a[104:107], a[156:159], v[172:175]\n\t"
"ds_write_addtid_b32 v192 offset:0\n\t"
"v_cvt_pk_bf16_f32 v219, v219, v223\n\t"
"v_mfma_f32_16x16x32_bf16 v[172:175], a[108:111], a[172:175], v[172:175]\n\t"
"s_nop 0\n\t"
"ds_write_addtid_b32 v194 offset:2048\n\t"
"ds_write_addtid_b32 v217 offset:12576\n\t"
"v_mfma_f32_16x16x32_bf16 v[156:159], a[96:99], a[156:159], v[156:159]\n\t"
"s_nop 0\n\t"
"ds_write_addtid_b32 v210 offset:6144\n\t"
"ds_write_addtid_b32 v216 offset:12320\n\t"
"v_mfma_f32_16x16x32_bf16 v[156:159], a[100:103], a[172:175], v[156:159]\n\t"
"s_waitcnt vmcnt(24) lgkmcnt(9)\n\t"
"buffer_load_dwordx4 a[112:115], v235, s[8:11], s56 offen offset:0 sc1\n\t"
"v_mfma_f32_16x16x32_bf16 v[96:99], a[120:123], a[144:147], v[96:99]\n\t"
"s_nop 0\n\t"
"ds_read_b128 a[156:159], v224 offset:17984\n\t"
"ds_write_addtid_b32 v201 offset:8480\n\t"
"v_mfma_f32_16x16x32_bf16 v[96:99], a[124:127], a[160:163], v[96:99]\n\t"
"s_nop 0\n\t"
"ds_write_addtid_b32 v209 offset:4352\n\t"
"ds_write_addtid_b32 v219 offset:14624\n\t"
"v_mfma_f32_16x16x32_bf16 v[112:115], a[128:131], a[144:147], v[112:115]\n\t"
"ds_write_addtid_b32 v203 offset:10528\n\t"
"v_mfma_f32_16x16x32_bf16 v[112:115], a[132:135], a[160:163], v[112:115]\n\t"
".p2align 3\n\t"
"buffer_load_dwordx4 a[116:119], v235, s[8:11], s56 offen offset:1024 sc1\n\t"
"v_pk_fma_f32 v[48:49], v[144:145], s[90:91], v[48:49]\n\t"
"v_pk_fma_f32 v[50:51], v[146:147], s[90:91], v[50:51]\n\t"
"v_pk_add_f32 v[144:145], v[48:49], v[144:145] neg_lo:[0,1] neg_hi:[0,1]\n\t"
"v_pk_add_f32 v[146:147], v[50:51], v[146:147] neg_lo:[0,1] neg_hi:[0,1]\n\t"
"v_pk_fma_f32 v[64:65], v[160:161], s[90:91], v[64:65]\n\t"
"v_pk_fma_f32 v[66:67], v[162:163], s[90:91], v[66:67]\n\t"
"v_pk_add_f32 v[160:161], v[64:65], v[160:161] neg_lo:[0,1] neg_hi:[0,1]\n\t"
"v_pk_add_f32 v[162:163], v[66:67], v[162:163] neg_lo:[0,1] neg_hi:[0,1]\n\t"
"v_pk_fma_f32 v[80:81], v[176:177], s[90:91], v[80:81]\n\t"
"v_pk_fma_f32 v[82:83], v[178:179], s[90:91], v[82:83]\n\t"
"v_pk_add_f32 v[176:177], v[80:81], v[176:177] neg_lo:[0,1] neg_hi:[0,1]\n\t"
"v_pk_add_f32 v[178:179], v[82:83], v[178:179] neg_lo:[0,1] neg_hi:[0,1]\n\t"
"v_pk_fma_f32 v[84:85], v[180:181], s[90:91], v[84:85]\n\t"
"v_pk_fma_f32 v[86:87], v[182:183], s[90:91], v[86:87]\n\t"
"v_pk_add_f32 v[180:181], v[84:85], v[180:181] neg_lo:[0,1] neg_hi:[0,1]\n\t"
"v_pk_add_f32 v[182:183], v[86:87], v[182:183] neg_lo:[0,1] neg_hi:[0,1]\n\t"
"v_pk_fma_f32 v[68:69], v[164:165], s[90:91], v[68:69]\n\t"
"v_pk_fma_f32 v[70:71], v[166:167], s[90:91], v[70:71]\n\t"
"v_pk_add_f32 v[164:165], v[68:69], v[164:165] neg_lo:[0,1] neg_hi:[0,1]\n\t"
"v_pk_add_f32 v[166:167], v[70:71], v[166:167] neg_lo:[0,1] neg_hi:[0,1]\n\t"
"v_pk_fma_f32 v[52:53], v[148:149], s[90:91], v[52:53]\n\t"
"v_pk_fma_f32 v[54:55], v[150:151], s[90:91], v[54:55]\n\t"
"v_pk_add_f32 v[148:149], v[52:53], v[148:149] neg_lo:[0,1] neg_hi:[0,1]\n\t"
"v_pk_add_f32 v[150:151], v[54:55], v[150:151] neg_lo:[0,1] neg_hi:[0,1]\n\t"
"buffer_load_dwordx4 a[108:111], v252, s[8:11], s55 offen offset:3072 sc1\n\t"
"v_pk_fma_f32 v[56:57], v[152:153], s[90:91], v[56:57]\n\t"
"v_pk_fma_f32 v[58:59], v[154:155], s[90:91], v[58:59]\n\t"
"v_pk_add_f32 v[152:153], v[56:57], v[152:153] neg_lo:[0,1] neg_hi:[0,1]\n\t"
"v_pk_add_f32 v[154:155], v[58:59], v[154:155] neg_lo:[0,1] neg_hi:[0,1]\n\t"
"v_pk_fma_f32 v[72:73], v[168:169], s[90:91], v[72:73]\n\t"
"v_pk_fma_f32 v[74:75], v[170:171], s[90:91], v[74:75]\n\t"
"v_pk_add_f32 v[168:169], v[72:73], v[168:169] neg_lo:[0,1] neg_hi:[0,1]\n\t"
"v_pk_add_f32 v[170:171], v[74:75], v[170:171] neg_lo:[0,1] neg_hi:[0,1]\n\t"
"v_pk_fma_f32 v[88:89], v[184:185], s[90:91], v[88:89]\n\t"
"v_pk_fma_f32 v[90:91], v[186:187], s[90:91], v[90:91]\n\t"
"v_pk_add_f32 v[184:185], v[88:89], v[184:185] neg_lo:[0,1] neg_hi:[0,1]\n\t"
"v_pk_add_f32 v[186:187], v[90:91], v[186:187] neg_lo:[0,1] neg_hi:[0,1]\n\t"
"v_pk_fma_f32 v[92:93], v[188:189], s[90:91], v[92:93]\n\t"
"v_pk_fma_f32 v[94:95], v[190:191], s[90:91], v[94:95]\n\t"
"v_pk_add_f32 v[188:189], v[92:93], v[188:189] neg_lo:[0,1] neg_hi:[0,1]\n\t"
"v_pk_add_f32 v[190:191], v[94:95], v[190:191] neg_lo:[0,1] neg_hi:[0,1]\n\t"
"v_pk_fma_f32 v[76:77], v[172:173], s[90:91], v[76:77]\n\t"
"v_pk_fma_f32 v[78:79], v[174:175], s[90:91], v[78:79]\n\t"
"v_pk_add_f32 v[172:173], v[76:77], v[172:173] neg_lo:[0,1] neg_hi:[0,1]\n\t"
"v_pk_add_f32 v[174:175], v[78:79], v[174:175] neg_lo:[0,1] neg_hi:[0,1]\n\t"
"v_pk_fma_f32 v[60:61], v[156:157], s[90:91], v[60:61]\n\t"
"v_pk_fma_f32 v[62:63], v[158:159], s[90:91], v[62:63]\n\t"
"v_pk_add_f32 v[156:157], v[60:61], v[156:157] neg_lo:[0,1] neg_hi:[0,1]\n\t"
"v_pk_add_f32 v[158:159], v[62:63], v[158:159] neg_lo:[0,1] neg_hi:[0,1]\n\t"
"v_mfma_f32_16x16x32_bf16 v[128:131], a[136:139], a[144:147], v[128:131]\n\t"
"ds_read_b128 a[172:175], v224 offset:22080\n\t"
"v_mfma_f32_16x16x32_bf16 v[128:131], a[140:143], a[160:163], v[128:131]\n\t"
"buffer_load_dwordx4 a[104:107], v252, s[8:11], s55 offen offset:2048 sc1\n\t"
"v_mfma_f32_16x16x32_bf16 v[132:135], a[136:139], a[148:151], v[132:135]\n\t"
"ds_read_b128 a[152:155], v224 offset:17472\n\t"
"v_mfma_f32_16x16x32_bf16 v[132:135], a[140:143], a[164:167], v[132:135]\n\t"
"buffer_load_dwordx4 a[96:99], v252, s[8:11], s55 offen offset:0 sc1\n\t"
"v_mfma_f32_16x16x32_bf16 v[116:119], a[128:131], a[148:151], v[116:119]\n\t"
"ds_read_b64_tr_b16 a[204:205], v251 offset:5760\n\t"
"v_mfma_f32_16x16x32_bf16 v[116:119], a[132:135], a[164:167], v[116:119]\n\t"
"buffer_load_dwordx4 a[100:103], v252, s[8:11], s55 offen offset:1024 sc1\n\t"
"v_mfma_f32_16x16x32_bf16 v[100:103], a[120:123], a[148:151], v[100:103]\n\t"
"v_mfma_f32_16x16x32_bf16 v[100:103], a[124:127], a[164:167], v[100:103]\n\t"
"s_waitcnt lgkmcnt(0)\n\t"
"s_barrier\n\t"
"v_mfma_f32_16x16x32_bf16 v[104:107], a[120:123], a[152:155], v[104:107]\n\t"
"s_add_u32 m0, s26, 34944\n\t"
"ds_read_b128 a[148:151], v224 offset:2560\n\t"
"v_add_u32 v248, 80896, v225\n\t"
"v_mfma_f32_16x16x32_bf16 v[104:107], a[124:127], a[168:171], v[104:107]\n\t"
"ds_read_b128 a[144:147], v224 offset:2048\n\t"
"v_xor_b32 v249, 32, v248\n\t"
"v_mfma_f32_16x16x32_bf16 v[120:123], a[128:131], a[152:155], v[120:123]\n\t"
"ds_read_b128 a[160:163], v224 offset:6144\n\t"
"v_xor_b32 v250, 64, v248\n\t"
"v_mfma_f32_16x16x32_bf16 v[120:123], a[132:135], a[168:171], v[120:123]\n\t"
"ds_read_b128 a[164:167], v224 offset:6656\n\t"
"v_xor_b32 v251, 96, v248\n\t"
"v_mfma_f32_16x16x32_bf16 v[136:139], a[136:139], a[152:155], v[136:139]\n\t"
"ds_read_b128 a[152:155], v224 offset:3072\n\t"
"v_mfma_f32_16x16x32_bf16 v[136:139], a[140:143], a[168:171], v[136:139]\n\t"
"buffer_load_dwordx4 v236, s[12:15], s22 offen offset:256 nt lds\n\t"
"v_mfma_f32_16x16x32_bf16 v[140:143], a[136:139], a[156:159], v[140:143]\n\t"
"ds_read_b128 a[228:231], v254 offset:39168\n\t"
"v_mfma_f32_16x16x32_bf16 v[140:143], a[140:143], a[172:175], v[140:143]\n\t"
"ds_read_b128 a[168:171], v224 offset:7168\n\t"
"v_mfma_f32_16x16x32_bf16 v[124:127], a[128:131], a[156:159], v[124:127]\n\t"
"v_mfma_f32_16x16x32_bf16 v[124:127], a[132:135], a[172:175], v[124:127]\n\t"
"buffer_load_dwordx4 v237, s[12:15], s22 offen offset:1280 nt lds\n\t"
"v_mfma_f32_16x16x32_bf16 v[108:111], a[120:123], a[156:159], v[108:111]\n\t"
"ds_read_b128 a[156:159], v224 offset:3584\n\t"
"v_mfma_f32_16x16x32_bf16 v[108:111], a[124:127], a[172:175], v[108:111]\n\t"
"ds_read_b128 a[172:175], v224 offset:7680\n\t"
"v_mfma_f32_16x16x32_bf16 v[192:195], a[212:215], a[176:179], 0\n\t"
"ds_read_b64_tr_b16 a[176:177], v248 offset:41472\n\t"
"v_mfma_f32_16x16x32_bf16 v[196:199], a[212:215], a[184:187], 0\n\t"
"ds_read_b64_tr_b16 a[184:185], v249 offset:41472\n\t"
"v_mfma_f32_16x16x32_bf16 v[200:203], a[212:215], a[178:181], 0\n\t"
"s_nop 0\n\t"
"ds_read_b64_tr_b16 a[178:179], v250 offset:43776\n\t"
"ds_read_b64_tr_b16 a[180:181], v248 offset:46080\n\t"
"v_mfma_f32_16x16x32_bf16 v[204:207], a[212:215], a[186:189], 0\n\t"
"s_nop 0\n\t"
"ds_read_b64_tr_b16 a[186:187], v251 offset:43776\n\t"
"ds_read_b64_tr_b16 a[188:189], v249 offset:46080\n\t"
"v_mfma_f32_16x16x32_bf16 v[208:211], a[212:215], a[192:195], 0\n\t"
"ds_read_b64_tr_b16 a[192:193], v250 offset:41472\n\t"
"v_mfma_f32_16x16x32_bf16 v[212:215], a[212:215], a[200:203], 0\n\t"
"buffer_load_dwordx4 v238, s[12:15], s22 offen offset:2304 nt lds\n\t"
"v_mfma_f32_16x16x32_bf16 v[216:219], a[212:215], a[194:197], 0\n\t"
"s_waitcnt lgkmcnt(9)\n\t"
"ds_write_b128 v254, a[228:231] offset:0\n\t"
"v_mfma_f32_16x16x32_bf16 v[220:223], a[212:215], a[202:205], 0\n\t"
"s_waitcnt vmcnt(27)\n\t"
".p2align 3\n\t"
"buffer_load_dwordx4 v239, s[12:15], s22 offen offset:3328 nt lds\n\t"
"v_pk_fma_f32 v[0:1], v[96:97], s[90:91], v[0:1]\n\t"
"v_pk_fma_f32 v[2:3], v[98:99], s[90:91], v[2:3]\n\t"
"v_pk_add_f32 v[96:97], v[0:1], v[96:97] neg_lo:[0,1] neg_hi:[0,1]\n\t"
"v_pk_add_f32 v[98:99], v[2:3], v[98:99] neg_lo:[0,1] neg_hi:[0,1]\n\t"
"v_pk_fma_f32 v[16:17], v[112:113], s[90:91], v[16:17]\n\t"
"v_pk_fma_f32 v[18:19], v[114:115], s[90:91], v[18:19]\n\t"
"v_pk_add_f32 v[112:113], v[16:17], v[112:113] neg_lo:[0,1] neg_hi:[0,1]\n\t"
"v_pk_add_f32 v[114:115], v[18:19], v[114:115] neg_lo:[0,1] neg_hi:[0,1]\n\t"
"v_pk_fma_f32 v[32:33], v[128:129], s[90:91], v[32:33]\n\t"
"v_pk_fma_f32 v[34:35], v[130:131], s[90:91], v[34:35]\n\t"
"v_pk_add_f32 v[128:129], v[32:33], v[128:129] neg_lo:[0,1] neg_hi:[0,1]\n\t"
"v_pk_add_f32 v[130:131], v[34:35], v[130:131] neg_lo:[0,1] neg_hi:[0,1]\n\t"
"v_pk_fma_f32 v[36:37], v[132:133], s[90:91], v[36:37]\n\t"
"v_pk_fma_f32 v[38:39], v[134:135], s[90:91], v[38:39]\n\t"
"v_pk_add_f32 v[132:133], v[36:37], v[132:133] neg_lo:[0,1] neg_hi:[0,1]\n\t"
"v_pk_add_f32 v[134:135], v[38:39], v[134:135] neg_lo:[0,1] neg_hi:[0,1]\n\t"
"v_pk_fma_f32 v[20:21], v[116:117], s[90:91], v[20:21]\n\t"
"v_pk_fma_f32 v[22:23], v[118:119], s[90:91], v[22:23]\n\t"
"v_pk_add_f32 v[116:117], v[20:21], v[116:117] neg_lo:[0,1] neg_hi:[0,1]\n\t"
"v_pk_add_f32 v[118:119], v[22:23], v[118:119] neg_lo:[0,1] neg_hi:[0,1]\n\t"
"v_pk_fma_f32 v[4:5], v[100:101], s[90:91], v[4:5]\n\t"
"v_pk_fma_f32 v[6:7], v[102:103], s[90:91], v[6:7]\n\t"
"v_pk_add_f32 v[100:101], v[4:5], v[100:101] neg_lo:[0,1] neg_hi:[0,1]\n\t"
"v_pk_add_f32 v[102:103], v[6:7], v[102:103] neg_lo:[0,1] neg_hi:[0,1]\n\t"
"v_pk_fma_f32 v[8:9], v[104:105], s[90:91], v[8:9]\n\t"
"v_pk_fma_f32 v[10:11], v[106:107], s[90:91], v[10:11]\n\t"
"v_pk_add_f32 v[104:105], v[8:9], v[104:105] neg_lo:[0,1] neg_hi:[0,1]\n\t"
"v_pk_add_f32 v[106:107], v[10:11], v[106:107] neg_lo:[0,1] neg_hi:[0,1]\n\t"
"v_pk_fma_f32 v[24:25], v[120:121], s[90:91], v[24:25]\n\t"
"v_pk_fma_f32 v[26:27], v[122:123], s[90:91], v[26:27]\n\t"
"v_pk_add_f32 v[120:121], v[24:25], v[120:121] neg_lo:[0,1] neg_hi:[0,1]\n\t"
"v_pk_add_f32 v[122:123], v[26:27], v[122:123] neg_lo:[0,1] neg_hi:[0,1]\n\t"
"v_pk_fma_f32 v[40:41], v[136:137], s[90:91], v[40:41]\n\t"
"v_pk_fma_f32 v[42:43], v[138:139], s[90:91], v[42:43]\n\t"
"v_pk_add_f32 v[136:137], v[40:41], v[136:137] neg_lo:[0,1] neg_hi:[0,1]\n\t"
"v_pk_add_f32 v[138:139], v[42:43], v[138:139] neg_lo:[0,1] neg_hi:[0,1]\n\t"
"v_pk_fma_f32 v[44:45], v[140:141], s[90:91], v[44:45]\n\t"
"v_pk_fma_f32 v[46:47], v[142:143], s[90:91], v[46:47]\n\t"
"v_pk_add_f32 v[140:141], v[44:45], v[140:141] neg_lo:[0,1] neg_hi:[0,1]\n\t"
"v_pk_add_f32 v[142:143], v[46:47], v[142:143] neg_lo:[0,1] neg_hi:[0,1]\n\t"
"v_pk_fma_f32 v[28:29], v[124:125], s[90:91], v[28:29]\n\t"
"v_pk_fma_f32 v[30:31], v[126:127], s[90:91], v[30:31]\n\t"
"v_pk_add_f32 v[124:125], v[28:29], v[124:125] neg_lo:[0,1] neg_hi:[0,1]\n\t"
"v_pk_add_f32 v[126:127], v[30:31], v[126:127] neg_lo:[0,1] neg_hi:[0,1]\n\t"
"v_pk_fma_f32 v[12:13], v[108:109], s[90:91], v[12:13]\n\t"
"v_pk_fma_f32 v[14:15], v[110:111], s[90:91], v[14:15]\n\t"
"v_pk_add_f32 v[108:109], v[12:13], v[108:109] neg_lo:[0,1] neg_hi:[0,1]\n\t"
"v_pk_add_f32 v[110:111], v[14:15], v[110:111] neg_lo:[0,1] neg_hi:[0,1]\n\t"
"v_mfma_f32_16x16x32_bf16 v[48:51], a[0:3], a[144:147], v[48:51]\n\t"
"s_lshl_b32 m0, s73, 9\n\t"
"v_cvt_pk_bf16_f32 v194, v194, v198\n\t"
"v_cvt_pk_bf16_f32 v195, v195, v199\n\t"
"v_mfma_f32_16x16x32_bf16 v[48:51], a[4:7], a[160:163], v[48:51]\n\t"
"v_cvt_pk_bf16_f32 v201, v201, v205\n\t"
"v_cvt_pk_bf16_f32 v202, v202, v206\n\t"
"v_mfma_f32_16x16x32_bf16 v[64:67], a[8:11], a[144:147], v[64:67]\n\t"
"ds_write_addtid_b32 v194 offset:18496\n\t"
"v_cvt_pk_bf16_f32 v192, v192, v196\n\t"
"v_mfma_f32_16x16x32_bf16 v[64:67], a[12:15], a[160:163], v[64:67]\n\t"
"s_add_u32 m0, s26, 34944\n\t"
"ds_read_b64_tr_b16 a[200:201], v251 offset:41472\n\t"
"v_cvt_pk_bf16_f32 v209, v209, v213\n\t"
"v_mfma_f32_16x16x32_bf16 v[80:83], a[16:19], a[144:147], v[80:83]\n\t"
"ds_read_b128 a[144:147], v224 offset:0\n\t"
"v_cvt_pk_bf16_f32 v203, v203, v207\n\t"
"v_mfma_f32_16x16x32_bf16 v[80:83], a[20:23], a[160:163], v[80:83]\n\t"
"s_mov_b64 exec, s[36:37]\n\t"
"buffer_load_dwordx4 v240, s[12:15], s22 offen offset:3840 nt lds\n\t"
"s_mov_b64 exec, -1\n\t"
"v_mfma_f32_16x16x32_bf16 v[84:87], a[16:19], a[148:151], v[84:87]\n\t"
"s_lshl_b32 m0, s73, 9\n\t"
"ds_read_b128 a[160:163], v224 offset:4096\n\t"
"v_cvt_pk_bf16_f32 v218, v218, v222\n\t"
"v_mfma_f32_16x16x32_bf16 v[84:87], a[20:23], a[164:167], v[84:87]\n\t"
"ds_write_addtid_b32 v195 offset:18752\n\t"
"v_cvt_pk_bf16_f32 v219, v219, v223\n\t"
"v_mfma_f32_16x16x32_bf16 v[68:71], a[8:11], a[148:151], v[68:71]\n\t"
"ds_write_addtid_b32 v218 offset:30816\n\t"
"v_cvt_pk_bf16_f32 v208, v208, v212\n\t"
"v_mfma_f32_16x16x32_bf16 v[68:71], a[12:15], a[164:167], v[68:71]\n\t"
"buffer_load_dwordx4 a[136:139], v235, s[8:11], s55 offen offset:2048 sc1\n\t"
"v_mfma_f32_16x16x32_bf16 v[52:55], a[0:3], a[148:151], v[52:55]\n\t"
"ds_read_b128 a[148:151], v224 offset:512\n\t"
"v_cvt_pk_bf16_f32 v193, v193, v197\n\t"
"v_mfma_f32_16x16x32_bf16 v[52:55], a[4:7], a[164:167], v[52:55]\n\t"
"ds_read_b64_tr_b16 a[194:195], v248 offset:43776\n\t"
"v_cvt_pk_bf16_f32 v200, v200, v204\n\t"
"v_mfma_f32_16x16x32_bf16 v[56:59], a[0:3], a[152:155], v[56:59]\n\t"
"ds_write_addtid_b32 v193 offset:16704\n\t"
"v_cvt_pk_bf16_f32 v217, v217, v221\n\t"
"v_mfma_f32_16x16x32_bf16 v[56:59], a[4:7], a[168:171], v[56:59]\n\t"
"ds_read_b128 a[164:167], v224 offset:4608\n\t"
"v_cvt_pk_bf16_f32 v210, v210, v214\n\t"
"v_mfma_f32_16x16x32_bf16 v[72:75], a[8:11], a[152:155], v[72:75]\n\t"
"ds_write_addtid_b32 v210 offset:22592\n\t"
"v_cvt_pk_bf16_f32 v216, v216, v220\n\t"
"v_mfma_f32_16x16x32_bf16 v[72:75], a[12:15], a[168:171], v[72:75]\n\t"
"ds_write_addtid_b32 v203 offset:26976\n\t"
"v_mfma_f32_16x16x32_bf16 v[88:91], a[16:19], a[152:155], v[88:91]\n\t"
"ds_write_addtid_b32 v219 offset:31072\n\t"
"v_mfma_f32_16x16x32_bf16 v[88:91], a[20:23], a[168:171], v[88:91]\n\t"
"s_waitcnt lgkmcnt(8)\n\t"
"ds_write_addtid_b32 v208 offset:20544\n\t"
"v_cvt_pk_bf16_f32 v211, v211, v215\n\t"
"v_mfma_f32_16x16x32_bf16 v[92:95], a[16:19], a[156:159], v[92:95]\n\t"
"ds_write_addtid_b32 v209 offset:20800\n\t"
"v_mfma_f32_16x16x32_bf16 v[92:95], a[20:23], a[172:175], v[92:95]\n\t"
"ds_write_addtid_b32 v202 offset:26720\n\t"
"v_mfma_f32_16x16x32_bf16 v[76:79], a[8:11], a[156:159], v[76:79]\n\t"
"ds_read_b128 a[168:171], v224 offset:5120\n\t"
"v_mfma_f32_16x16x32_bf16 v[76:79], a[12:15], a[172:175], v[76:79]\n\t"
"ds_read_b64_tr_b16 a[196:197], v250 offset:46080\n\t"
"v_mfma_f32_16x16x32_bf16 v[60:63], a[0:3], a[156:159], v[60:63]\n\t"
"ds_write_addtid_b32 v211 offset:22848\n\t"
"v_mfma_f32_16x16x32_bf16 v[60:63], a[4:7], a[172:175], v[60:63]\n\t"
"s_waitcnt vmcnt(24) lgkmcnt(9)\n\t"
"ds_write_addtid_b32 v216 offset:28768\n\t"
"v_mfma_f32_16x16x32_bf16 v[0:3], a[24:27], a[144:147], v[0:3]\n\t"
"ds_write_addtid_b32 v200 offset:24672\n\t"
"v_mfma_f32_16x16x32_bf16 v[0:3], a[28:31], a[160:163], v[0:3]\n\t"
"ds_write_addtid_b32 v201 offset:24928\n\t"
"v_mfma_f32_16x16x32_bf16 v[16:19], a[32:35], a[144:147], v[16:19]\n\t"
"ds_write_addtid_b32 v192 offset:16448\n\t"
"v_mfma_f32_16x16x32_bf16 v[16:19], a[36:39], a[160:163], v[16:19]\n\t"
"ds_write_addtid_b32 v217 offset:29024\n\t"
"v_mfma_f32_16x16x32_bf16 v[32:35], a[40:43], a[144:147], v[32:35]\n\t"
"ds_read_b128 a[156:159], v224 offset:1536\n\t"
"v_mfma_f32_16x16x32_bf16 v[32:35], a[44:47], a[160:163], v[32:35]\n\t"
"ds_read_b128 a[172:175], v224 offset:5632\n\t"
"v_mfma_f32_16x16x32_bf16 v[36:39], a[40:43], a[148:151], v[36:39]\n\t"
"ds_read_b128 a[152:155], v224 offset:1024\n\t"
"v_mfma_f32_16x16x32_bf16 v[36:39], a[44:47], a[164:167], v[36:39]\n\t"
"ds_read_b64_tr_b16 a[202:203], v249 offset:43776\n\t"
"v_mfma_f32_16x16x32_bf16 v[20:23], a[32:35], a[148:151], v[20:23]\n\t"
"ds_read_b64_tr_b16 a[204:205], v251 offset:46080\n\t"
"v_mfma_f32_16x16x32_bf16 v[20:23], a[36:39], a[164:167], v[20:23]\n\t"
"v_mfma_f32_16x16x32_bf16 v[4:7], a[24:27], a[148:151], v[4:7]\n\t"
"s_waitcnt lgkmcnt(2)\n\t"
"v_mfma_f32_16x16x32_bf16 v[4:7], a[28:31], a[164:167], v[4:7]\n\t"
"s_barrier\n\t"
"v_mfma_f32_16x16x32_bf16 v[8:11], a[24:27], a[152:155], v[8:11]\n\t"
"ds_read_b128 a[160:163], v224 offset:22592\n\t"
"v_mfma_f32_16x16x32_bf16 v[8:11], a[28:31], a[168:171], v[8:11]\n\t"
"ds_read_b128 a[144:147], v224 offset:18496\n\t"
"v_mfma_f32_16x16x32_bf16 v[24:27], a[32:35], a[152:155], v[24:27]\n\t"
"ds_read_b128 a[148:151], v224 offset:19008\n\t"
"v_mfma_f32_16x16x32_bf16 v[24:27], a[36:39], a[168:171], v[24:27]\n\t"
"ds_read_b128 a[164:167], v224 offset:23104\n\t"
"v_mfma_f32_16x16x32_bf16 v[40:43], a[40:43], a[152:155], v[40:43]\n\t"
"ds_read_b128 a[152:155], v224 offset:19520\n\t"
"v_mfma_f32_16x16x32_bf16 v[40:43], a[44:47], a[168:171], v[40:43]\n\t"
"buffer_load_dwordx4 a[140:143], v235, s[8:11], s55 offen offset:3072 sc1\n\t"
"v_mfma_f32_16x16x32_bf16 v[44:47], a[40:43], a[156:159], v[44:47]\n\t"
"ds_read_b128 a[168:171], v224 offset:23616\n\t"
"v_mfma_f32_16x16x32_bf16 v[44:47], a[44:47], a[172:175], v[44:47]\n\t"
"v_mfma_f32_16x16x32_bf16 v[28:31], a[32:35], a[156:159], v[28:31]\n\t"
"v_mfma_f32_16x16x32_bf16 v[28:31], a[36:39], a[172:175], v[28:31]\n\t"
"buffer_load_dwordx4 a[132:135], v235, s[8:11], s55 offen offset:1024 sc1\n\t"
"v_mfma_f32_16x16x32_bf16 v[12:15], a[24:27], a[156:159], v[12:15]\n\t"
"ds_read_b128 a[156:159], v224 offset:20032\n\t"
"v_mfma_f32_16x16x32_bf16 v[12:15], a[28:31], a[172:175], v[12:15]\n\t"
"ds_read_b128 a[172:175], v224 offset:24128\n\t"
"v_mfma_f32_16x16x32_bf16 v[192:195], a[212:215], a[176:179], 0\n\t"
"buffer_load_dwordx4 a[128:131], v235, s[8:11], s55 offen offset:0 sc1\n\t"
"v_mfma_f32_16x16x32_bf16 v[196:199], a[212:215], a[184:187], 0\n\t"
"s_nop 0\n\t"
"ds_read_b64_tr_b16 a[176:177], v248 offset:0\n\t"
"ds_read_b64_tr_b16 a[184:185], v249 offset:0\n\t"
"v_mfma_f32_16x16x32_bf16 v[200:203], a[212:215], a[178:181], 0\n\t"
"ds_read_b64_tr_b16 a[178:179], v250 offset:2304\n\t"
"v_mfma_f32_16x16x32_bf16 v[204:207], a[212:215], a[186:189], 0\n\t"
"ds_read_b64_tr_b16 a[180:181], v248 offset:4608\n\t"
"v_mfma_f32_16x16x32_bf16 v[208:211], a[212:215], a[192:195], 0\n\t"
"s_waitcnt lgkmcnt(6)\n\t"
"buffer_load_dwordx4 a[120:123], v252, s[8:11], s54 offen offset:2048 sc1\n\t"
"v_mfma_f32_16x16x32_bf16 v[212:215], a[212:215], a[200:203], 0\n\t"
"s_nop 0\n\t"
"ds_read_b64_tr_b16 a[186:187], v251 offset:2304\n\t"
"ds_read_b64_tr_b16 a[188:189], v249 offset:4608\n\t"
"v_mfma_f32_16x16x32_bf16 v[216:219], a[212:215], a[194:197], 0\n\t"
"s_nop 0\n\t"
"ds_read_b64_tr_b16 a[192:193], v250 offset:0\n\t"
"ds_read_b64_tr_b16 a[200:201], v251 offset:0\n\t"
"v_mfma_f32_16x16x32_bf16 v[220:223], a[212:215], a[202:205], 0\n\t"
"s_waitcnt vmcnt(22)\n\t"
"ds_read_b64_tr_b16 a[194:195], v248 offset:2304\n\t"
"v_cvt_pk_bf16_f32 v193, v193, v197\n\t"
"v_mfma_f32_16x16x32_bf16 v[144:147], a[48:51], a[144:147], v[144:147]\n\t"
"ds_read_b64_tr_b16 a[196:197], v250 offset:4608\n\t"
"v_cvt_pk_bf16_f32 v195, v195, v199\n\t"
"v_mfma_f32_16x16x32_bf16 v[144:147], a[52:55], a[160:163], v[144:147]\n\t"
"buffer_load_dwordx4 a[124:127], v252, s[8:11], s54 offen offset:3072 sc1\n\t"
"v_mfma_f32_16x16x32_bf16 v[160:163], a[56:59], a[144:147], v[160:163]\n\t"
"ds_write_addtid_b32 v193 offset:256\n\t"
"v_cvt_pk_bf16_f32 v202, v202, v206\n\t"
"v_mfma_f32_16x16x32_bf16 v[160:163], a[60:63], a[160:163], v[160:163]\n\t"
"ds_write_addtid_b32 v202 offset:10272\n\t"
"v_cvt_pk_bf16_f32 v211, v211, v215\n\t"
"v_mfma_f32_16x16x32_bf16 v[176:179], a[64:67], a[144:147], v[176:179]\n\t"
"ds_write_addtid_b32 v195 offset:2304\n\t"
"v_cvt_pk_bf16_f32 v203, v203, v207\n\t"
"v_mfma_f32_16x16x32_bf16 v[176:179], a[68:71], a[160:163], v[176:179]\n\t"
"buffer_load_dwordx4 a[16:19], v235, s[8:11], s41 offen offset:0 sc1\n\t"
"v_mfma_f32_16x16x32_bf16 v[180:183], a[64:67], a[148:151], v[180:183]\n\t"
"ds_read_b128 a[144:147], v224 offset:16448\n\t"
"v_cvt_pk_bf16_f32 v216, v216, v220\n\t"
"v_mfma_f32_16x16x32_bf16 v[180:183], a[68:71], a[164:167], v[180:183]\n\t"
"ds_read_b128 a[160:163], v224 offset:20544\n\t"
"v_cvt_pk_bf16_f32 v218, v218, v222\n\t"
"v_mfma_f32_16x16x32_bf16 v[164:167], a[56:59], a[148:151], v[164:167]\n\t"
"ds_write_addtid_b32 v218 offset:14368\n\t"
"v_cvt_pk_bf16_f32 v192, v192, v196\n\t"
"v_mfma_f32_16x16x32_bf16 v[164:167], a[60:63], a[164:167], v[164:167]\n\t"
"buffer_load_dwordx4 a[20:23], v235, s[8:11], s41 offen offset:1024 sc1\n\t"
"v_mfma_f32_16x16x32_bf16 v[148:151], a[48:51], a[148:151], v[148:151]\n\t"
"ds_read_b128 a[148:151], v224 offset:16960\n\t"
"v_cvt_pk_bf16_f32 v201, v201, v205\n\t"
"v_mfma_f32_16x16x32_bf16 v[148:151], a[52:55], a[164:167], v[148:151]\n\t"
"buffer_load_dwordx4 a[12:15], v252, s[8:11], s40 offen offset:3072 sc1\n\t"
"v_mfma_f32_16x16x32_bf16 v[152:155], a[48:51], a[152:155], v[152:155]\n\t"
"ds_write_addtid_b32 v192 offset:0\n\t"
"v_cvt_pk_bf16_f32 v219, v219, v223\n\t"
"v_mfma_f32_16x16x32_bf16 v[152:155], a[52:55], a[168:171], v[152:155]\n\t"
"ds_read_b128 a[164:167], v224 offset:21056\n\t"
"v_cvt_pk_bf16_f32 v200, v200, v204\n\t"
"v_mfma_f32_16x16x32_bf16 v[168:171], a[56:59], a[152:155], v[168:171]\n\t"
"ds_write_addtid_b32 v216 offset:12320\n\t"
"v_cvt_pk_bf16_f32 v209, v209, v213\n\t"
"v_mfma_f32_16x16x32_bf16 v[168:171], a[60:63], a[168:171], v[168:171]\n\t"
"buffer_load_dwordx4 a[8:11], v252, s[8:11], s40 offen offset:2048 sc1\n\t"
"v_mfma_f32_16x16x32_bf16 v[184:187], a[64:67], a[152:155], v[184:187]\n\t"
"ds_write_addtid_b32 v209 offset:4352\n\t"
"v_cvt_pk_bf16_f32 v217, v217, v221\n\t"
"v_mfma_f32_16x16x32_bf16 v[184:187], a[68:71], a[168:171], v[184:187]\n\t"
"s_waitcnt lgkmcnt(6)\n\t"
"ds_write_addtid_b32 v201 offset:8480\n\t"
"v_cvt_pk_bf16_f32 v194, v194, v198\n\t"
"v_mfma_f32_16x16x32_bf16 v[188:191], a[64:67], a[156:159], v[188:191]\n\t"
"ds_write_addtid_b32 v219 offset:14624\n\t"
"v_cvt_pk_bf16_f32 v208, v208, v212\n\t"
"v_mfma_f32_16x16x32_bf16 v[188:191], a[68:71], a[172:175], v[188:191]\n\t"
"buffer_load_dwordx4 a[0:3], v252, s[8:11], s40 offen offset:0 sc1\n\t"
"v_mfma_f32_16x16x32_bf16 v[172:175], a[56:59], a[156:159], v[172:175]\n\t"
"s_nop 0\n\t"
"ds_write_addtid_b32 v208 offset:4096\n\t"
"ds_write_addtid_b32 v211 offset:6400\n\t"
"v_mfma_f32_16x16x32_bf16 v[172:175], a[60:63], a[172:175], v[172:175]\n\t"
"ds_read_b128 a[152:155], v224 offset:17472\n\t"
"v_cvt_pk_bf16_f32 v210, v210, v214\n\t"
"v_mfma_f32_16x16x32_bf16 v[156:159], a[48:51], a[156:159], v[156:159]\n\t"
"s_nop 0\n\t"
"ds_write_addtid_b32 v194 offset:2048\n\t"
"ds_write_addtid_b32 v200 offset:8224\n\t"
"v_mfma_f32_16x16x32_bf16 v[156:159], a[52:55], a[172:175], v[156:159]\n\t"
"s_waitcnt vmcnt(22) lgkmcnt(9)\n\t"
"buffer_load_dwordx4 a[4:7], v252, s[8:11], s40 offen offset:1024 sc1\n\t"
"v_mfma_f32_16x16x32_bf16 v[96:99], a[72:75], a[144:147], v[96:99]\n\t"
"ds_write_addtid_b32 v210 offset:6144\n\t"
"v_mfma_f32_16x16x32_bf16 v[96:99], a[76:79], a[160:163], v[96:99]\n\t"
"ds_write_addtid_b32 v203 offset:10528\n\t"
"v_mfma_f32_16x16x32_bf16 v[112:115], a[80:83], a[144:147], v[112:115]\n\t"
"ds_write_addtid_b32 v217 offset:12576\n\t"
"v_mfma_f32_16x16x32_bf16 v[112:115], a[84:87], a[160:163], v[112:115]\n\t"
"buffer_load_dwordx4 a[40:43], v235, s[8:11], s40 offen offset:2048 sc1\n\t"
"v_mfma_f32_16x16x32_bf16 v[128:131], a[88:91], a[144:147], v[128:131]\n\t"
"ds_read_b128 a[168:171], v224 offset:21568\n\t"
"v_mfma_f32_16x16x32_bf16 v[128:131], a[92:95], a[160:163], v[128:131]\n\t"
"ds_read_b128 a[172:175], v224 offset:22080\n\t"
"v_mfma_f32_16x16x32_bf16 v[132:135], a[88:91], a[148:151], v[132:135]\n\t"
"ds_read_b128 a[156:159], v224 offset:17984\n\t"
"v_mfma_f32_16x16x32_bf16 v[132:135], a[92:95], a[164:167], v[132:135]\n\t"
"ds_read_b64_tr_b16 a[202:203], v249 offset:2304\n\t"
"v_mfma_f32_16x16x32_bf16 v[116:119], a[80:83], a[148:151], v[116:119]\n\t"
"ds_read_b64_tr_b16 a[204:205], v251 offset:4608\n\t"
"v_mfma_f32_16x16x32_bf16 v[116:119], a[84:87], a[164:167], v[116:119]\n\t"
"buffer_load_dwordx4 a[44:47], v235, s[8:11], s40 offen offset:3072 sc1\n\t"
"v_mfma_f32_16x16x32_bf16 v[100:103], a[72:75], a[148:151], v[100:103]\n\t"
"s_waitcnt lgkmcnt(2)\n\t"
"v_mfma_f32_16x16x32_bf16 v[100:103], a[76:79], a[164:167], v[100:103]\n\t"
"buffer_load_dwordx4 a[36:39], v235, s[8:11], s40 offen offset:1024 sc1\n\t"
"s_barrier\n\t"
"v_mfma_f32_16x16x32_bf16 v[104:107], a[72:75], a[152:155], v[104:107]\n\t"
"ds_read_b128 a[144:147], v224 offset:2048\n\t"
"v_mfma_f32_16x16x32_bf16 v[104:107], a[76:79], a[168:171], v[104:107]\n\t"
"ds_read_b128 a[148:151], v224 offset:2560\n\t"
"v_mfma_f32_16x16x32_bf16 v[120:123], a[80:83], a[152:155], v[120:123]\n\t"
"ds_read_b128 a[160:163], v224 offset:6144\n\t"
"v_mfma_f32_16x16x32_bf16 v[120:123], a[84:87], a[168:171], v[120:123]\n\t"
"ds_read_b128 a[164:167], v224 offset:6656\n\t"
"v_mfma_f32_16x16x32_bf16 v[136:139], a[88:91], a[152:155], v[136:139]\n\t"
"ds_read_b128 a[152:155], v224 offset:3072\n\t"
"v_mfma_f32_16x16x32_bf16 v[136:139], a[92:95], a[168:171], v[136:139]\n\t"
"buffer_load_dwordx4 a[32:35], v235, s[8:11], s40 offen offset:0 sc1\n\t"
"v_mfma_f32_16x16x32_bf16 v[140:143], a[88:91], a[156:159], v[140:143]\n\t"
"ds_read_b128 a[168:171], v224 offset:7168\n\t"
"v_mfma_f32_16x16x32_bf16 v[140:143], a[92:95], a[172:175], v[140:143]\n\t"
"v_mfma_f32_16x16x32_bf16 v[124:127], a[80:83], a[156:159], v[124:127]\n\t"
"v_mfma_f32_16x16x32_bf16 v[124:127], a[84:87], a[172:175], v[124:127]\n\t"
"buffer_load_dwordx4 a[24:27], v252, s[8:11], 0 offen offset:2048 sc1\n\t"
"v_mfma_f32_16x16x32_bf16 v[108:111], a[72:75], a[156:159], v[108:111]\n\t"
"ds_read_b128 a[156:159], v224 offset:3584\n\t"
"v_mfma_f32_16x16x32_bf16 v[108:111], a[76:79], a[172:175], v[108:111]\n\t"
"ds_read_b128 a[172:175], v224 offset:7680\n\t"
"v_mfma_f32_16x16x32_bf16 v[192:195], a[212:215], a[176:179], 0\n\t"
"buffer_load_dwordx4 a[28:31], v252, s[8:11], 0 offen offset:3072 sc1\n\t"
"v_mfma_f32_16x16x32_bf16 v[196:199], a[212:215], a[184:187], 0\n\t"
"s_nop 0\n\t"
"ds_read_b64_tr_b16 a[176:177], v248 offset:42624\n\t"
"ds_read_b64_tr_b16 a[184:185], v249 offset:42624\n\t"
"v_mfma_f32_16x16x32_bf16 v[200:203], a[212:215], a[178:181], 0\n\t"
"s_nop 0\n\t"
"ds_read_b64_tr_b16 a[178:179], v250 offset:44928\n\t"
"ds_read_b64_tr_b16 a[180:181], v248 offset:47232\n\t"
"v_mfma_f32_16x16x32_bf16 v[204:207], a[212:215], a[186:189], 0\n\t"
"s_nop 0\n\t"
"ds_read_b64_tr_b16 a[186:187], v251 offset:44928\n\t"
"ds_read_b64_tr_b16 a[188:189], v249 offset:47232\n\t"
"v_mfma_f32_16x16x32_bf16 v[208:211], a[212:215], a[192:195], 0\n\t"
"s_waitcnt lgkmcnt(8)\n\t"
"buffer_load_dwordx4 a[64:67], v235, s[8:11], s68 offen offset:0 sc1\n\t"
"v_mfma_f32_16x16x32_bf16 v[212:215], a[212:215], a[200:203], 0\n\t"
"ds_read_b64_tr_b16 a[192:193], v250 offset:42624\n\t"
"v_mfma_f32_16x16x32_bf16 v[216:219], a[212:215], a[194:197], 0\n\t"
"s_nop 0\n\t"
"ds_read_b64_tr_b16 a[200:201], v251 offset:42624\n\t"
"ds_read_b64_tr_b16 a[194:195], v248 offset:44928\n\t"
"v_mfma_f32_16x16x32_bf16 v[220:223], a[212:215], a[202:205], 0\n\t"
"s_waitcnt vmcnt(24)\n\t"
"ds_read_b64_tr_b16 a[196:197], v250 offset:47232\n\t"
"v_cvt_pk_bf16_f32 v192, v192, v196\n\t"
"v_mfma_f32_16x16x32_bf16 v[48:51], a[96:99], a[144:147], v[48:51]\n\t"
"ds_read_b64_tr_b16 a[202:203], v249 offset:44928\n\t"
"v_cvt_pk_bf16_f32 v194, v194, v198\n\t"
"v_mfma_f32_16x16x32_bf16 v[48:51], a[100:103], a[160:163], v[48:51]\n\t"
"buffer_load_dwordx4 a[68:71], v235, s[8:11], s68 offen offset:1024 sc1\n\t"
"v_mfma_f32_16x16x32_bf16 v[64:67], a[104:107], a[144:147], v[64:67]\n\t"
"ds_write_addtid_b32 v194 offset:18496\n\t"
"v_cvt_pk_bf16_f32 v201, v201, v205\n\t"
"v_mfma_f32_16x16x32_bf16 v[64:67], a[108:111], a[160:163], v[64:67]\n\t"
"ds_write_addtid_b32 v192 offset:16448\n\t"
"v_cvt_pk_bf16_f32 v208, v208, v212\n\t"
"v_mfma_f32_16x16x32_bf16 v[80:83], a[112:115], a[144:147], v[80:83]\n\t"
"ds_read_b128 a[144:147], v224 offset:0\n\t"
"v_cvt_pk_bf16_f32 v209, v209, v213\n\t"
"v_mfma_f32_16x16x32_bf16 v[80:83], a[116:119], a[160:163], v[80:83]\n\t"
"buffer_load_dwordx4 a[60:63], v252, s[8:11], s67 offen offset:3072 sc1\n\t"
"v_mfma_f32_16x16x32_bf16 v[84:87], a[112:115], a[148:151], v[84:87]\n\t"
"ds_read_b128 a[160:163], v224 offset:4096\n\t"
"v_cvt_pk_bf16_f32 v211, v211, v215\n\t"
"v_mfma_f32_16x16x32_bf16 v[84:87], a[116:119], a[164:167], v[84:87]\n\t"
"ds_write_addtid_b32 v211 offset:22848\n\t"
"v_cvt_pk_bf16_f32 v202, v202, v206\n\t"
"v_mfma_f32_16x16x32_bf16 v[68:71], a[104:107], a[148:151], v[68:71]\n\t"
"ds_write_addtid_b32 v201 offset:24928\n\t"
"v_cvt_pk_bf16_f32 v218, v218, v222\n\t"
"v_mfma_f32_16x16x32_bf16 v[68:71], a[108:111], a[164:167], v[68:71]\n\t"
"buffer_load_dwordx4 a[56:59], v252, s[8:11], s67 offen offset:2048 sc1\n\t"
"v_mfma_f32_16x16x32_bf16 v[52:55], a[96:99], a[148:151], v[52:55]\n\t"
"ds_read_b128 a[148:151], v224 offset:512\n\t"
"v_cvt_pk_bf16_f32 v203, v203, v207\n\t"
"v_mfma_f32_16x16x32_bf16 v[52:55], a[100:103], a[164:167], v[52:55]\n\t"
"ds_read_b128 a[164:167], v224 offset:4608\n\t"
"v_cvt_pk_bf16_f32 v210, v210, v214\n\t"
"v_mfma_f32_16x16x32_bf16 v[56:59], a[96:99], a[152:155], v[56:59]\n\t"
"ds_write_addtid_b32 v210 offset:22592\n\t"
"v_cvt_pk_bf16_f32 v217, v217, v221\n\t"
"v_mfma_f32_16x16x32_bf16 v[56:59], a[100:103], a[168:171], v[56:59]\n\t"
"buffer_load_dwordx4 a[48:51], v252, s[8:11], s67 offen offset:0 sc1\n\t"
"v_mfma_f32_16x16x32_bf16 v[72:75], a[104:107], a[152:155], v[72:75]\n\t"
"ds_write_addtid_b32 v202 offset:26720\n\t"
"v_cvt_pk_bf16_f32 v216, v216, v220\n\t"
"v_mfma_f32_16x16x32_bf16 v[72:75], a[108:111], a[168:171], v[72:75]\n\t"
"ds_write_addtid_b32 v216 offset:28768\n\t"
"v_cvt_pk_bf16_f32 v200, v200, v204\n\t"
"v_mfma_f32_16x16x32_bf16 v[88:91], a[112:115], a[152:155], v[88:91]\n\t"
"ds_write_addtid_b32 v203 offset:26976\n\t"
"v_cvt_pk_bf16_f32 v193, v193, v197\n\t"
"v_mfma_f32_16x16x32_bf16 v[88:91], a[116:119], a[168:171], v[88:91]\n\t"
"s_waitcnt lgkmcnt(7)\n\t"
"buffer_load_dwordx4 a[52:55], v252, s[8:11], s67 offen offset:1024 sc1\n\t"
"v_mfma_f32_16x16x32_bf16 v[92:95], a[112:115], a[156:159], v[92:95]\n\t"
"s_nop 0\n\t"
"ds_write_addtid_b32 v217 offset:29024\n\t"
"ds_write_addtid_b32 v218 offset:30816\n\t"
"v_mfma_f32_16x16x32_bf16 v[92:95], a[116:119], a[172:175], v[92:95]\n\t"
"ds_write_addtid_b32 v209 offset:20800\n\t"
"v_cvt_pk_bf16_f32 v195, v195, v199\n\t"
"v_mfma_f32_16x16x32_bf16 v[76:79], a[104:107], a[156:159], v[76:79]\n\t"
"ds_write_addtid_b32 v200 offset:24672\n\t"
"v_cvt_pk_bf16_f32 v219, v219, v223\n\t"
"v_mfma_f32_16x16x32_bf16 v[76:79], a[108:111], a[172:175], v[76:79]\n\t"
"buffer_load_dwordx4 a[88:91], v235, s[8:11], s67 offen offset:2048 sc1\n\t"
"v_mfma_f32_16x16x32_bf16 v[60:63], a[96:99], a[156:159], v[60:63]\n\t"
"ds_read_b128 a[168:171], v224 offset:5120\n\t"
"v_mfma_f32_16x16x32_bf16 v[60:63], a[100:103], a[172:175], v[60:63]\n\t"
"s_waitcnt vmcnt(19) lgkmcnt(8)\n\t"
"ds_write_addtid_b32 v195 offset:18752\n\t"
"v_mfma_f32_16x16x32_bf16 v[0:3], a[120:123], a[144:147], v[0:3]\n\t"
"ds_write_addtid_b32 v208 offset:20544\n\t"
"v_mfma_f32_16x16x32_bf16 v[0:3], a[124:127], a[160:163], v[0:3]\n\t"
"buffer_load_dwordx4 a[92:95], v235, s[8:11], s67 offen offset:3072 sc1\n\t"
"v_mfma_f32_16x16x32_bf16 v[16:19], a[128:131], a[144:147], v[16:19]\n\t"
"ds_write_addtid_b32 v219 offset:31072\n\t"
"v_mfma_f32_16x16x32_bf16 v[16:19], a[132:135], a[160:163], v[16:19]\n\t"
"ds_write_addtid_b32 v193 offset:16704\n\t"
"v_mfma_f32_16x16x32_bf16 v[32:35], a[136:139], a[144:147], v[32:35]\n\t"
"ds_read_b128 a[172:175], v224 offset:5632\n\t"
"v_mfma_f32_16x16x32_bf16 v[32:35], a[140:143], a[160:163], v[32:35]\n\t"
"ds_read_b128 a[152:155], v224 offset:1024\n\t"
"v_mfma_f32_16x16x32_bf16 v[36:39], a[136:139], a[148:151], v[36:39]\n\t"
"ds_read_b128 a[156:159], v224 offset:1536\n\t"
"v_mfma_f32_16x16x32_bf16 v[36:39], a[140:143], a[164:167], v[36:39]\n\t"
"buffer_load_dwordx4 a[84:87], v235, s[8:11], s67 offen offset:1024 sc1\n\t"
"v_mfma_f32_16x16x32_bf16 v[20:23], a[128:131], a[148:151], v[20:23]\n\t"
"ds_read_b64_tr_b16 a[204:205], v251 offset:47232\n\t"
"v_mfma_f32_16x16x32_bf16 v[20:23], a[132:135], a[164:167], v[20:23]\n\t"
"buffer_load_dwordx4 a[80:83], v235, s[8:11], s67 offen offset:0 sc1\n\t"
"v_mfma_f32_16x16x32_bf16 v[4:7], a[120:123], a[148:151], v[4:7]\n\t"
"v_mfma_f32_16x16x32_bf16 v[4:7], a[124:127], a[164:167], v[4:7]\n\t"
"buffer_load_dwordx4 a[72:75], v252, s[8:11], s66 offen offset:2048 sc1\n\t"
"s_waitcnt lgkmcnt(0)\n\t"
"s_barrier\n\t"
"v_mfma_f32_16x16x32_bf16 v[8:11], a[120:123], a[152:155], v[8:11]\n\t"
"ds_read_b128 a[148:151], v224 offset:19008\n\t"
"v_mfma_f32_16x16x32_bf16 v[8:11], a[124:127], a[168:171], v[8:11]\n\t"
"ds_read_b128 a[144:147], v224 offset:18496\n\t"
"v_mfma_f32_16x16x32_bf16 v[24:27], a[128:131], a[152:155], v[24:27]\n\t"
"ds_read_b128 a[160:163], v224 offset:22592\n\t"
"v_mfma_f32_16x16x32_bf16 v[24:27], a[132:135], a[168:171], v[24:27]\n\t"
"ds_read_b128 a[164:167], v224 offset:23104\n\t"
"v_mfma_f32_16x16x32_bf16 v[40:43], a[136:139], a[152:155], v[40:43]\n\t"
"ds_read_b128 a[228:231], v255 offset:62208\n\t"
"v_mfma_f32_16x16x32_bf16 v[40:43], a[140:143], a[168:171], v[40:43]\n\t"
"buffer_load_dwordx4 a[76:79], v252, s[8:11], s66 offen offset:3072 sc1\n\t"
"v_mfma_f32_16x16x32_bf16 v[44:47], a[136:139], a[156:159], v[44:47]\n\t"
"ds_read_b128 a[168:171], v224 offset:23616\n\t"
"v_mfma_f32_16x16x32_bf16 v[44:47], a[140:143], a[172:175], v[44:47]\n\t"
"buffer_load_dwordx4 a[112:115], v235, s[8:11], s53 offen offset:0 sc1\n\t"
"v_mfma_f32_16x16x32_bf16 v[28:31], a[128:131], a[156:159], v[28:31]\n\t"
"ds_read_b128 a[152:155], v224 offset:19520\n\t"
"v_mfma_f32_16x16x32_bf16 v[28:31], a[132:135], a[172:175], v[28:31]\n\t"
"buffer_load_dwordx4 a[116:119], v235, s[8:11], s53 offen offset:1024 sc1\n\t"
"v_mfma_f32_16x16x32_bf16 v[12:15], a[120:123], a[156:159], v[12:15]\n\t"
"ds_read_b128 a[156:159], v224 offset:20032\n\t"
"v_mfma_f32_16x16x32_bf16 v[12:15], a[124:127], a[172:175], v[12:15]\n\t"
"ds_read_b128 a[172:175], v224 offset:24128\n\t"
"v_mfma_f32_16x16x32_bf16 v[192:195], a[212:215], a[176:179], 0\n\t"
"ds_read_b64_tr_b16 a[176:177], v248 offset:1152\n\t"
"v_mfma_f32_16x16x32_bf16 v[196:199], a[212:215], a[184:187], 0\n\t"
"ds_read_b64_tr_b16 a[184:185], v249 offset:1152\n\t"
"v_mfma_f32_16x16x32_bf16 v[200:203], a[212:215], a[178:181], 0\n\t"
"s_nop 0\n\t"
"ds_read_b64_tr_b16 a[178:179], v250 offset:3456\n\t"
"ds_read_b64_tr_b16 a[180:181], v248 offset:5760\n\t"
"v_mfma_f32_16x16x32_bf16 v[204:207], a[212:215], a[186:189], 0\n\t"
"buffer_load_dwordx4 a[108:111], v252, s[8:11], s52 offen offset:3072 sc1\n\t"
"v_mfma_f32_16x16x32_bf16 v[208:211], a[212:215], a[192:195], 0\n\t"
"s_waitcnt lgkmcnt(7)\n\t"
"ds_write_b128 v255, a[228:231] offset:23040\n\t"
"v_mfma_f32_16x16x32_bf16 v[212:215], a[212:215], a[200:203], 0\n\t"
"buffer_load_dwordx4 a[104:107], v252, s[8:11], s52 offen offset:2048 sc1\n\t"
"v_mfma_f32_16x16x32_bf16 v[216:219], a[212:215], a[194:197], 0\n\t"
"v_mfma_f32_16x16x32_bf16 v[220:223], a[212:215], a[202:205], 0\n\t"
"s_waitcnt vmcnt(22) lgkmcnt(7)\n\t"
"v_cvt_pk_bf16_f32 v192, v192, v196\n\t"
"v_cvt_pk_bf16_f32 v195, v195, v199\n\t"
"v_mfma_f32_16x16x32_bf16 v[48:51], a[0:3], a[144:147], v[48:51]\n\t"
"ds_read_b64_tr_b16 a[186:187], v251 offset:3456\n\t"
"v_cvt_pk_bf16_f32 v194, v194, v198\n\t"
"v_mfma_f32_16x16x32_bf16 v[48:51], a[4:7], a[160:163], v[48:51]\n\t"
"buffer_load_dwordx4 a[96:99], v252, s[8:11], s52 offen offset:0 sc1\n\t"
"v_mfma_f32_16x16x32_bf16 v[64:67], a[8:11], a[144:147], v[64:67]\n\t"
"ds_read_b64_tr_b16 a[188:189], v249 offset:5760\n\t"
"v_cvt_pk_bf16_f32 v203, v203, v207\n\t"
"v_mfma_f32_16x16x32_bf16 v[64:67], a[12:15], a[160:163], v[64:67]\n\t"
"ds_write_addtid_b32 v195 offset:2304\n\t"
"v_cvt_pk_bf16_f32 v209, v209, v213\n\t"
"v_mfma_f32_16x16x32_bf16 v[80:83], a[16:19], a[144:147], v[80:83]\n\t"
"ds_read_b128 a[144:147], v224 offset:16448\n\t"
"v_cvt_pk_bf16_f32 v208, v208, v212\n\t"
"v_mfma_f32_16x16x32_bf16 v[80:83], a[20:23], a[160:163], v[80:83]\n\t"
"buffer_load_dwordx4 a[100:103], v252, s[8:11], s52 offen offset:1024 sc1\n\t"
"v_mfma_f32_16x16x32_bf16 v[84:87], a[16:19], a[148:151], v[84:87]\n\t"
"ds_read_b64_tr_b16 a[192:193], v250 offset:1152\n\t"
"v_cvt_pk_bf16_f32 v219, v219, v223\n\t"
"v_mfma_f32_16x16x32_bf16 v[84:87], a[20:23], a[164:167], v[84:87]\n\t"
"s_nop 0\n\t"
"ds_read_b128 a[160:163], v224 offset:20544\n\t"
"ds_write_addtid_b32 v219 offset:14624\n\t"
"v_mfma_f32_16x16x32_bf16 v[68:71], a[8:11], a[148:151], v[68:71]\n\t"
"ds_write_addtid_b32 v209 offset:4352\n\t"
"v_cvt_pk_bf16_f32 v193, v193, v197\n\t"
"v_mfma_f32_16x16x32_bf16 v[68:71], a[12:15], a[164:167], v[68:71]\n\t"
"buffer_load_dwordx4 a[136:139], v235, s[8:11], s52 offen offset:2048 sc1\n\t"
"v_mfma_f32_16x16x32_bf16 v[52:55], a[0:3], a[148:151], v[52:55]\n\t"
"ds_write_addtid_b32 v192 offset:0\n\t"
"v_cvt_pk_bf16_f32 v216, v216, v220\n\t"
"v_mfma_f32_16x16x32_bf16 v[52:55], a[4:7], a[164:167], v[52:55]\n\t"
"ds_read_b128 a[148:151], v224 offset:16960\n\t"
"v_cvt_pk_bf16_f32 v210, v210, v214\n\t"
"v_mfma_f32_16x16x32_bf16 v[56:59], a[0:3], a[152:155], v[56:59]\n\t"
"ds_read_b128 a[164:167], v224 offset:21056\n\t"
"v_cvt_pk_bf16_f32 v211, v211, v215\n\t"
"v_mfma_f32_16x16x32_bf16 v[56:59], a[4:7], a[168:171], v[56:59]\n\t"
"buffer_load_dwordx4 a[140:143], v235, s[8:11], s52 offen offset:3072 sc1\n\t"
"v_mfma_f32_16x16x32_bf16 v[72:75], a[8:11], a[152:155], v[72:75]\n\t"
"ds_write_addtid_b32 v193 offset:256\n\t"
"v_cvt_pk_bf16_f32 v217, v217, v221\n\t"
"v_mfma_f32_16x16x32_bf16 v[72:75], a[12:15], a[168:171], v[72:75]\n\t"
"ds_write_addtid_b32 v203 offset:10528\n\t"
"v_cvt_pk_bf16_f32 v201, v201, v205\n\t"
"v_mfma_f32_16x16x32_bf16 v[88:91], a[16:19], a[152:155], v[88:91]\n\t"
"ds_read_b64_tr_b16 a[200:201], v251 offset:1152\n\t"
"v_cvt_pk_bf16_f32 v200, v200, v204\n\t"
"v_mfma_f32_16x16x32_bf16 v[88:91], a[20:23], a[168:171], v[88:91]\n\t"
"s_waitcnt lgkmcnt(7)\n\t"
"buffer_load_dwordx4 a[132:135], v235, s[8:11], s52 offen offset:1024 sc1\n\t"
"v_mfma_f32_16x16x32_bf16 v[92:95], a[16:19], a[156:159], v[92:95]\n\t"
"s_nop 0\n\t"
"ds_write_addtid_b32 v194 offset:2048\n\t"
"ds_write_addtid_b32 v200 offset:8224\n\t"
"s_add_u32 m0, s26, 124544\n\t"
"v_mfma_f32_16x16x32_bf16 v[92:95], a[20:23], a[172:175], v[92:95]\n\t"
"buffer_load_dwordx4 v236, s[12:15], s22 offen offset:128 nt lds\n\t"
"s_lshl_b32 m0, s73, 9\n\t"
"v_mfma_f32_16x16x32_bf16 v[76:79], a[8:11], a[156:159], v[76:79]\n\t"
"s_nop 0\n\t"
"ds_read_b128 a[168:171], v224 offset:21568\n\t"
"ds_write_addtid_b32 v201 offset:8480\n\t"
"v_mfma_f32_16x16x32_bf16 v[76:79], a[12:15], a[172:175], v[76:79]\n\t"
"s_nop 0\n\t"
"ds_write_addtid_b32 v208 offset:4096\n\t"
"ds_write_addtid_b32 v216 offset:12320\n\t"
"v_mfma_f32_16x16x32_bf16 v[60:63], a[0:3], a[156:159], v[60:63]\n\t"
"s_add_u32 m0, s26, 124544\n\t"
"ds_read_b128 a[152:155], v224 offset:17472\n\t"
"v_cvt_pk_bf16_f32 v202, v202, v206\n\t"
"v_mfma_f32_16x16x32_bf16 v[60:63], a[4:7], a[172:175], v[60:63]\n\t"
"s_waitcnt vmcnt(22) lgkmcnt(10)\n\t"
"buffer_load_dwordx4 v237, s[12:15], s22 offen offset:1152 nt lds\n\t"
"s_lshl_b32 m0, s73, 9\n\t"
"v_mfma_f32_16x16x32_bf16 v[0:3], a[24:27], a[144:147], v[0:3]\n\t"
"ds_write_addtid_b32 v211 offset:6400\n\t"
"v_cvt_pk_bf16_f32 v218, v218, v222\n\t"
"s_add_u32 m0, s26, 124544\n\t"
"v_mfma_f32_16x16x32_bf16 v[0:3], a[28:31], a[160:163], v[0:3]\n\t"
"buffer_load_dwordx4 v238, s[12:15], s22 offen offset:2176 nt lds\n\t"
"s_lshl_b32 m0, s73, 9\n\t"
"v_mfma_f32_16x16x32_bf16 v[16:19], a[32:35], a[144:147], v[16:19]\n\t"
"s_nop 0\n\t"
"ds_write_addtid_b32 v202 offset:10272\n\t"
"ds_write_addtid_b32 v217 offset:12576\n\t"
"v_mfma_f32_16x16x32_bf16 v[16:19], a[36:39], a[160:163], v[16:19]\n\t"
"s_nop 0\n\t"
"ds_write_addtid_b32 v210 offset:6144\n\t"
"ds_write_addtid_b32 v218 offset:14368\n\t"
"v_mfma_f32_16x16x32_bf16 v[32:35], a[40:43], a[144:147], v[32:35]\n\t"
"s_add_u32 m0, s26, 124544\n\t"
"ds_read_b128 a[156:159], v224 offset:17984\n\t"
"ds_read_b64_tr_b16 a[194:195], v248 offset:3456\n\t"
"v_mfma_f32_16x16x32_bf16 v[32:35], a[44:47], a[160:163], v[32:35]\n\t"
"buffer_load_dwordx4 v239, s[12:15], s22 offen offset:3200 nt lds\n\t"
"v_mfma_f32_16x16x32_bf16 v[36:39], a[40:43], a[148:151], v[36:39]\n\t"
"s_nop 0\n\t"
"ds_read_b128 a[172:175], v224 offset:22080\n\t"
"ds_read_b64_tr_b16 a[196:197], v250 offset:5760\n\t"
"v_mfma_f32_16x16x32_bf16 v[36:39], a[44:47], a[164:167], v[36:39]\n\t"
"ds_read_b64_tr_b16 a[202:203], v249 offset:3456\n\t"
"v_mfma_f32_16x16x32_bf16 v[20:23], a[32:35], a[148:151], v[20:23]\n\t"
"ds_read_b64_tr_b16 a[204:205], v251 offset:5760\n\t"
"v_mfma_f32_16x16x32_bf16 v[20:23], a[36:39], a[164:167], v[20:23]\n\t"
"s_mov_b64 exec, s[36:37]\n\t"
"buffer_load_dwordx4 v240, s[12:15], s22 offen offset:3712 nt lds\n\t"
"s_mov_b64 exec, -1\n\t"
"v_mfma_f32_16x16x32_bf16 v[4:7], a[24:27], a[148:151], v[4:7]\n\t"
"s_lshl_b32 m0, s73, 9\n\t"
"v_mfma_f32_16x16x32_bf16 v[4:7], a[28:31], a[164:167], v[4:7]\n\t"
"s_waitcnt lgkmcnt(0)\n\t"
"s_barrier\n\t"
"v_mfma_f32_16x16x32_bf16 v[8:11], a[24:27], a[152:155], v[8:11]\n\t"
"ds_read_b128 a[144:147], v224 offset:2048\n\t"
"v_add_u32 v248, 32896, v227\n\t"
"v_mfma_f32_16x16x32_bf16 v[8:11], a[28:31], a[168:171], v[8:11]\n\t"
"ds_read_b128 a[148:151], v224 offset:2560\n\t"
"v_xor_b32 v249, 32, v248\n\t"
"v_mfma_f32_16x16x32_bf16 v[24:27], a[32:35], a[152:155], v[24:27]\n\t"
"ds_read_b128 a[160:163], v224 offset:6144\n\t"
"v_xor_b32 v250, 64, v248\n\t"
"v_mfma_f32_16x16x32_bf16 v[24:27], a[36:39], a[168:171], v[24:27]\n\t"
"ds_read_b128 a[164:167], v224 offset:6656\n\t"
"v_xor_b32 v251, 96, v248\n\t"
"v_mfma_f32_16x16x32_bf16 v[40:43], a[40:43], a[152:155], v[40:43]\n\t"
"ds_read_b128 a[152:155], v224 offset:3072\n\t"
"v_mfma_f32_16x16x32_bf16 v[40:43], a[44:47], a[168:171], v[40:43]\n\t"
"ds_read_b64_tr_b16 a[182:183], v249 offset:21248\n\t"
"v_mfma_f32_16x16x32_bf16 v[44:47], a[40:43], a[156:159], v[44:47]\n\t"
"ds_read_b64_tr_b16 a[190:191], v251 offset:21248\n\t"
"v_mfma_f32_16x16x32_bf16 v[44:47], a[44:47], a[172:175], v[44:47]\n\t"
"ds_read_b64_tr_b16 a[198:199], v249 offset:22400\n\t"
"v_mfma_f32_16x16x32_bf16 v[28:31], a[32:35], a[156:159], v[28:31]\n\t"
"ds_read_b128 a[168:171], v224 offset:7168\n\t"
"v_mfma_f32_16x16x32_bf16 v[28:31], a[36:39], a[172:175], v[28:31]\n\t"
"ds_read_b64_tr_b16 a[206:207], v251 offset:22400\n\t"
"v_mfma_f32_16x16x32_bf16 v[12:15], a[24:27], a[156:159], v[12:15]\n\t"
"ds_read_b128 a[156:159], v224 offset:3584\n\t"
"v_mfma_f32_16x16x32_bf16 v[12:15], a[28:31], a[172:175], v[12:15]\n\t"
"ds_read_b128 a[172:175], v224 offset:7680\n\t"
"v_mfma_f32_16x16x32_bf16 v[192:195], a[212:215], a[176:179], 0\n\t"
"ds_read_b64_tr_b16 a[176:177], v248 offset:20736\n\t"
"v_mfma_f32_16x16x32_bf16 v[196:199], a[212:215], a[184:187], 0\n\t"
"ds_read_b64_tr_b16 a[184:185], v250 offset:20736\n\t"
"v_mfma_f32_16x16x32_bf16 v[200:203], a[212:215], a[178:181], 0\n\t"
"s_nop 0\n\t"
"ds_read_b64_tr_b16 a[178:179], v248 offset:21248\n\t"
"ds_read_b64_tr_b16 a[180:181], v249 offset:20736\n\t"
"v_mfma_f32_16x16x32_bf16 v[204:207], a[212:215], a[186:189], 0\n\t"
"s_nop 0\n\t"
"ds_read_b64_tr_b16 a[186:187], v250 offset:21248\n\t"
"ds_read_b64_tr_b16 a[188:189], v251 offset:20736\n\t"
"v_mfma_f32_16x16x32_bf16 v[208:211], a[212:215], a[192:195], 0\n\t"
"ds_read_b64_tr_b16 a[192:193], v248 offset:21888\n\t"
"v_mfma_f32_16x16x32_bf16 v[212:215], a[212:215], a[200:203], 0\n\t"
".p2align 3\n\t"
"ds_read_b64_tr_b16 a[200:201], v250 offset:21888\n\t"
"v_pk_add_f32 v[0:1], v[0:1], v[48:49]\n\t"
"v_pk_add_f32 v[2:3], v[2:3], v[50:51]\n\t"
"v_pk_fma_f32 v[48:49], v[48:49], s[38:39], v[0:1]\n\t"
"v_pk_fma_f32 v[50:51], v[50:51], s[38:39], v[2:3]\n\t"
"v_pk_add_f32 v[16:17], v[16:17], v[64:65]\n\t"
"v_pk_add_f32 v[18:19], v[18:19], v[66:67]\n\t"
"v_pk_fma_f32 v[64:65], v[64:65], s[38:39], v[16:17]\n\t"
"v_pk_fma_f32 v[66:67], v[66:67], s[38:39], v[18:19]\n\t"
"v_pk_add_f32 v[32:33], v[32:33], v[80:81]\n\t"
"v_pk_add_f32 v[34:35], v[34:35], v[82:83]\n\t"
"v_pk_fma_f32 v[80:81], v[80:81], s[38:39], v[32:33]\n\t"
"v_pk_fma_f32 v[82:83], v[82:83], s[38:39], v[34:35]\n\t"
"v_pk_add_f32 v[36:37], v[36:37], v[84:85]\n\t"
"v_pk_add_f32 v[38:39], v[38:39], v[86:87]\n\t"
"v_pk_fma_f32 v[84:85], v[84:85], s[38:39], v[36:37]\n\t"
"v_pk_fma_f32 v[86:87], v[86:87], s[38:39], v[38:39]\n\t"
"v_pk_add_f32 v[20:21], v[20:21], v[68:69]\n\t"
"v_pk_add_f32 v[22:23], v[22:23], v[70:71]\n\t"
"v_pk_fma_f32 v[68:69], v[68:69], s[38:39], v[20:21]\n\t"
"v_pk_fma_f32 v[70:71], v[70:71], s[38:39], v[22:23]\n\t"
"v_pk_add_f32 v[4:5], v[4:5], v[52:53]\n\t"
"v_pk_add_f32 v[6:7], v[6:7], v[54:55]\n\t"
"v_pk_fma_f32 v[52:53], v[52:53], s[38:39], v[4:5]\n\t"
"v_pk_fma_f32 v[54:55], v[54:55], s[38:39], v[6:7]\n\t"
"v_pk_add_f32 v[8:9], v[8:9], v[56:57]\n\t"
"v_pk_add_f32 v[10:11], v[10:11], v[58:59]\n\t"
"v_pk_fma_f32 v[56:57], v[56:57], s[38:39], v[8:9]\n\t"
"v_pk_fma_f32 v[58:59], v[58:59], s[38:39], v[10:11]\n\t"
"v_pk_add_f32 v[24:25], v[24:25], v[72:73]\n\t"
"v_pk_add_f32 v[26:27], v[26:27], v[74:75]\n\t"
"v_pk_fma_f32 v[72:73], v[72:73], s[38:39], v[24:25]\n\t"
"v_pk_fma_f32 v[74:75], v[74:75], s[38:39], v[26:27]\n\t"
"v_pk_add_f32 v[40:41], v[40:41], v[88:89]\n\t"
"v_pk_add_f32 v[42:43], v[42:43], v[90:91]\n\t"
"v_pk_fma_f32 v[88:89], v[88:89], s[38:39], v[40:41]\n\t"
"v_pk_fma_f32 v[90:91], v[90:91], s[38:39], v[42:43]\n\t"
"v_pk_add_f32 v[44:45], v[44:45], v[92:93]\n\t"
"v_pk_add_f32 v[46:47], v[46:47], v[94:95]\n\t"
"v_pk_fma_f32 v[92:93], v[92:93], s[38:39], v[44:45]\n\t"
"v_pk_fma_f32 v[94:95], v[94:95], s[38:39], v[46:47]\n\t"
"v_pk_add_f32 v[28:29], v[28:29], v[76:77]\n\t"
"v_pk_add_f32 v[30:31], v[30:31], v[78:79]\n\t"
"v_pk_fma_f32 v[76:77], v[76:77], s[38:39], v[28:29]\n\t"
"v_pk_fma_f32 v[78:79], v[78:79], s[38:39], v[30:31]\n\t"
"v_pk_add_f32 v[12:13], v[12:13], v[60:61]\n\t"
"v_pk_add_f32 v[14:15], v[14:15], v[62:63]\n\t"
"v_pk_fma_f32 v[60:61], v[60:61], s[38:39], v[12:13]\n\t"
"v_pk_fma_f32 v[62:63], v[62:63], s[38:39], v[14:15]\n\t"
"v_cvt_pk_bf16_f32 v4, v4, v5\n\t"
"v_cvt_pk_bf16_f32 v32, v32, v33\n\t"
"v_mfma_f32_16x16x32_bf16 v[216:219], a[212:215], a[194:197], 0\n\t"
"v_cvt_pk_bf16_f32 v0, v0, v1\n\t"
"v_cvt_pk_bf16_f32 v36, v36, v37\n\t"
"v_mfma_f32_16x16x32_bf16 v[220:223], a[212:215], a[202:205], 0\n\t"
"s_waitcnt vmcnt(20) lgkmcnt(10)\n\t"
"v_cvt_pk_bf16_f32 v1, v2, v3\n\t"
"v_cvt_pk_bf16_f32 v8, v8, v9\n\t"
"v_mfma_f32_16x16x32_bf16 v[144:147], a[48:51], a[144:147], v[144:147]\n\t"
"v_cvt_pk_bf16_f32 v5, v6, v7\n\t"
"v_cvt_pk_bf16_f32 v20, v20, v21\n\t"
"v_mfma_f32_16x16x32_bf16 v[144:147], a[52:55], a[160:163], v[144:147]\n\t"
"ds_write2st64_b64 v228, v[0:1], v[4:5] offset0:39 offset1:52\n\t"
"v_mfma_f32_16x16x32_bf16 v[160:163], a[56:59], a[144:147], v[160:163]\n\t"
"v_cvt_pk_bf16_f32 v194, v194, v198\n\t"
"v_cvt_pk_bf16_f32 v200, v200, v204\n\t"
"v_mfma_f32_16x16x32_bf16 v[160:163], a[60:63], a[160:163], v[160:163]\n\t"
"v_cvt_pk_bf16_f32 v210, v210, v214\n\t"
"v_cvt_pk_bf16_f32 v21, v22, v23\n\t"
"v_mfma_f32_16x16x32_bf16 v[176:179], a[64:67], a[144:147], v[176:179]\n\t"
"v_cvt_pk_bf16_f32 v16, v16, v17\n\t"
"v_cvt_pk_bf16_f32 v17, v18, v19\n\t"
"v_mfma_f32_16x16x32_bf16 v[176:179], a[68:71], a[160:163], v[176:179]\n\t"
"s_nop 0\n\t"
"ds_read_b128 a[144:147], v224 offset:0\n\t"
"ds_read_b128 a[160:163], v224 offset:4096\n\t"
"ds_write_addtid_b32 v200 offset:24672\n\t"
"ds_write_addtid_b32 v210 offset:22592\n\t"
"ds_write2st64_b64 v246, v[16:17], v[20:21] offset0:39 offset1:52\n\t"
"v_mfma_f32_16x16x32_bf16 v[180:183], a[64:67], a[148:151], v[180:183]\n\t"
"v_cvt_pk_bf16_f32 v9, v10, v11\n\t"
"v_cvt_pk_bf16_f32 v24, v24, v25\n\t"
"v_mfma_f32_16x16x32_bf16 v[180:183], a[68:71], a[164:167], v[180:183]\n\t"
"v_cvt_pk_bf16_f32 v33, v34, v35\n\t"
"v_cvt_pk_bf16_f32 v44, v44, v45\n\t"
"v_mfma_f32_16x16x32_bf16 v[164:167], a[56:59], a[148:151], v[164:167]\n\t"
"v_cvt_pk_bf16_f32 v37, v38, v39\n\t"
"v_cvt_pk_bf16_f32 v28, v28, v29\n\t"
"v_mfma_f32_16x16x32_bf16 v[164:167], a[60:63], a[164:167], v[164:167]\n\t"
"ds_write2st64_b64 v247, v[32:33], v[36:37] offset0:39 offset1:52\n\t"
"v_mfma_f32_16x16x32_bf16 v[148:151], a[48:51], a[148:151], v[148:151]\n\t"
"v_cvt_pk_bf16_f32 v195, v195, v199\n\t"
"v_cvt_pk_bf16_f32 v216, v216, v220\n\t"
"v_mfma_f32_16x16x32_bf16 v[148:151], a[52:55], a[164:167], v[148:151]\n\t"
"v_cvt_pk_bf16_f32 v12, v12, v13\n\t"
"v_cvt_pk_bf16_f32 v40, v40, v41\n\t"
"v_mfma_f32_16x16x32_bf16 v[152:155], a[48:51], a[152:155], v[152:155]\n\t"
"v_cvt_pk_bf16_f32 v202, v202, v206\n\t"
"v_cvt_pk_bf16_f32 v13, v14, v15\n\t"
"v_mfma_f32_16x16x32_bf16 v[152:155], a[52:55], a[168:171], v[152:155]\n\t"
"s_nop 0\n\t"
"ds_read_b128 a[148:151], v224 offset:512\n\t"
"ds_read_b128 a[164:167], v224 offset:4608\n\t"
"ds_write_addtid_b32 v195 offset:18752\n\t"
"ds_write_addtid_b32 v202 offset:26720\n\t"
"ds_write_addtid_b32 v216 offset:28768\n\t"
"ds_write2st64_b64 v228, v[8:9], v[12:13] offset0:65 offset1:78\n\t"
"v_mfma_f32_16x16x32_bf16 v[168:171], a[56:59], a[152:155], v[168:171]\n\t"
"v_cvt_pk_bf16_f32 v25, v26, v27\n\t"
"v_cvt_pk_bf16_f32 v29, v30, v31\n\t"
"v_mfma_f32_16x16x32_bf16 v[168:171], a[60:63], a[168:171], v[168:171]\n\t"
"buffer_load_dwordx4 a[128:131], v235, s[8:11], s52 offen offset:0 sc1\n\t"
"v_mfma_f32_16x16x32_bf16 v[184:187], a[64:67], a[152:155], v[184:187]\n\t"
"ds_write2st64_b64 v246, v[24:25], v[28:29] offset0:65 offset1:78\n\t"
"v_mfma_f32_16x16x32_bf16 v[184:187], a[68:71], a[168:171], v[184:187]\n\t"
"s_waitcnt lgkmcnt(8)\n\t"
"v_cvt_pk_bf16_f32 v211, v211, v215\n\t"
"v_cvt_pk_bf16_f32 v217, v217, v221\n\t"
"v_mfma_f32_16x16x32_bf16 v[188:191], a[64:67], a[156:159], v[188:191]\n\t"
"v_cvt_pk_bf16_f32 v41, v42, v43\n\t"
"v_cvt_pk_bf16_f32 v45, v46, v47\n\t"
"v_mfma_f32_16x16x32_bf16 v[188:191], a[68:71], a[172:175], v[188:191]\n\t"
"ds_write2st64_b64 v247, v[40:41], v[44:45] offset0:65 offset1:78\n\t"
"v_mfma_f32_16x16x32_bf16 v[172:175], a[56:59], a[156:159], v[172:175]\n\t"
"v_cvt_pk_bf16_f32 v192, v192, v196\n\t"
"v_cvt_pk_bf16_f32 v208, v208, v212\n\t"
"v_mfma_f32_16x16x32_bf16 v[172:175], a[60:63], a[172:175], v[172:175]\n\t"
"v_cvt_pk_bf16_f32 v193, v193, v197\n\t"
"v_cvt_pk_bf16_f32 v209, v209, v213\n\t"
"v_mfma_f32_16x16x32_bf16 v[156:159], a[48:51], a[156:159], v[156:159]\n\t"
"v_cvt_pk_bf16_f32 v203, v203, v207\n\t"
"v_cvt_pk_bf16_f32 v218, v218, v222\n\t"
"v_mfma_f32_16x16x32_bf16 v[156:159], a[52:55], a[172:175], v[156:159]\n\t"
"s_waitcnt vmcnt(15) lgkmcnt(2)\n\t"
"ds_write_addtid_b32 v192 offset:16448\n\t"
"ds_write_addtid_b32 v194 offset:18496\n\t"
"ds_write_addtid_b32 v208 offset:20544\n\t"
"ds_write_addtid_b32 v211 offset:22848\n\t"
"ds_write_addtid_b32 v217 offset:29024\n\t"
"v_mfma_f32_16x16x32_bf16 v[96:99], a[72:75], a[144:147], v[96:99]\n\t"
"ds_write_addtid_b32 v218 offset:30816\n\t"
"v_cvt_pk_bf16_f32 v201, v201, v205\n\t"
"v_mfma_f32_16x16x32_bf16 v[96:99], a[76:79], a[160:163], v[96:99]\n\t"
"s_nop 0\n\t"
"ds_write_addtid_b32 v201 offset:24928\n\t"
"ds_write_addtid_b32 v203 offset:26976\n\t"
"v_mfma_f32_16x16x32_bf16 v[112:115], a[80:83], a[144:147], v[112:115]\n\t"
"ds_write_addtid_b32 v193 offset:16704\n\t"
"v_cvt_pk_bf16_f32 v219, v219, v223\n\t"
"v_mfma_f32_16x16x32_bf16 v[112:115], a[84:87], a[160:163], v[112:115]\n\t"
"s_nop 0\n\t"
"ds_write_addtid_b32 v209 offset:20800\n\t"
"ds_write_addtid_b32 v219 offset:31072\n\t"
"v_mfma_f32_16x16x32_bf16 v[128:131], a[88:91], a[144:147], v[128:131]\n\t"
"ds_read_b128 a[156:159], v224 offset:1536\n\t"
"v_mfma_f32_16x16x32_bf16 v[128:131], a[92:95], a[160:163], v[128:131]\n\t"
"s_nop 0\n\t"
"ds_read_b128 a[152:155], v224 offset:1024\n\t"
"ds_read_b64_tr_b16 a[194:195], v248 offset:22400\n\t"
"v_mfma_f32_16x16x32_bf16 v[132:135], a[88:91], a[148:151], v[132:135]\n\t"
"s_nop 0\n\t"
"ds_read_b128 a[168:171], v224 offset:5120\n\t"
"ds_read_b128 a[172:175], v224 offset:5632\n\t"
"v_mfma_f32_16x16x32_bf16 v[132:135], a[92:95], a[164:167], v[132:135]\n\t"
"s_nop 0\n\t"
"ds_read_b64_tr_b16 a[196:197], v249 offset:21888\n\t"
"ds_read_b64_tr_b16 a[202:203], v250 offset:22400\n\t"
"v_mfma_f32_16x16x32_bf16 v[116:119], a[80:83], a[148:151], v[116:119]\n\t"
"ds_read_b64_tr_b16 a[204:205], v251 offset:21888\n\t"
"v_mfma_f32_16x16x32_bf16 v[116:119], a[84:87], a[164:167], v[116:119]\n\t"
"v_mfma_f32_16x16x32_bf16 v[100:103], a[72:75], a[148:151], v[100:103]\n\t"
"v_mfma_f32_16x16x32_bf16 v[100:103], a[76:79], a[164:167], v[100:103]\n\t"
"buffer_load_dwordx4 a[120:123], v252, s[8:11], s51 offen offset:2048 sc1\n\t"
"s_waitcnt lgkmcnt(3)\n\t"
"s_barrier\n\t"
"s_cmp_eq_u32 s21, 1\n\t"
"s_cbranch_scc1 Ltail_%=\n\t"
"v_mfma_f32_16x16x32_bf16 v[104:107], a[72:75], a[152:155], v[104:107]\n\t"
"s_nop 0\n\t"
"ds_read_b128 a[144:147], v224 offset:18496\n\t"
"ds_read_b128 a[148:151], v224 offset:19008\n\t"
"v_mfma_f32_16x16x32_bf16 v[104:107], a[76:79], a[168:171], v[104:107]\n\t"
"buffer_load_dwordx4 a[124:127], v252, s[8:11], s51 offen offset:3072 sc1\n\t"
"v_mfma_f32_16x16x32_bf16 v[120:123], a[80:83], a[152:155], v[120:123]\n\t"
"ds_read_b128 a[160:163], v224 offset:22592\n\t"
"v_mfma_f32_16x16x32_bf16 v[120:123], a[84:87], a[168:171], v[120:123]\n\t"
"ds_read_b128 a[164:167], v224 offset:23104\n\t"
"v_mfma_f32_16x16x32_bf16 v[136:139], a[88:91], a[152:155], v[136:139]\n\t"
"ds_read_b128 v[8:11], v229 offset:19968\n\t"
"v_mfma_f32_16x16x32_bf16 v[136:139], a[92:95], a[168:171], v[136:139]\n\t"
"buffer_load_dwordx4 a[16:19], v252, s[84:87], s69 offen offset:0 sc1\n\t"
"v_mfma_f32_16x16x32_bf16 v[140:143], a[88:91], a[156:159], v[140:143]\n\t"
"ds_read_b128 a[152:155], v224 offset:19520\n\t"
"v_mfma_f32_16x16x32_bf16 v[140:143], a[92:95], a[172:175], v[140:143]\n\t"
"ds_read_b128 a[168:171], v224 offset:23616\n\t"
"v_mfma_f32_16x16x32_bf16 v[124:127], a[80:83], a[156:159], v[124:127]\n\t"
"buffer_load_dwordx4 a[20:23], v252, s[84:87], s69 offen offset:1024 sc1\n\t"
"v_mfma_f32_16x16x32_bf16 v[124:127], a[84:87], a[172:175], v[124:127]\n\t"
"v_mfma_f32_16x16x32_bf16 v[108:111], a[72:75], a[156:159], v[108:111]\n\t"
"ds_read_b128 a[156:159], v224 offset:20032\n\t"
"v_mfma_f32_16x16x32_bf16 v[108:111], a[76:79], a[172:175], v[108:111]\n\t"
"s_waitcnt lgkmcnt(3)\n\t"
"buffer_store_dwordx4 v[8:11], v232, s[16:19], s24 offen nt\n\t"
"v_mfma_f32_16x16x32_bf16 v[192:195], a[224:227], a[176:179], 0\n\t"
"s_nop 0\n\t"
"ds_read_b128 a[172:175], v224 offset:24128\n\t"
"ds_read_b64_tr_b16 a[176:177], v248 offset:0\n\t"
"ds_read_b64_tr_b16 a[178:179], v248 offset:512\n\t"
"v_mfma_f32_16x16x32_bf16 v[196:199], a[224:227], a[180:183], 0\n\t"
"s_nop 0\n\t"
"ds_read_b64_tr_b16 a[180:181], v249 offset:0\n\t"
"ds_read_b64_tr_b16 a[182:183], v249 offset:512\n\t"
"v_mfma_f32_16x16x32_bf16 v[200:203], a[224:227], a[184:187], 0\n\t"
"ds_read_b64_tr_b16 a[184:185], v250 offset:0\n\t"
"buffer_load_dwordx4 a[12:15], v235, s[84:87], s69 offen offset:3072 sc1\n\t"
"v_mfma_f32_16x16x32_bf16 v[204:207], a[224:227], a[188:191], 0\n\t"
"s_nop 0\n\t"
"ds_read_b64_tr_b16 a[186:187], v250 offset:512\n\t"
"ds_read_b64_tr_b16 a[188:189], v251 offset:0\n\t"
"v_mfma_f32_16x16x32_bf16 v[208:211], a[224:227], a[192:195], 0\n\t"
"s_nop 0\n\t"
"ds_read_b64_tr_b16 a[190:191], v251 offset:512\n\t"
"ds_read_b128 v[24:27], v230 offset:19968\n\t"
"v_mfma_f32_16x16x32_bf16 v[212:215], a[224:227], a[196:199], 0\n\t"
"buffer_load_dwordx4 a[8:11], v235, s[84:87], s69 offen offset:2048 sc1\n\t"
"v_mfma_f32_16x16x32_bf16 v[216:219], a[224:227], a[200:203], 0\n\t"
"s_nop 0\n\t"
"ds_read_b64_tr_b16 a[192:193], v248 offset:1152\n\t"
"ds_read_b64_tr_b16 a[194:195], v248 offset:1664\n\t"
"v_mfma_f32_16x16x32_bf16 v[220:223], a[224:227], a[204:207], 0\n\t"
"s_waitcnt vmcnt(16) lgkmcnt(13)\n\t"
"ds_read_b64_tr_b16 a[196:197], v249 offset:1152\n\t"
"v_cvt_pk_bf16_f32 v193, v193, v197\n\t"
"v_mfma_f32_16x16x32_bf16 v[144:147], a[96:99], a[144:147], v[144:147]\n\t"
"ds_write_addtid_b32 v193 offset:256\n\t"
"v_cvt_pk_bf16_f32 v194, v194, v198\n\t"
"v_mfma_f32_16x16x32_bf16 v[144:147], a[100:103], a[160:163], v[144:147]\n\t"
"buffer_load_dwordx4 a[0:3], v235, s[84:87], s69 offen offset:0 sc1\n\t"
"v_mfma_f32_16x16x32_bf16 v[160:163], a[104:107], a[144:147], v[160:163]\n\t"
"ds_read_b64_tr_b16 a[198:199], v249 offset:1664\n\t"
"v_cvt_pk_bf16_f32 v201, v201, v205\n\t"
"v_mfma_f32_16x16x32_bf16 v[160:163], a[108:111], a[160:163], v[160:163]\n\t"
"ds_write_addtid_b32 v201 offset:4352\n\t"
"v_cvt_pk_bf16_f32 v210, v210, v214\n\t"
"v_mfma_f32_16x16x32_bf16 v[176:179], a[112:115], a[144:147], v[176:179]\n\t"
"ds_read_b128 v[12:15], v229 offset:33280\n\t"
"v_cvt_pk_bf16_f32 v200, v200, v204\n\t"
"v_mfma_f32_16x16x32_bf16 v[176:179], a[116:119], a[160:163], v[176:179]\n\t"
"buffer_load_dwordx4 a[4:7], v235, s[84:87], s69 offen offset:1024 sc1\n\t"
"v_mfma_f32_16x16x32_bf16 v[180:183], a[112:115], a[148:151], v[180:183]\n\t"
"ds_read_b128 a[160:163], v224 offset:20544\n\t"
"v_cvt_pk_bf16_f32 v217, v217, v221\n\t"
"v_mfma_f32_16x16x32_bf16 v[180:183], a[116:119], a[164:167], v[180:183]\n\t"
"ds_read_b128 a[144:147], v224 offset:16448\n\t"
"v_cvt_pk_bf16_f32 v209, v209, v213\n\t"
"v_mfma_f32_16x16x32_bf16 v[164:167], a[104:107], a[148:151], v[164:167]\n\t"
"s_nop 0\n\t"
"ds_write_addtid_b32 v217 offset:6400\n\t"
"ds_read_b128 v[28:31], v230 offset:33280\n\t"
"v_mfma_f32_16x16x32_bf16 v[164:167], a[108:111], a[164:167], v[164:167]\n\t"
"s_waitcnt lgkmcnt(8)\n\t"
"buffer_store_dwordx4 v[24:27], v233, s[16:19], s24 offen nt\n\t"
"v_mfma_f32_16x16x32_bf16 v[148:151], a[96:99], a[148:151], v[148:151]\n\t"
"s_nop 0\n\t"
"ds_write_addtid_b32 v194 offset:8224\n\t"
"ds_read_b64_tr_b16 a[200:201], v250 offset:1152\n\t"
"v_mfma_f32_16x16x32_bf16 v[148:151], a[100:103], a[164:167], v[148:151]\n\t"
"ds_read_b128 a[148:151], v224 offset:16960\n\t"
"v_cvt_pk_bf16_f32 v203, v203, v207\n\t"
"v_mfma_f32_16x16x32_bf16 v[152:155], a[96:99], a[152:155], v[152:155]\n\t"
"ds_read_b128 a[164:167], v224 offset:21056\n\t"
"v_cvt_pk_bf16_f32 v218, v218, v222\n\t"
"v_mfma_f32_16x16x32_bf16 v[152:155], a[100:103], a[168:171], v[152:155]\n\t"
"buffer_load_dwordx4 a[40:43], v252, s[84:87], s81 offen offset:0 sc1\n\t"
"v_mfma_f32_16x16x32_bf16 v[168:171], a[104:107], a[152:155], v[168:171]\n\t"
"ds_write_addtid_b32 v210 offset:10272\n\t"
"v_cvt_pk_bf16_f32 v202, v202, v206\n\t"
"v_mfma_f32_16x16x32_bf16 v[168:171], a[108:111], a[168:171], v[168:171]\n\t"
"buffer_load_dwordx4 a[44:47], v252, s[84:87], s81 offen offset:1024 sc1\n\t"
"v_mfma_f32_16x16x32_bf16 v[184:187], a[112:115], a[152:155], v[184:187]\n\t"
"ds_write_addtid_b32 v202 offset:12320\n\t"
"v_cvt_pk_bf16_f32 v211, v211, v215\n\t"
"v_mfma_f32_16x16x32_bf16 v[184:187], a[116:119], a[168:171], v[184:187]\n\t"
"ds_write_addtid_b32 v209 offset:2304\n\t"
"v_cvt_pk_bf16_f32 v219, v219, v223\n\t"
"v_mfma_f32_16x16x32_bf16 v[188:191], a[112:115], a[156:159], v[188:191]\n\t"
"ds_read_b128 v[40:43], v231 offset:19968\n\t"
"v_cvt_pk_bf16_f32 v208, v208, v212\n\t"
"v_mfma_f32_16x16x32_bf16 v[188:191], a[116:119], a[172:175], v[188:191]\n\t"
"buffer_load_dwordx4 a[36:39], v235, s[84:87], s81 offen offset:3072 sc1\n\t"
"v_mfma_f32_16x16x32_bf16 v[172:175], a[104:107], a[156:159], v[172:175]\n\t"
"ds_read_b128 v[44:47], v231 offset:33280\n\t"
"v_cvt_pk_bf16_f32 v216, v216, v220\n\t"
"v_mfma_f32_16x16x32_bf16 v[172:175], a[108:111], a[172:175], v[172:175]\n\t"
"ds_write_addtid_b32 v216 offset:6144\n\t"
"v_cvt_pk_bf16_f32 v195, v195, v199\n\t"
"v_mfma_f32_16x16x32_bf16 v[156:159], a[96:99], a[156:159], v[156:159]\n\t"
"s_nop 0\n\t"
"ds_write_addtid_b32 v203 offset:12576\n\t"
"ds_write_addtid_b32 v219 offset:14624\n\t"
"v_mfma_f32_16x16x32_bf16 v[156:159], a[100:103], a[172:175], v[156:159]\n\t"
"s_waitcnt vmcnt(11) lgkmcnt(8)\n\t"
"ds_write_addtid_b32 v211 offset:10528\n\t"
"ds_write_addtid_b32 v218 offset:14368\n\t"
"v_mfma_f32_16x16x32_bf16 v[96:99], a[120:123], a[144:147], v[96:99]\n\t"
"ds_read_b128 a[156:159], v224 offset:17984\n\t"
"v_cvt_pk_bf16_f32 v192, v192, v196\n\t"
"v_mfma_f32_16x16x32_bf16 v[96:99], a[124:127], a[160:163], v[96:99]\n\t"
"s_waitcnt lgkmcnt(7)\n\t"
"buffer_store_dwordx4 v[40:43], v234, s[16:19], s24 offen nt\n\t"
"v_mfma_f32_16x16x32_bf16 v[112:115], a[128:131], a[144:147], v[112:115]\n\t"
"s_nop 0\n\t"
"ds_write_addtid_b32 v195 offset:8480\n\t"
"ds_write_addtid_b32 v200 offset:4096\n\t"
"v_mfma_f32_16x16x32_bf16 v[112:115], a[132:135], a[160:163], v[112:115]\n\t"
"s_nop 0\n\t"
"ds_write_addtid_b32 v192 offset:0\n\t"
"ds_write_addtid_b32 v208 offset:2048\n\t"
"v_mfma_f32_16x16x32_bf16 v[128:131], a[136:139], a[144:147], v[128:131]\n\t"
"s_nop 0\n\t"
"ds_read_b128 a[172:175], v224 offset:22080\n\t"
"ds_read_b64_tr_b16 a[202:203], v250 offset:1664\n\t"
"v_mfma_f32_16x16x32_bf16 v[128:131], a[140:143], a[160:163], v[128:131]\n\t"
"buffer_load_dwordx4 a[32:35], v235, s[84:87], s81 offen offset:2048 sc1\n\t"
"v_mfma_f32_16x16x32_bf16 v[132:135], a[136:139], a[148:151], v[132:135]\n\t"
"s_nop 0\n\t"
"ds_read_b128 a[152:155], v224 offset:17472\n\t"
"ds_read_b128 a[168:171], v224 offset:21568\n\t"
"v_mfma_f32_16x16x32_bf16 v[132:135], a[140:143], a[164:167], v[132:135]\n\t"
"buffer_load_dwordx4 a[24:27], v235, s[84:87], s81 offen offset:0 sc1\n\t"
"v_mfma_f32_16x16x32_bf16 v[116:119], a[128:131], a[148:151], v[116:119]\n\t"
"s_nop 0\n\t"
"ds_read_b64_tr_b16 a[204:205], v251 offset:1152\n\t"
"ds_read_b64_tr_b16 a[206:207], v251 offset:1664\n\t"
"v_mfma_f32_16x16x32_bf16 v[116:119], a[132:135], a[164:167], v[116:119]\n\t"
"buffer_load_dwordx4 a[28:31], v235, s[84:87], s81 offen offset:1024 sc1\n\t"
"v_mfma_f32_16x16x32_bf16 v[100:103], a[120:123], a[148:151], v[100:103]\n\t"
"s_waitcnt lgkmcnt(2)\n\t"
"v_mfma_f32_16x16x32_bf16 v[100:103], a[124:127], a[164:167], v[100:103]\n\t"
"s_barrier\n\t"
"v_mfma_f32_16x16x32_bf16 v[104:107], a[120:123], a[152:155], v[104:107]\n\t"
"ds_read_b128 a[148:151], v224 offset:512\n\t"
"v_add_u32 v248, 101632, v227\n\t"
"v_mfma_f32_16x16x32_bf16 v[104:107], a[124:127], a[168:171], v[104:107]\n\t"
"ds_read_b128 a[144:147], v224 offset:0\n\t"
"v_mfma_f32_16x16x32_bf16 v[120:123], a[128:131], a[152:155], v[120:123]\n\t"
"ds_read_b128 a[160:163], v224 offset:4096\n\t"
"v_xor_b32 v249, 32, v248\n\t"
"v_mfma_f32_16x16x32_bf16 v[120:123], a[132:135], a[168:171], v[120:123]\n\t"
"ds_read_b128 a[164:167], v224 offset:4608\n\t"
"v_mfma_f32_16x16x32_bf16 v[136:139], a[136:139], a[152:155], v[136:139]\n\t"
"ds_read_b128 a[152:155], v224 offset:1024\n\t"
"v_mfma_f32_16x16x32_bf16 v[136:139], a[140:143], a[168:171], v[136:139]\n\t"
"buffer_store_dwordx4 v[12:15], v232, s[16:19], s27 offen nt\n\t"
"v_mfma_f32_16x16x32_bf16 v[140:143], a[136:139], a[156:159], v[140:143]\n\t"
"v_xor_b32 v250, 64, v248\n\t"
"v_mfma_f32_16x16x32_bf16 v[140:143], a[140:143], a[172:175], v[140:143]\n\t"
"v_mfma_f32_16x16x32_bf16 v[124:127], a[128:131], a[156:159], v[124:127]\n\t"
"ds_read_b128 a[168:171], v224 offset:5120\n\t"
"v_xor_b32 v251, 96, v248\n\t"
"v_mfma_f32_16x16x32_bf16 v[124:127], a[132:135], a[172:175], v[124:127]\n\t"
"buffer_load_dwordx4 a[64:67], v252, s[84:87], s54 offen offset:0 sc1\n\t"
"v_mfma_f32_16x16x32_bf16 v[108:111], a[120:123], a[156:159], v[108:111]\n\t"
"ds_read_b128 a[156:159], v224 offset:1536\n\t"
"v_mfma_f32_16x16x32_bf16 v[108:111], a[124:127], a[172:175], v[108:111]\n\t"
"buffer_load_dwordx4 a[68:71], v252, s[84:87], s54 offen offset:1024 sc1\n\t"
"v_mfma_f32_16x16x32_bf16 v[192:195], a[216:219], a[176:179], 0\n\t"
"s_nop 0\n\t"
"ds_read_b128 a[172:175], v224 offset:5632\n\t"
"ds_read_b64_tr_b16 a[176:177], v248 offset:41472\n\t"
"ds_read_b64_tr_b16 a[178:179], v248 offset:41984\n\t"
"v_mfma_f32_16x16x32_bf16 v[196:199], a[216:219], a[180:183], 0\n\t"
"buffer_load_dwordx4 a[60:63], v235, s[84:87], s54 offen offset:3072 sc1\n\t"
"v_mfma_f32_16x16x32_bf16 v[200:203], a[216:219], a[184:187], 0\n\t"
"s_nop 0\n\t"
"ds_read_b64_tr_b16 a[180:181], v249 offset:41472\n\t"
"ds_read_b64_tr_b16 a[182:183], v249 offset:41984\n\t"
"ds_read_b64_tr_b16 a[184:185], v250 offset:41472\n\t"
"ds_read_b64_tr_b16 a[186:187], v250 offset:41984\n\t"
"v_mfma_f32_16x16x32_bf16 v[204:207], a[216:219], a[188:191], 0\n\t"
"ds_read_b64_tr_b16 a[188:189], v251 offset:41472\n\t"
"buffer_load_dwordx4 a[56:59], v235, s[84:87], s54 offen offset:2048 sc1\n\t"
"v_mfma_f32_16x16x32_bf16 v[208:211], a[216:219], a[192:195], 0\n\t"
"s_nop 0\n\t"
"ds_read_b64_tr_b16 a[190:191], v251 offset:41984\n\t"
"ds_read_b64_tr_b16 a[192:193], v248 offset:42624\n\t"
"ds_read_b64_tr_b16 a[194:195], v248 offset:43136\n\t"
"v_mfma_f32_16x16x32_bf16 v[212:215], a[216:219], a[196:199], 0\n\t"
".p2align 3\n\t"
"ds_read_b64_tr_b16 a[196:197], v249 offset:42624\n\t"
"buffer_load_dwordx4 a[48:51], v235, s[84:87], s54 offen offset:0 sc1\n\t"
"v_pk_add_f32 v[96:97], v[96:97], v[144:145]\n\t"
"v_pk_add_f32 v[98:99], v[98:99], v[146:147]\n\t"
"v_pk_fma_f32 v[144:145], v[144:145], s[38:39], v[96:97]\n\t"
"v_pk_fma_f32 v[146:147], v[146:147], s[38:39], v[98:99]\n\t"
"v_pk_add_f32 v[112:113], v[112:113], v[160:161]\n\t"
"v_pk_add_f32 v[114:115], v[114:115], v[162:163]\n\t"
"v_pk_fma_f32 v[160:161], v[160:161], s[38:39], v[112:113]\n\t"
"v_pk_fma_f32 v[162:163], v[162:163], s[38:39], v[114:115]\n\t"
"v_pk_add_f32 v[128:129], v[128:129], v[176:177]\n\t"
"v_pk_add_f32 v[130:131], v[130:131], v[178:179]\n\t"
"v_pk_fma_f32 v[176:177], v[176:177], s[38:39], v[128:129]\n\t"
"v_pk_fma_f32 v[178:179], v[178:179], s[38:39], v[130:131]\n\t"
"buffer_load_dwordx4 a[52:55], v235, s[84:87], s54 offen offset:1024 sc1\n\t"
"v_pk_add_f32 v[132:133], v[132:133], v[180:181]\n\t"
"v_pk_add_f32 v[134:135], v[134:135], v[182:183]\n\t"
"v_pk_fma_f32 v[180:181], v[180:181], s[38:39], v[132:133]\n\t"
"v_pk_fma_f32 v[182:183], v[182:183], s[38:39], v[134:135]\n\t"
"v_pk_add_f32 v[116:117], v[116:117], v[164:165]\n\t"
"v_pk_add_f32 v[118:119], v[118:119], v[166:167]\n\t"
"v_pk_fma_f32 v[164:165], v[164:165], s[38:39], v[116:117]\n\t"
"v_pk_fma_f32 v[166:167], v[166:167], s[38:39], v[118:119]\n\t"
"v_pk_add_f32 v[100:101], v[100:101], v[148:149]\n\t"
"v_pk_add_f32 v[102:103], v[102:103], v[150:151]\n\t"
"v_pk_fma_f32 v[148:149], v[148:149], s[38:39], v[100:101]\n\t"
"v_pk_fma_f32 v[150:151], v[150:151], s[38:39], v[102:103]\n\t"
"buffer_load_dwordx4 a[88:91], v252, s[84:87], s66 offen offset:0 sc1\n\t"
"v_pk_add_f32 v[104:105], v[104:105], v[152:153]\n\t"
"v_pk_add_f32 v[106:107], v[106:107], v[154:155]\n\t"
"v_pk_fma_f32 v[152:153], v[152:153], s[38:39], v[104:105]\n\t"
"v_pk_fma_f32 v[154:155], v[154:155], s[38:39], v[106:107]\n\t"
"v_pk_add_f32 v[120:121], v[120:121], v[168:169]\n\t"
"v_pk_add_f32 v[122:123], v[122:123], v[170:171]\n\t"
"v_pk_fma_f32 v[168:169], v[168:169], s[38:39], v[120:121]\n\t"
"v_pk_fma_f32 v[170:171], v[170:171], s[38:39], v[122:123]\n\t"
"v_pk_add_f32 v[136:137], v[136:137], v[184:185]\n\t"
"v_pk_add_f32 v[138:139], v[138:139], v[186:187]\n\t"
"v_pk_fma_f32 v[184:185], v[184:185], s[38:39], v[136:137]\n\t"
"v_pk_fma_f32 v[186:187], v[186:187], s[38:39], v[138:139]\n\t"
"buffer_load_dwordx4 a[92:95], v252, s[84:87], s66 offen offset:1024 sc1\n\t"
"v_pk_add_f32 v[140:141], v[140:141], v[188:189]\n\t"
"v_pk_add_f32 v[142:143], v[142:143], v[190:191]\n\t"
"v_pk_fma_f32 v[188:189], v[188:189], s[38:39], v[140:141]\n\t"
"v_pk_fma_f32 v[190:191], v[190:191], s[38:39], v[142:143]\n\t"
"v_pk_add_f32 v[124:125], v[124:125], v[172:173]\n\t"
"v_pk_add_f32 v[126:127], v[126:127], v[174:175]\n\t"
"v_pk_fma_f32 v[172:173], v[172:173], s[38:39], v[124:125]\n\t"
"v_pk_fma_f32 v[174:175], v[174:175], s[38:39], v[126:127]\n\t"
"v_pk_add_f32 v[108:109], v[108:109], v[156:157]\n\t"
"v_pk_add_f32 v[110:111], v[110:111], v[158:159]\n\t"
"v_pk_fma_f32 v[156:157], v[156:157], s[38:39], v[108:109]\n\t"
"v_pk_fma_f32 v[158:159], v[158:159], s[38:39], v[110:111]\n\t"
"v_cvt_pk_bf16_f32 v96, v96, v97\n\t"
"v_cvt_pk_bf16_f32 v100, v100, v101\n\t"
"v_cvt_pk_bf16_f32 v116, v116, v117\n\t"
"v_cvt_pk_bf16_f32 v117, v118, v119\n\t"
"v_cvt_pk_bf16_f32 v124, v124, v125\n\t"
"v_cvt_pk_bf16_f32 v136, v136, v137\n\t"
"v_mfma_f32_16x16x32_bf16 v[216:219], a[216:219], a[200:203], 0\n\t"
"s_waitcnt lgkmcnt(13)\n\t"
"v_cvt_pk_bf16_f32 v101, v102, v103\n\t"
"v_cvt_pk_bf16_f32 v128, v128, v129\n\t"
"v_mfma_f32_16x16x32_bf16 v[220:223], a[216:219], a[204:207], 0\n\t"
"s_waitcnt vmcnt(17)\n\t"
"ds_read_b64_tr_b16 a[198:199], v249 offset:43136\n\t"
"v_cvt_pk_bf16_f32 v193, v193, v197\n\t"
"v_cvt_pk_bf16_f32 v112, v112, v113\n\t"
"v_mfma_f32_16x16x32_bf16 v[48:51], a[0:3], a[144:147], v[48:51]\n\t"
"v_cvt_pk_bf16_f32 v97, v98, v99\n\t"
"v_cvt_pk_bf16_f32 v132, v132, v133\n\t"
"v_mfma_f32_16x16x32_bf16 v[48:51], a[4:7], a[160:163], v[48:51]\n\t"
"ds_write_addtid_b32 v193 offset:16704\n\t"
"buffer_load_dwordx4 a[84:87], v235, s[84:87], s66 offen offset:3072 sc1\n\t"
"ds_write2st64_b64 v228, v[96:97], v[100:101] offset0:39 offset1:52\n\t"
"v_mfma_f32_16x16x32_bf16 v[64:67], a[8:11], a[144:147], v[64:67]\n\t"
"v_cvt_pk_bf16_f32 v113, v114, v115\n\t"
"v_cvt_pk_bf16_f32 v129, v130, v131\n\t"
"v_mfma_f32_16x16x32_bf16 v[64:67], a[12:15], a[160:163], v[64:67]\n\t"
"v_cvt_pk_bf16_f32 v210, v210, v214\n\t"
"v_cvt_pk_bf16_f32 v140, v140, v141\n\t"
"v_mfma_f32_16x16x32_bf16 v[80:83], a[16:19], a[144:147], v[80:83]\n\t"
"v_cvt_pk_bf16_f32 v202, v202, v206\n\t"
"v_cvt_pk_bf16_f32 v108, v108, v109\n\t"
"v_mfma_f32_16x16x32_bf16 v[80:83], a[20:23], a[160:163], v[80:83]\n\t"
"s_nop 0\n\t"
"ds_read_b128 a[144:147], v224 offset:2048\n\t"
"ds_read_b128 a[160:163], v224 offset:6144\n\t"
"ds_write_addtid_b32 v202 offset:28768\n\t"
"ds_write_addtid_b32 v210 offset:26720\n\t"
"ds_read_b64_tr_b16 a[200:201], v250 offset:42624\n\t"
"buffer_store_dwordx4 v[28:31], v233, s[16:19], s27 offen nt\n\t"
"ds_write2st64_b64 v246, v[112:113], v[116:117] offset0:39 offset1:52\n\t"
"v_mfma_f32_16x16x32_bf16 v[84:87], a[16:19], a[148:151], v[84:87]\n\t"
"v_cvt_pk_bf16_f32 v133, v134, v135\n\t"
"v_cvt_pk_bf16_f32 v125, v126, v127\n\t"
"v_mfma_f32_16x16x32_bf16 v[84:87], a[20:23], a[164:167], v[84:87]\n\t"
"v_cvt_pk_bf16_f32 v200, v200, v204\n\t"
"v_cvt_pk_bf16_f32 v201, v201, v205\n\t"
"v_mfma_f32_16x16x32_bf16 v[68:71], a[8:11], a[148:151], v[68:71]\n\t"
"v_cvt_pk_bf16_f32 v217, v217, v221\n\t"
"v_cvt_pk_bf16_f32 v104, v104, v105\n\t"
"v_mfma_f32_16x16x32_bf16 v[68:71], a[12:15], a[164:167], v[68:71]\n\t"
"ds_write_addtid_b32 v200 offset:20544\n\t"
"buffer_load_dwordx4 a[80:83], v235, s[84:87], s66 offen offset:2048 sc1\n\t"
"ds_write2st64_b64 v247, v[128:129], v[132:133] offset0:39 offset1:52\n\t"
"v_mfma_f32_16x16x32_bf16 v[52:55], a[0:3], a[148:151], v[52:55]\n\t"
"v_cvt_pk_bf16_f32 v105, v106, v107\n\t"
"v_cvt_pk_bf16_f32 v109, v110, v111\n\t"
"v_mfma_f32_16x16x32_bf16 v[52:55], a[4:7], a[164:167], v[52:55]\n\t"
"buffer_load_dwordx4 a[72:75], v235, s[84:87], s66 offen offset:0 sc1\n\t"
"v_mfma_f32_16x16x32_bf16 v[56:59], a[0:3], a[152:155], v[56:59]\n\t"
"v_cvt_pk_bf16_f32 v209, v209, v213\n\t"
"v_cvt_pk_bf16_f32 v120, v120, v121\n\t"
"v_mfma_f32_16x16x32_bf16 v[56:59], a[4:7], a[168:171], v[56:59]\n\t"
"s_nop 0\n\t"
"ds_read_b128 a[148:151], v224 offset:2560\n\t"
"ds_read_b128 a[164:167], v224 offset:6656\n\t"
"ds_write_addtid_b32 v201 offset:20800\n\t"
"ds_write_addtid_b32 v209 offset:18752\n\t"
"ds_write2st64_b64 v228, v[104:105], v[108:109] offset0:65 offset1:78\n\t"
"v_mfma_f32_16x16x32_bf16 v[72:75], a[8:11], a[152:155], v[72:75]\n\t"
"v_cvt_pk_bf16_f32 v194, v194, v198\n\t"
"v_cvt_pk_bf16_f32 v137, v138, v139\n\t"
"v_mfma_f32_16x16x32_bf16 v[72:75], a[12:15], a[168:171], v[72:75]\n\t"
"buffer_load_dwordx4 a[76:79], v235, s[84:87], s66 offen offset:1024 sc1\n\t"
"v_mfma_f32_16x16x32_bf16 v[88:91], a[16:19], a[152:155], v[88:91]\n\t"
"v_cvt_pk_bf16_f32 v219, v219, v223\n\t"
"v_cvt_pk_bf16_f32 v121, v122, v123\n\t"
"v_mfma_f32_16x16x32_bf16 v[88:91], a[20:23], a[168:171], v[88:91]\n\t"
"s_waitcnt lgkmcnt(7)\n\t"
"ds_write2st64_b64 v246, v[120:121], v[124:125] offset0:65 offset1:78\n\t"
"v_mfma_f32_16x16x32_bf16 v[92:95], a[16:19], a[156:159], v[92:95]\n\t"
"v_cvt_pk_bf16_f32 v195, v195, v199\n\t"
"v_cvt_pk_bf16_f32 v211, v211, v215\n\t"
"v_mfma_f32_16x16x32_bf16 v[92:95], a[20:23], a[172:175], v[92:95]\n\t"
"buffer_load_dwordx4 a[112:115], v252, s[84:87], 0 offen offset:0 sc1\n\t"
"v_mfma_f32_16x16x32_bf16 v[76:79], a[8:11], a[156:159], v[76:79]\n\t"
"v_cvt_pk_bf16_f32 v218, v218, v222\n\t"
"v_cvt_pk_bf16_f32 v141, v142, v143\n\t"
"v_mfma_f32_16x16x32_bf16 v[76:79], a[12:15], a[172:175], v[76:79]\n\t"
"ds_write_addtid_b32 v194 offset:24672\n\t"
"ds_write2st64_b64 v247, v[136:137], v[140:141] offset0:65 offset1:78\n\t"
"v_mfma_f32_16x16x32_bf16 v[60:63], a[0:3], a[156:159], v[60:63]\n\t"
"v_cvt_pk_bf16_f32 v203, v203, v207\n\t"
"v_cvt_pk_bf16_f32 v208, v208, v212\n\t"
"v_mfma_f32_16x16x32_bf16 v[60:63], a[4:7], a[172:175], v[60:63]\n\t"
"s_waitcnt vmcnt(15) lgkmcnt(3)\n\t"
"buffer_store_dwordx4 v[44:47], v234, s[16:19], s27 offen nt\n\t"
"v_mfma_f32_16x16x32_bf16 v[144:147], a[24:27], a[144:147], v[144:147]\n\t"
"v_cvt_pk_bf16_f32 v192, v192, v196\n\t"
"v_cvt_pk_bf16_f32 v216, v216, v220\n\t"
"v_mfma_f32_16x16x32_bf16 v[144:147], a[28:31], a[160:163], v[144:147]\n\t"
"s_nop 0\n\t"
"ds_write_addtid_b32 v192 offset:16448\n\t"
"ds_write_addtid_b32 v195 offset:24928\n\t"
"ds_write_addtid_b32 v208 offset:18496\n\t"
"ds_write_addtid_b32 v211 offset:26976\n\t"
"ds_write_addtid_b32 v216 offset:22592\n\t"
"ds_write_addtid_b32 v218 offset:30816\n\t"
"v_mfma_f32_16x16x32_bf16 v[160:163], a[32:35], a[144:147], v[160:163]\n\t"
"s_nop 0\n\t"
"ds_read_b128 a[172:175], v224 offset:7680\n\t"
"ds_write_addtid_b32 v217 offset:22848\n\t"
"v_mfma_f32_16x16x32_bf16 v[160:163], a[36:39], a[160:163], v[160:163]\n\t"
"s_nop 0\n\t"
"ds_write_addtid_b32 v203 offset:29024\n\t"
"ds_write_addtid_b32 v219 offset:31072\n\t"
"v_mfma_f32_16x16x32_bf16 v[176:179], a[40:43], a[144:147], v[176:179]\n\t"
"s_nop 0\n\t"
"ds_read_b128 a[152:155], v224 offset:3072\n\t"
"ds_read_b64_tr_b16 a[202:203], v250 offset:43136\n\t"
"v_mfma_f32_16x16x32_bf16 v[176:179], a[44:47], a[160:163], v[176:179]\n\t"
"buffer_load_dwordx4 a[116:119], v252, s[84:87], 0 offen offset:1024 sc1\n\t"
"v_mfma_f32_16x16x32_bf16 v[180:183], a[40:43], a[148:151], v[180:183]\n\t"
"s_nop 0\n\t"
"ds_read_b128 a[168:171], v224 offset:7168\n\t"
"ds_read_b128 a[156:159], v224 offset:3584\n\t"
"v_mfma_f32_16x16x32_bf16 v[180:183], a[44:47], a[164:167], v[180:183]\n\t"
"buffer_load_dwordx4 a[108:111], v235, s[84:87], 0 offen offset:3072 sc1\n\t"
"v_mfma_f32_16x16x32_bf16 v[164:167], a[32:35], a[148:151], v[164:167]\n\t"
"s_nop 0\n\t"
"ds_read_b64_tr_b16 a[204:205], v251 offset:42624\n\t"
"ds_read_b64_tr_b16 a[206:207], v251 offset:43136\n\t"
"v_mfma_f32_16x16x32_bf16 v[164:167], a[36:39], a[164:167], v[164:167]\n\t"
"buffer_load_dwordx4 a[104:107], v235, s[84:87], 0 offen offset:2048 sc1\n\t"
"v_mfma_f32_16x16x32_bf16 v[148:151], a[24:27], a[148:151], v[148:151]\n\t"
"buffer_load_dwordx4 a[96:99], v235, s[84:87], 0 offen offset:0 sc1\n\t"
"v_mfma_f32_16x16x32_bf16 v[148:151], a[28:31], a[164:167], v[148:151]\n\t"
"s_waitcnt lgkmcnt(2)\n\t"
"s_barrier\n\t"
"v_mfma_f32_16x16x32_bf16 v[152:155], a[24:27], a[152:155], v[152:155]\n\t"
"s_nop 0\n\t"
"ds_read_b128 a[144:147], v224 offset:16448\n\t"
"ds_read_b128 a[148:151], v224 offset:16960\n\t"
"v_mfma_f32_16x16x32_bf16 v[152:155], a[28:31], a[168:171], v[152:155]\n\t"
"buffer_load_dwordx4 a[100:103], v235, s[84:87], 0 offen offset:1024 sc1\n\t"
"v_mfma_f32_16x16x32_bf16 v[168:171], a[32:35], a[152:155], v[168:171]\n\t"
"s_nop 0\n\t"
"ds_read_b128 a[160:163], v224 offset:20544\n\t"
"ds_read_b128 a[164:167], v224 offset:21056\n\t"
"v_mfma_f32_16x16x32_bf16 v[168:171], a[36:39], a[168:171], v[168:171]\n\t"
"buffer_load_dwordx4 a[136:139], v252, s[84:87], s51 offen offset:0 sc1\n\t"
"v_mfma_f32_16x16x32_bf16 v[184:187], a[40:43], a[152:155], v[184:187]\n\t"
"s_nop 0\n\t"
"ds_read_b128 v[120:123], v230 offset:19968\n\t"
"ds_read_b128 v[136:139], v231 offset:19968\n\t"
"v_mfma_f32_16x16x32_bf16 v[184:187], a[44:47], a[168:171], v[184:187]\n\t"
"s_nop 0\n\t"
"ds_read_b128 a[168:171], v224 offset:21568\n\t"
"ds_read_b128 v[104:107], v229 offset:19968\n\t"
"v_mfma_f32_16x16x32_bf16 v[188:191], a[40:43], a[156:159], v[188:191]\n\t"
"s_nop 0\n\t"
"ds_read_b128 a[152:155], v224 offset:17472\n\t"
"ds_read_b128 v[108:111], v229 offset:33280\n\t"
"v_mfma_f32_16x16x32_bf16 v[188:191], a[44:47], a[172:175], v[188:191]\n\t"
"buffer_load_dwordx4 a[140:143], v252, s[84:87], s51 offen offset:1024 sc1\n\t"
"v_mfma_f32_16x16x32_bf16 v[172:175], a[32:35], a[156:159], v[172:175]\n\t"
"ds_read_b128 a[232:235], v230 offset:33280\n\t"
"v_mfma_f32_16x16x32_bf16 v[172:175], a[36:39], a[172:175], v[172:175]\n\t"
"ds_read_b128 a[236:239], v231 offset:33280\n\t"
"v_mfma_f32_16x16x32_bf16 v[156:159], a[24:27], a[156:159], v[156:159]\n\t"
"ds_read_b128 a[156:159], v224 offset:17984\n\t"
"v_mfma_f32_16x16x32_bf16 v[156:159], a[28:31], a[172:175], v[156:159]\n\t"
"s_waitcnt lgkmcnt(5)\n\t"
"buffer_store_dwordx4 v[104:107], v232, s[16:19], s20 offen nt\n\t"
"v_mfma_f32_16x16x32_bf16 v[192:195], a[224:227], a[176:179], 0\n\t"
"s_nop 0\n\t"
"ds_read_b64_tr_b16 a[176:177], v248 offset:20736\n\t"
"ds_read_b64_tr_b16 a[178:179], v248 offset:21248\n\t"
"v_mfma_f32_16x16x32_bf16 v[196:199], a[224:227], a[180:183], 0\n\t"
"s_nop 0\n\t"
"ds_read_b128 a[172:175], v224 offset:22080\n\t"
"ds_read_b64_tr_b16 a[180:181], v249 offset:20736\n\t"
"v_mfma_f32_16x16x32_bf16 v[200:203], a[224:227], a[184:187], 0\n\t"
"buffer_load_dwordx4 a[132:135], v235, s[84:87], s51 offen offset:3072 sc1\n\t"
"v_mfma_f32_16x16x32_bf16 v[204:207], a[224:227], a[188:191], 0\n\t"
"s_waitcnt lgkmcnt(5)\n\t"
"s_barrier\n\t"
"v_mfma_f32_16x16x32_bf16 v[208:211], a[224:227], a[192:195], 0\n\t"
"buffer_load_dwordx4 a[128:131], v235, s[84:87], s51 offen offset:2048 sc1\n\t"
"v_mfma_f32_16x16x32_bf16 v[212:215], a[224:227], a[196:199], 0\n\t"
"s_nop 0\n\t"
"ds_read_b64_tr_b16 a[182:183], v249 offset:21248\n\t"
"ds_read_b64_tr_b16 a[184:185], v250 offset:20736\n\t"
"v_mfma_f32_16x16x32_bf16 v[216:219], a[224:227], a[200:203], 0\n\t"
"s_nop 0\n\t"
"ds_read_b64_tr_b16 a[186:187], v250 offset:21248\n\t"
"ds_read_b64_tr_b16 a[188:189], v251 offset:20736\n\t"
"v_mfma_f32_16x16x32_bf16 v[220:223], a[224:227], a[204:207], 0\n\t"
"ds_read_b64_tr_b16 a[190:191], v251 offset:21248\n\t"
"v_cvt_pk_bf16_f32 v194, v194, v198\n\t"
"v_mfma_f32_16x16x32_bf16 v[0:3], a[0:3], a[144:147], 0\n\t"
"ds_read_b64_tr_b16 a[192:193], v248 offset:21888\n\t"
"v_cvt_pk_bf16_f32 v195, v195, v199\n\t"
"v_mfma_f32_16x16x32_bf16 v[0:3], a[4:7], a[160:163], v[0:3]\n\t"
"buffer_store_dwordx4 v[120:123], v233, s[16:19], s20 offen nt\n\t"
"v_mfma_f32_16x16x32_bf16 v[16:19], a[8:11], a[144:147], 0\n\t"
"ds_write_addtid_b32 v195 offset:8480\n\t"
"v_cvt_pk_bf16_f32 v192, v192, v196\n\t"
"v_mfma_f32_16x16x32_bf16 v[16:19], a[12:15], a[160:163], v[16:19]\n\t"
"ds_read_b64_tr_b16 a[194:195], v248 offset:22400\n\t"
"v_cvt_pk_bf16_f32 v209, v209, v213\n\t"
"v_mfma_f32_16x16x32_bf16 v[32:35], a[16:19], a[144:147], 0\n\t"
"ds_read_b128 a[144:147], v224 offset:18496\n\t"
"v_cvt_pk_bf16_f32 v201, v201, v205\n\t"
"v_mfma_f32_16x16x32_bf16 v[32:35], a[20:23], a[160:163], v[32:35]\n\t"
"buffer_load_dwordx4 a[120:123], v235, s[84:87], s51 offen offset:0 sc1\n\t"
"v_mfma_f32_16x16x32_bf16 v[36:39], a[16:19], a[148:151], 0\n\t"
"ds_read_b128 a[160:163], v224 offset:22592\n\t"
"v_cvt_pk_bf16_f32 v208, v208, v212\n\t"
"v_mfma_f32_16x16x32_bf16 v[36:39], a[20:23], a[164:167], v[36:39]\n\t"
"buffer_load_dwordx4 a[124:127], v235, s[84:87], s51 offen offset:1024 sc1\n\t"
"v_mfma_f32_16x16x32_bf16 v[20:23], a[8:11], a[148:151], 0\n\t"
"ds_write_addtid_b32 v208 offset:2048\n\t"
"v_cvt_pk_bf16_f32 v203, v203, v207\n\t"
"v_mfma_f32_16x16x32_bf16 v[20:23], a[12:15], a[164:167], v[20:23]\n\t"
"ds_write_addtid_b32 v192 offset:0\n\t"
"v_cvt_pk_bf16_f32 v217, v217, v221\n\t"
"v_mfma_f32_16x16x32_bf16 v[4:7], a[0:3], a[148:151], 0\n\t"
"ds_read_b128 a[148:151], v224 offset:19008\n\t"
"v_cvt_pk_bf16_f32 v200, v200, v204\n\t"
"v_mfma_f32_16x16x32_bf16 v[4:7], a[4:7], a[164:167], v[4:7]\n\t"
"buffer_store_dwordx4 v[136:139], v234, s[16:19], s20 offen nt\n\t"
"v_mfma_f32_16x16x32_bf16 v[8:11], a[0:3], a[152:155], 0\n\t"
"s_nop 0\n\t"
"ds_write_addtid_b32 v203 offset:12576\n\t"
"ds_write_addtid_b32 v209 offset:2304\n\t"
"v_mfma_f32_16x16x32_bf16 v[8:11], a[4:7], a[168:171], v[8:11]\n\t"
"ds_read_b128 a[164:167], v224 offset:23104\n\t"
"v_cvt_pk_bf16_f32 v219, v219, v223\n\t"
"v_mfma_f32_16x16x32_bf16 v[24:27], a[8:11], a[152:155], 0\n\t"
"s_nop 0\n\t"
"ds_write_addtid_b32 v194 offset:8224\n\t"
"ds_read_b64_tr_b16 a[196:197], v249 offset:21888\n\t"
"s_add_u32 m0, s26, 83200\n\t"
"v_mfma_f32_16x16x32_bf16 v[24:27], a[12:15], a[168:171], v[24:27]\n\t"
"buffer_load_dwordx4 v236, s[12:15], s22 offen offset:0 nt lds\n\t"
"s_lshl_b32 m0, s73, 9\n\t"
"v_mfma_f32_16x16x32_bf16 v[40:43], a[16:19], a[152:155], 0\n\t"
"ds_write_addtid_b32 v217 offset:6400\n\t"
"v_cvt_pk_bf16_f32 v216, v216, v220\n\t"
"v_mfma_f32_16x16x32_bf16 v[40:43], a[20:23], a[168:171], v[40:43]\n\t"
"s_waitcnt lgkmcnt(9)\n\t"
"ds_read_b64_tr_b16 a[198:199], v249 offset:22400\n\t"
"v_cvt_pk_bf16_f32 v193, v193, v197\n\t"
"v_mfma_f32_16x16x32_bf16 v[44:47], a[16:19], a[156:159], 0\n\t"
"ds_write_addtid_b32 v193 offset:256\n\t"
"v_cvt_pk_bf16_f32 v202, v202, v206\n\t"
"s_add_u32 m0, s26, 83200\n\t"
"v_mfma_f32_16x16x32_bf16 v[44:47], a[20:23], a[172:175], v[44:47]\n\t"
"buffer_load_dwordx4 v237, s[12:15], s22 offen offset:1024 nt lds\n\t"
"s_lshl_b32 m0, s73, 9\n\t"
"v_mfma_f32_16x16x32_bf16 v[28:31], a[8:11], a[156:159], 0\n\t"
"s_nop 0\n\t"
"ds_read_b128 a[168:171], v224 offset:23616\n\t"
"ds_write_addtid_b32 v216 offset:6144\n\t"
"v_mfma_f32_16x16x32_bf16 v[28:31], a[12:15], a[172:175], v[28:31]\n\t"
"ds_write_addtid_b32 v202 offset:12320\n\t"
"v_cvt_pk_bf16_f32 v211, v211, v215\n\t"
"v_mfma_f32_16x16x32_bf16 v[12:15], a[0:3], a[156:159], 0\n\t"
"ds_write_addtid_b32 v200 offset:4096\n\t"
"v_cvt_pk_bf16_f32 v210, v210, v214\n\t"
"s_add_u32 m0, s26, 83200\n\t"
"v_mfma_f32_16x16x32_bf16 v[12:15], a[4:7], a[172:175], v[12:15]\n\t"
"buffer_load_dwordx4 v238, s[12:15], s22 offen offset:2048 nt lds\n\t"
"s_lshl_b32 m0, s73, 9\n\t"
"v_mfma_f32_16x16x32_bf16 v[96:99], a[24:27], a[144:147], 0\n\t"
"ds_write_addtid_b32 v201 offset:4352\n\t"
"v_cvt_pk_bf16_f32 v218, v218, v222\n\t"
"v_mfma_f32_16x16x32_bf16 v[96:99], a[28:31], a[160:163], v[96:99]\n\t"
"s_nop 0\n\t"
"ds_write_addtid_b32 v210 offset:10272\n\t"
"ds_write_addtid_b32 v219 offset:14624\n\t"
"v_mfma_f32_16x16x32_bf16 v[112:115], a[32:35], a[144:147], 0\n\t"
"s_nop 0\n\t"
"ds_write_addtid_b32 v211 offset:10528\n\t"
"ds_write_addtid_b32 v218 offset:14368\n\t"
"s_add_u32 m0, s26, 83200\n\t"
"v_mfma_f32_16x16x32_bf16 v[112:115], a[36:39], a[160:163], v[112:115]\n\t"
"buffer_load_dwordx4 v239, s[12:15], s22 offen offset:3072 nt lds\n\t"
"v_mfma_f32_16x16x32_bf16 v[128:131], a[40:43], a[144:147], 0\n\t"
"s_nop 0\n\t"
"ds_read_b128 a[156:159], v224 offset:20032\n\t"
"ds_read_b64_tr_b16 a[200:201], v250 offset:21888\n\t"
"v_mfma_f32_16x16x32_bf16 v[128:131], a[44:47], a[160:163], v[128:131]\n\t"
"s_waitcnt lgkmcnt(11)\n\t"
"ds_read_b128 a[152:155], v224 offset:19520\n\t"
"ds_read_b128 a[172:175], v224 offset:24128\n\t"
"v_mfma_f32_16x16x32_bf16 v[132:135], a[40:43], a[148:151], 0\n\t"
"s_mov_b64 exec, s[36:37]\n\t"
"buffer_load_dwordx4 v240, s[12:15], s22 offen offset:3584 nt lds\n\t"
"s_mov_b64 exec, -1\n\t"
"v_mfma_f32_16x16x32_bf16 v[132:135], a[44:47], a[164:167], v[132:135]\n\t"
"s_lshl_b32 m0, s73, 9\n\t"
"ds_read_b64_tr_b16 a[202:203], v250 offset:22400\n\t"
"ds_read_b64_tr_b16 a[204:205], v251 offset:21888\n\t"
"v_mfma_f32_16x16x32_bf16 v[116:119], a[32:35], a[148:151], 0\n\t"
"ds_read_b64_tr_b16 a[206:207], v251 offset:22400\n\t"
"v_mfma_f32_16x16x32_bf16 v[116:119], a[36:39], a[164:167], v[116:119]\n\t"
"buffer_store_dwordx4 v[108:111], v232, s[16:19], s25 offen nt\n\t"
"v_mfma_f32_16x16x32_bf16 v[100:103], a[24:27], a[148:151], 0\n\t"
"s_waitcnt lgkmcnt(3)\n\t"
"v_mfma_f32_16x16x32_bf16 v[100:103], a[28:31], a[164:167], v[100:103]\n\t"
"s_barrier\n\t"
"v_mfma_f32_16x16x32_bf16 v[104:107], a[24:27], a[152:155], 0\n\t"
"ds_read_b128 a[144:147], v224 offset:0\n\t"
"v_mfma_f32_16x16x32_bf16 v[104:107], a[28:31], a[168:171], v[104:107]\n\t"
"ds_read_b128 a[148:151], v224 offset:512\n\t"
"v_mfma_f32_16x16x32_bf16 v[120:123], a[32:35], a[152:155], 0\n\t"
"ds_read_b128 a[160:163], v224 offset:4096\n\t"
"v_mfma_f32_16x16x32_bf16 v[120:123], a[36:39], a[168:171], v[120:123]\n\t"
"ds_read_b128 a[164:167], v224 offset:4608\n\t"
"v_mfma_f32_16x16x32_bf16 v[136:139], a[40:43], a[152:155], 0\n\t"
"ds_read_b128 a[152:155], v224 offset:1024\n\t"
"v_mfma_f32_16x16x32_bf16 v[136:139], a[44:47], a[168:171], v[136:139]\n\t"
"ds_read_b128 a[228:231], v255 offset:20736\n\t"
"v_mfma_f32_16x16x32_bf16 v[140:143], a[40:43], a[156:159], 0\n\t"
"ds_read_b128 a[168:171], v224 offset:5120\n\t"
"v_mfma_f32_16x16x32_bf16 v[140:143], a[44:47], a[172:175], v[140:143]\n\t"
"v_mfma_f32_16x16x32_bf16 v[124:127], a[32:35], a[156:159], 0\n\t"
"v_mfma_f32_16x16x32_bf16 v[124:127], a[36:39], a[172:175], v[124:127]\n\t"
"v_mfma_f32_16x16x32_bf16 v[108:111], a[24:27], a[156:159], 0\n\t"
"ds_read_b128 a[156:159], v224 offset:1536\n\t"
"v_mfma_f32_16x16x32_bf16 v[108:111], a[28:31], a[172:175], v[108:111]\n\t"
"ds_read_b128 a[172:175], v224 offset:5632\n\t"
"v_mfma_f32_16x16x32_bf16 v[192:195], a[216:219], a[176:179], 0\n\t"
"s_nop 0\n\t"
"ds_read_b64_tr_b16 a[176:177], v248 offset:0\n\t"
"ds_read_b64_tr_b16 a[178:179], v248 offset:512\n\t"
"v_mfma_f32_16x16x32_bf16 v[196:199], a[216:219], a[180:183], 0\n\t"
"s_nop 0\n\t"
"ds_read_b64_tr_b16 a[180:181], v249 offset:0\n\t"
"ds_read_b64_tr_b16 a[182:183], v249 offset:512\n\t"
"v_mfma_f32_16x16x32_bf16 v[200:203], a[216:219], a[184:187], 0\n\t"
"ds_read_b64_tr_b16 a[184:185], v250 offset:0\n\t"
"v_mfma_f32_16x16x32_bf16 v[204:207], a[216:219], a[188:191], 0\n\t"
"ds_read_b64_tr_b16 a[186:187], v250 offset:512\n\t"
"v_mfma_f32_16x16x32_bf16 v[208:211], a[216:219], a[192:195], 0\n\t"
"s_nop 0\n\t"
"ds_read_b64_tr_b16 a[188:189], v251 offset:0\n\t"
"ds_read_b64_tr_b16 a[190:191], v251 offset:512\n\t"
"v_mfma_f32_16x16x32_bf16 v[212:215], a[216:219], a[196:199], 0\n\t"
"s_waitcnt lgkmcnt(10)\n\t"
"v_mfma_f32_16x16x32_bf16 v[216:219], a[216:219], a[200:203], 0\n\t"
"ds_write_b128 v254, a[228:231] offset:48000\n\t"
"v_mfma_f32_16x16x32_bf16 v[220:223], a[216:219], a[204:207], 0\n\t"
"s_waitcnt vmcnt(29)\n\t"
"v_cvt_pk_bf16_f32 v192, v192, v196\n\t"
"v_cvt_pk_bf16_f32 v195, v195, v199\n\t"
"v_mfma_f32_16x16x32_bf16 v[48:51], a[48:51], a[144:147], v[48:51]\n\t"
"v_cvt_pk_bf16_f32 v193, v193, v197\n\t"
"v_cvt_pk_bf16_f32 v194, v194, v198\n\t"
"v_mfma_f32_16x16x32_bf16 v[48:51], a[52:55], a[160:163], v[48:51]\n\t"
"v_cvt_pk_bf16_f32 v203, v203, v207\n\t"
"v_mfma_f32_16x16x32_bf16 v[64:67], a[56:59], a[144:147], v[64:67]\n\t"
"ds_read_b64_tr_b16 a[192:193], v248 offset:1152\n\t"
"v_cvt_pk_bf16_f32 v202, v202, v206\n\t"
"v_mfma_f32_16x16x32_bf16 v[64:67], a[60:63], a[160:163], v[64:67]\n\t"
"ds_write_addtid_b32 v194 offset:24672\n\t"
"v_cvt_pk_bf16_f32 v209, v209, v213\n\t"
"v_mfma_f32_16x16x32_bf16 v[80:83], a[64:67], a[144:147], v[80:83]\n\t"
"ds_read_b128 a[144:147], v224 offset:2048\n\t"
"v_cvt_pk_bf16_f32 v211, v211, v215\n\t"
"v_mfma_f32_16x16x32_bf16 v[80:83], a[68:71], a[160:163], v[80:83]\n\t"
"ds_read_b64_tr_b16 a[194:195], v248 offset:1664\n\t"
"v_cvt_pk_bf16_f32 v217, v217, v221\n\t"
"v_mfma_f32_16x16x32_bf16 v[84:87], a[64:67], a[148:151], v[84:87]\n\t"
"s_nop 0\n\t"
"ds_read_b128 a[160:163], v224 offset:6144\n\t"
"ds_write_addtid_b32 v192 offset:16448\n\t"
"v_mfma_f32_16x16x32_bf16 v[84:87], a[68:71], a[164:167], v[84:87]\n\t"
"ds_write_addtid_b32 v202 offset:28768\n\t"
"v_cvt_pk_bf16_f32 v216, v216, v220\n\t"
"v_mfma_f32_16x16x32_bf16 v[68:71], a[56:59], a[148:151], v[68:71]\n\t"
"ds_read_b64_tr_b16 a[196:197], v249 offset:1152\n\t"
"v_cvt_pk_bf16_f32 v201, v201, v205\n\t"
"v_mfma_f32_16x16x32_bf16 v[68:71], a[60:63], a[164:167], v[68:71]\n\t"
"ds_write_addtid_b32 v203 offset:29024\n\t"
"v_cvt_pk_bf16_f32 v208, v208, v212\n\t"
"v_mfma_f32_16x16x32_bf16 v[52:55], a[48:51], a[148:151], v[52:55]\n\t"
"ds_read_b128 a[148:151], v224 offset:2560\n\t"
"v_cvt_pk_bf16_f32 v218, v218, v222\n\t"
"v_mfma_f32_16x16x32_bf16 v[52:55], a[52:55], a[164:167], v[52:55]\n\t"
"ds_read_b128 a[164:167], v224 offset:6656\n\t"
"v_cvt_pk_bf16_f32 v210, v210, v214\n\t"
"v_mfma_f32_16x16x32_bf16 v[56:59], a[48:51], a[152:155], v[56:59]\n\t"
"ds_write_addtid_b32 v210 offset:26720\n\t"
"v_mfma_f32_16x16x32_bf16 v[56:59], a[52:55], a[168:171], v[56:59]\n\t"
"ds_write_addtid_b32 v209 offset:18752\n\t"
"v_cvt_pk_bf16_f32 v219, v219, v223\n\t"
"v_mfma_f32_16x16x32_bf16 v[72:75], a[56:59], a[152:155], v[72:75]\n\t"
"ds_write_addtid_b32 v219 offset:31072\n\t"
"v_mfma_f32_16x16x32_bf16 v[72:75], a[60:63], a[168:171], v[72:75]\n\t"
"ds_read_b64_tr_b16 a[198:199], v249 offset:1664\n\t"
"v_mfma_f32_16x16x32_bf16 v[88:91], a[64:67], a[152:155], v[88:91]\n\t"
"ds_write_addtid_b32 v216 offset:22592\n\t"
"v_mfma_f32_16x16x32_bf16 v[88:91], a[68:71], a[168:171], v[88:91]\n\t"
"s_waitcnt lgkmcnt(9)\n\t"
"ds_read_b128 a[152:155], v224 offset:3072\n\t"
"v_mfma_f32_16x16x32_bf16 v[92:95], a[64:67], a[156:159], v[92:95]\n\t"
"ds_read_b64_tr_b16 a[200:201], v250 offset:1152\n\t"
"v_cvt_pk_bf16_f32 v200, v200, v204\n\t"
"v_mfma_f32_16x16x32_bf16 v[92:95], a[68:71], a[172:175], v[92:95]\n\t"
"ds_write_addtid_b32 v211 offset:26976\n\t"
"v_mfma_f32_16x16x32_bf16 v[76:79], a[56:59], a[156:159], v[76:79]\n\t"
"ds_write_addtid_b32 v217 offset:22848\n\t"
"v_mfma_f32_16x16x32_bf16 v[76:79], a[60:63], a[172:175], v[76:79]\n\t"
"ds_write_addtid_b32 v200 offset:20544\n\t"
"v_mfma_f32_16x16x32_bf16 v[60:63], a[48:51], a[156:159], v[60:63]\n\t"
"ds_write_addtid_b32 v218 offset:30816\n\t"
"v_mfma_f32_16x16x32_bf16 v[60:63], a[52:55], a[172:175], v[60:63]\n\t"
"s_waitcnt vmcnt(22) lgkmcnt(9)\n\t"
"ds_write_addtid_b32 v193 offset:16704\n\t"
"v_mfma_f32_16x16x32_bf16 v[144:147], a[72:75], a[144:147], v[144:147]\n\t"
"ds_write_addtid_b32 v201 offset:20800\n\t"
"v_mfma_f32_16x16x32_bf16 v[144:147], a[76:79], a[160:163], v[144:147]\n\t"
"ds_write_addtid_b32 v208 offset:18496\n\t"
"v_mfma_f32_16x16x32_bf16 v[160:163], a[80:83], a[144:147], v[160:163]\n\t"
"ds_read_b128 a[172:175], v224 offset:7680\n\t"
"v_mfma_f32_16x16x32_bf16 v[160:163], a[84:87], a[160:163], v[160:163]\n\t"
"ds_write_addtid_b32 v195 offset:24928\n\t"
"v_mfma_f32_16x16x32_bf16 v[176:179], a[88:91], a[144:147], v[176:179]\n\t"
"ds_read_b128 a[168:171], v224 offset:7168\n\t"
"v_mfma_f32_16x16x32_bf16 v[176:179], a[92:95], a[160:163], v[176:179]\n\t"
"ds_read_b64_tr_b16 a[202:203], v250 offset:1664\n\t"
"v_mfma_f32_16x16x32_bf16 v[180:183], a[88:91], a[148:151], v[180:183]\n\t"
"ds_read_b128 a[156:159], v224 offset:3584\n\t"
"v_mfma_f32_16x16x32_bf16 v[180:183], a[92:95], a[164:167], v[180:183]\n\t"
"ds_read_b64_tr_b16 a[204:205], v251 offset:1152\n\t"
"v_mfma_f32_16x16x32_bf16 v[164:167], a[80:83], a[148:151], v[164:167]\n\t"
"ds_read_b64_tr_b16 a[206:207], v251 offset:1664\n\t"
"v_mfma_f32_16x16x32_bf16 v[164:167], a[84:87], a[164:167], v[164:167]\n\t"
"v_mfma_f32_16x16x32_bf16 v[148:151], a[72:75], a[148:151], v[148:151]\n\t"
"s_waitcnt lgkmcnt(2)\n\t"
"v_mfma_f32_16x16x32_bf16 v[148:151], a[76:79], a[164:167], v[148:151]\n\t"
"s_barrier\n\t"
"v_mfma_f32_16x16x32_bf16 v[152:155], a[72:75], a[152:155], v[152:155]\n\t"
"ds_read_b128 a[144:147], v224 offset:16448\n\t"
"v_add_u32 v248, 143104, v226\n\t"
"v_mfma_f32_16x16x32_bf16 v[152:155], a[76:79], a[168:171], v[152:155]\n\t"
"ds_read_b128 a[160:163], v224 offset:20544\n\t"
"v_mfma_f32_16x16x32_bf16 v[168:171], a[80:83], a[152:155], v[168:171]\n\t"
"ds_read_b128 a[148:151], v224 offset:16960\n\t"
"v_swap_b32 v236, v241\n\t"
"v_mfma_f32_16x16x32_bf16 v[168:171], a[84:87], a[168:171], v[168:171]\n\t"
"ds_read_b128 a[164:167], v224 offset:21056\n\t"
"v_xor_b32 v249, 32, v248\n\t"
"v_mfma_f32_16x16x32_bf16 v[184:187], a[88:91], a[152:155], v[184:187]\n\t"
"v_swap_b32 v237, v242\n\t"
"v_swap_b32 v238, v243\n\t"
"v_mfma_f32_16x16x32_bf16 v[184:187], a[92:95], a[168:171], v[184:187]\n\t"
"v_swap_b32 v239, v244\n\t"
"v_swap_b32 v240, v245\n\t"
"v_mfma_f32_16x16x32_bf16 v[188:191], a[88:91], a[156:159], v[188:191]\n\t"
"ds_read_b128 a[168:171], v224 offset:21568\n\t"
"v_xor_b32 v250, 64, v248\n\t"
"v_mfma_f32_16x16x32_bf16 v[188:191], a[92:95], a[172:175], v[188:191]\n\t"
"v_add_u32 v225, v225, v253\n\t"
"v_sub_u32 v253, 0, v253\n\t"
"v_mfma_f32_16x16x32_bf16 v[172:175], a[80:83], a[156:159], v[172:175]\n\t"
"ds_read_b128 a[152:155], v224 offset:17472\n\t"
"v_accvgpr_mov_b32 a228, a208\n\t"
"v_mfma_f32_16x16x32_bf16 v[172:175], a[84:87], a[172:175], v[172:175]\n\t"
"v_accvgpr_mov_b32 a208, a209\n\t"
"v_mfma_f32_16x16x32_bf16 v[156:159], a[72:75], a[156:159], v[156:159]\n\t"
"ds_read_b128 a[156:159], v224 offset:17984\n\t"
"v_xor_b32 v251, 96, v248\n\t"
"v_mfma_f32_16x16x32_bf16 v[156:159], a[76:79], a[172:175], v[156:159]\n\t"
"buffer_load_dwordx4 a[16:19], v252, s[84:87], s57 offen offset:0 sc1\n\t"
"v_mfma_f32_16x16x32_bf16 v[192:195], a[224:227], a[176:179], 0\n\t"
"s_nop 0\n\t"
"ds_read_b64_tr_b16 a[176:177], v248 offset:0\n\t"
"ds_read_b64_tr_b16 a[178:179], v248 offset:512\n\t"
"v_mfma_f32_16x16x32_bf16 v[196:199], a[224:227], a[180:183], 0\n\t"
"s_nop 0\n\t"
"ds_read_b128 a[172:175], v224 offset:22080\n\t"
"ds_read_b64_tr_b16 a[180:181], v248 offset:1024\n\t"
"v_mfma_f32_16x16x32_bf16 v[200:203], a[224:227], a[184:187], 0\n\t"
"ds_read_b64_tr_b16 a[184:185], v249 offset:0\n\t"
"v_mfma_f32_16x16x32_bf16 v[204:207], a[224:227], a[188:191], 0\n\t"
"buffer_load_dwordx4 a[20:23], v252, s[84:87], s57 offen offset:1024 sc1\n\t"
"v_mfma_f32_16x16x32_bf16 v[208:211], a[224:227], a[192:195], 0\n\t"
"ds_read_b64_tr_b16 a[186:187], v249 offset:512\n\t"
"v_accvgpr_mov_b32 a209, a228\n\t"
"v_mfma_f32_16x16x32_bf16 v[212:215], a[224:227], a[196:199], 0\n\t"
"ds_read_b64_tr_b16 a[188:189], v249 offset:1024\n\t"
"v_mfma_f32_16x16x32_bf16 v[216:219], a[224:227], a[200:203], 0\n\t"
"s_waitcnt lgkmcnt(8)\n\t"
"ds_read_b64_tr_b16 a[192:193], v250 offset:0\n\t"
"v_mfma_f32_16x16x32_bf16 v[220:223], a[224:227], a[204:207], 0\n\t"
"buffer_load_dwordx4 a[12:15], v235, s[84:87], s57 offen offset:3072 sc1\n\t"
"v_mfma_f32_16x16x32_bf16 v[0:3], a[48:51], a[144:147], v[0:3]\n\t"
"ds_read_b64_tr_b16 a[194:195], v250 offset:512\n\t"
"v_cvt_pk_bf16_f32 v194, v194, v198\n\t"
"v_mfma_f32_16x16x32_bf16 v[0:3], a[52:55], a[160:163], v[0:3]\n\t"
"ds_read_b64_tr_b16 a[196:197], v250 offset:1024\n\t"
"v_cvt_pk_bf16_f32 v202, v202, v206\n\t"
"v_mfma_f32_16x16x32_bf16 v[16:19], a[56:59], a[144:147], v[16:19]\n\t"
"ds_read_b64_tr_b16 a[200:201], v251 offset:0\n\t"
"v_cvt_pk_bf16_f32 v200, v200, v204\n\t"
"v_mfma_f32_16x16x32_bf16 v[16:19], a[60:63], a[160:163], v[16:19]\n\t"
"buffer_load_dwordx4 a[8:11], v235, s[84:87], s57 offen offset:2048 sc1\n\t"
"v_mfma_f32_16x16x32_bf16 v[32:35], a[64:67], a[144:147], v[32:35]\n\t"
"ds_write_addtid_b32 v202 offset:12320\n\t"
"v_cvt_pk_bf16_f32 v209, v209, v213\n\t"
"v_mfma_f32_16x16x32_bf16 v[32:35], a[68:71], a[160:163], v[32:35]\n\t"
"ds_read_b128 a[160:163], v224 offset:22592\n\t"
"v_cvt_pk_bf16_f32 v217, v217, v221\n\t"
"v_mfma_f32_16x16x32_bf16 v[36:39], a[64:67], a[148:151], v[36:39]\n\t"
"ds_read_b128 a[144:147], v224 offset:18496\n\t"
"v_cvt_pk_bf16_f32 v201, v201, v205\n\t"
"v_mfma_f32_16x16x32_bf16 v[36:39], a[68:71], a[164:167], v[36:39]\n\t"
"buffer_load_dwordx4 a[0:3], v235, s[84:87], s57 offen offset:0 sc1\n\t"
"v_mfma_f32_16x16x32_bf16 v[20:23], a[56:59], a[148:151], v[20:23]\n\t"
"ds_write_addtid_b32 v201 offset:4352\n\t"
"v_cvt_pk_bf16_f32 v216, v216, v220\n\t"
"v_mfma_f32_16x16x32_bf16 v[20:23], a[60:63], a[164:167], v[20:23]\n\t"
"ds_write_addtid_b32 v200 offset:4096\n\t"
"v_cvt_pk_bf16_f32 v218, v218, v222\n\t"
"v_mfma_f32_16x16x32_bf16 v[4:7], a[48:51], a[148:151], v[4:7]\n\t"
"ds_read_b128 a[148:151], v224 offset:19008\n\t"
"v_cvt_pk_bf16_f32 v211, v211, v215\n\t"
"v_mfma_f32_16x16x32_bf16 v[4:7], a[52:55], a[164:167], v[4:7]\n\t"
"buffer_load_dwordx4 a[4:7], v235, s[84:87], s57 offen offset:1024 sc1\n\t"
"v_mfma_f32_16x16x32_bf16 v[8:11], a[48:51], a[152:155], v[8:11]\n\t"
"ds_write_addtid_b32 v217 offset:6400\n\t"
"v_cvt_pk_bf16_f32 v208, v208, v212\n\t"
"v_mfma_f32_16x16x32_bf16 v[8:11], a[52:55], a[168:171], v[8:11]\n\t"
"s_nop 0\n\t"
"ds_read_b128 a[164:167], v224 offset:23104\n\t"
"ds_write_addtid_b32 v218 offset:14368\n\t"
"v_mfma_f32_16x16x32_bf16 v[24:27], a[56:59], a[152:155], v[24:27]\n\t"
"ds_write_addtid_b32 v211 offset:10528\n\t"
"v_cvt_pk_bf16_f32 v203, v203, v207\n\t"
"v_mfma_f32_16x16x32_bf16 v[24:27], a[60:63], a[168:171], v[24:27]\n\t"
"buffer_load_dwordx4 a[40:43], v252, s[84:87], s63 offen offset:0 sc1\n\t"
"v_mfma_f32_16x16x32_bf16 v[40:43], a[64:67], a[152:155], v[40:43]\n\t"
"ds_write_addtid_b32 v208 offset:2048\n\t"
"v_cvt_pk_bf16_f32 v192, v192, v196\n\t"
"v_mfma_f32_16x16x32_bf16 v[40:43], a[68:71], a[168:171], v[40:43]\n\t"
"s_waitcnt lgkmcnt(8)\n\t"
"ds_write_addtid_b32 v192 offset:0\n\t"
"v_cvt_pk_bf16_f32 v219, v219, v223\n\t"
"v_mfma_f32_16x16x32_bf16 v[44:47], a[64:67], a[156:159], v[44:47]\n\t"
"ds_write_addtid_b32 v209 offset:2304\n\t"
"v_cvt_pk_bf16_f32 v193, v193, v197\n\t"
"v_mfma_f32_16x16x32_bf16 v[44:47], a[68:71], a[172:175], v[44:47]\n\t"
"buffer_load_dwordx4 a[44:47], v252, s[84:87], s63 offen offset:1024 sc1\n\t"
"v_mfma_f32_16x16x32_bf16 v[28:31], a[56:59], a[156:159], v[28:31]\n\t"
"ds_write_addtid_b32 v194 offset:8224\n\t"
"v_cvt_pk_bf16_f32 v210, v210, v214\n\t"
"v_mfma_f32_16x16x32_bf16 v[28:31], a[60:63], a[172:175], v[28:31]\n\t"
"ds_write_addtid_b32 v193 offset:256\n\t"
"v_cvt_pk_bf16_f32 v195, v195, v199\n\t"
"v_mfma_f32_16x16x32_bf16 v[12:15], a[48:51], a[156:159], v[12:15]\n\t"
"s_nop 0\n\t"
"ds_read_b128 a[152:155], v224 offset:19520\n\t"
"ds_write_addtid_b32 v210 offset:10272\n\t"
"v_mfma_f32_16x16x32_bf16 v[12:15], a[52:55], a[172:175], v[12:15]\n\t"
"buffer_load_dwordx4 a[36:39], v235, s[84:87], s63 offen offset:3072 sc1\n\t"
"v_mfma_f32_16x16x32_bf16 v[96:99], a[72:75], a[144:147], v[96:99]\n\t"
"ds_write_addtid_b32 v216 offset:6144\n\t"
"v_mfma_f32_16x16x32_bf16 v[96:99], a[76:79], a[160:163], v[96:99]\n\t"
"s_nop 0\n\t"
"ds_write_addtid_b32 v203 offset:12576\n\t"
"ds_write_addtid_b32 v219 offset:14624\n\t"
"v_mfma_f32_16x16x32_bf16 v[112:115], a[80:83], a[144:147], v[112:115]\n\t"
"ds_write_addtid_b32 v195 offset:8480\n\t"
"v_mfma_f32_16x16x32_bf16 v[112:115], a[84:87], a[160:163], v[112:115]\n\t"
"buffer_load_dwordx4 a[32:35], v235, s[84:87], s63 offen offset:2048 sc1\n\t"
"v_mfma_f32_16x16x32_bf16 v[128:131], a[88:91], a[144:147], v[128:131]\n\t"
"ds_read_b128 a[156:159], v224 offset:20032\n\t"
"v_mfma_f32_16x16x32_bf16 v[128:131], a[92:95], a[160:163], v[128:131]\n\t"
"s_waitcnt lgkmcnt(9)\n\t"
"ds_read_b128 a[168:171], v224 offset:23616\n\t"
"v_mfma_f32_16x16x32_bf16 v[132:135], a[88:91], a[148:151], v[132:135]\n\t"
"ds_read_b128 a[172:175], v224 offset:24128\n\t"
"v_mfma_f32_16x16x32_bf16 v[132:135], a[92:95], a[164:167], v[132:135]\n\t"
"ds_read_b64_tr_b16 a[202:203], v251 offset:512\n\t"
"v_mfma_f32_16x16x32_bf16 v[116:119], a[80:83], a[148:151], v[116:119]\n\t"
"ds_read_b64_tr_b16 a[204:205], v251 offset:1024\n\t"
"v_mfma_f32_16x16x32_bf16 v[116:119], a[84:87], a[164:167], v[116:119]\n\t"
"buffer_load_dwordx4 a[24:27], v235, s[84:87], s63 offen offset:0 sc1\n\t"
"v_mfma_f32_16x16x32_bf16 v[100:103], a[72:75], a[148:151], v[100:103]\n\t"
"v_mfma_f32_16x16x32_bf16 v[100:103], a[76:79], a[164:167], v[100:103]\n\t"
"buffer_load_dwordx4 a[28:31], v235, s[84:87], s63 offen offset:1024 sc1\n\t"
"s_waitcnt vmcnt(13) lgkmcnt(6)\n\t"
"s_waitcnt lgkmcnt(0)\n\t"
"s_barrier\n\t"
"v_mfma_f32_16x16x32_bf16 v[104:107], a[72:75], a[152:155], v[104:107]\n\t"
"s_add_u32 m0, s26, 145280\n\t"
"ds_read_b128 a[144:147], v224 offset:0\n\t"
"ds_read_b128 a[148:151], v224 offset:512\n\t"
"v_mfma_f32_16x16x32_bf16 v[104:107], a[76:79], a[168:171], v[104:107]\n\t"
"buffer_load_dwordx4 v236, s[12:15], s22 offen offset:128 nt lds\n\t"
"v_mfma_f32_16x16x32_bf16 v[120:123], a[80:83], a[152:155], v[120:123]\n\t"
"ds_read_b128 a[160:163], v224 offset:4096\n\t"
"v_add_u32 v248, 80896, v227\n\t"
"v_mfma_f32_16x16x32_bf16 v[120:123], a[84:87], a[168:171], v[120:123]\n\t"
"ds_read_b128 a[164:167], v224 offset:4608\n\t"
"v_xor_b32 v249, 32, v248\n\t"
"v_mfma_f32_16x16x32_bf16 v[136:139], a[88:91], a[152:155], v[136:139]\n\t"
"ds_read_b128 a[152:155], v224 offset:1024\n\t"
"v_xor_b32 v250, 64, v248\n\t"
"v_mfma_f32_16x16x32_bf16 v[136:139], a[92:95], a[168:171], v[136:139]\n\t"
"buffer_store_dwordx4 a[232:235], v233, s[16:19], s25 offen nt\n\t"
"v_mfma_f32_16x16x32_bf16 v[140:143], a[88:91], a[156:159], v[140:143]\n\t"
"ds_read_b128 a[168:171], v224 offset:5120\n\t"
"v_xor_b32 v251, 96, v248\n\t"
"v_mfma_f32_16x16x32_bf16 v[140:143], a[92:95], a[172:175], v[140:143]\n\t"
"ds_read_b64_tr_b16 a[182:183], v249 offset:512\n\t"
"v_mfma_f32_16x16x32_bf16 v[124:127], a[80:83], a[156:159], v[124:127]\n\t"
"ds_read_b64_tr_b16 a[190:191], v251 offset:512\n\t"
"v_mfma_f32_16x16x32_bf16 v[124:127], a[84:87], a[172:175], v[124:127]\n\t"
"buffer_load_dwordx4 v237, s[12:15], s22 offen offset:1152 nt lds\n\t"
"v_mfma_f32_16x16x32_bf16 v[108:111], a[72:75], a[156:159], v[108:111]\n\t"
"s_nop 0\n\t"
"ds_read_b64_tr_b16 a[198:199], v249 offset:1664\n\t"
"ds_read_b64_tr_b16 a[206:207], v251 offset:1664\n\t"
"v_mfma_f32_16x16x32_bf16 v[108:111], a[76:79], a[172:175], v[108:111]\n\t"
"ds_read_b128 a[156:159], v224 offset:1536\n\t"
"v_mfma_f32_16x16x32_bf16 v[192:195], a[220:223], a[176:179], 0\n\t"
"ds_read_b64_tr_b16 a[176:177], v248 offset:0\n\t"
"v_mfma_f32_16x16x32_bf16 v[196:199], a[220:223], a[184:187], 0\n\t"
"s_nop 0\n\t"
"ds_read_b128 a[172:175], v224 offset:5632\n\t"
"ds_read_b64_tr_b16 a[184:185], v250 offset:0\n\t"
"v_mfma_f32_16x16x32_bf16 v[200:203], a[220:223], a[178:181], 0\n\t"
"ds_read_b64_tr_b16 a[178:179], v248 offset:512\n\t"
"v_mfma_f32_16x16x32_bf16 v[204:207], a[220:223], a[186:189], 0\n\t"
"s_nop 0\n\t"
"ds_read_b64_tr_b16 a[180:181], v249 offset:0\n\t"
"ds_read_b64_tr_b16 a[186:187], v250 offset:512\n\t"
"v_mfma_f32_16x16x32_bf16 v[208:211], a[220:223], a[192:195], 0\n\t"
"buffer_load_dwordx4 v238, s[12:15], s22 offen offset:2176 nt lds\n\t"
"v_mfma_f32_16x16x32_bf16 v[212:215], a[220:223], a[200:203], 0\n\t"
"ds_write_b128 v255, a[244:247] offset:43776\n\t"
"v_mfma_f32_16x16x32_bf16 v[216:219], a[220:223], a[194:197], 0\n\t"
"v_mfma_f32_16x16x32_bf16 v[220:223], a[220:223], a[202:205], 0\n\t"
"s_waitcnt lgkmcnt(10)\n\t"
"buffer_load_dwordx4 v239, s[12:15], s22 offen offset:3200 nt lds\n\t"
"v_mfma_f32_16x16x32_bf16 v[48:51], a[96:99], a[144:147], v[48:51]\n\t"
"v_cvt_pk_bf16_f32 v194, v194, v198\n\t"
"v_cvt_pk_bf16_f32 v195, v195, v199\n\t"
"v_mfma_f32_16x16x32_bf16 v[48:51], a[100:103], a[160:163], v[48:51]\n\t"
"ds_read_b64_tr_b16 a[188:189], v251 offset:0\n\t"
"v_cvt_pk_bf16_f32 v200, v200, v204\n\t"
"v_mfma_f32_16x16x32_bf16 v[64:67], a[104:107], a[144:147], v[64:67]\n\t"
"ds_read_b64_tr_b16 a[192:193], v248 offset:1152\n\t"
"v_cvt_pk_bf16_f32 v193, v193, v197\n\t"
"v_mfma_f32_16x16x32_bf16 v[64:67], a[108:111], a[160:163], v[64:67]\n\t"
"buffer_store_dwordx4 a[236:239], v234, s[16:19], s25 offen nt\n\t"
"v_mfma_f32_16x16x32_bf16 v[80:83], a[112:115], a[144:147], v[80:83]\n\t"
"ds_read_b128 a[144:147], v224 offset:2048\n\t"
"v_cvt_pk_bf16_f32 v208, v208, v212\n\t"
"v_mfma_f32_16x16x32_bf16 v[80:83], a[116:119], a[160:163], v[80:83]\n\t"
"ds_read_b64_tr_b16 a[200:201], v250 offset:1152\n\t"
"v_cvt_pk_bf16_f32 v209, v209, v213\n\t"
"v_mfma_f32_16x16x32_bf16 v[84:87], a[112:115], a[148:151], v[84:87]\n\t"
"ds_read_b128 a[160:163], v224 offset:6144\n\t"
"v_cvt_pk_bf16_f32 v216, v216, v220\n\t"
"v_mfma_f32_16x16x32_bf16 v[84:87], a[116:119], a[164:167], v[84:87]\n\t"
"s_mov_b64 exec, s[36:37]\n\t"
"buffer_load_dwordx4 v240, s[12:15], s22 offen offset:3712 nt lds\n\t"
"s_mov_b64 exec, -1\n\t"
"v_mfma_f32_16x16x32_bf16 v[68:71], a[104:107], a[148:151], v[68:71]\n\t"
"s_lshl_b32 m0, s73, 9\n\t"
"ds_read_b64_tr_b16 a[194:195], v248 offset:1664\n\t"
"v_cvt_pk_bf16_f32 v217, v217, v221\n\t"
"v_mfma_f32_16x16x32_bf16 v[68:71], a[108:111], a[164:167], v[68:71]\n\t"
"ds_write_addtid_b32 v209 offset:28768\n\t"
"v_cvt_pk_bf16_f32 v210, v210, v214\n\t"
"v_mfma_f32_16x16x32_bf16 v[52:55], a[96:99], a[148:151], v[52:55]\n\t"
"s_nop 0\n\t"
"ds_write_addtid_b32 v194 offset:18496\n\t"
"ds_write_addtid_b32 v210 offset:22592\n\t"
"v_mfma_f32_16x16x32_bf16 v[52:55], a[100:103], a[164:167], v[52:55]\n\t"
"ds_read_b64_tr_b16 a[196:197], v249 offset:1152\n\t"
"v_cvt_pk_bf16_f32 v201, v201, v205\n\t"
"v_mfma_f32_16x16x32_bf16 v[56:59], a[96:99], a[152:155], v[56:59]\n\t"
"ds_read_b128 a[164:167], v224 offset:6656\n\t"
"v_cvt_pk_bf16_f32 v218, v218, v222\n\t"
"v_mfma_f32_16x16x32_bf16 v[56:59], a[100:103], a[168:171], v[56:59]\n\t"
"ds_read_b128 a[148:151], v224 offset:2560\n\t"
"v_cvt_pk_bf16_f32 v203, v203, v207\n\t"
"v_mfma_f32_16x16x32_bf16 v[72:75], a[104:107], a[152:155], v[72:75]\n\t"
"ds_write_addtid_b32 v200 offset:16704\n\t"
"v_cvt_pk_bf16_f32 v211, v211, v215\n\t"
"v_mfma_f32_16x16x32_bf16 v[72:75], a[108:111], a[168:171], v[72:75]\n\t"
"ds_write_addtid_b32 v216 offset:20800\n\t"
"v_cvt_pk_bf16_f32 v202, v202, v206\n\t"
"v_mfma_f32_16x16x32_bf16 v[88:91], a[112:115], a[152:155], v[88:91]\n\t"
"ds_write_addtid_b32 v195 offset:26720\n\t"
"v_cvt_pk_bf16_f32 v192, v192, v196\n\t"
"v_mfma_f32_16x16x32_bf16 v[88:91], a[116:119], a[168:171], v[88:91]\n\t"
"s_waitcnt lgkmcnt(10)\n\t"
"ds_write_addtid_b32 v202 offset:18752\n\t"
"v_cvt_pk_bf16_f32 v219, v219, v223\n\t"
"v_mfma_f32_16x16x32_bf16 v[92:95], a[112:115], a[156:159], v[92:95]\n\t"
"ds_write_addtid_b32 v217 offset:29024\n\t"
"v_mfma_f32_16x16x32_bf16 v[92:95], a[116:119], a[172:175], v[92:95]\n\t"
"ds_write_addtid_b32 v208 offset:20544\n\t"
"v_mfma_f32_16x16x32_bf16 v[76:79], a[104:107], a[156:159], v[76:79]\n\t"
"ds_write_addtid_b32 v218 offset:22848\n\t"
"v_mfma_f32_16x16x32_bf16 v[76:79], a[108:111], a[172:175], v[76:79]\n\t"
"ds_write_addtid_b32 v201 offset:24928\n\t"
"v_mfma_f32_16x16x32_bf16 v[60:63], a[96:99], a[156:159], v[60:63]\n\t"
"ds_write_addtid_b32 v219 offset:31072\n\t"
"v_mfma_f32_16x16x32_bf16 v[60:63], a[100:103], a[172:175], v[60:63]\n\t"
"ds_write_addtid_b32 v211 offset:30816\n\t"
"v_mfma_f32_16x16x32_bf16 v[144:147], a[120:123], a[144:147], v[144:147]\n\t"
"ds_read_b128 a[156:159], v224 offset:3584\n\t"
"v_mfma_f32_16x16x32_bf16 v[144:147], a[124:127], a[160:163], v[144:147]\n\t"
"ds_write_addtid_b32 v203 offset:26976\n\t"
"v_mfma_f32_16x16x32_bf16 v[160:163], a[128:131], a[144:147], v[160:163]\n\t"
"ds_write_addtid_b32 v193 offset:24672\n\t"
"v_mfma_f32_16x16x32_bf16 v[160:163], a[132:135], a[160:163], v[160:163]\n\t"
"ds_write_addtid_b32 v192 offset:16448\n\t"
"v_mfma_f32_16x16x32_bf16 v[176:179], a[136:139], a[144:147], v[176:179]\n\t"
"ds_read_b128 a[172:175], v224 offset:7680\n\t"
"v_mfma_f32_16x16x32_bf16 v[176:179], a[140:143], a[160:163], v[176:179]\n\t"
"s_waitcnt lgkmcnt(9)\n\t"
"ds_read_b128 a[168:171], v224 offset:7168\n\t"
"v_mfma_f32_16x16x32_bf16 v[180:183], a[136:139], a[148:151], v[180:183]\n\t"
"ds_read_b128 a[152:155], v224 offset:3072\n\t"
"v_mfma_f32_16x16x32_bf16 v[180:183], a[140:143], a[164:167], v[180:183]\n\t"
"ds_read_b64_tr_b16 a[202:203], v250 offset:1664\n\t"
"v_mfma_f32_16x16x32_bf16 v[164:167], a[128:131], a[148:151], v[164:167]\n\t"
"ds_read_b64_tr_b16 a[204:205], v251 offset:1152\n\t"
"v_mfma_f32_16x16x32_bf16 v[164:167], a[132:135], a[164:167], v[164:167]\n\t"
"v_mfma_f32_16x16x32_bf16 v[148:151], a[120:123], a[148:151], v[148:151]\n\t"
"s_waitcnt lgkmcnt(2)\n\t"
"v_mfma_f32_16x16x32_bf16 v[148:151], a[124:127], a[164:167], v[148:151]\n\t"
"s_barrier\n\t"
"v_mfma_f32_16x16x32_bf16 v[152:155], a[120:123], a[152:155], v[152:155]\n\t"
"ds_read_b128 a[144:147], v224 offset:16448\n\t"
"v_add_u32 v248, 53632, v226\n\t"
"v_mfma_f32_16x16x32_bf16 v[152:155], a[124:127], a[168:171], v[152:155]\n\t"
"ds_read_b128 a[160:163], v224 offset:20544\n\t"
"v_xor_b32 v249, 32, v248\n\t"
"v_mfma_f32_16x16x32_bf16 v[168:171], a[128:131], a[152:155], v[168:171]\n\t"
"s_xor_b32 s24, s24, 384\n\t"
"ds_read_b128 a[148:151], v224 offset:16960\n\t"
"v_xor_b32 v250, 64, v248\n\t"
"v_mfma_f32_16x16x32_bf16 v[168:171], a[132:135], a[168:171], v[168:171]\n\t"
"ds_read_b128 a[164:167], v224 offset:21056\n\t"
"v_xor_b32 v251, 96, v248\n\t"
"v_mfma_f32_16x16x32_bf16 v[184:187], a[136:139], a[152:155], v[184:187]\n\t"
"v_mfma_f32_16x16x32_bf16 v[184:187], a[140:143], a[168:171], v[184:187]\n\t"
"v_mfma_f32_16x16x32_bf16 v[188:191], a[136:139], a[156:159], v[188:191]\n\t"
"ds_read_b128 a[152:155], v224 offset:17472\n\t"
"v_mfma_f32_16x16x32_bf16 v[188:191], a[140:143], a[172:175], v[188:191]\n\t"
"ds_read_b128 a[168:171], v224 offset:21568\n\t"
"v_mfma_f32_16x16x32_bf16 v[172:175], a[128:131], a[156:159], v[172:175]\n\t"
"v_mfma_f32_16x16x32_bf16 v[172:175], a[132:135], a[172:175], v[172:175]\n\t"
"v_mfma_f32_16x16x32_bf16 v[156:159], a[120:123], a[156:159], v[156:159]\n\t"
"ds_read_b128 a[156:159], v224 offset:17984\n\t"
"v_mfma_f32_16x16x32_bf16 v[156:159], a[124:127], a[172:175], v[156:159]\n\t"
"ds_read_b128 a[172:175], v224 offset:22080\n\t"
"v_mfma_f32_16x16x32_bf16 v[192:195], a[216:219], a[176:179], 0\n\t"
"ds_read_b64_tr_b16 a[176:177], v248 offset:48000\n\t"
"v_mfma_f32_16x16x32_bf16 v[196:199], a[216:219], a[180:183], 0\n\t"
"s_xor_b32 s20, s20, 384\n\t"
"ds_read_b64_tr_b16 a[178:179], v248 offset:48512\n\t"
"ds_read_b64_tr_b16 a[180:181], v248 offset:49024\n\t"
"v_mfma_f32_16x16x32_bf16 v[200:203], a[216:219], a[184:187], 0\n\t"
"ds_read_b64_tr_b16 a[184:185], v249 offset:48000\n\t"
"v_mfma_f32_16x16x32_bf16 v[204:207], a[216:219], a[188:191], 0\n\t"
".p2align 3\n\t"
"ds_read_b64_tr_b16 a[186:187], v249 offset:48512\n\t"
"v_pk_add_f32 v[144:145], v[48:49], v[144:145] neg_lo:[0,1] neg_hi:[0,1]\n\t"
"v_pk_add_f32 v[146:147], v[50:51], v[146:147] neg_lo:[0,1] neg_hi:[0,1]\n\t"
"v_pk_fma_f32 v[48:49], v[144:145], s[90:91], v[48:49] neg_lo:[1,0,0] neg_hi:[1,0,0]\n\t"
"v_pk_fma_f32 v[50:51], v[146:147], s[90:91], v[50:51] neg_lo:[1,0,0] neg_hi:[1,0,0]\n\t"
"v_pk_add_f32 v[160:161], v[64:65], v[160:161] neg_lo:[0,1] neg_hi:[0,1]\n\t"
"v_pk_add_f32 v[162:163], v[66:67], v[162:163] neg_lo:[0,1] neg_hi:[0,1]\n\t"
"v_pk_fma_f32 v[64:65], v[160:161], s[90:91], v[64:65] neg_lo:[1,0,0] neg_hi:[1,0,0]\n\t"
"v_pk_fma_f32 v[66:67], v[162:163], s[90:91], v[66:67] neg_lo:[1,0,0] neg_hi:[1,0,0]\n\t"
"v_pk_add_f32 v[176:177], v[80:81], v[176:177] neg_lo:[0,1] neg_hi:[0,1]\n\t"
"v_pk_add_f32 v[178:179], v[82:83], v[178:179] neg_lo:[0,1] neg_hi:[0,1]\n\t"
"v_pk_fma_f32 v[80:81], v[176:177], s[90:91], v[80:81] neg_lo:[1,0,0] neg_hi:[1,0,0]\n\t"
"v_pk_fma_f32 v[82:83], v[178:179], s[90:91], v[82:83] neg_lo:[1,0,0] neg_hi:[1,0,0]\n\t"
"v_pk_add_f32 v[180:181], v[84:85], v[180:181] neg_lo:[0,1] neg_hi:[0,1]\n\t"
"v_pk_add_f32 v[182:183], v[86:87], v[182:183] neg_lo:[0,1] neg_hi:[0,1]\n\t"
"v_pk_fma_f32 v[84:85], v[180:181], s[90:91], v[84:85] neg_lo:[1,0,0] neg_hi:[1,0,0]\n\t"
"v_pk_fma_f32 v[86:87], v[182:183], s[90:91], v[86:87] neg_lo:[1,0,0] neg_hi:[1,0,0]\n\t"
"v_pk_add_f32 v[164:165], v[68:69], v[164:165] neg_lo:[0,1] neg_hi:[0,1]\n\t"
"v_pk_add_f32 v[166:167], v[70:71], v[166:167] neg_lo:[0,1] neg_hi:[0,1]\n\t"
"v_pk_fma_f32 v[68:69], v[164:165], s[90:91], v[68:69] neg_lo:[1,0,0] neg_hi:[1,0,0]\n\t"
"v_pk_fma_f32 v[70:71], v[166:167], s[90:91], v[70:71] neg_lo:[1,0,0] neg_hi:[1,0,0]\n\t"
"v_pk_add_f32 v[148:149], v[52:53], v[148:149] neg_lo:[0,1] neg_hi:[0,1]\n\t"
"v_pk_add_f32 v[150:151], v[54:55], v[150:151] neg_lo:[0,1] neg_hi:[0,1]\n\t"
"v_pk_fma_f32 v[52:53], v[148:149], s[90:91], v[52:53] neg_lo:[1,0,0] neg_hi:[1,0,0]\n\t"
"v_pk_fma_f32 v[54:55], v[150:151], s[90:91], v[54:55] neg_lo:[1,0,0] neg_hi:[1,0,0]\n\t"
"v_pk_add_f32 v[152:153], v[56:57], v[152:153] neg_lo:[0,1] neg_hi:[0,1]\n\t"
"v_pk_add_f32 v[154:155], v[58:59], v[154:155] neg_lo:[0,1] neg_hi:[0,1]\n\t"
"v_pk_fma_f32 v[56:57], v[152:153], s[90:91], v[56:57] neg_lo:[1,0,0] neg_hi:[1,0,0]\n\t"
"v_pk_fma_f32 v[58:59], v[154:155], s[90:91], v[58:59] neg_lo:[1,0,0] neg_hi:[1,0,0]\n\t"
"v_pk_add_f32 v[168:169], v[72:73], v[168:169] neg_lo:[0,1] neg_hi:[0,1]\n\t"
"v_pk_add_f32 v[170:171], v[74:75], v[170:171] neg_lo:[0,1] neg_hi:[0,1]\n\t"
"v_pk_fma_f32 v[72:73], v[168:169], s[90:91], v[72:73] neg_lo:[1,0,0] neg_hi:[1,0,0]\n\t"
"v_pk_fma_f32 v[74:75], v[170:171], s[90:91], v[74:75] neg_lo:[1,0,0] neg_hi:[1,0,0]\n\t"
"v_pk_add_f32 v[184:185], v[88:89], v[184:185] neg_lo:[0,1] neg_hi:[0,1]\n\t"
"v_pk_add_f32 v[186:187], v[90:91], v[186:187] neg_lo:[0,1] neg_hi:[0,1]\n\t"
"v_pk_fma_f32 v[88:89], v[184:185], s[90:91], v[88:89] neg_lo:[1,0,0] neg_hi:[1,0,0]\n\t"
"v_pk_fma_f32 v[90:91], v[186:187], s[90:91], v[90:91] neg_lo:[1,0,0] neg_hi:[1,0,0]\n\t"
"v_pk_add_f32 v[188:189], v[92:93], v[188:189] neg_lo:[0,1] neg_hi:[0,1]\n\t"
"v_pk_add_f32 v[190:191], v[94:95], v[190:191] neg_lo:[0,1] neg_hi:[0,1]\n\t"
"v_pk_fma_f32 v[92:93], v[188:189], s[90:91], v[92:93] neg_lo:[1,0,0] neg_hi:[1,0,0]\n\t"
"v_pk_fma_f32 v[94:95], v[190:191], s[90:91], v[94:95] neg_lo:[1,0,0] neg_hi:[1,0,0]\n\t"
"v_pk_add_f32 v[172:173], v[76:77], v[172:173] neg_lo:[0,1] neg_hi:[0,1]\n\t"
"v_pk_add_f32 v[174:175], v[78:79], v[174:175] neg_lo:[0,1] neg_hi:[0,1]\n\t"
"v_pk_fma_f32 v[76:77], v[172:173], s[90:91], v[76:77] neg_lo:[1,0,0] neg_hi:[1,0,0]\n\t"
"v_pk_fma_f32 v[78:79], v[174:175], s[90:91], v[78:79] neg_lo:[1,0,0] neg_hi:[1,0,0]\n\t"
"v_pk_add_f32 v[156:157], v[60:61], v[156:157] neg_lo:[0,1] neg_hi:[0,1]\n\t"
"v_pk_add_f32 v[158:159], v[62:63], v[158:159] neg_lo:[0,1] neg_hi:[0,1]\n\t"
"v_pk_fma_f32 v[60:61], v[156:157], s[90:91], v[60:61] neg_lo:[1,0,0] neg_hi:[1,0,0]\n\t"
"v_pk_fma_f32 v[62:63], v[158:159], s[90:91], v[62:63] neg_lo:[1,0,0] neg_hi:[1,0,0]\n\t"
"v_mfma_f32_16x16x32_bf16 v[208:211], a[216:219], a[192:195], 0\n\t"
"ds_read_b64_tr_b16 a[188:189], v249 offset:49024\n\t"
"v_mfma_f32_16x16x32_bf16 v[212:215], a[216:219], a[196:199], 0\n\t"
"s_waitcnt lgkmcnt(8)\n\t"
"ds_read_b64_tr_b16 a[192:193], v250 offset:48000\n\t"
"v_mfma_f32_16x16x32_bf16 v[216:219], a[216:219], a[200:203], 0\n\t"
"ds_read_b64_tr_b16 a[194:195], v250 offset:48512\n\t"
"v_mfma_f32_16x16x32_bf16 v[220:223], a[216:219], a[204:207], 0\n\t"
"s_waitcnt vmcnt(13)\n\t"
"ds_read_b64_tr_b16 a[196:197], v250 offset:49024\n\t"
"v_cvt_pk_bf16_f32 v193, v193, v197\n\t"
"v_mfma_f32_16x16x32_bf16 v[48:51], a[0:3], a[144:147], v[48:51]\n\t"
"ds_read_b64_tr_b16 a[200:201], v251 offset:48000\n\t"
"v_cvt_pk_bf16_f32 v194, v194, v198\n\t"
"v_mfma_f32_16x16x32_bf16 v[48:51], a[4:7], a[160:163], v[48:51]\n\t"
"ds_write_addtid_b32 v193 offset:256\n\t"
"v_cvt_pk_bf16_f32 v192, v192, v196\n\t"
"v_mfma_f32_16x16x32_bf16 v[64:67], a[8:11], a[144:147], v[64:67]\n\t"
"ds_read_b64_tr_b16 a[202:203], v251 offset:48512\n\t"
"v_cvt_pk_bf16_f32 v200, v200, v204\n\t"
"v_mfma_f32_16x16x32_bf16 v[64:67], a[12:15], a[160:163], v[64:67]\n\t"
"ds_write_addtid_b32 v192 offset:0\n\t"
"v_cvt_pk_bf16_f32 v211, v211, v215\n\t"
"v_mfma_f32_16x16x32_bf16 v[80:83], a[16:19], a[144:147], v[80:83]\n\t"
"ds_write_addtid_b32 v211 offset:10528\n\t"
"v_cvt_pk_bf16_f32 v202, v202, v206\n\t"
"v_mfma_f32_16x16x32_bf16 v[80:83], a[20:23], a[160:163], v[80:83]\n\t"
"buffer_load_dwordx4 a[64:67], v252, s[84:87], s42 offen offset:0 sc1\n\t"
"v_mfma_f32_16x16x32_bf16 v[84:87], a[16:19], a[148:151], v[84:87]\n\t"
"ds_read_b128 a[160:163], v224 offset:22592\n\t"
"v_cvt_pk_bf16_f32 v217, v217, v221\n\t"
"v_mfma_f32_16x16x32_bf16 v[84:87], a[20:23], a[164:167], v[84:87]\n\t"
"ds_read_b128 a[144:147], v224 offset:18496\n\t"
"v_cvt_pk_bf16_f32 v208, v208, v212\n\t"
"v_mfma_f32_16x16x32_bf16 v[68:71], a[8:11], a[148:151], v[68:71]\n\t"
"ds_write_addtid_b32 v194 offset:8224\n\t"
"v_cvt_pk_bf16_f32 v203, v203, v207\n\t"
"v_mfma_f32_16x16x32_bf16 v[68:71], a[12:15], a[164:167], v[68:71]\n\t"
"buffer_load_dwordx4 a[68:71], v252, s[84:87], s42 offen offset:1024 sc1\n\t"
"v_mfma_f32_16x16x32_bf16 v[52:55], a[0:3], a[148:151], v[52:55]\n\t"
"s_xor_b32 s27, s27, 384\n\t"
"ds_write_addtid_b32 v203 offset:12576\n\t"
"ds_write_addtid_b32 v217 offset:6400\n\t"
"v_mfma_f32_16x16x32_bf16 v[52:55], a[4:7], a[164:167], v[52:55]\n\t"
"ds_read_b128 a[164:167], v224 offset:23104\n\t"
"v_cvt_pk_bf16_f32 v209, v209, v213\n\t"
"v_mfma_f32_16x16x32_bf16 v[56:59], a[0:3], a[152:155], v[56:59]\n\t"
"ds_read_b128 a[148:151], v224 offset:19008\n\t"
"v_cvt_pk_bf16_f32 v219, v219, v223\n\t"
"v_mfma_f32_16x16x32_bf16 v[56:59], a[4:7], a[168:171], v[56:59]\n\t"
"buffer_load_dwordx4 a[60:63], v235, s[84:87], s42 offen offset:3072 sc1\n\t"
"v_mfma_f32_16x16x32_bf16 v[72:75], a[8:11], a[152:155], v[72:75]\n\t"
"ds_write_addtid_b32 v219 offset:14624\n\t"
"v_cvt_pk_bf16_f32 v216, v216, v220\n\t"
"v_mfma_f32_16x16x32_bf16 v[72:75], a[12:15], a[168:171], v[72:75]\n\t"
"ds_write_addtid_b32 v216 offset:6144\n\t"
"v_cvt_pk_bf16_f32 v218, v218, v222\n\t"
"v_mfma_f32_16x16x32_bf16 v[88:91], a[16:19], a[152:155], v[88:91]\n\t"
"ds_write_addtid_b32 v208 offset:2048\n\t"
"v_cvt_pk_bf16_f32 v195, v195, v199\n\t"
"v_mfma_f32_16x16x32_bf16 v[88:91], a[20:23], a[168:171], v[88:91]\n\t"
"s_waitcnt lgkmcnt(8)\n\t"
"buffer_load_dwordx4 a[56:59], v235, s[84:87], s42 offen offset:2048 sc1\n\t"
"v_mfma_f32_16x16x32_bf16 v[92:95], a[16:19], a[156:159], v[92:95]\n\t"
"ds_write_addtid_b32 v200 offset:4096\n\t"
"v_cvt_pk_bf16_f32 v210, v210, v214\n\t"
"v_mfma_f32_16x16x32_bf16 v[92:95], a[20:23], a[172:175], v[92:95]\n\t"
"s_xor_b32 s25, s25, 384\n\t"
"ds_write_addtid_b32 v202 offset:12320\n\t"
"ds_write_addtid_b32 v210 offset:10272\n\t"
"v_mfma_f32_16x16x32_bf16 v[76:79], a[8:11], a[156:159], v[76:79]\n\t"
"ds_write_addtid_b32 v218 offset:14368\n\t"
"v_cvt_pk_bf16_f32 v201, v201, v205\n\t"
"v_mfma_f32_16x16x32_bf16 v[76:79], a[12:15], a[172:175], v[76:79]\n\t"
"buffer_load_dwordx4 a[48:51], v235, s[84:87], s42 offen offset:0 sc1\n\t"
"v_mfma_f32_16x16x32_bf16 v[60:63], a[0:3], a[156:159], v[60:63]\n\t"
"ds_read_b128 a[152:155], v224 offset:19520\n\t"
"v_mfma_f32_16x16x32_bf16 v[60:63], a[4:7], a[172:175], v[60:63]\n\t"
"s_waitcnt vmcnt(12) lgkmcnt(8)\n\t"
"ds_write_addtid_b32 v209 offset:2304\n\t"
"v_mfma_f32_16x16x32_bf16 v[144:147], a[24:27], a[144:147], v[144:147]\n\t"
"ds_write_addtid_b32 v195 offset:8480\n\t"
"v_mfma_f32_16x16x32_bf16 v[144:147], a[28:31], a[160:163], v[144:147]\n\t"
"buffer_load_dwordx4 a[52:55], v235, s[84:87], s42 offen offset:1024 sc1\n\t"
"v_mfma_f32_16x16x32_bf16 v[160:163], a[32:35], a[144:147], v[160:163]\n\t"
"ds_write_addtid_b32 v201 offset:4352\n\t"
"v_mfma_f32_16x16x32_bf16 v[160:163], a[36:39], a[160:163], v[160:163]\n\t"
"ds_read_b128 a[156:159], v224 offset:20032\n\t"
"v_mfma_f32_16x16x32_bf16 v[176:179], a[40:43], a[144:147], v[176:179]\n\t"
"ds_read_b128 a[168:171], v224 offset:23616\n\t"
"v_mfma_f32_16x16x32_bf16 v[176:179], a[44:47], a[160:163], v[176:179]\n\t"
"buffer_load_dwordx4 a[88:91], v252, s[84:87], s48 offen offset:0 sc1\n\t"
"v_mfma_f32_16x16x32_bf16 v[180:183], a[40:43], a[148:151], v[180:183]\n\t"
"ds_read_b128 a[172:175], v224 offset:24128\n\t"
"v_mfma_f32_16x16x32_bf16 v[180:183], a[44:47], a[164:167], v[180:183]\n\t"
"v_mfma_f32_16x16x32_bf16 v[164:167], a[32:35], a[148:151], v[164:167]\n\t"
"ds_read_b64_tr_b16 a[204:205], v251 offset:49024\n\t"
"v_mfma_f32_16x16x32_bf16 v[164:167], a[36:39], a[164:167], v[164:167]\n\t"
"buffer_load_dwordx4 a[92:95], v252, s[84:87], s48 offen offset:1024 sc1\n\t"
"v_mfma_f32_16x16x32_bf16 v[148:151], a[24:27], a[148:151], v[148:151]\n\t"
"v_mfma_f32_16x16x32_bf16 v[148:151], a[28:31], a[164:167], v[148:151]\n\t"
"s_waitcnt lgkmcnt(0)\n\t"
"s_barrier\n\t"
"v_mfma_f32_16x16x32_bf16 v[152:155], a[24:27], a[152:155], v[152:155]\n\t"
"s_nop 0\n\t"
"ds_read_b128 a[144:147], v224 offset:0\n\t"
"ds_read_b128 a[148:151], v224 offset:512\n\t"
"v_mfma_f32_16x16x32_bf16 v[152:155], a[28:31], a[168:171], v[152:155]\n\t"
"buffer_load_dwordx4 a[84:87], v235, s[84:87], s48 offen offset:3072 sc1\n\t"
"v_mfma_f32_16x16x32_bf16 v[168:171], a[32:35], a[152:155], v[168:171]\n\t"
"ds_read_b128 a[160:163], v224 offset:4096\n\t"
"v_mfma_f32_16x16x32_bf16 v[168:171], a[36:39], a[168:171], v[168:171]\n\t"
"ds_read_b128 a[164:167], v224 offset:4608\n\t"
"v_mfma_f32_16x16x32_bf16 v[184:187], a[40:43], a[152:155], v[184:187]\n\t"
"ds_read_b128 a[152:155], v224 offset:1024\n\t"
"v_mfma_f32_16x16x32_bf16 v[184:187], a[44:47], a[168:171], v[184:187]\n\t"
"buffer_load_dwordx4 a[80:83], v235, s[84:87], s48 offen offset:2048 sc1\n\t"
"v_mfma_f32_16x16x32_bf16 v[188:191], a[40:43], a[156:159], v[188:191]\n\t"
"v_mfma_f32_16x16x32_bf16 v[188:191], a[44:47], a[172:175], v[188:191]\n\t"
"ds_write_b128 v255, a[240:243] offset:2304\n\t"
"v_mfma_f32_16x16x32_bf16 v[172:175], a[32:35], a[156:159], v[172:175]\n\t"
"v_mfma_f32_16x16x32_bf16 v[172:175], a[36:39], a[172:175], v[172:175]\n\t"
"buffer_load_dwordx4 a[72:75], v235, s[84:87], s48 offen offset:0 sc1\n\t"
"v_mfma_f32_16x16x32_bf16 v[156:159], a[24:27], a[156:159], v[156:159]\n\t"
"v_mfma_f32_16x16x32_bf16 v[156:159], a[28:31], a[172:175], v[156:159]\n\t"
"buffer_load_dwordx4 a[76:79], v235, s[84:87], s48 offen offset:1024 sc1\n\t"
"v_mfma_f32_16x16x32_bf16 v[192:195], a[220:223], a[176:179], 0\n\t"
"ds_read_b128 a[168:171], v224 offset:5120\n\t"
"v_mfma_f32_16x16x32_bf16 v[196:199], a[220:223], a[184:187], 0\n\t"
"s_nop 0\n\t"
"ds_read_b64_tr_b16 a[176:177], v248 offset:0\n\t"
"ds_read_b64_tr_b16 a[184:185], v249 offset:0\n\t"
"v_mfma_f32_16x16x32_bf16 v[200:203], a[220:223], a[178:181], 0\n\t"
"s_nop 0\n\t"
"ds_read_b128 a[156:159], v224 offset:1536\n\t"
"ds_read_b64_tr_b16 a[178:179], v248 offset:512\n\t"
"v_mfma_f32_16x16x32_bf16 v[204:207], a[220:223], a[186:189], 0\n\t"
"ds_read_b64_tr_b16 a[180:181], v248 offset:1024\n\t"
"v_mfma_f32_16x16x32_bf16 v[208:211], a[220:223], a[192:195], 0\n\t"
"ds_read_b128 a[172:175], v224 offset:5632\n\t"
"v_mfma_f32_16x16x32_bf16 v[212:215], a[220:223], a[200:203], 0\n\t"
"ds_read_b64_tr_b16 a[186:187], v249 offset:512\n\t"
"v_mfma_f32_16x16x32_bf16 v[216:219], a[220:223], a[194:197], 0\n\t"
"ds_read_b64_tr_b16 a[188:189], v249 offset:1024\n\t"
"v_mfma_f32_16x16x32_bf16 v[220:223], a[220:223], a[202:205], 0\n\t"
"s_waitcnt lgkmcnt(9)\n\t"
"ds_read_b64_tr_b16 a[192:193], v250 offset:0\n\t"
"v_cvt_pk_bf16_f32 v195, v195, v199\n\t"
"v_mfma_f32_16x16x32_bf16 v[0:3], a[96:99], a[144:147], v[0:3]\n\t"
"ds_read_b64_tr_b16 a[200:201], v251 offset:0\n\t"
"v_cvt_pk_bf16_f32 v193, v193, v197\n\t"
"v_mfma_f32_16x16x32_bf16 v[0:3], a[100:103], a[160:163], v[0:3]\n\t"
"ds_write_addtid_b32 v193 offset:24672\n\t"
"v_cvt_pk_bf16_f32 v202, v202, v206\n\t"
"v_mfma_f32_16x16x32_bf16 v[16:19], a[104:107], a[144:147], v[16:19]\n\t"
"ds_write_addtid_b32 v202 offset:18752\n\t"
"v_cvt_pk_bf16_f32 v200, v200, v204\n\t"
"v_mfma_f32_16x16x32_bf16 v[16:19], a[108:111], a[160:163], v[16:19]\n\t"
"ds_read_b64_tr_b16 a[194:195], v250 offset:512\n\t"
"v_cvt_pk_bf16_f32 v192, v192, v196\n\t"
"v_mfma_f32_16x16x32_bf16 v[32:35], a[112:115], a[144:147], v[32:35]\n\t"
"ds_read_b128 a[144:147], v224 offset:2048\n\t"
"v_cvt_pk_bf16_f32 v203, v203, v207\n\t"
"v_mfma_f32_16x16x32_bf16 v[32:35], a[116:119], a[160:163], v[32:35]\n\t"
"ds_read_b64_tr_b16 a[196:197], v250 offset:1024\n\t"
"v_cvt_pk_bf16_f32 v201, v201, v205\n\t"
"v_mfma_f32_16x16x32_bf16 v[36:39], a[112:115], a[148:151], v[36:39]\n\t"
"ds_write_addtid_b32 v200 offset:16704\n\t"
"v_cvt_pk_bf16_f32 v216, v216, v220\n\t"
"v_mfma_f32_16x16x32_bf16 v[36:39], a[116:119], a[164:167], v[36:39]\n\t"
"ds_read_b128 a[160:163], v224 offset:6144\n\t"
"v_cvt_pk_bf16_f32 v210, v210, v214\n\t"
"v_mfma_f32_16x16x32_bf16 v[20:23], a[104:107], a[148:151], v[20:23]\n\t"
"ds_write_addtid_b32 v210 offset:22592\n\t"
"v_cvt_pk_bf16_f32 v211, v211, v215\n\t"
"v_mfma_f32_16x16x32_bf16 v[20:23], a[108:111], a[164:167], v[20:23]\n\t"
"ds_write_addtid_b32 v195 offset:26720\n\t"
"v_cvt_pk_bf16_f32 v217, v217, v221\n\t"
"v_mfma_f32_16x16x32_bf16 v[4:7], a[96:99], a[148:151], v[4:7]\n\t"
"ds_write_addtid_b32 v203 offset:26976\n\t"
"v_cvt_pk_bf16_f32 v194, v194, v198\n\t"
"v_mfma_f32_16x16x32_bf16 v[4:7], a[100:103], a[164:167], v[4:7]\n\t"
"s_waitcnt lgkmcnt(9)\n\t"
"ds_read_b128 a[148:151], v224 offset:2560\n\t"
"ds_read_b128 a[164:167], v224 offset:6656\n\t"
"v_mfma_f32_16x16x32_bf16 v[8:11], a[96:99], a[152:155], v[8:11]\n\t"
"ds_write_addtid_b32 v192 offset:16448\n\t"
"v_cvt_pk_bf16_f32 v219, v219, v223\n\t"
"v_mfma_f32_16x16x32_bf16 v[8:11], a[100:103], a[168:171], v[8:11]\n\t"
"ds_write_addtid_b32 v217 offset:29024\n\t"
"v_cvt_pk_bf16_f32 v209, v209, v213\n\t"
"v_mfma_f32_16x16x32_bf16 v[24:27], a[104:107], a[152:155], v[24:27]\n\t"
"ds_write_addtid_b32 v211 offset:30816\n\t"
"v_cvt_pk_bf16_f32 v208, v208, v212\n\t"
"v_mfma_f32_16x16x32_bf16 v[24:27], a[108:111], a[168:171], v[24:27]\n\t"
"ds_write_addtid_b32 v219 offset:31072\n\t"
"v_cvt_pk_bf16_f32 v218, v218, v222\n\t"
"v_mfma_f32_16x16x32_bf16 v[40:43], a[112:115], a[152:155], v[40:43]\n\t"
"ds_write_addtid_b32 v209 offset:28768\n\t"
"v_mfma_f32_16x16x32_bf16 v[40:43], a[116:119], a[168:171], v[40:43]\n\t"
"ds_write_addtid_b32 v201 offset:24928\n\t"
"v_mfma_f32_16x16x32_bf16 v[44:47], a[112:115], a[156:159], v[44:47]\n\t"
"ds_read_b128 a[168:171], v224 offset:7168\n\t"
"v_mfma_f32_16x16x32_bf16 v[44:47], a[116:119], a[172:175], v[44:47]\n\t"
"buffer_load_dwordx4 a[112:115], v252, s[84:87], s75 offen offset:0 sc1\n\t"
"v_mfma_f32_16x16x32_bf16 v[28:31], a[104:107], a[156:159], v[28:31]\n\t"
"ds_write_addtid_b32 v218 offset:22848\n\t"
"v_mfma_f32_16x16x32_bf16 v[28:31], a[108:111], a[172:175], v[28:31]\n\t"
"buffer_load_dwordx4 a[116:119], v252, s[84:87], s75 offen offset:1024 sc1\n\t"
"v_mfma_f32_16x16x32_bf16 v[12:15], a[96:99], a[156:159], v[12:15]\n\t"
"ds_write_addtid_b32 v216 offset:20800\n\t"
"v_mfma_f32_16x16x32_bf16 v[12:15], a[100:103], a[172:175], v[12:15]\n\t"
"s_waitcnt lgkmcnt(7)\n\t"
"buffer_load_dwordx4 a[108:111], v235, s[84:87], s75 offen offset:3072 sc1\n\t"
"v_mfma_f32_16x16x32_bf16 v[96:99], a[120:123], a[144:147], v[96:99]\n\t"
"ds_write_addtid_b32 v194 offset:18496\n\t"
"v_mfma_f32_16x16x32_bf16 v[96:99], a[124:127], a[160:163], v[96:99]\n\t"
"buffer_load_dwordx4 a[104:107], v235, s[84:87], s75 offen offset:2048 sc1\n\t"
"v_mfma_f32_16x16x32_bf16 v[112:115], a[128:131], a[144:147], v[112:115]\n\t"
"ds_write_addtid_b32 v208 offset:20544\n\t"
"v_mfma_f32_16x16x32_bf16 v[112:115], a[132:135], a[160:163], v[112:115]\n\t"
"buffer_load_dwordx4 a[96:99], v235, s[84:87], s75 offen offset:0 sc1\n\t"
"v_mfma_f32_16x16x32_bf16 v[128:131], a[136:139], a[144:147], v[128:131]\n\t"
"ds_read_b128 a[152:155], v224 offset:3072\n\t"
"v_mfma_f32_16x16x32_bf16 v[128:131], a[140:143], a[160:163], v[128:131]\n\t"
"ds_read_b128 a[172:175], v224 offset:7680\n\t"
"v_mfma_f32_16x16x32_bf16 v[132:135], a[136:139], a[148:151], v[132:135]\n\t"
"ds_read_b128 a[156:159], v224 offset:3584\n\t"
"v_mfma_f32_16x16x32_bf16 v[132:135], a[140:143], a[164:167], v[132:135]\n\t"
"ds_read_b64_tr_b16 a[202:203], v251 offset:512\n\t"
"v_mfma_f32_16x16x32_bf16 v[116:119], a[128:131], a[148:151], v[116:119]\n\t"
"ds_read_b64_tr_b16 a[204:205], v251 offset:1024\n\t"
"v_mfma_f32_16x16x32_bf16 v[116:119], a[132:135], a[164:167], v[116:119]\n\t"
"buffer_load_dwordx4 a[100:103], v235, s[84:87], s75 offen offset:1024 sc1\n\t"
"v_mfma_f32_16x16x32_bf16 v[100:103], a[120:123], a[148:151], v[100:103]\n\t"
"v_mfma_f32_16x16x32_bf16 v[100:103], a[124:127], a[164:167], v[100:103]\n\t"
"s_waitcnt lgkmcnt(0)\n\t"
"s_barrier\n\t"
"v_mfma_f32_16x16x32_bf16 v[104:107], a[120:123], a[152:155], v[104:107]\n\t"
"ds_read_b128 a[160:163], v224 offset:20544\n\t"
"v_add_u32 v248, 80896, v226\n\t"
"v_mfma_f32_16x16x32_bf16 v[104:107], a[124:127], a[168:171], v[104:107]\n\t"
"ds_read_b128 a[144:147], v224 offset:16448\n\t"
"v_mfma_f32_16x16x32_bf16 v[120:123], a[128:131], a[152:155], v[120:123]\n\t"
"ds_read_b128 a[148:151], v224 offset:16960\n\t"
"v_xor_b32 v249, 32, v248\n\t"
"v_mfma_f32_16x16x32_bf16 v[120:123], a[132:135], a[168:171], v[120:123]\n\t"
"ds_read_b128 a[164:167], v224 offset:21056\n\t"
"v_xor_b32 v250, 64, v248\n\t"
"v_mfma_f32_16x16x32_bf16 v[136:139], a[136:139], a[152:155], v[136:139]\n\t"
"ds_read_b128 a[152:155], v224 offset:17472\n\t"
"v_xor_b32 v251, 96, v248\n\t"
"v_mfma_f32_16x16x32_bf16 v[136:139], a[140:143], a[168:171], v[136:139]\n\t"
"v_mfma_f32_16x16x32_bf16 v[140:143], a[136:139], a[156:159], v[140:143]\n\t"
"ds_read_b128 a[168:171], v224 offset:21568\n\t"
"v_mfma_f32_16x16x32_bf16 v[140:143], a[140:143], a[172:175], v[140:143]\n\t"
"buffer_load_dwordx4 a[136:139], v252, s[84:87], s78 offen offset:0 sc1\n\t"
"v_mfma_f32_16x16x32_bf16 v[124:127], a[128:131], a[156:159], v[124:127]\n\t"
"v_mfma_f32_16x16x32_bf16 v[124:127], a[132:135], a[172:175], v[124:127]\n\t"
"buffer_load_dwordx4 a[140:143], v252, s[84:87], s78 offen offset:1024 sc1\n\t"
"v_mfma_f32_16x16x32_bf16 v[108:111], a[120:123], a[156:159], v[108:111]\n\t"
"ds_read_b128 a[156:159], v224 offset:17984\n\t"
"v_mfma_f32_16x16x32_bf16 v[108:111], a[124:127], a[172:175], v[108:111]\n\t"
"buffer_load_dwordx4 a[132:135], v235, s[84:87], s78 offen offset:3072 sc1\n\t"
"v_mfma_f32_16x16x32_bf16 v[192:195], a[220:223], a[176:179], 0\n\t"
"ds_read_b64_tr_b16 a[176:177], v248 offset:41472\n\t"
"v_mfma_f32_16x16x32_bf16 v[196:199], a[220:223], a[184:187], 0\n\t"
"buffer_load_dwordx4 a[128:131], v235, s[84:87], s78 offen offset:2048 sc1\n\t"
"v_mfma_f32_16x16x32_bf16 v[200:203], a[220:223], a[178:181], 0\n\t"
"ds_read_b64_tr_b16 a[184:185], v249 offset:41472\n\t"
"v_mfma_f32_16x16x32_bf16 v[204:207], a[220:223], a[186:189], 0\n\t"
".p2align 3\n\t"
"buffer_load_dwordx4 a[120:123], v235, s[84:87], s78 offen offset:0 sc1\n\t"
"v_pk_add_f32 v[96:97], v[0:1], v[96:97] neg_lo:[0,1] neg_hi:[0,1]\n\t"
"v_pk_add_f32 v[98:99], v[2:3], v[98:99] neg_lo:[0,1] neg_hi:[0,1]\n\t"
"v_pk_fma_f32 v[0:1], v[96:97], s[90:91], v[0:1] neg_lo:[1,0,0] neg_hi:[1,0,0]\n\t"
"v_pk_fma_f32 v[2:3], v[98:99], s[90:91], v[2:3] neg_lo:[1,0,0] neg_hi:[1,0,0]\n\t"
"v_pk_add_f32 v[112:113], v[16:17], v[112:113] neg_lo:[0,1] neg_hi:[0,1]\n\t"
"v_pk_add_f32 v[114:115], v[18:19], v[114:115] neg_lo:[0,1] neg_hi:[0,1]\n\t"
"v_pk_fma_f32 v[16:17], v[112:113], s[90:91], v[16:17] neg_lo:[1,0,0] neg_hi:[1,0,0]\n\t"
"v_pk_fma_f32 v[18:19], v[114:115], s[90:91], v[18:19] neg_lo:[1,0,0] neg_hi:[1,0,0]\n\t"
"v_pk_add_f32 v[128:129], v[32:33], v[128:129] neg_lo:[0,1] neg_hi:[0,1]\n\t"
"v_pk_add_f32 v[130:131], v[34:35], v[130:131] neg_lo:[0,1] neg_hi:[0,1]\n\t"
"v_pk_fma_f32 v[32:33], v[128:129], s[90:91], v[32:33] neg_lo:[1,0,0] neg_hi:[1,0,0]\n\t"
"v_pk_fma_f32 v[34:35], v[130:131], s[90:91], v[34:35] neg_lo:[1,0,0] neg_hi:[1,0,0]\n\t"
"v_pk_add_f32 v[132:133], v[36:37], v[132:133] neg_lo:[0,1] neg_hi:[0,1]\n\t"
"v_pk_add_f32 v[134:135], v[38:39], v[134:135] neg_lo:[0,1] neg_hi:[0,1]\n\t"
"v_pk_fma_f32 v[36:37], v[132:133], s[90:91], v[36:37] neg_lo:[1,0,0] neg_hi:[1,0,0]\n\t"
"v_pk_fma_f32 v[38:39], v[134:135], s[90:91], v[38:39] neg_lo:[1,0,0] neg_hi:[1,0,0]\n\t"
"v_pk_add_f32 v[116:117], v[20:21], v[116:117] neg_lo:[0,1] neg_hi:[0,1]\n\t"
"v_pk_add_f32 v[118:119], v[22:23], v[118:119] neg_lo:[0,1] neg_hi:[0,1]\n\t"
"v_pk_fma_f32 v[20:21], v[116:117], s[90:91], v[20:21] neg_lo:[1,0,0] neg_hi:[1,0,0]\n\t"
"v_pk_fma_f32 v[22:23], v[118:119], s[90:91], v[22:23] neg_lo:[1,0,0] neg_hi:[1,0,0]\n\t"
"v_pk_add_f32 v[100:101], v[4:5], v[100:101] neg_lo:[0,1] neg_hi:[0,1]\n\t"
"v_pk_add_f32 v[102:103], v[6:7], v[102:103] neg_lo:[0,1] neg_hi:[0,1]\n\t"
"v_pk_fma_f32 v[4:5], v[100:101], s[90:91], v[4:5] neg_lo:[1,0,0] neg_hi:[1,0,0]\n\t"
"v_pk_fma_f32 v[6:7], v[102:103], s[90:91], v[6:7] neg_lo:[1,0,0] neg_hi:[1,0,0]\n\t"
"v_pk_add_f32 v[104:105], v[8:9], v[104:105] neg_lo:[0,1] neg_hi:[0,1]\n\t"
"v_pk_add_f32 v[106:107], v[10:11], v[106:107] neg_lo:[0,1] neg_hi:[0,1]\n\t"
"v_pk_fma_f32 v[8:9], v[104:105], s[90:91], v[8:9] neg_lo:[1,0,0] neg_hi:[1,0,0]\n\t"
"v_pk_fma_f32 v[10:11], v[106:107], s[90:91], v[10:11] neg_lo:[1,0,0] neg_hi:[1,0,0]\n\t"
"v_pk_add_f32 v[120:121], v[24:25], v[120:121] neg_lo:[0,1] neg_hi:[0,1]\n\t"
"v_pk_add_f32 v[122:123], v[26:27], v[122:123] neg_lo:[0,1] neg_hi:[0,1]\n\t"
"v_pk_fma_f32 v[24:25], v[120:121], s[90:91], v[24:25] neg_lo:[1,0,0] neg_hi:[1,0,0]\n\t"
"v_pk_fma_f32 v[26:27], v[122:123], s[90:91], v[26:27] neg_lo:[1,0,0] neg_hi:[1,0,0]\n\t"
"v_pk_add_f32 v[136:137], v[40:41], v[136:137] neg_lo:[0,1] neg_hi:[0,1]\n\t"
"v_pk_add_f32 v[138:139], v[42:43], v[138:139] neg_lo:[0,1] neg_hi:[0,1]\n\t"
"v_pk_fma_f32 v[40:41], v[136:137], s[90:91], v[40:41] neg_lo:[1,0,0] neg_hi:[1,0,0]\n\t"
"v_pk_fma_f32 v[42:43], v[138:139], s[90:91], v[42:43] neg_lo:[1,0,0] neg_hi:[1,0,0]\n\t"
"v_pk_add_f32 v[140:141], v[44:45], v[140:141] neg_lo:[0,1] neg_hi:[0,1]\n\t"
"v_pk_add_f32 v[142:143], v[46:47], v[142:143] neg_lo:[0,1] neg_hi:[0,1]\n\t"
"v_pk_fma_f32 v[44:45], v[140:141], s[90:91], v[44:45] neg_lo:[1,0,0] neg_hi:[1,0,0]\n\t"
"v_pk_fma_f32 v[46:47], v[142:143], s[90:91], v[46:47] neg_lo:[1,0,0] neg_hi:[1,0,0]\n\t"
"v_pk_add_f32 v[124:125], v[28:29], v[124:125] neg_lo:[0,1] neg_hi:[0,1]\n\t"
"v_pk_add_f32 v[126:127], v[30:31], v[126:127] neg_lo:[0,1] neg_hi:[0,1]\n\t"
"v_pk_fma_f32 v[28:29], v[124:125], s[90:91], v[28:29] neg_lo:[1,0,0] neg_hi:[1,0,0]\n\t"
"v_pk_fma_f32 v[30:31], v[126:127], s[90:91], v[30:31] neg_lo:[1,0,0] neg_hi:[1,0,0]\n\t"
"v_pk_add_f32 v[108:109], v[12:13], v[108:109] neg_lo:[0,1] neg_hi:[0,1]\n\t"
"v_pk_add_f32 v[110:111], v[14:15], v[110:111] neg_lo:[0,1] neg_hi:[0,1]\n\t"
"v_pk_fma_f32 v[12:13], v[108:109], s[90:91], v[12:13] neg_lo:[1,0,0] neg_hi:[1,0,0]\n\t"
"v_pk_fma_f32 v[14:15], v[110:111], s[90:91], v[14:15] neg_lo:[1,0,0] neg_hi:[1,0,0]\n\t"
"v_mfma_f32_16x16x32_bf16 v[208:211], a[220:223], a[192:195], 0\n\t"
"buffer_load_dwordx4 a[124:127], v235, s[84:87], s78 offen offset:1024 sc1\n\t"
"v_mfma_f32_16x16x32_bf16 v[212:215], a[220:223], a[200:203], 0\n\t"
"ds_read_b64_tr_b16 a[178:179], v248 offset:41984\n\t"
"v_mfma_f32_16x16x32_bf16 v[216:219], a[220:223], a[194:197], 0\n\t"
"ds_read_b64_tr_b16 a[180:181], v248 offset:42496\n\t"
"v_mfma_f32_16x16x32_bf16 v[220:223], a[220:223], a[202:205], 0\n\t"
"s_waitcnt vmcnt(18) lgkmcnt(5)\n\t"
"ds_read_b64_tr_b16 a[186:187], v249 offset:41984\n\t"
"v_cvt_pk_bf16_f32 v193, v193, v197\n\t"
"v_mfma_f32_16x16x32_bf16 v[48:51], a[48:51], a[144:147], v[48:51]\n\t"
"ds_read_b128 a[172:175], v224 offset:22080\n\t"
"v_cvt_pk_bf16_f32 v192, v192, v196\n\t"
"v_mfma_f32_16x16x32_bf16 v[48:51], a[52:55], a[160:163], v[48:51]\n\t"
"ds_write_addtid_b32 v192 offset:0\n\t"
"v_cvt_pk_bf16_f32 v201, v201, v205\n\t"
"v_mfma_f32_16x16x32_bf16 v[64:67], a[56:59], a[144:147], v[64:67]\n\t"
"ds_read_b64_tr_b16 a[188:189], v249 offset:42496\n\t"
"v_cvt_pk_bf16_f32 v200, v200, v204\n\t"
"v_mfma_f32_16x16x32_bf16 v[64:67], a[60:63], a[160:163], v[64:67]\n\t"
"ds_read_b64_tr_b16 a[192:193], v250 offset:41472\n\t"
"v_cvt_pk_bf16_f32 v211, v211, v215\n\t"
"v_mfma_f32_16x16x32_bf16 v[80:83], a[64:67], a[144:147], v[80:83]\n\t"
"ds_write_addtid_b32 v200 offset:256\n\t"
"v_cvt_pk_bf16_f32 v209, v209, v213\n\t"
"v_mfma_f32_16x16x32_bf16 v[80:83], a[68:71], a[160:163], v[80:83]\n\t"
"ds_read_b128 a[144:147], v224 offset:18496\n\t"
"v_cvt_pk_bf16_f32 v203, v203, v207\n\t"
"v_mfma_f32_16x16x32_bf16 v[84:87], a[64:67], a[148:151], v[84:87]\n\t"
"ds_write_addtid_b32 v201 offset:8480\n\t"
"v_cvt_pk_bf16_f32 v216, v216, v220\n\t"
"v_mfma_f32_16x16x32_bf16 v[84:87], a[68:71], a[164:167], v[84:87]\n\t"
"ds_read_b128 a[160:163], v224 offset:22592\n\t"
"v_cvt_pk_bf16_f32 v194, v194, v198\n\t"
"v_mfma_f32_16x16x32_bf16 v[68:71], a[56:59], a[148:151], v[68:71]\n\t"
"ds_write_addtid_b32 v211 offset:14368\n\t"
"v_cvt_pk_bf16_f32 v217, v217, v221\n\t"
"v_mfma_f32_16x16x32_bf16 v[68:71], a[60:63], a[164:167], v[68:71]\n\t"
"ds_write_addtid_b32 v217 offset:12576\n\t"
"v_cvt_pk_bf16_f32 v208, v208, v212\n\t"
"v_mfma_f32_16x16x32_bf16 v[52:55], a[48:51], a[148:151], v[52:55]\n\t"
"ds_read_b128 a[148:151], v224 offset:19008\n\t"
"v_cvt_pk_bf16_f32 v195, v195, v199\n\t"
"v_mfma_f32_16x16x32_bf16 v[52:55], a[52:55], a[164:167], v[52:55]\n\t"
"ds_read_b128 a[164:167], v224 offset:23104\n\t"
"v_cvt_pk_bf16_f32 v218, v218, v222\n\t"
"v_mfma_f32_16x16x32_bf16 v[56:59], a[48:51], a[152:155], v[56:59]\n\t"
"ds_write_addtid_b32 v209 offset:12320\n\t"
"v_cvt_pk_bf16_f32 v219, v219, v223\n\t"
"v_mfma_f32_16x16x32_bf16 v[56:59], a[52:55], a[168:171], v[56:59]\n\t"
"ds_read_b64_tr_b16 a[200:201], v251 offset:41472\n\t"
"v_cvt_pk_bf16_f32 v210, v210, v214\n\t"
"v_mfma_f32_16x16x32_bf16 v[72:75], a[56:59], a[152:155], v[72:75]\n\t"
"ds_write_addtid_b32 v216 offset:4352\n\t"
"v_cvt_pk_bf16_f32 v202, v202, v206\n\t"
"v_mfma_f32_16x16x32_bf16 v[72:75], a[60:63], a[168:171], v[72:75]\n\t"
"ds_write_addtid_b32 v193 offset:8224\n\t"
"v_mfma_f32_16x16x32_bf16 v[88:91], a[64:67], a[152:155], v[88:91]\n\t"
"ds_read_b64_tr_b16 a[194:195], v250 offset:41984\n\t"
"v_mfma_f32_16x16x32_bf16 v[88:91], a[68:71], a[168:171], v[88:91]\n\t"
"s_waitcnt lgkmcnt(9)\n\t"
"ds_write_addtid_b32 v219 offset:14624\n\t"
"v_mfma_f32_16x16x32_bf16 v[92:95], a[64:67], a[156:159], v[92:95]\n\t"
"ds_write_addtid_b32 v210 offset:6144\n\t"
"v_mfma_f32_16x16x32_bf16 v[92:95], a[68:71], a[172:175], v[92:95]\n\t"
"ds_read_b64_tr_b16 a[196:197], v250 offset:42496\n\t"
"v_mfma_f32_16x16x32_bf16 v[76:79], a[56:59], a[156:159], v[76:79]\n\t"
"ds_write_addtid_b32 v202 offset:2304\n\t"
"v_mfma_f32_16x16x32_bf16 v[76:79], a[60:63], a[172:175], v[76:79]\n\t"
"ds_write_addtid_b32 v203 offset:10528\n\t"
"v_mfma_f32_16x16x32_bf16 v[60:63], a[48:51], a[156:159], v[60:63]\n\t"
"ds_write_addtid_b32 v208 offset:4096\n\t"
"v_mfma_f32_16x16x32_bf16 v[60:63], a[52:55], a[172:175], v[60:63]\n\t"
"s_waitcnt vmcnt(12) lgkmcnt(9)\n\t"
"ds_write_addtid_b32 v194 offset:2048\n\t"
"v_mfma_f32_16x16x32_bf16 v[144:147], a[72:75], a[144:147], v[144:147]\n\t"
"ds_read_b128 a[172:175], v224 offset:24128\n\t"
"v_mfma_f32_16x16x32_bf16 v[144:147], a[76:79], a[160:163], v[144:147]\n\t"
"ds_write_addtid_b32 v218 offset:6400\n\t"
"v_mfma_f32_16x16x32_bf16 v[160:163], a[80:83], a[144:147], v[160:163]\n\t"
"ds_write_addtid_b32 v195 offset:10272\n\t"
"v_mfma_f32_16x16x32_bf16 v[160:163], a[84:87], a[160:163], v[160:163]\n\t"
"ds_read_b128 a[152:155], v224 offset:19520\n\t"
"v_mfma_f32_16x16x32_bf16 v[176:179], a[88:91], a[144:147], v[176:179]\n\t"
"ds_read_b128 a[156:159], v224 offset:20032\n\t"
"v_mfma_f32_16x16x32_bf16 v[176:179], a[92:95], a[160:163], v[176:179]\n\t"
"ds_read_b128 a[168:171], v224 offset:23616\n\t"
"v_mfma_f32_16x16x32_bf16 v[180:183], a[88:91], a[148:151], v[180:183]\n\t"
"ds_read_b64_tr_b16 a[202:203], v251 offset:41984\n\t"
"v_mfma_f32_16x16x32_bf16 v[180:183], a[92:95], a[164:167], v[180:183]\n\t"
"ds_read_b64_tr_b16 a[204:205], v251 offset:42496\n\t"
"v_mfma_f32_16x16x32_bf16 v[164:167], a[80:83], a[148:151], v[164:167]\n\t"
"v_mfma_f32_16x16x32_bf16 v[164:167], a[84:87], a[164:167], v[164:167]\n\t"
"v_mfma_f32_16x16x32_bf16 v[148:151], a[72:75], a[148:151], v[148:151]\n\t"
"s_waitcnt lgkmcnt(2)\n\t"
"v_mfma_f32_16x16x32_bf16 v[148:151], a[76:79], a[164:167], v[148:151]\n\t"
"s_barrier\n\t"
"v_mfma_f32_16x16x32_bf16 v[152:155], a[72:75], a[152:155], v[152:155]\n\t"
"s_add_u32 m0, s26, 103936\n\t"
"ds_read_b128 a[144:147], v224 offset:0\n\t"
"ds_read_b128 a[160:163], v224 offset:4096\n\t"
"v_mfma_f32_16x16x32_bf16 v[152:155], a[76:79], a[168:171], v[152:155]\n\t"
"buffer_load_dwordx4 v236, s[12:15], s22 offen offset:0 nt lds\n\t"
"v_mfma_f32_16x16x32_bf16 v[168:171], a[80:83], a[152:155], v[168:171]\n\t"
"ds_read_b128 a[148:151], v224 offset:512\n\t"
"v_mfma_f32_16x16x32_bf16 v[168:171], a[84:87], a[168:171], v[168:171]\n\t"
"ds_read_b128 a[164:167], v224 offset:4608\n\t"
"v_mfma_f32_16x16x32_bf16 v[184:187], a[88:91], a[152:155], v[184:187]\n\t"
"ds_read_b128 a[152:155], v224 offset:1024\n\t"
"v_mfma_f32_16x16x32_bf16 v[184:187], a[92:95], a[168:171], v[184:187]\n\t"
"buffer_load_dwordx4 v237, s[12:15], s22 offen offset:1024 nt lds\n\t"
"v_mfma_f32_16x16x32_bf16 v[188:191], a[88:91], a[156:159], v[188:191]\n\t"
"ds_read_b128 a[168:171], v224 offset:5120\n\t"
"v_mfma_f32_16x16x32_bf16 v[188:191], a[92:95], a[172:175], v[188:191]\n\t"
"v_mfma_f32_16x16x32_bf16 v[172:175], a[80:83], a[156:159], v[172:175]\n\t"
"v_mfma_f32_16x16x32_bf16 v[172:175], a[84:87], a[172:175], v[172:175]\n\t"
"buffer_load_dwordx4 v238, s[12:15], s22 offen offset:2048 nt lds\n\t"
"v_mfma_f32_16x16x32_bf16 v[156:159], a[72:75], a[156:159], v[156:159]\n\t"
"ds_read_b128 a[156:159], v224 offset:1536\n\t"
"v_mfma_f32_16x16x32_bf16 v[156:159], a[76:79], a[172:175], v[156:159]\n\t"
"ds_read_b128 a[172:175], v224 offset:5632\n\t"
"v_mfma_f32_16x16x32_bf16 v[192:195], a[252:255], a[176:179], 0\n\t"
"ds_read_b64_tr_b16 a[176:177], v248 offset:0\n\t"
"v_mfma_f32_16x16x32_bf16 v[196:199], a[252:255], a[184:187], 0\n\t"
"buffer_load_dwordx4 v239, s[12:15], s22 offen offset:3072 nt lds\n\t"
"v_mfma_f32_16x16x32_bf16 v[200:203], a[252:255], a[178:181], 0\n\t"
"ds_read_b64_tr_b16 a[184:185], v249 offset:0\n\t"
"v_mfma_f32_16x16x32_bf16 v[204:207], a[252:255], a[186:189], 0\n\t"
"ds_read_b64_tr_b16 a[178:179], v248 offset:512\n\t"
"v_mfma_f32_16x16x32_bf16 v[208:211], a[252:255], a[192:195], 0\n\t"
"s_waitcnt lgkmcnt(5)\n\t"
"ds_read_b64_tr_b16 a[180:181], v248 offset:1024\n\t"
"v_mfma_f32_16x16x32_bf16 v[212:215], a[252:255], a[200:203], 0\n\t"
"s_mov_b64 exec, s[36:37]\n\t"
"buffer_load_dwordx4 v240, s[12:15], s22 offen offset:3584 nt lds\n\t"
"s_mov_b64 exec, -1\n\t"
"v_mfma_f32_16x16x32_bf16 v[216:219], a[252:255], a[194:197], 0\n\t"
"s_lshl_b32 m0, s73, 9\n\t"
"ds_read_b64_tr_b16 a[186:187], v249 offset:512\n\t"
"v_mfma_f32_16x16x32_bf16 v[220:223], a[252:255], a[202:205], 0\n\t"
"s_waitcnt vmcnt(11)\n\t"
"ds_read_b64_tr_b16 a[188:189], v249 offset:1024\n\t"
"v_cvt_pk_bf16_f32 v195, v195, v199\n\t"
"v_mfma_f32_16x16x32_bf16 v[48:51], a[96:99], a[144:147], v[48:51]\n\t"
"ds_read_b64_tr_b16 a[192:193], v250 offset:0\n\t"
"v_cvt_pk_bf16_f32 v193, v193, v197\n\t"
"v_mfma_f32_16x16x32_bf16 v[48:51], a[100:103], a[160:163], v[48:51]\n\t"
"ds_read_b64_tr_b16 a[200:201], v251 offset:0\n\t"
"v_cvt_pk_bf16_f32 v201, v201, v205\n\t"
"v_mfma_f32_16x16x32_bf16 v[64:67], a[104:107], a[144:147], v[64:67]\n\t"
"ds_read_b64_tr_b16 a[194:195], v250 offset:512\n\t"
"v_cvt_pk_bf16_f32 v200, v200, v204\n\t"
"v_mfma_f32_16x16x32_bf16 v[64:67], a[108:111], a[160:163], v[64:67]\n\t"
"ds_read_b64_tr_b16 a[196:197], v250 offset:1024\n\t"
"v_cvt_pk_bf16_f32 v202, v202, v206\n\t"
"v_mfma_f32_16x16x32_bf16 v[80:83], a[112:115], a[144:147], v[80:83]\n\t"
"ds_read_b128 a[144:147], v224 offset:2048\n\t"
"v_cvt_pk_bf16_f32 v192, v192, v196\n\t"
"v_mfma_f32_16x16x32_bf16 v[80:83], a[116:119], a[160:163], v[80:83]\n\t"
"ds_write_addtid_b32 v201 offset:24928\n\t"
"v_cvt_pk_bf16_f32 v211, v211, v215\n\t"
"v_mfma_f32_16x16x32_bf16 v[84:87], a[112:115], a[148:151], v[84:87]\n\t"
"ds_read_b128 a[160:163], v224 offset:6144\n\t"
"v_cvt_pk_bf16_f32 v209, v209, v213\n\t"
"v_mfma_f32_16x16x32_bf16 v[84:87], a[116:119], a[164:167], v[84:87]\n\t"
"ds_read_b64_tr_b16 a[202:203], v251 offset:512\n\t"
"v_cvt_pk_bf16_f32 v203, v203, v207\n\t"
"v_mfma_f32_16x16x32_bf16 v[68:71], a[104:107], a[148:151], v[68:71]\n\t"
"ds_write_addtid_b32 v200 offset:16704\n\t"
"v_cvt_pk_bf16_f32 v194, v194, v198\n\t"
"v_mfma_f32_16x16x32_bf16 v[68:71], a[108:111], a[164:167], v[68:71]\n\t"
"ds_write_addtid_b32 v203 offset:26976\n\t"
"v_cvt_pk_bf16_f32 v208, v208, v212\n\t"
"v_mfma_f32_16x16x32_bf16 v[52:55], a[96:99], a[148:151], v[52:55]\n\t"
"ds_write_addtid_b32 v209 offset:28768\n\t"
"v_cvt_pk_bf16_f32 v216, v216, v220\n\t"
"v_mfma_f32_16x16x32_bf16 v[52:55], a[100:103], a[164:167], v[52:55]\n\t"
"ds_write_addtid_b32 v216 offset:20800\n\t"
"v_cvt_pk_bf16_f32 v210, v210, v214\n\t"
"v_mfma_f32_16x16x32_bf16 v[56:59], a[96:99], a[152:155], v[56:59]\n\t"
"ds_read_b128 a[164:167], v224 offset:6656\n\t"
"v_cvt_pk_bf16_f32 v219, v219, v223\n\t"
"v_mfma_f32_16x16x32_bf16 v[56:59], a[100:103], a[168:171], v[56:59]\n\t"
"ds_read_b128 a[148:151], v224 offset:2560\n\t"
"v_mfma_f32_16x16x32_bf16 v[72:75], a[104:107], a[152:155], v[72:75]\n\t"
"ds_write_addtid_b32 v211 offset:30816\n\t"
"v_cvt_pk_bf16_f32 v217, v217, v221\n\t"
"v_mfma_f32_16x16x32_bf16 v[72:75], a[108:111], a[168:171], v[72:75]\n\t"
"ds_write_addtid_b32 v193 offset:24672\n\t"
"v_mfma_f32_16x16x32_bf16 v[88:91], a[112:115], a[152:155], v[88:91]\n\t"
"ds_write_addtid_b32 v208 offset:20544\n\t"
"v_cvt_pk_bf16_f32 v218, v218, v222\n\t"
"v_mfma_f32_16x16x32_bf16 v[88:91], a[116:119], a[168:171], v[88:91]\n\t"
"s_waitcnt lgkmcnt(9)\n\t"
"ds_write_addtid_b32 v217 offset:29024\n\t"
"v_mfma_f32_16x16x32_bf16 v[92:95], a[112:115], a[156:159], v[92:95]\n\t"
"ds_read_b64_tr_b16 a[204:205], v251 offset:1024\n\t"
"v_mfma_f32_16x16x32_bf16 v[92:95], a[116:119], a[172:175], v[92:95]\n\t"
"ds_write_addtid_b32 v192 offset:16448\n\t"
"v_mfma_f32_16x16x32_bf16 v[76:79], a[104:107], a[156:159], v[76:79]\n\t"
"ds_write_addtid_b32 v202 offset:18752\n\t"
"v_mfma_f32_16x16x32_bf16 v[76:79], a[108:111], a[172:175], v[76:79]\n\t"
"ds_write_addtid_b32 v218 offset:22848\n\t"
"v_mfma_f32_16x16x32_bf16 v[60:63], a[96:99], a[156:159], v[60:63]\n\t"
"ds_read_b128 a[168:171], v224 offset:7168\n\t"
"v_mfma_f32_16x16x32_bf16 v[60:63], a[100:103], a[172:175], v[60:63]\n\t"
"s_waitcnt vmcnt(5) lgkmcnt(9)\n\t"
"ds_write_addtid_b32 v210 offset:22592\n\t"
"v_mfma_f32_16x16x32_bf16 v[144:147], a[120:123], a[144:147], v[144:147]\n\t"
"ds_write_addtid_b32 v219 offset:31072\n\t"
"v_mfma_f32_16x16x32_bf16 v[144:147], a[124:127], a[160:163], v[144:147]\n\t"
"v_mfma_f32_16x16x32_bf16 v[160:163], a[128:131], a[144:147], v[160:163]\n\t"
"ds_write_addtid_b32 v195 offset:26720\n\t"
"v_mfma_f32_16x16x32_bf16 v[160:163], a[132:135], a[160:163], v[160:163]\n\t"
"ds_write_addtid_b32 v194 offset:18496\n\t"
"v_mfma_f32_16x16x32_bf16 v[176:179], a[136:139], a[144:147], v[176:179]\n\t"
"ds_read_b128 a[172:175], v224 offset:7680\n\t"
"v_mfma_f32_16x16x32_bf16 v[176:179], a[140:143], a[160:163], v[176:179]\n\t"
"ds_read_b128 a[152:155], v224 offset:3072\n\t"
"v_mfma_f32_16x16x32_bf16 v[180:183], a[136:139], a[148:151], v[180:183]\n\t"
"ds_read_b128 a[156:159], v224 offset:3584\n\t"
"v_mfma_f32_16x16x32_bf16 v[180:183], a[140:143], a[164:167], v[180:183]\n\t"
"v_mfma_f32_16x16x32_bf16 v[164:167], a[128:131], a[148:151], v[164:167]\n\t"
"v_mfma_f32_16x16x32_bf16 v[164:167], a[132:135], a[164:167], v[164:167]\n\t"
"v_mfma_f32_16x16x32_bf16 v[148:151], a[120:123], a[148:151], v[148:151]\n\t"
"s_waitcnt lgkmcnt(0)\n\t"
"v_mfma_f32_16x16x32_bf16 v[148:151], a[124:127], a[164:167], v[148:151]\n\t"
"s_barrier\n\t"
"v_mfma_f32_16x16x32_bf16 v[152:155], a[120:123], a[152:155], v[152:155]\n\t"
"ds_read_b128 a[148:151], v224 offset:16960\n\t"
"v_mfma_f32_16x16x32_bf16 v[152:155], a[124:127], a[168:171], v[152:155]\n\t"
"ds_read_b128 a[144:147], v224 offset:16448\n\t"
"v_mfma_f32_16x16x32_bf16 v[168:171], a[128:131], a[152:155], v[168:171]\n\t"
"ds_read_b128 a[160:163], v224 offset:20544\n\t"
"v_add_u32 v248, 32896, v226\n\t"
"v_mfma_f32_16x16x32_bf16 v[168:171], a[132:135], a[168:171], v[168:171]\n\t"
"ds_read_b128 a[164:167], v224 offset:21056\n\t"
"v_xor_b32 v249, 32, v248\n\t"
"v_mfma_f32_16x16x32_bf16 v[184:187], a[136:139], a[152:155], v[184:187]\n\t"
"ds_read_b128 a[152:155], v224 offset:17472\n\t"
"v_xor_b32 v250, 64, v248\n\t"
"v_mfma_f32_16x16x32_bf16 v[184:187], a[140:143], a[168:171], v[184:187]\n\t"
"v_mfma_f32_16x16x32_bf16 v[188:191], a[136:139], a[156:159], v[188:191]\n\t"
"ds_read_b128 a[168:171], v224 offset:21568\n\t"
"v_xor_b32 v251, 96, v248\n\t"
"v_mfma_f32_16x16x32_bf16 v[188:191], a[140:143], a[172:175], v[188:191]\n\t"
"v_mfma_f32_16x16x32_bf16 v[172:175], a[128:131], a[156:159], v[172:175]\n\t"
"v_mfma_f32_16x16x32_bf16 v[172:175], a[132:135], a[172:175], v[172:175]\n\t"
"v_mfma_f32_16x16x32_bf16 v[156:159], a[120:123], a[156:159], v[156:159]\n\t"
"ds_read_b128 a[156:159], v224 offset:17984\n\t"
"v_mfma_f32_16x16x32_bf16 v[156:159], a[124:127], a[172:175], v[156:159]\n\t"
"ds_read_b128 a[172:175], v224 offset:22080\n\t"
"v_mfma_f32_16x16x32_bf16 v[192:195], a[252:255], a[176:179], 0\n\t"
"ds_read_b64_tr_b16 a[176:177], v248 offset:0\n\t"
"v_mfma_f32_16x16x32_bf16 v[196:199], a[252:255], a[184:187], 0\n\t"
"ds_read_b64_tr_b16 a[184:185], v249 offset:0\n\t"
"v_mfma_f32_16x16x32_bf16 v[200:203], a[252:255], a[178:181], 0\n\t"
"s_nop 0\n\t"
"ds_read_b64_tr_b16 a[178:179], v248 offset:512\n\t"
"ds_read_b64_tr_b16 a[180:181], v248 offset:1024\n\t"
"v_mfma_f32_16x16x32_bf16 v[204:207], a[252:255], a[186:189], 0\n\t"
"s_nop 0\n\t"
".p2align 3\n\t"
"ds_read_b64_tr_b16 a[186:187], v249 offset:512\n\t"
"ds_read_b64_tr_b16 a[188:189], v249 offset:1024\n\t"
"v_pk_fma_f32 v[48:49], v[144:145], s[90:91], v[48:49]\n\t"
"v_pk_fma_f32 v[50:51], v[146:147], s[90:91], v[50:51]\n\t"
"v_pk_add_f32 v[144:145], v[48:49], v[144:145] neg_lo:[0,1] neg_hi:[0,1]\n\t"
"v_pk_add_f32 v[146:147], v[50:51], v[146:147] neg_lo:[0,1] neg_hi:[0,1]\n\t"
"v_pk_fma_f32 v[64:65], v[160:161], s[90:91], v[64:65]\n\t"
"v_pk_fma_f32 v[66:67], v[162:163], s[90:91], v[66:67]\n\t"
"v_pk_add_f32 v[160:161], v[64:65], v[160:161] neg_lo:[0,1] neg_hi:[0,1]\n\t"
"v_pk_add_f32 v[162:163], v[66:67], v[162:163] neg_lo:[0,1] neg_hi:[0,1]\n\t"
"v_pk_fma_f32 v[80:81], v[176:177], s[90:91], v[80:81]\n\t"
"v_pk_fma_f32 v[82:83], v[178:179], s[90:91], v[82:83]\n\t"
"v_pk_add_f32 v[176:177], v[80:81], v[176:177] neg_lo:[0,1] neg_hi:[0,1]\n\t"
"v_pk_add_f32 v[178:179], v[82:83], v[178:179] neg_lo:[0,1] neg_hi:[0,1]\n\t"
"v_pk_fma_f32 v[84:85], v[180:181], s[90:91], v[84:85]\n\t"
"v_pk_fma_f32 v[86:87], v[182:183], s[90:91], v[86:87]\n\t"
"v_pk_add_f32 v[180:181], v[84:85], v[180:181] neg_lo:[0,1] neg_hi:[0,1]\n\t"
"v_pk_add_f32 v[182:183], v[86:87], v[182:183] neg_lo:[0,1] neg_hi:[0,1]\n\t"
"v_pk_fma_f32 v[68:69], v[164:165], s[90:91], v[68:69]\n\t"
"v_pk_fma_f32 v[70:71], v[166:167], s[90:91], v[70:71]\n\t"
"v_pk_add_f32 v[164:165], v[68:69], v[164:165] neg_lo:[0,1] neg_hi:[0,1]\n\t"
"v_pk_add_f32 v[166:167], v[70:71], v[166:167] neg_lo:[0,1] neg_hi:[0,1]\n\t"
"v_pk_fma_f32 v[52:53], v[148:149], s[90:91], v[52:53]\n\t"
"v_pk_fma_f32 v[54:55], v[150:151], s[90:91], v[54:55]\n\t"
"v_pk_add_f32 v[148:149], v[52:53], v[148:149] neg_lo:[0,1] neg_hi:[0,1]\n\t"
"v_pk_add_f32 v[150:151], v[54:55], v[150:151] neg_lo:[0,1] neg_hi:[0,1]\n\t"
"v_pk_fma_f32 v[56:57], v[152:153], s[90:91], v[56:57]\n\t"
"v_pk_fma_f32 v[58:59], v[154:155], s[90:91], v[58:59]\n\t"
"v_pk_add_f32 v[152:153], v[56:57], v[152:153] neg_lo:[0,1] neg_hi:[0,1]\n\t"
"v_pk_add_f32 v[154:155], v[58:59], v[154:155] neg_lo:[0,1] neg_hi:[0,1]\n\t"
"v_pk_fma_f32 v[72:73], v[168:169], s[90:91], v[72:73]\n\t"
"v_pk_fma_f32 v[74:75], v[170:171], s[90:91], v[74:75]\n\t"
"v_pk_add_f32 v[168:169], v[72:73], v[168:169] neg_lo:[0,1] neg_hi:[0,1]\n\t"
"v_pk_add_f32 v[170:171], v[74:75], v[170:171] neg_lo:[0,1] neg_hi:[0,1]\n\t"
"v_pk_fma_f32 v[88:89], v[184:185], s[90:91], v[88:89]\n\t"
"v_pk_fma_f32 v[90:91], v[186:187], s[90:91], v[90:91]\n\t"
"v_pk_add_f32 v[184:185], v[88:89], v[184:185] neg_lo:[0,1] neg_hi:[0,1]\n\t"
"v_pk_add_f32 v[186:187], v[90:91], v[186:187] neg_lo:[0,1] neg_hi:[0,1]\n\t"
"v_pk_fma_f32 v[92:93], v[188:189], s[90:91], v[92:93]\n\t"
"v_pk_fma_f32 v[94:95], v[190:191], s[90:91], v[94:95]\n\t"
"v_pk_add_f32 v[188:189], v[92:93], v[188:189] neg_lo:[0,1] neg_hi:[0,1]\n\t"
"v_pk_add_f32 v[190:191], v[94:95], v[190:191] neg_lo:[0,1] neg_hi:[0,1]\n\t"
"v_pk_fma_f32 v[76:77], v[172:173], s[90:91], v[76:77]\n\t"
"v_pk_fma_f32 v[78:79], v[174:175], s[90:91], v[78:79]\n\t"
"v_pk_add_f32 v[172:173], v[76:77], v[172:173] neg_lo:[0,1] neg_hi:[0,1]\n\t"
"v_pk_add_f32 v[174:175], v[78:79], v[174:175] neg_lo:[0,1] neg_hi:[0,1]\n\t"
"v_pk_fma_f32 v[60:61], v[156:157], s[90:91], v[60:61]\n\t"
"v_pk_fma_f32 v[62:63], v[158:159], s[90:91], v[62:63]\n\t"
"v_pk_add_f32 v[156:157], v[60:61], v[156:157] neg_lo:[0,1] neg_hi:[0,1]\n\t"
"v_pk_add_f32 v[158:159], v[62:63], v[158:159] neg_lo:[0,1] neg_hi:[0,1]\n\t"
"v_cvt_pk_bf16_f32 v80, v80, v81\n\t"
"v_cvt_pk_bf16_f32 v92, v92, v93\n\t"
"v_mfma_f32_16x16x32_bf16 v[208:211], a[252:255], a[192:195], 0\n\t"
"ds_read_b64_tr_b16 a[192:193], v250 offset:0\n\t"
"v_cvt_pk_bf16_f32 v48, v48, v49\n\t"
"v_cvt_pk_bf16_f32 v52, v52, v53\n\t"
"v_cvt_pk_bf16_f32 v56, v56, v57\n\t"
"v_mfma_f32_16x16x32_bf16 v[212:215], a[252:255], a[200:203], 0\n\t"
"v_cvt_pk_bf16_f32 v49, v50, v51\n\t"
"v_cvt_pk_bf16_f32 v53, v54, v55\n\t"
"v_cvt_pk_bf16_f32 v60, v60, v61\n\t"
"v_mfma_f32_16x16x32_bf16 v[216:219], a[252:255], a[194:197], 0\n\t"
"ds_read_b64_tr_b16 a[200:201], v251 offset:0\n\t"
"ds_write2st64_b64 v228, v[48:49], v[52:53] offset0:0 offset1:13\n\t"
"v_mfma_f32_16x16x32_bf16 v[220:223], a[252:255], a[202:205], 0\n\t"
"s_waitcnt lgkmcnt(11)\n\t"
"v_cvt_pk_bf16_f32 v68, v68, v69\n\t"
"v_cvt_pk_bf16_f32 v57, v58, v59\n\t"
"v_mfma_f32_16x16x32_bf16 v[0:3], a[0:3], a[144:147], v[0:3]\n\t"
"v_cvt_pk_bf16_f32 v61, v62, v63\n\t"
"v_cvt_pk_bf16_f32 v72, v72, v73\n\t"
"v_mfma_f32_16x16x32_bf16 v[0:3], a[4:7], a[160:163], v[0:3]\n\t"
"ds_write2st64_b64 v228, v[56:57], v[60:61] offset0:26 offset1:39\n\t"
"v_mfma_f32_16x16x32_bf16 v[16:19], a[8:11], a[144:147], v[16:19]\n\t"
"v_cvt_pk_bf16_f32 v64, v64, v65\n\t"
"v_cvt_pk_bf16_f32 v69, v70, v71\n\t"
"v_mfma_f32_16x16x32_bf16 v[16:19], a[12:15], a[160:163], v[16:19]\n\t"
"v_cvt_pk_bf16_f32 v209, v209, v213\n\t"
"v_cvt_pk_bf16_f32 v65, v66, v67\n\t"
"v_mfma_f32_16x16x32_bf16 v[32:35], a[16:19], a[144:147], v[32:35]\n\t"
"v_cvt_pk_bf16_f32 v201, v201, v205\n\t"
"v_cvt_pk_bf16_f32 v211, v211, v215\n\t"
"v_mfma_f32_16x16x32_bf16 v[32:35], a[20:23], a[160:163], v[32:35]\n\t"
"s_nop 0\n\t"
"ds_read_b128 a[144:147], v224 offset:18496\n\t"
"ds_read_b128 a[160:163], v224 offset:22592\n\t"
"ds_write_addtid_b32 v201 offset:8480\n\t"
"ds_write2st64_b64 v246, v[64:65], v[68:69] offset0:0 offset1:13\n\t"
"v_mfma_f32_16x16x32_bf16 v[36:39], a[16:19], a[148:151], v[36:39]\n\t"
"v_cvt_pk_bf16_f32 v216, v216, v220\n\t"
"v_cvt_pk_bf16_f32 v81, v82, v83\n\t"
"v_mfma_f32_16x16x32_bf16 v[36:39], a[20:23], a[164:167], v[36:39]\n\t"
"v_cvt_pk_bf16_f32 v84, v84, v85\n\t"
"v_cvt_pk_bf16_f32 v73, v74, v75\n\t"
"v_mfma_f32_16x16x32_bf16 v[20:23], a[8:11], a[148:151], v[20:23]\n\t"
"v_cvt_pk_bf16_f32 v208, v208, v212\n\t"
"v_cvt_pk_bf16_f32 v85, v86, v87\n\t"
"v_mfma_f32_16x16x32_bf16 v[20:23], a[12:15], a[164:167], v[20:23]\n\t"
"ds_write2st64_b64 v247, v[80:81], v[84:85] offset0:0 offset1:13\n\t"
"v_mfma_f32_16x16x32_bf16 v[4:7], a[0:3], a[148:151], v[4:7]\n\t"
"v_cvt_pk_bf16_f32 v200, v200, v204\n\t"
"v_cvt_pk_bf16_f32 v219, v219, v223\n\t"
"v_mfma_f32_16x16x32_bf16 v[4:7], a[4:7], a[164:167], v[4:7]\n\t"
"v_cvt_pk_bf16_f32 v193, v193, v197\n\t"
"v_cvt_pk_bf16_f32 v203, v203, v207\n\t"
"v_mfma_f32_16x16x32_bf16 v[8:11], a[0:3], a[152:155], v[8:11]\n\t"
"v_cvt_pk_bf16_f32 v192, v192, v196\n\t"
"v_cvt_pk_bf16_f32 v195, v195, v199\n\t"
"v_mfma_f32_16x16x32_bf16 v[8:11], a[4:7], a[168:171], v[8:11]\n\t"
"s_nop 0\n\t"
".p2align 3\n\t"
"ds_read_b128 a[148:151], v224 offset:19008\n\t"
"ds_read_b128 a[164:167], v224 offset:23104\n\t"
"ds_write_addtid_b32 v192 offset:0\n\t"
"ds_write_addtid_b32 v193 offset:8224\n\t"
"ds_write_addtid_b32 v195 offset:10272\n\t"
"ds_write_addtid_b32 v209 offset:12320\n\t"
"ds_write_addtid_b32 v216 offset:4352\n\t"
"ds_write_addtid_b32 v219 offset:14624\n\t"
"ds_read_b64_tr_b16 a[194:195], v250 offset:512\n\t"
"v_mfma_f32_16x16x32_bf16 v[24:27], a[8:11], a[152:155], v[24:27]\n\t"
"ds_write_addtid_b32 v200 offset:256\n\t"
"v_cvt_pk_bf16_f32 v76, v76, v77\n\t"
"v_mfma_f32_16x16x32_bf16 v[24:27], a[12:15], a[168:171], v[24:27]\n\t"
"ds_write_addtid_b32 v211 offset:14368\n\t"
"v_cvt_pk_bf16_f32 v218, v218, v222\n\t"
"v_mfma_f32_16x16x32_bf16 v[40:43], a[16:19], a[152:155], v[40:43]\n\t"
"v_cvt_pk_bf16_f32 v77, v78, v79\n\t"
"v_cvt_pk_bf16_f32 v88, v88, v89\n\t"
"v_mfma_f32_16x16x32_bf16 v[40:43], a[20:23], a[168:171], v[40:43]\n\t"
"s_waitcnt lgkmcnt(12)\n\t"
"ds_write_addtid_b32 v203 offset:10528\n\t"
"ds_write2st64_b64 v246, v[72:73], v[76:77] offset0:26 offset1:39\n\t"
"v_mfma_f32_16x16x32_bf16 v[44:47], a[16:19], a[156:159], v[44:47]\n\t"
"buffer_load_dwordx4 a[16:19], v235, s[8:11], s59 offen offset:0 sc1\n\t"
"v_mfma_f32_16x16x32_bf16 v[44:47], a[20:23], a[172:175], v[44:47]\n\t"
"v_cvt_pk_bf16_f32 v89, v90, v91\n\t"
"v_cvt_pk_bf16_f32 v93, v94, v95\n\t"
"v_mfma_f32_16x16x32_bf16 v[28:31], a[8:11], a[156:159], v[28:31]\n\t"
"buffer_load_dwordx4 a[20:23], v235, s[8:11], s59 offen offset:1024 sc1\n\t"
"v_mfma_f32_16x16x32_bf16 v[28:31], a[12:15], a[172:175], v[28:31]\n\t"
"ds_write_addtid_b32 v218 offset:6400\n\t"
"ds_write2st64_b64 v247, v[88:89], v[92:93] offset0:26 offset1:39\n\t"
"v_mfma_f32_16x16x32_bf16 v[12:15], a[0:3], a[156:159], v[12:15]\n\t"
"v_cvt_pk_bf16_f32 v194, v194, v198\n\t"
"v_cvt_pk_bf16_f32 v217, v217, v221\n\t"
"v_mfma_f32_16x16x32_bf16 v[12:15], a[4:7], a[172:175], v[12:15]\n\t"
"buffer_load_dwordx4 a[12:15], v252, s[8:11], s58 offen offset:3072 sc1\n\t"
"v_mfma_f32_16x16x32_bf16 v[96:99], a[24:27], a[144:147], v[96:99]\n\t"
"v_cvt_pk_bf16_f32 v202, v202, v206\n\t"
"v_cvt_pk_bf16_f32 v210, v210, v214\n\t"
"v_mfma_f32_16x16x32_bf16 v[96:99], a[28:31], a[160:163], v[96:99]\n\t"
"s_nop 0\n\t"
"ds_write_addtid_b32 v210 offset:6144\n\t"
"ds_write_addtid_b32 v217 offset:12576\n\t"
"v_mfma_f32_16x16x32_bf16 v[112:115], a[32:35], a[144:147], v[112:115]\n\t"
"s_nop 0\n\t"
"ds_write_addtid_b32 v202 offset:2304\n\t"
"ds_write_addtid_b32 v208 offset:4096\n\t"
"v_mfma_f32_16x16x32_bf16 v[112:115], a[36:39], a[160:163], v[112:115]\n\t"
"s_nop 0\n\t"
"ds_read_b128 a[156:159], v224 offset:20032\n\t"
"ds_write_addtid_b32 v194 offset:2048\n\t"
"v_mfma_f32_16x16x32_bf16 v[128:131], a[40:43], a[144:147], v[128:131]\n\t"
"s_nop 0\n\t"
"ds_read_b128 a[152:155], v224 offset:19520\n\t"
"ds_read_b64_tr_b16 a[196:197], v250 offset:1024\n\t"
"v_mfma_f32_16x16x32_bf16 v[128:131], a[44:47], a[160:163], v[128:131]\n\t"
"s_waitcnt lgkmcnt(10)\n\t"
"buffer_load_dwordx4 a[8:11], v252, s[8:11], s58 offen offset:2048 sc1\n\t"
"v_mfma_f32_16x16x32_bf16 v[132:135], a[40:43], a[148:151], v[132:135]\n\t"
"s_nop 0\n\t"
"ds_read_b128 a[168:171], v224 offset:23616\n\t"
"ds_read_b128 a[172:175], v224 offset:24128\n\t"
"v_mfma_f32_16x16x32_bf16 v[132:135], a[44:47], a[164:167], v[132:135]\n\t"
"buffer_load_dwordx4 a[0:3], v252, s[8:11], s58 offen offset:0 sc1\n\t"
"v_mfma_f32_16x16x32_bf16 v[116:119], a[32:35], a[148:151], v[116:119]\n\t"
"s_nop 0\n\t"
"ds_read_b64_tr_b16 a[202:203], v251 offset:512\n\t"
"ds_read_b64_tr_b16 a[204:205], v251 offset:1024\n\t"
"v_mfma_f32_16x16x32_bf16 v[116:119], a[36:39], a[164:167], v[116:119]\n\t"
"buffer_load_dwordx4 a[4:7], v252, s[8:11], s58 offen offset:1024 sc1\n\t"
"v_mfma_f32_16x16x32_bf16 v[100:103], a[24:27], a[148:151], v[100:103]\n\t"
"s_waitcnt lgkmcnt(2)\n\t"
"v_mfma_f32_16x16x32_bf16 v[100:103], a[28:31], a[164:167], v[100:103]\n\t"
"s_barrier\n\t"
"v_mfma_f32_16x16x32_bf16 v[104:107], a[24:27], a[152:155], v[104:107]\n\t"
"ds_read_b128 a[144:147], v224 offset:0\n\t"
"v_cvt_pk_bf16_f32 v160, v160, v161\n\t"
"v_mfma_f32_16x16x32_bf16 v[104:107], a[28:31], a[168:171], v[104:107]\n\t"
"ds_read_b128 a[148:151], v224 offset:512\n\t"
"v_accvgpr_read_b32 v248, a208\n\t"
"v_cvt_pk_bf16_f32 v148, v148, v149\n\t"
"v_mfma_f32_16x16x32_bf16 v[120:123], a[32:35], a[152:155], v[120:123]\n\t"
"ds_read_b128 a[160:163], v224 offset:4096\n\t"
"v_cvt_pk_bf16_f32 v176, v176, v177\n\t"
"v_mfma_f32_16x16x32_bf16 v[120:123], a[36:39], a[168:171], v[120:123]\n\t"
"ds_read_b128 a[164:167], v224 offset:4608\n\t"
"v_cvt_pk_bf16_f32 v156, v156, v157\n\t"
"v_cvt_pk_bf16_f32 v172, v172, v173\n\t"
"v_mfma_f32_16x16x32_bf16 v[136:139], a[40:43], a[152:155], v[136:139]\n\t"
"ds_read_b128 v[60:63], v229 offset:13312\n\t"
"v_cvt_pk_bf16_f32 v152, v152, v153\n\t"
"v_mfma_f32_16x16x32_bf16 v[136:139], a[44:47], a[168:171], v[136:139]\n\t"
"ds_read_b128 v[92:95], v231 offset:13312\n\t"
"v_cvt_pk_bf16_f32 v149, v150, v151\n\t"
"v_cvt_pk_bf16_f32 v188, v188, v189\n\t"
"v_mfma_f32_16x16x32_bf16 v[140:143], a[40:43], a[156:159], v[140:143]\n\t"
"ds_read_b128 a[152:155], v224 offset:1024\n\t"
"v_cvt_pk_bf16_f32 v177, v178, v179\n\t"
"v_mfma_f32_16x16x32_bf16 v[140:143], a[44:47], a[172:175], v[140:143]\n\t"
"ds_read_b128 v[56:59], v229 offset:0\n\t"
"buffer_load_dwordx4 a[40:43], v235, s[8:11], s58 offen offset:2048 sc1\n\t"
"buffer_load_dwordx4 a[44:47], v235, s[8:11], s58 offen offset:3072 sc1\n\t"
"v_cvt_pk_bf16_f32 v168, v168, v169\n\t"
"v_mfma_f32_16x16x32_bf16 v[124:127], a[32:35], a[156:159], v[124:127]\n\t"
"ds_read_b128 v[76:79], v230 offset:13312\n\t"
"v_cvt_pk_bf16_f32 v161, v162, v163\n\t"
"v_mfma_f32_16x16x32_bf16 v[124:127], a[36:39], a[172:175], v[124:127]\n\t"
"ds_read_b128 v[72:75], v230 offset:0\n\t"
"buffer_load_dwordx4 a[36:39], v235, s[8:11], s58 offen offset:1024 sc1\n\t"
"v_mfma_f32_16x16x32_bf16 v[108:111], a[24:27], a[156:159], v[108:111]\n\t"
"ds_read_b128 v[88:91], v231 offset:0\n\t"
"v_cvt_pk_bf16_f32 v164, v164, v165\n\t"
"v_mfma_f32_16x16x32_bf16 v[108:111], a[28:31], a[172:175], v[108:111]\n\t"
"buffer_load_dwordx4 a[32:35], v235, s[8:11], s58 offen offset:0 sc1\n\t"
"v_cvt_pk_bf16_f32 v144, v144, v145\n\t"
"v_cvt_pk_bf16_f32 v145, v146, v147\n\t"
"v_mfma_f32_16x16x32_bf16 v[192:195], a[252:255], a[176:179], 0\n\t"
"ds_read_b128 a[168:171], v224 offset:5120\n\t"
"v_cvt_pk_bf16_f32 v184, v184, v185\n\t"
"v_mfma_f32_16x16x32_bf16 v[196:199], a[252:255], a[184:187], 0\n\t"
"ds_read_b128 a[156:159], v224 offset:1536\n\t"
"v_cvt_pk_bf16_f32 v180, v180, v181\n\t"
"v_mfma_f32_16x16x32_bf16 v[200:203], a[252:255], a[178:181], 0\n\t"
"s_waitcnt lgkmcnt(5)\n\t"
"buffer_store_dwordx4 v[56:59], v232, s[16:19], s24 offen nt\n\t"
"v_mfma_f32_16x16x32_bf16 v[204:207], a[252:255], a[186:189], 0\n\t"
"ds_read_b128 a[172:175], v224 offset:5632\n\t"
"v_cvt_pk_bf16_f32 v181, v182, v183\n\t"
"v_mfma_f32_16x16x32_bf16 v[208:211], a[252:255], a[192:195], 0\n\t"
"s_waitcnt lgkmcnt(3)\n\t"
"s_barrier\n\t"
"v_mfma_f32_16x16x32_bf16 v[212:215], a[252:255], a[200:203], 0\n\t"
"buffer_load_dwordx4 a[24:27], v252, s[8:11], s57 offen offset:2048 sc1\n\t"
"v_mfma_f32_16x16x32_bf16 v[216:219], a[252:255], a[194:197], 0\n\t"
"ds_write2st64_b64 v228, v[144:145], v[148:149] offset0:0 offset1:13\n\t"
"v_mfma_f32_16x16x32_bf16 v[220:223], a[252:255], a[202:205], 0\n\t"
"buffer_load_dwordx4 a[28:31], v252, s[8:11], s57 offen offset:3072 sc1\n\t"
"v_mfma_f32_16x16x32_bf16 v[0:3], a[48:51], a[144:147], v[0:3]\n\t"
"v_cvt_pk_bf16_f32 v193, v193, v197\n\t"
"v_cvt_pk_bf16_f32 v195, v195, v199\n\t"
"v_mfma_f32_16x16x32_bf16 v[0:3], a[52:55], a[160:163], v[0:3]\n\t"
"ds_write2st64_b64 v247, v[176:177], v[180:181] offset0:0 offset1:13\n\t"
"v_mfma_f32_16x16x32_bf16 v[16:19], a[56:59], a[144:147], v[16:19]\n\t"
"v_cvt_pk_bf16_f32 v165, v166, v167\n\t"
"v_cvt_pk_bf16_f32 v173, v174, v175\n\t"
"v_mfma_f32_16x16x32_bf16 v[16:19], a[60:63], a[160:163], v[16:19]\n\t"
"buffer_store_dwordx4 v[72:75], v233, s[16:19], s24 offen nt\n\t"
"v_mfma_f32_16x16x32_bf16 v[32:35], a[64:67], a[144:147], v[32:35]\n\t"
"v_cvt_pk_bf16_f32 v200, v200, v204\n\t"
"v_cvt_pk_bf16_f32 v210, v210, v214\n\t"
"v_mfma_f32_16x16x32_bf16 v[32:35], a[68:71], a[160:163], v[32:35]\n\t"
"s_nop 0\n\t"
"ds_read_b128 a[144:147], v224 offset:2048\n\t"
"ds_read_b128 a[160:163], v224 offset:6144\n\t"
"ds_write_addtid_b32 v195 offset:26720\n\t"
"ds_write_addtid_b32 v200 offset:16704\n\t"
"ds_write_addtid_b32 v210 offset:22592\n\t"
"ds_write2st64_b64 v246, v[160:161], v[164:165] offset0:0 offset1:13\n\t"
"v_mfma_f32_16x16x32_bf16 v[36:39], a[64:67], a[148:151], v[36:39]\n\t"
"v_cvt_pk_bf16_f32 v203, v203, v207\n\t"
"v_cvt_pk_bf16_f32 v208, v208, v212\n\t"
"v_mfma_f32_16x16x32_bf16 v[36:39], a[68:71], a[164:167], v[36:39]\n\t"
"buffer_store_dwordx4 v[88:91], v234, s[16:19], s24 offen nt\n\t"
"v_mfma_f32_16x16x32_bf16 v[20:23], a[56:59], a[148:151], v[20:23]\n\t"
"v_cvt_pk_bf16_f32 v211, v211, v215\n\t"
"v_cvt_pk_bf16_f32 v217, v217, v221\n\t"
"v_mfma_f32_16x16x32_bf16 v[20:23], a[60:63], a[164:167], v[20:23]\n\t"
"ds_write_addtid_b32 v211 offset:30816\n\t"
"v_cvt_pk_bf16_f32 v153, v154, v155\n\t"
"v_mfma_f32_16x16x32_bf16 v[4:7], a[48:51], a[148:151], v[4:7]\n\t"
"ds_write_addtid_b32 v193 offset:24672\n\t"
"v_cvt_pk_bf16_f32 v192, v192, v196\n\t"
"v_mfma_f32_16x16x32_bf16 v[4:7], a[52:55], a[164:167], v[4:7]\n\t"
"s_waitcnt lgkmcnt(8)\n\t"
"ds_read_b128 a[148:151], v224 offset:2560\n\t"
"ds_read_b128 a[164:167], v224 offset:6656\n\t"
"ds_write_addtid_b32 v192 offset:16448\n\t"
"ds_write_addtid_b32 v203 offset:26976\n\t"
"ds_write_addtid_b32 v208 offset:20544\n\t"
"ds_write_addtid_b32 v217 offset:29024\n\t"
"v_mfma_f32_16x16x32_bf16 v[8:11], a[48:51], a[152:155], v[8:11]\n\t"
"v_cvt_pk_bf16_f32 v202, v202, v206\n\t"
"v_cvt_pk_bf16_f32 v157, v158, v159\n\t"
"v_mfma_f32_16x16x32_bf16 v[8:11], a[52:55], a[168:171], v[8:11]\n\t"
"ds_write_addtid_b32 v202 offset:18752\n\t"
"ds_write2st64_b64 v228, v[152:153], v[156:157] offset0:26 offset1:39\n\t"
"v_mfma_f32_16x16x32_bf16 v[24:27], a[56:59], a[152:155], v[24:27]\n\t"
"v_cvt_pk_bf16_f32 v169, v170, v171\n\t"
"v_cvt_pk_bf16_f32 v189, v190, v191\n\t"
"v_mfma_f32_16x16x32_bf16 v[24:27], a[60:63], a[168:171], v[24:27]\n\t"
"buffer_store_dwordx4 v[60:63], v232, s[16:19], s27 offen nt\n\t"
"v_mfma_f32_16x16x32_bf16 v[40:43], a[64:67], a[152:155], v[40:43]\n\t"
"ds_write2st64_b64 v246, v[168:169], v[172:173] offset0:26 offset1:39\n\t"
"v_mfma_f32_16x16x32_bf16 v[40:43], a[68:71], a[168:171], v[40:43]\n\t"
"v_cvt_pk_bf16_f32 v218, v218, v222\n\t"
"v_cvt_pk_bf16_f32 v185, v186, v187\n\t"
"v_mfma_f32_16x16x32_bf16 v[44:47], a[64:67], a[156:159], v[44:47]\n\t"
"buffer_load_dwordx4 a[64:67], v235, s[8:11], s65 offen offset:0 sc1\n\t"
"v_mfma_f32_16x16x32_bf16 v[44:47], a[68:71], a[172:175], v[44:47]\n\t"
"ds_write2st64_b64 v247, v[184:185], v[188:189] offset0:26 offset1:39\n\t"
"v_mfma_f32_16x16x32_bf16 v[28:31], a[56:59], a[156:159], v[28:31]\n\t"
"v_cvt_pk_bf16_f32 v209, v209, v213\n\t"
"v_cvt_pk_bf16_f32 v216, v216, v220\n\t"
"v_mfma_f32_16x16x32_bf16 v[28:31], a[60:63], a[172:175], v[28:31]\n\t"
"buffer_store_dwordx4 v[76:79], v233, s[16:19], s27 offen nt\n\t"
"v_mfma_f32_16x16x32_bf16 v[12:15], a[48:51], a[156:159], v[12:15]\n\t"
"v_cvt_pk_bf16_f32 v194, v194, v198\n\t"
"v_cvt_pk_bf16_f32 v201, v201, v205\n\t"
"v_mfma_f32_16x16x32_bf16 v[12:15], a[52:55], a[172:175], v[12:15]\n\t"
"s_waitcnt lgkmcnt(2)\n\t"
"ds_write_addtid_b32 v201 offset:24928\n\t"
"v_cvt_pk_bf16_f32 v219, v219, v223\n\t"
"v_mfma_f32_16x16x32_bf16 v[96:99], a[72:75], a[144:147], v[96:99]\n\t"
"s_nop 0\n\t"
"ds_write_addtid_b32 v194 offset:18496\n\t"
"ds_write_addtid_b32 v218 offset:22848\n\t"
"v_mfma_f32_16x16x32_bf16 v[96:99], a[76:79], a[160:163], v[96:99]\n\t"
"ds_write_addtid_b32 v219 offset:31072\n\t"
"buffer_load_dwordx4 a[68:71], v235, s[8:11], s65 offen offset:1024 sc1\n\t"
"buffer_load_dwordx4 a[60:63], v252, s[8:11], s64 offen offset:3072 sc1\n\t"
"v_mfma_f32_16x16x32_bf16 v[112:115], a[80:83], a[144:147], v[112:115]\n\t"
"s_nop 0\n\t"
"ds_write_addtid_b32 v209 offset:28768\n\t"
"ds_write_addtid_b32 v216 offset:20800\n\t"
"v_mfma_f32_16x16x32_bf16 v[112:115], a[84:87], a[160:163], v[112:115]\n\t"
"buffer_load_dwordx4 a[56:59], v252, s[8:11], s64 offen offset:2048 sc1\n\t"
"v_mfma_f32_16x16x32_bf16 v[128:131], a[88:91], a[144:147], v[128:131]\n\t"
"s_nop 0\n\t"
"ds_read_b128 a[168:171], v224 offset:7168\n\t"
"ds_read_b128 a[156:159], v224 offset:3584\n\t"
"v_mfma_f32_16x16x32_bf16 v[128:131], a[92:95], a[160:163], v[128:131]\n\t"
"buffer_load_dwordx4 a[48:51], v252, s[8:11], s64 offen offset:0 sc1\n\t"
"v_mfma_f32_16x16x32_bf16 v[132:135], a[88:91], a[148:151], v[132:135]\n\t"
"s_nop 0\n\t"
"ds_read_b128 a[152:155], v224 offset:3072\n\t"
"ds_read_b128 a[172:175], v224 offset:7680\n\t"
"v_mfma_f32_16x16x32_bf16 v[132:135], a[92:95], a[164:167], v[132:135]\n\t"
"buffer_load_dwordx4 a[52:55], v252, s[8:11], s64 offen offset:1024 sc1\n\t"
"v_mfma_f32_16x16x32_bf16 v[116:119], a[80:83], a[148:151], v[116:119]\n\t"
"v_add_u32 v248, 80896, v248\n\t"
"v_xor_b32 v249, 32, v248\n\t"
"v_mfma_f32_16x16x32_bf16 v[116:119], a[84:87], a[164:167], v[116:119]\n\t"
"buffer_store_dwordx4 v[92:95], v234, s[16:19], s27 offen nt\n\t"
"v_mfma_f32_16x16x32_bf16 v[100:103], a[72:75], a[148:151], v[100:103]\n\t"
"v_xor_b32 v250, 64, v248\n\t"
"v_xor_b32 v251, 96, v248\n\t"
"v_mfma_f32_16x16x32_bf16 v[100:103], a[76:79], a[164:167], v[100:103]\n\t"
"s_waitcnt lgkmcnt(0)\n\t"
"s_barrier\n\t"
"v_mfma_f32_16x16x32_bf16 v[104:107], a[72:75], a[152:155], v[104:107]\n\t"
"s_nop 0\n\t"
"ds_read_b128 a[164:167], v224 offset:21056\n\t"
"ds_read_b64_tr_b16 a[176:177], v248 offset:41472\n\t"
"v_mfma_f32_16x16x32_bf16 v[104:107], a[76:79], a[168:171], v[104:107]\n\t"
"s_nop 0\n\t"
"ds_read_b128 a[144:147], v224 offset:16448\n\t"
"ds_read_b128 a[160:163], v224 offset:20544\n\t"
"ds_read_b64_tr_b16 a[178:179], v249 offset:41472\n\t"
"v_mfma_f32_16x16x32_bf16 v[120:123], a[80:83], a[152:155], v[120:123]\n\t"
"s_nop 0\n\t"
"ds_read_b128 a[148:151], v224 offset:16960\n\t"
"ds_read_b64_tr_b16 a[180:181], v248 offset:41984\n\t"
"v_mfma_f32_16x16x32_bf16 v[120:123], a[84:87], a[168:171], v[120:123]\n\t"
"s_nop 0\n\t"
"ds_read_b64_tr_b16 a[182:183], v249 offset:41984\n\t"
"ds_read_b64_tr_b16 a[184:185], v250 offset:43776\n\t"
"v_mfma_f32_16x16x32_bf16 v[136:139], a[88:91], a[152:155], v[136:139]\n\t"
"s_nop 0\n\t"
"ds_read_b64_tr_b16 a[186:187], v251 offset:43776\n\t"
"ds_read_b64_tr_b16 a[188:189], v250 offset:44288\n\t"
"v_mfma_f32_16x16x32_bf16 v[136:139], a[92:95], a[168:171], v[136:139]\n\t"
"s_nop 0\n\t"
"ds_read_b128 a[168:171], v224 offset:21568\n\t"
"ds_read_b64_tr_b16 a[190:191], v251 offset:44288\n\t"
"v_mfma_f32_16x16x32_bf16 v[140:143], a[88:91], a[156:159], v[140:143]\n\t"
"s_nop 0\n\t"
"ds_read_b64_tr_b16 a[192:193], v250 offset:41472\n\t"
"ds_read_b128 v[152:155], v229 offset:0\n\t"
"v_mfma_f32_16x16x32_bf16 v[140:143], a[92:95], a[172:175], v[140:143]\n\t"
"buffer_load_dwordx4 a[88:91], v235, s[8:11], s64 offen offset:2048 sc1\n\t"
"buffer_load_dwordx4 a[92:95], v235, s[8:11], s64 offen offset:3072 sc1\n\t"
"v_mfma_f32_16x16x32_bf16 v[124:127], a[80:83], a[156:159], v[124:127]\n\t"
"s_nop 0\n\t"
"ds_read_b64_tr_b16 a[194:195], v251 offset:41472\n\t"
"ds_read_b64_tr_b16 a[196:197], v250 offset:41984\n\t"
"v_mfma_f32_16x16x32_bf16 v[124:127], a[84:87], a[172:175], v[124:127]\n\t"
"s_waitcnt lgkmcnt(9)\n\t"
"buffer_load_dwordx4 a[84:87], v235, s[8:11], s64 offen offset:1024 sc1\n\t"
"v_mfma_f32_4x4x4_16b_bf16 v[192:195], a[210:211], a[176:177], 0\n\t"
"ds_read_b128 a[152:155], v224 offset:17472\n\t"
"v_mfma_f32_4x4x4_16b_bf16 v[196:199], a[210:211], a[178:179], 0\n\t"
"ds_read_b128 v[168:171], v230 offset:0\n\t"
"v_mfma_f32_4x4x4_16b_bf16 v[200:203], a[210:211], a[180:181], 0\n\t"
"ds_read_b64_tr_b16 a[198:199], v251 offset:41984\n\t"
"v_mfma_f32_4x4x4_16b_bf16 v[204:207], a[210:211], a[182:183], 0\n\t"
"buffer_load_dwordx4 a[80:83], v235, s[8:11], s64 offen offset:0 sc1\n\t"
"v_mfma_f32_16x16x32_bf16 v[108:111], a[72:75], a[156:159], v[108:111]\n\t"
"s_nop 0\n\t"
"ds_read_b128 a[156:159], v224 offset:17984\n\t"
"ds_read_b64_tr_b16 a[200:201], v248 offset:43776\n\t"
"v_mfma_f32_16x16x32_bf16 v[108:111], a[76:79], a[172:175], v[108:111]\n\t"
"ds_read_b128 v[184:187], v231 offset:0\n\t"
"v_cvt_pk_bf16_f32 v197, v193, v197\n\t"
"v_mfma_f32_16x16x32_bf16 v[0:3], a[96:99], a[144:147], v[0:3]\n\t"
"ds_read_b64_tr_b16 a[202:203], v249 offset:43776\n\t"
"v_cvt_pk_bf16_f32 v194, v194, v198\n\t"
"v_mfma_f32_16x16x32_bf16 v[0:3], a[100:103], a[160:163], v[0:3]\n\t"
"s_waitcnt lgkmcnt(9)\n\t"
"buffer_store_dwordx4 v[152:155], v232, s[16:19], s20 offen nt\n\t"
"v_mfma_f32_16x16x32_bf16 v[16:19], a[104:107], a[144:147], v[16:19]\n\t"
"ds_write_addtid_b32 v197 offset:2048\n\t"
"v_cvt_pk_bf16_f32 v193, v203, v207\n\t"
"v_mfma_f32_16x16x32_bf16 v[16:19], a[108:111], a[160:163], v[16:19]\n\t"
"ds_read_b128 a[172:175], v224 offset:22080\n\t"
"v_cvt_pk_bf16_f32 v198, v200, v204\n\t"
"v_mfma_f32_16x16x32_bf16 v[32:35], a[112:115], a[144:147], v[32:35]\n\t"
"v_cvt_pk_bf16_f32 v196, v192, v196\n\t"
"v_cvt_pk_bf16_f32 v195, v195, v199\n\t"
"v_mfma_f32_16x16x32_bf16 v[32:35], a[116:119], a[160:163], v[32:35]\n\t"
"ds_read_b128 v[156:159], v229 offset:13312\n\t"
"buffer_load_dwordx4 a[72:75], v252, s[8:11], s63 offen offset:2048 sc1\n\t"
"buffer_load_dwordx4 a[76:79], v252, s[8:11], s63 offen offset:3072 sc1\n\t"
"v_mfma_f32_4x4x4_16b_bf16 v[208:211], a[210:211], a[184:185], 0\n\t"
"ds_read_b128 a[144:147], v224 offset:18496\n\t"
"v_mfma_f32_4x4x4_16b_bf16 v[212:215], a[210:211], a[186:187], 0\n\t"
"ds_read_b128 a[160:163], v224 offset:22592\n\t"
"v_mfma_f32_4x4x4_16b_bf16 v[216:219], a[210:211], a[188:189], 0\n\t"
"s_waitcnt lgkmcnt(7)\n\t"
"buffer_store_dwordx4 v[168:171], v233, s[16:19], s20 offen nt\n\t"
"v_mfma_f32_4x4x4_16b_bf16 v[220:223], a[210:211], a[190:191], 0\n\t"
"ds_read_b64_tr_b16 a[204:205], v248 offset:44288\n\t"
"v_cvt_pk_bf16_f32 v199, v201, v205\n\t"
"v_mfma_f32_16x16x32_bf16 v[36:39], a[112:115], a[148:151], v[36:39]\n\t"
"ds_write_addtid_b32 v199 offset:2304\n\t"
"v_cvt_pk_bf16_f32 v192, v202, v206\n\t"
"v_mfma_f32_16x16x32_bf16 v[36:39], a[116:119], a[164:167], v[36:39]\n\t"
"s_waitcnt lgkmcnt(7)\n\t"
"buffer_store_dwordx4 v[184:187], v234, s[16:19], s20 offen nt\n\t"
"v_mfma_f32_16x16x32_bf16 v[20:23], a[104:107], a[148:151], v[20:23]\n\t"
"ds_read_b128 v[172:175], v230 offset:13312\n\t"
"v_cvt_pk_bf16_f32 v212, v208, v212\n\t"
"v_mfma_f32_16x16x32_bf16 v[20:23], a[108:111], a[164:167], v[20:23]\n\t"
"v_cvt_pk_bf16_f32 v210, v210, v214\n\t"
"v_cvt_pk_bf16_f32 v213, v209, v213\n\t"
"v_mfma_f32_16x16x32_bf16 v[4:7], a[96:99], a[148:151], v[4:7]\n\t"
"ds_read_b128 a[148:151], v224 offset:19008\n\t"
"v_cvt_pk_bf16_f32 v209, v219, v223\n\t"
"v_mfma_f32_16x16x32_bf16 v[4:7], a[100:103], a[164:167], v[4:7]\n\t"
"ds_write_addtid_b32 v196 offset:0\n\t"
"v_cvt_pk_bf16_f32 v211, v211, v215\n\t"
"v_mfma_f32_16x16x32_bf16 v[8:11], a[96:99], a[152:155], v[8:11]\n\t"
"ds_read_b64_tr_b16 a[206:207], v249 offset:44288\n\t"
"v_cvt_pk_bf16_f32 v208, v218, v222\n\t"
"v_mfma_f32_16x16x32_bf16 v[8:11], a[100:103], a[168:171], v[8:11]\n\t"
"ds_write_addtid_b32 v198 offset:256\n\t"
"v_cvt_pk_bf16_f32 v214, v216, v220\n\t"
"v_mfma_f32_4x4x4_16b_bf16 v[200:203], a[210:211], a[192:193], 0\n\t"
"ds_read_b128 a[164:167], v224 offset:23104\n\t"
"v_mfma_f32_4x4x4_16b_bf16 v[204:207], a[210:211], a[194:195], 0\n\t"
"ds_write_addtid_b32 v212 offset:8224\n\t"
"v_mfma_f32_4x4x4_16b_bf16 v[196:199], a[210:211], a[196:197], 0\n\t"
"v_cvt_pk_bf16_f32 v215, v217, v221\n\t"
"v_mfma_f32_4x4x4_16b_bf16 v[216:219], a[210:211], a[198:199], 0\n\t"
"s_waitcnt lgkmcnt(7)\n\t"
"buffer_store_dwordx4 v[156:159], v232, s[16:19], s25 offen nt\n\t"
"v_mfma_f32_16x16x32_bf16 v[24:27], a[104:107], a[152:155], v[24:27]\n\t"
"s_nop 0\n\t"
"ds_write_addtid_b32 v214 offset:8480\n\t"
"ds_read_b128 v[188:191], v231 offset:13312\n\t"
"v_mfma_f32_16x16x32_bf16 v[24:27], a[108:111], a[168:171], v[24:27]\n\t"
"v_cvt_pk_bf16_f32 v204, v200, v204\n\t"
"v_cvt_pk_bf16_f32 v203, v203, v207\n\t"
"v_mfma_f32_16x16x32_bf16 v[40:43], a[112:115], a[152:155], v[40:43]\n\t"
"ds_write_addtid_b32 v213 offset:10272\n\t"
"v_cvt_pk_bf16_f32 v205, v201, v205\n\t"
"v_mfma_f32_16x16x32_bf16 v[40:43], a[116:119], a[168:171], v[40:43]\n\t"
"s_waitcnt lgkmcnt(7)\n\t"
"buffer_store_dwordx4 v[172:175], v233, s[16:19], s25 offen nt\n\t"
"v_mfma_f32_16x16x32_bf16 v[44:47], a[112:115], a[156:159], v[44:47]\n\t"
"ds_write_addtid_b32 v215 offset:10528\n\t"
"v_cvt_pk_bf16_f32 v207, v197, v217\n\t"
"v_mfma_f32_16x16x32_bf16 v[44:47], a[116:119], a[172:175], v[44:47]\n\t"
"v_cvt_pk_bf16_f32 v202, v202, v206\n\t"
"v_cvt_pk_bf16_f32 v200, v198, v218\n\t"
"v_mfma_f32_16x16x32_bf16 v[28:31], a[104:107], a[156:159], v[28:31]\n\t"
"ds_write_addtid_b32 v205 offset:6144\n\t"
"v_cvt_pk_bf16_f32 v201, v199, v219\n\t"
"v_mfma_f32_16x16x32_bf16 v[28:31], a[108:111], a[172:175], v[28:31]\n\t"
"s_waitcnt lgkmcnt(5)\n\t"
"buffer_load_dwordx4 a[112:115], v235, s[8:11], s47 offen offset:0 sc1\n\t"
"buffer_load_dwordx4 a[116:119], v235, s[8:11], s47 offen offset:1024 sc1\n\t"
"v_mfma_f32_4x4x4_16b_bf16 v[220:223], a[210:211], a[200:201], 0\n\t"
"v_cvt_pk_bf16_f32 v206, v196, v216\n\t"
"v_mfma_f32_4x4x4_16b_bf16 v[212:215], a[210:211], a[202:203], 0\n\t"
"buffer_load_dwordx4 a[108:111], v252, s[8:11], s46 offen offset:3072 sc1\n\t"
"v_mfma_f32_4x4x4_16b_bf16 v[196:199], a[210:211], a[204:205], 0\n\t"
"ds_write_addtid_b32 v206 offset:4352\n\t"
"v_mfma_f32_4x4x4_16b_bf16 v[216:219], a[210:211], a[206:207], 0\n\t"
"buffer_load_dwordx4 a[104:107], v252, s[8:11], s46 offen offset:2048 sc1\n\t"
"v_mfma_f32_16x16x32_bf16 v[12:15], a[96:99], a[156:159], v[12:15]\n\t"
"s_nop 0\n\t"
"ds_write_addtid_b32 v204 offset:4096\n\t"
"ds_write_addtid_b32 v207 offset:6400\n\t"
"v_mfma_f32_16x16x32_bf16 v[12:15], a[100:103], a[172:175], v[12:15]\n\t"
"v_cvt_pk_bf16_f32 v212, v220, v212\n\t"
"v_cvt_pk_bf16_f32 v223, v223, v215\n\t"
"v_mfma_f32_16x16x32_bf16 v[96:99], a[120:123], a[144:147], v[96:99]\n\t"
"v_cvt_pk_bf16_f32 v222, v222, v214\n\t"
"v_cvt_pk_bf16_f32 v213, v221, v213\n\t"
"v_mfma_f32_16x16x32_bf16 v[96:99], a[124:127], a[160:163], v[96:99]\n\t"
"ds_write_addtid_b32 v213 offset:14368\n\t"
"s_waitcnt lgkmcnt(5)\n\t"
"buffer_store_dwordx4 v[188:191], v234, s[16:19], s25 offen nt\n\t"
"v_cvt_pk_bf16_f32 v215, v197, v217\n\t"
"v_mfma_f32_16x16x32_bf16 v[112:115], a[128:131], a[144:147], v[112:115]\n\t"
"ds_write_addtid_b32 v212 offset:12320\n\t"
"v_cvt_pk_bf16_f32 v214, v196, v216\n\t"
"v_mfma_f32_16x16x32_bf16 v[112:115], a[132:135], a[160:163], v[112:115]\n\t"
"s_nop 0\n\t"
"ds_write_addtid_b32 v214 offset:12576\n\t"
"ds_write_addtid_b32 v215 offset:14624\n\t"
"v_mfma_f32_16x16x32_bf16 v[128:131], a[136:139], a[144:147], v[128:131]\n\t"
"s_nop 0\n\t"
"ds_read_b128 a[152:155], v224 offset:19520\n\t"
"ds_read_b128 a[168:171], v224 offset:23616\n\t"
"v_mfma_f32_16x16x32_bf16 v[128:131], a[140:143], a[160:163], v[128:131]\n\t"
"buffer_load_dwordx4 a[96:99], v252, s[8:11], s46 offen offset:0 sc1\n\t"
"v_mfma_f32_16x16x32_bf16 v[132:135], a[136:139], a[148:151], v[132:135]\n\t"
"s_nop 0\n\t"
"ds_read_b128 a[156:159], v224 offset:20032\n\t"
"ds_read_b128 a[172:175], v224 offset:24128\n\t"
"v_mfma_f32_16x16x32_bf16 v[132:135], a[140:143], a[164:167], v[132:135]\n\t"
"buffer_load_dwordx4 a[100:103], v252, s[8:11], s46 offen offset:1024 sc1\n\t"
"v_mfma_f32_16x16x32_bf16 v[116:119], a[128:131], a[148:151], v[116:119]\n\t"
"v_cvt_pk_bf16_f32 v220, v198, v218\n\t"
"v_cvt_pk_bf16_f32 v221, v199, v219\n\t"
"v_mfma_f32_16x16x32_bf16 v[116:119], a[132:135], a[164:167], v[116:119]\n\t"
"v_mfma_f32_16x16x32_bf16 v[100:103], a[120:123], a[148:151], v[100:103]\n\t"
"s_waitcnt lgkmcnt(0)\n\t"
"v_mfma_f32_16x16x32_bf16 v[100:103], a[124:127], a[164:167], v[100:103]\n\t"
"s_barrier\n\t"
"s_add_u32 s71, s71, 1\n\t"
"v_mov_b32 v215, s71\n\t"
"s_mov_b64 exec, 1\n\t"
"buffer_store_dword v215, off, s[8:11], s70 sc1\n\t"
"s_mov_b64 exec, -1\n\t"
"s_sub_u32 s21, s21, 1\n\t"
"s_xor_b64 s[84:85], s[84:85], s[88:89]\n\t"
"s_branch Lunit_%=\n\t"
"Lfb_%=:\n\t"
"v_mbcnt_lo_u32_b32 v162, -1, 0\n\t"
"v_mbcnt_hi_u32_b32 v162, -1, v162\n\t"
"v_bfe_u32 v161, v162, 1, 2\n\t"
"v_lshlrev_b32 v163, 4, v162\n\t"
"v_and_or_b32 v161, v163, 16, v161\n\t"
"v_mul_u32_u24 v161, 0xc0, v161\n\t"
"v_lshrrev_b32 v163, 5, v162\n\t"
"v_mul_u32_u24 v163, 12, v163\n\t"
"v_bfe_u32 v164, v162, 3, 2\n\t"
"v_add3_u32 v161, v161, v163, v164\n\t"
"v_lshlrev_b32 v161, 1, v161\n\t"
"v_lshlrev_b32 v162, 1, v162\n\t"
"v_mov_b32 v152, 0\n\t"
"v_mov_b32 v153, 0\n\t"
"v_mov_b32 v154, 0\n\t"
"v_mov_b32 v155, 0\n\t"
"v_mov_b32 v156, 0\n\t"
"v_mov_b32 v157, 0\n\t"
"v_mov_b32 v158, 0\n\t"
"v_mov_b32 v159, 0\n\t"
"v_mov_b32 v160, 0\n\t"
"s_mov_b32 s67, 0\n\t"
"Lfb0_%=:\n\t"
"s_mov_b32 s69, 0\n\t"
"Lfb1_%=:\n\t"
"s_lshr_b32 s60, s69, 5\n\t"
"s_mul_i32 s56, s60, 12288\n\t"
"s_bfe_u32 s61, s69, 0x20001\n\t"
"s_mul_i32 s61, s61, 1536\n\t"
"s_add_u32 s56, s56, s61\n\t"
"s_bitcmp1_b32 s69, 0\n\t"
"s_cselect_b32 s61, 48, 0\n\t"
"s_add_u32 s56, s56, s61\n\t"
"s_bfe_u32 s62, s69, 0x20003\n\t"
"s_mul_i32 s61, s62, 96\n\t"
"s_add_u32 s56, s56, s61\n\t"
"s_lshl3_add_u32 s56, s67, s56\n\t"
"s_lshr_b32 s60, s69, 5\n\t"
"s_bfe_u32 s62, s69, 0x20003\n\t"
"s_and_b32 s61, s60, 1\n\t"
"s_lshl1_add_u32 s61, s67, s61\n\t"
"s_mul_i32 s62, s62, 288\n\t"
"s_add_u32 s61, s61, s62\n\t"
"s_lshr_b32 s60, s60, 1\n\t"
"s_mul_i32 s62, s60, 96\n\t"
"s_add_u32 s61, s61, s62\n\t"
"s_lshl_b32 s57, s61, 10\n\t"
"s_and_b32 s61, s69, 7\n\t"
"s_lshl_b32 s61, s61, 7\n\t"
"s_add_u32 s57, s57, s61\n\t"
"s_add_u32 s60, s56, 0\n\t"
"buffer_load_short_d16_hi v152, v161, s[52:55], s60 offen\n\t"
"s_add_u32 s60, s56, 73728\n\t"
"buffer_load_short_d16_hi v153, v161, s[52:55], s60 offen\n\t"
"s_add_u32 s60, s56, 147456\n\t"
"buffer_load_short_d16_hi v154, v161, s[52:55], s60 offen\n\t"
"s_add_u32 s60, s56, 221184\n\t"
"buffer_load_short_d16_hi v155, v161, s[52:55], s60 offen\n\t"
"s_add_u32 s60, s56, 294912\n\t"
"buffer_load_short_d16_hi v156, v161, s[52:55], s60 offen\n\t"
"s_add_u32 s60, s56, 368640\n\t"
"buffer_load_short_d16_hi v157, v161, s[52:55], s60 offen\n\t"
"s_add_u32 s60, s56, 442368\n\t"
"buffer_load_short_d16_hi v158, v161, s[52:55], s60 offen\n\t"
"s_add_u32 s60, s56, 516096\n\t"
"buffer_load_short_d16_hi v159, v161, s[52:55], s60 offen\n\t"
"s_add_u32 s60, s56, 589824\n\t"
"buffer_load_short_d16_hi v160, v161, s[52:55], s60 offen\n\t"
"s_waitcnt vmcnt(2)\n\t"
"v_add_f32 v168, v152, v158\n\t"
"s_waitcnt vmcnt(1)\n\t"
"v_add_f32 v169, v153, v159\n\t"
"s_waitcnt vmcnt(0)\n\t"
"v_add_f32 v170, v154, v160\n\t"
"v_add_f32 v171, v168, v155\n\t"
"v_sub_f32 v174, v168, v155\n\t"
"v_add_f32 v172, v169, v156\n\t"
"v_sub_f32 v175, v169, v156\n\t"
"v_add_f32 v173, v170, v157\n\t"
"v_sub_f32 v176, v170, v157\n\t"
"v_add_f32 v177, v152, v154\n\t"
"v_add_f32 v178, v171, v173\n\t"
"v_add_f32 v179, v174, v176\n\t"
"v_add_f32 v180, v158, v160\n\t"
"v_add_f32 v182, v177, v153\n\t"
"v_sub_f32 v183, v177, v153\n\t"
"v_add_f32 v184, v178, v172\n\t"
"v_sub_f32 v185, v178, v172\n\t"
"v_add_f32 v186, v179, v175\n\t"
"v_sub_f32 v187, v179, v175\n\t"
"v_add_f32 v188, v180, v159\n\t"
"v_sub_f32 v189, v180, v159\n\t"
"v_mul_f32 v191, 0.5, v182\n\t"
"v_mul_f32 v192, 0.5, v183\n\t"
"v_mul_f32 v194, 0.5, v171\n\t"
"v_mul_f32 v195, 0x3e800000, v184\n\t"
"v_mul_f32 v196, 0x3e800000, v185\n\t"
"v_mul_f32 v197, -0.5, v173\n\t"
"v_mul_f32 v199, 0.5, v186\n\t"
"v_mul_f32 v200, 0.5, v187\n\t"
"v_mul_f32 v203, -0.5, v188\n\t"
"v_mul_f32 v204, -0.5, v189\n\t"
"v_cvt_pk_bf16_f32 v206, v152, v191\n\t"
"v_cvt_pk_bf16_f32 v207, v192, -v154\n\t"
"v_cvt_pk_bf16_f32 v208, v194, v195\n\t"
"v_cvt_pk_bf16_f32 v209, v196, v197\n\t"
"v_cvt_pk_bf16_f32 v210, v174, v199\n\t"
"v_cvt_pk_bf16_f32 v211, v200, -v176\n\t"
"v_cvt_pk_bf16_f32 v212, -v158, v203\n\t"
"v_cvt_pk_bf16_f32 v213, v204, v160\n\t"
"s_add_u32 s60, s57, 0\n\t"
"buffer_store_short v206, v162, s[8:11], s60 offen sc1\n\t"
"s_add_u32 s60, s57, 6144\n\t"
"buffer_store_short_d16_hi v206, v162, s[8:11], s60 offen sc1\n\t"
"s_add_u32 s60, s57, 12288\n\t"
"buffer_store_short v207, v162, s[8:11], s60 offen sc1\n\t"
"s_add_u32 s60, s57, 18432\n\t"
"buffer_store_short_d16_hi v207, v162, s[8:11], s60 offen sc1\n\t"
"s_add_u32 s60, s57, 24576\n\t"
"buffer_store_short v208, v162, s[8:11], s60 offen sc1\n\t"
"s_add_u32 s60, s57, 30720\n\t"
"buffer_store_short_d16_hi v208, v162, s[8:11], s60 offen sc1\n\t"
"s_add_u32 s60, s57, 36864\n\t"
"buffer_store_short v209, v162, s[8:11], s60 offen sc1\n\t"
"s_add_u32 s60, s57, 43008\n\t"
"buffer_store_short_d16_hi v209, v162, s[8:11], s60 offen sc1\n\t"
"s_add_u32 s60, s57, 49152\n\t"
"buffer_store_short v210, v162, s[8:11], s60 offen sc1\n\t"
"s_add_u32 s60, s57, 55296\n\t"
"buffer_store_short_d16_hi v210, v162, s[8:11], s60 offen sc1\n\t"
"s_add_u32 s60, s57, 61440\n\t"
"buffer_store_short v211, v162, s[8:11], s60 offen sc1\n\t"
"s_add_u32 s60, s57, 67584\n\t"
"buffer_store_short_d16_hi v211, v162, s[8:11], s60 offen sc1\n\t"
"s_add_u32 s60, s57, 73728\n\t"
"buffer_store_short v212, v162, s[8:11], s60 offen sc1\n\t"
"s_add_u32 s60, s57, 79872\n\t"
"buffer_store_short_d16_hi v212, v162, s[8:11], s60 offen sc1\n\t"
"s_add_u32 s60, s57, 86016\n\t"
"buffer_store_short v213, v162, s[8:11], s60 offen sc1\n\t"
"s_add_u32 s60, s57, 92160\n\t"
"buffer_store_short_d16_hi v213, v162, s[8:11], s60 offen sc1\n\t"
"s_waitcnt vmcnt(0)\n\t"
"s_add_u32 s69, s69, 1\n\t"
"s_cmp_lt_u32 s69, 192\n\t"
"s_cbranch_scc1 Lfb1_%=\n\t"
"s_add_u32 s67, s67, 1\n\t"
"s_cmp_lt_u32 s67, 3\n\t"
"s_cbranch_scc1 Lfb0_%=\n\t"
"s_branch Lgo_%=\n\t"
"Ltail_%=:\n\t"
"v_mfma_f32_16x16x32_bf16 v[104:107], a[72:75], a[152:155], v[104:107]\n\t"
"s_nop 0\n\t"
"ds_read_b128 a[144:147], v224 offset:18496\n\t"
"ds_read_b128 a[164:167], v224 offset:23104\n\t"
"v_mfma_f32_16x16x32_bf16 v[104:107], a[76:79], a[168:171], v[104:107]\n\t"
"buffer_load_dwordx4 a[124:127], v252, s[8:11], s51 offen offset:3072 sc1\n\t"
"v_mfma_f32_16x16x32_bf16 v[120:123], a[80:83], a[152:155], v[120:123]\n\t"
"s_nop 0\n\t"
"ds_read_b128 a[160:163], v224 offset:22592\n\t"
"ds_read_b128 a[148:151], v224 offset:19008\n\t"
"v_mfma_f32_16x16x32_bf16 v[120:123], a[84:87], a[168:171], v[120:123]\n\t"
"buffer_load_dwordx4 a[16:19], v252, s[84:87], s69 offen offset:0 sc1\n\t"
"v_mfma_f32_16x16x32_bf16 v[136:139], a[88:91], a[152:155], v[136:139]\n\t"
"ds_read_b128 v[8:11], v229 offset:19968\n\t"
"v_add_u32 v248, 101632, v226\n\t"
"v_mfma_f32_16x16x32_bf16 v[136:139], a[92:95], a[168:171], v[136:139]\n\t"
"buffer_load_dwordx4 a[20:23], v252, s[84:87], s69 offen offset:1024 sc1\n\t"
"v_mfma_f32_16x16x32_bf16 v[140:143], a[88:91], a[156:159], v[140:143]\n\t"
"ds_read_b128 v[24:27], v230 offset:19968\n\t"
"v_xor_b32 v249, 32, v248\n\t"
"v_mfma_f32_16x16x32_bf16 v[140:143], a[92:95], a[172:175], v[140:143]\n\t"
"buffer_load_dwordx4 a[12:15], v235, s[84:87], s69 offen offset:3072 sc1\n\t"
"v_mfma_f32_16x16x32_bf16 v[124:127], a[80:83], a[156:159], v[124:127]\n\t"
"ds_read_b128 a[152:155], v224 offset:19520\n\t"
"v_xor_b32 v250, 64, v248\n\t"
"v_mfma_f32_16x16x32_bf16 v[124:127], a[84:87], a[172:175], v[124:127]\n\t"
"buffer_load_dwordx4 a[8:11], v235, s[84:87], s69 offen offset:2048 sc1\n\t"
"v_mfma_f32_16x16x32_bf16 v[108:111], a[72:75], a[156:159], v[108:111]\n\t"
"ds_read_b128 a[168:171], v224 offset:23616\n\t"
"v_xor_b32 v251, 96, v248\n\t"
"v_mfma_f32_16x16x32_bf16 v[108:111], a[76:79], a[172:175], v[108:111]\n\t"
"s_nop 0\n\t"
"ds_read_b128 a[156:159], v224 offset:20032\n\t"
"ds_read_b128 a[172:175], v224 offset:24128\n\t"
"v_mfma_f32_16x16x32_bf16 v[192:195], a[224:227], a[176:179], 0\n\t"
"s_waitcnt lgkmcnt(5)\n\t"
"buffer_store_dwordx4 v[8:11], v232, s[16:19], s24 offen nt\n\t"
"v_mfma_f32_16x16x32_bf16 v[196:199], a[224:227], a[180:183], 0\n\t"
"s_nop 0\n\t"
"ds_read_b64_tr_b16 a[176:177], v248 offset:41472\n\t"
"ds_read_b64_tr_b16 a[178:179], v248 offset:41984\n\t"
"v_mfma_f32_16x16x32_bf16 v[200:203], a[224:227], a[184:187], 0\n\t"
"s_nop 0\n\t"
"ds_read_b64_tr_b16 a[180:181], v248 offset:42496\n\t"
"ds_read_b128 v[40:43], v231 offset:19968\n\t"
"v_mfma_f32_16x16x32_bf16 v[204:207], a[224:227], a[188:191], 0\n\t"
"buffer_load_dwordx4 a[0:3], v235, s[84:87], s69 offen offset:0 sc1\n\t"
"v_mfma_f32_16x16x32_bf16 v[208:211], a[224:227], a[192:195], 0\n\t"
"s_nop 0\n\t"
"ds_read_b64_tr_b16 a[184:185], v249 offset:41472\n\t"
"ds_read_b64_tr_b16 a[186:187], v249 offset:41984\n\t"
"v_mfma_f32_16x16x32_bf16 v[212:215], a[224:227], a[196:199], 0\n\t"
"s_nop 0\n\t"
"ds_read_b64_tr_b16 a[188:189], v249 offset:42496\n\t"
"ds_read_b64_tr_b16 a[192:193], v250 offset:41472\n\t"
"v_mfma_f32_16x16x32_bf16 v[216:219], a[224:227], a[200:203], 0\n\t"
"s_waitcnt lgkmcnt(11)\n\t"
"buffer_store_dwordx4 v[24:27], v233, s[16:19], s24 offen nt\n\t"
"v_mfma_f32_16x16x32_bf16 v[220:223], a[224:227], a[204:207], 0\n\t"
"s_waitcnt vmcnt(18)\n\t"
"ds_read_b64_tr_b16 a[194:195], v250 offset:41984\n\t"
"v_cvt_pk_bf16_f32 v193, v193, v197\n\t"
"v_mfma_f32_16x16x32_bf16 v[144:147], a[96:99], a[144:147], v[144:147]\n\t"
"ds_write_addtid_b32 v193 offset:256\n\t"
"v_cvt_pk_bf16_f32 v194, v194, v198\n\t"
"v_mfma_f32_16x16x32_bf16 v[144:147], a[100:103], a[160:163], v[144:147]\n\t"
"buffer_load_dwordx4 a[4:7], v235, s[84:87], s69 offen offset:1024 sc1\n\t"
"v_mfma_f32_16x16x32_bf16 v[160:163], a[104:107], a[144:147], v[160:163]\n\t"
"ds_read_b128 v[12:15], v229 offset:33280\n\t"
"v_cvt_pk_bf16_f32 v203, v203, v207\n\t"
"v_mfma_f32_16x16x32_bf16 v[160:163], a[108:111], a[160:163], v[160:163]\n\t"
"ds_write_addtid_b32 v203 offset:12576\n\t"
"buffer_load_dwordx4 a[40:43], v252, s[84:87], s81 offen offset:0 sc1\n\t"
"v_mfma_f32_16x16x32_bf16 v[176:179], a[112:115], a[144:147], v[176:179]\n\t"
"ds_read_b128 a[144:147], v224 offset:16448\n\t"
"v_cvt_pk_bf16_f32 v201, v201, v205\n\t"
"v_mfma_f32_16x16x32_bf16 v[176:179], a[116:119], a[160:163], v[176:179]\n\t"
"ds_write_addtid_b32 v201 offset:4352\n\t"
"v_cvt_pk_bf16_f32 v195, v195, v199\n\t"
"v_mfma_f32_16x16x32_bf16 v[180:183], a[112:115], a[148:151], v[180:183]\n\t"
"ds_read_b128 a[160:163], v224 offset:20544\n\t"
"v_cvt_pk_bf16_f32 v211, v211, v215\n\t"
"v_mfma_f32_16x16x32_bf16 v[180:183], a[116:119], a[164:167], v[180:183]\n\t"
"s_waitcnt lgkmcnt(7)\n\t"
"buffer_store_dwordx4 v[40:43], v234, s[16:19], s24 offen nt\n\t"
"v_mfma_f32_16x16x32_bf16 v[164:167], a[104:107], a[148:151], v[164:167]\n\t"
"ds_write_addtid_b32 v194 offset:8224\n\t"
"v_cvt_pk_bf16_f32 v217, v217, v221\n\t"
"v_mfma_f32_16x16x32_bf16 v[164:167], a[108:111], a[164:167], v[164:167]\n\t"
"ds_read_b128 v[28:31], v230 offset:33280\n\t"
"v_cvt_pk_bf16_f32 v219, v219, v223\n\t"
"v_mfma_f32_16x16x32_bf16 v[148:151], a[96:99], a[148:151], v[148:151]\n\t"
"ds_write_addtid_b32 v211 offset:10528\n\t"
"v_cvt_pk_bf16_f32 v208, v208, v212\n\t"
"v_mfma_f32_16x16x32_bf16 v[148:151], a[100:103], a[164:167], v[148:151]\n\t"
"buffer_load_dwordx4 a[44:47], v252, s[84:87], s81 offen offset:1024 sc1\n\t"
"v_mfma_f32_16x16x32_bf16 v[152:155], a[96:99], a[152:155], v[152:155]\n\t"
"ds_read_b128 a[164:167], v224 offset:21056\n\t"
"v_cvt_pk_bf16_f32 v210, v210, v214\n\t"
"v_mfma_f32_16x16x32_bf16 v[152:155], a[100:103], a[168:171], v[152:155]\n\t"
"ds_read_b128 a[148:151], v224 offset:16960\n\t"
"v_cvt_pk_bf16_f32 v209, v209, v213\n\t"
"v_mfma_f32_16x16x32_bf16 v[168:171], a[104:107], a[152:155], v[168:171]\n\t"
"ds_write_addtid_b32 v217 offset:6400\n\t"
"v_cvt_pk_bf16_f32 v192, v192, v196\n\t"
"v_mfma_f32_16x16x32_bf16 v[168:171], a[108:111], a[168:171], v[168:171]\n\t"
"s_waitcnt lgkmcnt(7)\n\t"
"buffer_store_dwordx4 v[12:15], v232, s[16:19], s27 offen nt\n\t"
"v_mfma_f32_16x16x32_bf16 v[184:187], a[112:115], a[152:155], v[184:187]\n\t"
"s_nop 0\n\t"
"ds_read_b128 a[152:155], v224 offset:17472\n\t"
"ds_read_b128 v[44:47], v231 offset:33280\n\t"
"v_mfma_f32_16x16x32_bf16 v[184:187], a[116:119], a[168:171], v[184:187]\n\t"
"ds_write_addtid_b32 v210 offset:10272\n\t"
"v_cvt_pk_bf16_f32 v200, v200, v204\n\t"
"v_mfma_f32_16x16x32_bf16 v[188:191], a[112:115], a[156:159], v[188:191]\n\t"
"ds_write_addtid_b32 v195 offset:8480\n\t"
"v_cvt_pk_bf16_f32 v216, v216, v220\n\t"
"v_mfma_f32_16x16x32_bf16 v[188:191], a[116:119], a[172:175], v[188:191]\n\t"
"buffer_load_dwordx4 a[36:39], v235, s[84:87], s81 offen offset:3072 sc1\n\t"
"v_mfma_f32_16x16x32_bf16 v[172:175], a[104:107], a[156:159], v[172:175]\n\t"
"s_nop 0\n\t"
"ds_write_addtid_b32 v216 offset:6144\n\t"
"ds_write_addtid_b32 v219 offset:14624\n\t"
"v_mfma_f32_16x16x32_bf16 v[172:175], a[108:111], a[172:175], v[172:175]\n\t"
"buffer_load_dwordx4 a[32:35], v235, s[84:87], s81 offen offset:2048 sc1\n\t"
"v_mfma_f32_16x16x32_bf16 v[156:159], a[96:99], a[156:159], v[156:159]\n\t"
"s_nop 0\n\t"
"ds_write_addtid_b32 v200 offset:4096\n\t"
"ds_write_addtid_b32 v208 offset:2048\n\t"
"v_mfma_f32_16x16x32_bf16 v[156:159], a[100:103], a[172:175], v[156:159]\n\t"
"s_waitcnt vmcnt(14) lgkmcnt(9)\n\t"
"ds_write_addtid_b32 v192 offset:0\n\t"
"ds_write_addtid_b32 v209 offset:2304\n\t"
"v_mfma_f32_16x16x32_bf16 v[96:99], a[120:123], a[144:147], v[96:99]\n\t"
"ds_read_b128 a[168:171], v224 offset:21568\n\t"
"v_cvt_pk_bf16_f32 v218, v218, v222\n\t"
"v_mfma_f32_16x16x32_bf16 v[96:99], a[124:127], a[160:163], v[96:99]\n\t"
"buffer_store_dwordx4 v[28:31], v233, s[16:19], s27 offen nt\n\t"
"v_mfma_f32_16x16x32_bf16 v[112:115], a[128:131], a[144:147], v[112:115]\n\t"
"ds_read_b128 a[172:175], v224 offset:22080\n\t"
"v_cvt_pk_bf16_f32 v202, v202, v206\n\t"
"v_mfma_f32_16x16x32_bf16 v[112:115], a[132:135], a[160:163], v[112:115]\n\t"
"s_nop 0\n\t"
"ds_write_addtid_b32 v202 offset:12320\n\t"
"ds_write_addtid_b32 v218 offset:14368\n\t"
"v_mfma_f32_16x16x32_bf16 v[128:131], a[136:139], a[144:147], v[128:131]\n\t"
"buffer_load_dwordx4 a[24:27], v235, s[84:87], s81 offen offset:0 sc1\n\t"
"v_mfma_f32_16x16x32_bf16 v[128:131], a[140:143], a[160:163], v[128:131]\n\t"
"s_nop 0\n\t"
"ds_read_b128 a[156:159], v224 offset:17984\n\t"
"ds_read_b64_tr_b16 a[196:197], v250 offset:42496\n\t"
"v_mfma_f32_16x16x32_bf16 v[132:135], a[136:139], a[148:151], v[132:135]\n\t"
"buffer_load_dwordx4 a[28:31], v235, s[84:87], s81 offen offset:1024 sc1\n\t"
"v_mfma_f32_16x16x32_bf16 v[132:135], a[140:143], a[164:167], v[132:135]\n\t"
"s_nop 0\n\t"
"ds_read_b64_tr_b16 a[200:201], v251 offset:41472\n\t"
"ds_read_b64_tr_b16 a[202:203], v251 offset:41984\n\t"
"v_mfma_f32_16x16x32_bf16 v[116:119], a[128:131], a[148:151], v[116:119]\n\t"
"ds_read_b64_tr_b16 a[204:205], v251 offset:42496\n\t"
"v_mfma_f32_16x16x32_bf16 v[116:119], a[132:135], a[164:167], v[116:119]\n\t"
"s_waitcnt lgkmcnt(9)\n\t"
"buffer_store_dwordx4 v[44:47], v234, s[16:19], s27 offen nt\n\t"
"v_mfma_f32_16x16x32_bf16 v[100:103], a[120:123], a[148:151], v[100:103]\n\t"
"v_mfma_f32_16x16x32_bf16 v[100:103], a[124:127], a[164:167], v[100:103]\n\t"
"s_waitcnt lgkmcnt(0)\n\t"
"s_barrier\n\t"
"v_mfma_f32_16x16x32_bf16 v[104:107], a[120:123], a[152:155], v[104:107]\n\t"
"ds_read_b128 a[160:163], v224 offset:4096\n\t"
"v_mfma_f32_16x16x32_bf16 v[104:107], a[124:127], a[168:171], v[104:107]\n\t"
"ds_read_b128 a[144:147], v224 offset:0\n\t"
"v_mfma_f32_16x16x32_bf16 v[120:123], a[128:131], a[152:155], v[120:123]\n\t"
"ds_read_b128 a[148:151], v224 offset:512\n\t"
"v_mfma_f32_16x16x32_bf16 v[120:123], a[132:135], a[168:171], v[120:123]\n\t"
"ds_read_b128 a[164:167], v224 offset:4608\n\t"
"v_mfma_f32_16x16x32_bf16 v[136:139], a[136:139], a[152:155], v[136:139]\n\t"
"ds_read_b128 a[152:155], v224 offset:1024\n\t"
"v_mfma_f32_16x16x32_bf16 v[136:139], a[140:143], a[168:171], v[136:139]\n\t"
"buffer_load_dwordx4 a[64:67], v252, s[84:87], s57 offen offset:0 sc1\n\t"
"v_mfma_f32_16x16x32_bf16 v[140:143], a[136:139], a[156:159], v[140:143]\n\t"
"ds_read_b128 a[168:171], v224 offset:5120\n\t"
"v_mfma_f32_16x16x32_bf16 v[140:143], a[140:143], a[172:175], v[140:143]\n\t"
"buffer_load_dwordx4 a[68:71], v252, s[84:87], s57 offen offset:1024 sc1\n\t"
"v_mfma_f32_16x16x32_bf16 v[124:127], a[128:131], a[156:159], v[124:127]\n\t"
"v_mfma_f32_16x16x32_bf16 v[124:127], a[132:135], a[172:175], v[124:127]\n\t"
"v_mfma_f32_16x16x32_bf16 v[108:111], a[120:123], a[156:159], v[108:111]\n\t"
"ds_read_b128 a[156:159], v224 offset:1536\n\t"
"v_mfma_f32_16x16x32_bf16 v[108:111], a[124:127], a[172:175], v[108:111]\n\t"
"buffer_load_dwordx4 a[60:63], v235, s[84:87], s57 offen offset:3072 sc1\n\t"
"v_mfma_f32_16x16x32_bf16 v[192:195], a[220:223], a[176:179], 0\n\t"
"s_nop 0\n\t"
"ds_read_b128 a[172:175], v224 offset:5632\n\t"
"ds_read_b64_tr_b16 a[176:177], v248 offset:0\n\t"
"v_mfma_f32_16x16x32_bf16 v[196:199], a[220:223], a[184:187], 0\n\t"
"buffer_load_dwordx4 a[56:59], v235, s[84:87], s57 offen offset:2048 sc1\n\t"
"v_mfma_f32_16x16x32_bf16 v[200:203], a[220:223], a[178:181], 0\n\t"
"s_nop 0\n\t"
"ds_read_b64_tr_b16 a[184:185], v249 offset:0\n\t"
"ds_read_b64_tr_b16 a[178:179], v248 offset:512\n\t"
"v_mfma_f32_16x16x32_bf16 v[204:207], a[220:223], a[186:189], 0\n\t"
"s_nop 0\n\t"
"ds_read_b64_tr_b16 a[180:181], v248 offset:1024\n\t"
"ds_read_b64_tr_b16 a[186:187], v249 offset:512\n\t"
"v_mfma_f32_16x16x32_bf16 v[208:211], a[220:223], a[192:195], 0\n\t"
"s_nop 0\n\t"
"ds_read_b64_tr_b16 a[188:189], v249 offset:1024\n\t"
"ds_read_b64_tr_b16 a[192:193], v250 offset:0\n\t"
"v_mfma_f32_16x16x32_bf16 v[212:215], a[220:223], a[200:203], 0\n\t"
".p2align 3\n\t"
"ds_read_b64_tr_b16 a[200:201], v251 offset:0\n\t"
"buffer_load_dwordx4 a[48:51], v235, s[84:87], s57 offen offset:0 sc1\n\t"
"v_pk_add_f32 v[96:97], v[96:97], v[144:145]\n\t"
"v_pk_add_f32 v[98:99], v[98:99], v[146:147]\n\t"
"v_pk_fma_f32 v[144:145], v[144:145], s[38:39], v[96:97]\n\t"
"v_pk_fma_f32 v[146:147], v[146:147], s[38:39], v[98:99]\n\t"
"v_pk_add_f32 v[112:113], v[112:113], v[160:161]\n\t"
"v_pk_add_f32 v[114:115], v[114:115], v[162:163]\n\t"
"v_pk_fma_f32 v[160:161], v[160:161], s[38:39], v[112:113]\n\t"
"v_pk_fma_f32 v[162:163], v[162:163], s[38:39], v[114:115]\n\t"
"v_pk_add_f32 v[128:129], v[128:129], v[176:177]\n\t"
"v_pk_add_f32 v[130:131], v[130:131], v[178:179]\n\t"
"v_pk_fma_f32 v[176:177], v[176:177], s[38:39], v[128:129]\n\t"
"v_pk_fma_f32 v[178:179], v[178:179], s[38:39], v[130:131]\n\t"
"v_pk_add_f32 v[132:133], v[132:133], v[180:181]\n\t"
"v_pk_add_f32 v[134:135], v[134:135], v[182:183]\n\t"
"v_pk_fma_f32 v[180:181], v[180:181], s[38:39], v[132:133]\n\t"
"v_pk_fma_f32 v[182:183], v[182:183], s[38:39], v[134:135]\n\t"
"buffer_load_dwordx4 a[52:55], v235, s[84:87], s57 offen offset:1024 sc1\n\t"
"v_pk_add_f32 v[116:117], v[116:117], v[164:165]\n\t"
"v_pk_add_f32 v[118:119], v[118:119], v[166:167]\n\t"
"v_pk_fma_f32 v[164:165], v[164:165], s[38:39], v[116:117]\n\t"
"v_pk_fma_f32 v[166:167], v[166:167], s[38:39], v[118:119]\n\t"
"v_pk_add_f32 v[100:101], v[100:101], v[148:149]\n\t"
"v_pk_add_f32 v[102:103], v[102:103], v[150:151]\n\t"
"v_pk_fma_f32 v[148:149], v[148:149], s[38:39], v[100:101]\n\t"
"v_pk_fma_f32 v[150:151], v[150:151], s[38:39], v[102:103]\n\t"
"v_pk_add_f32 v[104:105], v[104:105], v[152:153]\n\t"
"v_pk_add_f32 v[106:107], v[106:107], v[154:155]\n\t"
"v_pk_fma_f32 v[152:153], v[152:153], s[38:39], v[104:105]\n\t"
"v_pk_fma_f32 v[154:155], v[154:155], s[38:39], v[106:107]\n\t"
"v_pk_add_f32 v[120:121], v[120:121], v[168:169]\n\t"
"v_pk_add_f32 v[122:123], v[122:123], v[170:171]\n\t"
"v_pk_fma_f32 v[168:169], v[168:169], s[38:39], v[120:121]\n\t"
"v_pk_fma_f32 v[170:171], v[170:171], s[38:39], v[122:123]\n\t"
"buffer_load_dwordx4 a[88:91], v252, s[84:87], s63 offen offset:0 sc1\n\t"
"v_pk_add_f32 v[136:137], v[136:137], v[184:185]\n\t"
"v_pk_add_f32 v[138:139], v[138:139], v[186:187]\n\t"
"v_pk_fma_f32 v[184:185], v[184:185], s[38:39], v[136:137]\n\t"
"v_pk_fma_f32 v[186:187], v[186:187], s[38:39], v[138:139]\n\t"
"v_pk_add_f32 v[140:141], v[140:141], v[188:189]\n\t"
"v_pk_add_f32 v[142:143], v[142:143], v[190:191]\n\t"
"v_pk_fma_f32 v[188:189], v[188:189], s[38:39], v[140:141]\n\t"
"v_pk_fma_f32 v[190:191], v[190:191], s[38:39], v[142:143]\n\t"
"v_pk_add_f32 v[124:125], v[124:125], v[172:173]\n\t"
"v_pk_add_f32 v[126:127], v[126:127], v[174:175]\n\t"
"v_pk_fma_f32 v[172:173], v[172:173], s[38:39], v[124:125]\n\t"
"v_pk_fma_f32 v[174:175], v[174:175], s[38:39], v[126:127]\n\t"
"v_pk_add_f32 v[108:109], v[108:109], v[156:157]\n\t"
"v_pk_add_f32 v[110:111], v[110:111], v[158:159]\n\t"
"v_pk_fma_f32 v[156:157], v[156:157], s[38:39], v[108:109]\n\t"
"v_pk_fma_f32 v[158:159], v[158:159], s[38:39], v[110:111]\n\t"
"v_cvt_pk_bf16_f32 v96, v96, v97\n\t"
"v_cvt_pk_bf16_f32 v100, v100, v101\n\t"
"v_cvt_pk_bf16_f32 v101, v102, v103\n\t"
"v_cvt_pk_bf16_f32 v120, v120, v121\n\t"
"v_mfma_f32_16x16x32_bf16 v[216:219], a[220:223], a[194:197], 0\n\t"
"v_cvt_pk_bf16_f32 v97, v98, v99\n\t"
"v_cvt_pk_bf16_f32 v132, v132, v133\n\t"
"v_cvt_pk_bf16_f32 v133, v134, v135\n\t"
"v_mfma_f32_16x16x32_bf16 v[220:223], a[220:223], a[202:205], 0\n\t"
"s_waitcnt vmcnt(17) lgkmcnt(10)\n\t"
"ds_write2st64_b64 v228, v[96:97], v[100:101] offset0:39 offset1:52\n\t"
"v_cvt_pk_bf16_f32 v112, v112, v113\n\t"
"v_cvt_pk_bf16_f32 v124, v124, v125\n\t"
"v_mfma_f32_16x16x32_bf16 v[48:51], a[0:3], a[144:147], v[48:51]\n\t"
"v_cvt_pk_bf16_f32 v128, v128, v129\n\t"
"v_cvt_pk_bf16_f32 v108, v108, v109\n\t"
"v_mfma_f32_16x16x32_bf16 v[48:51], a[4:7], a[160:163], v[48:51]\n\t"
"v_cvt_pk_bf16_f32 v203, v203, v207\n\t"
"v_cvt_pk_bf16_f32 v129, v130, v131\n\t"
"v_mfma_f32_16x16x32_bf16 v[64:67], a[8:11], a[144:147], v[64:67]\n\t"
"v_cvt_pk_bf16_f32 v116, v116, v117\n\t"
"v_cvt_pk_bf16_f32 v104, v104, v105\n\t"
"v_mfma_f32_16x16x32_bf16 v[64:67], a[12:15], a[160:163], v[64:67]\n\t"
"ds_write_addtid_b32 v203 offset:26976\n\t"
"ds_write2st64_b64 v247, v[128:129], v[132:133] offset0:39 offset1:52\n\t"
"v_mfma_f32_16x16x32_bf16 v[80:83], a[16:19], a[144:147], v[80:83]\n\t"
"v_cvt_pk_bf16_f32 v113, v114, v115\n\t"
"v_cvt_pk_bf16_f32 v117, v118, v119\n\t"
"v_mfma_f32_16x16x32_bf16 v[80:83], a[20:23], a[160:163], v[80:83]\n\t"
"v_cvt_pk_bf16_f32 v210, v210, v214\n\t"
"v_cvt_pk_bf16_f32 v105, v106, v107\n\t"
"v_mfma_f32_16x16x32_bf16 v[84:87], a[16:19], a[148:151], v[84:87]\n\t"
"v_cvt_pk_bf16_f32 v200, v200, v204\n\t"
"v_cvt_pk_bf16_f32 v109, v110, v111\n\t"
"v_mfma_f32_16x16x32_bf16 v[84:87], a[20:23], a[164:167], v[84:87]\n\t"
"s_nop 0\n\t"
"ds_read_b128 a[144:147], v224 offset:2048\n\t"
"ds_read_b128 a[160:163], v224 offset:6144\n\t"
"ds_write_addtid_b32 v200 offset:16704\n\t"
"ds_write_addtid_b32 v210 offset:22592\n\t"
"ds_write2st64_b64 v246, v[112:113], v[116:117] offset0:39 offset1:52\n\t"
"v_mfma_f32_16x16x32_bf16 v[68:71], a[8:11], a[148:151], v[68:71]\n\t"
"v_cvt_pk_bf16_f32 v193, v193, v197\n\t"
"v_cvt_pk_bf16_f32 v121, v122, v123\n\t"
"v_mfma_f32_16x16x32_bf16 v[68:71], a[12:15], a[164:167], v[68:71]\n\t"
"buffer_load_dwordx4 a[92:95], v252, s[84:87], s63 offen offset:1024 sc1\n\t"
"v_mfma_f32_16x16x32_bf16 v[52:55], a[0:3], a[148:151], v[52:55]\n\t"
"v_cvt_pk_bf16_f32 v195, v195, v199\n\t"
"v_cvt_pk_bf16_f32 v136, v136, v137\n\t"
"v_mfma_f32_16x16x32_bf16 v[52:55], a[4:7], a[164:167], v[52:55]\n\t"
"s_nop 0\n\t"
"ds_read_b128 a[148:151], v224 offset:2560\n\t"
"ds_read_b128 a[164:167], v224 offset:6656\n\t"
"ds_write_addtid_b32 v193 offset:24672\n\t"
"ds_write_addtid_b32 v195 offset:26720\n\t"
"ds_write2st64_b64 v228, v[104:105], v[108:109] offset0:65 offset1:78\n\t"
"v_mfma_f32_16x16x32_bf16 v[56:59], a[0:3], a[152:155], v[56:59]\n\t"
"v_cvt_pk_bf16_f32 v137, v138, v139\n\t"
"v_cvt_pk_bf16_f32 v140, v140, v141\n\t"
"v_mfma_f32_16x16x32_bf16 v[56:59], a[4:7], a[168:171], v[56:59]\n\t"
"buffer_load_dwordx4 a[84:87], v235, s[84:87], s63 offen offset:3072 sc1\n\t"
"v_mfma_f32_16x16x32_bf16 v[72:75], a[8:11], a[152:155], v[72:75]\n\t"
"v_cvt_pk_bf16_f32 v209, v209, v213\n\t"
"v_cvt_pk_bf16_f32 v125, v126, v127\n\t"
"v_mfma_f32_16x16x32_bf16 v[72:75], a[12:15], a[168:171], v[72:75]\n\t"
"ds_write2st64_b64 v246, v[120:121], v[124:125] offset0:65 offset1:78\n\t"
"v_mfma_f32_16x16x32_bf16 v[88:91], a[16:19], a[152:155], v[88:91]\n\t"
"v_cvt_pk_bf16_f32 v208, v208, v212\n\t"
"v_cvt_pk_bf16_f32 v216, v216, v220\n\t"
"v_mfma_f32_16x16x32_bf16 v[88:91], a[20:23], a[168:171], v[88:91]\n\t"
"s_waitcnt lgkmcnt(6)\n\t"
"buffer_load_dwordx4 a[80:83], v235, s[84:87], s63 offen offset:2048 sc1\n\t"
"v_mfma_f32_16x16x32_bf16 v[92:95], a[16:19], a[156:159], v[92:95]\n\t"
"v_cvt_pk_bf16_f32 v194, v194, v198\n\t"
"v_cvt_pk_bf16_f32 v141, v142, v143\n\t"
"v_mfma_f32_16x16x32_bf16 v[92:95], a[20:23], a[172:175], v[92:95]\n\t"
"ds_write2st64_b64 v247, v[136:137], v[140:141] offset0:65 offset1:78\n\t"
"v_mfma_f32_16x16x32_bf16 v[76:79], a[8:11], a[156:159], v[76:79]\n\t"
"v_cvt_pk_bf16_f32 v211, v211, v215\n\t"
"v_cvt_pk_bf16_f32 v219, v219, v223\n\t"
"v_mfma_f32_16x16x32_bf16 v[76:79], a[12:15], a[172:175], v[76:79]\n\t"
"buffer_load_dwordx4 a[72:75], v235, s[84:87], s63 offen offset:0 sc1\n\t"
"v_mfma_f32_16x16x32_bf16 v[60:63], a[0:3], a[156:159], v[60:63]\n\t"
"v_cvt_pk_bf16_f32 v217, v217, v221\n\t"
"v_cvt_pk_bf16_f32 v218, v218, v222\n\t"
"v_mfma_f32_16x16x32_bf16 v[60:63], a[4:7], a[172:175], v[60:63]\n\t"
"s_waitcnt vmcnt(12) lgkmcnt(2)\n\t"
"ds_write_addtid_b32 v194 offset:18496\n\t"
"v_cvt_pk_bf16_f32 v202, v202, v206\n\t"
"v_mfma_f32_16x16x32_bf16 v[144:147], a[24:27], a[144:147], v[144:147]\n\t"
"ds_write_addtid_b32 v218 offset:22848\n\t"
"v_cvt_pk_bf16_f32 v192, v192, v196\n\t"
"v_mfma_f32_16x16x32_bf16 v[144:147], a[28:31], a[160:163], v[144:147]\n\t"
"s_nop 0\n\t"
"ds_write_addtid_b32 v208 offset:20544\n\t"
"ds_write_addtid_b32 v209 offset:28768\n\t"
"ds_write_addtid_b32 v216 offset:20800\n\t"
"v_mfma_f32_16x16x32_bf16 v[160:163], a[32:35], a[144:147], v[160:163]\n\t"
"ds_write_addtid_b32 v217 offset:29024\n\t"
"v_cvt_pk_bf16_f32 v201, v201, v205\n\t"
"v_mfma_f32_16x16x32_bf16 v[160:163], a[36:39], a[160:163], v[160:163]\n\t"
"s_nop 0\n\t"
"ds_write_addtid_b32 v192 offset:16448\n\t"
"ds_write_addtid_b32 v201 offset:24928\n\t"
"ds_write_addtid_b32 v202 offset:18752\n\t"
"ds_write_addtid_b32 v211 offset:30816\n\t"
"ds_write_addtid_b32 v219 offset:31072\n\t"
"v_mfma_f32_16x16x32_bf16 v[176:179], a[40:43], a[144:147], v[176:179]\n\t"
"s_nop 0\n\t"
"ds_read_b128 a[168:171], v224 offset:7168\n\t"
"ds_read_b128 a[156:159], v224 offset:3584\n\t"
"v_mfma_f32_16x16x32_bf16 v[176:179], a[44:47], a[160:163], v[176:179]\n\t"
"buffer_load_dwordx4 a[76:79], v235, s[84:87], s63 offen offset:1024 sc1\n\t"
"v_mfma_f32_16x16x32_bf16 v[180:183], a[40:43], a[148:151], v[180:183]\n\t"
"s_nop 0\n\t"
"ds_read_b128 a[152:155], v224 offset:3072\n\t"
"ds_read_b128 a[172:175], v224 offset:7680\n\t"
"v_mfma_f32_16x16x32_bf16 v[180:183], a[44:47], a[164:167], v[180:183]\n\t"
"s_nop 0\n\t"
"ds_read_b64_tr_b16 a[194:195], v250 offset:512\n\t"
"ds_read_b64_tr_b16 a[196:197], v250 offset:1024\n\t"
"v_mfma_f32_16x16x32_bf16 v[164:167], a[32:35], a[148:151], v[164:167]\n\t"
"s_nop 0\n\t"
"ds_read_b64_tr_b16 a[202:203], v251 offset:512\n\t"
"ds_read_b64_tr_b16 a[204:205], v251 offset:1024\n\t"
"v_mfma_f32_16x16x32_bf16 v[164:167], a[36:39], a[164:167], v[164:167]\n\t"
"buffer_load_dwordx4 a[112:115], v252, s[84:87], s42 offen offset:0 sc1\n\t"
"v_mfma_f32_16x16x32_bf16 v[148:151], a[24:27], a[148:151], v[148:151]\n\t"
"v_mfma_f32_16x16x32_bf16 v[148:151], a[28:31], a[164:167], v[148:151]\n\t"
"s_waitcnt lgkmcnt(0)\n\t"
"s_barrier\n\t"
"v_mfma_f32_16x16x32_bf16 v[152:155], a[24:27], a[152:155], v[152:155]\n\t"
"ds_read_b128 a[144:147], v224 offset:16448\n\t"
"v_add_u32 v248, 53632, v226\n\t"
"v_mfma_f32_16x16x32_bf16 v[152:155], a[28:31], a[168:171], v[152:155]\n\t"
"ds_read_b128 a[160:163], v224 offset:20544\n\t"
"v_xor_b32 v249, 32, v248\n\t"
"v_mfma_f32_16x16x32_bf16 v[168:171], a[32:35], a[152:155], v[168:171]\n\t"
"ds_read_b128 a[148:151], v224 offset:16960\n\t"
"v_xor_b32 v250, 64, v248\n\t"
"v_mfma_f32_16x16x32_bf16 v[168:171], a[36:39], a[168:171], v[168:171]\n\t"
"ds_read_b128 a[164:167], v224 offset:21056\n\t"
"v_xor_b32 v251, 96, v248\n\t"
"v_mfma_f32_16x16x32_bf16 v[184:187], a[40:43], a[152:155], v[184:187]\n\t"
"ds_read_b128 v[104:107], v229 offset:19968\n\t"
"v_mfma_f32_16x16x32_bf16 v[184:187], a[44:47], a[168:171], v[184:187]\n\t"
"buffer_load_dwordx4 a[116:119], v252, s[84:87], s42 offen offset:1024 sc1\n\t"
"v_mfma_f32_16x16x32_bf16 v[188:191], a[40:43], a[156:159], v[188:191]\n\t"
"ds_read_b128 a[168:171], v224 offset:21568\n\t"
"v_mfma_f32_16x16x32_bf16 v[188:191], a[44:47], a[172:175], v[188:191]\n\t"
"buffer_load_dwordx4 a[108:111], v235, s[84:87], s42 offen offset:3072 sc1\n\t"
"v_mfma_f32_16x16x32_bf16 v[172:175], a[32:35], a[156:159], v[172:175]\n\t"
"ds_read_b128 a[152:155], v224 offset:17472\n\t"
"v_mfma_f32_16x16x32_bf16 v[172:175], a[36:39], a[172:175], v[172:175]\n\t"
"v_mfma_f32_16x16x32_bf16 v[156:159], a[24:27], a[156:159], v[156:159]\n\t"
"ds_read_b128 a[156:159], v224 offset:17984\n\t"
"v_mfma_f32_16x16x32_bf16 v[156:159], a[28:31], a[172:175], v[156:159]\n\t"
"s_waitcnt lgkmcnt(3)\n\t"
"buffer_store_dwordx4 v[104:107], v232, s[16:19], s20 offen nt\n\t"
"v_mfma_f32_16x16x32_bf16 v[192:195], a[220:223], a[176:179], 0\n\t"
"s_nop 0\n\t"
"ds_read_b128 a[172:175], v224 offset:22080\n\t"
"ds_read_b64_tr_b16 a[176:177], v248 offset:0\n\t"
"v_mfma_f32_16x16x32_bf16 v[196:199], a[220:223], a[184:187], 0\n\t"
"ds_read_b64_tr_b16 a[184:185], v249 offset:0\n\t"
"v_mfma_f32_16x16x32_bf16 v[200:203], a[220:223], a[178:181], 0\n\t"
"s_nop 0\n\t"
"ds_read_b64_tr_b16 a[178:179], v248 offset:512\n\t"
"ds_read_b64_tr_b16 a[180:181], v248 offset:1024\n\t"
"v_mfma_f32_16x16x32_bf16 v[204:207], a[220:223], a[186:189], 0\n\t"
".p2align 3\n\t"
"buffer_load_dwordx4 a[104:107], v235, s[84:87], s42 offen offset:2048 sc1\n\t"
"v_pk_add_f32 v[144:145], v[48:49], v[144:145] neg_lo:[0,1] neg_hi:[0,1]\n\t"
"v_pk_add_f32 v[146:147], v[50:51], v[146:147] neg_lo:[0,1] neg_hi:[0,1]\n\t"
"v_pk_fma_f32 v[48:49], v[144:145], s[90:91], v[48:49] neg_lo:[1,0,0] neg_hi:[1,0,0]\n\t"
"v_pk_fma_f32 v[50:51], v[146:147], s[90:91], v[50:51] neg_lo:[1,0,0] neg_hi:[1,0,0]\n\t"
"v_pk_add_f32 v[160:161], v[64:65], v[160:161] neg_lo:[0,1] neg_hi:[0,1]\n\t"
"v_pk_add_f32 v[162:163], v[66:67], v[162:163] neg_lo:[0,1] neg_hi:[0,1]\n\t"
"v_pk_fma_f32 v[64:65], v[160:161], s[90:91], v[64:65] neg_lo:[1,0,0] neg_hi:[1,0,0]\n\t"
"v_pk_fma_f32 v[66:67], v[162:163], s[90:91], v[66:67] neg_lo:[1,0,0] neg_hi:[1,0,0]\n\t"
"v_pk_add_f32 v[176:177], v[80:81], v[176:177] neg_lo:[0,1] neg_hi:[0,1]\n\t"
"v_pk_add_f32 v[178:179], v[82:83], v[178:179] neg_lo:[0,1] neg_hi:[0,1]\n\t"
"v_pk_fma_f32 v[80:81], v[176:177], s[90:91], v[80:81] neg_lo:[1,0,0] neg_hi:[1,0,0]\n\t"
"v_pk_fma_f32 v[82:83], v[178:179], s[90:91], v[82:83] neg_lo:[1,0,0] neg_hi:[1,0,0]\n\t"
"v_pk_add_f32 v[180:181], v[84:85], v[180:181] neg_lo:[0,1] neg_hi:[0,1]\n\t"
"v_pk_add_f32 v[182:183], v[86:87], v[182:183] neg_lo:[0,1] neg_hi:[0,1]\n\t"
"v_pk_fma_f32 v[84:85], v[180:181], s[90:91], v[84:85] neg_lo:[1,0,0] neg_hi:[1,0,0]\n\t"
"v_pk_fma_f32 v[86:87], v[182:183], s[90:91], v[86:87] neg_lo:[1,0,0] neg_hi:[1,0,0]\n\t"
"v_pk_add_f32 v[164:165], v[68:69], v[164:165] neg_lo:[0,1] neg_hi:[0,1]\n\t"
"v_pk_add_f32 v[166:167], v[70:71], v[166:167] neg_lo:[0,1] neg_hi:[0,1]\n\t"
"v_pk_fma_f32 v[68:69], v[164:165], s[90:91], v[68:69] neg_lo:[1,0,0] neg_hi:[1,0,0]\n\t"
"v_pk_fma_f32 v[70:71], v[166:167], s[90:91], v[70:71] neg_lo:[1,0,0] neg_hi:[1,0,0]\n\t"
"v_pk_add_f32 v[148:149], v[52:53], v[148:149] neg_lo:[0,1] neg_hi:[0,1]\n\t"
"v_pk_add_f32 v[150:151], v[54:55], v[150:151] neg_lo:[0,1] neg_hi:[0,1]\n\t"
"v_pk_fma_f32 v[52:53], v[148:149], s[90:91], v[52:53] neg_lo:[1,0,0] neg_hi:[1,0,0]\n\t"
"v_pk_fma_f32 v[54:55], v[150:151], s[90:91], v[54:55] neg_lo:[1,0,0] neg_hi:[1,0,0]\n\t"
"buffer_load_dwordx4 a[96:99], v235, s[84:87], s42 offen offset:0 sc1\n\t"
"v_pk_add_f32 v[152:153], v[56:57], v[152:153] neg_lo:[0,1] neg_hi:[0,1]\n\t"
"v_pk_add_f32 v[154:155], v[58:59], v[154:155] neg_lo:[0,1] neg_hi:[0,1]\n\t"
"v_pk_fma_f32 v[56:57], v[152:153], s[90:91], v[56:57] neg_lo:[1,0,0] neg_hi:[1,0,0]\n\t"
"v_pk_fma_f32 v[58:59], v[154:155], s[90:91], v[58:59] neg_lo:[1,0,0] neg_hi:[1,0,0]\n\t"
"v_pk_add_f32 v[168:169], v[72:73], v[168:169] neg_lo:[0,1] neg_hi:[0,1]\n\t"
"v_pk_add_f32 v[170:171], v[74:75], v[170:171] neg_lo:[0,1] neg_hi:[0,1]\n\t"
"v_pk_fma_f32 v[72:73], v[168:169], s[90:91], v[72:73] neg_lo:[1,0,0] neg_hi:[1,0,0]\n\t"
"v_pk_fma_f32 v[74:75], v[170:171], s[90:91], v[74:75] neg_lo:[1,0,0] neg_hi:[1,0,0]\n\t"
"v_pk_add_f32 v[184:185], v[88:89], v[184:185] neg_lo:[0,1] neg_hi:[0,1]\n\t"
"v_pk_add_f32 v[186:187], v[90:91], v[186:187] neg_lo:[0,1] neg_hi:[0,1]\n\t"
"v_pk_fma_f32 v[88:89], v[184:185], s[90:91], v[88:89] neg_lo:[1,0,0] neg_hi:[1,0,0]\n\t"
"v_pk_fma_f32 v[90:91], v[186:187], s[90:91], v[90:91] neg_lo:[1,0,0] neg_hi:[1,0,0]\n\t"
"v_pk_add_f32 v[188:189], v[92:93], v[188:189] neg_lo:[0,1] neg_hi:[0,1]\n\t"
"v_pk_add_f32 v[190:191], v[94:95], v[190:191] neg_lo:[0,1] neg_hi:[0,1]\n\t"
"v_pk_fma_f32 v[92:93], v[188:189], s[90:91], v[92:93] neg_lo:[1,0,0] neg_hi:[1,0,0]\n\t"
"v_pk_fma_f32 v[94:95], v[190:191], s[90:91], v[94:95] neg_lo:[1,0,0] neg_hi:[1,0,0]\n\t"
"v_pk_add_f32 v[172:173], v[76:77], v[172:173] neg_lo:[0,1] neg_hi:[0,1]\n\t"
"v_pk_add_f32 v[174:175], v[78:79], v[174:175] neg_lo:[0,1] neg_hi:[0,1]\n\t"
"v_pk_fma_f32 v[76:77], v[172:173], s[90:91], v[76:77] neg_lo:[1,0,0] neg_hi:[1,0,0]\n\t"
"v_pk_fma_f32 v[78:79], v[174:175], s[90:91], v[78:79] neg_lo:[1,0,0] neg_hi:[1,0,0]\n\t"
"v_pk_add_f32 v[156:157], v[60:61], v[156:157] neg_lo:[0,1] neg_hi:[0,1]\n\t"
"v_pk_add_f32 v[158:159], v[62:63], v[158:159] neg_lo:[0,1] neg_hi:[0,1]\n\t"
"v_pk_fma_f32 v[60:61], v[156:157], s[90:91], v[60:61] neg_lo:[1,0,0] neg_hi:[1,0,0]\n\t"
"v_pk_fma_f32 v[62:63], v[158:159], s[90:91], v[62:63] neg_lo:[1,0,0] neg_hi:[1,0,0]\n\t"
"v_mfma_f32_16x16x32_bf16 v[208:211], a[220:223], a[192:195], 0\n\t"
"buffer_load_dwordx4 a[100:103], v235, s[84:87], s42 offen offset:1024 sc1\n\t"
"v_mfma_f32_16x16x32_bf16 v[212:215], a[220:223], a[200:203], 0\n\t"
"s_nop 0\n\t"
"ds_read_b64_tr_b16 a[186:187], v249 offset:512\n\t"
"ds_read_b64_tr_b16 a[188:189], v249 offset:1024\n\t"
"v_mfma_f32_16x16x32_bf16 v[216:219], a[220:223], a[194:197], 0\n\t"
"buffer_load_dwordx4 a[136:139], v252, s[84:87], s48 offen offset:0 sc1\n\t"
"v_mfma_f32_16x16x32_bf16 v[220:223], a[220:223], a[202:205], 0\n\t"
"s_waitcnt vmcnt(14) lgkmcnt(8)\n\t"
"ds_read_b128 v[120:123], v230 offset:19968\n\t"
"v_cvt_pk_bf16_f32 v193, v193, v197\n\t"
"v_mfma_f32_16x16x32_bf16 v[48:51], a[48:51], a[144:147], v[48:51]\n\t"
"ds_read_b128 v[108:111], v229 offset:33280\n\t"
"v_cvt_pk_bf16_f32 v194, v194, v198\n\t"
"v_mfma_f32_16x16x32_bf16 v[48:51], a[52:55], a[160:163], v[48:51]\n\t"
"buffer_load_dwordx4 a[140:143], v252, s[84:87], s48 offen offset:1024 sc1\n\t"
"v_mfma_f32_16x16x32_bf16 v[64:67], a[56:59], a[144:147], v[64:67]\n\t"
"ds_read_b64_tr_b16 a[192:193], v250 offset:0\n\t"
"v_cvt_pk_bf16_f32 v203, v203, v207\n\t"
"v_mfma_f32_16x16x32_bf16 v[64:67], a[60:63], a[160:163], v[64:67]\n\t"
"buffer_load_dwordx4 a[132:135], v235, s[84:87], s48 offen offset:3072 sc1\n\t"
"v_mfma_f32_16x16x32_bf16 v[80:83], a[64:67], a[144:147], v[80:83]\n\t"
"ds_write_addtid_b32 v194 offset:2048\n\t"
"v_cvt_pk_bf16_f32 v200, v200, v204\n\t"
"v_mfma_f32_16x16x32_bf16 v[80:83], a[68:71], a[160:163], v[80:83]\n\t"
"ds_read_b64_tr_b16 a[200:201], v251 offset:0\n\t"
"v_cvt_pk_bf16_f32 v219, v219, v223\n\t"
"v_mfma_f32_16x16x32_bf16 v[84:87], a[64:67], a[148:151], v[84:87]\n\t"
"ds_read_b128 a[144:147], v224 offset:18496\n\t"
"v_cvt_pk_bf16_f32 v211, v211, v215\n\t"
"v_mfma_f32_16x16x32_bf16 v[84:87], a[68:71], a[164:167], v[84:87]\n\t"
"ds_read_b128 a[160:163], v224 offset:22592\n\t"
"v_cvt_pk_bf16_f32 v217, v217, v221\n\t"
"v_mfma_f32_16x16x32_bf16 v[68:71], a[56:59], a[148:151], v[68:71]\n\t"
"ds_read_b128 a[232:235], v230 offset:33280\n\t"
"v_cvt_pk_bf16_f32 v216, v216, v220\n\t"
"v_mfma_f32_16x16x32_bf16 v[68:71], a[60:63], a[164:167], v[68:71]\n\t"
"s_waitcnt lgkmcnt(7)\n\t"
"buffer_store_dwordx4 v[120:123], v233, s[16:19], s20 offen nt\n\t"
"v_mfma_f32_16x16x32_bf16 v[52:55], a[48:51], a[148:151], v[52:55]\n\t"
"ds_read_b128 a[148:151], v224 offset:19008\n\t"
"v_cvt_pk_bf16_f32 v210, v210, v214\n\t"
"v_mfma_f32_16x16x32_bf16 v[52:55], a[52:55], a[164:167], v[52:55]\n\t"
"ds_write_addtid_b32 v211 offset:14368\n\t"
"v_cvt_pk_bf16_f32 v195, v195, v199\n\t"
"v_mfma_f32_16x16x32_bf16 v[56:59], a[48:51], a[152:155], v[56:59]\n\t"
"ds_read_b128 a[164:167], v224 offset:23104\n\t"
"v_cvt_pk_bf16_f32 v192, v192, v196\n\t"
"v_mfma_f32_16x16x32_bf16 v[56:59], a[52:55], a[168:171], v[56:59]\n\t"
"buffer_load_dwordx4 a[128:131], v235, s[84:87], s48 offen offset:2048 sc1\n\t"
"v_mfma_f32_16x16x32_bf16 v[72:75], a[56:59], a[152:155], v[72:75]\n\t"
"ds_read_b128 a[236:239], v231 offset:33280\n\t"
"v_cvt_pk_bf16_f32 v218, v218, v222\n\t"
"v_mfma_f32_16x16x32_bf16 v[72:75], a[60:63], a[168:171], v[72:75]\n\t"
"buffer_load_dwordx4 a[120:123], v235, s[84:87], s48 offen offset:0 sc1\n\t"
"v_mfma_f32_16x16x32_bf16 v[88:91], a[64:67], a[152:155], v[88:91]\n\t"
"ds_write_addtid_b32 v218 offset:6400\n\t"
"v_cvt_pk_bf16_f32 v208, v208, v212\n\t"
"v_mfma_f32_16x16x32_bf16 v[88:91], a[68:71], a[168:171], v[88:91]\n\t"
"s_nop 0\n\t"
"ds_write_addtid_b32 v193 offset:8224\n\t"
"ds_write_addtid_b32 v195 offset:10272\n\t"
"v_mfma_f32_16x16x32_bf16 v[92:95], a[64:67], a[156:159], v[92:95]\n\t"
"ds_write_addtid_b32 v217 offset:12576\n\t"
"v_cvt_pk_bf16_f32 v201, v201, v205\n\t"
"v_mfma_f32_16x16x32_bf16 v[92:95], a[68:71], a[172:175], v[92:95]\n\t"
"s_nop 0\n\t"
"ds_write_addtid_b32 v192 offset:0\n\t"
"ds_write_addtid_b32 v208 offset:4096\n\t"
"v_mfma_f32_16x16x32_bf16 v[76:79], a[56:59], a[156:159], v[76:79]\n\t"
"s_nop 0\n\t"
"ds_write_addtid_b32 v200 offset:256\n\t"
"ds_read_b128 v[136:139], v231 offset:19968\n\t"
"v_mfma_f32_16x16x32_bf16 v[76:79], a[60:63], a[172:175], v[76:79]\n\t"
"buffer_load_dwordx4 a[124:127], v235, s[84:87], s48 offen offset:1024 sc1\n\t"
"v_mfma_f32_16x16x32_bf16 v[60:63], a[48:51], a[156:159], v[60:63]\n\t"
"ds_write_addtid_b32 v216 offset:4352\n\t"
"v_cvt_pk_bf16_f32 v202, v202, v206\n\t"
"v_mfma_f32_16x16x32_bf16 v[60:63], a[52:55], a[172:175], v[60:63]\n\t"
"s_waitcnt vmcnt(14) lgkmcnt(10)\n\t"
"ds_write_addtid_b32 v202 offset:2304\n\t"
"v_cvt_pk_bf16_f32 v209, v209, v213\n\t"
"v_mfma_f32_16x16x32_bf16 v[144:147], a[72:75], a[144:147], v[144:147]\n\t"
"s_nop 0\n\t"
"ds_write_addtid_b32 v203 offset:10528\n\t"
"ds_write_addtid_b32 v209 offset:12320\n\t"
"v_mfma_f32_16x16x32_bf16 v[144:147], a[76:79], a[160:163], v[144:147]\n\t"
"buffer_load_dwordx4 a[16:19], v252, s[84:87], s75 offen offset:0 sc1\n\t"
"v_mfma_f32_16x16x32_bf16 v[160:163], a[80:83], a[144:147], v[160:163]\n\t"
"s_nop 0\n\t"
"ds_write_addtid_b32 v210 offset:6144\n\t"
"ds_write_addtid_b32 v219 offset:14624\n\t"
"v_mfma_f32_16x16x32_bf16 v[160:163], a[84:87], a[160:163], v[160:163]\n\t"
"s_nop 0\n\t"
"ds_read_b128 a[156:159], v224 offset:20032\n\t"
"ds_write_addtid_b32 v201 offset:8480\n\t"
"v_mfma_f32_16x16x32_bf16 v[176:179], a[88:91], a[144:147], v[176:179]\n\t"
"s_nop 0\n\t"
"ds_read_b128 a[168:171], v224 offset:23616\n\t"
"ds_read_b128 a[172:175], v224 offset:24128\n\t"
"v_mfma_f32_16x16x32_bf16 v[176:179], a[92:95], a[160:163], v[176:179]\n\t"
"s_waitcnt lgkmcnt(10)\n\t"
"buffer_store_dwordx4 v[136:139], v234, s[16:19], s20 offen nt\n\t"
"v_mfma_f32_16x16x32_bf16 v[180:183], a[88:91], a[148:151], v[180:183]\n\t"
"s_nop 0\n\t"
"ds_read_b128 a[152:155], v224 offset:19520\n\t"
"ds_read_b64_tr_b16 a[194:195], v250 offset:512\n\t"
"v_mfma_f32_16x16x32_bf16 v[180:183], a[92:95], a[164:167], v[180:183]\n\t"
"s_nop 0\n\t"
"ds_read_b64_tr_b16 a[196:197], v250 offset:1024\n\t"
"ds_read_b64_tr_b16 a[202:203], v251 offset:512\n\t"
"v_mfma_f32_16x16x32_bf16 v[164:167], a[80:83], a[148:151], v[164:167]\n\t"
"ds_read_b64_tr_b16 a[204:205], v251 offset:1024\n\t"
"v_mfma_f32_16x16x32_bf16 v[164:167], a[84:87], a[164:167], v[164:167]\n\t"
"buffer_load_dwordx4 a[20:23], v252, s[84:87], s75 offen offset:1024 sc1\n\t"
"v_mfma_f32_16x16x32_bf16 v[148:151], a[72:75], a[148:151], v[148:151]\n\t"
"v_mfma_f32_16x16x32_bf16 v[148:151], a[76:79], a[164:167], v[148:151]\n\t"
"s_waitcnt lgkmcnt(0)\n\t"
"s_barrier\n\t"
"v_mfma_f32_16x16x32_bf16 v[152:155], a[72:75], a[152:155], v[152:155]\n\t"
"ds_read_b128 a[144:147], v224 offset:0\n\t"
"v_add_u32 v248, 101632, v227\n\t"
"v_mfma_f32_16x16x32_bf16 v[152:155], a[76:79], a[168:171], v[152:155]\n\t"
"ds_read_b128 a[160:163], v224 offset:4096\n\t"
"v_xor_b32 v249, 32, v248\n\t"
"v_mfma_f32_16x16x32_bf16 v[168:171], a[80:83], a[152:155], v[168:171]\n\t"
"ds_read_b128 a[148:151], v224 offset:512\n\t"
"v_xor_b32 v250, 64, v248\n\t"
"v_mfma_f32_16x16x32_bf16 v[168:171], a[84:87], a[168:171], v[168:171]\n\t"
"ds_read_b128 a[164:167], v224 offset:4608\n\t"
"v_xor_b32 v251, 96, v248\n\t"
"v_mfma_f32_16x16x32_bf16 v[184:187], a[88:91], a[152:155], v[184:187]\n\t"
"s_nop 0\n\t"
"ds_read_b64_tr_b16 a[182:183], v249 offset:41984\n\t"
"ds_read_b64_tr_b16 a[190:191], v251 offset:41984\n\t"
"v_mfma_f32_16x16x32_bf16 v[184:187], a[92:95], a[168:171], v[184:187]\n\t"
"buffer_load_dwordx4 a[12:15], v235, s[84:87], s75 offen offset:3072 sc1\n\t"
"v_mfma_f32_16x16x32_bf16 v[188:191], a[88:91], a[156:159], v[188:191]\n\t"
"s_nop 0\n\t"
"ds_read_b128 a[168:171], v224 offset:5120\n\t"
"ds_read_b64_tr_b16 a[198:199], v249 offset:512\n\t"
"v_mfma_f32_16x16x32_bf16 v[188:191], a[92:95], a[172:175], v[188:191]\n\t"
"buffer_load_dwordx4 a[8:11], v235, s[84:87], s75 offen offset:2048 sc1\n\t"
"v_mfma_f32_16x16x32_bf16 v[172:175], a[80:83], a[156:159], v[172:175]\n\t"
"ds_read_b64_tr_b16 a[206:207], v251 offset:512\n\t"
"v_mfma_f32_16x16x32_bf16 v[172:175], a[84:87], a[172:175], v[172:175]\n\t"
"ds_read_b128 a[152:155], v224 offset:1024\n\t"
"v_mfma_f32_16x16x32_bf16 v[156:159], a[72:75], a[156:159], v[156:159]\n\t"
"ds_read_b128 a[156:159], v224 offset:1536\n\t"
"v_mfma_f32_16x16x32_bf16 v[156:159], a[76:79], a[172:175], v[156:159]\n\t"
"buffer_store_dwordx4 v[108:111], v232, s[16:19], s25 offen nt\n\t"
"v_mfma_f32_16x16x32_bf16 v[192:195], a[220:223], a[176:179], 0\n\t"
"ds_read_b64_tr_b16 a[176:177], v248 offset:41472\n\t"
"v_mfma_f32_16x16x32_bf16 v[196:199], a[220:223], a[184:187], 0\n\t"
"s_nop 0\n\t"
"ds_read_b128 a[172:175], v224 offset:5632\n\t"
"ds_read_b64_tr_b16 a[184:185], v250 offset:41472\n\t"
"v_mfma_f32_16x16x32_bf16 v[200:203], a[220:223], a[178:181], 0\n\t"
"buffer_load_dwordx4 a[0:3], v235, s[84:87], s75 offen offset:0 sc1\n\t"
"v_mfma_f32_16x16x32_bf16 v[204:207], a[220:223], a[186:189], 0\n\t"
"ds_read_b64_tr_b16 a[178:179], v248 offset:41984\n\t"
"v_mfma_f32_16x16x32_bf16 v[208:211], a[220:223], a[192:195], 0\n\t"
"ds_read_b64_tr_b16 a[180:181], v249 offset:41472\n\t"
"v_mfma_f32_16x16x32_bf16 v[212:215], a[220:223], a[200:203], 0\n\t"
"buffer_load_dwordx4 a[4:7], v235, s[84:87], s75 offen offset:1024 sc1\n\t"
"v_mfma_f32_16x16x32_bf16 v[216:219], a[220:223], a[194:197], 0\n\t"
"ds_read_b64_tr_b16 a[186:187], v250 offset:41984\n\t"
"v_mfma_f32_16x16x32_bf16 v[220:223], a[220:223], a[202:205], 0\n\t"
"s_waitcnt vmcnt(15) lgkmcnt(7)\n\t"
"ds_read_b64_tr_b16 a[188:189], v251 offset:41472\n\t"
"v_cvt_pk_bf16_f32 v195, v195, v199\n\t"
"v_mfma_f32_16x16x32_bf16 v[48:51], a[96:99], a[144:147], v[48:51]\n\t"
"ds_read_b64_tr_b16 a[192:193], v248 offset:0\n\t"
"v_cvt_pk_bf16_f32 v193, v193, v197\n\t"
"v_mfma_f32_16x16x32_bf16 v[48:51], a[100:103], a[160:163], v[48:51]\n\t"
"ds_write_addtid_b32 v195 offset:26720\n\t"
"v_cvt_pk_bf16_f32 v203, v203, v207\n\t"
"v_mfma_f32_16x16x32_bf16 v[64:67], a[104:107], a[144:147], v[64:67]\n\t"
"ds_read_b64_tr_b16 a[200:201], v250 offset:0\n\t"
"v_cvt_pk_bf16_f32 v201, v201, v205\n\t"
"v_mfma_f32_16x16x32_bf16 v[64:67], a[108:111], a[160:163], v[64:67]\n\t"
"buffer_load_dwordx4 a[40:43], v252, s[84:87], s78 offen offset:0 sc1\n\t"
"v_mfma_f32_16x16x32_bf16 v[80:83], a[112:115], a[144:147], v[80:83]\n\t"
"ds_read_b128 a[144:147], v224 offset:2048\n\t"
"v_cvt_pk_bf16_f32 v200, v200, v204\n\t"
"v_mfma_f32_16x16x32_bf16 v[80:83], a[116:119], a[160:163], v[80:83]\n\t"
"ds_read_b128 a[160:163], v224 offset:6144\n\t"
"v_cvt_pk_bf16_f32 v208, v208, v212\n\t"
"v_mfma_f32_16x16x32_bf16 v[84:87], a[112:115], a[148:151], v[84:87]\n\t"
"ds_write_addtid_b32 v208 offset:20544\n\t"
"v_cvt_pk_bf16_f32 v192, v192, v196\n\t"
"v_mfma_f32_16x16x32_bf16 v[84:87], a[116:119], a[164:167], v[84:87]\n\t"
"buffer_store_dwordx4 a[232:235], v233, s[16:19], s25 offen nt\n\t"
"v_mfma_f32_16x16x32_bf16 v[68:71], a[104:107], a[148:151], v[68:71]\n\t"
"ds_read_b64_tr_b16 a[194:195], v248 offset:512\n\t"
"v_cvt_pk_bf16_f32 v218, v218, v222\n\t"
"v_mfma_f32_16x16x32_bf16 v[68:71], a[108:111], a[164:167], v[68:71]\n\t"
"ds_write_addtid_b32 v192 offset:16448\n\t"
"v_cvt_pk_bf16_f32 v209, v209, v213\n\t"
"v_mfma_f32_16x16x32_bf16 v[52:55], a[96:99], a[148:151], v[52:55]\n\t"
"ds_write_addtid_b32 v203 offset:26976\n\t"
"v_cvt_pk_bf16_f32 v217, v217, v221\n\t"
"v_mfma_f32_16x16x32_bf16 v[52:55], a[100:103], a[164:167], v[52:55]\n\t"
"buffer_load_dwordx4 a[44:47], v252, s[84:87], s78 offen offset:1024 sc1\n\t"
"v_mfma_f32_16x16x32_bf16 v[56:59], a[96:99], a[152:155], v[56:59]\n\t"
"ds_read_b128 a[148:151], v224 offset:2560\n\t"
"v_cvt_pk_bf16_f32 v219, v219, v223\n\t"
"v_mfma_f32_16x16x32_bf16 v[56:59], a[100:103], a[168:171], v[56:59]\n\t"
"ds_read_b128 a[164:167], v224 offset:6656\n\t"
"v_cvt_pk_bf16_f32 v202, v202, v206\n\t"
"v_mfma_f32_16x16x32_bf16 v[72:75], a[104:107], a[152:155], v[72:75]\n\t"
"s_nop 0\n\t"
"ds_write_addtid_b32 v193 offset:24672\n\t"
"ds_write_addtid_b32 v218 offset:22848\n\t"
"v_mfma_f32_16x16x32_bf16 v[72:75], a[108:111], a[168:171], v[72:75]\n\t"
"buffer_load_dwordx4 a[36:39], v235, s[84:87], s78 offen offset:3072 sc1\n\t"
"v_mfma_f32_16x16x32_bf16 v[88:91], a[112:115], a[152:155], v[88:91]\n\t"
"ds_write_addtid_b32 v200 offset:16704\n\t"
"v_cvt_pk_bf16_f32 v211, v211, v215\n\t"
"v_mfma_f32_16x16x32_bf16 v[88:91], a[116:119], a[168:171], v[88:91]\n\t"
"s_waitcnt lgkmcnt(8)\n\t"
"ds_write_addtid_b32 v219 offset:31072\n\t"
"v_cvt_pk_bf16_f32 v216, v216, v220\n\t"
"v_mfma_f32_16x16x32_bf16 v[92:95], a[112:115], a[156:159], v[92:95]\n\t"
"s_nop 0\n\t"
"ds_read_b128 a[152:155], v224 offset:3072\n\t"
"ds_write_addtid_b32 v202 offset:18752\n\t"
"v_mfma_f32_16x16x32_bf16 v[92:95], a[116:119], a[172:175], v[92:95]\n\t"
"buffer_load_dwordx4 a[32:35], v235, s[84:87], s78 offen offset:2048 sc1\n\t"
"v_mfma_f32_16x16x32_bf16 v[76:79], a[104:107], a[156:159], v[76:79]\n\t"
"s_nop 0\n\t"
"ds_write_addtid_b32 v211 offset:30816\n\t"
"ds_write_addtid_b32 v216 offset:20800\n\t"
"v_mfma_f32_16x16x32_bf16 v[76:79], a[108:111], a[172:175], v[76:79]\n\t"
"ds_write_addtid_b32 v201 offset:24928\n\t"
"v_cvt_pk_bf16_f32 v194, v194, v198\n\t"
"v_mfma_f32_16x16x32_bf16 v[60:63], a[96:99], a[156:159], v[60:63]\n\t"
"s_nop 0\n\t"
"ds_read_b128 a[156:159], v224 offset:3584\n\t"
"ds_write_addtid_b32 v194 offset:18496\n\t"
"v_mfma_f32_16x16x32_bf16 v[60:63], a[100:103], a[172:175], v[60:63]\n\t"
"s_waitcnt vmcnt(13) lgkmcnt(11)\n\t"
"ds_write_addtid_b32 v209 offset:28768\n\t"
"v_mfma_f32_16x16x32_bf16 v[144:147], a[120:123], a[144:147], v[144:147]\n\t"
"ds_write_addtid_b32 v217 offset:29024\n\t"
"v_cvt_pk_bf16_f32 v210, v210, v214\n\t"
"v_mfma_f32_16x16x32_bf16 v[144:147], a[124:127], a[160:163], v[144:147]\n\t"
"buffer_store_dwordx4 a[236:239], v234, s[16:19], s25 offen nt\n\t"
"v_mfma_f32_16x16x32_bf16 v[160:163], a[128:131], a[144:147], v[160:163]\n\t"
"ds_read_b128 a[168:171], v224 offset:7168\n\t"
"v_mfma_f32_16x16x32_bf16 v[160:163], a[132:135], a[160:163], v[160:163]\n\t"
"ds_write_addtid_b32 v210 offset:22592\n\t"
"v_mfma_f32_16x16x32_bf16 v[176:179], a[136:139], a[144:147], v[176:179]\n\t"
"ds_read_b128 a[172:175], v224 offset:7680\n\t"
"v_mfma_f32_16x16x32_bf16 v[176:179], a[140:143], a[160:163], v[176:179]\n\t"
"buffer_load_dwordx4 a[24:27], v235, s[84:87], s78 offen offset:0 sc1\n\t"
"v_mfma_f32_16x16x32_bf16 v[180:183], a[136:139], a[148:151], v[180:183]\n\t"
"ds_read_b64_tr_b16 a[196:197], v249 offset:0\n\t"
"v_mfma_f32_16x16x32_bf16 v[180:183], a[140:143], a[164:167], v[180:183]\n\t"
"ds_read_b64_tr_b16 a[202:203], v250 offset:512\n\t"
"v_mfma_f32_16x16x32_bf16 v[164:167], a[128:131], a[148:151], v[164:167]\n\t"
"ds_read_b64_tr_b16 a[204:205], v251 offset:0\n\t"
"v_mfma_f32_16x16x32_bf16 v[164:167], a[132:135], a[164:167], v[164:167]\n\t"
"buffer_load_dwordx4 a[28:31], v235, s[84:87], s78 offen offset:1024 sc1\n\t"
"v_mfma_f32_16x16x32_bf16 v[148:151], a[120:123], a[148:151], v[148:151]\n\t"
"v_mfma_f32_16x16x32_bf16 v[148:151], a[124:127], a[164:167], v[148:151]\n\t"
"s_waitcnt lgkmcnt(0)\n\t"
"s_barrier\n\t"
"v_mfma_f32_16x16x32_bf16 v[152:155], a[120:123], a[152:155], v[152:155]\n\t"
"ds_read_b128 a[160:163], v224 offset:20544\n\t"
"v_mfma_f32_16x16x32_bf16 v[152:155], a[124:127], a[168:171], v[152:155]\n\t"
"ds_read_b128 a[144:147], v224 offset:16448\n\t"
"v_mfma_f32_16x16x32_bf16 v[168:171], a[128:131], a[152:155], v[168:171]\n\t"
"ds_read_b128 a[148:151], v224 offset:16960\n\t"
"v_mfma_f32_16x16x32_bf16 v[168:171], a[132:135], a[168:171], v[168:171]\n\t"
"ds_read_b128 a[164:167], v224 offset:21056\n\t"
"v_mfma_f32_16x16x32_bf16 v[184:187], a[136:139], a[152:155], v[184:187]\n\t"
"ds_read_b128 a[152:155], v224 offset:17472\n\t"
"v_mfma_f32_16x16x32_bf16 v[184:187], a[140:143], a[168:171], v[184:187]\n\t"
"buffer_load_dwordx4 a[64:67], v252, s[84:87], s54 offen offset:0 sc1\n\t"
"v_mfma_f32_16x16x32_bf16 v[188:191], a[136:139], a[156:159], v[188:191]\n\t"
"ds_read_b128 a[168:171], v224 offset:21568\n\t"
"v_mfma_f32_16x16x32_bf16 v[188:191], a[140:143], a[172:175], v[188:191]\n\t"
"buffer_load_dwordx4 a[68:71], v252, s[84:87], s54 offen offset:1024 sc1\n\t"
"v_mfma_f32_16x16x32_bf16 v[172:175], a[128:131], a[156:159], v[172:175]\n\t"
"v_mfma_f32_16x16x32_bf16 v[172:175], a[132:135], a[172:175], v[172:175]\n\t"
"v_mfma_f32_16x16x32_bf16 v[156:159], a[120:123], a[156:159], v[156:159]\n\t"
"ds_read_b128 a[156:159], v224 offset:17984\n\t"
"v_mfma_f32_16x16x32_bf16 v[156:159], a[124:127], a[172:175], v[156:159]\n\t"
"buffer_load_dwordx4 a[60:63], v235, s[84:87], s54 offen offset:3072 sc1\n\t"
"v_mfma_f32_16x16x32_bf16 v[192:195], a[224:227], a[176:179], 0\n\t"
"s_xor_b32 s24, s24, 384\n\t"
"ds_read_b64_tr_b16 a[176:177], v248 offset:42624\n\t"
"ds_read_b64_tr_b16 a[178:179], v248 offset:43136\n\t"
"v_mfma_f32_16x16x32_bf16 v[196:199], a[224:227], a[180:183], 0\n\t"
"s_xor_b32 s20, s20, 384\n\t"
"ds_read_b64_tr_b16 a[180:181], v249 offset:42624\n\t"
"ds_read_b64_tr_b16 a[182:183], v249 offset:43136\n\t"
"v_mfma_f32_16x16x32_bf16 v[200:203], a[224:227], a[184:187], 0\n\t"
"s_xor_b32 s27, s27, 384\n\t"
"ds_read_b64_tr_b16 a[184:185], v250 offset:42624\n\t"
"ds_read_b64_tr_b16 a[186:187], v250 offset:43136\n\t"
"v_mfma_f32_16x16x32_bf16 v[204:207], a[224:227], a[188:191], 0\n\t"
"s_xor_b32 s25, s25, 384\n\t"
"ds_read_b64_tr_b16 a[188:189], v251 offset:42624\n\t"
"ds_read_b64_tr_b16 a[190:191], v251 offset:43136\n\t"
"v_mfma_f32_16x16x32_bf16 v[208:211], a[224:227], a[192:195], 0\n\t"
"buffer_load_dwordx4 a[56:59], v235, s[84:87], s54 offen offset:2048 sc1\n\t"
"v_mfma_f32_16x16x32_bf16 v[212:215], a[224:227], a[196:199], 0\n\t"
"s_nop 0\n\t"
"ds_read_b128 a[172:175], v224 offset:22080\n\t"
"ds_read_b64_tr_b16 a[192:193], v248 offset:1152\n\t"
"v_mfma_f32_16x16x32_bf16 v[216:219], a[224:227], a[200:203], 0\n\t"
"ds_read_b64_tr_b16 a[194:195], v248 offset:1664\n\t"
"v_mfma_f32_16x16x32_bf16 v[220:223], a[224:227], a[204:207], 0\n\t"
"s_waitcnt vmcnt(12) lgkmcnt(12)\n\t"
"ds_read_b64_tr_b16 a[196:197], v249 offset:1152\n\t"
"v_cvt_pk_bf16_f32 v195, v195, v199\n\t"
"v_mfma_f32_16x16x32_bf16 v[48:51], a[0:3], a[144:147], v[48:51]\n\t"
"ds_read_b64_tr_b16 a[198:199], v249 offset:1664\n\t"
"v_cvt_pk_bf16_f32 v192, v192, v196\n\t"
"v_mfma_f32_16x16x32_bf16 v[48:51], a[4:7], a[160:163], v[48:51]\n\t"
"ds_read_b64_tr_b16 a[200:201], v250 offset:1152\n\t"
"v_cvt_pk_bf16_f32 v194, v194, v198\n\t"
"v_mfma_f32_16x16x32_bf16 v[64:67], a[8:11], a[144:147], v[64:67]\n\t"
"ds_write_addtid_b32 v192 offset:0\n\t"
"v_cvt_pk_bf16_f32 v203, v203, v207\n\t"
"v_mfma_f32_16x16x32_bf16 v[64:67], a[12:15], a[160:163], v[64:67]\n\t"
"buffer_load_dwordx4 a[48:51], v235, s[84:87], s54 offen offset:0 sc1\n\t"
"v_mfma_f32_16x16x32_bf16 v[80:83], a[16:19], a[144:147], v[80:83]\n\t"
"ds_read_b128 a[144:147], v224 offset:18496\n\t"
"v_cvt_pk_bf16_f32 v201, v201, v205\n\t"
"v_mfma_f32_16x16x32_bf16 v[80:83], a[20:23], a[160:163], v[80:83]\n\t"
"ds_read_b64_tr_b16 a[202:203], v250 offset:1664\n\t"
"v_cvt_pk_bf16_f32 v211, v211, v215\n\t"
"v_mfma_f32_16x16x32_bf16 v[84:87], a[16:19], a[148:151], v[84:87]\n\t"
"ds_read_b128 a[160:163], v224 offset:22592\n\t"
"v_cvt_pk_bf16_f32 v210, v210, v214\n\t"
"v_mfma_f32_16x16x32_bf16 v[84:87], a[20:23], a[164:167], v[84:87]\n\t"
"ds_write_addtid_b32 v201 offset:4352\n\t"
"v_cvt_pk_bf16_f32 v208, v208, v212\n\t"
"v_mfma_f32_16x16x32_bf16 v[68:71], a[8:11], a[148:151], v[68:71]\n\t"
"ds_write_addtid_b32 v194 offset:8224\n\t"
"v_cvt_pk_bf16_f32 v218, v218, v222\n\t"
"v_mfma_f32_16x16x32_bf16 v[68:71], a[12:15], a[164:167], v[68:71]\n\t"
"buffer_load_dwordx4 a[52:55], v235, s[84:87], s54 offen offset:1024 sc1\n\t"
"v_mfma_f32_16x16x32_bf16 v[52:55], a[0:3], a[148:151], v[52:55]\n\t"
"ds_read_b128 a[148:151], v224 offset:19008\n\t"
"v_cvt_pk_bf16_f32 v219, v219, v223\n\t"
"v_mfma_f32_16x16x32_bf16 v[52:55], a[4:7], a[164:167], v[52:55]\n\t"
"ds_read_b128 a[164:167], v224 offset:23104\n\t"
"v_cvt_pk_bf16_f32 v216, v216, v220\n\t"
"v_mfma_f32_16x16x32_bf16 v[56:59], a[0:3], a[152:155], v[56:59]\n\t"
"ds_write_addtid_b32 v219 offset:14624\n\t"
"v_cvt_pk_bf16_f32 v202, v202, v206\n\t"
"v_mfma_f32_16x16x32_bf16 v[56:59], a[4:7], a[168:171], v[56:59]\n\t"
"buffer_load_dwordx4 a[88:91], v252, s[84:87], 0 offen offset:0 sc1\n\t"
"v_mfma_f32_16x16x32_bf16 v[72:75], a[8:11], a[152:155], v[72:75]\n\t"
"ds_write_addtid_b32 v218 offset:14368\n\t"
"v_cvt_pk_bf16_f32 v209, v209, v213\n\t"
"v_mfma_f32_16x16x32_bf16 v[72:75], a[12:15], a[168:171], v[72:75]\n\t"
"ds_write_addtid_b32 v195 offset:8480\n\t"
"v_cvt_pk_bf16_f32 v193, v193, v197\n\t"
"v_mfma_f32_16x16x32_bf16 v[88:91], a[16:19], a[152:155], v[88:91]\n\t"
"ds_write_addtid_b32 v202 offset:12320\n\t"
"v_cvt_pk_bf16_f32 v217, v217, v221\n\t"
"v_mfma_f32_16x16x32_bf16 v[88:91], a[20:23], a[168:171], v[88:91]\n\t"
"s_waitcnt lgkmcnt(7)\n\t"
"ds_write_addtid_b32 v203 offset:12576\n\t"
"ds_write_addtid_b32 v217 offset:6400\n\t"
"v_mfma_f32_16x16x32_bf16 v[92:95], a[16:19], a[156:159], v[92:95]\n\t"
"ds_write_addtid_b32 v210 offset:10272\n\t"
"v_cvt_pk_bf16_f32 v200, v200, v204\n\t"
"v_mfma_f32_16x16x32_bf16 v[92:95], a[20:23], a[172:175], v[92:95]\n\t"
"buffer_load_dwordx4 a[92:95], v252, s[84:87], 0 offen offset:1024 sc1\n\t"
"v_mfma_f32_16x16x32_bf16 v[76:79], a[8:11], a[156:159], v[76:79]\n\t"
"ds_write_addtid_b32 v200 offset:4096\n\t"
"v_mfma_f32_16x16x32_bf16 v[76:79], a[12:15], a[172:175], v[76:79]\n\t"
"ds_write_addtid_b32 v211 offset:10528\n\t"
"v_mfma_f32_16x16x32_bf16 v[60:63], a[0:3], a[156:159], v[60:63]\n\t"
"ds_write_addtid_b32 v208 offset:2048\n\t"
"v_mfma_f32_16x16x32_bf16 v[60:63], a[4:7], a[172:175], v[60:63]\n\t"
"s_waitcnt vmcnt(8) lgkmcnt(9)\n\t"
"ds_write_addtid_b32 v216 offset:6144\n\t"
"v_mfma_f32_16x16x32_bf16 v[144:147], a[24:27], a[144:147], v[144:147]\n\t"
"ds_read_b128 a[156:159], v224 offset:20032\n\t"
"v_mfma_f32_16x16x32_bf16 v[144:147], a[28:31], a[160:163], v[144:147]\n\t"
"buffer_load_dwordx4 a[84:87], v235, s[84:87], 0 offen offset:3072 sc1\n\t"
"v_mfma_f32_16x16x32_bf16 v[160:163], a[32:35], a[144:147], v[160:163]\n\t"
"ds_write_addtid_b32 v193 offset:256\n\t"
"v_mfma_f32_16x16x32_bf16 v[160:163], a[36:39], a[160:163], v[160:163]\n\t"
"ds_write_addtid_b32 v209 offset:2304\n\t"
"v_mfma_f32_16x16x32_bf16 v[176:179], a[40:43], a[144:147], v[176:179]\n\t"
"ds_read_b128 a[172:175], v224 offset:24128\n\t"
"v_mfma_f32_16x16x32_bf16 v[176:179], a[44:47], a[160:163], v[176:179]\n\t"
"ds_read_b128 a[152:155], v224 offset:19520\n\t"
"v_mfma_f32_16x16x32_bf16 v[180:183], a[40:43], a[148:151], v[180:183]\n\t"
"ds_read_b128 a[168:171], v224 offset:23616\n\t"
"v_mfma_f32_16x16x32_bf16 v[180:183], a[44:47], a[164:167], v[180:183]\n\t"
"ds_read_b64_tr_b16 a[204:205], v251 offset:1152\n\t"
"v_mfma_f32_16x16x32_bf16 v[164:167], a[32:35], a[148:151], v[164:167]\n\t"
"ds_read_b64_tr_b16 a[206:207], v251 offset:1664\n\t"
"v_mfma_f32_16x16x32_bf16 v[164:167], a[36:39], a[164:167], v[164:167]\n\t"
"buffer_load_dwordx4 a[80:83], v235, s[84:87], 0 offen offset:2048 sc1\n\t"
"v_mfma_f32_16x16x32_bf16 v[148:151], a[24:27], a[148:151], v[148:151]\n\t"
"v_mfma_f32_16x16x32_bf16 v[148:151], a[28:31], a[164:167], v[148:151]\n\t"
"buffer_load_dwordx4 a[72:75], v235, s[84:87], 0 offen offset:0 sc1\n\t"
"s_waitcnt lgkmcnt(0)\n\t"
"s_barrier\n\t"
"v_mfma_f32_16x16x32_bf16 v[152:155], a[24:27], a[152:155], v[152:155]\n\t"
"ds_read_b128 a[160:163], v224 offset:4096\n\t"
"v_mfma_f32_16x16x32_bf16 v[152:155], a[28:31], a[168:171], v[152:155]\n\t"
"ds_read_b128 a[144:147], v224 offset:0\n\t"
"v_mfma_f32_16x16x32_bf16 v[168:171], a[32:35], a[152:155], v[168:171]\n\t"
"ds_read_b128 a[148:151], v224 offset:512\n\t"
"v_mfma_f32_16x16x32_bf16 v[168:171], a[36:39], a[168:171], v[168:171]\n\t"
"ds_read_b128 a[164:167], v224 offset:4608\n\t"
"v_mfma_f32_16x16x32_bf16 v[184:187], a[40:43], a[152:155], v[184:187]\n\t"
"ds_read_b128 a[152:155], v224 offset:2048\n\t"
"v_mfma_f32_16x16x32_bf16 v[184:187], a[44:47], a[168:171], v[184:187]\n\t"
"buffer_load_dwordx4 a[76:79], v235, s[84:87], 0 offen offset:1024 sc1\n\t"
"v_mfma_f32_16x16x32_bf16 v[188:191], a[40:43], a[156:159], v[188:191]\n\t"
"ds_read_b128 a[168:171], v224 offset:6144\n\t"
"v_mfma_f32_16x16x32_bf16 v[188:191], a[44:47], a[172:175], v[188:191]\n\t"
"v_mfma_f32_16x16x32_bf16 v[172:175], a[32:35], a[156:159], v[172:175]\n\t"
"v_mfma_f32_16x16x32_bf16 v[172:175], a[36:39], a[172:175], v[172:175]\n\t"
"v_mfma_f32_16x16x32_bf16 v[156:159], a[24:27], a[156:159], v[156:159]\n\t"
"ds_read_b128 a[156:159], v224 offset:2560\n\t"
"v_mfma_f32_16x16x32_bf16 v[156:159], a[28:31], a[172:175], v[156:159]\n\t"
"buffer_load_dwordx4 a[112:115], v252, s[84:87], s66 offen offset:0 sc1\n\t"
"v_mfma_f32_16x16x32_bf16 v[192:195], a[224:227], a[176:179], 0\n\t"
"ds_read_b128 a[172:175], v224 offset:6656\n\t"
"v_mfma_f32_16x16x32_bf16 v[196:199], a[224:227], a[180:183], 0\n\t"
"buffer_load_dwordx4 a[116:119], v252, s[84:87], s66 offen offset:1024 sc1\n\t"
"v_mfma_f32_16x16x32_bf16 v[200:203], a[224:227], a[184:187], 0\n\t"
"v_mfma_f32_16x16x32_bf16 v[204:207], a[224:227], a[188:191], 0\n\t"
".p2align 3\n\t"
"buffer_load_dwordx4 a[108:111], v235, s[84:87], s66 offen offset:3072 sc1\n\t"
"v_pk_fma_f32 v[48:49], v[144:145], s[90:91], v[48:49]\n\t"
"v_pk_fma_f32 v[50:51], v[146:147], s[90:91], v[50:51]\n\t"
"v_pk_add_f32 v[144:145], v[48:49], v[144:145] neg_lo:[0,1] neg_hi:[0,1]\n\t"
"v_pk_add_f32 v[146:147], v[50:51], v[146:147] neg_lo:[0,1] neg_hi:[0,1]\n\t"
"v_pk_fma_f32 v[64:65], v[160:161], s[90:91], v[64:65]\n\t"
"v_pk_fma_f32 v[66:67], v[162:163], s[90:91], v[66:67]\n\t"
"v_pk_add_f32 v[160:161], v[64:65], v[160:161] neg_lo:[0,1] neg_hi:[0,1]\n\t"
"v_pk_add_f32 v[162:163], v[66:67], v[162:163] neg_lo:[0,1] neg_hi:[0,1]\n\t"
"v_pk_fma_f32 v[80:81], v[176:177], s[90:91], v[80:81]\n\t"
"v_pk_fma_f32 v[82:83], v[178:179], s[90:91], v[82:83]\n\t"
"v_pk_add_f32 v[176:177], v[80:81], v[176:177] neg_lo:[0,1] neg_hi:[0,1]\n\t"
"v_pk_add_f32 v[178:179], v[82:83], v[178:179] neg_lo:[0,1] neg_hi:[0,1]\n\t"
"v_pk_fma_f32 v[84:85], v[180:181], s[90:91], v[84:85]\n\t"
"v_pk_fma_f32 v[86:87], v[182:183], s[90:91], v[86:87]\n\t"
"v_pk_add_f32 v[180:181], v[84:85], v[180:181] neg_lo:[0,1] neg_hi:[0,1]\n\t"
"v_pk_add_f32 v[182:183], v[86:87], v[182:183] neg_lo:[0,1] neg_hi:[0,1]\n\t"
"v_pk_fma_f32 v[68:69], v[164:165], s[90:91], v[68:69]\n\t"
"v_pk_fma_f32 v[70:71], v[166:167], s[90:91], v[70:71]\n\t"
"v_pk_add_f32 v[164:165], v[68:69], v[164:165] neg_lo:[0,1] neg_hi:[0,1]\n\t"
"v_pk_add_f32 v[166:167], v[70:71], v[166:167] neg_lo:[0,1] neg_hi:[0,1]\n\t"
"v_pk_fma_f32 v[52:53], v[148:149], s[90:91], v[52:53]\n\t"
"v_pk_fma_f32 v[54:55], v[150:151], s[90:91], v[54:55]\n\t"
"v_pk_add_f32 v[148:149], v[52:53], v[148:149] neg_lo:[0,1] neg_hi:[0,1]\n\t"
"v_pk_add_f32 v[150:151], v[54:55], v[150:151] neg_lo:[0,1] neg_hi:[0,1]\n\t"
"v_pk_fma_f32 v[56:57], v[152:153], s[90:91], v[56:57]\n\t"
"v_pk_fma_f32 v[58:59], v[154:155], s[90:91], v[58:59]\n\t"
"v_pk_add_f32 v[152:153], v[56:57], v[152:153] neg_lo:[0,1] neg_hi:[0,1]\n\t"
"v_pk_add_f32 v[154:155], v[58:59], v[154:155] neg_lo:[0,1] neg_hi:[0,1]\n\t"
"v_pk_fma_f32 v[72:73], v[168:169], s[90:91], v[72:73]\n\t"
"v_pk_fma_f32 v[74:75], v[170:171], s[90:91], v[74:75]\n\t"
"v_pk_add_f32 v[168:169], v[72:73], v[168:169] neg_lo:[0,1] neg_hi:[0,1]\n\t"
"v_pk_add_f32 v[170:171], v[74:75], v[170:171] neg_lo:[0,1] neg_hi:[0,1]\n\t"
"v_pk_fma_f32 v[88:89], v[184:185], s[90:91], v[88:89]\n\t"
"v_pk_fma_f32 v[90:91], v[186:187], s[90:91], v[90:91]\n\t"
"v_pk_add_f32 v[184:185], v[88:89], v[184:185] neg_lo:[0,1] neg_hi:[0,1]\n\t"
"v_pk_add_f32 v[186:187], v[90:91], v[186:187] neg_lo:[0,1] neg_hi:[0,1]\n\t"
"v_pk_fma_f32 v[92:93], v[188:189], s[90:91], v[92:93]\n\t"
"v_pk_fma_f32 v[94:95], v[190:191], s[90:91], v[94:95]\n\t"
"v_pk_add_f32 v[188:189], v[92:93], v[188:189] neg_lo:[0,1] neg_hi:[0,1]\n\t"
"v_pk_add_f32 v[190:191], v[94:95], v[190:191] neg_lo:[0,1] neg_hi:[0,1]\n\t"
"v_pk_fma_f32 v[76:77], v[172:173], s[90:91], v[76:77]\n\t"
"v_pk_fma_f32 v[78:79], v[174:175], s[90:91], v[78:79]\n\t"
"v_pk_add_f32 v[172:173], v[76:77], v[172:173] neg_lo:[0,1] neg_hi:[0,1]\n\t"
"v_pk_add_f32 v[174:175], v[78:79], v[174:175] neg_lo:[0,1] neg_hi:[0,1]\n\t"
"v_pk_fma_f32 v[60:61], v[156:157], s[90:91], v[60:61]\n\t"
"v_pk_fma_f32 v[62:63], v[158:159], s[90:91], v[62:63]\n\t"
"v_pk_add_f32 v[156:157], v[60:61], v[156:157] neg_lo:[0,1] neg_hi:[0,1]\n\t"
"v_pk_add_f32 v[158:159], v[62:63], v[158:159] neg_lo:[0,1] neg_hi:[0,1]\n\t"
"v_mfma_f32_16x16x32_bf16 v[208:211], a[224:227], a[192:195], 0\n\t"
"buffer_load_dwordx4 a[104:107], v235, s[84:87], s66 offen offset:2048 sc1\n\t"
"v_mfma_f32_16x16x32_bf16 v[212:215], a[224:227], a[196:199], 0\n\t"
"v_mfma_f32_16x16x32_bf16 v[216:219], a[224:227], a[200:203], 0\n\t"
"buffer_load_dwordx4 a[96:99], v235, s[84:87], s66 offen offset:0 sc1\n\t"
"v_mfma_f32_16x16x32_bf16 v[220:223], a[224:227], a[204:207], 0\n\t"
"s_waitcnt vmcnt(11) lgkmcnt(2)\n\t"
"v_cvt_pk_bf16_f32 v193, v193, v197\n\t"
"v_cvt_pk_bf16_f32 v194, v194, v198\n\t"
"v_mfma_f32_16x16x32_bf16 v[48:51], a[48:51], a[144:147], v[48:51]\n\t"
"ds_write_addtid_b32 v193 offset:16704\n\t"
"v_cvt_pk_bf16_f32 v192, v192, v196\n\t"
"v_mfma_f32_16x16x32_bf16 v[48:51], a[52:55], a[160:163], v[48:51]\n\t"
"ds_write_addtid_b32 v194 offset:24672\n\t"
"v_cvt_pk_bf16_f32 v201, v201, v205\n\t"
"v_mfma_f32_16x16x32_bf16 v[64:67], a[56:59], a[144:147], v[64:67]\n\t"
"v_cvt_pk_bf16_f32 v195, v195, v199\n\t"
"v_cvt_pk_bf16_f32 v202, v202, v206\n\t"
"v_mfma_f32_16x16x32_bf16 v[64:67], a[60:63], a[160:163], v[64:67]\n\t"
"buffer_load_dwordx4 a[100:103], v235, s[84:87], s66 offen offset:1024 sc1\n\t"
"v_mfma_f32_16x16x32_bf16 v[80:83], a[64:67], a[144:147], v[80:83]\n\t"
"ds_write_addtid_b32 v201 offset:20800\n\t"
"v_cvt_pk_bf16_f32 v200, v200, v204\n\t"
"v_mfma_f32_16x16x32_bf16 v[80:83], a[68:71], a[160:163], v[80:83]\n\t"
"ds_read_b128 a[144:147], v224 offset:1024\n\t"
"v_cvt_pk_bf16_f32 v219, v219, v223\n\t"
"v_mfma_f32_16x16x32_bf16 v[84:87], a[64:67], a[148:151], v[84:87]\n\t"
"ds_read_b128 a[160:163], v224 offset:5120\n\t"
"v_cvt_pk_bf16_f32 v210, v210, v214\n\t"
"v_mfma_f32_16x16x32_bf16 v[84:87], a[68:71], a[164:167], v[84:87]\n\t"
"ds_write_addtid_b32 v200 offset:20544\n\t"
"v_cvt_pk_bf16_f32 v218, v218, v222\n\t"
"v_mfma_f32_16x16x32_bf16 v[68:71], a[56:59], a[148:151], v[68:71]\n\t"
"ds_write_addtid_b32 v195 offset:24928\n\t"
"v_cvt_pk_bf16_f32 v209, v209, v213\n\t"
"v_mfma_f32_16x16x32_bf16 v[68:71], a[60:63], a[164:167], v[68:71]\n\t"
"ds_write_addtid_b32 v219 offset:31072\n\t"
"buffer_load_dwordx4 a[136:139], v252, s[84:87], s51 offen offset:0 sc1\n\t"
"v_mfma_f32_16x16x32_bf16 v[52:55], a[48:51], a[148:151], v[52:55]\n\t"
"ds_read_b128 a[148:151], v224 offset:1536\n\t"
"v_cvt_pk_bf16_f32 v217, v217, v221\n\t"
"v_mfma_f32_16x16x32_bf16 v[52:55], a[52:55], a[164:167], v[52:55]\n\t"
"s_waitcnt vmcnt(7) lgkmcnt(7)\n\t"
"ds_write_addtid_b32 v210 offset:26720\n\t"
"v_cvt_pk_bf16_f32 v208, v208, v212\n\t"
"v_mfma_f32_16x16x32_bf16 v[48:51], a[72:75], a[152:155], v[48:51]\n\t"
"ds_read_b128 a[164:167], v224 offset:5632\n\t"
"v_cvt_pk_bf16_f32 v216, v216, v220\n\t"
"v_mfma_f32_16x16x32_bf16 v[48:51], a[76:79], a[168:171], v[48:51]\n\t"
"ds_write_addtid_b32 v192 offset:16448\n\t"
"buffer_load_dwordx4 a[140:143], v252, s[84:87], s51 offen offset:1024 sc1\n\t"
"v_mfma_f32_16x16x32_bf16 v[64:67], a[80:83], a[152:155], v[64:67]\n\t"
"ds_write_addtid_b32 v202 offset:28768\n\t"
"v_cvt_pk_bf16_f32 v203, v203, v207\n\t"
"v_mfma_f32_16x16x32_bf16 v[64:67], a[84:87], a[168:171], v[64:67]\n\t"
"s_nop 0\n\t"
"ds_write_addtid_b32 v203 offset:29024\n\t"
"ds_write_addtid_b32 v218 offset:30816\n\t"
"v_mfma_f32_16x16x32_bf16 v[80:83], a[88:91], a[152:155], v[80:83]\n\t"
"ds_read_b128 a[152:155], v224 offset:3072\n\t"
"v_cvt_pk_bf16_f32 v211, v211, v215\n\t"
"v_mfma_f32_16x16x32_bf16 v[80:83], a[92:95], a[168:171], v[80:83]\n\t"
"ds_write_addtid_b32 v208 offset:18496\n\t"
"v_cvt_pk_bf16_f32 v48, v48, v49\n\t"
"v_mfma_f32_16x16x32_bf16 v[84:87], a[88:91], a[156:159], v[84:87]\n\t"
"ds_read_b128 a[168:171], v224 offset:7168\n\t"
"v_cvt_pk_bf16_f32 v49, v50, v51\n\t"
"v_mfma_f32_16x16x32_bf16 v[84:87], a[92:95], a[172:175], v[84:87]\n\t"
"s_nop 0\n\t"
"ds_write_addtid_b32 v209 offset:18752\n\t"
"ds_write_addtid_b32 v217 offset:22848\n\t"
"v_mfma_f32_16x16x32_bf16 v[68:71], a[80:83], a[156:159], v[68:71]\n\t"
"ds_write_addtid_b32 v211 offset:26976\n\t"
"v_cvt_pk_bf16_f32 v80, v80, v81\n\t"
"v_mfma_f32_16x16x32_bf16 v[68:71], a[84:87], a[172:175], v[68:71]\n\t"
"ds_write_addtid_b32 v216 offset:22592\n\t"
"v_cvt_pk_bf16_f32 v64, v64, v65\n\t"
"v_mfma_f32_16x16x32_bf16 v[52:55], a[72:75], a[156:159], v[52:55]\n\t"
"ds_read_b128 a[156:159], v224 offset:3584\n\t"
"v_cvt_pk_bf16_f32 v84, v84, v85\n\t"
"v_mfma_f32_16x16x32_bf16 v[52:55], a[76:79], a[172:175], v[52:55]\n\t"
"s_waitcnt lgkmcnt(11)\n\t"
"ds_read_b128 a[172:175], v224 offset:7680\n\t"
"v_cvt_pk_bf16_f32 v85, v86, v87\n\t"
"v_mfma_f32_16x16x32_bf16 v[56:59], a[48:51], a[144:147], v[56:59]\n\t"
"v_cvt_pk_bf16_f32 v68, v68, v69\n\t"
"v_cvt_pk_bf16_f32 v81, v82, v83\n\t"
"v_mfma_f32_16x16x32_bf16 v[56:59], a[52:55], a[160:163], v[56:59]\n\t"
"ds_write2st64_b64 v247, v[80:81], v[84:85] offset0:0 offset1:13\n\t"
"v_mfma_f32_16x16x32_bf16 v[72:75], a[56:59], a[144:147], v[72:75]\n\t"
"v_cvt_pk_bf16_f32 v52, v52, v53\n\t"
"v_cvt_pk_bf16_f32 v53, v54, v55\n\t"
"v_mfma_f32_16x16x32_bf16 v[72:75], a[60:63], a[160:163], v[72:75]\n\t"
"buffer_load_dwordx4 a[132:135], v235, s[84:87], s51 offen offset:3072 sc1\n\t"
"v_mfma_f32_16x16x32_bf16 v[88:91], a[64:67], a[144:147], v[88:91]\n\t"
"ds_write2st64_b64 v228, v[48:49], v[52:53] offset0:0 offset1:13\n\t"
"v_mfma_f32_16x16x32_bf16 v[88:91], a[68:71], a[160:163], v[88:91]\n\t"
"v_cvt_pk_bf16_f32 v65, v66, v67\n\t"
"v_cvt_pk_bf16_f32 v69, v70, v71\n\t"
"v_mfma_f32_16x16x32_bf16 v[92:95], a[64:67], a[148:151], v[92:95]\n\t"
"buffer_load_dwordx4 a[128:131], v235, s[84:87], s51 offen offset:2048 sc1\n\t"
"v_mfma_f32_16x16x32_bf16 v[92:95], a[68:71], a[164:167], v[92:95]\n\t"
"ds_write2st64_b64 v246, v[64:65], v[68:69] offset0:0 offset1:13\n\t"
"v_mfma_f32_16x16x32_bf16 v[76:79], a[56:59], a[148:151], v[76:79]\n\t"
"buffer_load_dwordx4 a[120:123], v235, s[84:87], s51 offen offset:0 sc1\n\t"
"v_mfma_f32_16x16x32_bf16 v[76:79], a[60:63], a[164:167], v[76:79]\n\t"
"v_mfma_f32_16x16x32_bf16 v[60:63], a[48:51], a[148:151], v[60:63]\n\t"
"buffer_load_dwordx4 a[124:127], v235, s[84:87], s51 offen offset:1024 sc1\n\t"
"v_mfma_f32_16x16x32_bf16 v[60:63], a[52:55], a[164:167], v[60:63]\n\t"
"s_waitcnt lgkmcnt(0)\n\t"
"s_barrier\n\t"
"v_mfma_f32_16x16x32_bf16 v[56:59], a[72:75], a[152:155], v[56:59]\n\t"
"ds_read_b128 a[144:147], v224 offset:16448\n\t"
"v_mfma_f32_16x16x32_bf16 v[56:59], a[76:79], a[168:171], v[56:59]\n\t"
"ds_read_b128 a[160:163], v224 offset:20544\n\t"
"v_mfma_f32_16x16x32_bf16 v[72:75], a[80:83], a[152:155], v[72:75]\n\t"
"s_nop 0\n\t"
"ds_read_b128 a[148:151], v224 offset:16960\n\t"
"ds_read_b128 a[164:167], v224 offset:21056\n\t"
"v_mfma_f32_16x16x32_bf16 v[72:75], a[84:87], a[168:171], v[72:75]\n\t"
"ds_read_b128 a[0:3], v229 offset:0\n\t"
"v_mfma_f32_16x16x32_bf16 v[88:91], a[88:91], a[152:155], v[88:91]\n\t"
"ds_read_b128 a[4:7], v230 offset:0\n\t"
"v_cvt_pk_bf16_f32 v56, v56, v57\n\t"
"v_mfma_f32_16x16x32_bf16 v[88:91], a[92:95], a[168:171], v[88:91]\n\t"
"ds_read_b128 a[8:11], v231 offset:0\n\t"
"v_mfma_f32_16x16x32_bf16 v[92:95], a[88:91], a[156:159], v[92:95]\n\t"
"ds_read_b128 a[168:171], v224 offset:22592\n\t"
"v_mfma_f32_16x16x32_bf16 v[92:95], a[92:95], a[172:175], v[92:95]\n\t"
"ds_read_b128 a[152:155], v224 offset:18496\n\t"
"v_cvt_pk_bf16_f32 v57, v58, v59\n\t"
"v_mfma_f32_16x16x32_bf16 v[76:79], a[80:83], a[156:159], v[76:79]\n\t"
"v_cvt_pk_bf16_f32 v72, v72, v73\n\t"
"v_cvt_pk_bf16_f32 v88, v88, v89\n\t"
"v_mfma_f32_16x16x32_bf16 v[76:79], a[84:87], a[172:175], v[76:79]\n\t"
"s_waitcnt lgkmcnt(4)\n\t"
"buffer_store_dwordx4 a[0:3], v232, s[16:19], s24 offen nt\n\t"
"v_mfma_f32_16x16x32_bf16 v[60:63], a[72:75], a[156:159], v[60:63]\n\t"
"v_cvt_pk_bf16_f32 v89, v90, v91\n\t"
"v_cvt_pk_bf16_f32 v92, v92, v93\n\t"
"v_mfma_f32_16x16x32_bf16 v[60:63], a[76:79], a[172:175], v[60:63]\n\t"
"s_waitcnt vmcnt(7)\n\t"
"ds_read_b128 a[172:175], v224 offset:23104\n\t"
"v_cvt_pk_bf16_f32 v73, v74, v75\n\t"
"v_mfma_f32_16x16x32_bf16 v[144:147], a[96:99], a[144:147], v[144:147]\n\t"
"ds_read_b128 a[156:159], v224 offset:19008\n\t"
"v_cvt_pk_bf16_f32 v76, v76, v77\n\t"
"v_mfma_f32_16x16x32_bf16 v[144:147], a[100:103], a[160:163], v[144:147]\n\t"
"s_waitcnt lgkmcnt(5)\n\t"
"buffer_store_dwordx4 a[4:7], v233, s[16:19], s24 offen nt\n\t"
"v_mfma_f32_16x16x32_bf16 v[160:163], a[104:107], a[144:147], v[160:163]\n\t"
"v_cvt_pk_bf16_f32 v77, v78, v79\n\t"
"v_cvt_pk_bf16_f32 v93, v94, v95\n\t"
"v_mfma_f32_16x16x32_bf16 v[160:163], a[108:111], a[160:163], v[160:163]\n\t"
"ds_write2st64_b64 v246, v[72:73], v[76:77] offset0:26 offset1:39\n\t"
"v_mfma_f32_16x16x32_bf16 v[176:179], a[112:115], a[144:147], v[176:179]\n\t"
"v_mfma_f32_16x16x32_bf16 v[176:179], a[116:119], a[160:163], v[176:179]\n\t"
"s_waitcnt lgkmcnt(3)\n\t"
"buffer_store_dwordx4 a[8:11], v234, s[16:19], s24 offen nt\n\t"
"v_mfma_f32_16x16x32_bf16 v[180:183], a[112:115], a[148:151], v[180:183]\n\t"
"v_mfma_f32_16x16x32_bf16 v[180:183], a[116:119], a[164:167], v[180:183]\n\t"
"s_nop 0\n\t"
"ds_read_b128 a[144:147], v224 offset:17472\n\t"
"ds_read_b128 a[160:163], v224 offset:21568\n\t"
"v_mfma_f32_16x16x32_bf16 v[164:167], a[104:107], a[148:151], v[164:167]\n\t"
"ds_write2st64_b64 v247, v[88:89], v[92:93] offset0:26 offset1:39\n\t"
"v_mfma_f32_16x16x32_bf16 v[164:167], a[108:111], a[164:167], v[164:167]\n\t"
"v_cvt_pk_bf16_f32 v60, v60, v61\n\t"
"v_cvt_pk_bf16_f32 v61, v62, v63\n\t"
"v_mfma_f32_16x16x32_bf16 v[148:151], a[96:99], a[148:151], v[148:151]\n\t"
"v_mfma_f32_16x16x32_bf16 v[148:151], a[100:103], a[164:167], v[148:151]\n\t"
"s_waitcnt vmcnt(3) lgkmcnt(4)\n\t"
"ds_write2st64_b64 v228, v[56:57], v[60:61] offset0:26 offset1:39\n\t"
"v_mfma_f32_16x16x32_bf16 v[144:147], a[120:123], a[152:155], v[144:147]\n\t"
"v_mfma_f32_16x16x32_bf16 v[144:147], a[124:127], a[168:171], v[144:147]\n\t"
"s_nop 0\n\t"
"ds_read_b128 a[148:151], v224 offset:17984\n\t"
"ds_read_b128 a[164:167], v224 offset:22080\n\t"
"v_mfma_f32_16x16x32_bf16 v[160:163], a[128:131], a[152:155], v[160:163]\n\t"
"v_mfma_f32_16x16x32_bf16 v[160:163], a[132:135], a[168:171], v[160:163]\n\t"
"s_waitcnt lgkmcnt(2)\n\t"
"s_barrier\n\t"
"v_mfma_f32_16x16x32_bf16 v[176:179], a[136:139], a[152:155], v[176:179]\n\t"
"ds_read_b128 a[12:15], v229 offset:13312\n\t"
"v_mfma_f32_16x16x32_bf16 v[176:179], a[140:143], a[168:171], v[176:179]\n\t"
"ds_read_b128 a[168:171], v224 offset:23616\n\t"
"v_cvt_pk_bf16_f32 v144, v144, v145\n\t"
"v_mfma_f32_16x16x32_bf16 v[180:183], a[136:139], a[156:159], v[180:183]\n\t"
"ds_read_b128 a[16:19], v230 offset:13312\n\t"
"v_cvt_pk_bf16_f32 v160, v160, v161\n\t"
"v_mfma_f32_16x16x32_bf16 v[180:183], a[140:143], a[172:175], v[180:183]\n\t"
"ds_read_b128 a[152:155], v224 offset:19520\n\t"
"v_cvt_pk_bf16_f32 v145, v146, v147\n\t"
"v_mfma_f32_16x16x32_bf16 v[164:167], a[128:131], a[156:159], v[164:167]\n\t"
"v_cvt_pk_bf16_f32 v176, v176, v177\n\t"
"v_mfma_f32_16x16x32_bf16 v[164:167], a[132:135], a[172:175], v[164:167]\n\t"
"ds_read_b128 a[20:23], v231 offset:13312\n\t"
"v_cvt_pk_bf16_f32 v177, v178, v179\n\t"
"v_mfma_f32_16x16x32_bf16 v[148:151], a[120:123], a[156:159], v[148:151]\n\t"
"ds_read_b128 a[156:159], v224 offset:20032\n\t"
"v_cvt_pk_bf16_f32 v180, v180, v181\n\t"
"v_mfma_f32_16x16x32_bf16 v[148:151], a[124:127], a[172:175], v[148:151]\n\t"
"s_waitcnt lgkmcnt(5)\n\t"
"buffer_store_dwordx4 a[12:15], v232, s[16:19], s27 offen nt\n\t"
"v_mfma_f32_16x16x32_bf16 v[152:155], a[96:99], a[144:147], v[152:155]\n\t"
"v_cvt_pk_bf16_f32 v164, v164, v165\n\t"
"v_cvt_pk_bf16_f32 v181, v182, v183\n\t"
"v_mfma_f32_16x16x32_bf16 v[152:155], a[100:103], a[160:163], v[152:155]\n\t"
"ds_read_b128 a[172:175], v224 offset:24128\n\t"
"ds_write2st64_b64 v247, v[176:177], v[180:181] offset0:52 offset1:65\n\t"
"v_mfma_f32_16x16x32_bf16 v[168:171], a[104:107], a[144:147], v[168:171]\n\t"
"v_cvt_pk_bf16_f32 v148, v148, v149\n\t"
"v_cvt_pk_bf16_f32 v161, v162, v163\n\t"
"v_mfma_f32_16x16x32_bf16 v[168:171], a[108:111], a[160:163], v[168:171]\n\t"
"v_cvt_pk_bf16_f32 v149, v150, v151\n\t"
"v_cvt_pk_bf16_f32 v165, v166, v167\n\t"
"v_mfma_f32_16x16x32_bf16 v[184:187], a[112:115], a[144:147], v[184:187]\n\t"
"ds_write2st64_b64 v246, v[160:161], v[164:165] offset0:52 offset1:65\n\t"
"v_mfma_f32_16x16x32_bf16 v[184:187], a[116:119], a[160:163], v[184:187]\n\t"
"s_waitcnt lgkmcnt(5)\n\t"
"buffer_store_dwordx4 a[16:19], v233, s[16:19], s27 offen nt\n\t"
"v_mfma_f32_16x16x32_bf16 v[188:191], a[112:115], a[148:151], v[188:191]\n\t"
"v_mfma_f32_16x16x32_bf16 v[188:191], a[116:119], a[164:167], v[188:191]\n\t"
"ds_write2st64_b64 v228, v[144:145], v[148:149] offset0:52 offset1:65\n\t"
"v_mfma_f32_16x16x32_bf16 v[172:175], a[104:107], a[148:151], v[172:175]\n\t"
"v_mfma_f32_16x16x32_bf16 v[172:175], a[108:111], a[164:167], v[172:175]\n\t"
"s_waitcnt lgkmcnt(4)\n\t"
"buffer_store_dwordx4 a[20:23], v234, s[16:19], s27 offen nt\n\t"
"v_mfma_f32_16x16x32_bf16 v[156:159], a[96:99], a[148:151], v[156:159]\n\t"
"v_mfma_f32_16x16x32_bf16 v[156:159], a[100:103], a[164:167], v[156:159]\n\t"
"s_waitcnt lgkmcnt(0)\n\t"
"s_barrier\n\t"
"v_mfma_f32_16x16x32_bf16 v[152:155], a[120:123], a[152:155], v[152:155]\n\t"
"ds_read_b128 a[32:35], v231 offset:26624\n\t"
"v_mfma_f32_16x16x32_bf16 v[152:155], a[124:127], a[168:171], v[152:155]\n\t"
"ds_read_b128 a[28:31], v230 offset:26624\n\t"
"v_mfma_f32_16x16x32_bf16 v[168:171], a[128:131], a[152:155], v[168:171]\n\t"
"ds_read_b128 a[24:27], v229 offset:26624\n\t"
"v_mfma_f32_16x16x32_bf16 v[168:171], a[132:135], a[168:171], v[168:171]\n\t"
"v_mfma_f32_16x16x32_bf16 v[184:187], a[136:139], a[152:155], v[184:187]\n\t"
"v_mfma_f32_16x16x32_bf16 v[184:187], a[140:143], a[168:171], v[184:187]\n\t"
"s_nop 1\n\t"
"v_cvt_pk_bf16_f32 v152, v152, v153\n\t"
"v_cvt_pk_bf16_f32 v153, v154, v155\n\t"
"v_mfma_f32_16x16x32_bf16 v[188:191], a[136:139], a[156:159], v[188:191]\n\t"
"s_waitcnt lgkmcnt(2)\n\t"
"buffer_store_dwordx4 a[32:35], v234, s[16:19], s20 offen nt\n\t"
"v_mfma_f32_16x16x32_bf16 v[188:191], a[140:143], a[172:175], v[188:191]\n\t"
"v_cvt_pk_bf16_f32 v168, v168, v169\n\t"
"v_cvt_pk_bf16_f32 v169, v170, v171\n\t"
"v_mfma_f32_16x16x32_bf16 v[172:175], a[128:131], a[156:159], v[172:175]\n\t"
"v_cvt_pk_bf16_f32 v184, v184, v185\n\t"
"v_cvt_pk_bf16_f32 v185, v186, v187\n\t"
"v_mfma_f32_16x16x32_bf16 v[172:175], a[132:135], a[172:175], v[172:175]\n\t"
"s_waitcnt lgkmcnt(0)\n\t"
"buffer_store_dwordx4 a[24:27], v232, s[16:19], s20 offen nt\n\t"
"v_mfma_f32_16x16x32_bf16 v[156:159], a[120:123], a[156:159], v[156:159]\n\t"
"v_cvt_pk_bf16_f32 v188, v188, v189\n\t"
"v_cvt_pk_bf16_f32 v189, v190, v191\n\t"
"v_mfma_f32_16x16x32_bf16 v[156:159], a[124:127], a[172:175], v[156:159]\n\t"
"buffer_store_dwordx4 a[28:31], v233, s[16:19], s20 offen nt\n\t"
"s_nop 6\n\t"
"v_cvt_pk_bf16_f32 v156, v156, v157\n\t"
"v_cvt_pk_bf16_f32 v157, v158, v159\n\t"
"ds_write2st64_b64 v228, v[152:153], v[156:157] offset0:78 offset1:91\n\t"
"v_cvt_pk_bf16_f32 v172, v172, v173\n\t"
"v_cvt_pk_bf16_f32 v173, v174, v175\n\t"
"ds_write2st64_b64 v246, v[168:169], v[172:173] offset0:78 offset1:91\n\t"
"ds_write2st64_b64 v247, v[184:185], v[188:189] offset0:78 offset1:91\n\t"
"s_waitcnt lgkmcnt(0)\n\t"
"s_barrier\n\t"
"ds_read_b128 a[36:39], v229 offset:39936\n\t"
"ds_read_b128 a[40:43], v230 offset:39936\n\t"
"ds_read_b128 a[44:47], v231 offset:39936\n\t"
"s_waitcnt lgkmcnt(2)\n\t"
"buffer_store_dwordx4 a[36:39], v232, s[16:19], s25 offen nt\n\t"
"s_waitcnt lgkmcnt(1)\n\t"
"buffer_store_dwordx4 a[40:43], v233, s[16:19], s25 offen nt\n\t"
"s_waitcnt lgkmcnt(0)\n\t"
"buffer_store_dwordx4 a[44:47], v234, s[16:19], s25 offen nt\n\t"
"s_waitcnt vmcnt(0)\n\t"
"Lwino_end:\n\t"
  : "+{v205}"(tid)
  : "s"(__builtin_amdgcn_kernarg_segment_ptr()), "s"(blockIdx.x), "s"((unsigned)(size_t)(__attribute__((address_space(3))) char*)smem)
  : "v0", "v1", "v2", "v3", "v4", "v5", "v6", "v7", "v8", "v9", "v10", "v11", "v12", "v13", "v14", "v15", "v16", "v17", "v18", "v19", "v20", "v21", "v22", "v23", "v24", "v25", "v26", "v27", "v28", "v29", "v30", "v31", "v32", "v33", "v34", "v35", "v36", "v37", "v38", "v39", "v40", "v41", "v42", "v43", "v44", "v45", "v46", "v47", "v48", "v49", "v50", "v51", "v52", "v53", "v54", "v55", "v56", "v57", "v58", "v59", "v60", "v61", "v62", "v63", "v64", "v65", "v66", "v67", "v68", "v69", "v70", "v71", "v72", "v73", "v74", "v75", "v76", "v77", "v78", "v79", "v80", "v81", "v82", "v83", "v84", "v85", "v86", "v87", "v88", "v89", "v90", "v91", "v92", "v93", "v94", "v95", "v96", "v97", "v98", "v99", "v100", "v101", "v102", "v103", "v104", "v105", "v106", "v107", "v108", "v109", "v110", "v111", "v112", "v113", "v114", "v115", "v116", "v117", "v118", "v119", "v120", "v121", "v122", "v123", "v124", "v125", "v126", "v127", "v128", "v129", "v130", "v131", "v132", "v133", "v134", "v135", "v136", "v137", "v138", "v139", "v140", "v141", "v142", "v143", "v144", "v145", "v146", "v147", "v148", "v149", "v150", "v151", "v152", "v153", "v154", "v155", "v156", "v157", "v158", "v159", "v160", "v161", "v162", "v163", "v164", "v165", "v166", "v167", "v168", "v169", "v170", "v171", "v172", "v173", "v174", "v175", "v176", "v177", "v178", "v179", "v180", "v181", "v182", "v183", "v184", "v185", "v186", "v187", "v188", "v189", "v190", "v191", "v192", "v193", "v194", "v195", "v196", "v197", "v198", "v199", "v200", "v201", "v202", "v203", "v204", "v206", "v207", "v208", "v209", "v210", "v211", "v212", "v213", "v214", "v215", "v216", "v217", "v218", "v219", "v220", "v221", "v222", "v223", "v224", "v225", "v226", "v227", "v228", "v229", "v230", "v231", "v232", "v233", "v234", "v235", "v236", "v237", "v238", "v239", "v240", "v241", "v242", "v243", "v244", "v245", "v246", "v247", "v248", "v249", "v250", "v251", "v252", "v253", "v254", "v255", "a0", "a1", "a2", "a3", "a4", "a5", "a6", "a7", "a8", "a9", "a10", "a11", "a12", "a13", "a14", "a15", "a16", "a17", "a18", "a19", "a20", "a21", "a22", "a23", "a24", "a25", "a26", "a27", "a28", "a29", "a30", "a31", "a32", "a33", "a34", "a35", "a36", "a37", "a38", "a39", "a40", "a41", "a42", "a43", "a44", "a45", "a46", "a47", "a48", "a49", "a50", "a51", "a52", "a53", "a54", "a55", "a56", "a57", "a58", "a59", "a60", "a61", "a62", "a63", "a64", "a65", "a66", "a67", "a68", "a69", "a70", "a71", "a72", "a73", "a74", "a75", "a76", "a77", "a78", "a79", "a80", "a81", "a82", "a83", "a84", "a85", "a86", "a87", "a88", "a89", "a90", "a91", "a92", "a93", "a94", "a95", "a96", "a97", "a98", "a99", "a100", "a101", "a102", "a103", "a104", "a105", "a106", "a107", "a108", "a109", "a110", "a111", "a112", "a113", "a114", "a115", "a116", "a117", "a118", "a119", "a120", "a121", "a122", "a123", "a124", "a125", "a126", "a127", "a128", "a129", "a130", "a131", "a132", "a133", "a134", "a135", "a136", "a137", "a138", "a139", "a140", "a141", "a142", "a143", "a144", "a145", "a146", "a147", "a148", "a149", "a150", "a151", "a152", "a153", "a154", "a155", "a156", "a157", "a158", "a159", "a160", "a161", "a162", "a163", "a164", "a165", "a166", "a167", "a168", "a169", "a170", "a171", "a172", "a173", "a174", "a175", "a176", "a177", "a178", "a179", "a180", "a181", "a182", "a183", "a184", "a185", "a186", "a187", "a188", "a189", "a190", "a191", "a192", "a193", "a194", "a195", "a196", "a197", "a198", "a199", "a200", "a201", "a202", "a203", "a204", "a205", "a206", "a207", "a208", "a209", "a210", "a211", "a212", "a213", "a214", "a215", "a216", "a217", "a218", "a219", "a220", "a221", "a222", "a223", "a224", "a225", "a226", "a227", "a228", "a229", "a230", "a231", "a232", "a233", "a234", "a235", "a236", "a237", "a238", "a239", "a240", "a241", "a242", "a243", "a244", "a245", "a246", "a247", "a248", "a249", "a250", "a251", "a252", "a253", "a254", "a255", "s6", "s7", "s8", "s9", "s10", "s11", "s12", "s13", "s14", "s15", "s16", "s17", "s18", "s19", "s20", "s21", "s22", "s23", "s24", "s25", "s26", "s27", "s28", "s29", "s30", "s31", "s32", "s33", "s34", "s35", "s36", "s37", "s38", "s39", "s40", "s41", "s42", "s43", "s44", "s45", "s46", "s47", "s48", "s49", "s50", "s51", "s52", "s53", "s54", "s55", "s56", "s57", "s58", "s59", "s60", "s61", "s62", "s63", "s64", "s65", "s66", "s67", "s68", "s69", "s70", "s71", "s72", "s73", "s74", "s75", "s76", "s77", "s78", "s79", "s80", "s81", "s82", "s83", "s84", "s85", "s86", "s87", "s88", "s89", "s90", "s91", "scc", "vcc", "memory");
}

extern "C" void kernel_launch(void* const* d_in, const int* in_sizes, int n_in, void* d_out, int out_size, void* d_ws,
                              size_t ws_size, hipStream_t stream) {
  wino_main_kernel<<<256, 256, 0, stream>>>((const char*)d_in[0], (char*)d_ws, (char*)d_out, (const char*)d_in[1]);
}
```

```cpp
#include <hip/hip_runtime.h>
#include <hip/hip_bf16.h>
#include <stdint.h>

#define U_BYTES 1179648

__global__ __launch_bounds__(256, 1) void wino_main_kernel(const char* __restrict__ x, char* __restrict__ Uq, char* __restrict__ out, const char* __restrict__ wt) {
  __shared__ __attribute__((aligned(1024))) char smem[163840];
  unsigned tid = threadIdx.x;
  asm volatile(
"s_load_dwordx8 s[76:83], %1, 0x0\n\t"
"s_mov_b32 s66, %2\n\t"
"s_getpc_b64 s[6:7]\n\t"
"Lwino_pc:\n\t"
"s_bfe_u32 s42, %2, 0x50003\n\t"
"s_lshl_b32 s42, s42, 12\n\t"
"v_lshl_add_u32 v203, v205, 4, s42\n\t"
"v_min_u32 v203, (Lwino_end-Lwino_pc-16)&-16, v203\n\t"
"global_load_dwordx4 v[196:199], v203, s[6:7]\n\t"
"v_and_b32 v204, 63, v205\n\t"
"v_readfirstlane_b32 s73, v205\n\t"
"v_lshrrev_b32 v120, 3, v204\n\t"
"v_and_b32 v121, 7, v204\n\t"
"s_lshr_b32 s73, s73, 6\n\t"
"s_lshr_b32 s56, s66, 5\n\t"
"s_mul_i32 s56, s56, 12288\n\t"
"s_bfe_u32 s61, s66, 0x20001\n\t"
"s_mul_i32 s61, s61, 1536\n\t"
"s_add_u32 s56, s56, s61\n\t"
"s_bitcmp1_b32 s66, 0\n\t"
"s_cselect_b32 s61, 48, 0\n\t"
"s_add_u32 s56, s56, s61\n\t"
"s_bfe_u32 s62, s66, 0x20003\n\t"
"s_mul_i32 s61, s62, 96\n\t"
"s_add_u32 s56, s56, s61\n\t"
"s_cmp_lt_u32 s66, 192\n\t"
"s_cselect_b32 s56, s56, 2.0\n\t"
"v_bfe_u32 v42, v204, 3, 2\n\t"
"v_lshlrev_b32 v43, 2, v204\n\t"
"v_and_or_b32 v42, v43, 16, v42\n\t"
"v_mul_u32_u24 v42, 0x180, v42\n\t"
"v_lshrrev_b32 v44, 5, v204\n\t"
"v_mul_u32_u24 v43, 0x12000, v44\n\t"
"v_and_b32 v45, 3, v204\n\t"
"v_min_u32 v41, 2, v45\n\t"
"v_lshl_add_u32 v41, v41, 4, v42\n\t"
"v_add_u32 v41, v41, v43\n\t"
"v_lshlrev_b32 v43, 4, v204\n\t"
"v_bfe_u32 v45, v204, 1, 2\n\t"
"v_and_or_b32 v45, v43, 16, v45\n\t"
"v_mul_u32_u24 v45, 0x180, v45\n\t"
"v_lshl_add_u32 v44, v44, 3, v120\n\t"
"v_lshl_add_u32 v45, v44, 1, v45\n\t"
"v_mov_b32 v40, 0\n\t"
"s_mul_i32 s61, s73, 147456\n\t"
"s_add_u32 s61, s61, s56\n\t"
"s_lshl3_add_u32 s62, s73, s56\n\t"
"s_add_u32 s62, s62, 589824\n\t"
"s_lshl_b32 s64, s73, 10\n\t"
"s_and_b32 s60, s66, 7\n\t"
"s_lshr_b32 s65, s66, 3\n\t"
"s_mul_i32 s24, s60, 12582912\n\t"
"s_mul_i32 s63, s65, 6144\n\t"
"s_add_u32 s24, s24, s63\n\t"
"s_add_u32 s22, s24, 196224\n\t"
"s_mul_i32 s63, s73, 786432\n\t"
"s_mul_i32 s26, s73, 4608\n\t"
"s_mov_b32 s48, 189696\n\t"
"s_cmp_eq_u32 s65, 0\n\t"
"s_cselect_b64 s[40:41], 0xff, 0\n\t"
"s_cselect_b64 s[46:47], -1, 0\n\t"
"v_mul_u32_u24 v122, 0x300, v120\n\t"
"v_xor_b32 v123, 4, v121\n\t"
"v_add_u32 v122, s63, v122\n\t"
"v_and_b32 v124, 2, v120\n\t"
"v_xor_b32 v124, v124, v123\n\t"
"v_lshl_add_u32 v128, v124, 4, v122\n\t"
"v_cndmask_b32 v241, v128, 2.0, s[40:41]\n\t"
"v_add_u32 v122, 194816, v122\n\t"
"v_add_u32 v124, -1, v120\n\t"
"v_ashrrev_i32 v125, 31, v124\n\t"
"v_sub_u32 v124, v124, v125\n\t"
"v_and_b32 v124, 2, v124\n\t"
"v_xor_b32 v124, v124, v123\n\t"
"v_mad_i32_i24 v125, v125, s48, v122\n\t"
"v_lshl_add_u32 v129, v124, 4, v125\n\t"
"s_lshl_b64 s[40:41], s[40:41], 8\n\t"
"v_cndmask_b32 v242, v129, 2.0, s[40:41]\n\t"
"v_add_u32 v122, 194816, v122\n\t"
"v_add_u32 v124, -2, v120\n\t"
"v_ashrrev_i32 v125, 31, v124\n\t"
"v_sub_u32 v124, v124, v125\n\t"
"v_and_b32 v124, 2, v124\n\t"
"v_and_or_b32 v124, v125, 4, v124\n\t"
"v_xor_b32 v124, v124, v121\n\t"
"v_mad_i32_i24 v125, v125, s48, v122\n\t"
"v_lshl_add_u32 v130, v124, 4, v125\n\t"
"s_lshl_b64 s[40:41], s[40:41], 8\n\t"
"v_cndmask_b32 v243, v130, 2.0, s[40:41]\n\t"
"v_add_u32 v122, 194816, v122\n\t"
"v_add_u32 v124, -3, v120\n\t"
"v_ashrrev_i32 v125, 31, v124\n\t"
"v_sub_u32 v124, v124, v125\n\t"
"v_and_b32 v124, 2, v124\n\t"
"v_xor_b32 v124, v124, v121\n\t"
"v_mad_i32_i24 v125, v125, s48, v122\n\t"
"v_lshl_add_u32 v131, v124, 4, v125\n\t"
"s_lshl_b64 s[40:41], s[40:41], 8\n\t"
"v_cndmask_b32 v244, v131, 2.0, s[40:41]\n\t"
"v_add_u32 v124, 5, v120\n\t"
"v_and_b32 v124, 2, v124\n\t"
"v_xor_b32 v124, v124, v121\n\t"
"v_add_u32 v122, 2560, v122\n\t"
"v_lshl_add_u32 v245, v124, 4, v122\n\t"
"s_mov_b32 s37, -1\n\t"
"s_mov_b32 s36, 0\n\t"
"s_mul_i32 s67, s73, 48\n\t"
"s_add_u32 s67, s67, 118784\n\t"
"s_mov_b32 s10, 0x200000\n\t"
"s_mov_b32 s11, 0x20000\n\t"
"v_min_u32 v189, 47, v204\n\t"
"v_add_u32 v189, s67, v189\n\t"
"v_mul_u32_u24 v189, 16, v189\n\t"
"s_mov_b32 s54, 663552\n\t"
"s_mov_b32 s55, 0x20000\n\t"
"s_mov_b32 s14, 100663296\n\t"
"s_mov_b32 s15, 0x20000\n\t"
"s_add_u32 m0, s64, 0\n\t"
"s_waitcnt lgkmcnt(0)\n\t"
"s_mov_b32 s52, s82\n\t"
"s_and_b32 s53, s83, 0xffff\n\t"
"buffer_load_dwordx4 v41, s[52:55], s61 offen lds\n\t"
"buffer_load_short_d16_hi v40, v45, s[52:55], s62 offen\n\t"
"s_mov_b32 s8, s78\n\t"
"s_and_b32 s9, s79, 0xffff\n\t"
"buffer_load_dword v188, v189, s[8:11], 0 offen sc1\n\t"
"s_mov_b32 s12, s76\n\t"
"s_and_b32 s13, s77, 0xffff\n\t"
"s_add_u32 m0, s26, 103936\n\t"
"s_nop 0\n\t"
"buffer_load_dwordx4 v241, s[12:15], s22 offen offset:0 nt lds\n\t"
"buffer_load_dwordx4 v242, s[12:15], s22 offen offset:1024 nt lds\n\t"
"buffer_load_dwordx4 v243, s[12:15], s22 offen offset:2048 nt lds\n\t"
"buffer_load_dwordx4 v244, s[12:15], s22 offen offset:3072 nt lds\n\t"
"s_mov_b64 exec, s[36:37]\n\t"
"buffer_load_dwordx4 v245, s[12:15], s22 offen offset:3584 nt lds\n\t"
"s_mov_b64 exec, -1\n\t"
"s_mul_i32 s49, s73, 36\n\t"
"s_mul_i32 s43, s73, 576\n\t"
"s_sub_u32 s42, s22, 203520\n\t"
"s_mov_b32 s44, -1\n\t"
"s_mov_b32 s45, 15\n\t"
"v_min_u32 v124, 35, v204\n\t"
"v_add_u32 v124, s49, v124\n\t"
"v_lshrrev_b32 v119, 3, v124\n\t"
"v_and_b32 v124, 7, v124\n\t"
"v_mul_u32_u24 v127, 0x300, v119\n\t"
"v_cmp_gt_u32 vcc, 9, v119\n\t"
"v_add_u32 v119, -9, v119\n\t"
"v_ashrrev_i32 v125, 31, v119\n\t"
"v_add_u32 v127, s42, v127\n\t"
"v_mad_i32_i24 v127, v125, s48, v127\n\t"
"v_and_b32 v125, 9, v125\n\t"
"v_add_u32 v119, v119, v125\n\t"
"v_and_b32 v125, 2, v119\n\t"
"v_xor_b32 v124, v124, v125\n\t"
"v_lshl_add_u32 v133, v124, 4, v127\n\t"
"s_cmp_eq_u32 s60, 0\n\t"
"s_cselect_b64 s[50:51], vcc, 0\n\t"
"v_cmp_eq_u32 vcc, 0, v119\n\t"
"v_cndmask_b32 v246, v133, 2.0, s[50:51]\n\t"
"s_and_b64 vcc, vcc, s[46:47]\n\t"
"v_cndmask_b32 v246, v246, 2.0, vcc\n\t"
"s_add_u32 m0, s43, 101632\n\t"
"s_mov_b64 exec, s[44:45]\n\t"
"buffer_load_dwordx4 v246, s[12:15], 0 offen offset:0 nt lds\n\t"
"s_mov_b64 exec, -1\n\t"
"s_mul_i32 s70, s66, 16\n\t"
"s_add_u32 s70, s70, 1900544\n\t"
"s_lshl_b32 s61, s73, 8\n\t"
"s_or_b32 s61, s61, s66\n\t"
"s_cmp_lt_u32 s61, 192\n\t"
"s_cselect_b32 s70, s70, 2.0\n\t"
"s_lshr_b32 s61, s66, 3\n\t"
"s_lshl2_add_u32 s61, s61, s73\n\t"
"s_lshl_b32 s60, s61, 8\n\t"
"s_cmp_lt_u32 s61, 96\n\t"
"s_cselect_b32 s60, s60, 2.0\n\t"
"v_lshrrev_b32 v118, 1, v204\n\t"
"v_mul_u32_u24 v118, 220928, v118\n\t"
"v_lshl_add_u32 v118, v204, 7, v118\n\t"
"s_mov_b64 exec, 63\n\t"
"buffer_load_dword v118, v118, s[52:55], s60 offen\n\t"
"s_mov_b64 exec, -1\n\t"
"buffer_load_dword v187, off, s[8:11], s70 sc1\n\t"
"v_add_u32 v107, 0, v204\n\t"
"v_mul_u32_u24 v104, 0xab, v107\n\t"
"v_lshrrev_b32 v104, 10, v104\n\t"
"v_mul_u32_u24 v104, 0x120, v104\n\t"
"v_lshl_add_u32 v104, v107, 4, v104\n\t"
"v_add_u32 v107, 64, v204\n\t"
"v_mul_u32_u24 v105, 0xab, v107\n\t"
"v_lshrrev_b32 v105, 10, v105\n\t"
"v_mul_u32_u24 v105, 0x120, v105\n\t"
"v_lshl_add_u32 v105, v107, 4, v105\n\t"
"v_add_u32 v107, 128, v204\n\t"
"v_mul_u32_u24 v106, 0xab, v107\n\t"
"v_lshrrev_b32 v106, 10, v106\n\t"
"v_mul_u32_u24 v106, 0x120, v106\n\t"
"v_lshl_add_u32 v106, v107, 4, v106\n\t"
"s_mul_i32 s74, s73, 20736\n\t"
"s_mul_i32 s72, s73, 96\n\t"
"s_add_u32 s74, s74, 33280\n\t"
"s_add_u32 s75, s74, 12288\n\t"
"s_cmp_eq_u32 s73, 3\n\t"
"s_cselect_b32 s74, 16544, s74\n\t"
"s_cselect_b32 s75, 95488, s75\n\t"
"s_lshr_b32 s60, s66, 5\n\t"
"s_bfe_u32 s62, s66, 0x20003\n\t"
"s_and_b32 s61, s60, 1\n\t"
"s_lshl1_add_u32 s61, s73, s61\n\t"
"s_mul_i32 s62, s62, 288\n\t"
"s_add_u32 s61, s61, s62\n\t"
"s_lshr_b32 s60, s60, 1\n\t"
"s_mul_i32 s62, s60, 96\n\t"
"s_add_u32 s61, s61, s62\n\t"
"s_lshl_b32 s57, s61, 10\n\t"
"s_and_b32 s61, s66, 7\n\t"
"s_lshl_b32 s61, s61, 7\n\t"
"s_add_u32 s57, s57, s61\n\t"
"s_cmp_lt_u32 s73, 3\n\t"
"s_cselect_b32 s67, s66, 0xff\n\t"
"s_cmp_lt_u32 s67, 192\n\t"
"s_cselect_b32 s57, s57, 2.0\n\t"
"s_lshl_b32 s60, s73, 3\n\t"
"s_add_u32 s60, s60, 0\n\t"
"s_lshl_b32 s61, s73, 11\n\t"
"s_add_u32 s61, s61, 4096\n\t"
"v_lshl_add_u32 v46, v121, 6, s60\n\t"
"v_lshl_add_u32 v46, v44, 1, v46\n\t"
"v_lshl_add_u32 v42, v204, 1, s61\n\t"
"v_lshl_add_u32 v43, v204, 4, s61\n\t"
"v_mul_u32_u24 v44, 6144, v120\n\t"
"v_lshl_add_u32 v44, v121, 4, v44\n\t"
"v_mov_b32 v32, 0\n\t"
"v_mov_b32 v33, 0\n\t"
"v_mov_b32 v34, 0\n\t"
"v_mov_b32 v35, 0\n\t"
"v_mov_b32 v36, 0\n\t"
"v_mov_b32 v37, 0\n\t"
"v_mov_b32 v38, 0\n\t"
"v_mov_b32 v39, 0\n\t"
"s_waitcnt vmcnt(9)\n\t"
"s_barrier\n\t"
".p2align 3\n\t"
"ds_read_u16_d16_hi v32, v46 offset:0\n\t"
"ds_read_u16_d16_hi v33, v46 offset:512\n\t"
"ds_read_u16_d16_hi v34, v46 offset:1024\n\t"
"ds_read_u16_d16_hi v35, v46 offset:1536\n\t"
"ds_read_u16_d16_hi v36, v46 offset:2048\n\t"
"ds_read_u16_d16_hi v37, v46 offset:2560\n\t"
"ds_read_u16_d16_hi v38, v46 offset:3072\n\t"
"ds_read_u16_d16_hi v39, v46 offset:3584\n\t"
"s_waitcnt lgkmcnt(1)\n\t"
"v_add_f32 v48, v32, v38\n\t"
"s_waitcnt lgkmcnt(0)\n\t"
"v_add_f32 v49, v33, v39\n\t"
"v_add_f32 v50, v34, v40\n\t"
"v_add_f32 v51, v48, v35\n\t"
"v_sub_f32 v54, v48, v35\n\t"
"v_add_f32 v52, v49, v36\n\t"
"v_sub_f32 v55, v49, v36\n\t"
"v_add_f32 v53, v50, v37\n\t"
"v_sub_f32 v56, v50, v37\n\t"
"v_add_f32 v57, v32, v34\n\t"
"v_add_f32 v58, v51, v53\n\t"
"v_add_f32 v59, v54, v56\n\t"
"v_add_f32 v60, v38, v40\n\t"
"v_add_f32 v62, v57, v33\n\t"
"v_sub_f32 v63, v57, v33\n\t"
"v_add_f32 v64, v58, v52\n\t"
"v_sub_f32 v65, v58, v52\n\t"
"v_add_f32 v66, v59, v55\n\t"
"v_sub_f32 v67, v59, v55\n\t"
"v_add_f32 v68, v60, v39\n\t"
"v_sub_f32 v69, v60, v39\n\t"
"v_mul_f32 v71, 0.5, v62\n\t"
"v_mul_f32 v72, 0.5, v63\n\t"
"v_mul_f32 v74, 0.5, v51\n\t"
"v_mul_f32 v75, 0x3e800000, v64\n\t"
"v_mul_f32 v76, 0x3e800000, v65\n\t"
"v_mul_f32 v77, -0.5, v53\n\t"
"v_mul_f32 v79, 0.5, v66\n\t"
"v_mul_f32 v80, 0.5, v67\n\t"
"v_mul_f32 v83, -0.5, v68\n\t"
"v_mul_f32 v84, -0.5, v69\n\t"
".p2align 3\n\t"
"v_cvt_pk_bf16_f32 v86, v32, v71\n\t"
"v_cvt_pk_bf16_f32 v87, v72, -v34\n\t"
"v_cvt_pk_bf16_f32 v88, v74, v75\n\t"
"v_cvt_pk_bf16_f32 v89, v76, v77\n\t"
"v_cvt_pk_bf16_f32 v90, v54, v79\n\t"
"v_cvt_pk_bf16_f32 v91, v80, -v56\n\t"
"v_cvt_pk_bf16_f32 v92, -v38, v83\n\t"
"v_cvt_pk_bf16_f32 v93, v84, v40\n\t"
"ds_write_b16 v42, v86 offset:0\n\t"
"ds_write_b16_d16_hi v42, v86 offset:128\n\t"
"ds_write_b16 v42, v87 offset:256\n\t"
"ds_write_b16_d16_hi v42, v87 offset:384\n\t"
"ds_write_b16 v42, v88 offset:512\n\t"
"ds_write_b16_d16_hi v42, v88 offset:640\n\t"
"ds_write_b16 v42, v89 offset:768\n\t"
"ds_write_b16_d16_hi v42, v89 offset:896\n\t"
"ds_write_b16 v42, v90 offset:1024\n\t"
"ds_write_b16_d16_hi v42, v90 offset:1152\n\t"
"ds_write_b16 v42, v91 offset:1280\n\t"
"ds_write_b16_d16_hi v42, v91 offset:1408\n\t"
"ds_write_b16 v42, v92 offset:1536\n\t"
"ds_write_b16_d16_hi v42, v92 offset:1664\n\t"
"ds_write_b16 v42, v93 offset:1792\n\t"
"ds_write_b16_d16_hi v42, v93 offset:1920\n\t"
"ds_read_b128 v[96:99], v43 offset:0\n\t"
"ds_read_b128 v[100:103], v43 offset:1024\n\t"
"s_add_u32 s60, s57, 0\n\t"
"s_waitcnt lgkmcnt(1)\n\t"
"buffer_store_dwordx4 v[96:99], v44, s[8:11], s60 offen sc1\n\t"
"s_add_u32 s60, s57, 49152\n\t"
"s_waitcnt lgkmcnt(0)\n\t"
"buffer_store_dwordx4 v[100:103], v44, s[8:11], s60 offen sc1\n\t"
"v_lshrrev_b32 v123, 4, v204\n\t"
"v_bfe_u32 v124, v204, 2, 2\n\t"
"v_and_b32 v125, 3, v204\n\t"
"v_lshlrev_b32 v125, 3, v125\n\t"
"v_and_b32 v138, 49, v204\n\t"
"v_lshl_or_b32 v138, v124, 1, v138\n\t"
"v_and_b32 v139, 2, v204\n\t"
"v_lshl_or_b32 v138, v139, 2, v138\n\t"
"s_add_u32 s63, s26, 1152\n\t"
"v_and_b32 v126, 48, v204\n\t"
"v_and_b32 v127, 6, v204\n\t"
"v_lshl_add_u32 v126, v127, 5, v126\n\t"
"v_and_b32 v127, 8, v204\n\t"
"v_mul_u32_u24 v134, 516, v127\n\t"
"s_add_u32 m0, s74, 0\n\t"
"s_add_u32 s27, s72, 0\n\t"
"buffer_load_dwordx4 v104, s[52:55], s27 offen lds\n\t"
"v_and_b32 v127, 1, v204\n\t"
"v_mul_u32_u24 v127, 8320, v127\n\t"
"v_add3_u32 v224, v134, v126, v127\n\t"
"v_and_b32 v127, 16, v204\n\t"
"v_lshl_add_u32 v127, v127, 1, v125\n\t"
"v_lshl_add_u32 v127, v123, 8, v127\n\t"
"v_add_u32 v126, 4, v204\n\t"
"v_and_b32 v126, 8, v126\n\t"
"v_lshl_add_u32 v127, v126, 3, v127\n\t"
"v_mul_u32_u24 v126, 1152, v124\n\t"
"v_add3_u32 v226, v126, v127, s63\n\t"
"v_min_u32 v126, 2, v124\n\t"
"v_cmp_gt_u32 vcc, 48, v204\n\t"
"s_mov_b32 s48, 384\n\t"
"s_add_u32 m0, s74, 1024\n\t"
"s_add_u32 s27, s72, 0\n\t"
"buffer_load_dwordx4 v105, s[52:55], s27 offen lds\n\t"
"s_cmp_eq_u32 s65, 31\n\t"
"v_cndmask_b32 v126, v126, v123, vcc\n\t"
"v_cndmask_b32 v127, 4, v124, vcc\n\t"
"v_and_b32 v134, 1, v126\n\t"
"v_lshl_add_u32 v134, v134, 6, v125\n\t"
"v_lshl_add_u32 v134, v127, 7, v134\n\t"
"v_and_b32 v127, 2, v127\n\t"
"v_lshl_add_u32 v134, v127, 4, v134\n\t"
"v_mul_u32_u24 v126, 2304, v126\n\t"
"v_add3_u32 v227, v126, v134, s26\n\t"
"v_lshlrev_b32 v126, 14, v204\n\t"
"v_and_b32 v126, 0x8000, v126\n\t"
"v_or_b32 v134, 0x3f803f80, v126\n\t"
"v_and_b32 v126, 1, v204\n\t"
"v_cmp_eq_u32 vcc, 0, v126\n\t"
"s_cselect_b64 s[40:41], 0xff, 0\n\t"
"s_cselect_b64 s[46:47], -1, 0\n\t"
"s_add_u32 m0, s74, 2048\n\t"
"s_add_u32 s27, s72, 0\n\t"
"buffer_load_dwordx4 v106, s[52:55], s27 offen lds\n\t"
"v_cndmask_b32 v135, v134, 0, vcc\n\t"
"v_cndmask_b32 v134, 0, v134, vcc\n\t"
"v_xor_b32 v136, 0x80008000, v134\n\t"
"v_xor_b32 v137, 0x80008000, v135\n\t"
"v_accvgpr_write_b32 a220, v134\n\t"
"v_accvgpr_write_b32 a221, v135\n\t"
"v_accvgpr_write_b32 a222, v136\n\t"
"v_accvgpr_write_b32 a223, v137\n\t"
"v_xad_u32 v236, v128, 64, s48\n\t"
"v_xad_u32 v237, v129, 64, s48\n\t"
"s_add_u32 m0, s74, 3072\n\t"
"s_add_u32 s27, s72, 12288\n\t"
"buffer_load_dwordx4 v104, s[52:55], s27 offen lds\n\t"
"v_cndmask_b32 v237, v237, 2.0, s[40:41]\n\t"
"s_lshl_b64 s[40:41], s[40:41], 8\n\t"
"v_xad_u32 v238, v130, 64, s48\n\t"
"v_cndmask_b32 v238, v238, 2.0, s[40:41]\n\t"
"s_lshl_b64 s[40:41], s[40:41], 8\n\t"
"v_xad_u32 v239, v131, 64, s48\n\t"
"v_cndmask_b32 v239, v239, 2.0, s[40:41]\n\t"
"s_lshl_b64 s[40:41], s[40:41], 40\n\t"
"v_xad_u32 v240, v245, 64, s48\n\t"
"v_cndmask_b32 v240, v240, 2.0, s[40:41]\n\t"
"v_cmp_eq_u32 vcc, 8, v119\n\t"
"v_xad_u32 v247, v133, 64, s48\n\t"
"s_and_b64 vcc, vcc, s[46:47]\n\t"
"s_add_u32 m0, s74, 4096\n\t"
"s_add_u32 s27, s72, 12288\n\t"
"buffer_load_dwordx4 v105, s[52:55], s27 offen lds\n\t"
"v_cndmask_b32 v247, v247, 2.0, s[50:51]\n\t"
"v_cndmask_b32 v247, v247, 2.0, vcc\n\t"
"v_and_b32 v139, 7, v138\n\t"
"v_bfe_u32 v126, v138, 3, 1\n\t"
"v_lshrrev_b32 v127, 4, v138\n\t"
"v_sub_u32 v127, v127, v126\n\t"
"v_mov_b32 v134, 0xbf803f80\n\t"
"v_mov_b32 v135, 0x3f80bf80\n\t"
"v_cmp_eq_u32 vcc, 0, v127\n\t"
"v_add_u32 v126, 4, v126\n\t"
"s_nop 0\n\t"
"v_cndmask_b32 v134, 0, v134, vcc\n\t"
"v_cmp_eq_u32 vcc, 1, v127\n\t"
"s_nop 1\n\t"
"v_cndmask_b32 v134, v134, v135, vcc\n\t"
"v_cmp_eq_u32 vcc, 7, v139\n\t"
"s_add_u32 m0, s74, 5120\n\t"
"s_add_u32 s27, s72, 12288\n\t"
"buffer_load_dwordx4 v106, s[52:55], s27 offen lds\n\t"
"v_cndmask_b32 v135, 0, v135, vcc\n\t"
"v_cmp_gt_u32 vcc, 48, v138\n\t"
"s_nop 1\n\t"
"v_cndmask_b32 v134, v135, v134, vcc\n\t"
"v_mov_b32 v135, 0\n\t"
"v_cndmask_b32 v136, v126, v139, vcc\n\t"
"v_and_b32 v137, 1, v136\n\t"
"v_lshlrev_b32 v137, 4, v137\n\t"
"v_lshrrev_b32 v136, 1, v136\n\t"
"v_lshlrev_b64 v[134:135], v137, v[134:135]\n\t"
"v_cmp_eq_u32 vcc, 0, v136\n\t"
"s_nop 1\n\t"
"v_cndmask_b32 v140, 0, v134, vcc\n\t"
"v_accvgpr_write_b32 a224, v140\n\t"
"v_cndmask_b32 v141, 0, v135, vcc\n\t"
"s_add_u32 m0, s74, 6144\n\t"
"s_add_u32 s27, s72, 442368\n\t"
"buffer_load_dwordx4 v104, s[52:55], s27 offen lds\n\t"
"v_cmp_eq_u32 vcc, 1, v136\n\t"
"v_and_b32 v140, 0xff7fff7f, v140\n\t"
"v_xor_b32 v148, 0x80008000, v140\n\t"
"v_cndmask_b32 v141, v141, v134, vcc\n\t"
"v_cndmask_b32 v142, 0, v135, vcc\n\t"
"v_cmp_eq_u32 vcc, 2, v136\n\t"
"v_accvgpr_write_b32 a225, v141\n\t"
"v_accvgpr_write_b32 a216, v140\n\t"
"v_cndmask_b32 v142, v142, v134, vcc\n\t"
"v_cndmask_b32 v143, 0, v135, vcc\n\t"
"v_cmp_eq_u32 vcc, 3, v136\n\t"
"v_accvgpr_write_b32 a226, v142\n\t"
"v_and_b32 v141, 0xff7fff7f, v141\n\t"
"v_cndmask_b32 v143, v143, v134, vcc\n\t"
"s_add_u32 m0, s74, 7168\n\t"
"s_add_u32 s27, s72, 442368\n\t"
"buffer_load_dwordx4 v105, s[52:55], s27 offen lds\n\t"
"v_accvgpr_write_b32 a227, v143\n\t"
"v_xor_b32 v149, 0x80008000, v141\n\t"
"v_accvgpr_write_b32 a217, v141\n\t"
"v_and_b32 v142, 0xff7fff7f, v142\n\t"
"v_xor_b32 v150, 0x80008000, v142\n\t"
"v_accvgpr_write_b32 a218, v142\n\t"
"v_and_b32 v143, 0xff7fff7f, v143\n\t"
"v_xor_b32 v151, 0x80008000, v143\n\t"
"v_accvgpr_write_b32 a219, v143\n\t"
"s_mov_b32 s68, 3000\n\t"
"s_mul_i32 s61, s73, 294912\n\t"
"v_lshl_add_u32 v235, v204, 4, s61\n\t"
"v_and_b32 v107, 51, v204\n\t"
"s_add_u32 m0, s74, 8192\n\t"
"s_add_u32 s27, s72, 442368\n\t"
"buffer_load_dwordx4 v106, s[52:55], s27 offen lds\n\t"
"v_mul_u32_u24 v107, 24, v107\n\t"
"v_and_b32 v47, 8, v204\n\t"
"v_mad_u32_u24 v107, v47, 12, v107\n\t"
"v_and_b32 v47, 4, v204\n\t"
"v_mul_u32_u24 v47, 0x180, v47\n\t"
"v_add_u32 v107, v47, v107\n\t"
"v_add_u32 v47, s75, v107\n\t"
"v_add_u32 v107, s74, v107\n\t"
"v_lshrrev_b32 v112, 4, v204\n\t"
"v_bfe_u32 v113, v204, 2, 2\n\t"
"v_cmp_eq_u32 vcc, v112, v113\n\t"
"v_lshlrev_b32 v112, 4, v204\n\t"
"v_mov_b32 v113, 0x3e80\n\t"
"v_lshlrev_b32 v113, v112, v113\n\t"
"s_add_u32 m0, s74, 9216\n\t"
"s_add_u32 s27, s72, 454656\n\t"
"buffer_load_dwordx4 v104, s[52:55], s27 offen lds\n\t"
"v_cndmask_b32 v113, 0, v113, vcc\n\t"
"v_and_b32 v112, 2, v204\n\t"
"v_cmp_eq_u32 vcc, 0, v112\n\t"
"v_mov_b32 v108, 0\n\t"
"v_mov_b32 v109, 0\n\t"
"v_cndmask_b32 v110, 0, v113, vcc\n\t"
"v_cndmask_b32 v111, v113, 0, vcc\n\t"
"v_xor_b32 v114, 0x80008000, v110\n\t"
"v_xor_b32 v115, 0x80008000, v111\n\t"
"v_mov_b32 v112, 0\n\t"
"v_mov_b32 v113, 0\n\t"
"v_mov_b32 v116, 0\n\t"
"v_mov_b32 v117, 0\n\t"
"s_add_u32 m0, s74, 10240\n\t"
"s_add_u32 s27, s72, 454656\n\t"
"buffer_load_dwordx4 v105, s[52:55], s27 offen lds\n\t"
"s_waitcnt vmcnt(15)\n\t"
"s_waitcnt vmcnt(11)\n\t"
"v_readfirstlane_b32 s71, v187\n\t"
"s_barrier\n\t"
"s_add_u32 s71, s71, 1\n\t"
"v_mov_b32 v215, s71\n\t"
"s_mov_b64 exec, 1\n\t"
"buffer_store_dword v215, off, s[8:11], s70 sc1\n\t"
"s_mov_b64 exec, -1\n\t"
"v_add_u32 v248, 101632, v226\n\t"
"v_xor_b32 v249, 32, v248\n\t"
"v_xor_b32 v250, 64, v248\n\t"
"v_xor_b32 v251, 96, v248\n\t"
"ds_read_b64_tr_b16 a[178:179], v248 offset:128\n\t"
"ds_read_b64_tr_b16 a[186:187], v249 offset:128\n\t"
"s_add_u32 m0, s74, 11264\n\t"
"s_add_u32 s27, s72, 454656\n\t"
"buffer_load_dwordx4 v106, s[52:55], s27 offen lds\n\t"
"ds_read_b64_tr_b16 a[194:195], v250 offset:128\n\t"
"ds_read_b64_tr_b16 a[202:203], v251 offset:128\n\t"
"ds_read_b64_tr_b16 a[176:177], v248 offset:0\n\t"
"s_add_u32 m0, s75, 0\n\t"
"s_add_u32 s27, s72, 221184\n\t"
"buffer_load_dwordx4 v104, s[52:55], s27 offen lds\n\t"
"ds_read_b64_tr_b16 a[184:185], v249 offset:0\n\t"
"ds_read_b64_tr_b16 a[180:181], v249 offset:256\n\t"
"ds_read_b64_tr_b16 a[188:189], v248 offset:256\n\t"
"s_add_u32 m0, s75, 1024\n\t"
"s_add_u32 s27, s72, 221184\n\t"
"buffer_load_dwordx4 v105, s[52:55], s27 offen lds\n\t"
"ds_read_b64_tr_b16 a[192:193], v250 offset:0\n\t"
"ds_read_b64_tr_b16 a[200:201], v251 offset:0\n\t"
"ds_read_b64_tr_b16 a[196:197], v251 offset:256\n\t"
"s_add_u32 m0, s75, 2048\n\t"
"s_add_u32 s27, s72, 221184\n\t"
"buffer_load_dwordx4 v106, s[52:55], s27 offen lds\n\t"
"ds_read_b64_tr_b16 a[204:205], v250 offset:256\n\t"
"s_waitcnt lgkmcnt(11)\n\t"
"v_mfma_f32_4x4x4_16b_bf16 v[8:11], a[220:221], a[178:179], 0\n\t"
"s_waitcnt lgkmcnt(10)\n\t"
"v_mfma_f32_4x4x4_16b_bf16 v[12:15], a[220:221], a[186:187], 0\n\t"
"s_waitcnt lgkmcnt(9)\n\t"
"v_mfma_f32_4x4x4_16b_bf16 v[24:27], a[220:221], a[194:195], 0\n\t"
"s_waitcnt lgkmcnt(8)\n\t"
"v_mfma_f32_4x4x4_16b_bf16 v[28:31], a[220:221], a[202:203], 0\n\t"
"s_waitcnt lgkmcnt(7)\n\t"
"v_mfma_f32_4x4x4_16b_bf16 v[0:3], a[222:223], a[176:177], v[8:11]\n\t"
"s_waitcnt lgkmcnt(6)\n\t"
"v_mfma_f32_4x4x4_16b_bf16 v[4:7], a[222:223], a[184:185], v[12:15]\n\t"
"s_add_u32 m0, s75, 3072\n\t"
"s_add_u32 s27, s72, 233472\n\t"
"buffer_load_dwordx4 v104, s[52:55], s27 offen lds\n\t"
"s_add_u32 m0, s75, 4096\n\t"
"s_add_u32 s27, s72, 233472\n\t"
"buffer_load_dwordx4 v105, s[52:55], s27 offen lds\n\t"
"s_waitcnt lgkmcnt(5)\n\t"
"v_mfma_f32_4x4x4_16b_bf16 v[8:11], a[222:223], a[180:181], v[8:11]\n\t"
"s_waitcnt lgkmcnt(4)\n\t"
"v_mfma_f32_4x4x4_16b_bf16 v[12:15], a[222:223], a[188:189], v[12:15]\n\t"
"s_waitcnt lgkmcnt(3)\n\t"
"v_mfma_f32_4x4x4_16b_bf16 v[16:19], a[222:223], a[192:193], v[24:27]\n\t"
"s_waitcnt lgkmcnt(2)\n\t"
"v_mfma_f32_4x4x4_16b_bf16 v[20:23], a[222:223], a[200:201], v[28:31]\n\t"
"s_waitcnt lgkmcnt(1)\n\t"
"v_mfma_f32_4x4x4_16b_bf16 v[24:27], a[222:223], a[196:197], v[24:27]\n\t"
"s_waitcnt lgkmcnt(0)\n\t"
"v_mfma_f32_4x4x4_16b_bf16 v[28:31], a[222:223], a[204:205], v[28:31]\n\t"
"s_add_u32 m0, s75, 5120\n\t"
"s_add_u32 s27, s72, 233472\n\t"
"buffer_load_dwordx4 v106, s[52:55], s27 offen lds\n\t"
"s_add_u32 m0, s26, 124544\n\t"
"s_nop 0\n\t"
"buffer_load_dwordx4 v241, s[12:15], s22 offen offset:128 nt lds\n\t"
"v_cvt_pk_bf16_f32 v0, -v0, -v4\n\t"
"v_cvt_pk_bf16_f32 v1, -v1, -v5\n\t"
"v_cvt_pk_bf16_f32 v2, -v2, -v6\n\t"
"v_cvt_pk_bf16_f32 v3, -v3, -v7\n\t"
"s_lshl_b32 m0, s73, 8\n\t"
"s_nop 0\n\t"
".p2align 3\n\t"
"ds_write_addtid_b32 v0 offset:0\n\t"
"ds_write_addtid_b32 v1 offset:4128\n\t"
"ds_write_addtid_b32 v2 offset:1024\n\t"
"ds_write_addtid_b32 v3 offset:5152\n\t"
"v_cvt_pk_bf16_f32 v8, v8, v12\n\t"
"v_cvt_pk_bf16_f32 v9, v9, v13\n\t"
"v_cvt_pk_bf16_f32 v10, v10, v14\n\t"
"v_cvt_pk_bf16_f32 v11, v11, v15\n\t"
"ds_write_addtid_b32 v8 offset:8320\n\t"
"ds_write_addtid_b32 v9 offset:12448\n\t"
"ds_write_addtid_b32 v10 offset:9344\n\t"
"ds_write_addtid_b32 v11 offset:13472\n\t"
"v_cvt_pk_bf16_f32 v16, -v16, -v20\n\t"
"v_cvt_pk_bf16_f32 v17, -v17, -v21\n\t"
"v_cvt_pk_bf16_f32 v18, -v18, -v22\n\t"
"v_cvt_pk_bf16_f32 v19, -v19, -v23\n\t"
"ds_write_addtid_b32 v16 offset:2048\n\t"
"ds_write_addtid_b32 v17 offset:6176\n\t"
"ds_write_addtid_b32 v18 offset:3072\n\t"
"ds_write_addtid_b32 v19 offset:7200\n\t"
"v_cvt_pk_bf16_f32 v24, v24, v28\n\t"
"v_cvt_pk_bf16_f32 v25, v25, v29\n\t"
"v_cvt_pk_bf16_f32 v26, v26, v30\n\t"
"v_cvt_pk_bf16_f32 v27, v27, v31\n\t"
"ds_write_addtid_b32 v24 offset:10368\n\t"
"ds_write_addtid_b32 v25 offset:14496\n\t"
"ds_write_addtid_b32 v26 offset:11392\n\t"
"ds_write_addtid_b32 v27 offset:15520\n\t"
"v_add_u32 v248, 101632, v227\n\t"
"v_xor_b32 v249, 32, v248\n\t"
"v_xor_b32 v250, 64, v248\n\t"
"v_xor_b32 v251, 96, v248\n\t"
"ds_read_b64_tr_b16 a[176:177], v248 offset:0\n\t"
"ds_read_b64_tr_b16 a[178:179], v248 offset:512\n\t"
"ds_read_b64_tr_b16 a[180:181], v249 offset:0\n\t"
"ds_read_b64_tr_b16 a[182:183], v249 offset:512\n\t"
"s_add_u32 m0, s26, 124544\n\t"
"s_nop 0\n\t"
".p2align 3\n\t"
"buffer_load_dwordx4 v242, s[12:15], s22 offen offset:1152 nt lds\n\t"
"ds_read_b64_tr_b16 a[184:185], v250 offset:0\n\t"
"ds_read_b64_tr_b16 a[186:187], v250 offset:512\n\t"
"ds_read_b64_tr_b16 a[188:189], v251 offset:0\n\t"
"ds_read_b64_tr_b16 a[190:191], v251 offset:512\n\t"
"buffer_load_dwordx4 v243, s[12:15], s22 offen offset:2176 nt lds\n\t"
"ds_read_b64_tr_b16 a[192:193], v248 offset:1152\n\t"
"ds_read_b64_tr_b16 a[194:195], v248 offset:1664\n\t"
"ds_read_b64_tr_b16 a[196:197], v249 offset:1152\n\t"
"ds_read_b64_tr_b16 a[198:199], v249 offset:1664\n\t"
"buffer_load_dwordx4 v244, s[12:15], s22 offen offset:3200 nt lds\n\t"
"ds_read_b64_tr_b16 a[200:201], v250 offset:1152\n\t"
"ds_read_b64_tr_b16 a[202:203], v250 offset:1664\n\t"
"ds_read_b64_tr_b16 a[204:205], v251 offset:1152\n\t"
"ds_read_b64_tr_b16 a[206:207], v251 offset:1664\n\t"
"s_mov_b64 exec, s[36:37]\n\t"
"buffer_load_dwordx4 v245, s[12:15], s22 offen offset:3712 nt lds\n\t"
"s_mov_b64 exec, -1\n\t"
"s_waitcnt vmcnt(18)\n\t"
".p2align 3\n\t"
"ds_read_b64_tr_b16 a[48:49], v107 offset:0\n\t"
"ds_read_b64_tr_b16 a[50:51], v107 offset:192\n\t"
"ds_read_b64_tr_b16 a[52:53], v107 offset:3072\n\t"
"ds_read_b64_tr_b16 a[54:55], v107 offset:3264\n\t"
"ds_read_b64_tr_b16 a[56:57], v107 offset:8\n\t"
"ds_read_b64_tr_b16 a[58:59], v107 offset:200\n\t"
"ds_read_b64_tr_b16 a[60:61], v107 offset:3080\n\t"
"ds_read_b64_tr_b16 a[62:63], v107 offset:3272\n\t"
"ds_read_b64_tr_b16 a[64:65], v107 offset:16\n\t"
"ds_read_b64_tr_b16 a[66:67], v107 offset:208\n\t"
"ds_read_b64_tr_b16 a[68:69], v107 offset:3088\n\t"
"ds_read_b64_tr_b16 a[70:71], v107 offset:3280\n\t"
"s_waitcnt vmcnt(11)\n\t"
".p2align 3\n\t"
"ds_read_b64_tr_b16 a[72:73], v107 offset:6144\n\t"
"ds_read_b64_tr_b16 a[74:75], v107 offset:6336\n\t"
"ds_read_b64_tr_b16 a[76:77], v107 offset:9216\n\t"
"ds_read_b64_tr_b16 a[78:79], v107 offset:9408\n\t"
"ds_read_b64_tr_b16 a[80:81], v107 offset:6152\n\t"
"ds_read_b64_tr_b16 a[82:83], v107 offset:6344\n\t"
"ds_read_b64_tr_b16 a[84:85], v107 offset:9224\n\t"
"ds_read_b64_tr_b16 a[86:87], v107 offset:9416\n\t"
"ds_read_b64_tr_b16 a[88:89], v107 offset:6160\n\t"
"ds_read_b64_tr_b16 a[90:91], v107 offset:6352\n\t"
"ds_read_b64_tr_b16 a[92:93], v107 offset:9232\n\t"
"ds_read_b64_tr_b16 a[94:95], v107 offset:9424\n\t"
"s_waitcnt lgkmcnt(15)\n\t"
"v_mfma_f32_16x16x32_bf16 v[0:3], v[110:113], a[48:51], 0\n\t"
"s_waitcnt lgkmcnt(10)\n\t"
"v_mfma_f32_16x16x32_bf16 v[0:3], v[110:113], a[72:75], v[0:3]\n\t"
"v_mfma_f32_16x16x32_bf16 v[8:11], v[108:111], a[48:51], 0\n\t"
"v_mfma_f32_16x16x32_bf16 v[8:11], v[108:111], a[72:75], v[8:11]\n\t"
"s_add_u32 m0, s43, 122240\n\t"
"s_mov_b64 exec, s[44:45]\n\t"
"buffer_load_dwordx4 v246, s[12:15], 0 offen offset:128 nt lds\n\t"
"s_mov_b64 exec, -1\n\t"
"v_mfma_f32_16x16x32_bf16 v[16:19], v[110:113], a[52:55], 0\n\t"
"s_waitcnt lgkmcnt(8)\n\t"
"v_mfma_f32_16x16x32_bf16 v[16:19], v[110:113], a[76:79], v[16:19]\n\t"
"v_mfma_f32_16x16x32_bf16 v[24:27], v[108:111], a[52:55], 0\n\t"
"v_mfma_f32_16x16x32_bf16 v[24:27], v[108:111], a[76:79], v[24:27]\n\t"
"s_add_u32 m0, s26, 145152\n\t"
"s_nop 0\n\t"
"buffer_load_dwordx4 v241, s[12:15], s22 offen offset:256 nt lds\n\t"
"s_waitcnt vmcnt(7)\n\t"
".p2align 3\n\t"
"ds_read_b64_tr_b16 a[0:1], v47 offset:0\n\t"
"ds_read_b64_tr_b16 a[2:3], v47 offset:192\n\t"
"ds_read_b64_tr_b16 a[4:5], v47 offset:3072\n\t"
"ds_read_b64_tr_b16 a[6:7], v47 offset:3264\n\t"
"ds_read_b64_tr_b16 a[8:9], v47 offset:8\n\t"
"ds_read_b64_tr_b16 a[10:11], v47 offset:200\n\t"
"ds_read_b64_tr_b16 a[12:13], v47 offset:3080\n\t"
"ds_read_b64_tr_b16 a[14:15], v47 offset:3272\n\t"
"ds_read_b64_tr_b16 a[16:17], v47 offset:16\n\t"
"ds_read_b64_tr_b16 a[18:19], v47 offset:208\n\t"
"ds_read_b64_tr_b16 a[20:21], v47 offset:3088\n\t"
"ds_read_b64_tr_b16 a[22:23], v47 offset:3280\n\t"
"v_mfma_f32_16x16x32_bf16 v[32:35], v[110:113], a[56:59], 0\n\t"
"s_waitcnt lgkmcnt(15)\n\t"
"v_mfma_f32_16x16x32_bf16 v[32:35], v[110:113], a[80:83], v[32:35]\n\t"
"v_mfma_f32_16x16x32_bf16 v[96:99], v[108:111], a[56:59], 0\n\t"
"v_mfma_f32_16x16x32_bf16 v[96:99], v[108:111], a[80:83], v[96:99]\n\t"
"buffer_load_dwordx4 v242, s[12:15], s22 offen offset:1280 nt lds\n\t"
"v_mfma_f32_16x16x32_bf16 v[120:123], v[110:113], a[60:63], 0\n\t"
"v_mfma_f32_16x16x32_bf16 v[120:123], v[110:113], a[84:87], v[120:123]\n\t"
"v_mfma_f32_16x16x32_bf16 v[128:131], v[108:111], a[60:63], 0\n\t"
"v_mfma_f32_16x16x32_bf16 v[128:131], v[108:111], a[84:87], v[128:131]\n\t"
"buffer_load_dwordx4 v243, s[12:15], s22 offen offset:2304 nt lds\n\t"
"v_mfma_f32_16x16x32_bf16 v[136:139], v[110:113], a[64:67], 0\n\t"
"s_waitcnt lgkmcnt(14)\n\t"
"v_mfma_f32_16x16x32_bf16 v[136:139], v[110:113], a[88:91], v[136:139]\n\t"
"v_mfma_f32_16x16x32_bf16 v[152:155], v[108:111], a[64:67], 0\n\t"
"v_mfma_f32_16x16x32_bf16 v[152:155], v[108:111], a[88:91], v[152:155]\n\t"
"buffer_load_dwordx4 v244, s[12:15], s22 offen offset:3328 nt lds\n\t"
"v_mfma_f32_16x16x32_bf16 v[160:163], v[110:113], a[68:71], 0\n\t"
"s_waitcnt lgkmcnt(12)\n\t"
"v_mfma_f32_16x16x32_bf16 v[160:163], v[110:113], a[92:95], v[160:163]\n\t"
"v_mfma_f32_16x16x32_bf16 v[168:171], v[108:111], a[68:71], 0\n\t"
"v_mfma_f32_16x16x32_bf16 v[168:171], v[108:111], a[92:95], v[168:171]\n\t"
"s_mov_b64 exec, s[36:37]\n\t"
"buffer_load_dwordx4 v245, s[12:15], s22 offen offset:3840 nt lds\n\t"
"s_mov_b64 exec, -1\n\t"
"s_waitcnt lgkmcnt(10)\n\t"
"v_mfma_f32_16x16x32_bf16 v[4:7], v[110:113], a[0:3], v[0:3]\n\t"
"v_mfma_f32_16x16x32_bf16 v[0:3], v[114:117], a[0:3], v[0:3]\n\t"
"s_add_u32 m0, s43, 142848\n\t"
"s_mov_b64 exec, s[44:45]\n\t"
"buffer_load_dwordx4 v246, s[12:15], 0 offen offset:256 nt lds\n\t"
"s_mov_b64 exec, -1\n\t"
"v_mfma_f32_16x16x32_bf16 v[12:15], v[108:111], a[0:3], v[8:11]\n\t"
"v_mfma_f32_16x16x32_bf16 v[8:11], v[112:115], a[0:3], v[8:11]\n\t"
"s_waitcnt lgkmcnt(8)\n\t"
"v_mfma_f32_16x16x32_bf16 v[20:23], v[110:113], a[4:7], v[16:19]\n\t"
"v_mfma_f32_16x16x32_bf16 v[16:19], v[114:117], a[4:7], v[16:19]\n\t"
"v_mfma_f32_16x16x32_bf16 v[28:31], v[108:111], a[4:7], v[24:27]\n\t"
"v_cvt_pk_bf16_f32 v48, v4, v5\n\t"
"v_cvt_pk_bf16_f32 v49, v6, v7\n\t"
"v_mfma_f32_16x16x32_bf16 v[24:27], v[112:115], a[4:7], v[24:27]\n\t"
"v_cvt_pk_bf16_f32 v72, v0, v1\n\t"
"v_cvt_pk_bf16_f32 v73, v2, v3\n\t"
"s_waitcnt lgkmcnt(6)\n\t"
"v_mfma_f32_16x16x32_bf16 v[36:39], v[110:113], a[8:11], v[32:35]\n\t"
"v_cvt_pk_bf16_f32 v50, v12, v13\n\t"
"v_cvt_pk_bf16_f32 v51, v14, v15\n\t"
"v_mfma_f32_16x16x32_bf16 v[32:35], v[114:117], a[8:11], v[32:35]\n\t"
"v_cvt_pk_bf16_f32 v74, v8, v9\n\t"
"v_cvt_pk_bf16_f32 v75, v10, v11\n\t"
"v_mfma_f32_16x16x32_bf16 v[100:103], v[108:111], a[8:11], v[96:99]\n\t"
"v_cvt_pk_bf16_f32 v52, v20, v21\n\t"
"v_cvt_pk_bf16_f32 v53, v22, v23\n\t"
"v_mfma_f32_16x16x32_bf16 v[96:99], v[112:115], a[8:11], v[96:99]\n\t"
"v_cvt_pk_bf16_f32 v76, v16, v17\n\t"
"v_cvt_pk_bf16_f32 v77, v18, v19\n\t"
"s_waitcnt lgkmcnt(4)\n\t"
"v_mfma_f32_16x16x32_bf16 v[124:127], v[110:113], a[12:15], v[120:123]\n\t"
"v_cvt_pk_bf16_f32 v54, v28, v29\n\t"
"v_cvt_pk_bf16_f32 v55, v30, v31\n\t"
"v_mfma_f32_16x16x32_bf16 v[120:123], v[114:117], a[12:15], v[120:123]\n\t"
"v_cvt_pk_bf16_f32 v78, v24, v25\n\t"
"v_cvt_pk_bf16_f32 v79, v26, v27\n\t"
"v_mfma_f32_16x16x32_bf16 v[132:135], v[108:111], a[12:15], v[128:131]\n\t"
"v_cvt_pk_bf16_f32 v56, v36, v37\n\t"
"v_cvt_pk_bf16_f32 v57, v38, v39\n\t"
"v_mfma_f32_16x16x32_bf16 v[128:131], v[112:115], a[12:15], v[128:131]\n\t"
"v_cvt_pk_bf16_f32 v80, v32, v33\n\t"
"v_cvt_pk_bf16_f32 v81, v34, v35\n\t"
"s_waitcnt lgkmcnt(2)\n\t"
"v_mfma_f32_16x16x32_bf16 v[140:143], v[110:113], a[16:19], v[136:139]\n\t"
"v_cvt_pk_bf16_f32 v58, v100, v101\n\t"
"v_cvt_pk_bf16_f32 v59, v102, v103\n\t"
"v_mfma_f32_16x16x32_bf16 v[136:139], v[114:117], a[16:19], v[136:139]\n\t"
"v_cvt_pk_bf16_f32 v82, v96, v97\n\t"
"v_cvt_pk_bf16_f32 v83, v98, v99\n\t"
"v_mfma_f32_16x16x32_bf16 v[156:159], v[108:111], a[16:19], v[152:155]\n\t"
"v_cvt_pk_bf16_f32 v60, v124, v125\n\t"
"v_cvt_pk_bf16_f32 v61, v126, v127\n\t"
"v_mfma_f32_16x16x32_bf16 v[152:155], v[112:115], a[16:19], v[152:155]\n\t"
"v_cvt_pk_bf16_f32 v84, v120, v121\n\t"
"v_cvt_pk_bf16_f32 v85, v122, v123\n\t"
"s_waitcnt lgkmcnt(0)\n\t"
"v_mfma_f32_16x16x32_bf16 v[164:167], v[110:113], a[20:23], v[160:163]\n\t"
"v_cvt_pk_bf16_f32 v62, v132, v133\n\t"
"v_cvt_pk_bf16_f32 v63, v134, v135\n\t"
"v_mfma_f32_16x16x32_bf16 v[160:163], v[114:117], a[20:23], v[160:163]\n\t"
"v_cvt_pk_bf16_f32 v86, v128, v129\n\t"
"v_cvt_pk_bf16_f32 v87, v130, v131\n\t"
"v_mfma_f32_16x16x32_bf16 v[172:175], v[108:111], a[20:23], v[168:171]\n\t"
"v_cvt_pk_bf16_f32 v64, v140, v141\n\t"
"v_cvt_pk_bf16_f32 v65, v142, v143\n\t"
"v_mfma_f32_16x16x32_bf16 v[168:171], v[112:115], a[20:23], v[168:171]\n\t"
".p2align 3\n\t"
"v_cvt_pk_bf16_f32 v88, v136, v137\n\t"
"v_cvt_pk_bf16_f32 v89, v138, v139\n\t"
"v_cvt_pk_bf16_f32 v66, v156, v157\n\t"
"v_cvt_pk_bf16_f32 v67, v158, v159\n\t"
"v_cvt_pk_bf16_f32 v90, v152, v153\n\t"
"v_cvt_pk_bf16_f32 v91, v154, v155\n\t"
"v_cvt_pk_bf16_f32 v68, v164, v165\n\t"
"v_cvt_pk_bf16_f32 v69, v166, v167\n\t"
"v_cvt_pk_bf16_f32 v92, v160, v161\n\t"
"v_cvt_pk_bf16_f32 v93, v162, v163\n\t"
"v_cvt_pk_bf16_f32 v70, v172, v173\n\t"
"v_cvt_pk_bf16_f32 v71, v174, v175\n\t"
"v_cvt_pk_bf16_f32 v94, v168, v169\n\t"
"v_cvt_pk_bf16_f32 v95, v170, v171\n\t"
"s_barrier\n\t"
"v_mfma_f32_16x16x32_bf16 v[192:195], a[216:219], a[176:179], 0\n\t"
"s_add_u32 m0, s43, 53888\n\t"
"s_mov_b64 exec, s[44:45]\n\t"
"buffer_load_dwordx4 v247, s[12:15], 0 offen offset:128 nt lds\n\t"
"s_mov_b64 exec, -1\n\t"
"v_mfma_f32_16x16x32_bf16 v[196:199], a[216:219], a[180:183], 0\n\t"
"s_add_u32 m0, s26, 56192\n\t"
"ds_read_b128 a[144:147], v224 offset:0\n\t"
"ds_read_b128 a[160:163], v224 offset:2048\n\t"
"v_mfma_f32_16x16x32_bf16 v[200:203], a[216:219], a[184:187], 0\n\t"
"s_mov_b32 s16, s80\n\t"
"ds_read_b128 a[148:151], v224 offset:256\n\t"
"ds_read_b128 a[152:155], v224 offset:512\n\t"
"v_mfma_f32_16x16x32_bf16 v[204:207], a[216:219], a[188:191], 0\n\t"
"s_and_b32 s17, s81, 0xffff\n\t"
"ds_read_b128 a[164:167], v224 offset:2304\n\t"
"ds_read_b128 a[172:175], v224 offset:2816\n\t"
"v_mfma_f32_16x16x32_bf16 v[208:211], v[148:151], a[192:195], 0\n\t"
"s_mov_b32 s18, 100663296\n\t"
"ds_read_b128 a[168:171], v224 offset:2560\n\t"
"ds_read_b128 a[156:159], v224 offset:768\n\t"
"v_mfma_f32_16x16x32_bf16 v[212:215], v[148:151], a[196:199], 0\n\t"
"s_waitcnt lgkmcnt(6)\n\t"
"s_mov_b32 s19, 0x20000\n\t"
"s_mov_b32 s21, 4\n\t"
"s_mov_b32 s38, -2.0\n\t"
"v_mfma_f32_16x16x32_bf16 v[96:99], v[48:51], a[144:147], 0\n\t"
"s_mov_b32 s39, -2.0\n\t"
"v_mbcnt_lo_u32_b32 v176, -1, 0\n\t"
"v_mbcnt_hi_u32_b32 v176, -1, v176\n\t"
"v_mfma_f32_16x16x32_bf16 v[96:99], v[52:55], a[160:163], v[96:99]\n\t"
"buffer_load_dwordx4 v236, s[12:15], s22 offen offset:128 nt lds\n\t"
"v_bfe_u32 v156, v176, 2, 2\n\t"
"v_and_b32 v157, 3, v176\n\t"
"v_mfma_f32_16x16x32_bf16 v[112:115], v[56:59], a[144:147], 0\n\t"
"s_lshl_b32 m0, s73, 8\n\t"
"v_lshlrev_b32 v157, 3, v157\n\t"
"v_and_b32 v158, 48, v176\n\t"
"v_mfma_f32_16x16x32_bf16 v[112:115], v[60:63], a[160:163], v[112:115]\n\t"
"v_lshl_add_u32 v158, v157, 3, v158\n\t"
"v_and_b32 v159, 8, v176\n\t"
"v_cvt_pk_bf16_f32 v202, v202, v206\n\t"
"v_mfma_f32_16x16x32_bf16 v[128:131], v[64:67], a[144:147], 0\n\t"
"v_mul_u32_u24 v166, 516, v159\n\t"
"v_cvt_pk_bf16_f32 v200, v200, v204\n\t"
"v_mfma_f32_16x16x32_bf16 v[128:131], v[68:71], a[160:163], v[128:131]\n\t"
"s_waitcnt lgkmcnt(3)\n\t"
"ds_read_b128 a[144:147], v224 offset:1024\n\t"
"v_and_b32 v159, 4, v176\n\t"
"v_mfma_f32_16x16x32_bf16 v[132:135], v[64:67], a[148:151], 0\n\t"
"v_mul_u32_u24 v159, 2080, v159\n\t"
"v_cvt_pk_bf16_f32 v194, v194, v198\n\t"
"v_mfma_f32_16x16x32_bf16 v[132:135], v[68:71], a[164:167], v[132:135]\n\t"
"ds_read_b128 a[160:163], v224 offset:3072\n\t"
"v_cvt_pk_bf16_f32 v208, v208, v212\n\t"
"v_mfma_f32_16x16x32_bf16 v[116:119], v[56:59], a[148:151], 0\n\t"
"ds_write_addtid_b32 v200 offset:18592\n\t"
"v_cvt_pk_bf16_f32 v210, v210, v214\n\t"
"v_mfma_f32_16x16x32_bf16 v[116:119], v[60:63], a[164:167], v[116:119]\n\t"
"ds_write_addtid_b32 v202 offset:22720\n\t"
"buffer_load_dword v190, v189, s[8:11], 0 offen sc1\n\t"
"v_cvt_pk_bf16_f32 v201, v201, v205\n\t"
"s_waitcnt vmcnt(9)\n\t"
"s_barrier\n\t"
"v_mfma_f32_16x16x32_bf16 v[216:219], v[148:151], a[200:203], 0\n\t"
"s_add_u32 m0, s26, 56192\n\t"
"ds_read_b64_tr_b16 a[176:177], v248 offset:20736\n\t"
"v_cvt_pk_bf16_f32 v193, v193, v197\n\t"
"v_mfma_f32_16x16x32_bf16 v[100:103], v[48:51], a[148:151], 0\n\t"
"ds_read_b128 a[148:151], v224 offset:1280\n\t"
"v_cvt_pk_bf16_f32 v211, v211, v215\n\t"
"v_mfma_f32_16x16x32_bf16 v[100:103], v[52:55], a[164:167], v[100:103]\n\t"
"s_waitcnt lgkmcnt(7)\n\t"
"ds_read_b128 a[164:167], v224 offset:3328\n\t"
"v_cvt_pk_bf16_f32 v192, v192, v196\n\t"
"v_cvt_pk_bf16_f32 v203, v203, v207\n\t"
"v_cvt_pk_bf16_f32 v209, v209, v213\n\t"
"v_mfma_f32_16x16x32_bf16 v[104:107], v[48:51], a[152:155], 0\n\t"
"ds_read_b64_tr_b16 a[178:179], v248 offset:21248\n\t"
"v_add3_u32 v166, v166, v158, v159\n\t"
"v_mfma_f32_16x16x32_bf16 v[104:107], v[52:55], a[168:171], v[104:107]\n\t"
"buffer_load_dwordx4 v237, s[12:15], s22 offen offset:1152 nt lds\n\t"
"v_mfma_f32_16x16x32_bf16 v[220:223], v[148:151], a[204:207], 0\n\t"
"s_lshl_b32 m0, s73, 8\n\t"
"ds_read_b64_tr_b16 a[180:181], v249 offset:20736\n\t"
"v_accvgpr_write_b32 a212, v224\n\t"
"v_mfma_f32_16x16x32_bf16 v[120:123], v[56:59], a[152:155], 0\n\t"
"ds_read_b64_tr_b16 a[182:183], v249 offset:21248\n\t"
"v_accvgpr_write_b32 a213, v166\n\t"
"v_mfma_f32_16x16x32_bf16 v[120:123], v[60:63], a[168:171], v[120:123]\n\t"
"v_lshlrev_b32 v158, 15, v176\n\t"
"v_and_b32 v158, 0x8000, v158\n\t"
"v_cvt_pk_bf16_f32 v195, v195, v199\n\t"
"v_mfma_f32_16x16x32_bf16 v[136:139], v[64:67], a[152:155], 0\n\t"
"ds_read_b128 a[152:155], v224 offset:1536\n\t"
"v_or_b32 v167, 0x3f803f80, v158\n\t"
"v_mfma_f32_16x16x32_bf16 v[136:139], v[68:71], a[168:171], v[136:139]\n\t"
"s_waitcnt lgkmcnt(11)\n\t"
"ds_write_addtid_b32 v194 offset:20672\n\t"
"v_and_b32 v158, 2, v176\n\t"
"v_mfma_f32_16x16x32_bf16 v[140:143], v[64:67], a[156:159], 0\n\t"
"ds_write_addtid_b32 v193 offset:24864\n\t"
"v_mul_u32_u24 v158, 0x4000, v158\n\t"
"v_mfma_f32_16x16x32_bf16 v[140:143], v[68:71], a[172:175], v[140:143]\n\t"
"ds_write_addtid_b32 v211 offset:30016\n\t"
"v_lshl_or_b32 v158, v158, 16, v158\n\t"
"v_cvt_pk_bf16_f32 v216, v216, v220\n\t"
"v_cvt_pk_bf16_f32 v217, v217, v221\n\t"
"v_mfma_f32_16x16x32_bf16 v[124:127], v[56:59], a[156:159], 0\n\t"
"ds_write_addtid_b32 v192 offset:16544\n\t"
"v_cvt_pk_bf16_f32 v218, v218, v222\n\t"
"v_mfma_f32_16x16x32_bf16 v[124:127], v[60:63], a[172:175], v[124:127]\n\t"
"ds_write_addtid_b32 v203 offset:31040\n\t"
"buffer_load_dword v191, v189, s[8:11], 0 offen sc1\n\t"
"v_mfma_f32_16x16x32_bf16 v[108:111], v[48:51], a[156:159], 0\n\t"
"v_xor_b32 v166, v167, v158\n\t"
"v_accvgpr_write_b32 a210, v166\n\t"
"v_mfma_f32_16x16x32_bf16 v[108:111], v[52:55], a[172:175], v[108:111]\n\t"
"s_waitcnt lgkmcnt(14)\n\t"
"ds_write_addtid_b32 v201 offset:26912\n\t"
"ds_write_addtid_b32 v210 offset:21696\n\t"
"ds_read_b64_tr_b16 a[184:185], v250 offset:20736\n\t"
"ds_read_b64_tr_b16 a[186:187], v250 offset:21248\n\t"
"ds_read_b64_tr_b16 a[188:189], v251 offset:20736\n\t"
"v_accvgpr_write_b32 a211, v167\n\t"
"v_cvt_pk_bf16_f32 v219, v219, v223\n\t"
"v_mfma_f32_16x16x32_bf16 v[0:3], v[72:75], a[144:147], v[96:99]\n\t"
"ds_read_b64_tr_b16 a[190:191], v251 offset:21248\n\t"
"v_add_u32 v252, 4096, v235\n\t"
"v_mfma_f32_16x16x32_bf16 v[0:3], v[76:79], a[160:163], v[0:3]\n\t"
"s_add_u32 s61, s26, 4294900096\n\t"
".p2align 3\n\t"
"ds_read_b128 a[168:171], v224 offset:3584\n\t"
"ds_read_b128 a[156:159], v224 offset:1792\n\t"
"ds_write_addtid_b32 v195 offset:28992\n\t"
"ds_write_addtid_b32 v208 offset:17568\n\t"
"ds_write_addtid_b32 v209 offset:25888\n\t"
"ds_write_addtid_b32 v216 offset:19616\n\t"
"ds_write_addtid_b32 v218 offset:23744\n\t"
"ds_write_addtid_b32 v219 offset:32064\n\t"
"ds_read_b64_tr_b16 a[192:193], v248 offset:21888\n\t"
"ds_read_b64_tr_b16 a[194:195], v248 offset:22400\n\t"
"ds_read_b64_tr_b16 a[196:197], v249 offset:21888\n\t"
"ds_read_b64_tr_b16 a[198:199], v249 offset:22400\n\t"
"v_lshrrev_b32 v155, 4, v176\n\t"
"v_and_b32 v158, 1, v156\n\t"
"v_lshrrev_b32 v159, 1, v156\n\t"
"v_mfma_f32_16x16x32_bf16 v[16:19], v[80:83], a[144:147], v[112:115]\n\t"
"ds_write_addtid_b32 v217 offset:27936\n\t"
"v_mul_u32_u24 v166, 1152, v159\n\t"
"v_mfma_f32_16x16x32_bf16 v[16:19], v[84:87], a[160:163], v[16:19]\n\t"
"ds_read_b64_tr_b16 a[200:201], v250 offset:21888\n\t"
"v_mul_u32_u24 v167, 68352, v158\n\t"
"v_add3_u32 v166, v166, v157, s61\n\t"
"v_add_u32 v166, v166, v167\n\t"
"v_xor_b32 v159, v159, v158\n\t"
"v_mfma_f32_16x16x32_bf16 v[32:35], v[88:91], a[144:147], v[128:131]\n\t"
"ds_read_b128 a[172:175], v224 offset:3840\n\t"
"v_lshl_add_u32 v166, v159, 6, v166\n\t"
"v_mfma_f32_16x16x32_bf16 v[32:35], v[92:95], a[160:163], v[32:35]\n\t"
"s_waitcnt lgkmcnt(15)\n\t"
"ds_read_b64_tr_b16 a[202:203], v250 offset:22400\n\t"
"v_xad_u32 v167, v158, 0, v155\n\t"
"v_mfma_f32_16x16x32_bf16 v[36:39], v[88:91], a[148:151], v[132:135]\n\t"
"v_lshl_add_u32 v168, v167, 7, v166\n\t"
"v_and_b32 v167, 2, v167\n\t"
"v_mfma_f32_16x16x32_bf16 v[36:39], v[92:95], a[164:167], v[36:39]\n\t"
"ds_read_b64_tr_b16 a[204:205], v251 offset:21888\n\t"
"v_lshl_add_u32 v168, v167, 4, v168\n\t"
"v_xor_b32 v168, 64, v168\n\t"
"v_mfma_f32_16x16x32_bf16 v[20:23], v[80:83], a[148:151], v[116:119]\n\t"
"ds_read_b64_tr_b16 a[206:207], v251 offset:22400\n\t"
"v_accvgpr_write_b32 a208, v168\n\t"
"v_mfma_f32_16x16x32_bf16 v[20:23], v[84:87], a[164:167], v[20:23]\n\t"
"v_xad_u32 v167, v158, 1, v155\n\t"
"v_lshl_add_u32 v168, v167, 7, v166\n\t"
"v_and_b32 v167, 2, v167\n\t"
"v_mfma_f32_16x16x32_bf16 v[4:7], v[72:75], a[148:151], v[100:103]\n\t"
"v_lshl_add_u32 v168, v167, 4, v168\n\t"
"v_accvgpr_write_b32 a209, v168\n\t"
"v_mfma_f32_16x16x32_bf16 v[4:7], v[76:79], a[164:167], v[4:7]\n\t"
"s_waitcnt lgkmcnt(3)\n\t"
"s_waitcnt vmcnt(2)\n\t"
"v_cmp_eq_u32 vcc, v190, v188\n\t"
"s_nop 4\n\t"
"s_cmp_eq_u64 vcc, 0\n\t"
"s_cbranch_scc1 Lgo_%=\n\t"
"s_sub_u32 s68, s68, 1\n\t"
"s_waitcnt vmcnt(0)\n\t"
"Lchk_%=:\n\t"
"v_cmp_eq_u32 vcc, v191, v188\n\t"
"s_nop 4\n\t"
"s_cmp_eq_u64 vcc, 0\n\t"
"s_cbranch_scc1 Lgo_%=\n\t"
"s_sub_u32 s68, s68, 1\n\t"
"s_cmp_eq_u32 s68, 0\n\t"
"s_cbranch_scc1 Lfb_%=\n\t"
"buffer_load_dword v191, v189, s[8:11], 0 offen sc1\n\t"
"s_waitcnt vmcnt(0)\n\t"
"s_branch Lchk_%=\n\t"
"Lgo_%=:\n\t"
"s_barrier\n\t"
"v_mfma_f32_16x16x32_bf16 v[8:11], v[72:75], a[152:155], v[104:107]\n\t"
"s_nop 0\n\t"
"ds_read_b128 a[144:147], v224 offset:16544\n\t"
"ds_read_b128 a[148:151], v224 offset:16800\n\t"
"v_mfma_f32_16x16x32_bf16 v[8:11], v[76:79], a[168:171], v[8:11]\n\t"
"s_mov_b32 s54, 98304\n\t"
"buffer_load_dwordx4 a[96:99], v235, s[8:11], s54 offen offset:0 sc1\n\t"
"buffer_load_dwordx4 a[100:103], v235, s[8:11], s54 offen offset:1024 sc1\n\t"
"v_mfma_f32_16x16x32_bf16 v[24:27], v[80:83], a[152:155], v[120:123]\n\t"
"ds_read_b128 a[160:163], v224 offset:18592\n\t"
"v_mbcnt_lo_u32_b32 v176, -1, 0\n\t"
"v_mfma_f32_16x16x32_bf16 v[24:27], v[84:87], a[168:171], v[24:27]\n\t"
"ds_read_b128 a[164:167], v224 offset:18848\n\t"
"buffer_load_dwordx4 a[104:107], v235, s[8:11], s54 offen offset:2048 sc1\n\t"
"buffer_load_dwordx4 a[108:111], v235, s[8:11], s54 offen offset:3072 sc1\n\t"
"v_mbcnt_hi_u32_b32 v176, -1, v176\n\t"
"v_and_b32 v154, 15, v176\n\t"
"v_mfma_f32_16x16x32_bf16 v[40:43], v[88:91], a[152:155], v[136:139]\n\t"
"v_lshrrev_b32 v155, 4, v176\n\t"
"v_bfe_u32 v156, v176, 2, 2\n\t"
"v_mfma_f32_16x16x32_bf16 v[40:43], v[92:95], a[168:171], v[40:43]\n\t"
"buffer_load_dwordx4 a[112:115], v252, s[8:11], s54 offen offset:0 sc1\n\t"
"v_mfma_f32_16x16x32_bf16 v[44:47], v[88:91], a[156:159], v[140:143]\n\t"
"v_and_b32 v157, 3, v176\n\t"
"v_lshlrev_b32 v157, 3, v157\n\t"
"s_add_u32 s60, s43, 33280\n\t"
"v_mfma_f32_16x16x32_bf16 v[44:47], v[92:95], a[172:175], v[44:47]\n\t"
"buffer_load_dwordx4 a[116:119], v252, s[8:11], s54 offen offset:1024 sc1\n\t"
"v_mfma_f32_16x16x32_bf16 v[28:31], v[80:83], a[156:159], v[124:127]\n\t"
"s_add_u32 s61, s26, 4294899008\n\t"
"ds_read_b128 a[168:171], v224 offset:19104\n\t"
"s_mul_i32 s62, s73, 96\n\t"
"v_mfma_f32_16x16x32_bf16 v[28:31], v[84:87], a[172:175], v[28:31]\n\t"
"s_mov_b32 s66, 172032\n\t"
"buffer_load_dwordx4 a[120:123], v235, s[8:11], s66 offen offset:0 sc1\n\t"
"v_mfma_f32_16x16x32_bf16 v[12:15], v[72:75], a[156:159], v[108:111]\n\t"
"s_add_u32 s62, s62, 75008\n\t"
"ds_read_b128 a[156:159], v224 offset:17312\n\t"
"v_min_u32 v158, 35, v176\n\t"
"v_mfma_f32_16x16x32_bf16 v[12:15], v[76:79], a[172:175], v[12:15]\n\t"
"ds_read_b128 a[172:175], v224 offset:19360\n\t"
"v_lshl_add_u32 v254, v158, 4, s60\n\t"
"v_add_u32 v255, 68352, v254\n\t"
"v_mfma_f32_16x16x32_bf16 v[192:195], a[216:219], a[176:179], 0\n\t"
"v_and_b32 v158, 1, v156\n\t"
"v_lshrrev_b32 v156, 3, v176\n\t"
"v_add_u32 v159, v156, v158\n\t"
"v_mfma_f32_16x16x32_bf16 v[196:199], a[216:219], a[180:183], 0\n\t"
"v_lshl_add_u32 v166, v159, 7, v157\n\t"
"v_and_b32 v159, 2, v159\n\t"
"v_lshl_add_u32 v166, v159, 4, v166\n\t"
"v_mfma_f32_16x16x32_bf16 v[200:203], a[216:219], a[184:187], 0\n\t"
"v_xad_u32 v167, v158, 1, v156\n\t"
"v_lshl_add_u32 v168, v167, 7, v157\n\t"
"v_mfma_f32_16x16x32_bf16 v[204:207], a[216:219], a[188:191], 0\n\t"
".p2align 3\n\t"
"buffer_load_dwordx4 a[124:127], v235, s[8:11], s66 offen offset:1024 sc1\n\t"
"v_pk_fma_f32 v[96:97], v[96:97], s[38:39], v[0:1] neg_lo:[1,0,1] neg_hi:[1,0,1]\n\t"
"v_pk_fma_f32 v[98:99], v[98:99], s[38:39], v[2:3] neg_lo:[1,0,1] neg_hi:[1,0,1]\n\t"
"v_pk_fma_f32 v[112:113], v[112:113], s[38:39], v[16:17] neg_lo:[1,0,1] neg_hi:[1,0,1]\n\t"
"v_pk_fma_f32 v[114:115], v[114:115], s[38:39], v[18:19] neg_lo:[1,0,1] neg_hi:[1,0,1]\n\t"
"v_pk_fma_f32 v[128:129], v[128:129], s[38:39], v[32:33] neg_lo:[1,0,1] neg_hi:[1,0,1]\n\t"
"v_pk_fma_f32 v[130:131], v[130:131], s[38:39], v[34:35] neg_lo:[1,0,1] neg_hi:[1,0,1]\n\t"
"v_pk_fma_f32 v[132:133], v[132:133], s[38:39], v[36:37] neg_lo:[1,0,1] neg_hi:[1,0,1]\n\t"
"v_pk_fma_f32 v[134:135], v[134:135], s[38:39], v[38:39] neg_lo:[1,0,1] neg_hi:[1,0,1]\n\t"
"v_pk_fma_f32 v[116:117], v[116:117], s[38:39], v[20:21] neg_lo:[1,0,1] neg_hi:[1,0,1]\n\t"
"v_pk_fma_f32 v[118:119], v[118:119], s[38:39], v[22:23] neg_lo:[1,0,1] neg_hi:[1,0,1]\n\t"
"v_pk_fma_f32 v[100:101], v[100:101], s[38:39], v[4:5] neg_lo:[1,0,1] neg_hi:[1,0,1]\n\t"
"v_pk_fma_f32 v[102:103], v[102:103], s[38:39], v[6:7] neg_lo:[1,0,1] neg_hi:[1,0,1]\n\t"
"buffer_load_dwordx4 a[128:131], v235, s[8:11], s66 offen offset:2048 sc1\n\t"
"v_pk_fma_f32 v[104:105], v[104:105], s[38:39], v[8:9] neg_lo:[1,0,1] neg_hi:[1,0,1]\n\t"
"v_pk_fma_f32 v[106:107], v[106:107], s[38:39], v[10:11] neg_lo:[1,0,1] neg_hi:[1,0,1]\n\t"
"v_pk_fma_f32 v[120:121], v[120:121], s[38:39], v[24:25] neg_lo:[1,0,1] neg_hi:[1,0,1]\n\t"
"v_pk_fma_f32 v[122:123], v[122:123], s[38:39], v[26:27] neg_lo:[1,0,1] neg_hi:[1,0,1]\n\t"
"v_pk_fma_f32 v[136:137], v[136:137], s[38:39], v[40:41] neg_lo:[1,0,1] neg_hi:[1,0,1]\n\t"
"v_pk_fma_f32 v[138:139], v[138:139], s[38:39], v[42:43] neg_lo:[1,0,1] neg_hi:[1,0,1]\n\t"
"v_pk_fma_f32 v[140:141], v[140:141], s[38:39], v[44:45] neg_lo:[1,0,1] neg_hi:[1,0,1]\n\t"
"v_pk_fma_f32 v[142:143], v[142:143], s[38:39], v[46:47] neg_lo:[1,0,1] neg_hi:[1,0,1]\n\t"
"v_pk_fma_f32 v[124:125], v[124:125], s[38:39], v[28:29] neg_lo:[1,0,1] neg_hi:[1,0,1]\n\t"
"v_pk_fma_f32 v[126:127], v[126:127], s[38:39], v[30:31] neg_lo:[1,0,1] neg_hi:[1,0,1]\n\t"
"v_pk_fma_f32 v[108:109], v[108:109], s[38:39], v[12:13] neg_lo:[1,0,1] neg_hi:[1,0,1]\n\t"
"v_pk_fma_f32 v[110:111], v[110:111], s[38:39], v[14:15] neg_lo:[1,0,1] neg_hi:[1,0,1]\n\t"
"v_mfma_f32_16x16x32_bf16 v[208:211], a[216:219], a[192:195], 0\n\t"
"ds_read_b128 a[152:155], v224 offset:17056\n\t"
"v_and_b32 v167, 2, v167\n\t"
"v_mfma_f32_16x16x32_bf16 v[212:215], a[216:219], a[196:199], 0\n\t"
"s_waitcnt lgkmcnt(10)\n\t"
"v_lshl_add_u32 v168, v167, 4, v168\n\t"
"v_sub_u32 v168, v168, v166\n\t"
"v_mfma_f32_16x16x32_bf16 v[216:219], a[216:219], a[200:203], 0\n\t"
"s_waitcnt lgkmcnt(8)\n\t"
"buffer_load_dwordx4 a[132:135], v235, s[8:11], s66 offen offset:3072 sc1\n\t"
"v_mfma_f32_16x16x32_bf16 v[220:223], a[216:219], a[204:207], 0\n\t"
"s_waitcnt lgkmcnt(5)\n\t"
"v_mul_u32_u24 v159, 68288, v158\n\t"
"v_add_u32 v166, v166, v159\n\t"
"v_mfma_f32_16x16x32_bf16 v[0:3], a[48:51], a[144:147], v[0:3]\n\t"
"v_lshl_add_u32 v168, v158, 7, v168\n\t"
"v_cvt_pk_bf16_f32 v192, v192, v196\n\t"
"v_mfma_f32_16x16x32_bf16 v[0:3], a[52:55], a[160:163], v[0:3]\n\t"
"buffer_load_dwordx4 a[136:139], v252, s[8:11], s66 offen offset:0 sc1\n\t"
"v_cvt_pk_bf16_f32 v194, v194, v198\n\t"
"v_cvt_pk_bf16_f32 v200, v200, v204\n\t"
"v_mfma_f32_16x16x32_bf16 v[16:19], a[56:59], a[144:147], v[16:19]\n\t"
"v_subrev_u32 v253, 64, v168\n\t"
"v_cvt_pk_bf16_f32 v201, v201, v205\n\t"
"v_mfma_f32_16x16x32_bf16 v[16:19], a[60:63], a[160:163], v[16:19]\n\t"
"v_cvt_pk_bf16_f32 v209, v209, v213\n\t"
"v_cvt_pk_bf16_f32 v210, v210, v214\n\t"
"v_mfma_f32_16x16x32_bf16 v[32:35], a[64:67], a[144:147], v[32:35]\n\t"
"ds_read_b128 a[144:147], v224 offset:17568\n\t"
"v_add_u32 v225, s61, v166\n\t"
"v_mfma_f32_16x16x32_bf16 v[32:35], a[68:71], a[160:163], v[32:35]\n\t"
"s_waitcnt lgkmcnt(5)\n\t"
"ds_read_b128 a[160:163], v224 offset:19616\n\t"
"buffer_load_dwordx4 a[140:143], v252, s[8:11], s66 offen offset:1024 sc1\n\t"
"v_mul_u32_u24 v158, 400, v154\n\t"
"v_cvt_pk_bf16_f32 v217, v217, v221\n\t"
"v_mfma_f32_16x16x32_bf16 v[36:39], a[64:67], a[148:151], v[36:39]\n\t"
"ds_write_addtid_b32 v192 offset:0\n\t"
"v_cvt_pk_bf16_f32 v193, v193, v197\n\t"
"v_mfma_f32_16x16x32_bf16 v[36:39], a[68:71], a[164:167], v[36:39]\n\t"
"ds_write_addtid_b32 v193 offset:8320\n\t"
"v_mul_u32_u24 v159, 24, v155\n\t"
"s_barrier\n\t"
"v_mfma_f32_16x16x32_bf16 v[20:23], a[56:59], a[148:151], v[20:23]\n\t"
"ds_read_b64_tr_b16 a[176:177], v248 offset:41472\n\t"
"v_add3_u32 v228, v158, v159, s62\n\t"
"v_mfma_f32_16x16x32_bf16 v[20:23], a[60:63], a[164:167], v[20:23]\n\t"
"ds_read_b64_tr_b16 a[178:179], v248 offset:41984\n\t"
"s_mov_b32 s69, 196608\n\t"
"buffer_load_dwordx4 a[0:3], v235, s[8:11], s69 offen offset:0 sc1\n\t"
"v_mfma_f32_16x16x32_bf16 v[4:7], a[48:51], a[148:151], v[4:7]\n\t"
"s_mul_i32 s60, s73, 3200\n\t"
"ds_write_addtid_b32 v209 offset:9344\n\t"
"v_cvt_pk_bf16_f32 v211, v211, v215\n\t"
"v_mfma_f32_16x16x32_bf16 v[4:7], a[52:55], a[164:167], v[4:7]\n\t"
"s_waitcnt lgkmcnt(7)\n\t"
"ds_read_b64_tr_b16 a[180:181], v249 offset:41472\n\t"
"buffer_load_dwordx4 a[4:7], v235, s[8:11], s69 offen offset:1024 sc1\n\t"
"v_cvt_pk_bf16_f32 v208, v208, v212\n\t"
"v_mfma_f32_16x16x32_bf16 v[8:11], a[48:51], a[152:155], v[8:11]\n\t"
"ds_read_b128 a[164:167], v224 offset:19872\n\t"
"v_cvt_pk_bf16_f32 v195, v195, v199\n\t"
"v_mfma_f32_16x16x32_bf16 v[8:11], a[52:55], a[168:171], v[8:11]\n\t"
"s_add_u32 s60, s60, 75008\n\t"
"ds_write_addtid_b32 v195 offset:12448\n\t"
"ds_read_b64_tr_b16 a[182:183], v249 offset:41984\n\t"
"ds_read_b64_tr_b16 a[184:185], v250 offset:41472\n\t"
"ds_read_b64_tr_b16 a[186:187], v250 offset:41984\n\t"
"v_mfma_f32_16x16x32_bf16 v[24:27], a[56:59], a[152:155], v[24:27]\n\t"
"ds_read_b128 a[148:151], v224 offset:17824\n\t"
"v_cvt_pk_bf16_f32 v202, v202, v206\n\t"
"v_mfma_f32_16x16x32_bf16 v[24:27], a[60:63], a[168:171], v[24:27]\n\t"
"ds_read_b64_tr_b16 a[188:189], v251 offset:41472\n\t"
"buffer_load_dwordx4 a[8:11], v235, s[8:11], s69 offen offset:2048 sc1\n\t"
"v_cvt_pk_bf16_f32 v203, v203, v207\n\t"
"v_mfma_f32_16x16x32_bf16 v[40:43], a[64:67], a[152:155], v[40:43]\n\t"
"s_and_b32 s61, s73, 2\n\t"
"ds_read_b64_tr_b16 a[190:191], v251 offset:41984\n\t"
"ds_read_b64_tr_b16 a[192:193], v248 offset:42624\n\t"
"v_mfma_f32_16x16x32_bf16 v[40:43], a[68:71], a[168:171], v[40:43]\n\t"
"buffer_load_dwordx4 a[12:15], v235, s[8:11], s69 offen offset:3072 sc1\n\t"
"v_cvt_pk_bf16_f32 v216, v216, v220\n\t"
"v_mfma_f32_16x16x32_bf16 v[44:47], a[64:67], a[156:159], v[44:47]\n\t"
"s_lshl_b32 s61, s61, 7\n\t"
"ds_write_addtid_b32 v203 offset:14496\n\t"
"ds_write_addtid_b32 v217 offset:11392\n\t"
"v_mfma_f32_16x16x32_bf16 v[44:47], a[68:71], a[172:175], v[44:47]\n\t"
"s_add_u32 s60, s60, s61\n\t"
"ds_write_addtid_b32 v200 offset:2048\n\t"
"ds_write_addtid_b32 v216 offset:3072\n\t"
"ds_read_b64_tr_b16 a[194:195], v248 offset:43136\n\t"
"v_mfma_f32_16x16x32_bf16 v[28:31], a[56:59], a[156:159], v[28:31]\n\t"
"ds_write_addtid_b32 v210 offset:5152\n\t"
"v_cvt_pk_bf16_f32 v218, v218, v222\n\t"
"v_mfma_f32_16x16x32_bf16 v[28:31], a[60:63], a[172:175], v[28:31]\n\t"
"buffer_load_dwordx4 a[16:19], v252, s[8:11], s69 offen offset:0 sc1\n\t"
"v_mfma_f32_16x16x32_bf16 v[12:15], a[48:51], a[156:159], v[12:15]\n\t"
"ds_read_b128 a[168:171], v224 offset:20128\n\t"
"v_cvt_pk_bf16_f32 v219, v219, v223\n\t"
"v_mfma_f32_16x16x32_bf16 v[12:15], a[52:55], a[172:175], v[12:15]\n\t"
"s_waitcnt lgkmcnt(15)\n\t"
".p2align 3\n\t"
"ds_write_addtid_b32 v194 offset:4128\n\t"
"ds_write_addtid_b32 v201 offset:10368\n\t"
"ds_write_addtid_b32 v202 offset:6176\n\t"
"ds_write_addtid_b32 v208 offset:1024\n\t"
"ds_write_addtid_b32 v211 offset:13472\n\t"
"ds_read_b64_tr_b16 a[196:197], v249 offset:42624\n\t"
"ds_read_b64_tr_b16 a[198:199], v249 offset:43136\n\t"
"ds_read_b64_tr_b16 a[200:201], v250 offset:42624\n\t"
"s_mul_i32 s61, s73, 393216\n\t"
"v_add_u32 v158, 0, v176\n\t"
"v_mul_u32_u24 v159, 0xab, v158\n\t"
"v_lshrrev_b32 v159, 12, v159\n\t"
"v_add_lshl_u32 v166, v158, v159, 4\n\t"
"v_add_u32 v229, s60, v166\n\t"
"v_mad_u32_u24 v158, v159, 24, v158\n\t"
"v_lshl_add_u32 v232, v158, 4, s61\n\t"
"v_mfma_f32_16x16x32_bf16 v[96:99], a[72:75], a[144:147], v[96:99]\n\t"
"ds_write_addtid_b32 v218 offset:7200\n\t"
"v_add_u32 v158, 64, v176\n\t"
"v_mfma_f32_16x16x32_bf16 v[96:99], a[76:79], a[160:163], v[96:99]\n\t"
"ds_read_b64_tr_b16 a[202:203], v250 offset:43136\n\t"
"buffer_load_dwordx4 a[20:23], v252, s[8:11], s69 offen offset:1024 sc1\n\t"
"v_mfma_f32_16x16x32_bf16 v[112:115], a[80:83], a[144:147], v[112:115]\n\t"
"ds_write_addtid_b32 v219 offset:15520\n\t"
"v_mul_u32_u24 v159, 0xab, v158\n\t"
"v_mfma_f32_16x16x32_bf16 v[112:115], a[84:87], a[160:163], v[112:115]\n\t"
"ds_read_b128 a[152:155], v224 offset:18080\n\t"
"s_mov_b32 s81, 270336\n\t"
"buffer_load_dwordx4 a[24:27], v235, s[8:11], s81 offen offset:0 sc1\n\t"
"v_lshrrev_b32 v159, 12, v159\n\t"
"v_mfma_f32_16x16x32_bf16 v[128:131], a[88:91], a[144:147], v[128:131]\n\t"
"ds_read_b128 a[156:159], v224 offset:18336\n\t"
"v_add_lshl_u32 v166, v158, v159, 4\n\t"
"v_mfma_f32_16x16x32_bf16 v[128:131], a[92:95], a[160:163], v[128:131]\n\t"
"s_waitcnt lgkmcnt(15)\n\t"
"ds_read_b128 a[172:175], v224 offset:20384\n\t"
"v_add_u32 v230, s60, v166\n\t"
"v_mfma_f32_16x16x32_bf16 v[132:135], a[88:91], a[148:151], v[132:135]\n\t"
"v_mad_u32_u24 v158, v159, 24, v158\n\t"
"v_lshl_add_u32 v233, v158, 4, s61\n\t"
"v_mfma_f32_16x16x32_bf16 v[132:135], a[92:95], a[164:167], v[132:135]\n\t"
"ds_read_b64_tr_b16 a[204:205], v251 offset:42624\n\t"
"buffer_load_dwordx4 a[28:31], v235, s[8:11], s81 offen offset:1024 sc1\n\t"
"v_add_u32 v158, 128, v176\n\t"
"v_mul_u32_u24 v159, 0xab, v158\n\t"
"v_mfma_f32_16x16x32_bf16 v[116:119], a[80:83], a[148:151], v[116:119]\n\t"
"ds_read_b64_tr_b16 a[206:207], v251 offset:43136\n\t"
"v_lshrrev_b32 v159, 12, v159\n\t"
"v_mfma_f32_16x16x32_bf16 v[116:119], a[84:87], a[164:167], v[116:119]\n\t"
"v_add_lshl_u32 v166, v158, v159, 4\n\t"
"v_add_u32 v231, s60, v166\n\t"
"v_mfma_f32_16x16x32_bf16 v[100:103], a[72:75], a[148:151], v[100:103]\n\t"
"v_mad_u32_u24 v158, v159, 24, v158\n\t"
"v_lshl_add_u32 v234, v158, 4, s61\n\t"
"v_mfma_f32_16x16x32_bf16 v[100:103], a[76:79], a[164:167], v[100:103]\n\t"
"buffer_load_dwordx4 a[32:35], v235, s[8:11], s81 offen offset:2048 sc1\n\t"
"s_waitcnt lgkmcnt(2)\n\t"
"s_add_u32 s71, s71, 1\n\t"
"v_mov_b32 v215, s71\n\t"
"s_mov_b64 exec, 1\n\t"
"buffer_store_dword v215, off, s[8:11], s70 sc1\n\t"
"s_mov_b64 exec, -1\n\t"
"s_barrier\n\t"
"v_mfma_f32_16x16x32_bf16 v[104:107], a[72:75], a[152:155], v[104:107]\n\t"
"ds_read_b128 a[144:147], v224 offset:0\n\t"
"v_add_u32 v248, 122368, v226\n\t"
"v_mfma_f32_16x16x32_bf16 v[104:107], a[76:79], a[168:171], v[104:107]\n\t"
"buffer_load_dwordx4 a[36:39], v235, s[8:11], s81 offen offset:3072 sc1\n\t"
"v_mfma_f32_16x16x32_bf16 v[120:123], a[80:83], a[152:155], v[120:123]\n\t"
"s_nop 0\n\t"
"ds_read_b128 a[160:163], v224 offset:2048\n\t"
"ds_read_b128 a[148:151], v224 offset:256\n\t"
"v_mfma_f32_16x16x32_bf16 v[120:123], a[84:87], a[168:171], v[120:123]\n\t"
"ds_read_b128 a[164:167], v224 offset:2304\n\t"
"v_accvgpr_read_b32 v172, a220\n\t"
"v_mfma_f32_16x16x32_bf16 v[136:139], a[88:91], a[152:155], v[136:139]\n\t"
"v_and_b32 v172, 0xff7fff7f, v172\n\t"
"v_xor_b32 v249, 32, v248\n\t"
"v_mfma_f32_16x16x32_bf16 v[136:139], a[92:95], a[168:171], v[136:139]\n\t"
"buffer_load_dwordx4 a[40:43], v252, s[8:11], s81 offen offset:0 sc1\n\t"
"v_mfma_f32_16x16x32_bf16 v[140:143], a[88:91], a[156:159], v[140:143]\n\t"
"v_accvgpr_write_b32 a252, v172\n\t"
"v_xor_b32 v250, 64, v248\n\t"
"v_mfma_f32_16x16x32_bf16 v[140:143], a[92:95], a[172:175], v[140:143]\n\t"
"v_accvgpr_read_b32 v173, a221\n\t"
"v_xor_b32 v251, 96, v248\n\t"
"v_mfma_f32_16x16x32_bf16 v[124:127], a[80:83], a[156:159], v[124:127]\n\t"
"ds_read_b128 a[168:171], v224 offset:2560\n\t"
"v_and_b32 v173, 0xff7fff7f, v173\n\t"
"v_mfma_f32_16x16x32_bf16 v[124:127], a[84:87], a[172:175], v[124:127]\n\t"
"buffer_load_dwordx4 a[44:47], v252, s[8:11], s81 offen offset:1024 sc1\n\t"
"v_mfma_f32_16x16x32_bf16 v[108:111], a[72:75], a[156:159], v[108:111]\n\t"
"ds_read_b128 a[152:155], v224 offset:512\n\t"
"v_accvgpr_write_b32 a253, v173\n\t"
"v_mfma_f32_16x16x32_bf16 v[108:111], a[76:79], a[172:175], v[108:111]\n\t"
"s_mov_b32 s57, 122880\n\t"
"buffer_load_dwordx4 a[48:51], v235, s[8:11], s57 offen offset:0 sc1\n\t"
"v_mfma_f32_16x16x32_bf16 v[192:195], a[216:219], a[176:179], 0\n\t"
"s_mov_b32 s40, 8192\n\t"
"ds_read_b128 a[156:159], v224 offset:768\n\t"
"ds_read_b64_tr_b16 a[178:179], v248 offset:128\n\t"
"v_mfma_f32_16x16x32_bf16 v[196:199], a[216:219], a[180:183], 0\n\t"
"ds_read_b128 a[172:175], v224 offset:2816\n\t"
"v_accvgpr_read_b32 v174, a222\n\t"
"v_mfma_f32_16x16x32_bf16 v[200:203], a[216:219], a[184:187], 0\n\t"
"s_mov_b32 s41, 16384\n\t"
"ds_read_b64_tr_b16 a[176:177], v248 offset:0\n\t"
"v_and_b32 v174, 0xff7fff7f, v174\n\t"
"v_mfma_f32_16x16x32_bf16 v[204:207], a[216:219], a[188:191], 0\n\t"
"buffer_load_dwordx4 a[52:55], v235, s[8:11], s57 offen offset:1024 sc1\n\t"
"v_mfma_f32_16x16x32_bf16 v[208:211], a[216:219], a[192:195], 0\n\t"
"v_accvgpr_write_b32 a254, v174\n\t"
"v_accvgpr_read_b32 v175, a223\n\t"
"v_mfma_f32_16x16x32_bf16 v[212:215], a[216:219], a[196:199], 0\n\t"
"v_and_b32 v175, 0xff7fff7f, v175\n\t"
"v_accvgpr_write_b32 a255, v175\n\t"
"v_mfma_f32_16x16x32_bf16 v[216:219], a[216:219], a[200:203], 0\n\t"
"s_waitcnt lgkmcnt(10)\n\t"
"buffer_load_dwordx4 a[56:59], v235, s[8:11], s57 offen offset:2048 sc1\n\t"
"v_mfma_f32_16x16x32_bf16 v[220:223], a[216:219], a[204:207], 0\n\t"
"s_waitcnt vmcnt(26) lgkmcnt(8)\n\t"
"ds_read_b64_tr_b16 a[180:181], v249 offset:256\n\t"
"v_cvt_pk_bf16_f32 v194, v194, v198\n\t"
"s_mov_b32 s42, 24576\n\t"
"v_mfma_f32_16x16x32_bf16 v[0:3], a[96:99], a[144:147], v[0:3]\n\t"
"ds_write_addtid_b32 v194 offset:20672\n\t"
"v_cvt_pk_bf16_f32 v195, v195, v199\n\t"
"v_mfma_f32_16x16x32_bf16 v[0:3], a[100:103], a[160:163], v[0:3]\n\t"
"s_waitcnt vmcnt(24)\n\t"
"buffer_load_dwordx4 a[60:63], v235, s[8:11], s57 offen offset:3072 sc1\n\t"
"v_mfma_f32_16x16x32_bf16 v[16:19], a[104:107], a[144:147], v[16:19]\n\t"
"ds_read_b64_tr_b16 a[186:187], v249 offset:128\n\t"
"v_cvt_pk_bf16_f32 v202, v202, v206\n\t"
"v_mfma_f32_16x16x32_bf16 v[16:19], a[108:111], a[160:163], v[16:19]\n\t"
"s_waitcnt vmcnt(23)\n\t"
"ds_read_b64_tr_b16 a[184:185], v249 offset:0\n\t"
"v_cvt_pk_bf16_f32 v203, v203, v207\n\t"
"v_mfma_f32_16x16x32_bf16 v[32:35], a[112:115], a[144:147], v[32:35]\n\t"
"s_mov_b32 s90, 0.5\n\t"
"ds_write_addtid_b32 v203 offset:31040\n\t"
"v_cvt_pk_bf16_f32 v201, v201, v205\n\t"
"v_mfma_f32_16x16x32_bf16 v[32:35], a[116:119], a[160:163], v[32:35]\n\t"
"s_waitcnt lgkmcnt(11)\n\t"
"ds_read_b128 a[144:147], v224 offset:1024\n\t"
"buffer_load_dwordx4 a[64:67], v252, s[8:11], s57 offen offset:0 sc1\n\t"
"v_mfma_f32_16x16x32_bf16 v[36:39], a[112:115], a[148:151], v[36:39]\n\t"
"v_cvt_pk_bf16_f32 v192, v192, v196\n\t"
"v_cvt_pk_bf16_f32 v200, v200, v204\n\t"
"v_mfma_f32_16x16x32_bf16 v[36:39], a[116:119], a[164:167], v[36:39]\n\t"
"ds_write_addtid_b32 v195 offset:28992\n\t"
"v_cvt_pk_bf16_f32 v193, v193, v197\n\t"
"v_mfma_f32_16x16x32_bf16 v[20:23], a[104:107], a[148:151], v[20:23]\n\t"
"ds_read_b128 a[160:163], v224 offset:3072\n\t"
"v_cvt_pk_bf16_f32 v210, v210, v214\n\t"
"v_mfma_f32_16x16x32_bf16 v[20:23], a[108:111], a[164:167], v[20:23]\n\t"
"buffer_load_dwordx4 a[68:71], v252, s[8:11], s57 offen offset:1024 sc1\n\t"
"v_mfma_f32_16x16x32_bf16 v[4:7], a[96:99], a[148:151], v[4:7]\n\t"
"s_mov_b32 s91, 0.5\n\t"
"ds_write_addtid_b32 v202 offset:22720\n\t"
"v_cvt_pk_bf16_f32 v219, v219, v223\n\t"
"v_mfma_f32_16x16x32_bf16 v[4:7], a[100:103], a[164:167], v[4:7]\n\t"
"s_waitcnt lgkmcnt(13)\n\t"
"s_mov_b32 s63, 147456\n\t"
"buffer_load_dwordx4 a[72:75], v235, s[8:11], s63 offen offset:0 sc1\n\t"
"v_mfma_f32_16x16x32_bf16 v[8:11], a[96:99], a[152:155], v[8:11]\n\t"
"ds_read_b128 a[164:167], v224 offset:3328\n\t"
"v_cvt_pk_bf16_f32 v208, v208, v212\n\t"
"v_mfma_f32_16x16x32_bf16 v[8:11], a[100:103], a[168:171], v[8:11]\n\t"
"ds_write_addtid_b32 v210 offset:21696\n\t"
"v_cvt_pk_bf16_f32 v211, v211, v215\n\t"
"v_mfma_f32_16x16x32_bf16 v[24:27], a[104:107], a[152:155], v[24:27]\n\t"
"s_add_u32 s84, s8, 18432\n\t"
"s_addc_u32 s85, s9, 0\n\t"
"ds_read_b128 a[148:151], v224 offset:1280\n\t"
"v_mfma_f32_16x16x32_bf16 v[24:27], a[108:111], a[168:171], v[24:27]\n\t"
"buffer_load_dwordx4 a[76:79], v235, s[8:11], s63 offen offset:1024 sc1\n\t"
"v_mfma_f32_16x16x32_bf16 v[40:43], a[112:115], a[152:155], v[40:43]\n\t"
"ds_write_addtid_b32 v200 offset:18592\n\t"
"v_cvt_pk_bf16_f32 v209, v209, v213\n\t"
"v_mfma_f32_16x16x32_bf16 v[40:43], a[116:119], a[168:171], v[40:43]\n\t"
"s_waitcnt lgkmcnt(14)\n\t"
"ds_write_addtid_b32 v201 offset:26912\n\t"
"v_cvt_pk_bf16_f32 v216, v216, v220\n\t"
"s_mov_b64 s[86:87], s[10:11]\n\t"
"v_mfma_f32_16x16x32_bf16 v[44:47], a[112:115], a[156:159], v[44:47]\n\t"
"s_xor_b64 s[88:89], s[84:85], s[8:9]\n\t"
"ds_write_addtid_b32 v193 offset:24864\n\t"
"ds_write_addtid_b32 v209 offset:25888\n\t"
"v_mfma_f32_16x16x32_bf16 v[44:47], a[116:119], a[172:175], v[44:47]\n\t"
"buffer_load_dwordx4 a[80:83], v235, s[8:11], s63 offen offset:2048 sc1\n\t"
"v_mfma_f32_16x16x32_bf16 v[28:31], a[104:107], a[156:159], v[28:31]\n\t"
"ds_write_addtid_b32 v208 offset:17568\n\t"
"v_cvt_pk_bf16_f32 v217, v217, v221\n\t"
"v_mfma_f32_16x16x32_bf16 v[28:31], a[108:111], a[172:175], v[28:31]\n\t"
"buffer_load_dwordx4 a[84:87], v235, s[8:11], s63 offen offset:3072 sc1\n\t"
"v_mfma_f32_16x16x32_bf16 v[12:15], a[96:99], a[156:159], v[12:15]\n\t"
"s_mov_b32 s46, 32768\n\t"
"ds_write_addtid_b32 v192 offset:16544\n\t"
"ds_write_addtid_b32 v217 offset:27936\n\t"
"v_mfma_f32_16x16x32_bf16 v[12:15], a[100:103], a[172:175], v[12:15]\n\t"
"s_waitcnt vmcnt(27) lgkmcnt(11)\n\t"
"ds_read_b128 a[156:159], v224 offset:1792\n\t"
"ds_write_addtid_b32 v219 offset:32064\n\t"
"s_add_u32 s25, s24, 0xffeb0000\n\t"
"v_mfma_f32_16x16x32_bf16 v[96:99], a[120:123], a[144:147], v[96:99]\n\t"
"ds_write_addtid_b32 v211 offset:30016\n\t"
"v_cvt_pk_bf16_f32 v218, v218, v222\n\t"
"v_mfma_f32_16x16x32_bf16 v[96:99], a[124:127], a[160:163], v[96:99]\n\t"
"s_waitcnt vmcnt(25)\n\t"
"buffer_load_dwordx4 a[88:91], v252, s[8:11], s63 offen offset:0 sc1\n\t"
"v_mfma_f32_16x16x32_bf16 v[112:115], a[128:131], a[144:147], v[112:115]\n\t"
"s_add_u32 s27, s24, 0xffe80000\n\t"
"ds_write_addtid_b32 v216 offset:19616\n\t"
"ds_write_addtid_b32 v218 offset:23744\n\t"
"v_mfma_f32_16x16x32_bf16 v[112:115], a[132:135], a[160:163], v[112:115]\n\t"
"s_waitcnt vmcnt(24)\n\t"
"ds_read_b128 a[152:155], v224 offset:1536\n\t"
"ds_read_b128 a[172:175], v224 offset:3840\n\t"
"s_add_u32 s20, s24, 0xffd30000\n\t"
"v_mfma_f32_16x16x32_bf16 v[128:131], a[136:139], a[144:147], v[128:131]\n\t"
"s_add_u32 m0, s26, 56192\n\t"
"ds_read_b128 a[168:171], v224 offset:3584\n\t"
"ds_read_b64_tr_b16 a[188:189], v248 offset:256\n\t"
"s_mov_b32 s49, 57344\n\t"
"v_mfma_f32_16x16x32_bf16 v[128:131], a[140:143], a[160:163], v[128:131]\n\t"
"s_waitcnt lgkmcnt(15)\n\t"
"buffer_load_dwordx4 a[92:95], v252, s[8:11], s63 offen offset:1024 sc1\n\t"
"v_mfma_f32_16x16x32_bf16 v[132:135], a[136:139], a[148:151], v[132:135]\n\t"
"s_mov_b32 s50, 65536\n\t"
"ds_read_b64_tr_b16 a[194:195], v250 offset:128\n\t"
"ds_read_b64_tr_b16 a[192:193], v250 offset:0\n\t"
"v_mfma_f32_16x16x32_bf16 v[132:135], a[140:143], a[164:167], v[132:135]\n\t"
"s_mov_b32 s47, 40960\n\t"
"ds_read_b64_tr_b16 a[196:197], v251 offset:256\n\t"
"ds_read_b64_tr_b16 a[202:203], v251 offset:128\n\t"
"v_mfma_f32_16x16x32_bf16 v[116:119], a[128:131], a[148:151], v[116:119]\n\t"
"s_add_u32 s24, s24, 0xffd00000\n\t"
"ds_read_b64_tr_b16 a[200:201], v251 offset:0\n\t"
"ds_read_b64_tr_b16 a[204:205], v250 offset:256\n\t"
"v_mfma_f32_16x16x32_bf16 v[116:119], a[132:135], a[164:167], v[116:119]\n\t"
"buffer_load_dwordx4 v238, s[12:15], s22 offen offset:2176 nt lds\n\t"
"v_mfma_f32_16x16x32_bf16 v[100:103], a[120:123], a[148:151], v[100:103]\n\t"
"s_lshl_b32 m0, s73, 8\n\t"
"s_mov_b32 s48, 49152\n\t"
"v_mfma_f32_16x16x32_bf16 v[100:103], a[124:127], a[164:167], v[100:103]\n\t"
"s_waitcnt lgkmcnt(7)\n\t"
"s_barrier\n\t"
"v_mfma_f32_16x16x32_bf16 v[104:107], a[120:123], a[152:155], v[104:107]\n\t"
"s_add_u32 m0, s26, 56192\n\t"
"ds_read_b128 a[144:147], v224 offset:16544\n\t"
"v_mfma_f32_16x16x32_bf16 v[104:107], a[124:127], a[168:171], v[104:107]\n\t"
"ds_read_b128 a[148:151], v224 offset:16800\n\t"
"v_mfma_f32_16x16x32_bf16 v[120:123], a[128:131], a[152:155], v[120:123]\n\t"
"ds_read_b128 a[160:163], v224 offset:18592\n\t"
"v_mfma_f32_16x16x32_bf16 v[120:123], a[132:135], a[168:171], v[120:123]\n\t"
"ds_read_b128 a[164:167], v224 offset:18848\n\t"
"v_mfma_f32_16x16x32_bf16 v[136:139], a[136:139], a[152:155], v[136:139]\n\t"
"v_mfma_f32_16x16x32_bf16 v[136:139], a[140:143], a[168:171], v[136:139]\n\t"
"v_mfma_f32_16x16x32_bf16 v[140:143], a[136:139], a[156:159], v[140:143]\n\t"
"v_mfma_f32_16x16x32_bf16 v[140:143], a[140:143], a[172:175], v[140:143]\n\t"
"v_mfma_f32_16x16x32_bf16 v[124:127], a[128:131], a[156:159], v[124:127]\n\t"
"ds_read_b128 a[152:155], v224 offset:17056\n\t"
"v_mfma_f32_16x16x32_bf16 v[124:127], a[132:135], a[172:175], v[124:127]\n\t"
"buffer_load_dwordx4 v239, s[12:15], s22 offen offset:3200 nt lds\n\t"
"v_mfma_f32_16x16x32_bf16 v[108:111], a[120:123], a[156:159], v[108:111]\n\t"
"v_mfma_f32_16x16x32_bf16 v[108:111], a[124:127], a[172:175], v[108:111]\n\t"
"s_waitcnt lgkmcnt(5)\n\t"
"ds_read_b128 a[168:171], v224 offset:19104\n\t"
"v_mfma_f32_4x4x4_16b_bf16 v[200:203], a[252:253], a[178:179], 0\n\t"
"ds_read_b128 a[156:159], v224 offset:17312\n\t"
"v_mfma_f32_4x4x4_16b_bf16 v[204:207], a[252:253], a[186:187], 0\n\t"
"ds_read_b64_tr_b16 a[178:179], v248 offset:20864\n\t"
"v_mfma_f32_4x4x4_16b_bf16 v[216:219], a[252:253], a[194:195], 0\n\t"
"v_mfma_f32_4x4x4_16b_bf16 v[220:223], a[252:253], a[202:203], 0\n\t"
"ds_read_b64_tr_b16 a[186:187], v249 offset:20864\n\t"
"v_mfma_f32_4x4x4_16b_bf16 v[192:195], a[254:255], a[176:177], v[200:203]\n\t"
"ds_read_b64_tr_b16 a[194:195], v250 offset:20864\n\t"
"v_mfma_f32_4x4x4_16b_bf16 v[196:199], a[254:255], a[184:185], v[204:207]\n\t"
"ds_read_b128 a[172:175], v224 offset:19360\n\t"
"v_mfma_f32_4x4x4_16b_bf16 v[200:203], a[254:255], a[180:181], v[200:203]\n\t"
"v_mfma_f32_4x4x4_16b_bf16 v[204:207], a[254:255], a[188:189], v[204:207]\n\t"
"ds_read_b64_tr_b16 a[202:203], v251 offset:20864\n\t"
"v_mfma_f32_4x4x4_16b_bf16 v[208:211], a[254:255], a[192:193], v[216:219]\n\t"
"ds_read_b64_tr_b16 a[176:177], v248 offset:20736\n\t"
"v_mfma_f32_4x4x4_16b_bf16 v[212:215], a[254:255], a[200:201], v[220:223]\n\t"
"ds_read_b64_tr_b16 a[184:185], v249 offset:20736\n\t"
"v_mfma_f32_4x4x4_16b_bf16 v[216:219], a[254:255], a[196:197], v[216:219]\n\t"
"ds_read_b64_tr_b16 a[180:181], v249 offset:20992\n\t"
"v_mfma_f32_4x4x4_16b_bf16 v[220:223], a[254:255], a[204:205], v[220:223]\n\t"
"s_waitcnt vmcnt(25) lgkmcnt(12)\n\t"
"s_mov_b64 exec, s[36:37]\n\t"
"buffer_load_dwordx4 v240, s[12:15], s22 offen offset:3712 nt lds\n\t"
"s_mov_b64 exec, -1\n\t"
"v_mfma_f32_16x16x32_bf16 v[0:3], a[0:3], a[144:147], v[0:3]\n\t"
"s_lshl_b32 m0, s73, 8\n\t"
"v_cvt_pk_bf16_f32 v193, -v193, -v197\n\t"
"v_cvt_pk_bf16_f32 v194, -v194, -v198\n\t"
"v_mfma_f32_16x16x32_bf16 v[0:3], a[4:7], a[160:163], v[0:3]\n\t"
"s_waitcnt vmcnt(24)\n\t"
"ds_read_b64_tr_b16 a[188:189], v248 offset:20992\n\t"
"v_cvt_pk_bf16_f32 v195, -v195, -v199\n\t"
"v_mfma_f32_16x16x32_bf16 v[16:19], a[8:11], a[144:147], v[16:19]\n\t"
"ds_write_addtid_b32 v193 offset:4128\n\t"
"v_cvt_pk_bf16_f32 v203, v203, v207\n\t"
"v_mfma_f32_16x16x32_bf16 v[16:19], a[12:15], a[160:163], v[16:19]\n\t"
"s_waitcnt vmcnt(22)\n\t"
"ds_write_addtid_b32 v195 offset:5152\n\t"
"v_cvt_pk_bf16_f32 v192, -v192, -v196\n\t"
"s_mov_b32 s51, 73728\n\t"
"v_mfma_f32_16x16x32_bf16 v[32:35], a[16:19], a[144:147], v[32:35]\n\t"
"ds_write_addtid_b32 v194 offset:1024\n\t"
"v_cvt_pk_bf16_f32 v202, v202, v206\n\t"
"v_mfma_f32_16x16x32_bf16 v[32:35], a[20:23], a[160:163], v[32:35]\n\t"
"s_waitcnt lgkmcnt(15)\n\t"
"ds_read_b128 a[160:163], v224 offset:19616\n\t"
"v_cvt_pk_bf16_f32 v208, -v208, -v212\n\t"
"v_mfma_f32_16x16x32_bf16 v[36:39], a[16:19], a[148:151], v[36:39]\n\t"
"ds_write_addtid_b32 v203 offset:13472\n\t"
"v_cvt_pk_bf16_f32 v209, -v209, -v213\n\t"
"v_mfma_f32_16x16x32_bf16 v[36:39], a[20:23], a[164:167], v[36:39]\n\t"
"s_mov_b32 s75, 221184\n\t"
"buffer_load_dwordx4 a[96:99], v235, s[8:11], s75 offen offset:0 sc1\n\t"
"v_mfma_f32_16x16x32_bf16 v[20:23], a[8:11], a[148:151], v[20:23]\n\t"
"ds_read_b128 a[144:147], v224 offset:17568\n\t"
"v_mfma_f32_16x16x32_bf16 v[20:23], a[12:15], a[164:167], v[20:23]\n\t"
"v_cvt_pk_bf16_f32 v211, -v211, -v215\n\t"
"v_cvt_pk_bf16_f32 v218, v218, v222\n\t"
"v_mfma_f32_16x16x32_bf16 v[4:7], a[0:3], a[148:151], v[4:7]\n\t"
"ds_write_addtid_b32 v211 offset:7200\n\t"
"v_cvt_pk_bf16_f32 v201, v201, v205\n\t"
"v_mfma_f32_16x16x32_bf16 v[4:7], a[4:7], a[164:167], v[4:7]\n\t"
"s_waitcnt lgkmcnt(15)\n\t"
"buffer_load_dwordx4 a[100:103], v235, s[8:11], s75 offen offset:1024 sc1\n\t"
"v_mfma_f32_16x16x32_bf16 v[8:11], a[0:3], a[152:155], v[8:11]\n\t"
"ds_read_b128 a[148:151], v224 offset:17824\n\t"
"v_mfma_f32_16x16x32_bf16 v[8:11], a[4:7], a[168:171], v[8:11]\n\t"
"buffer_load_dwordx4 a[104:107], v235, s[8:11], s75 offen offset:2048 sc1\n\t"
"v_mfma_f32_16x16x32_bf16 v[24:27], a[8:11], a[152:155], v[24:27]\n\t"
"s_mov_b32 s55, 106496\n\t"
"ds_read_b128 a[164:167], v224 offset:19872\n\t"
"v_cvt_pk_bf16_f32 v217, v217, v221\n\t"
"v_mfma_f32_16x16x32_bf16 v[24:27], a[12:15], a[168:171], v[24:27]\n\t"
"s_mov_b32 s56, 114688\n\t"
"ds_write_addtid_b32 v202 offset:9344\n\t"
"ds_write_addtid_b32 v209 offset:6176\n\t"
"v_mfma_f32_16x16x32_bf16 v[40:43], a[16:19], a[152:155], v[40:43]\n\t"
"ds_write_addtid_b32 v217 offset:14496\n\t"
"v_cvt_pk_bf16_f32 v200, v200, v204\n\t"
"v_mfma_f32_16x16x32_bf16 v[40:43], a[20:23], a[168:171], v[40:43]\n\t"
"s_waitcnt lgkmcnt(15)\n\t"
"buffer_load_dwordx4 a[108:111], v235, s[8:11], s75 offen offset:3072 sc1\n\t"
"v_mfma_f32_16x16x32_bf16 v[44:47], a[16:19], a[156:159], v[44:47]\n\t"
"ds_write_addtid_b32 v208 offset:2048\n\t"
"v_cvt_pk_bf16_f32 v219, v219, v223\n\t"
"v_mfma_f32_16x16x32_bf16 v[44:47], a[20:23], a[172:175], v[44:47]\n\t"
"s_mov_b32 s58, 131072\n\t"
"ds_write_addtid_b32 v200 offset:8320\n\t"
"ds_write_addtid_b32 v219 offset:15520\n\t"
"v_mfma_f32_16x16x32_bf16 v[28:31], a[8:11], a[156:159], v[28:31]\n\t"
"ds_write_addtid_b32 v192 offset:0\n\t"
"v_cvt_pk_bf16_f32 v210, -v210, -v214\n\t"
"v_mfma_f32_16x16x32_bf16 v[28:31], a[12:15], a[172:175], v[28:31]\n\t"
"buffer_load_dwordx4 a[112:115], v252, s[8:11], s75 offen offset:0 sc1\n\t"
"v_mfma_f32_16x16x32_bf16 v[12:15], a[0:3], a[156:159], v[12:15]\n\t"
"ds_read_b128 a[168:171], v224 offset:20128\n\t"
"v_cvt_pk_bf16_f32 v216, v216, v220\n\t"
"v_mfma_f32_16x16x32_bf16 v[12:15], a[4:7], a[172:175], v[12:15]\n\t"
"s_waitcnt vmcnt(25) lgkmcnt(11)\n\t"
"ds_read_b128 a[156:159], v224 offset:18336\n\t"
"v_mfma_f32_16x16x32_bf16 v[96:99], a[24:27], a[144:147], v[96:99]\n\t"
"s_mov_b32 s59, 139264\n\t"
"ds_write_addtid_b32 v210 offset:3072\n\t"
"ds_write_addtid_b32 v216 offset:10368\n\t"
"v_mfma_f32_16x16x32_bf16 v[96:99], a[28:31], a[160:163], v[96:99]\n\t"
"s_waitcnt vmcnt(22)\n\t"
"buffer_load_dwordx4 a[116:119], v252, s[8:11], s75 offen offset:1024 sc1\n\t"
"v_mfma_f32_16x16x32_bf16 v[112:115], a[32:35], a[144:147], v[112:115]\n\t"
"s_mov_b32 s52, 81920\n\t"
"ds_write_addtid_b32 v201 offset:12448\n\t"
"ds_write_addtid_b32 v218 offset:11392\n\t"
"v_mfma_f32_16x16x32_bf16 v[112:115], a[36:39], a[160:163], v[112:115]\n\t"
"s_waitcnt vmcnt(21)\n\t"
"ds_read_b128 a[172:175], v224 offset:20384\n\t"
"ds_read_b64_tr_b16 a[192:193], v250 offset:20736\n\t"
"s_mov_b32 s53, 90112\n\t"
"v_mfma_f32_16x16x32_bf16 v[128:131], a[40:43], a[144:147], v[128:131]\n\t"
"ds_read_b128 a[152:155], v224 offset:18080\n\t"
"v_mfma_f32_16x16x32_bf16 v[128:131], a[44:47], a[160:163], v[128:131]\n\t"
"s_waitcnt lgkmcnt(15)\n\t"
"s_mov_b32 s78, 245760\n\t"
"buffer_load_dwordx4 a[120:123], v235, s[8:11], s78 offen offset:0 sc1\n\t"
"v_mfma_f32_16x16x32_bf16 v[132:135], a[40:43], a[148:151], v[132:135]\n\t"
"ds_read_b64_tr_b16 a[200:201], v251 offset:20736\n\t"
"v_mfma_f32_16x16x32_bf16 v[132:135], a[44:47], a[164:167], v[132:135]\n\t"
"ds_read_b64_tr_b16 a[196:197], v251 offset:20992\n\t"
"v_mfma_f32_16x16x32_bf16 v[116:119], a[32:35], a[148:151], v[116:119]\n\t"
"ds_read_b64_tr_b16 a[204:205], v250 offset:20992\n\t"
"v_mfma_f32_16x16x32_bf16 v[116:119], a[36:39], a[164:167], v[116:119]\n\t"
"buffer_load_dwordx4 a[124:127], v235, s[8:11], s78 offen offset:1024 sc1\n\t"
"v_mfma_f32_16x16x32_bf16 v[100:103], a[24:27], a[148:151], v[100:103]\n\t"
"v_mfma_f32_16x16x32_bf16 v[100:103], a[28:31], a[164:167], v[100:103]\n\t"
"buffer_load_dwordx4 a[128:131], v235, s[8:11], s78 offen offset:2048 sc1\n\t"
"s_waitcnt lgkmcnt(3)\n\t"
"s_add_u32 s71, s71, 1\n\t"
"v_mov_b32 v215, s71\n\t"
"s_mov_b64 exec, 1\n\t"
"buffer_store_dword v215, off, s[8:11], s70 sc1\n\t"
"s_mov_b64 exec, -1\n\t"
"s_barrier\n\t"
"v_mfma_f32_16x16x32_bf16 v[104:107], a[24:27], a[152:155], v[104:107]\n\t"
"ds_read_b128 a[160:163], v224 offset:2048\n\t"
"v_mfma_f32_16x16x32_bf16 v[104:107], a[28:31], a[168:171], v[104:107]\n\t"
"ds_read_b128 a[144:147], v224 offset:0\n\t"
"v_mfma_f32_16x16x32_bf16 v[120:123], a[32:35], a[152:155], v[120:123]\n\t"
"ds_read_b128 a[148:151], v224 offset:256\n\t"
"v_mfma_f32_16x16x32_bf16 v[120:123], a[36:39], a[168:171], v[120:123]\n\t"
"ds_read_b128 a[164:167], v224 offset:2304\n\t"
"v_mfma_f32_16x16x32_bf16 v[136:139], a[40:43], a[152:155], v[136:139]\n\t"
"ds_read_b128 a[152:155], v224 offset:512\n\t"
"v_mfma_f32_16x16x32_bf16 v[136:139], a[44:47], a[168:171], v[136:139]\n\t"
"buffer_load_dwordx4 a[132:135], v235, s[8:11], s78 offen offset:3072 sc1\n\t"
"v_mfma_f32_16x16x32_bf16 v[140:143], a[40:43], a[156:159], v[140:143]\n\t"
"v_mfma_f32_16x16x32_bf16 v[140:143], a[44:47], a[172:175], v[140:143]\n\t"
"buffer_load_dwordx4 a[136:139], v252, s[8:11], s78 offen offset:0 sc1\n\t"
"v_mfma_f32_16x16x32_bf16 v[124:127], a[32:35], a[156:159], v[124:127]\n\t"
"v_mfma_f32_16x16x32_bf16 v[124:127], a[36:39], a[172:175], v[124:127]\n\t"
"ds_read_b128 a[168:171], v224 offset:2560\n\t"
"v_mfma_f32_16x16x32_bf16 v[108:111], a[24:27], a[156:159], v[108:111]\n\t"
"v_mfma_f32_16x16x32_bf16 v[108:111], a[28:31], a[172:175], v[108:111]\n\t"
"s_waitcnt lgkmcnt(6)\n\t"
"buffer_load_dwordx4 a[140:143], v252, s[8:11], s78 offen offset:1024 sc1\n\t"
"v_mfma_f32_4x4x4_16b_bf16 v[200:203], a[252:253], a[178:179], 0\n\t"
"ds_read_b128 a[172:175], v224 offset:2816\n\t"
"v_mfma_f32_4x4x4_16b_bf16 v[204:207], a[252:253], a[186:187], 0\n\t"
"buffer_load_dwordx4 a[248:251], v247, s[12:15], 0 offen offset:256 nt\n\t"
"v_mfma_f32_4x4x4_16b_bf16 v[216:219], a[252:253], a[194:195], 0\n\t"
"v_accvgpr_read_b32 v248, a208\n\t"
"v_mfma_f32_4x4x4_16b_bf16 v[220:223], a[252:253], a[202:203], 0\n\t"
".p2align 3\n\t"
"v_pk_add_f32 v[96:97], v[0:1], v[96:97] neg_lo:[0,1] neg_hi:[0,1]\n\t"
"v_pk_add_f32 v[98:99], v[2:3], v[98:99] neg_lo:[0,1] neg_hi:[0,1]\n\t"
"v_pk_fma_f32 v[0:1], v[96:97], s[90:91], v[0:1] neg_lo:[1,0,0] neg_hi:[1,0,0]\n\t"
"v_pk_fma_f32 v[2:3], v[98:99], s[90:91], v[2:3] neg_lo:[1,0,0] neg_hi:[1,0,0]\n\t"
"v_pk_add_f32 v[112:113], v[16:17], v[112:113] neg_lo:[0,1] neg_hi:[0,1]\n\t"
"v_pk_add_f32 v[114:115], v[18:19], v[114:115] neg_lo:[0,1] neg_hi:[0,1]\n\t"
"v_pk_fma_f32 v[16:17], v[112:113], s[90:91], v[16:17] neg_lo:[1,0,0] neg_hi:[1,0,0]\n\t"
"v_pk_fma_f32 v[18:19], v[114:115], s[90:91], v[18:19] neg_lo:[1,0,0] neg_hi:[1,0,0]\n\t"
"v_pk_add_f32 v[128:129], v[32:33], v[128:129] neg_lo:[0,1] neg_hi:[0,1]\n\t"
"v_pk_add_f32 v[130:131], v[34:35], v[130:131] neg_lo:[0,1] neg_hi:[0,1]\n\t"
"v_pk_fma_f32 v[32:33], v[128:129], s[90:91], v[32:33] neg_lo:[1,0,0] neg_hi:[1,0,0]\n\t"
"v_pk_fma_f32 v[34:35], v[130:131], s[90:91], v[34:35] neg_lo:[1,0,0] neg_hi:[1,0,0]\n\t"
"v_pk_add_f32 v[132:133], v[36:37], v[132:133] neg_lo:[0,1] neg_hi:[0,1]\n\t"
"v_pk_add_f32 v[134:135], v[38:39], v[134:135] neg_lo:[0,1] neg_hi:[0,1]\n\t"
"v_pk_fma_f32 v[36:37], v[132:133], s[90:91], v[36:37] neg_lo:[1,0,0] neg_hi:[1,0,0]\n\t"
"v_pk_fma_f32 v[38:39], v[134:135], s[90:91], v[38:39] neg_lo:[1,0,0] neg_hi:[1,0,0]\n\t"
"v_pk_add_f32 v[116:117], v[20:21], v[116:117] neg_lo:[0,1] neg_hi:[0,1]\n\t"
"v_pk_add_f32 v[118:119], v[22:23], v[118:119] neg_lo:[0,1] neg_hi:[0,1]\n\t"
"v_pk_fma_f32 v[20:21], v[116:117], s[90:91], v[20:21] neg_lo:[1,0,0] neg_hi:[1,0,0]\n\t"
"v_pk_fma_f32 v[22:23], v[118:119], s[90:91], v[22:23] neg_lo:[1,0,0] neg_hi:[1,0,0]\n\t"
"v_pk_add_f32 v[100:101], v[4:5], v[100:101] neg_lo:[0,1] neg_hi:[0,1]\n\t"
"v_pk_add_f32 v[102:103], v[6:7], v[102:103] neg_lo:[0,1] neg_hi:[0,1]\n\t"
"v_pk_fma_f32 v[4:5], v[100:101], s[90:91], v[4:5] neg_lo:[1,0,0] neg_hi:[1,0,0]\n\t"
"v_pk_fma_f32 v[6:7], v[102:103], s[90:91], v[6:7] neg_lo:[1,0,0] neg_hi:[1,0,0]\n\t"
"v_pk_add_f32 v[104:105], v[8:9], v[104:105] neg_lo:[0,1] neg_hi:[0,1]\n\t"
"v_pk_add_f32 v[106:107], v[10:11], v[106:107] neg_lo:[0,1] neg_hi:[0,1]\n\t"
"v_pk_fma_f32 v[8:9], v[104:105], s[90:91], v[8:9] neg_lo:[1,0,0] neg_hi:[1,0,0]\n\t"
"v_pk_fma_f32 v[10:11], v[106:107], s[90:91], v[10:11] neg_lo:[1,0,0] neg_hi:[1,0,0]\n\t"
"v_pk_add_f32 v[120:121], v[24:25], v[120:121] neg_lo:[0,1] neg_hi:[0,1]\n\t"
"v_pk_add_f32 v[122:123], v[26:27], v[122:123] neg_lo:[0,1] neg_hi:[0,1]\n\t"
"v_pk_fma_f32 v[24:25], v[120:121], s[90:91], v[24:25] neg_lo:[1,0,0] neg_hi:[1,0,0]\n\t"
"v_pk_fma_f32 v[26:27], v[122:123], s[90:91], v[26:27] neg_lo:[1,0,0] neg_hi:[1,0,0]\n\t"
"v_pk_add_f32 v[136:137], v[40:41], v[136:137] neg_lo:[0,1] neg_hi:[0,1]\n\t"
"v_pk_add_f32 v[138:139], v[42:43], v[138:139] neg_lo:[0,1] neg_hi:[0,1]\n\t"
"v_pk_fma_f32 v[40:41], v[136:137], s[90:91], v[40:41] neg_lo:[1,0,0] neg_hi:[1,0,0]\n\t"
"v_pk_fma_f32 v[42:43], v[138:139], s[90:91], v[42:43] neg_lo:[1,0,0] neg_hi:[1,0,0]\n\t"
"v_pk_add_f32 v[140:141], v[44:45], v[140:141] neg_lo:[0,1] neg_hi:[0,1]\n\t"
"v_pk_add_f32 v[142:143], v[46:47], v[142:143] neg_lo:[0,1] neg_hi:[0,1]\n\t"
"v_pk_fma_f32 v[44:45], v[140:141], s[90:91], v[44:45] neg_lo:[1,0,0] neg_hi:[1,0,0]\n\t"
"v_pk_fma_f32 v[46:47], v[142:143], s[90:91], v[46:47] neg_lo:[1,0,0] neg_hi:[1,0,0]\n\t"
"v_pk_add_f32 v[124:125], v[28:29], v[124:125] neg_lo:[0,1] neg_hi:[0,1]\n\t"
"v_pk_add_f32 v[126:127], v[30:31], v[126:127] neg_lo:[0,1] neg_hi:[0,1]\n\t"
"v_pk_fma_f32 v[28:29], v[124:125], s[90:91], v[28:29] neg_lo:[1,0,0] neg_hi:[1,0,0]\n\t"
"v_pk_fma_f32 v[30:31], v[126:127], s[90:91], v[30:31] neg_lo:[1,0,0] neg_hi:[1,0,0]\n\t"
"v_pk_add_f32 v[108:109], v[12:13], v[108:109] neg_lo:[0,1] neg_hi:[0,1]\n\t"
"v_pk_add_f32 v[110:111], v[14:15], v[110:111] neg_lo:[0,1] neg_hi:[0,1]\n\t"
"v_pk_fma_f32 v[12:13], v[108:109], s[90:91], v[12:13] neg_lo:[1,0,0] neg_hi:[1,0,0]\n\t"
"v_pk_fma_f32 v[14:15], v[110:111], s[90:91], v[14:15] neg_lo:[1,0,0] neg_hi:[1,0,0]\n\t"
"v_mfma_f32_4x4x4_16b_bf16 v[192:195], a[254:255], a[176:177], v[200:203]\n\t"
"v_add_u32 v248, 101632, v248\n\t"
"v_mfma_f32_4x4x4_16b_bf16 v[196:199], a[254:255], a[184:185], v[204:207]\n\t"
"v_xor_b32 v249, 32, v248\n\t"
"v_mfma_f32_4x4x4_16b_bf16 v[200:203], a[254:255], a[180:181], v[200:203]\n\t"
"v_xor_b32 v250, 64, v248\n\t"
"v_mfma_f32_4x4x4_16b_bf16 v[204:207], a[254:255], a[188:189], v[204:207]\n\t"
"v_mfma_f32_4x4x4_16b_bf16 v[208:211], a[254:255], a[192:193], v[216:219]\n\t"
"v_mfma_f32_4x4x4_16b_bf16 v[212:215], a[254:255], a[200:201], v[220:223]\n\t"
"v_mfma_f32_4x4x4_16b_bf16 v[216:219], a[254:255], a[196:197], v[216:219]\n\t"
"ds_read_b128 a[156:159], v224 offset:768\n\t"
"v_mfma_f32_4x4x4_16b_bf16 v[220:223], a[254:255], a[204:205], v[220:223]\n\t"
"s_waitcnt vmcnt(27) lgkmcnt(6)\n\t"
"buffer_load_dwordx4 a[0:3], v252, s[8:11], s58 offen offset:0 sc1\n\t"
"v_mfma_f32_16x16x32_bf16 v[0:3], a[48:51], a[144:147], v[0:3]\n\t"
"v_cvt_pk_bf16_f32 v194, -v194, -v198\n\t"
"v_cvt_pk_bf16_f32 v195, -v195, -v199\n\t"
"v_mfma_f32_16x16x32_bf16 v[0:3], a[52:55], a[160:163], v[0:3]\n\t"
"s_waitcnt vmcnt(26)\n\t"
"buffer_load_dwordx4 a[4:7], v252, s[8:11], s58 offen offset:1024 sc1\n\t"
"v_mfma_f32_16x16x32_bf16 v[16:19], a[56:59], a[144:147], v[16:19]\n\t"
"v_cvt_pk_bf16_f32 v193, -v193, -v197\n\t"
"v_cvt_pk_bf16_f32 v201, v201, v205\n\t"
"v_mfma_f32_16x16x32_bf16 v[16:19], a[60:63], a[160:163], v[16:19]\n\t"
"s_waitcnt vmcnt(25)\n\t"
"buffer_load_dwordx4 a[8:11], v252, s[8:11], s58 offen offset:2048 sc1\n\t"
"v_mfma_f32_16x16x32_bf16 v[32:35], a[64:67], a[144:147], v[32:35]\n\t"
"ds_write_addtid_b32 v193 offset:20672\n\t"
"v_cvt_pk_bf16_f32 v203, v203, v207\n\t"
"v_mfma_f32_16x16x32_bf16 v[32:35], a[68:71], a[160:163], v[32:35]\n\t"
"s_waitcnt lgkmcnt(5)\n\t"
"ds_write_addtid_b32 v203 offset:30016\n\t"
"v_cvt_pk_bf16_f32 v202, v202, v206\n\t"
"v_mfma_f32_16x16x32_bf16 v[36:39], a[64:67], a[148:151], v[36:39]\n\t"
"ds_read_b128 a[160:163], v224 offset:3072\n\t"
"v_cvt_pk_bf16_f32 v218, v218, v222\n\t"
"v_mfma_f32_16x16x32_bf16 v[36:39], a[68:71], a[164:167], v[36:39]\n\t"
"buffer_load_dwordx4 a[12:15], v252, s[8:11], s58 offen offset:3072 sc1\n\t"
"v_mfma_f32_16x16x32_bf16 v[20:23], a[56:59], a[148:151], v[20:23]\n\t"
"ds_read_b128 a[144:147], v224 offset:1024\n\t"
"v_cvt_pk_bf16_f32 v192, -v192, -v196\n\t"
"v_mfma_f32_16x16x32_bf16 v[20:23], a[60:63], a[164:167], v[20:23]\n\t"
"s_mov_b32 s64, 155648\n\t"
"ds_write_addtid_b32 v218 offset:27936\n\t"
"v_cvt_pk_bf16_f32 v209, -v209, -v213\n\t"
"v_mfma_f32_16x16x32_bf16 v[4:7], a[48:51], a[148:151], v[4:7]\n\t"
"ds_write_addtid_b32 v194 offset:17568\n\t"
"v_cvt_pk_bf16_f32 v217, v217, v221\n\t"
"v_mfma_f32_16x16x32_bf16 v[4:7], a[52:55], a[164:167], v[4:7]\n\t"
"s_waitcnt lgkmcnt(8)\n\t"
"buffer_load_dwordx4 a[16:19], v235, s[8:11], s59 offen offset:0 sc1\n\t"
"v_mfma_f32_16x16x32_bf16 v[8:11], a[48:51], a[152:155], v[8:11]\n\t"
"ds_read_b128 a[148:151], v224 offset:1280\n\t"
"v_cvt_pk_bf16_f32 v210, -v210, -v214\n\t"
"v_mfma_f32_16x16x32_bf16 v[8:11], a[52:55], a[168:171], v[8:11]\n\t"
"buffer_load_dwordx4 a[20:23], v235, s[8:11], s59 offen offset:1024 sc1\n\t"
"v_mfma_f32_16x16x32_bf16 v[24:27], a[56:59], a[152:155], v[24:27]\n\t"
"ds_read_b128 a[164:167], v224 offset:3328\n\t"
"v_cvt_pk_bf16_f32 v200, v200, v204\n\t"
"v_mfma_f32_16x16x32_bf16 v[24:27], a[60:63], a[168:171], v[24:27]\n\t"
"ds_write_addtid_b32 v210 offset:19616\n\t"
"v_cvt_pk_bf16_f32 v216, v216, v220\n\t"
"v_mfma_f32_16x16x32_bf16 v[40:43], a[64:67], a[152:155], v[40:43]\n\t"
"s_mov_b32 s68, 188416\n\t"
"ds_write_addtid_b32 v209 offset:22720\n\t"
"ds_write_addtid_b32 v217 offset:31040\n\t"
"v_mfma_f32_16x16x32_bf16 v[40:43], a[68:71], a[168:171], v[40:43]\n\t"
"s_waitcnt lgkmcnt(11)\n\t"
"buffer_load_dwordx4 a[24:27], v252, s[8:11], s57 offen offset:2048 sc1\n\t"
"v_mfma_f32_16x16x32_bf16 v[44:47], a[64:67], a[156:159], v[44:47]\n\t"
"ds_write_addtid_b32 v201 offset:28992\n\t"
"v_cvt_pk_bf16_f32 v208, -v208, -v212\n\t"
"v_mfma_f32_16x16x32_bf16 v[44:47], a[68:71], a[172:175], v[44:47]\n\t"
"ds_write_addtid_b32 v216 offset:26912\n\t"
"v_mfma_f32_16x16x32_bf16 v[28:31], a[56:59], a[156:159], v[28:31]\n\t"
"ds_write_addtid_b32 v202 offset:25888\n\t"
"v_cvt_pk_bf16_f32 v211, -v211, -v215\n\t"
"v_mfma_f32_16x16x32_bf16 v[28:31], a[60:63], a[172:175], v[28:31]\n\t"
"buffer_load_dwordx4 a[28:31], v252, s[8:11], s57 offen offset:3072 sc1\n\t"
"v_mfma_f32_16x16x32_bf16 v[12:15], a[48:51], a[156:159], v[12:15]\n\t"
"s_mov_b32 s67, 180224\n\t"
"ds_write_addtid_b32 v195 offset:21696\n\t"
"ds_write_addtid_b32 v200 offset:24864\n\t"
"v_mfma_f32_16x16x32_bf16 v[12:15], a[52:55], a[172:175], v[12:15]\n\t"
"s_waitcnt vmcnt(29) lgkmcnt(12)\n\t"
"ds_write_addtid_b32 v192 offset:16544\n\t"
"v_cvt_pk_bf16_f32 v219, v219, v223\n\t"
"v_mfma_f32_16x16x32_bf16 v[96:99], a[72:75], a[144:147], v[96:99]\n\t"
"s_mov_b32 s72, 204800\n\t"
"ds_write_addtid_b32 v208 offset:18592\n\t"
"ds_write_addtid_b32 v211 offset:23744\n\t"
"v_mfma_f32_16x16x32_bf16 v[96:99], a[76:79], a[160:163], v[96:99]\n\t"
"s_waitcnt vmcnt(27)\n\t"
"buffer_load_dwordx4 a[32:35], v235, s[8:11], s58 offen offset:0 sc1\n\t"
"v_mfma_f32_16x16x32_bf16 v[112:115], a[80:83], a[144:147], v[112:115]\n\t"
"s_mov_b32 s65, 163840\n\t"
"ds_read_b128 a[172:175], v224 offset:3840\n\t"
"ds_write_addtid_b32 v219 offset:32064\n\t"
"v_mfma_f32_16x16x32_bf16 v[112:115], a[84:87], a[160:163], v[112:115]\n\t"
"s_waitcnt vmcnt(26)\n\t"
"buffer_load_dwordx4 a[36:39], v235, s[8:11], s58 offen offset:1024 sc1\n\t"
"v_mfma_f32_16x16x32_bf16 v[128:131], a[88:91], a[144:147], v[128:131]\n\t"
"ds_read_b128 a[156:159], v224 offset:1792\n\t"
"v_mfma_f32_16x16x32_bf16 v[128:131], a[92:95], a[160:163], v[128:131]\n\t"
"s_waitcnt lgkmcnt(14)\n\t"
"ds_read_b128 a[152:155], v224 offset:1536\n\t"
"ds_read_b128 a[168:171], v224 offset:3584\n\t"
"s_mov_b32 s74, 212992\n\t"
"v_mfma_f32_16x16x32_bf16 v[132:135], a[88:91], a[148:151], v[132:135]\n\t"
"v_xor_b32 v251, 96, v248\n\t"
"v_mfma_f32_16x16x32_bf16 v[132:135], a[92:95], a[164:167], v[132:135]\n\t"
"buffer_load_dwordx4 a[40:43], v235, s[8:11], s58 offen offset:2048 sc1\n\t"
"v_mfma_f32_16x16x32_bf16 v[116:119], a[80:83], a[148:151], v[116:119]\n\t"
"v_mfma_f32_16x16x32_bf16 v[116:119], a[84:87], a[164:167], v[116:119]\n\t"
"v_mfma_f32_16x16x32_bf16 v[100:103], a[72:75], a[148:151], v[100:103]\n\t"
"buffer_load_dwordx4 a[44:47], v235, s[8:11], s58 offen offset:3072 sc1\n\t"
"v_mfma_f32_16x16x32_bf16 v[100:103], a[76:79], a[164:167], v[100:103]\n\t"
"s_waitcnt vmcnt(26)\n\t"
"s_waitcnt lgkmcnt(0)\n\t"
"s_barrier\n\t"
"v_mfma_f32_16x16x32_bf16 v[104:107], a[72:75], a[152:155], v[104:107]\n\t"
"s_add_u32 m0, s43, 33280\n\t"
"ds_read_b128 a[160:163], v224 offset:18592\n\t"
"v_mfma_f32_16x16x32_bf16 v[104:107], a[76:79], a[168:171], v[104:107]\n\t"
"ds_read_b128 a[144:147], v224 offset:16544\n\t"
"v_mfma_f32_16x16x32_bf16 v[120:123], a[80:83], a[152:155], v[120:123]\n\t"
"ds_read_b128 a[148:151], v224 offset:16800\n\t"
"v_mfma_f32_16x16x32_bf16 v[120:123], a[84:87], a[168:171], v[120:123]\n\t"
"ds_read_b128 a[164:167], v224 offset:18848\n\t"
"v_mfma_f32_16x16x32_bf16 v[136:139], a[88:91], a[152:155], v[136:139]\n\t"
"ds_read_b64_tr_b16 a[176:177], v248 offset:20736\n\t"
"v_mfma_f32_16x16x32_bf16 v[136:139], a[92:95], a[168:171], v[136:139]\n\t"
"s_mov_b64 exec, s[44:45]\n\t"
"buffer_load_dwordx4 v247, s[12:15], 0 offen offset:0 nt lds\n\t"
"s_mov_b64 exec, -1\n\t"
"v_mfma_f32_16x16x32_bf16 v[140:143], a[88:91], a[156:159], v[140:143]\n\t"
"s_add_u32 m0, s26, 35584\n\t"
"ds_read_b64_tr_b16 a[178:179], v249 offset:20736\n\t"
"v_mfma_f32_16x16x32_bf16 v[140:143], a[92:95], a[172:175], v[140:143]\n\t"
"s_mov_b32 s76, 229376\n\t"
"ds_read_b64_tr_b16 a[180:181], v248 offset:21248\n\t"
"ds_read_b64_tr_b16 a[182:183], v249 offset:21248\n\t"
"v_mfma_f32_16x16x32_bf16 v[124:127], a[80:83], a[156:159], v[124:127]\n\t"
"ds_read_b128 a[168:171], v224 offset:19104\n\t"
"v_mfma_f32_16x16x32_bf16 v[124:127], a[84:87], a[172:175], v[124:127]\n\t"
"buffer_load_dwordx4 v236, s[12:15], s22 offen offset:0 nt lds\n\t"
"v_mfma_f32_16x16x32_bf16 v[108:111], a[72:75], a[156:159], v[108:111]\n\t"
"ds_read_b64_tr_b16 a[184:185], v250 offset:23040\n\t"
"v_mfma_f32_16x16x32_bf16 v[108:111], a[76:79], a[172:175], v[108:111]\n\t"
"s_waitcnt lgkmcnt(2)\n\t"
"ds_read_b128 a[152:155], v224 offset:17056\n\t"
"v_mfma_f32_4x4x4_16b_bf16 v[192:195], a[210:211], a[176:177], 0\n\t"
"ds_read_b64_tr_b16 a[186:187], v251 offset:23040\n\t"
"v_mfma_f32_4x4x4_16b_bf16 v[196:199], a[210:211], a[178:179], 0\n\t"
"ds_read_b64_tr_b16 a[188:189], v250 offset:23552\n\t"
"v_mfma_f32_4x4x4_16b_bf16 v[200:203], a[210:211], a[180:181], 0\n\t"
"ds_read_b64_tr_b16 a[190:191], v251 offset:23552\n\t"
"v_mfma_f32_4x4x4_16b_bf16 v[204:207], a[210:211], a[182:183], 0\n\t"
"s_waitcnt vmcnt(26)\n\t"
"ds_read_b64_tr_b16 a[192:193], v250 offset:20736\n\t"
"v_mfma_f32_16x16x32_bf16 v[0:3], a[96:99], a[144:147], v[0:3]\n\t"
"ds_read_b64_tr_b16 a[194:195], v251 offset:20736\n\t"
"v_mfma_f32_16x16x32_bf16 v[0:3], a[100:103], a[160:163], v[0:3]\n\t"
"s_waitcnt vmcnt(24)\n\t"
"buffer_load_dwordx4 v237, s[12:15], s22 offen offset:1024 nt lds\n\t"
"v_mfma_f32_16x16x32_bf16 v[16:19], a[104:107], a[144:147], v[16:19]\n\t"
"s_lshl_b32 m0, s73, 8\n\t"
"ds_read_b64_tr_b16 a[196:197], v250 offset:21248\n\t"
"v_mfma_f32_16x16x32_bf16 v[16:19], a[108:111], a[160:163], v[16:19]\n\t"
"s_waitcnt vmcnt(23)\n\t"
"ds_read_b128 a[172:175], v224 offset:19360\n\t"
"v_cvt_pk_bf16_f32 v197, v193, v197\n\t"
"v_mfma_f32_16x16x32_bf16 v[32:35], a[112:115], a[144:147], v[32:35]\n\t"
"ds_read_b64_tr_b16 a[198:199], v251 offset:21248\n\t"
"v_cvt_pk_bf16_f32 v194, v194, v198\n\t"
"v_mfma_f32_16x16x32_bf16 v[32:35], a[116:119], a[160:163], v[32:35]\n\t"
"ds_read_b64_tr_b16 a[200:201], v248 offset:23040\n\t"
"v_cvt_pk_bf16_f32 v193, v203, v207\n\t"
"v_mfma_f32_16x16x32_bf16 v[36:39], a[112:115], a[148:151], v[36:39]\n\t"
"ds_read_b128 a[144:147], v224 offset:17568\n\t"
"v_cvt_pk_bf16_f32 v196, v192, v196\n\t"
"v_mfma_f32_16x16x32_bf16 v[36:39], a[116:119], a[164:167], v[36:39]\n\t"
"s_waitcnt lgkmcnt(7)\n\t"
"ds_read_b64_tr_b16 a[202:203], v249 offset:23040\n\t"
"v_cvt_pk_bf16_f32 v198, v200, v204\n\t"
"v_mfma_f32_4x4x4_16b_bf16 v[208:211], a[210:211], a[184:185], 0\n\t"
"ds_write_addtid_b32 v197 offset:1024\n\t"
"v_mfma_f32_4x4x4_16b_bf16 v[212:215], a[210:211], a[186:187], 0\n\t"
"ds_read_b128 a[156:159], v224 offset:17312\n\t"
"v_mfma_f32_4x4x4_16b_bf16 v[216:219], a[210:211], a[188:189], 0\n\t"
"s_add_u32 m0, s26, 35584\n\t"
"ds_read_b128 a[160:163], v224 offset:19616\n\t"
"v_mfma_f32_4x4x4_16b_bf16 v[220:223], a[210:211], a[190:191], 0\n\t"
"buffer_load_dwordx4 v238, s[12:15], s22 offen offset:2048 nt lds\n\t"
"v_mfma_f32_16x16x32_bf16 v[20:23], a[104:107], a[148:151], v[20:23]\n\t"
"s_lshl_b32 m0, s73, 8\n\t"
"ds_read_b64_tr_b16 a[204:205], v248 offset:23552\n\t"
"v_cvt_pk_bf16_f32 v195, v195, v199\n\t"
"v_mfma_f32_16x16x32_bf16 v[20:23], a[108:111], a[164:167], v[20:23]\n\t"
"ds_write_addtid_b32 v198 offset:8320\n\t"
"v_cvt_pk_bf16_f32 v199, v201, v205\n\t"
"v_mfma_f32_16x16x32_bf16 v[4:7], a[96:99], a[148:151], v[4:7]\n\t"
"ds_write_addtid_b32 v199 offset:9344\n\t"
"v_cvt_pk_bf16_f32 v192, v202, v206\n\t"
"v_mfma_f32_16x16x32_bf16 v[4:7], a[100:103], a[164:167], v[4:7]\n\t"
"ds_read_b128 a[148:151], v224 offset:17824\n\t"
"v_cvt_pk_bf16_f32 v211, v211, v215\n\t"
"v_mfma_f32_16x16x32_bf16 v[8:11], a[96:99], a[152:155], v[8:11]\n\t"
"v_cvt_pk_bf16_f32 v210, v210, v214\n\t"
"v_cvt_pk_bf16_f32 v213, v209, v213\n\t"
"v_mfma_f32_16x16x32_bf16 v[8:11], a[100:103], a[168:171], v[8:11]\n\t"
"ds_write_addtid_b32 v213 offset:5152\n\t"
"v_cvt_pk_bf16_f32 v212, v208, v212\n\t"
"v_mfma_f32_16x16x32_bf16 v[24:27], a[104:107], a[152:155], v[24:27]\n\t"
"v_cvt_pk_bf16_f32 v208, v218, v222\n\t"
"v_cvt_pk_bf16_f32 v209, v219, v223\n\t"
"v_mfma_f32_16x16x32_bf16 v[24:27], a[108:111], a[168:171], v[24:27]\n\t"
"s_waitcnt lgkmcnt(11)\n\t"
"ds_write_addtid_b32 v196 offset:0\n\t"
"v_cvt_pk_bf16_f32 v215, v217, v221\n\t"
"v_mfma_f32_4x4x4_16b_bf16 v[200:203], a[210:211], a[192:193], 0\n\t"
"ds_read_b128 a[164:167], v224 offset:19872\n\t"
"v_mfma_f32_4x4x4_16b_bf16 v[204:207], a[210:211], a[194:195], 0\n\t"
"ds_write_addtid_b32 v215 offset:13472\n\t"
"v_mfma_f32_4x4x4_16b_bf16 v[196:199], a[210:211], a[196:197], 0\n\t"
"s_add_u32 m0, s26, 35584\n\t"
"v_cvt_pk_bf16_f32 v214, v216, v220\n\t"
"v_mfma_f32_4x4x4_16b_bf16 v[216:219], a[210:211], a[198:199], 0\n\t"
"buffer_load_dwordx4 v239, s[12:15], s22 offen offset:3072 nt lds\n\t"
"s_lshl_b32 m0, s73, 8\n\t"
"v_mfma_f32_16x16x32_bf16 v[40:43], a[112:115], a[152:155], v[40:43]\n\t"
"ds_write_addtid_b32 v214 offset:12448\n\t"
"v_mfma_f32_16x16x32_bf16 v[40:43], a[116:119], a[168:171], v[40:43]\n\t"
"s_waitcnt lgkmcnt(10)\n\t"
"ds_read_b128 a[168:171], v224 offset:20128\n\t"
"ds_write_addtid_b32 v212 offset:4128\n\t"
"s_mov_b32 s79, 253952\n\t"
"v_mfma_f32_16x16x32_bf16 v[44:47], a[112:115], a[156:159], v[44:47]\n\t"
"s_add_u32 m0, s26, 35584\n\t"
"ds_read_b128 a[152:155], v224 offset:18080\n\t"
"ds_read_b64_tr_b16 a[206:207], v249 offset:23552\n\t"
"s_mov_b32 s77, 237568\n\t"
"v_mfma_f32_16x16x32_bf16 v[44:47], a[116:119], a[172:175], v[44:47]\n\t"
"s_mov_b64 exec, s[36:37]\n\t"
"buffer_load_dwordx4 v240, s[12:15], s22 offen offset:3584 nt lds\n\t"
"s_mov_b64 exec, -1\n\t"
"v_mfma_f32_16x16x32_bf16 v[28:31], a[104:107], a[156:159], v[28:31]\n\t"
"s_lshl_b32 m0, s73, 8\n\t"
"v_cvt_pk_bf16_f32 v204, v200, v204\n\t"
"v_cvt_pk_bf16_f32 v205, v201, v205\n\t"
"v_mfma_f32_16x16x32_bf16 v[28:31], a[108:111], a[172:175], v[28:31]\n\t"
"buffer_load_dwordx4 a[48:51], v252, s[8:11], s64 offen offset:0 sc1\n\t"
"buffer_load_dwordx4 a[52:55], v252, s[8:11], s64 offen offset:1024 sc1\n\t"
"v_mfma_f32_16x16x32_bf16 v[12:15], a[96:99], a[156:159], v[12:15]\n\t"
"v_cvt_pk_bf16_f32 v202, v202, v206\n\t"
"v_cvt_pk_bf16_f32 v203, v203, v207\n\t"
"v_mfma_f32_16x16x32_bf16 v[12:15], a[100:103], a[172:175], v[12:15]\n\t"
"s_waitcnt lgkmcnt(0)\n\t"
"buffer_load_dwordx4 a[56:59], v252, s[8:11], s64 offen offset:2048 sc1\n\t"
"buffer_load_dwordx4 a[60:63], v252, s[8:11], s64 offen offset:3072 sc1\n\t"
"v_cvt_pk_bf16_f32 v206, v196, v216\n\t"
"v_cvt_pk_bf16_f32 v201, v199, v219\n\t"
"v_cvt_pk_bf16_f32 v207, v197, v217\n\t"
"v_mfma_f32_4x4x4_16b_bf16 v[220:223], a[210:211], a[200:201], 0\n\t"
"v_cvt_pk_bf16_f32 v200, v198, v218\n\t"
"v_mfma_f32_4x4x4_16b_bf16 v[212:215], a[210:211], a[202:203], 0\n\t"
"buffer_load_dwordx4 a[64:67], v235, s[8:11], s65 offen offset:0 sc1\n\t"
"buffer_load_dwordx4 a[68:71], v235, s[8:11], s65 offen offset:1024 sc1\n\t"
"v_mfma_f32_4x4x4_16b_bf16 v[196:199], a[210:211], a[204:205], 0\n\t"
"ds_write_addtid_b32 v207 offset:11392\n\t"
"v_mfma_f32_4x4x4_16b_bf16 v[216:219], a[210:211], a[206:207], 0\n\t"
"s_waitcnt vmcnt(30)\n\t"
"ds_write_addtid_b32 v204 offset:2048\n\t"
"buffer_load_dwordx4 a[72:75], v252, s[8:11], s63 offen offset:2048 sc1\n\t"
"buffer_load_dwordx4 a[76:79], v252, s[8:11], s63 offen offset:3072 sc1\n\t"
"v_mfma_f32_16x16x32_bf16 v[96:99], a[120:123], a[144:147], v[96:99]\n\t"
"s_mov_b32 s83, 286720\n\t"
"ds_read_b128 a[156:159], v224 offset:18336\n\t"
"ds_read_b128 a[172:175], v224 offset:20384\n\t"
"v_mfma_f32_16x16x32_bf16 v[96:99], a[124:127], a[160:163], v[96:99]\n\t"
"s_waitcnt vmcnt(29)\n\t"
"buffer_load_dwordx4 a[80:83], v235, s[8:11], s64 offen offset:0 sc1\n\t"
"buffer_load_dwordx4 a[84:87], v235, s[8:11], s64 offen offset:1024 sc1\n\t"
"v_mfma_f32_16x16x32_bf16 v[112:115], a[128:131], a[144:147], v[112:115]\n\t"
"s_mov_b32 s82, 278528\n\t"
"ds_write_addtid_b32 v206 offset:10368\n\t"
"ds_write_addtid_b32 v205 offset:3072\n\t"
"v_mfma_f32_16x16x32_bf16 v[112:115], a[132:135], a[160:163], v[112:115]\n\t"
"s_waitcnt vmcnt(29)\n\t"
"buffer_load_dwordx4 a[88:91], v235, s[8:11], s64 offen offset:2048 sc1\n\t"
"buffer_load_dwordx4 a[92:95], v235, s[8:11], s64 offen offset:3072 sc1\n\t"
"v_cvt_pk_bf16_f32 v212, v220, v212\n\t"
"v_cvt_pk_bf16_f32 v223, v223, v215\n\t"
"v_mfma_f32_16x16x32_bf16 v[128:131], a[136:139], a[144:147], v[128:131]\n\t"
"v_cvt_pk_bf16_f32 v222, v222, v214\n\t"
"v_cvt_pk_bf16_f32 v213, v221, v213\n\t"
"v_mfma_f32_16x16x32_bf16 v[128:131], a[140:143], a[160:163], v[128:131]\n\t"
"s_nop 0\n\t"
"ds_write_addtid_b32 v212 offset:6176\n\t"
"ds_write_addtid_b32 v213 offset:7200\n\t"
"buffer_load_dwordx4 a[96:99], v252, s[8:11], s46 offen offset:0 sc1\n\t"
"buffer_load_dwordx4 a[100:103], v252, s[8:11], s46 offen offset:1024 sc1\n\t"
"v_cvt_pk_bf16_f32 v214, v196, v216\n\t"
"v_cvt_pk_bf16_f32 v215, v197, v217\n\t"
"v_mfma_f32_16x16x32_bf16 v[132:135], a[136:139], a[148:151], v[132:135]\n\t"
"s_mov_b32 s80, 262144\n\t"
"ds_write_addtid_b32 v214 offset:14496\n\t"
"ds_write_addtid_b32 v215 offset:15520\n\t"
"v_mfma_f32_16x16x32_bf16 v[132:135], a[140:143], a[164:167], v[132:135]\n\t"
"buffer_load_dwordx4 a[104:107], v252, s[8:11], s46 offen offset:2048 sc1\n\t"
"buffer_load_dwordx4 a[108:111], v252, s[8:11], s46 offen offset:3072 sc1\n\t"
"v_mfma_f32_16x16x32_bf16 v[116:119], a[128:131], a[148:151], v[116:119]\n\t"
"v_accvgpr_read_b32 v224, a213\n\t"
"v_cvt_pk_bf16_f32 v221, v199, v219\n\t"
"v_mfma_f32_16x16x32_bf16 v[116:119], a[132:135], a[164:167], v[116:119]\n\t"
"buffer_load_dwordx4 a[112:115], v235, s[8:11], s47 offen offset:0 sc1\n\t"
"buffer_load_dwordx4 a[116:119], v235, s[8:11], s47 offen offset:1024 sc1\n\t"
"v_mfma_f32_16x16x32_bf16 v[100:103], a[120:123], a[148:151], v[100:103]\n\t"
"v_cvt_pk_bf16_f32 v220, v198, v218\n\t"
"v_mfma_f32_16x16x32_bf16 v[100:103], a[124:127], a[164:167], v[100:103]\n\t"
"v_add_u32 v246, 8, v228\n\t"
"v_add_u32 v247, 16, v228\n\t"
"s_waitcnt lgkmcnt(0)\n\t"
"s_barrier\n\t"
"Lunit_%=:\n\t"
"v_mfma_f32_16x16x32_bf16 v[104:107], a[120:123], a[152:155], v[104:107]\n\t"
"ds_read_b128 a[164:167], v224 offset:3328\n\t"
"v_mfma_f32_16x16x32_bf16 v[104:107], a[124:127], a[168:171], v[104:107]\n\t"
"ds_read_b128 a[144:147], v224 offset:1024\n\t"
"v_mfma_f32_16x16x32_bf16 v[120:123], a[128:131], a[152:155], v[120:123]\n\t"
"s_nop 0\n\t"
"ds_read_b128 a[160:163], v224 offset:3072\n\t"
"ds_read_b128 a[148:151], v224 offset:1280\n\t"
"v_mfma_f32_16x16x32_bf16 v[120:123], a[132:135], a[168:171], v[120:123]\n\t"
"ds_write_addtid_b32 v208 offset:28992\n\t"
"v_mfma_f32_16x16x32_bf16 v[136:139], a[136:139], a[152:155], v[136:139]\n\t"
"ds_read_b128 a[152:155], v224 offset:1536\n\t"
"v_mfma_f32_16x16x32_bf16 v[136:139], a[140:143], a[168:171], v[136:139]\n\t"
"ds_read_b128 a[168:171], v224 offset:3584\n\t"
"v_mfma_f32_16x16x32_bf16 v[140:143], a[136:139], a[156:159], v[140:143]\n\t"
"buffer_load_dwordx4 a[136:139], v235, s[8:11], s46 offen offset:2048 sc1\n\t"
"v_mfma_f32_16x16x32_bf16 v[140:143], a[140:143], a[172:175], v[140:143]\n\t"
"s_waitcnt vmcnt(37)\n\t"
"ds_write_b128 v254, a[248:251] offset:41472\n\t"
"v_mfma_f32_16x16x32_bf16 v[124:127], a[128:131], a[156:159], v[124:127]\n\t"
"v_mfma_f32_16x16x32_bf16 v[124:127], a[132:135], a[172:175], v[124:127]\n\t"
"buffer_load_dwordx4 a[140:143], v235, s[8:11], s46 offen offset:3072 sc1\n\t"
"v_mfma_f32_16x16x32_bf16 v[108:111], a[120:123], a[156:159], v[108:111]\n\t"
"v_mfma_f32_16x16x32_bf16 v[108:111], a[124:127], a[172:175], v[108:111]\n\t"
"s_waitcnt vmcnt(36) lgkmcnt(5)\n\t"
"buffer_load_dwordx4 a[132:135], v235, s[8:11], s46 offen offset:1024 sc1\n\t"
"v_mfma_f32_16x16x32_bf16 v[48:51], a[0:3], a[144:147], v[0:3]\n\t"
"ds_write_addtid_b32 v203 offset:19616\n\t"
"v_mfma_f32_16x16x32_bf16 v[48:51], a[4:7], a[160:163], v[48:51]\n\t"
"s_waitcnt vmcnt(35) lgkmcnt(4)\n\t"
"buffer_load_dwordx4 a[128:131], v235, s[8:11], s46 offen offset:0 sc1\n\t"
"v_mfma_f32_16x16x32_bf16 v[64:67], a[8:11], a[144:147], v[16:19]\n\t"
"s_nop 0\n\t"
"ds_read_b128 a[156:159], v224 offset:1792\n\t"
"ds_read_b128 a[172:175], v224 offset:3840\n\t"
"v_mfma_f32_16x16x32_bf16 v[64:67], a[12:15], a[160:163], v[64:67]\n\t"
"s_waitcnt vmcnt(34) lgkmcnt(4)\n\t"
"buffer_load_dwordx4 a[120:123], v252, s[8:11], s42 offen offset:2048 sc1\n\t"
"v_mfma_f32_16x16x32_bf16 v[80:83], a[16:19], a[144:147], v[32:35]\n\t"
"ds_write_addtid_b32 v223 offset:23744\n\t"
"v_mfma_f32_16x16x32_bf16 v[80:83], a[20:23], a[160:163], v[80:83]\n\t"
"buffer_load_dwordx4 a[124:127], v252, s[8:11], s42 offen offset:3072 sc1\n\t"
"v_mfma_f32_16x16x32_bf16 v[84:87], a[16:19], a[148:151], v[36:39]\n\t"
"ds_read_b128 a[160:163], v224 offset:2048\n\t"
"v_mfma_f32_16x16x32_bf16 v[84:87], a[20:23], a[164:167], v[84:87]\n\t"
"ds_write_addtid_b32 v195 offset:17568\n\t"
"v_mfma_f32_16x16x32_bf16 v[68:71], a[8:11], a[148:151], v[20:23]\n\t"
"ds_read_b128 a[144:147], v224 offset:0\n\t"
"v_mfma_f32_16x16x32_bf16 v[68:71], a[12:15], a[164:167], v[68:71]\n\t"
"s_nop 0\n\t"
"ds_write_addtid_b32 v201 offset:27936\n\t"
"ds_write_addtid_b32 v221 offset:32064\n\t"
"v_mfma_f32_16x16x32_bf16 v[52:55], a[0:3], a[148:151], v[4:7]\n\t"
"ds_write_addtid_b32 v192 offset:24864\n\t"
"v_mfma_f32_16x16x32_bf16 v[52:55], a[4:7], a[164:167], v[52:55]\n\t"
"ds_write_addtid_b32 v210 offset:20672\n\t"
"v_mfma_f32_16x16x32_bf16 v[56:59], a[0:3], a[152:155], v[8:11]\n\t"
"ds_write_addtid_b32 v211 offset:21696\n\t"
"v_mfma_f32_16x16x32_bf16 v[56:59], a[4:7], a[168:171], v[56:59]\n\t"
"ds_read_b128 a[148:151], v224 offset:256\n\t"
"v_mfma_f32_16x16x32_bf16 v[72:75], a[8:11], a[152:155], v[24:27]\n\t"
"ds_read_b128 a[164:167], v224 offset:2304\n\t"
"v_mfma_f32_16x16x32_bf16 v[72:75], a[12:15], a[168:171], v[72:75]\n\t"
"ds_write_addtid_b32 v209 offset:30016\n\t"
"v_mfma_f32_16x16x32_bf16 v[88:91], a[16:19], a[152:155], v[40:43]\n\t"
"ds_write_addtid_b32 v194 offset:16544\n\t"
"v_mfma_f32_16x16x32_bf16 v[88:91], a[20:23], a[168:171], v[88:91]\n\t"
"s_waitcnt lgkmcnt(10)\n\t"
"ds_write_addtid_b32 v220 offset:31040\n\t"
"v_mfma_f32_16x16x32_bf16 v[92:95], a[16:19], a[156:159], v[44:47]\n\t"
"ds_write_addtid_b32 v222 offset:22720\n\t"
"v_mfma_f32_16x16x32_bf16 v[92:95], a[20:23], a[172:175], v[92:95]\n\t"
"buffer_load_dwordx4 a[16:19], v235, s[8:11], s50 offen offset:0 sc1\n\t"
"v_mfma_f32_16x16x32_bf16 v[76:79], a[8:11], a[156:159], v[28:31]\n\t"
"ds_write_addtid_b32 v193 offset:25888\n\t"
"v_mfma_f32_16x16x32_bf16 v[76:79], a[12:15], a[172:175], v[76:79]\n\t"
"buffer_load_dwordx4 a[20:23], v235, s[8:11], s50 offen offset:1024 sc1\n\t"
"v_mfma_f32_16x16x32_bf16 v[60:63], a[0:3], a[156:159], v[12:15]\n\t"
"ds_write_addtid_b32 v200 offset:26912\n\t"
"v_mfma_f32_16x16x32_bf16 v[60:63], a[4:7], a[172:175], v[60:63]\n\t"
"s_waitcnt vmcnt(36) lgkmcnt(7)\n\t"
"buffer_load_dwordx4 a[12:15], v252, s[8:11], s49 offen offset:3072 sc1\n\t"
"v_mfma_f32_16x16x32_bf16 v[0:3], a[24:27], a[144:147], v[0:3]\n\t"
"ds_write_addtid_b32 v202 offset:18592\n\t"
"v_mfma_f32_16x16x32_bf16 v[0:3], a[28:31], a[160:163], v[0:3]\n\t"
"s_waitcnt vmcnt(35) lgkmcnt(6)\n\t"
"buffer_load_dwordx4 a[8:11], v252, s[8:11], s49 offen offset:2048 sc1\n\t"
"v_mfma_f32_16x16x32_bf16 v[16:19], a[32:35], a[144:147], v[16:19]\n\t"
"ds_read_b128 a[152:155], v224 offset:512\n\t"
"v_mfma_f32_16x16x32_bf16 v[16:19], a[36:39], a[160:163], v[16:19]\n\t"
"s_waitcnt vmcnt(34) lgkmcnt(5)\n\t"
"ds_read_b128 a[156:159], v224 offset:768\n\t"
"v_mfma_f32_16x16x32_bf16 v[32:35], a[40:43], a[144:147], v[32:35]\n\t"
"ds_read_b128 a[172:175], v224 offset:2816\n\t"
"v_mfma_f32_16x16x32_bf16 v[32:35], a[44:47], a[160:163], v[32:35]\n\t"
"ds_read_b128 a[168:171], v224 offset:2560\n\t"
"v_mfma_f32_16x16x32_bf16 v[36:39], a[40:43], a[148:151], v[36:39]\n\t"
"v_mfma_f32_16x16x32_bf16 v[36:39], a[44:47], a[164:167], v[36:39]\n\t"
"buffer_load_dwordx4 a[0:3], v252, s[8:11], s49 offen offset:0 sc1\n\t"
"v_mfma_f32_16x16x32_bf16 v[20:23], a[32:35], a[148:151], v[20:23]\n\t"
"v_mfma_f32_16x16x32_bf16 v[20:23], a[36:39], a[164:167], v[20:23]\n\t"
"buffer_load_dwordx4 a[4:7], v252, s[8:11], s49 offen offset:1024 sc1\n\t"
"v_mfma_f32_16x16x32_bf16 v[4:7], a[24:27], a[148:151], v[4:7]\n\t"
"v_mfma_f32_16x16x32_bf16 v[4:7], a[28:31], a[164:167], v[4:7]\n\t"
"s_waitcnt vmcnt(30) lgkmcnt(4)\n\t"
"s_waitcnt lgkmcnt(0)\n\t"
"s_barrier\n\t"
"v_mfma_f32_16x16x32_bf16 v[8:11], a[24:27], a[152:155], v[8:11]\n\t"
"s_add_u32 m0, s26, 76800\n\t"
"ds_read_b128 a[144:147], v224 offset:17568\n\t"
"v_mfma_f32_16x16x32_bf16 v[8:11], a[28:31], a[168:171], v[8:11]\n\t"
"buffer_load_dwordx4 v236, s[12:15], s22 offen offset:256 nt lds\n\t"
"v_mfma_f32_16x16x32_bf16 v[24:27], a[32:35], a[152:155], v[24:27]\n\t"
"s_add_u32 s24, s24, 3145728\n\t"
"ds_read_b128 a[160:163], v224 offset:19616\n\t"
"ds_read_b128 a[148:151], v224 offset:17824\n\t"
"v_mfma_f32_16x16x32_bf16 v[24:27], a[36:39], a[168:171], v[24:27]\n\t"
"s_add_u32 s20, s20, 3145728\n\t"
"ds_read_b128 a[164:167], v224 offset:19872\n\t"
"ds_read_b64_tr_b16 a[176:177], v248 offset:0\n\t"
"v_mfma_f32_16x16x32_bf16 v[40:43], a[40:43], a[152:155], v[40:43]\n\t"
"ds_read_b64_tr_b16 a[178:179], v249 offset:0\n\t"
"v_mfma_f32_16x16x32_bf16 v[40:43], a[44:47], a[168:171], v[40:43]\n\t"
"ds_read_b64_tr_b16 a[180:181], v248 offset:512\n\t"
"v_mfma_f32_16x16x32_bf16 v[44:47], a[40:43], a[156:159], v[44:47]\n\t"
"ds_read_b64_tr_b16 a[182:183], v249 offset:512\n\t"
"v_mfma_f32_16x16x32_bf16 v[44:47], a[44:47], a[172:175], v[44:47]\n\t"
"buffer_load_dwordx4 v237, s[12:15], s22 offen offset:1280 nt lds\n\t"
"v_mfma_f32_16x16x32_bf16 v[28:31], a[32:35], a[156:159], v[28:31]\n\t"
"ds_read_b64_tr_b16 a[184:185], v250 offset:2304\n\t"
"v_mfma_f32_16x16x32_bf16 v[28:31], a[36:39], a[172:175], v[28:31]\n\t"
"ds_read_b64_tr_b16 a[186:187], v251 offset:2304\n\t"
"v_mfma_f32_16x16x32_bf16 v[12:15], a[24:27], a[156:159], v[12:15]\n\t"
"ds_read_b128 a[152:155], v224 offset:18080\n\t"
"v_mfma_f32_16x16x32_bf16 v[12:15], a[28:31], a[172:175], v[12:15]\n\t"
"s_waitcnt lgkmcnt(3)\n\t"
"buffer_load_dwordx4 v238, s[12:15], s22 offen offset:2304 nt lds\n\t"
"v_mfma_f32_4x4x4_16b_bf16 v[192:195], a[210:211], a[176:177], 0\n\t"
"ds_read_b64_tr_b16 a[188:189], v250 offset:2816\n\t"
"v_mfma_f32_4x4x4_16b_bf16 v[196:199], a[210:211], a[178:179], 0\n\t"
"ds_read_b128 a[168:171], v224 offset:20128\n\t"
"v_mfma_f32_4x4x4_16b_bf16 v[200:203], a[210:211], a[180:181], 0\n\t"
"ds_read_b64_tr_b16 a[190:191], v251 offset:2816\n\t"
"v_mfma_f32_4x4x4_16b_bf16 v[204:207], a[210:211], a[182:183], 0\n\t"
"s_waitcnt vmcnt(31)\n\t"
"ds_read_b64_tr_b16 a[192:193], v250 offset:0\n\t"
"v_mfma_f32_16x16x32_bf16 v[144:147], a[48:51], a[144:147], v[96:99]\n\t"
"ds_read_b64_tr_b16 a[194:195], v251 offset:0\n\t"
"v_mfma_f32_16x16x32_bf16 v[144:147], a[52:55], a[160:163], v[144:147]\n\t"
"s_waitcnt vmcnt(29)\n\t"
"buffer_load_dwordx4 v239, s[12:15], s22 offen offset:3328 nt lds\n\t"
"v_mfma_f32_16x16x32_bf16 v[160:163], a[56:59], a[144:147], v[112:115]\n\t"
"ds_read_b64_tr_b16 a[196:197], v250 offset:512\n\t"
"v_mfma_f32_16x16x32_bf16 v[160:163], a[60:63], a[160:163], v[160:163]\n\t"
"s_waitcnt vmcnt(28) lgkmcnt(7)\n\t"
"v_cvt_pk_bf16_f32 v194, v194, v198\n\t"
"v_cvt_pk_bf16_f32 v197, v193, v197\n\t"
"v_mfma_f32_16x16x32_bf16 v[176:179], a[64:67], a[144:147], v[128:131]\n\t"
"ds_read_b128 a[156:159], v224 offset:18336\n\t"
"v_cvt_pk_bf16_f32 v196, v192, v196\n\t"
"v_mfma_f32_16x16x32_bf16 v[176:179], a[68:71], a[160:163], v[176:179]\n\t"
"buffer_load_dwordx4 a[40:43], v235, s[8:11], s49 offen offset:2048 sc1\n\t"
"v_mfma_f32_16x16x32_bf16 v[180:183], a[64:67], a[148:151], v[132:135]\n\t"
"ds_read_b64_tr_b16 a[198:199], v251 offset:512\n\t"
"v_cvt_pk_bf16_f32 v198, v200, v204\n\t"
"v_mfma_f32_16x16x32_bf16 v[180:183], a[68:71], a[164:167], v[180:183]\n\t"
"s_waitcnt lgkmcnt(5)\n\t"
"s_mov_b64 exec, s[36:37]\n\t"
"buffer_load_dwordx4 v240, s[12:15], s22 offen offset:3840 nt lds\n\t"
"s_mov_b64 exec, -1\n\t"
"v_mfma_f32_4x4x4_16b_bf16 v[208:211], a[210:211], a[184:185], 0\n\t"
"ds_read_b128 a[160:163], v224 offset:18592\n\t"
"v_mfma_f32_4x4x4_16b_bf16 v[212:215], a[210:211], a[186:187], 0\n\t"
"ds_read_b128 a[172:175], v224 offset:20384\n\t"
"v_mfma_f32_4x4x4_16b_bf16 v[216:219], a[210:211], a[188:189], 0\n\t"
"s_lshl_b32 m0, s73, 8\n\t"
"ds_read_b128 a[144:147], v224 offset:16544\n\t"
"v_mfma_f32_4x4x4_16b_bf16 v[220:223], a[210:211], a[190:191], 0\n\t"
"ds_write_addtid_b32 v197 offset:1024\n\t"
"v_cvt_pk_bf16_f32 v195, v195, v199\n\t"
"v_mfma_f32_16x16x32_bf16 v[164:167], a[56:59], a[148:151], v[116:119]\n\t"
"ds_read_b64_tr_b16 a[200:201], v248 offset:2304\n\t"
"v_cvt_pk_bf16_f32 v199, v201, v205\n\t"
"v_mfma_f32_16x16x32_bf16 v[164:167], a[60:63], a[164:167], v[164:167]\n\t"
"ds_read_b64_tr_b16 a[202:203], v249 offset:2304\n\t"
"v_cvt_pk_bf16_f32 v192, v202, v206\n\t"
"v_mfma_f32_16x16x32_bf16 v[148:151], a[48:51], a[148:151], v[100:103]\n\t"
"ds_write_addtid_b32 v199 offset:9344\n\t"
"v_cvt_pk_bf16_f32 v193, v203, v207\n\t"
"v_mfma_f32_16x16x32_bf16 v[148:151], a[52:55], a[164:167], v[148:151]\n\t"
"ds_read_b128 a[148:151], v224 offset:16800\n\t"
"v_cvt_pk_bf16_f32 v211, v211, v215\n\t"
"v_mfma_f32_16x16x32_bf16 v[152:155], a[48:51], a[152:155], v[104:107]\n\t"
"v_cvt_pk_bf16_f32 v212, v208, v212\n\t"
"v_cvt_pk_bf16_f32 v213, v209, v213\n\t"
"v_mfma_f32_16x16x32_bf16 v[152:155], a[52:55], a[168:171], v[152:155]\n\t"
"ds_write_addtid_b32 v196 offset:0\n\t"
"v_cvt_pk_bf16_f32 v210, v210, v214\n\t"
"v_mfma_f32_16x16x32_bf16 v[168:171], a[56:59], a[152:155], v[120:123]\n\t"
"ds_write_addtid_b32 v198 offset:8320\n\t"
"v_cvt_pk_bf16_f32 v209, v219, v223\n\t"
"v_mfma_f32_16x16x32_bf16 v[168:171], a[60:63], a[168:171], v[168:171]\n\t"
"s_waitcnt lgkmcnt(8)\n\t"
"v_cvt_pk_bf16_f32 v208, v218, v222\n\t"
"v_cvt_pk_bf16_f32 v215, v217, v221\n\t"
"v_mfma_f32_4x4x4_16b_bf16 v[200:203], a[210:211], a[192:193], 0\n\t"
"ds_write_addtid_b32 v212 offset:4128\n\t"
"v_mfma_f32_4x4x4_16b_bf16 v[204:207], a[210:211], a[194:195], 0\n\t"
"v_cvt_pk_bf16_f32 v214, v216, v220\n\t"
"v_mfma_f32_4x4x4_16b_bf16 v[196:199], a[210:211], a[196:197], 0\n\t"
"ds_write_addtid_b32 v215 offset:13472\n\t"
"v_mfma_f32_4x4x4_16b_bf16 v[216:219], a[210:211], a[198:199], 0\n\t"
"ds_write_addtid_b32 v213 offset:5152\n\t"
"v_mfma_f32_16x16x32_bf16 v[184:187], a[64:67], a[152:155], v[136:139]\n\t"
"ds_read_b128 a[164:167], v224 offset:18848\n\t"
"v_mfma_f32_16x16x32_bf16 v[184:187], a[68:71], a[168:171], v[184:187]\n\t"
"ds_read_b64_tr_b16 a[204:205], v248 offset:2816\n\t"
"v_mfma_f32_16x16x32_bf16 v[188:191], a[64:67], a[156:159], v[140:143]\n\t"
"ds_read_b64_tr_b16 a[206:207], v249 offset:2816\n\t"
"v_mfma_f32_16x16x32_bf16 v[188:191], a[68:71], a[172:175], v[188:191]\n\t"
"ds_read_b128 a[168:171], v224 offset:19104\n\t"
"v_cvt_pk_bf16_f32 v202, v202, v206\n\t"
"v_mfma_f32_16x16x32_bf16 v[172:175], a[56:59], a[156:159], v[124:127]\n\t"
"ds_write_addtid_b32 v214 offset:12448\n\t"
"v_cvt_pk_bf16_f32 v204, v200, v204\n\t"
"v_mfma_f32_16x16x32_bf16 v[172:175], a[60:63], a[172:175], v[172:175]\n\t"
"v_cvt_pk_bf16_f32 v205, v201, v205\n\t"
"v_cvt_pk_bf16_f32 v203, v203, v207\n\t"
"v_mfma_f32_16x16x32_bf16 v[156:159], a[48:51], a[156:159], v[108:111]\n\t"
"ds_read_b128 a[152:155], v224 offset:17056\n\t"
"v_cvt_pk_bf16_f32 v206, v196, v216\n\t"
"v_mfma_f32_16x16x32_bf16 v[156:159], a[52:55], a[172:175], v[156:159]\n\t"
"s_waitcnt lgkmcnt(3)\n\t"
"ds_read_b128 a[172:175], v224 offset:19360\n\t"
"v_cvt_pk_bf16_f32 v200, v198, v218\n\t"
"v_mfma_f32_4x4x4_16b_bf16 v[220:223], a[210:211], a[200:201], 0\n\t"
"v_cvt_pk_bf16_f32 v207, v197, v217\n\t"
"v_mfma_f32_4x4x4_16b_bf16 v[212:215], a[210:211], a[202:203], 0\n\t"
"v_cvt_pk_bf16_f32 v201, v199, v219\n\t"
"v_mfma_f32_4x4x4_16b_bf16 v[196:199], a[210:211], a[204:205], 0\n\t"
"ds_write_addtid_b32 v207 offset:11392\n\t"
"v_mfma_f32_4x4x4_16b_bf16 v[216:219], a[210:211], a[206:207], 0\n\t"
"s_waitcnt vmcnt(28)\n\t"
"ds_read_b128 a[156:159], v224 offset:17312\n\t"
"v_mfma_f32_16x16x32_bf16 v[96:99], a[72:75], a[144:147], v[96:99]\n\t"
"ds_write_addtid_b32 v206 offset:10368\n\t"
"v_mfma_f32_16x16x32_bf16 v[96:99], a[76:79], a[160:163], v[96:99]\n\t"
"s_waitcnt vmcnt(26) lgkmcnt(6)\n\t"
"ds_write_addtid_b32 v204 offset:2048\n\t"
"v_mfma_f32_16x16x32_bf16 v[112:115], a[80:83], a[144:147], v[112:115]\n\t"
"ds_write_addtid_b32 v205 offset:3072\n\t"
"v_mfma_f32_16x16x32_bf16 v[112:115], a[84:87], a[160:163], v[112:115]\n\t"
"s_waitcnt vmcnt(24) lgkmcnt(7)\n\t"
"v_cvt_pk_bf16_f32 v222, v222, v214\n\t"
"v_cvt_pk_bf16_f32 v223, v223, v215\n\t"
"v_mfma_f32_16x16x32_bf16 v[128:131], a[88:91], a[144:147], v[128:131]\n\t"
"v_cvt_pk_bf16_f32 v212, v220, v212\n\t"
"v_cvt_pk_bf16_f32 v213, v221, v213\n\t"
"v_mfma_f32_16x16x32_bf16 v[128:131], a[92:95], a[160:163], v[128:131]\n\t"
"s_add_u32 s27, s27, 3145728\n\t"
"ds_write_addtid_b32 v212 offset:6176\n\t"
"ds_write_addtid_b32 v213 offset:7200\n\t"
"v_cvt_pk_bf16_f32 v214, v196, v216\n\t"
"v_cvt_pk_bf16_f32 v215, v197, v217\n\t"
"v_mfma_f32_16x16x32_bf16 v[132:135], a[88:91], a[148:151], v[132:135]\n\t"
"s_add_u32 s25, s25, 3145728\n\t"
"ds_write_addtid_b32 v214 offset:14496\n\t"
"ds_write_addtid_b32 v215 offset:15520\n\t"
"v_mfma_f32_16x16x32_bf16 v[132:135], a[92:95], a[164:167], v[132:135]\n\t"
"buffer_load_dwordx4 a[44:47], v235, s[8:11], s49 offen offset:3072 sc1\n\t"
"v_mfma_f32_16x16x32_bf16 v[116:119], a[80:83], a[148:151], v[116:119]\n\t"
"v_cvt_pk_bf16_f32 v221, v199, v219\n\t"
"v_mfma_f32_16x16x32_bf16 v[116:119], a[84:87], a[164:167], v[116:119]\n\t"
"buffer_load_dwordx4 a[36:39], v235, s[8:11], s49 offen offset:1024 sc1\n\t"
"v_mfma_f32_16x16x32_bf16 v[100:103], a[72:75], a[148:151], v[100:103]\n\t"
"s_waitcnt lgkmcnt(0)\n\t"
"v_cvt_pk_bf16_f32 v220, v198, v218\n\t"
"v_mfma_f32_16x16x32_bf16 v[100:103], a[76:79], a[164:167], v[100:103]\n\t"
"s_barrier\n\t"
"v_mfma_f32_16x16x32_bf16 v[104:107], a[72:75], a[152:155], v[104:107]\n\t"
"ds_read_b128 a[144:147], v224 offset:1024\n\t"
"v_mfma_f32_16x16x32_bf16 v[104:107], a[76:79], a[168:171], v[104:107]\n\t"
"buffer_load_dwordx4 a[32:35], v235, s[8:11], s49 offen offset:0 sc1\n\t"
"v_mfma_f32_16x16x32_bf16 v[120:123], a[80:83], a[152:155], v[120:123]\n\t"
"s_nop 0\n\t"
"ds_read_b128 a[160:163], v224 offset:3072\n\t"
"ds_read_b128 a[148:151], v224 offset:1280\n\t"
"v_mfma_f32_16x16x32_bf16 v[120:123], a[84:87], a[168:171], v[120:123]\n\t"
"ds_read_b128 a[164:167], v224 offset:3328\n\t"
"v_mfma_f32_16x16x32_bf16 v[136:139], a[88:91], a[152:155], v[136:139]\n\t"
"ds_read_b128 a[152:155], v224 offset:1536\n\t"
"v_mfma_f32_16x16x32_bf16 v[136:139], a[92:95], a[168:171], v[136:139]\n\t"
"buffer_load_dwordx4 a[24:27], v252, s[8:11], s48 offen offset:2048 sc1\n\t"
"v_mfma_f32_16x16x32_bf16 v[140:143], a[88:91], a[156:159], v[140:143]\n\t"
"ds_write_addtid_b32 v210 offset:20672\n\t"
"v_mfma_f32_16x16x32_bf16 v[140:143], a[92:95], a[172:175], v[140:143]\n\t"
"ds_read_b128 a[168:171], v224 offset:3584\n\t"
"v_mfma_f32_16x16x32_bf16 v[124:127], a[80:83], a[156:159], v[124:127]\n\t"
"ds_write_addtid_b32 v193 offset:25888\n\t"
"v_mfma_f32_16x16x32_bf16 v[124:127], a[84:87], a[172:175], v[124:127]\n\t"
"buffer_load_dwordx4 a[28:31], v252, s[8:11], s48 offen offset:3072 sc1\n\t"
"v_mfma_f32_16x16x32_bf16 v[108:111], a[72:75], a[156:159], v[108:111]\n\t"
"ds_write_addtid_b32 v223 offset:23744\n\t"
"v_mfma_f32_16x16x32_bf16 v[108:111], a[76:79], a[172:175], v[108:111]\n\t"
"s_waitcnt vmcnt(24) lgkmcnt(7)\n\t"
"ds_read_b128 a[156:159], v224 offset:1792\n\t"
"v_xor_b32 v226, 64, v226\n\t"
"v_mfma_f32_16x16x32_bf16 v[48:51], a[96:99], a[144:147], v[48:51]\n\t"
"ds_write_addtid_b32 v208 offset:28992\n\t"
"v_mfma_f32_16x16x32_bf16 v[48:51], a[100:103], a[160:163], v[48:51]\n\t"
"buffer_load_dwordx4 a[64:67], v235, s[8:11], s77 offen offset:0 sc1\n\t"
"v_mfma_f32_16x16x32_bf16 v[64:67], a[104:107], a[144:147], v[64:67]\n\t"
"ds_read_b128 a[172:175], v224 offset:3840\n\t"
"v_mfma_f32_16x16x32_bf16 v[64:67], a[108:111], a[160:163], v[64:67]\n\t"
"s_waitcnt vmcnt(24) lgkmcnt(7)\n\t"
"ds_write_addtid_b32 v194 offset:16544\n\t"
"v_mfma_f32_16x16x32_bf16 v[80:83], a[112:115], a[144:147], v[80:83]\n\t"
"ds_read_b128 a[248:251], v255 offset:59904\n\t"
"v_xor_b32 v227, 64, v227\n\t"
"v_mfma_f32_16x16x32_bf16 v[80:83], a[116:119], a[160:163], v[80:83]\n\t"
"buffer_load_dwordx4 a[68:71], v235, s[8:11], s77 offen offset:1024 sc1\n\t"
"v_mfma_f32_16x16x32_bf16 v[84:87], a[112:115], a[148:151], v[84:87]\n\t"
"ds_read_b128 a[144:147], v224 offset:0\n\t"
"v_mfma_f32_16x16x32_bf16 v[84:87], a[116:119], a[164:167], v[84:87]\n\t"
"ds_write_addtid_b32 v211 offset:21696\n\t"
"v_mfma_f32_16x16x32_bf16 v[68:71], a[104:107], a[148:151], v[68:71]\n\t"
"ds_read_b128 a[160:163], v224 offset:2048\n\t"
"v_mfma_f32_16x16x32_bf16 v[68:71], a[108:111], a[164:167], v[68:71]\n\t"
"buffer_load_dwordx4 a[60:63], v252, s[8:11], s76 offen offset:3072 sc1\n\t"
"v_mfma_f32_16x16x32_bf16 v[52:55], a[96:99], a[148:151], v[52:55]\n\t"
"ds_write_addtid_b32 v200 offset:26912\n\t"
"v_mfma_f32_16x16x32_bf16 v[52:55], a[100:103], a[164:167], v[52:55]\n\t"
"s_waitcnt lgkmcnt(7)\n\t"
"ds_read_b128 a[148:151], v224 offset:256\n\t"
"ds_write_addtid_b32 v221 offset:32064\n\t"
"s_add_u32 s22, s22, 3145728\n\t"
"v_mfma_f32_16x16x32_bf16 v[56:59], a[96:99], a[152:155], v[56:59]\n\t"
"ds_read_b128 a[164:167], v224 offset:2304\n\t"
"v_mfma_f32_16x16x32_bf16 v[56:59], a[100:103], a[168:171], v[56:59]\n\t"
"buffer_load_dwordx4 a[56:59], v252, s[8:11], s76 offen offset:2048 sc1\n\t"
"v_mfma_f32_16x16x32_bf16 v[72:75], a[104:107], a[152:155], v[72:75]\n\t"
"ds_write_addtid_b32 v202 offset:18592\n\t"
"v_mfma_f32_16x16x32_bf16 v[72:75], a[108:111], a[168:171], v[72:75]\n\t"
"ds_write_addtid_b32 v203 offset:19616\n\t"
"v_mfma_f32_16x16x32_bf16 v[88:91], a[112:115], a[152:155], v[88:91]\n\t"
"ds_write_addtid_b32 v220 offset:31040\n\t"
"v_mfma_f32_16x16x32_bf16 v[88:91], a[116:119], a[168:171], v[88:91]\n\t"
"s_waitcnt lgkmcnt(8)\n\t"
"buffer_load_dwordx4 a[48:51], v252, s[8:11], s76 offen offset:0 sc1\n\t"
"v_mfma_f32_16x16x32_bf16 v[92:95], a[112:115], a[156:159], v[92:95]\n\t"
"ds_read_b128 a[168:171], v224 offset:2560\n\t"
"v_mfma_f32_16x16x32_bf16 v[92:95], a[116:119], a[172:175], v[92:95]\n\t"
"s_cmp_eq_u32 s21, 1\n\t"
"s_cselect_b32 s22, 2.0, s22\n\t"
"ds_write_addtid_b32 v192 offset:24864\n\t"
"v_mfma_f32_16x16x32_bf16 v[76:79], a[104:107], a[156:159], v[76:79]\n\t"
"ds_write_addtid_b32 v209 offset:30016\n\t"
"v_mfma_f32_16x16x32_bf16 v[76:79], a[108:111], a[172:175], v[76:79]\n\t"
"buffer_load_dwordx4 a[52:55], v252, s[8:11], s76 offen offset:1024 sc1\n\t"
"v_mfma_f32_16x16x32_bf16 v[60:63], a[96:99], a[156:159], v[60:63]\n\t"
"ds_write_addtid_b32 v201 offset:27936\n\t"
"v_mfma_f32_16x16x32_bf16 v[60:63], a[100:103], a[172:175], v[60:63]\n\t"
"s_waitcnt vmcnt(23) lgkmcnt(7)\n\t"
"ds_read_b128 a[172:175], v224 offset:2816\n\t"
"v_mfma_f32_16x16x32_bf16 v[0:3], a[120:123], a[144:147], v[0:3]\n\t"
"ds_write_addtid_b32 v195 offset:17568\n\t"
"v_mfma_f32_16x16x32_bf16 v[0:3], a[124:127], a[160:163], v[0:3]\n\t"
"buffer_load_dwordx4 a[88:91], v235, s[8:11], s76 offen offset:2048 sc1\n\t"
"v_mfma_f32_16x16x32_bf16 v[16:19], a[128:131], a[144:147], v[16:19]\n\t"
"ds_write_addtid_b32 v222 offset:22720\n\t"
"v_mfma_f32_16x16x32_bf16 v[16:19], a[132:135], a[160:163], v[16:19]\n\t"
"ds_read_b128 a[152:155], v224 offset:512\n\t"
"v_mfma_f32_16x16x32_bf16 v[32:35], a[136:139], a[144:147], v[32:35]\n\t"
"ds_read_b128 a[156:159], v224 offset:768\n\t"
"v_mfma_f32_16x16x32_bf16 v[32:35], a[140:143], a[160:163], v[32:35]\n\t"
"buffer_load_dwordx4 a[92:95], v235, s[8:11], s76 offen offset:3072 sc1\n\t"
"v_mfma_f32_16x16x32_bf16 v[36:39], a[136:139], a[148:151], v[36:39]\n\t"
"v_mfma_f32_16x16x32_bf16 v[36:39], a[140:143], a[164:167], v[36:39]\n\t"
"v_mfma_f32_16x16x32_bf16 v[20:23], a[128:131], a[148:151], v[20:23]\n\t"
"v_mfma_f32_16x16x32_bf16 v[20:23], a[132:135], a[164:167], v[20:23]\n\t"
"buffer_load_dwordx4 a[84:87], v235, s[8:11], s76 offen offset:1024 sc1\n\t"
"v_mfma_f32_16x16x32_bf16 v[4:7], a[120:123], a[148:151], v[4:7]\n\t"
"v_mfma_f32_16x16x32_bf16 v[4:7], a[124:127], a[164:167], v[4:7]\n\t"
"s_waitcnt vmcnt(14) lgkmcnt(3)\n\t"
"s_waitcnt lgkmcnt(0)\n\t"
"s_barrier\n\t"
"v_mfma_f32_16x16x32_bf16 v[8:11], a[120:123], a[152:155], v[8:11]\n\t"
"s_nop 0\n\t"
"ds_read_b128 a[144:147], v224 offset:17568\n\t"
"ds_read_b128 a[148:151], v224 offset:17824\n\t"
"v_mfma_f32_16x16x32_bf16 v[8:11], a[124:127], a[168:171], v[8:11]\n\t"
"buffer_load_dwordx4 a[80:83], v235, s[8:11], s76 offen offset:0 sc1\n\t"
"v_mfma_f32_16x16x32_bf16 v[24:27], a[128:131], a[152:155], v[24:27]\n\t"
"s_nop 0\n\t"
"ds_read_b128 a[160:163], v224 offset:19616\n\t"
"ds_read_b128 a[164:167], v224 offset:19872\n\t"
"v_mfma_f32_16x16x32_bf16 v[24:27], a[132:135], a[168:171], v[24:27]\n\t"
"buffer_load_dwordx4 a[72:75], v252, s[8:11], s75 offen offset:2048 sc1\n\t"
"v_mfma_f32_16x16x32_bf16 v[40:43], a[136:139], a[152:155], v[40:43]\n\t"
"s_nop 0\n\t"
"ds_read_b64_tr_b16 a[176:177], v248 offset:41472\n\t"
"ds_read_b64_tr_b16 a[178:179], v249 offset:41472\n\t"
"v_mfma_f32_16x16x32_bf16 v[40:43], a[140:143], a[168:171], v[40:43]\n\t"
"buffer_load_dwordx4 a[76:79], v252, s[8:11], s75 offen offset:3072 sc1\n\t"
"v_mfma_f32_16x16x32_bf16 v[44:47], a[136:139], a[156:159], v[44:47]\n\t"
"s_nop 0\n\t"
"ds_read_b64_tr_b16 a[180:181], v248 offset:41984\n\t"
"ds_read_b64_tr_b16 a[182:183], v249 offset:41984\n\t"
"v_mfma_f32_16x16x32_bf16 v[44:47], a[140:143], a[172:175], v[44:47]\n\t"
"s_nop 0\n\t"
"ds_read_b64_tr_b16 a[184:185], v250 offset:43776\n\t"
"ds_read_b64_tr_b16 a[186:187], v251 offset:43776\n\t"
"v_mfma_f32_16x16x32_bf16 v[28:31], a[128:131], a[156:159], v[28:31]\n\t"
"ds_read_b64_tr_b16 a[188:189], v250 offset:44288\n\t"
"v_mfma_f32_16x16x32_bf16 v[28:31], a[132:135], a[172:175], v[28:31]\n\t"
"buffer_load_dwordx4 a[112:115], v235, s[8:11], s80 offen offset:0 sc1\n\t"
"v_mfma_f32_16x16x32_bf16 v[12:15], a[120:123], a[156:159], v[12:15]\n\t"
"s_nop 0\n\t"
"ds_read_b128 a[152:155], v224 offset:18080\n\t"
"ds_read_b128 a[156:159], v224 offset:18336\n\t"
"v_mfma_f32_16x16x32_bf16 v[12:15], a[124:127], a[172:175], v[12:15]\n\t"
"s_waitcnt lgkmcnt(5)\n\t"
"ds_read_b128 a[172:175], v224 offset:20384\n\t"
"ds_read_b64_tr_b16 a[190:191], v251 offset:44288\n\t"
"v_mfma_f32_4x4x4_16b_bf16 v[192:195], a[210:211], a[176:177], 0\n\t"
"ds_read_b64_tr_b16 a[192:193], v250 offset:41472\n\t"
"v_mfma_f32_4x4x4_16b_bf16 v[196:199], a[210:211], a[178:179], 0\n\t"
"ds_read_b64_tr_b16 a[194:195], v251 offset:41472\n\t"
"v_mfma_f32_4x4x4_16b_bf16 v[200:203], a[210:211], a[180:181], 0\n\t"
"ds_read_b128 a[168:171], v224 offset:20128\n\t"
"v_mfma_f32_4x4x4_16b_bf16 v[204:207], a[210:211], a[182:183], 0\n\t"
"buffer_load_dwordx4 a[116:119], v235, s[8:11], s80 offen offset:1024 sc1\n\t"
"v_mfma_f32_16x16x32_bf16 v[144:147], a[0:3], a[144:147], v[144:147]\n\t"
"ds_read_b128 a[244:247], v255 offset:39168\n\t"
"v_mfma_f32_16x16x32_bf16 v[144:147], a[4:7], a[160:163], v[144:147]\n\t"
"s_nop 0\n\t"
"ds_read_b64_tr_b16 a[196:197], v250 offset:41984\n\t"
"ds_read_b64_tr_b16 a[198:199], v251 offset:41984\n\t"
"v_mfma_f32_16x16x32_bf16 v[160:163], a[8:11], a[144:147], v[160:163]\n\t"
"s_nop 0\n\t"
"ds_read_b64_tr_b16 a[200:201], v248 offset:43776\n\t"
"ds_read_b64_tr_b16 a[202:203], v249 offset:43776\n\t"
"v_mfma_f32_16x16x32_bf16 v[160:163], a[12:15], a[160:163], v[160:163]\n\t"
"buffer_load_dwordx4 a[108:111], v252, s[8:11], s79 offen offset:3072 sc1\n\t"
"v_mfma_f32_16x16x32_bf16 v[176:179], a[16:19], a[144:147], v[176:179]\n\t"
"ds_read_b64_tr_b16 a[204:205], v248 offset:44288\n\t"
"v_cvt_pk_bf16_f32 v197, v193, v197\n\t"
"v_mfma_f32_16x16x32_bf16 v[176:179], a[20:23], a[160:163], v[176:179]\n\t"
"ds_read_b128 a[144:147], v224 offset:16544\n\t"
"v_cvt_pk_bf16_f32 v195, v195, v199\n\t"
"v_mfma_f32_16x16x32_bf16 v[180:183], a[16:19], a[148:151], v[180:183]\n\t"
"v_cvt_pk_bf16_f32 v196, v192, v196\n\t"
"v_cvt_pk_bf16_f32 v199, v201, v205\n\t"
"v_mfma_f32_16x16x32_bf16 v[180:183], a[20:23], a[164:167], v[180:183]\n\t"
"s_waitcnt lgkmcnt(8)\n\t"
"ds_read_b64_tr_b16 a[206:207], v249 offset:44288\n\t"
"v_cvt_pk_bf16_f32 v192, v202, v206\n\t"
"v_mfma_f32_4x4x4_16b_bf16 v[208:211], a[210:211], a[184:185], 0\n\t"
"ds_write_addtid_b32 v199 offset:9344\n\t"
"v_mfma_f32_4x4x4_16b_bf16 v[212:215], a[210:211], a[186:187], 0\n\t"
"ds_read_b128 a[160:163], v224 offset:18592\n\t"
"v_mfma_f32_4x4x4_16b_bf16 v[216:219], a[210:211], a[188:189], 0\n\t"
"ds_write_addtid_b32 v197 offset:1024\n\t"
"v_mfma_f32_4x4x4_16b_bf16 v[220:223], a[210:211], a[190:191], 0\n\t"
"buffer_load_dwordx4 a[104:107], v252, s[8:11], s79 offen offset:2048 sc1\n\t"
"v_mfma_f32_16x16x32_bf16 v[164:167], a[8:11], a[148:151], v[164:167]\n\t"
"v_cvt_pk_bf16_f32 v194, v194, v198\n\t"
"v_cvt_pk_bf16_f32 v198, v200, v204\n\t"
"v_mfma_f32_16x16x32_bf16 v[164:167], a[12:15], a[164:167], v[164:167]\n\t"
"buffer_load_dwordx4 a[96:99], v252, s[8:11], s79 offen offset:0 sc1\n\t"
"v_mfma_f32_16x16x32_bf16 v[148:151], a[0:3], a[148:151], v[148:151]\n\t"
"ds_write_addtid_b32 v198 offset:8320\n\t"
"v_cvt_pk_bf16_f32 v193, v203, v207\n\t"
"v_mfma_f32_16x16x32_bf16 v[148:151], a[4:7], a[164:167], v[148:151]\n\t"
"s_waitcnt lgkmcnt(5)\n\t"
"buffer_load_dwordx4 a[100:103], v252, s[8:11], s79 offen offset:1024 sc1\n\t"
"v_mfma_f32_16x16x32_bf16 v[152:155], a[0:3], a[152:155], v[152:155]\n\t"
"v_cvt_pk_bf16_f32 v212, v208, v212\n\t"
"v_cvt_pk_bf16_f32 v210, v210, v214\n\t"
"v_mfma_f32_16x16x32_bf16 v[152:155], a[4:7], a[168:171], v[152:155]\n\t"
"ds_write_addtid_b32 v196 offset:0\n\t"
"v_cvt_pk_bf16_f32 v208, v218, v222\n\t"
"v_mfma_f32_16x16x32_bf16 v[168:171], a[8:11], a[152:155], v[168:171]\n\t"
"ds_read_b128 a[164:167], v224 offset:18848\n\t"
"v_cvt_pk_bf16_f32 v211, v211, v215\n\t"
"v_mfma_f32_16x16x32_bf16 v[168:171], a[12:15], a[168:171], v[168:171]\n\t"
"ds_read_b128 a[148:151], v224 offset:16800\n\t"
"v_cvt_pk_bf16_f32 v214, v216, v220\n\t"
"v_mfma_f32_4x4x4_16b_bf16 v[200:203], a[210:211], a[192:193], 0\n\t"
"v_cvt_pk_bf16_f32 v213, v209, v213\n\t"
"v_mfma_f32_4x4x4_16b_bf16 v[204:207], a[210:211], a[194:195], 0\n\t"
"v_cvt_pk_bf16_f32 v215, v217, v221\n\t"
"v_mfma_f32_4x4x4_16b_bf16 v[196:199], a[210:211], a[196:197], 0\n\t"
"v_cvt_pk_bf16_f32 v209, v219, v223\n\t"
"v_mfma_f32_4x4x4_16b_bf16 v[216:219], a[210:211], a[198:199], 0\n\t"
"buffer_load_dwordx4 a[136:139], v235, s[8:11], s79 offen offset:2048 sc1\n\t"
"v_mfma_f32_16x16x32_bf16 v[184:187], a[16:19], a[152:155], v[184:187]\n\t"
"s_nop 0\n\t"
"ds_write_addtid_b32 v213 offset:5152\n\t"
"ds_write_addtid_b32 v215 offset:13472\n\t"
"v_mfma_f32_16x16x32_bf16 v[184:187], a[20:23], a[168:171], v[184:187]\n\t"
"buffer_load_dwordx4 a[140:143], v235, s[8:11], s79 offen offset:3072 sc1\n\t"
"v_mfma_f32_16x16x32_bf16 v[188:191], a[16:19], a[156:159], v[188:191]\n\t"
"s_nop 0\n\t"
"ds_read_b128 a[168:171], v224 offset:19104\n\t"
"ds_write_addtid_b32 v214 offset:12448\n\t"
"v_mfma_f32_16x16x32_bf16 v[188:191], a[20:23], a[172:175], v[188:191]\n\t"
"ds_read_b128 a[152:155], v224 offset:17056\n\t"
"v_cvt_pk_bf16_f32 v203, v203, v207\n\t"
"v_mfma_f32_16x16x32_bf16 v[172:175], a[8:11], a[156:159], v[172:175]\n\t"
"ds_write_addtid_b32 v212 offset:4128\n\t"
"v_cvt_pk_bf16_f32 v204, v200, v204\n\t"
"v_mfma_f32_16x16x32_bf16 v[172:175], a[12:15], a[172:175], v[172:175]\n\t"
"buffer_load_dwordx4 a[132:135], v235, s[8:11], s79 offen offset:1024 sc1\n\t"
"v_cvt_pk_bf16_f32 v205, v201, v205\n\t"
"v_mfma_f32_16x16x32_bf16 v[156:159], a[0:3], a[156:159], v[156:159]\n\t"
"v_cvt_pk_bf16_f32 v202, v202, v206\n\t"
"v_cvt_pk_bf16_f32 v207, v197, v217\n\t"
"v_mfma_f32_16x16x32_bf16 v[156:159], a[4:7], a[172:175], v[156:159]\n\t"
"s_waitcnt lgkmcnt(6)\n\t"
"ds_read_b128 a[172:175], v224 offset:19360\n\t"
"v_cvt_pk_bf16_f32 v206, v196, v216\n\t"
"v_mfma_f32_4x4x4_16b_bf16 v[220:223], a[210:211], a[200:201], 0\n\t"
"v_cvt_pk_bf16_f32 v200, v198, v218\n\t"
"v_mfma_f32_4x4x4_16b_bf16 v[212:215], a[210:211], a[202:203], 0\n\t"
"v_cvt_pk_bf16_f32 v201, v199, v219\n\t"
"v_mfma_f32_4x4x4_16b_bf16 v[196:199], a[210:211], a[204:205], 0\n\t"
"ds_read_b128 a[156:159], v224 offset:17312\n\t"
"v_mfma_f32_4x4x4_16b_bf16 v[216:219], a[210:211], a[206:207], 0\n\t"
"s_waitcnt vmcnt(21) lgkmcnt(6)\n\t"
"buffer_load_dwordx4 a[128:131], v235, s[8:11], s79 offen offset:0 sc1\n\t"
"v_mfma_f32_16x16x32_bf16 v[96:99], a[24:27], a[144:147], v[96:99]\n\t"
"s_nop 0\n\t"
"ds_write_addtid_b32 v204 offset:2048\n\t"
"ds_write_addtid_b32 v207 offset:11392\n\t"
"v_mfma_f32_16x16x32_bf16 v[96:99], a[28:31], a[160:163], v[96:99]\n\t"
"s_nop 0\n\t"
"ds_write_addtid_b32 v206 offset:10368\n\t"
"ds_write_addtid_b32 v205 offset:3072\n\t"
"v_mfma_f32_16x16x32_bf16 v[112:115], a[32:35], a[144:147], v[112:115]\n\t"
"buffer_load_dwordx4 a[120:123], v252, s[8:11], s78 offen offset:2048 sc1\n\t"
"v_mfma_f32_16x16x32_bf16 v[112:115], a[36:39], a[160:163], v[112:115]\n\t"
"v_cvt_pk_bf16_f32 v222, v222, v214\n\t"
"v_cvt_pk_bf16_f32 v223, v223, v215\n\t"
"v_mfma_f32_16x16x32_bf16 v[128:131], a[40:43], a[144:147], v[128:131]\n\t"
"v_cvt_pk_bf16_f32 v212, v220, v212\n\t"
"v_cvt_pk_bf16_f32 v213, v221, v213\n\t"
"v_mfma_f32_16x16x32_bf16 v[128:131], a[44:47], a[160:163], v[128:131]\n\t"
"s_nop 0\n\t"
"ds_write_addtid_b32 v212 offset:6176\n\t"
"ds_write_addtid_b32 v213 offset:7200\n\t"
"v_cvt_pk_bf16_f32 v214, v196, v216\n\t"
"v_cvt_pk_bf16_f32 v215, v197, v217\n\t"
"v_mfma_f32_16x16x32_bf16 v[132:135], a[40:43], a[148:151], v[132:135]\n\t"
"s_nop 0\n\t"
"ds_write_addtid_b32 v214 offset:14496\n\t"
"ds_write_addtid_b32 v215 offset:15520\n\t"
"v_mfma_f32_16x16x32_bf16 v[132:135], a[44:47], a[164:167], v[132:135]\n\t"
"buffer_load_dwordx4 a[124:127], v252, s[8:11], s78 offen offset:3072 sc1\n\t"
"v_mfma_f32_16x16x32_bf16 v[116:119], a[32:35], a[148:151], v[116:119]\n\t"
"v_cvt_pk_bf16_f32 v220, v198, v218\n\t"
"v_cvt_pk_bf16_f32 v221, v199, v219\n\t"
"v_mfma_f32_16x16x32_bf16 v[116:119], a[36:39], a[164:167], v[116:119]\n\t"
"v_mfma_f32_16x16x32_bf16 v[100:103], a[24:27], a[148:151], v[100:103]\n\t"
"s_waitcnt lgkmcnt(0)\n\t"
"buffer_load_dwordx4 a[16:19], v235, s[8:11], s74 offen offset:0 sc1\n\t"
"v_mfma_f32_16x16x32_bf16 v[100:103], a[28:31], a[164:167], v[100:103]\n\t"
"s_barrier\n\t"
"v_mfma_f32_16x16x32_bf16 v[104:107], a[24:27], a[152:155], v[104:107]\n\t"
"ds_read_b128 a[164:167], v224 offset:3328\n\t"
"v_add_u32 v248, 101632, v225\n\t"
"v_mfma_f32_16x16x32_bf16 v[104:107], a[28:31], a[168:171], v[104:107]\n\t"
"s_nop 0\n\t"
"ds_read_b128 a[144:147], v224 offset:1024\n\t"
"ds_read_b128 a[148:151], v224 offset:1280\n\t"
"v_mfma_f32_16x16x32_bf16 v[120:123], a[32:35], a[152:155], v[120:123]\n\t"
"ds_read_b128 a[160:163], v224 offset:3072\n\t"
"v_xor_b32 v249, 32, v248\n\t"
"v_mfma_f32_16x16x32_bf16 v[120:123], a[36:39], a[168:171], v[120:123]\n\t"
"buffer_load_dwordx4 a[20:23], v235, s[8:11], s74 offen offset:1024 sc1\n\t"
"v_mfma_f32_16x16x32_bf16 v[136:139], a[40:43], a[152:155], v[136:139]\n\t"
"ds_write_addtid_b32 v202 offset:18592\n\t"
"v_mfma_f32_16x16x32_bf16 v[136:139], a[44:47], a[168:171], v[136:139]\n\t"
"ds_write_addtid_b32 v193 offset:25888\n\t"
"v_xor_b32 v250, 64, v248\n\t"
"v_mfma_f32_16x16x32_bf16 v[140:143], a[40:43], a[156:159], v[140:143]\n\t"
"ds_read_b128 a[168:171], v224 offset:3584\n\t"
"v_xor_b32 v251, 96, v248\n\t"
"v_mfma_f32_16x16x32_bf16 v[140:143], a[44:47], a[172:175], v[140:143]\n\t"
"buffer_load_dwordx4 a[12:15], v252, s[8:11], s72 offen offset:3072 sc1\n\t"
"v_mfma_f32_16x16x32_bf16 v[124:127], a[32:35], a[156:159], v[124:127]\n\t"
"s_nop 0\n\t"
"ds_read_b128 a[152:155], v224 offset:1536\n\t"
"ds_write_addtid_b32 v194 offset:16544\n\t"
"v_mfma_f32_16x16x32_bf16 v[124:127], a[36:39], a[172:175], v[124:127]\n\t"
"buffer_load_dwordx4 a[8:11], v252, s[8:11], s72 offen offset:2048 sc1\n\t"
"v_mfma_f32_16x16x32_bf16 v[108:111], a[24:27], a[156:159], v[108:111]\n\t"
"s_nop 0\n\t"
"ds_read_b128 a[156:159], v224 offset:1792\n\t"
"ds_write_addtid_b32 v222 offset:22720\n\t"
"v_mfma_f32_16x16x32_bf16 v[108:111], a[28:31], a[172:175], v[108:111]\n\t"
"s_waitcnt vmcnt(22) lgkmcnt(7)\n\t"
"ds_read_b64_tr_b16 a[178:179], v250 offset:43776\n\t"
"v_mfma_f32_16x16x32_bf16 v[48:51], a[48:51], a[144:147], v[48:51]\n\t"
"ds_write_addtid_b32 v208 offset:28992\n\t"
"v_mfma_f32_16x16x32_bf16 v[48:51], a[52:55], a[160:163], v[48:51]\n\t"
"buffer_load_dwordx4 a[0:3], v252, s[8:11], s72 offen offset:0 sc1\n\t"
"v_mfma_f32_16x16x32_bf16 v[64:67], a[56:59], a[144:147], v[64:67]\n\t"
"s_nop 0\n\t"
"ds_read_b128 a[172:175], v224 offset:3840\n\t"
"ds_read_b128 a[240:243], v255 offset:18432\n\t"
"v_mfma_f32_16x16x32_bf16 v[64:67], a[60:63], a[160:163], v[64:67]\n\t"
"ds_write_addtid_b32 v200 offset:26912\n\t"
"v_mfma_f32_16x16x32_bf16 v[80:83], a[64:67], a[144:147], v[80:83]\n\t"
"ds_write_addtid_b32 v192 offset:24864\n\t"
"v_mfma_f32_16x16x32_bf16 v[80:83], a[68:71], a[160:163], v[80:83]\n\t"
"buffer_load_dwordx4 a[4:7], v252, s[8:11], s72 offen offset:1024 sc1\n\t"
"v_mfma_f32_16x16x32_bf16 v[84:87], a[64:67], a[148:151], v[84:87]\n\t"
"ds_read_b128 a[144:147], v224 offset:0\n\t"
"v_mfma_f32_16x16x32_bf16 v[84:87], a[68:71], a[164:167], v[84:87]\n\t"
"ds_read_b128 a[160:163], v224 offset:2048\n\t"
"v_mfma_f32_16x16x32_bf16 v[68:71], a[56:59], a[148:151], v[68:71]\n\t"
"ds_write_addtid_b32 v220 offset:31040\n\t"
"v_mfma_f32_16x16x32_bf16 v[68:71], a[60:63], a[164:167], v[68:71]\n\t"
"buffer_load_dwordx4 a[40:43], v235, s[8:11], s72 offen offset:2048 sc1\n\t"
"v_mfma_f32_16x16x32_bf16 v[52:55], a[48:51], a[148:151], v[52:55]\n\t"
"ds_read_b64_tr_b16 a[186:187], v251 offset:43776\n\t"
"v_mfma_f32_16x16x32_bf16 v[52:55], a[52:55], a[164:167], v[52:55]\n\t"
"s_waitcnt lgkmcnt(8)\n\t"
"ds_read_b128 a[148:151], v224 offset:256\n\t"
"v_mfma_f32_16x16x32_bf16 v[56:59], a[48:51], a[152:155], v[56:59]\n\t"
"s_nop 0\n\t"
"ds_read_b128 a[164:167], v224 offset:2304\n\t"
"ds_write_addtid_b32 v210 offset:20672\n\t"
"v_mfma_f32_16x16x32_bf16 v[56:59], a[52:55], a[168:171], v[56:59]\n\t"
"buffer_load_dwordx4 a[44:47], v235, s[8:11], s72 offen offset:3072 sc1\n\t"
"v_mfma_f32_16x16x32_bf16 v[72:75], a[56:59], a[152:155], v[72:75]\n\t"
"s_nop 0\n\t"
"ds_write_addtid_b32 v201 offset:27936\n\t"
"ds_read_b64_tr_b16 a[194:195], v248 offset:43776\n\t"
"v_mfma_f32_16x16x32_bf16 v[72:75], a[60:63], a[168:171], v[72:75]\n\t"
"ds_read_b64_tr_b16 a[202:203], v249 offset:43776\n\t"
"v_mfma_f32_16x16x32_bf16 v[88:91], a[64:67], a[152:155], v[88:91]\n\t"
"s_nop 0\n\t"
"ds_write_addtid_b32 v195 offset:17568\n\t"
"ds_write_addtid_b32 v221 offset:32064\n\t"
"v_mfma_f32_16x16x32_bf16 v[88:91], a[68:71], a[168:171], v[88:91]\n\t"
"s_waitcnt lgkmcnt(10)\n\t"
"buffer_load_dwordx4 a[36:39], v235, s[8:11], s72 offen offset:1024 sc1\n\t"
"v_mfma_f32_16x16x32_bf16 v[92:95], a[64:67], a[156:159], v[92:95]\n\t"
"ds_write_addtid_b32 v223 offset:23744\n\t"
"v_mfma_f32_16x16x32_bf16 v[92:95], a[68:71], a[172:175], v[92:95]\n\t"
"s_nop 0\n\t"
"ds_read_b128 a[152:155], v224 offset:512\n\t"
"ds_read_b128 a[168:171], v224 offset:2560\n\t"
"v_mfma_f32_16x16x32_bf16 v[76:79], a[56:59], a[156:159], v[76:79]\n\t"
"ds_write_addtid_b32 v209 offset:30016\n\t"
"v_mfma_f32_16x16x32_bf16 v[76:79], a[60:63], a[172:175], v[76:79]\n\t"
"buffer_load_dwordx4 a[32:35], v235, s[8:11], s72 offen offset:0 sc1\n\t"
"v_mfma_f32_16x16x32_bf16 v[60:63], a[48:51], a[156:159], v[60:63]\n\t"
"ds_read_b64_tr_b16 a[176:177], v248 offset:41472\n\t"
"v_mfma_f32_16x16x32_bf16 v[60:63], a[52:55], a[172:175], v[60:63]\n\t"
"s_waitcnt vmcnt(22) lgkmcnt(10)\n\t"
"ds_write_addtid_b32 v211 offset:21696\n\t"
"v_mfma_f32_16x16x32_bf16 v[0:3], a[72:75], a[144:147], v[0:3]\n\t"
"s_nop 0\n\t"
"ds_read_b128 a[172:175], v224 offset:2816\n\t"
"ds_write_addtid_b32 v203 offset:19616\n\t"
"v_mfma_f32_16x16x32_bf16 v[0:3], a[76:79], a[160:163], v[0:3]\n\t"
"buffer_load_dwordx4 a[24:27], v252, s[8:11], s69 offen offset:2048 sc1\n\t"
"v_mfma_f32_16x16x32_bf16 v[16:19], a[80:83], a[144:147], v[16:19]\n\t"
"ds_read_b64_tr_b16 a[184:185], v249 offset:41472\n\t"
"v_mfma_f32_16x16x32_bf16 v[16:19], a[84:87], a[160:163], v[16:19]\n\t"
"ds_read_b128 a[156:159], v224 offset:768\n\t"
"v_mfma_f32_16x16x32_bf16 v[32:35], a[88:91], a[144:147], v[32:35]\n\t"
"ds_read_b64_tr_b16 a[180:181], v248 offset:46080\n\t"
"v_mfma_f32_16x16x32_bf16 v[32:35], a[92:95], a[160:163], v[32:35]\n\t"
"buffer_load_dwordx4 a[28:31], v252, s[8:11], s69 offen offset:3072 sc1\n\t"
"v_mfma_f32_16x16x32_bf16 v[36:39], a[88:91], a[148:151], v[36:39]\n\t"
"ds_read_b64_tr_b16 a[188:189], v249 offset:46080\n\t"
"v_mfma_f32_16x16x32_bf16 v[36:39], a[92:95], a[164:167], v[36:39]\n\t"
"s_nop 0\n\t"
"ds_read_b64_tr_b16 a[192:193], v250 offset:41472\n\t"
"ds_read_b64_tr_b16 a[200:201], v251 offset:41472\n\t"
"v_mfma_f32_16x16x32_bf16 v[20:23], a[80:83], a[148:151], v[20:23]\n\t"
"s_nop 0\n\t"
"ds_read_b64_tr_b16 a[196:197], v250 offset:46080\n\t"
"ds_read_b64_tr_b16 a[204:205], v251 offset:46080\n\t"
"v_mfma_f32_16x16x32_bf16 v[20:23], a[84:87], a[164:167], v[20:23]\n\t"
"buffer_load_dwordx4 a[64:67], v235, s[8:11], s83 offen offset:0 sc1\n\t"
"v_mfma_f32_16x16x32_bf16 v[4:7], a[72:75], a[148:151], v[4:7]\n\t"
"s_waitcnt lgkmcnt(6)\n\t"
"v_mfma_f32_16x16x32_bf16 v[4:7], a[76:79], a[164:167], v[4:7]\n\t"
"s_barrier\n\t"
"v_mfma_f32_16x16x32_bf16 v[8:11], a[72:75], a[152:155], v[8:11]\n\t"
"s_nop 0\n\t"
"ds_read_b128 a[144:147], v224 offset:17568\n\t"
"ds_read_b128 a[148:151], v224 offset:17824\n\t"
"v_mfma_f32_16x16x32_bf16 v[8:11], a[76:79], a[168:171], v[8:11]\n\t"
"buffer_load_dwordx4 a[68:71], v235, s[8:11], s83 offen offset:1024 sc1\n\t"
"v_mfma_f32_16x16x32_bf16 v[24:27], a[80:83], a[152:155], v[24:27]\n\t"
"ds_read_b128 a[160:163], v224 offset:19616\n\t"
"v_mfma_f32_16x16x32_bf16 v[24:27], a[84:87], a[168:171], v[24:27]\n\t"
"ds_read_b128 a[164:167], v224 offset:19872\n\t"
"v_mfma_f32_16x16x32_bf16 v[40:43], a[88:91], a[152:155], v[40:43]\n\t"
"v_mfma_f32_16x16x32_bf16 v[40:43], a[92:95], a[168:171], v[40:43]\n\t"
"buffer_load_dwordx4 a[60:63], v252, s[8:11], s82 offen offset:3072 sc1\n\t"
"v_mfma_f32_16x16x32_bf16 v[44:47], a[88:91], a[156:159], v[44:47]\n\t"
"ds_read_b128 a[168:171], v224 offset:20128\n\t"
"v_mfma_f32_16x16x32_bf16 v[44:47], a[92:95], a[172:175], v[44:47]\n\t"
"buffer_load_dwordx4 a[56:59], v252, s[8:11], s82 offen offset:2048 sc1\n\t"
"v_mfma_f32_16x16x32_bf16 v[28:31], a[80:83], a[156:159], v[28:31]\n\t"
"v_mfma_f32_16x16x32_bf16 v[28:31], a[84:87], a[172:175], v[28:31]\n\t"
"buffer_load_dwordx4 a[48:51], v252, s[8:11], s82 offen offset:0 sc1\n\t"
"v_mfma_f32_16x16x32_bf16 v[12:15], a[72:75], a[156:159], v[12:15]\n\t"
"ds_read_b128 a[156:159], v224 offset:18336\n\t"
"v_mfma_f32_16x16x32_bf16 v[12:15], a[76:79], a[172:175], v[12:15]\n\t"
"s_waitcnt lgkmcnt(4)\n\t"
"ds_read_b128 a[172:175], v224 offset:20384\n\t"
"v_mfma_f32_4x4x4_16b_bf16 v[200:203], a[220:221], a[178:179], 0\n\t"
"ds_read_b64_tr_b16 a[178:179], v250 offset:44928\n\t"
"v_mfma_f32_4x4x4_16b_bf16 v[204:207], a[220:221], a[186:187], 0\n\t"
"ds_read_b64_tr_b16 a[186:187], v251 offset:44928\n\t"
"v_mfma_f32_4x4x4_16b_bf16 v[216:219], a[220:221], a[194:195], 0\n\t"
"ds_read_b64_tr_b16 a[194:195], v248 offset:44928\n\t"
"v_mfma_f32_4x4x4_16b_bf16 v[220:223], a[220:221], a[202:203], 0\n\t"
"ds_read_b64_tr_b16 a[202:203], v249 offset:44928\n\t"
"v_mfma_f32_4x4x4_16b_bf16 v[192:195], a[222:223], a[176:177], v[200:203]\n\t"
"ds_read_b128 a[152:155], v224 offset:18080\n\t"
"v_mfma_f32_4x4x4_16b_bf16 v[196:199], a[222:223], a[184:185], v[204:207]\n\t"
"ds_read_b64_tr_b16 a[176:177], v248 offset:42624\n\t"
"v_mfma_f32_4x4x4_16b_bf16 v[200:203], a[222:223], a[180:181], v[200:203]\n\t"
"ds_read_b64_tr_b16 a[184:185], v249 offset:42624\n\t"
"v_mfma_f32_4x4x4_16b_bf16 v[204:207], a[222:223], a[188:189], v[204:207]\n\t"
"ds_read_b64_tr_b16 a[180:181], v248 offset:47232\n\t"
"v_mfma_f32_4x4x4_16b_bf16 v[208:211], a[222:223], a[192:193], v[216:219]\n\t"
"ds_read_b64_tr_b16 a[188:189], v249 offset:47232\n\t"
"v_mfma_f32_4x4x4_16b_bf16 v[212:215], a[222:223], a[200:201], v[220:223]\n\t"
"ds_read_b64_tr_b16 a[192:193], v250 offset:42624\n\t"
"v_mfma_f32_4x4x4_16b_bf16 v[216:219], a[222:223], a[196:197], v[216:219]\n\t"
"ds_read_b64_tr_b16 a[200:201], v251 offset:42624\n\t"
"v_mfma_f32_4x4x4_16b_bf16 v[220:223], a[222:223], a[204:205], v[220:223]\n\t"
"s_waitcnt vmcnt(23) lgkmcnt(9)\n\t"
"buffer_load_dwordx4 a[52:55], v252, s[8:11], s82 offen offset:1024 sc1\n\t"
"v_mfma_f32_16x16x32_bf16 v[144:147], a[96:99], a[144:147], v[144:147]\n\t"
"v_cvt_pk_bf16_f32 v193, -v193, -v197\n\t"
"v_cvt_pk_bf16_f32 v194, -v194, -v198\n\t"
"v_mfma_f32_16x16x32_bf16 v[144:147], a[100:103], a[160:163], v[144:147]\n\t"
"buffer_load_dwordx4 a[88:91], v235, s[8:11], s82 offen offset:2048 sc1\n\t"
"v_mfma_f32_16x16x32_bf16 v[160:163], a[104:107], a[144:147], v[160:163]\n\t"
"ds_write_addtid_b32 v193 offset:8320\n\t"
"v_cvt_pk_bf16_f32 v201, v201, v205\n\t"
"v_mfma_f32_16x16x32_bf16 v[160:163], a[108:111], a[160:163], v[160:163]\n\t"
"buffer_load_dwordx4 a[92:95], v235, s[8:11], s82 offen offset:3072 sc1\n\t"
"v_mfma_f32_16x16x32_bf16 v[176:179], a[112:115], a[144:147], v[176:179]\n\t"
"ds_read_b128 a[144:147], v224 offset:16544\n\t"
"v_cvt_pk_bf16_f32 v208, -v208, -v212\n\t"
"v_mfma_f32_16x16x32_bf16 v[176:179], a[116:119], a[160:163], v[176:179]\n\t"
"buffer_load_dwordx4 a[84:87], v235, s[8:11], s82 offen offset:1024 sc1\n\t"
"v_mfma_f32_16x16x32_bf16 v[180:183], a[112:115], a[148:151], v[180:183]\n\t"
"ds_write_addtid_b32 v194 offset:1024\n\t"
"v_cvt_pk_bf16_f32 v200, v200, v204\n\t"
"v_mfma_f32_16x16x32_bf16 v[180:183], a[116:119], a[164:167], v[180:183]\n\t"
"ds_write_addtid_b32 v200 offset:4128\n\t"
"v_cvt_pk_bf16_f32 v218, v218, v222\n\t"
"v_mfma_f32_16x16x32_bf16 v[164:167], a[104:107], a[148:151], v[164:167]\n\t"
"ds_read_b128 a[160:163], v224 offset:18592\n\t"
"v_cvt_pk_bf16_f32 v192, -v192, -v196\n\t"
"v_mfma_f32_16x16x32_bf16 v[164:167], a[108:111], a[164:167], v[164:167]\n\t"
"buffer_load_dwordx4 a[80:83], v235, s[8:11], s82 offen offset:0 sc1\n\t"
"v_mfma_f32_16x16x32_bf16 v[148:151], a[96:99], a[148:151], v[148:151]\n\t"
"ds_read_b128 a[148:151], v224 offset:16800\n\t"
"v_cvt_pk_bf16_f32 v216, v216, v220\n\t"
"v_mfma_f32_16x16x32_bf16 v[148:151], a[100:103], a[164:167], v[148:151]\n\t"
"s_waitcnt lgkmcnt(6)\n\t"
"ds_read_b128 a[164:167], v224 offset:18848\n\t"
"v_cvt_pk_bf16_f32 v202, v202, v206\n\t"
"v_mfma_f32_16x16x32_bf16 v[152:155], a[96:99], a[152:155], v[152:155]\n\t"
"ds_write_addtid_b32 v218 offset:7200\n\t"
"v_cvt_pk_bf16_f32 v209, -v209, -v213\n\t"
"v_mfma_f32_16x16x32_bf16 v[152:155], a[100:103], a[168:171], v[152:155]\n\t"
"buffer_load_dwordx4 a[72:75], v252, s[8:11], s81 offen offset:2048 sc1\n\t"
"v_mfma_f32_16x16x32_bf16 v[168:171], a[104:107], a[152:155], v[168:171]\n\t"
"ds_write_addtid_b32 v209 offset:10368\n\t"
"v_cvt_pk_bf16_f32 v210, -v210, -v214\n\t"
"v_mfma_f32_16x16x32_bf16 v[168:171], a[108:111], a[168:171], v[168:171]\n\t"
"ds_write_addtid_b32 v210 offset:3072\n\t"
"v_cvt_pk_bf16_f32 v203, v203, v207\n\t"
"v_mfma_f32_16x16x32_bf16 v[184:187], a[112:115], a[152:155], v[184:187]\n\t"
"ds_write_addtid_b32 v208 offset:2048\n\t"
"v_cvt_pk_bf16_f32 v211, -v211, -v215\n\t"
"v_mfma_f32_16x16x32_bf16 v[184:187], a[116:119], a[168:171], v[184:187]\n\t"
"buffer_load_dwordx4 a[76:79], v252, s[8:11], s81 offen offset:3072 sc1\n\t"
"v_mfma_f32_16x16x32_bf16 v[188:191], a[112:115], a[156:159], v[188:191]\n\t"
"s_nop 0\n\t"
"ds_read_b128 a[152:155], v224 offset:17056\n\t"
"ds_write_addtid_b32 v216 offset:6176\n\t"
"v_mfma_f32_16x16x32_bf16 v[188:191], a[116:119], a[172:175], v[188:191]\n\t"
"ds_write_addtid_b32 v202 offset:5152\n\t"
"v_cvt_pk_bf16_f32 v195, -v195, -v199\n\t"
"v_mfma_f32_16x16x32_bf16 v[172:175], a[104:107], a[156:159], v[172:175]\n\t"
"ds_write_addtid_b32 v195 offset:9344\n\t"
"v_cvt_pk_bf16_f32 v219, v219, v223\n\t"
"v_mfma_f32_16x16x32_bf16 v[172:175], a[108:111], a[172:175], v[172:175]\n\t"
"buffer_load_dwordx4 a[112:115], v235, s[8:11], s56 offen offset:0 sc1\n\t"
"v_mfma_f32_16x16x32_bf16 v[156:159], a[96:99], a[156:159], v[156:159]\n\t"
"s_nop 0\n\t"
"ds_write_addtid_b32 v192 offset:0\n\t"
"ds_write_addtid_b32 v203 offset:13472\n\t"
"v_mfma_f32_16x16x32_bf16 v[156:159], a[100:103], a[172:175], v[156:159]\n\t"
"s_waitcnt vmcnt(25) lgkmcnt(9)\n\t"
"ds_read_b128 a[168:171], v224 offset:19104\n\t"
"v_mfma_f32_16x16x32_bf16 v[96:99], a[120:123], a[144:147], v[96:99]\n\t"
"ds_write_addtid_b32 v219 offset:15520\n\t"
"v_cvt_pk_bf16_f32 v217, v217, v221\n\t"
"v_mfma_f32_16x16x32_bf16 v[96:99], a[124:127], a[160:163], v[96:99]\n\t"
"s_nop 0\n\t"
"ds_write_addtid_b32 v201 offset:12448\n\t"
"ds_write_addtid_b32 v217 offset:14496\n\t"
"v_mfma_f32_16x16x32_bf16 v[112:115], a[128:131], a[144:147], v[112:115]\n\t"
"s_nop 0\n\t"
"ds_read_b128 a[156:159], v224 offset:17312\n\t"
"ds_write_addtid_b32 v211 offset:11392\n\t"
"v_mfma_f32_16x16x32_bf16 v[112:115], a[132:135], a[160:163], v[112:115]\n\t"
".p2align 3\n\t"
"buffer_load_dwordx4 a[116:119], v235, s[8:11], s56 offen offset:1024 sc1\n\t"
"v_pk_fma_f32 v[48:49], v[144:145], s[90:91], v[48:49]\n\t"
"v_pk_fma_f32 v[50:51], v[146:147], s[90:91], v[50:51]\n\t"
"v_pk_add_f32 v[144:145], v[48:49], v[144:145] neg_lo:[0,1] neg_hi:[0,1]\n\t"
"v_pk_add_f32 v[146:147], v[50:51], v[146:147] neg_lo:[0,1] neg_hi:[0,1]\n\t"
"v_pk_fma_f32 v[64:65], v[160:161], s[90:91], v[64:65]\n\t"
"v_pk_fma_f32 v[66:67], v[162:163], s[90:91], v[66:67]\n\t"
"v_pk_add_f32 v[160:161], v[64:65], v[160:161] neg_lo:[0,1] neg_hi:[0,1]\n\t"
"v_pk_add_f32 v[162:163], v[66:67], v[162:163] neg_lo:[0,1] neg_hi:[0,1]\n\t"
"v_pk_fma_f32 v[80:81], v[176:177], s[90:91], v[80:81]\n\t"
"v_pk_fma_f32 v[82:83], v[178:179], s[90:91], v[82:83]\n\t"
"v_pk_add_f32 v[176:177], v[80:81], v[176:177] neg_lo:[0,1] neg_hi:[0,1]\n\t"
"v_pk_add_f32 v[178:179], v[82:83], v[178:179] neg_lo:[0,1] neg_hi:[0,1]\n\t"
"v_pk_fma_f32 v[84:85], v[180:181], s[90:91], v[84:85]\n\t"
"v_pk_fma_f32 v[86:87], v[182:183], s[90:91], v[86:87]\n\t"
"v_pk_add_f32 v[180:181], v[84:85], v[180:181] neg_lo:[0,1] neg_hi:[0,1]\n\t"
"v_pk_add_f32 v[182:183], v[86:87], v[182:183] neg_lo:[0,1] neg_hi:[0,1]\n\t"
"v_pk_fma_f32 v[68:69], v[164:165], s[90:91], v[68:69]\n\t"
"v_pk_fma_f32 v[70:71], v[166:167], s[90:91], v[70:71]\n\t"
"v_pk_add_f32 v[164:165], v[68:69], v[164:165] neg_lo:[0,1] neg_hi:[0,1]\n\t"
"v_pk_add_f32 v[166:167], v[70:71], v[166:167] neg_lo:[0,1] neg_hi:[0,1]\n\t"
"v_pk_fma_f32 v[52:53], v[148:149], s[90:91], v[52:53]\n\t"
"v_pk_fma_f32 v[54:55], v[150:151], s[90:91], v[54:55]\n\t"
"v_pk_add_f32 v[148:149], v[52:53], v[148:149] neg_lo:[0,1] neg_hi:[0,1]\n\t"
"v_pk_add_f32 v[150:151], v[54:55], v[150:151] neg_lo:[0,1] neg_hi:[0,1]\n\t"
"buffer_load_dwordx4 a[108:111], v252, s[8:11], s55 offen offset:3072 sc1\n\t"
"v_pk_fma_f32 v[56:57], v[152:153], s[90:91], v[56:57]\n\t"
"v_pk_fma_f32 v[58:59], v[154:155], s[90:91], v[58:59]\n\t"
"v_pk_add_f32 v[152:153], v[56:57], v[152:153] neg_lo:[0,1] neg_hi:[0,1]\n\t"
"v_pk_add_f32 v[154:155], v[58:59], v[154:155] neg_lo:[0,1] neg_hi:[0,1]\n\t"
"v_pk_fma_f32 v[72:73], v[168:169], s[90:91], v[72:73]\n\t"
"v_pk_fma_f32 v[74:75], v[170:171], s[90:91], v[74:75]\n\t"
"v_pk_add_f32 v[168:169], v[72:73], v[168:169] neg_lo:[0,1] neg_hi:[0,1]\n\t"
"v_pk_add_f32 v[170:171], v[74:75], v[170:171] neg_lo:[0,1] neg_hi:[0,1]\n\t"
"v_pk_fma_f32 v[88:89], v[184:185], s[90:91], v[88:89]\n\t"
"v_pk_fma_f32 v[90:91], v[186:187], s[90:91], v[90:91]\n\t"
"v_pk_add_f32 v[184:185], v[88:89], v[184:185] neg_lo:[0,1] neg_hi:[0,1]\n\t"
"v_pk_add_f32 v[186:187], v[90:91], v[186:187] neg_lo:[0,1] neg_hi:[0,1]\n\t"
"v_pk_fma_f32 v[92:93], v[188:189], s[90:91], v[92:93]\n\t"
"v_pk_fma_f32 v[94:95], v[190:191], s[90:91], v[94:95]\n\t"
"v_pk_add_f32 v[188:189], v[92:93], v[188:189] neg_lo:[0,1] neg_hi:[0,1]\n\t"
"v_pk_add_f32 v[190:191], v[94:95], v[190:191] neg_lo:[0,1] neg_hi:[0,1]\n\t"
"v_pk_fma_f32 v[76:77], v[172:173], s[90:91], v[76:77]\n\t"
"v_pk_fma_f32 v[78:79], v[174:175], s[90:91], v[78:79]\n\t"
"v_pk_add_f32 v[172:173], v[76:77], v[172:173] neg_lo:[0,1] neg_hi:[0,1]\n\t"
"v_pk_add_f32 v[174:175], v[78:79], v[174:175] neg_lo:[0,1] neg_hi:[0,1]\n\t"
"v_pk_fma_f32 v[60:61], v[156:157], s[90:91], v[60:61]\n\t"
"v_pk_fma_f32 v[62:63], v[158:159], s[90:91], v[62:63]\n\t"
"v_pk_add_f32 v[156:157], v[60:61], v[156:157] neg_lo:[0,1] neg_hi:[0,1]\n\t"
"v_pk_add_f32 v[158:159], v[62:63], v[158:159] neg_lo:[0,1] neg_hi:[0,1]\n\t"
"v_mfma_f32_16x16x32_bf16 v[128:131], a[136:139], a[144:147], v[128:131]\n\t"
"ds_read_b128 a[172:175], v224 offset:19360\n\t"
"v_mfma_f32_16x16x32_bf16 v[128:131], a[140:143], a[160:163], v[128:131]\n\t"
"buffer_load_dwordx4 a[104:107], v252, s[8:11], s55 offen offset:2048 sc1\n\t"
"v_mfma_f32_16x16x32_bf16 v[132:135], a[136:139], a[148:151], v[132:135]\n\t"
"ds_read_b64_tr_b16 a[196:197], v250 offset:47232\n\t"
"v_mfma_f32_16x16x32_bf16 v[132:135], a[140:143], a[164:167], v[132:135]\n\t"
"buffer_load_dwordx4 a[96:99], v252, s[8:11], s55 offen offset:0 sc1\n\t"
"v_mfma_f32_16x16x32_bf16 v[116:119], a[128:131], a[148:151], v[116:119]\n\t"
"ds_read_b64_tr_b16 a[204:205], v251 offset:47232\n\t"
"v_accvgpr_read_b32 v224, a212\n\t"
"v_mfma_f32_16x16x32_bf16 v[116:119], a[132:135], a[164:167], v[116:119]\n\t"
"buffer_load_dwordx4 a[100:103], v252, s[8:11], s55 offen offset:1024 sc1\n\t"
"v_mfma_f32_16x16x32_bf16 v[100:103], a[120:123], a[148:151], v[100:103]\n\t"
"v_mfma_f32_16x16x32_bf16 v[100:103], a[124:127], a[164:167], v[100:103]\n\t"
"s_waitcnt lgkmcnt(0)\n\t"
"s_barrier\n\t"
"v_mfma_f32_16x16x32_bf16 v[104:107], a[120:123], a[152:155], v[104:107]\n\t"
"s_add_u32 m0, s26, 145152\n\t"
"ds_read_b128 a[144:147], v224 offset:1024\n\t"
"v_mfma_f32_16x16x32_bf16 v[104:107], a[124:127], a[168:171], v[104:107]\n\t"
"ds_read_b128 a[148:151], v224 offset:1280\n\t"
"v_mfma_f32_16x16x32_bf16 v[120:123], a[128:131], a[152:155], v[120:123]\n\t"
"ds_read_b128 a[160:163], v224 offset:3072\n\t"
"v_mfma_f32_16x16x32_bf16 v[120:123], a[132:135], a[168:171], v[120:123]\n\t"
"ds_read_b128 a[164:167], v224 offset:3328\n\t"
"v_mfma_f32_16x16x32_bf16 v[136:139], a[136:139], a[152:155], v[136:139]\n\t"
"ds_read_b128 a[152:155], v224 offset:1536\n\t"
"v_mfma_f32_16x16x32_bf16 v[136:139], a[140:143], a[168:171], v[136:139]\n\t"
"buffer_load_dwordx4 v236, s[12:15], s22 offen offset:256 nt lds\n\t"
"v_mfma_f32_16x16x32_bf16 v[140:143], a[136:139], a[156:159], v[140:143]\n\t"
"ds_read_b128 a[168:171], v224 offset:3584\n\t"
"v_mfma_f32_16x16x32_bf16 v[140:143], a[140:143], a[172:175], v[140:143]\n\t"
"v_mfma_f32_16x16x32_bf16 v[124:127], a[128:131], a[156:159], v[124:127]\n\t"
"ds_read_b128 a[228:231], v254 offset:59904\n\t"
"v_mfma_f32_16x16x32_bf16 v[124:127], a[132:135], a[172:175], v[124:127]\n\t"
"buffer_load_dwordx4 v237, s[12:15], s22 offen offset:1280 nt lds\n\t"
"v_mfma_f32_16x16x32_bf16 v[108:111], a[120:123], a[156:159], v[108:111]\n\t"
"ds_read_b128 a[156:159], v224 offset:1792\n\t"
"v_mfma_f32_16x16x32_bf16 v[108:111], a[124:127], a[172:175], v[108:111]\n\t"
"ds_read_b128 a[172:175], v224 offset:3840\n\t"
"v_mfma_f32_4x4x4_16b_bf16 v[200:203], a[220:221], a[178:179], 0\n\t"
"ds_read_b64_tr_b16 a[178:179], v250 offset:23040\n\t"
"v_mfma_f32_4x4x4_16b_bf16 v[204:207], a[220:221], a[186:187], 0\n\t"
"buffer_load_dwordx4 v238, s[12:15], s22 offen offset:2304 nt lds\n\t"
"v_mfma_f32_4x4x4_16b_bf16 v[216:219], a[220:221], a[194:195], 0\n\t"
"ds_read_b64_tr_b16 a[186:187], v251 offset:23040\n\t"
"v_mfma_f32_4x4x4_16b_bf16 v[220:223], a[220:221], a[202:203], 0\n\t"
"ds_read_b64_tr_b16 a[194:195], v248 offset:23040\n\t"
"v_mfma_f32_4x4x4_16b_bf16 v[192:195], a[222:223], a[176:177], v[200:203]\n\t"
"ds_read_b64_tr_b16 a[202:203], v249 offset:23040\n\t"
"v_mfma_f32_4x4x4_16b_bf16 v[196:199], a[222:223], a[184:185], v[204:207]\n\t"
"ds_read_b64_tr_b16 a[176:177], v248 offset:20736\n\t"
"v_mfma_f32_4x4x4_16b_bf16 v[200:203], a[222:223], a[180:181], v[200:203]\n\t"
"buffer_load_dwordx4 v239, s[12:15], s22 offen offset:3328 nt lds\n\t"
"v_mfma_f32_4x4x4_16b_bf16 v[204:207], a[222:223], a[188:189], v[204:207]\n\t"
".p2align 3\n\t"
"ds_read_b64_tr_b16 a[184:185], v249 offset:20736\n\t"
"v_pk_fma_f32 v[0:1], v[96:97], s[90:91], v[0:1]\n\t"
"v_pk_fma_f32 v[2:3], v[98:99], s[90:91], v[2:3]\n\t"
"v_pk_add_f32 v[96:97], v[0:1], v[96:97] neg_lo:[0,1] neg_hi:[0,1]\n\t"
"v_pk_add_f32 v[98:99], v[2:3], v[98:99] neg_lo:[0,1] neg_hi:[0,1]\n\t"
"v_pk_fma_f32 v[16:17], v[112:113], s[90:91], v[16:17]\n\t"
"v_pk_fma_f32 v[18:19], v[114:115], s[90:91], v[18:19]\n\t"
"v_pk_add_f32 v[112:113], v[16:17], v[112:113] neg_lo:[0,1] neg_hi:[0,1]\n\t"
"v_pk_add_f32 v[114:115], v[18:19], v[114:115] neg_lo:[0,1] neg_hi:[0,1]\n\t"
"v_pk_fma_f32 v[32:33], v[128:129], s[90:91], v[32:33]\n\t"
"v_pk_fma_f32 v[34:35], v[130:131], s[90:91], v[34:35]\n\t"
"v_pk_add_f32 v[128:129], v[32:33], v[128:129] neg_lo:[0,1] neg_hi:[0,1]\n\t"
"v_pk_add_f32 v[130:131], v[34:35], v[130:131] neg_lo:[0,1] neg_hi:[0,1]\n\t"
"v_pk_fma_f32 v[36:37], v[132:133], s[90:91], v[36:37]\n\t"
"v_pk_fma_f32 v[38:39], v[134:135], s[90:91], v[38:39]\n\t"
"v_pk_add_f32 v[132:133], v[36:37], v[132:133] neg_lo:[0,1] neg_hi:[0,1]\n\t"
"v_pk_add_f32 v[134:135], v[38:39], v[134:135] neg_lo:[0,1] neg_hi:[0,1]\n\t"
"v_pk_fma_f32 v[20:21], v[116:117], s[90:91], v[20:21]\n\t"
"v_pk_fma_f32 v[22:23], v[118:119], s[90:91], v[22:23]\n\t"
"v_pk_add_f32 v[116:117], v[20:21], v[116:117] neg_lo:[0,1] neg_hi:[0,1]\n\t"
"v_pk_add_f32 v[118:119], v[22:23], v[118:119] neg_lo:[0,1] neg_hi:[0,1]\n\t"
"v_pk_fma_f32 v[4:5], v[100:101], s[90:91], v[4:5]\n\t"
"v_pk_fma_f32 v[6:7], v[102:103], s[90:91], v[6:7]\n\t"
"v_pk_add_f32 v[100:101], v[4:5], v[100:101] neg_lo:[0,1] neg_hi:[0,1]\n\t"
"v_pk_add_f32 v[102:103], v[6:7], v[102:103] neg_lo:[0,1] neg_hi:[0,1]\n\t"
"v_pk_fma_f32 v[8:9], v[104:105], s[90:91], v[8:9]\n\t"
"v_pk_fma_f32 v[10:11], v[106:107], s[90:91], v[10:11]\n\t"
"v_pk_add_f32 v[104:105], v[8:9], v[104:105] neg_lo:[0,1] neg_hi:[0,1]\n\t"
"v_pk_add_f32 v[106:107], v[10:11], v[106:107] neg_lo:[0,1] neg_hi:[0,1]\n\t"
"v_pk_fma_f32 v[24:25], v[120:121], s[90:91], v[24:25]\n\t"
"v_pk_fma_f32 v[26:27], v[122:123], s[90:91], v[26:27]\n\t"
"v_pk_add_f32 v[120:121], v[24:25], v[120:121] neg_lo:[0,1] neg_hi:[0,1]\n\t"
"v_pk_add_f32 v[122:123], v[26:27], v[122:123] neg_lo:[0,1] neg_hi:[0,1]\n\t"
"v_pk_fma_f32 v[40:41], v[136:137], s[90:91], v[40:41]\n\t"
"v_pk_fma_f32 v[42:43], v[138:139], s[90:91], v[42:43]\n\t"
"v_pk_add_f32 v[136:137], v[40:41], v[136:137] neg_lo:[0,1] neg_hi:[0,1]\n\t"
"v_pk_add_f32 v[138:139], v[42:43], v[138:139] neg_lo:[0,1] neg_hi:[0,1]\n\t"
"v_pk_fma_f32 v[44:45], v[140:141], s[90:91], v[44:45]\n\t"
"v_pk_fma_f32 v[46:47], v[142:143], s[90:91], v[46:47]\n\t"
"v_pk_add_f32 v[140:141], v[44:45], v[140:141] neg_lo:[0,1] neg_hi:[0,1]\n\t"
"v_pk_add_f32 v[142:143], v[46:47], v[142:143] neg_lo:[0,1] neg_hi:[0,1]\n\t"
"v_pk_fma_f32 v[28:29], v[124:125], s[90:91], v[28:29]\n\t"
"v_pk_fma_f32 v[30:31], v[126:127], s[90:91], v[30:31]\n\t"
"v_pk_add_f32 v[124:125], v[28:29], v[124:125] neg_lo:[0,1] neg_hi:[0,1]\n\t"
"v_pk_add_f32 v[126:127], v[30:31], v[126:127] neg_lo:[0,1] neg_hi:[0,1]\n\t"
"v_pk_fma_f32 v[12:13], v[108:109], s[90:91], v[12:13]\n\t"
"v_pk_fma_f32 v[14:15], v[110:111], s[90:91], v[14:15]\n\t"
"v_pk_add_f32 v[108:109], v[12:13], v[108:109] neg_lo:[0,1] neg_hi:[0,1]\n\t"
"v_pk_add_f32 v[110:111], v[14:15], v[110:111] neg_lo:[0,1] neg_hi:[0,1]\n\t"
"v_mfma_f32_4x4x4_16b_bf16 v[208:211], a[222:223], a[192:193], v[216:219]\n\t"
"ds_read_b64_tr_b16 a[180:181], v248 offset:25344\n\t"
"v_mfma_f32_4x4x4_16b_bf16 v[212:215], a[222:223], a[200:201], v[220:223]\n\t"
"ds_read_b64_tr_b16 a[188:189], v249 offset:25344\n\t"
"v_mfma_f32_4x4x4_16b_bf16 v[216:219], a[222:223], a[196:197], v[216:219]\n\t"
"ds_read_b64_tr_b16 a[192:193], v250 offset:20736\n\t"
"v_mfma_f32_4x4x4_16b_bf16 v[220:223], a[222:223], a[204:205], v[220:223]\n\t"
"s_waitcnt vmcnt(28) lgkmcnt(8)\n\t"
"buffer_load_dwordx4 a[136:139], v235, s[8:11], s55 offen offset:2048 sc1\n\t"
"v_mfma_f32_16x16x32_bf16 v[48:51], a[0:3], a[144:147], v[48:51]\n\t"
"ds_read_b64_tr_b16 a[200:201], v251 offset:20736\n\t"
"v_cvt_pk_bf16_f32 v194, -v194, -v198\n\t"
"v_mfma_f32_16x16x32_bf16 v[48:51], a[4:7], a[160:163], v[48:51]\n\t"
"s_mov_b64 exec, s[36:37]\n\t"
"buffer_load_dwordx4 v240, s[12:15], s22 offen offset:3840 nt lds\n\t"
"s_mov_b64 exec, -1\n\t"
"v_mfma_f32_16x16x32_bf16 v[64:67], a[8:11], a[144:147], v[64:67]\n\t"
"s_lshl_b32 m0, s73, 8\n\t"
"v_cvt_pk_bf16_f32 v201, v201, v205\n\t"
"v_cvt_pk_bf16_f32 v202, v202, v206\n\t"
"v_mfma_f32_16x16x32_bf16 v[64:67], a[12:15], a[160:163], v[64:67]\n\t"
"ds_write_b128 v255, a[228:231] offset:41472\n\t"
"v_mfma_f32_16x16x32_bf16 v[80:83], a[16:19], a[144:147], v[80:83]\n\t"
"v_mfma_f32_16x16x32_bf16 v[80:83], a[20:23], a[160:163], v[80:83]\n\t"
"v_mfma_f32_16x16x32_bf16 v[84:87], a[16:19], a[148:151], v[84:87]\n\t"
"v_cvt_pk_bf16_f32 v208, -v208, -v212\n\t"
"v_cvt_pk_bf16_f32 v217, v217, v221\n\t"
"v_mfma_f32_16x16x32_bf16 v[84:87], a[20:23], a[164:167], v[84:87]\n\t"
"ds_read_b128 a[144:147], v224 offset:0\n\t"
"v_cvt_pk_bf16_f32 v211, -v211, -v215\n\t"
"v_mfma_f32_16x16x32_bf16 v[68:71], a[8:11], a[148:151], v[68:71]\n\t"
"ds_read_b128 a[160:163], v224 offset:2048\n\t"
"v_cvt_pk_bf16_f32 v219, v219, v223\n\t"
"v_mfma_f32_16x16x32_bf16 v[68:71], a[12:15], a[164:167], v[68:71]\n\t"
"ds_write_addtid_b32 v211 offset:27936\n\t"
"v_cvt_pk_bf16_f32 v192, -v192, -v196\n\t"
"v_mfma_f32_16x16x32_bf16 v[52:55], a[0:3], a[148:151], v[52:55]\n\t"
"ds_read_b128 a[148:151], v224 offset:256\n\t"
"v_cvt_pk_bf16_f32 v200, v200, v204\n\t"
"v_mfma_f32_16x16x32_bf16 v[52:55], a[4:7], a[164:167], v[52:55]\n\t"
"ds_read_b128 a[164:167], v224 offset:2304\n\t"
"v_cvt_pk_bf16_f32 v216, v216, v220\n\t"
"v_mfma_f32_16x16x32_bf16 v[56:59], a[0:3], a[152:155], v[56:59]\n\t"
"ds_write_addtid_b32 v194 offset:17568\n\t"
"v_cvt_pk_bf16_f32 v203, v203, v207\n\t"
"v_mfma_f32_16x16x32_bf16 v[56:59], a[4:7], a[168:171], v[56:59]\n\t"
"ds_write_addtid_b32 v219 offset:32064\n\t"
"v_cvt_pk_bf16_f32 v218, v218, v222\n\t"
"v_mfma_f32_16x16x32_bf16 v[72:75], a[8:11], a[152:155], v[72:75]\n\t"
"ds_write_addtid_b32 v218 offset:23744\n\t"
"v_mfma_f32_16x16x32_bf16 v[72:75], a[12:15], a[168:171], v[72:75]\n\t"
"ds_write_addtid_b32 v208 offset:18592\n\t"
"v_mfma_f32_16x16x32_bf16 v[88:91], a[16:19], a[152:155], v[88:91]\n\t"
"ds_write_addtid_b32 v202 offset:21696\n\t"
"v_cvt_pk_bf16_f32 v193, -v193, -v197\n\t"
"v_mfma_f32_16x16x32_bf16 v[88:91], a[20:23], a[168:171], v[88:91]\n\t"
"ds_write_addtid_b32 v193 offset:24864\n\t"
"v_cvt_pk_bf16_f32 v209, -v209, -v213\n\t"
"v_mfma_f32_16x16x32_bf16 v[92:95], a[16:19], a[156:159], v[92:95]\n\t"
"ds_write_addtid_b32 v200 offset:20672\n\t"
"v_mfma_f32_16x16x32_bf16 v[92:95], a[20:23], a[172:175], v[92:95]\n\t"
"ds_write_addtid_b32 v209 offset:26912\n\t"
"v_mfma_f32_16x16x32_bf16 v[76:79], a[8:11], a[156:159], v[76:79]\n\t"
"ds_write_addtid_b32 v192 offset:16544\n\t"
"v_mfma_f32_16x16x32_bf16 v[76:79], a[12:15], a[172:175], v[76:79]\n\t"
"ds_write_addtid_b32 v203 offset:30016\n\t"
"v_cvt_pk_bf16_f32 v195, -v195, -v199\n\t"
"v_mfma_f32_16x16x32_bf16 v[60:63], a[0:3], a[156:159], v[60:63]\n\t"
"s_nop 0\n\t"
"ds_write_addtid_b32 v201 offset:28992\n\t"
"ds_write_addtid_b32 v217 offset:31040\n\t"
"v_mfma_f32_16x16x32_bf16 v[60:63], a[4:7], a[172:175], v[60:63]\n\t"
"s_waitcnt vmcnt(24) lgkmcnt(10)\n\t"
"ds_read_b128 a[156:159], v224 offset:768\n\t"
"v_cvt_pk_bf16_f32 v210, -v210, -v214\n\t"
"v_mfma_f32_16x16x32_bf16 v[0:3], a[24:27], a[144:147], v[0:3]\n\t"
"ds_write_addtid_b32 v210 offset:19616\n\t"
"v_mfma_f32_16x16x32_bf16 v[0:3], a[28:31], a[160:163], v[0:3]\n\t"
"s_nop 0\n\t"
"ds_read_b128 a[168:171], v224 offset:2560\n\t"
"ds_write_addtid_b32 v216 offset:22720\n\t"
"v_mfma_f32_16x16x32_bf16 v[16:19], a[32:35], a[144:147], v[16:19]\n\t"
"ds_write_addtid_b32 v195 offset:25888\n\t"
"v_mfma_f32_16x16x32_bf16 v[16:19], a[36:39], a[160:163], v[16:19]\n\t"
"buffer_load_dwordx4 a[140:143], v235, s[8:11], s55 offen offset:3072 sc1\n\t"
"v_mfma_f32_16x16x32_bf16 v[32:35], a[40:43], a[144:147], v[32:35]\n\t"
"ds_read_b128 a[152:155], v224 offset:512\n\t"
"v_mfma_f32_16x16x32_bf16 v[32:35], a[44:47], a[160:163], v[32:35]\n\t"
"ds_read_b128 a[172:175], v224 offset:2816\n\t"
"v_mfma_f32_16x16x32_bf16 v[36:39], a[40:43], a[148:151], v[36:39]\n\t"
"ds_read_b64_tr_b16 a[196:197], v250 offset:25344\n\t"
"v_mfma_f32_16x16x32_bf16 v[36:39], a[44:47], a[164:167], v[36:39]\n\t"
"buffer_load_dwordx4 a[132:135], v235, s[8:11], s55 offen offset:1024 sc1\n\t"
"v_mfma_f32_16x16x32_bf16 v[20:23], a[32:35], a[148:151], v[20:23]\n\t"
"ds_read_b64_tr_b16 a[204:205], v251 offset:25344\n\t"
"v_mfma_f32_16x16x32_bf16 v[20:23], a[36:39], a[164:167], v[20:23]\n\t"
"v_mfma_f32_16x16x32_bf16 v[4:7], a[24:27], a[148:151], v[4:7]\n\t"
"s_waitcnt lgkmcnt(2)\n\t"
"buffer_load_dwordx4 a[128:131], v235, s[8:11], s55 offen offset:0 sc1\n\t"
"v_mfma_f32_16x16x32_bf16 v[4:7], a[28:31], a[164:167], v[4:7]\n\t"
"s_barrier\n\t"
"v_mfma_f32_16x16x32_bf16 v[8:11], a[24:27], a[152:155], v[8:11]\n\t"
"ds_read_b128 a[148:151], v224 offset:17824\n\t"
"v_mfma_f32_16x16x32_bf16 v[8:11], a[28:31], a[168:171], v[8:11]\n\t"
"ds_read_b128 a[144:147], v224 offset:17568\n\t"
"v_mfma_f32_16x16x32_bf16 v[24:27], a[32:35], a[152:155], v[24:27]\n\t"
"ds_read_b128 a[160:163], v224 offset:19616\n\t"
"v_mfma_f32_16x16x32_bf16 v[24:27], a[36:39], a[168:171], v[24:27]\n\t"
"ds_read_b128 a[164:167], v224 offset:19872\n\t"
"v_mfma_f32_16x16x32_bf16 v[40:43], a[40:43], a[152:155], v[40:43]\n\t"
"v_mfma_f32_16x16x32_bf16 v[40:43], a[44:47], a[168:171], v[40:43]\n\t"
"buffer_load_dwordx4 a[120:123], v252, s[8:11], s54 offen offset:2048 sc1\n\t"
"v_mfma_f32_16x16x32_bf16 v[44:47], a[40:43], a[156:159], v[44:47]\n\t"
"v_mfma_f32_16x16x32_bf16 v[44:47], a[44:47], a[172:175], v[44:47]\n\t"
"buffer_load_dwordx4 a[124:127], v252, s[8:11], s54 offen offset:3072 sc1\n\t"
"v_mfma_f32_16x16x32_bf16 v[28:31], a[32:35], a[156:159], v[28:31]\n\t"
"ds_read_b128 a[152:155], v224 offset:18080\n\t"
"v_mfma_f32_16x16x32_bf16 v[28:31], a[36:39], a[172:175], v[28:31]\n\t"
"buffer_load_dwordx4 a[16:19], v235, s[8:11], s41 offen offset:0 sc1\n\t"
"v_mfma_f32_16x16x32_bf16 v[12:15], a[24:27], a[156:159], v[12:15]\n\t"
"ds_read_b128 a[168:171], v224 offset:20128\n\t"
"v_mfma_f32_16x16x32_bf16 v[12:15], a[28:31], a[172:175], v[12:15]\n\t"
"s_waitcnt lgkmcnt(4)\n\t"
"buffer_load_dwordx4 a[20:23], v235, s[8:11], s41 offen offset:1024 sc1\n\t"
"v_mfma_f32_4x4x4_16b_bf16 v[200:203], a[220:221], a[178:179], 0\n\t"
"ds_read_b64_tr_b16 a[178:179], v250 offset:2304\n\t"
"v_mfma_f32_4x4x4_16b_bf16 v[204:207], a[220:221], a[186:187], 0\n\t"
"ds_read_b64_tr_b16 a[186:187], v251 offset:2304\n\t"
"v_mfma_f32_4x4x4_16b_bf16 v[216:219], a[220:221], a[194:195], 0\n\t"
"ds_read_b64_tr_b16 a[194:195], v248 offset:2304\n\t"
"v_mfma_f32_4x4x4_16b_bf16 v[220:223], a[220:221], a[202:203], 0\n\t"
"ds_read_b64_tr_b16 a[202:203], v249 offset:2304\n\t"
"v_mfma_f32_4x4x4_16b_bf16 v[192:195], a[222:223], a[176:177], v[200:203]\n\t"
"ds_read_b64_tr_b16 a[176:177], v248 offset:0\n\t"
"v_mfma_f32_4x4x4_16b_bf16 v[196:199], a[222:223], a[184:185], v[204:207]\n\t"
"ds_read_b128 a[156:159], v224 offset:18336\n\t"
"v_mfma_f32_4x4x4_16b_bf16 v[200:203], a[222:223], a[180:181], v[200:203]\n\t"
"ds_read_b64_tr_b16 a[184:185], v249 offset:0\n\t"
"v_mfma_f32_4x4x4_16b_bf16 v[204:207], a[222:223], a[188:189], v[204:207]\n\t"
"ds_read_b64_tr_b16 a[180:181], v248 offset:4608\n\t"
"v_mfma_f32_4x4x4_16b_bf16 v[208:211], a[222:223], a[192:193], v[216:219]\n\t"
"ds_read_b64_tr_b16 a[188:189], v249 offset:4608\n\t"
"v_mfma_f32_4x4x4_16b_bf16 v[212:215], a[222:223], a[200:201], v[220:223]\n\t"
"ds_read_b128 a[172:175], v224 offset:20384\n\t"
"v_mfma_f32_4x4x4_16b_bf16 v[216:219], a[222:223], a[196:197], v[216:219]\n\t"
"ds_read_b64_tr_b16 a[192:193], v250 offset:0\n\t"
"v_mfma_f32_4x4x4_16b_bf16 v[220:223], a[222:223], a[204:205], v[220:223]\n\t"
"s_waitcnt vmcnt(25) lgkmcnt(9)\n\t"
"buffer_load_dwordx4 a[12:15], v252, s[8:11], s40 offen offset:3072 sc1\n\t"
"v_mfma_f32_16x16x32_bf16 v[144:147], a[48:51], a[144:147], v[144:147]\n\t"
"ds_read_b64_tr_b16 a[200:201], v251 offset:0\n\t"
"v_cvt_pk_bf16_f32 v192, -v192, -v196\n\t"
"v_mfma_f32_16x16x32_bf16 v[144:147], a[52:55], a[160:163], v[144:147]\n\t"
"buffer_load_dwordx4 a[8:11], v252, s[8:11], s40 offen offset:2048 sc1\n\t"
"v_mfma_f32_16x16x32_bf16 v[160:163], a[56:59], a[144:147], v[160:163]\n\t"
"ds_write_addtid_b32 v192 offset:0\n\t"
"v_cvt_pk_bf16_f32 v195, -v195, -v199\n\t"
"v_mfma_f32_16x16x32_bf16 v[160:163], a[60:63], a[160:163], v[160:163]\n\t"
"buffer_load_dwordx4 a[0:3], v252, s[8:11], s40 offen offset:0 sc1\n\t"
"v_mfma_f32_16x16x32_bf16 v[176:179], a[64:67], a[144:147], v[176:179]\n\t"
"ds_read_b128 a[144:147], v224 offset:16544\n\t"
"v_cvt_pk_bf16_f32 v200, v200, v204\n\t"
"v_mfma_f32_16x16x32_bf16 v[176:179], a[68:71], a[160:163], v[176:179]\n\t"
"ds_write_addtid_b32 v195 offset:9344\n\t"
"v_cvt_pk_bf16_f32 v193, -v193, -v197\n\t"
"v_mfma_f32_16x16x32_bf16 v[180:183], a[64:67], a[148:151], v[180:183]\n\t"
"ds_read_b128 a[160:163], v224 offset:18592\n\t"
"v_cvt_pk_bf16_f32 v208, -v208, -v212\n\t"
"v_mfma_f32_16x16x32_bf16 v[180:183], a[68:71], a[164:167], v[180:183]\n\t"
"buffer_load_dwordx4 a[4:7], v252, s[8:11], s40 offen offset:1024 sc1\n\t"
"v_mfma_f32_16x16x32_bf16 v[164:167], a[56:59], a[148:151], v[164:167]\n\t"
"ds_write_addtid_b32 v200 offset:4128\n\t"
"v_cvt_pk_bf16_f32 v210, -v210, -v214\n\t"
"v_mfma_f32_16x16x32_bf16 v[164:167], a[60:63], a[164:167], v[164:167]\n\t"
"ds_write_addtid_b32 v210 offset:3072\n\t"
"v_cvt_pk_bf16_f32 v219, v219, v223\n\t"
"v_mfma_f32_16x16x32_bf16 v[148:151], a[48:51], a[148:151], v[148:151]\n\t"
"ds_read_b128 a[148:151], v224 offset:16800\n\t"
"v_cvt_pk_bf16_f32 v201, v201, v205\n\t"
"v_mfma_f32_16x16x32_bf16 v[148:151], a[52:55], a[164:167], v[148:151]\n\t"
"buffer_load_dwordx4 a[40:43], v235, s[8:11], s40 offen offset:2048 sc1\n\t"
"v_mfma_f32_16x16x32_bf16 v[152:155], a[48:51], a[152:155], v[152:155]\n\t"
"ds_write_addtid_b32 v193 offset:8320\n\t"
"v_cvt_pk_bf16_f32 v202, v202, v206\n\t"
"v_mfma_f32_16x16x32_bf16 v[152:155], a[52:55], a[168:171], v[152:155]\n\t"
"ds_read_b128 a[164:167], v224 offset:18848\n\t"
"v_cvt_pk_bf16_f32 v218, v218, v222\n\t"
"v_mfma_f32_16x16x32_bf16 v[168:171], a[56:59], a[152:155], v[168:171]\n\t"
"ds_write_addtid_b32 v201 offset:12448\n\t"
"v_cvt_pk_bf16_f32 v216, v216, v220\n\t"
"v_mfma_f32_16x16x32_bf16 v[168:171], a[60:63], a[168:171], v[168:171]\n\t"
"buffer_load_dwordx4 a[44:47], v235, s[8:11], s40 offen offset:3072 sc1\n\t"
"v_mfma_f32_16x16x32_bf16 v[184:187], a[64:67], a[152:155], v[184:187]\n\t"
"ds_write_addtid_b32 v202 offset:5152\n\t"
"v_cvt_pk_bf16_f32 v203, v203, v207\n\t"
"v_mfma_f32_16x16x32_bf16 v[184:187], a[68:71], a[168:171], v[184:187]\n\t"
"s_waitcnt lgkmcnt(7)\n\t"
"ds_write_addtid_b32 v208 offset:2048\n\t"
"v_cvt_pk_bf16_f32 v211, -v211, -v215\n\t"
"v_mfma_f32_16x16x32_bf16 v[188:191], a[64:67], a[156:159], v[188:191]\n\t"
"ds_read_b128 a[152:155], v224 offset:17056\n\t"
"v_cvt_pk_bf16_f32 v194, -v194, -v198\n\t"
"v_mfma_f32_16x16x32_bf16 v[188:191], a[68:71], a[172:175], v[188:191]\n\t"
"buffer_load_dwordx4 a[36:39], v235, s[8:11], s40 offen offset:1024 sc1\n\t"
"v_mfma_f32_16x16x32_bf16 v[172:175], a[56:59], a[156:159], v[172:175]\n\t"
"ds_write_addtid_b32 v216 offset:6176\n\t"
"v_cvt_pk_bf16_f32 v209, -v209, -v213\n\t"
"v_mfma_f32_16x16x32_bf16 v[172:175], a[60:63], a[172:175], v[172:175]\n\t"
"ds_write_addtid_b32 v194 offset:1024\n\t"
"v_mfma_f32_16x16x32_bf16 v[156:159], a[48:51], a[156:159], v[156:159]\n\t"
"ds_write_addtid_b32 v218 offset:7200\n\t"
"v_cvt_pk_bf16_f32 v217, v217, v221\n\t"
"v_mfma_f32_16x16x32_bf16 v[156:159], a[52:55], a[172:175], v[156:159]\n\t"
"s_waitcnt vmcnt(26) lgkmcnt(7)\n\t"
"buffer_load_dwordx4 a[32:35], v235, s[8:11], s40 offen offset:0 sc1\n\t"
"v_mfma_f32_16x16x32_bf16 v[96:99], a[72:75], a[144:147], v[96:99]\n\t"
"s_nop 0\n\t"
"ds_write_addtid_b32 v203 offset:13472\n\t"
"ds_write_addtid_b32 v209 offset:10368\n\t"
"v_mfma_f32_16x16x32_bf16 v[96:99], a[76:79], a[160:163], v[96:99]\n\t"
"s_nop 0\n\t"
"ds_read_b128 a[168:171], v224 offset:19104\n\t"
"ds_write_addtid_b32 v217 offset:14496\n\t"
"v_mfma_f32_16x16x32_bf16 v[112:115], a[80:83], a[144:147], v[112:115]\n\t"
"s_nop 0\n\t"
"ds_write_addtid_b32 v211 offset:11392\n\t"
"ds_write_addtid_b32 v219 offset:15520\n\t"
"v_mfma_f32_16x16x32_bf16 v[112:115], a[84:87], a[160:163], v[112:115]\n\t"
"buffer_load_dwordx4 a[24:27], v252, s[8:11], 0 offen offset:2048 sc1\n\t"
"v_mfma_f32_16x16x32_bf16 v[128:131], a[88:91], a[144:147], v[128:131]\n\t"
"ds_read_b128 a[172:175], v224 offset:19360\n\t"
"v_mfma_f32_16x16x32_bf16 v[128:131], a[92:95], a[160:163], v[128:131]\n\t"
"ds_read_b128 a[156:159], v224 offset:17312\n\t"
"v_mfma_f32_16x16x32_bf16 v[132:135], a[88:91], a[148:151], v[132:135]\n\t"
"ds_read_b64_tr_b16 a[196:197], v250 offset:4608\n\t"
"v_mfma_f32_16x16x32_bf16 v[132:135], a[92:95], a[164:167], v[132:135]\n\t"
"buffer_load_dwordx4 a[28:31], v252, s[8:11], 0 offen offset:3072 sc1\n\t"
"v_mfma_f32_16x16x32_bf16 v[116:119], a[80:83], a[148:151], v[116:119]\n\t"
"ds_read_b64_tr_b16 a[204:205], v251 offset:4608\n\t"
"v_mfma_f32_16x16x32_bf16 v[116:119], a[84:87], a[164:167], v[116:119]\n\t"
"v_mfma_f32_16x16x32_bf16 v[100:103], a[72:75], a[148:151], v[100:103]\n\t"
"s_waitcnt lgkmcnt(2)\n\t"
"buffer_load_dwordx4 a[64:67], v235, s[8:11], s68 offen offset:0 sc1\n\t"
"v_mfma_f32_16x16x32_bf16 v[100:103], a[76:79], a[164:167], v[100:103]\n\t"
"s_barrier\n\t"
"v_mfma_f32_16x16x32_bf16 v[104:107], a[72:75], a[152:155], v[104:107]\n\t"
"ds_read_b128 a[160:163], v224 offset:3072\n\t"
"v_mfma_f32_16x16x32_bf16 v[104:107], a[76:79], a[168:171], v[104:107]\n\t"
"ds_read_b128 a[144:147], v224 offset:1024\n\t"
"v_mfma_f32_16x16x32_bf16 v[120:123], a[80:83], a[152:155], v[120:123]\n\t"
"ds_read_b128 a[148:151], v224 offset:1280\n\t"
"v_mfma_f32_16x16x32_bf16 v[120:123], a[84:87], a[168:171], v[120:123]\n\t"
"ds_read_b128 a[164:167], v224 offset:3328\n\t"
"v_mfma_f32_16x16x32_bf16 v[136:139], a[88:91], a[152:155], v[136:139]\n\t"
"v_mfma_f32_16x16x32_bf16 v[136:139], a[92:95], a[168:171], v[136:139]\n\t"
"buffer_load_dwordx4 a[68:71], v235, s[8:11], s68 offen offset:1024 sc1\n\t"
"v_mfma_f32_16x16x32_bf16 v[140:143], a[88:91], a[156:159], v[140:143]\n\t"
"v_mfma_f32_16x16x32_bf16 v[140:143], a[92:95], a[172:175], v[140:143]\n\t"
"ds_read_b128 a[152:155], v224 offset:1536\n\t"
"v_mfma_f32_16x16x32_bf16 v[124:127], a[80:83], a[156:159], v[124:127]\n\t"
"v_mfma_f32_16x16x32_bf16 v[124:127], a[84:87], a[172:175], v[124:127]\n\t"
"buffer_load_dwordx4 a[60:63], v252, s[8:11], s67 offen offset:3072 sc1\n\t"
"v_mfma_f32_16x16x32_bf16 v[108:111], a[72:75], a[156:159], v[108:111]\n\t"
"ds_read_b128 a[168:171], v224 offset:3584\n\t"
"v_mfma_f32_16x16x32_bf16 v[108:111], a[76:79], a[172:175], v[108:111]\n\t"
"s_waitcnt lgkmcnt(4)\n\t"
"ds_read_b128 a[172:175], v224 offset:3840\n\t"
"v_mfma_f32_4x4x4_16b_bf16 v[200:203], a[220:221], a[178:179], 0\n\t"
"ds_read_b64_tr_b16 a[178:179], v250 offset:24192\n\t"
"v_mfma_f32_4x4x4_16b_bf16 v[204:207], a[220:221], a[186:187], 0\n\t"
"v_mfma_f32_4x4x4_16b_bf16 v[216:219], a[220:221], a[194:195], 0\n\t"
"ds_read_b64_tr_b16 a[186:187], v251 offset:24192\n\t"
"v_mfma_f32_4x4x4_16b_bf16 v[220:223], a[220:221], a[202:203], 0\n\t"
"ds_read_b64_tr_b16 a[194:195], v248 offset:24192\n\t"
"v_mfma_f32_4x4x4_16b_bf16 v[192:195], a[222:223], a[176:177], v[200:203]\n\t"
"ds_read_b64_tr_b16 a[202:203], v249 offset:24192\n\t"
"v_mfma_f32_4x4x4_16b_bf16 v[196:199], a[222:223], a[184:185], v[204:207]\n\t"
"ds_read_b64_tr_b16 a[176:177], v248 offset:21888\n\t"
"v_mfma_f32_4x4x4_16b_bf16 v[200:203], a[222:223], a[180:181], v[200:203]\n\t"
"ds_read_b128 a[156:159], v224 offset:1792\n\t"
"v_mfma_f32_4x4x4_16b_bf16 v[204:207], a[222:223], a[188:189], v[204:207]\n\t"
"ds_read_b64_tr_b16 a[184:185], v249 offset:21888\n\t"
"v_mfma_f32_4x4x4_16b_bf16 v[208:211], a[222:223], a[192:193], v[216:219]\n\t"
"ds_read_b64_tr_b16 a[180:181], v248 offset:26496\n\t"
"v_mfma_f32_4x4x4_16b_bf16 v[212:215], a[222:223], a[200:201], v[220:223]\n\t"
"ds_read_b64_tr_b16 a[188:189], v249 offset:26496\n\t"
"v_mfma_f32_4x4x4_16b_bf16 v[216:219], a[222:223], a[196:197], v[216:219]\n\t"
"ds_read_b64_tr_b16 a[192:193], v250 offset:21888\n\t"
"v_mfma_f32_4x4x4_16b_bf16 v[220:223], a[222:223], a[204:205], v[220:223]\n\t"
"s_waitcnt vmcnt(26) lgkmcnt(9)\n\t"
"buffer_load_dwordx4 a[56:59], v252, s[8:11], s67 offen offset:2048 sc1\n\t"
"v_mfma_f32_16x16x32_bf16 v[48:51], a[96:99], a[144:147], v[48:51]\n\t"
"ds_read_b64_tr_b16 a[200:201], v251 offset:21888\n\t"
"v_cvt_pk_bf16_f32 v192, -v192, -v196\n\t"
"v_mfma_f32_16x16x32_bf16 v[48:51], a[100:103], a[160:163], v[48:51]\n\t"
"buffer_load_dwordx4 a[48:51], v252, s[8:11], s67 offen offset:0 sc1\n\t"
"v_mfma_f32_16x16x32_bf16 v[64:67], a[104:107], a[144:147], v[64:67]\n\t"
"ds_write_addtid_b32 v192 offset:16544\n\t"
"v_cvt_pk_bf16_f32 v201, v201, v205\n\t"
"v_mfma_f32_16x16x32_bf16 v[64:67], a[108:111], a[160:163], v[64:67]\n\t"
"buffer_load_dwordx4 a[52:55], v252, s[8:11], s67 offen offset:1024 sc1\n\t"
"v_mfma_f32_16x16x32_bf16 v[80:83], a[112:115], a[144:147], v[80:83]\n\t"
"ds_write_addtid_b32 v201 offset:28992\n\t"
"v_cvt_pk_bf16_f32 v195, -v195, -v199\n\t"
"v_mfma_f32_16x16x32_bf16 v[80:83], a[116:119], a[160:163], v[80:83]\n\t"
"ds_read_b128 a[160:163], v224 offset:2048\n\t"
"v_cvt_pk_bf16_f32 v194, -v194, -v198\n\t"
"v_mfma_f32_16x16x32_bf16 v[84:87], a[112:115], a[148:151], v[84:87]\n\t"
"ds_read_b128 a[144:147], v224 offset:0\n\t"
"v_cvt_pk_bf16_f32 v193, -v193, -v197\n\t"
"v_mfma_f32_16x16x32_bf16 v[84:87], a[116:119], a[164:167], v[84:87]\n\t"
"buffer_load_dwordx4 a[88:91], v235, s[8:11], s67 offen offset:2048 sc1\n\t"
"v_mfma_f32_16x16x32_bf16 v[68:71], a[104:107], a[148:151], v[68:71]\n\t"
"ds_write_addtid_b32 v194 offset:17568\n\t"
"v_cvt_pk_bf16_f32 v202, v202, v206\n\t"
"v_mfma_f32_16x16x32_bf16 v[68:71], a[108:111], a[164:167], v[68:71]\n\t"
"buffer_load_dwordx4 a[92:95], v235, s[8:11], s67 offen offset:3072 sc1\n\t"
"v_mfma_f32_16x16x32_bf16 v[52:55], a[96:99], a[148:151], v[52:55]\n\t"
"ds_read_b128 a[148:151], v224 offset:256\n\t"
"v_cvt_pk_bf16_f32 v200, v200, v204\n\t"
"v_mfma_f32_16x16x32_bf16 v[52:55], a[100:103], a[164:167], v[52:55]\n\t"
"ds_read_b128 a[164:167], v224 offset:2304\n\t"
"v_cvt_pk_bf16_f32 v219, v219, v223\n\t"
"v_mfma_f32_16x16x32_bf16 v[56:59], a[96:99], a[152:155], v[56:59]\n\t"
"ds_write_addtid_b32 v200 offset:20672\n\t"
"v_cvt_pk_bf16_f32 v218, v218, v222\n\t"
"v_mfma_f32_16x16x32_bf16 v[56:59], a[100:103], a[168:171], v[56:59]\n\t"
"buffer_load_dwordx4 a[84:87], v235, s[8:11], s67 offen offset:1024 sc1\n\t"
"v_mfma_f32_16x16x32_bf16 v[72:75], a[104:107], a[152:155], v[72:75]\n\t"
"ds_write_addtid_b32 v219 offset:32064\n\t"
"v_cvt_pk_bf16_f32 v203, v203, v207\n\t"
"v_mfma_f32_16x16x32_bf16 v[72:75], a[108:111], a[168:171], v[72:75]\n\t"
"ds_write_addtid_b32 v203 offset:30016\n\t"
"v_cvt_pk_bf16_f32 v216, v216, v220\n\t"
"v_mfma_f32_16x16x32_bf16 v[88:91], a[112:115], a[152:155], v[88:91]\n\t"
"ds_write_addtid_b32 v193 offset:24864\n\t"
"v_cvt_pk_bf16_f32 v209, -v209, -v213\n\t"
"v_mfma_f32_16x16x32_bf16 v[88:91], a[116:119], a[168:171], v[88:91]\n\t"
"s_waitcnt lgkmcnt(7)\n\t"
"buffer_load_dwordx4 a[80:83], v235, s[8:11], s67 offen offset:0 sc1\n\t"
"v_mfma_f32_16x16x32_bf16 v[92:95], a[112:115], a[156:159], v[92:95]\n\t"
"ds_write_addtid_b32 v209 offset:26912\n\t"
"v_cvt_pk_bf16_f32 v208, -v208, -v212\n\t"
"v_mfma_f32_16x16x32_bf16 v[92:95], a[116:119], a[172:175], v[92:95]\n\t"
"ds_write_addtid_b32 v202 offset:21696\n\t"
"v_cvt_pk_bf16_f32 v211, -v211, -v215\n\t"
"v_mfma_f32_16x16x32_bf16 v[76:79], a[104:107], a[156:159], v[76:79]\n\t"
"ds_write_addtid_b32 v216 offset:22720\n\t"
"v_cvt_pk_bf16_f32 v210, -v210, -v214\n\t"
"v_mfma_f32_16x16x32_bf16 v[76:79], a[108:111], a[172:175], v[76:79]\n\t"
"buffer_load_dwordx4 a[72:75], v252, s[8:11], s66 offen offset:2048 sc1\n\t"
"v_mfma_f32_16x16x32_bf16 v[60:63], a[96:99], a[156:159], v[60:63]\n\t"
"ds_write_addtid_b32 v218 offset:23744\n\t"
"v_cvt_pk_bf16_f32 v217, v217, v221\n\t"
"v_mfma_f32_16x16x32_bf16 v[60:63], a[100:103], a[172:175], v[60:63]\n\t"
"s_waitcnt vmcnt(23) lgkmcnt(7)\n\t"
"ds_read_b128 a[168:171], v224 offset:2560\n\t"
"ds_write_addtid_b32 v211 offset:27936\n\t"
"v_mfma_f32_16x16x32_bf16 v[0:3], a[120:123], a[144:147], v[0:3]\n\t"
"s_nop 0\n\t"
"ds_read_b128 a[172:175], v224 offset:2816\n\t"
"ds_write_addtid_b32 v208 offset:18592\n\t"
"v_mfma_f32_16x16x32_bf16 v[0:3], a[124:127], a[160:163], v[0:3]\n\t"
"ds_write_addtid_b32 v217 offset:31040\n\t"
"v_mfma_f32_16x16x32_bf16 v[16:19], a[128:131], a[144:147], v[16:19]\n\t"
"s_nop 0\n\t"
"ds_write_addtid_b32 v195 offset:25888\n\t"
"ds_write_addtid_b32 v210 offset:19616\n\t"
"v_mfma_f32_16x16x32_bf16 v[16:19], a[132:135], a[160:163], v[16:19]\n\t"
"buffer_load_dwordx4 a[76:79], v252, s[8:11], s66 offen offset:3072 sc1\n\t"
"v_mfma_f32_16x16x32_bf16 v[32:35], a[136:139], a[144:147], v[32:35]\n\t"
"ds_read_b128 a[152:155], v224 offset:512\n\t"
"v_mfma_f32_16x16x32_bf16 v[32:35], a[140:143], a[160:163], v[32:35]\n\t"
"ds_read_b128 a[156:159], v224 offset:768\n\t"
"v_mfma_f32_16x16x32_bf16 v[36:39], a[136:139], a[148:151], v[36:39]\n\t"
"ds_read_b64_tr_b16 a[196:197], v250 offset:26496\n\t"
"v_mfma_f32_16x16x32_bf16 v[36:39], a[140:143], a[164:167], v[36:39]\n\t"
"buffer_load_dwordx4 a[112:115], v235, s[8:11], s53 offen offset:0 sc1\n\t"
"v_mfma_f32_16x16x32_bf16 v[20:23], a[128:131], a[148:151], v[20:23]\n\t"
"ds_read_b64_tr_b16 a[204:205], v251 offset:26496\n\t"
"v_mfma_f32_16x16x32_bf16 v[20:23], a[132:135], a[164:167], v[20:23]\n\t"
"buffer_load_dwordx4 a[116:119], v235, s[8:11], s53 offen offset:1024 sc1\n\t"
"v_mfma_f32_16x16x32_bf16 v[4:7], a[120:123], a[148:151], v[4:7]\n\t"
"v_mfma_f32_16x16x32_bf16 v[4:7], a[124:127], a[164:167], v[4:7]\n\t"
"s_waitcnt lgkmcnt(0)\n\t"
"s_barrier\n\t"
"v_mfma_f32_16x16x32_bf16 v[8:11], a[120:123], a[152:155], v[8:11]\n\t"
"s_nop 0\n\t"
"ds_read_b128 a[144:147], v224 offset:17568\n\t"
"ds_read_b128 a[148:151], v224 offset:17824\n\t"
"v_mfma_f32_16x16x32_bf16 v[8:11], a[124:127], a[168:171], v[8:11]\n\t"
"buffer_load_dwordx4 a[108:111], v252, s[8:11], s52 offen offset:3072 sc1\n\t"
"v_mfma_f32_16x16x32_bf16 v[24:27], a[128:131], a[152:155], v[24:27]\n\t"
"ds_read_b128 a[160:163], v224 offset:19616\n\t"
"v_mfma_f32_16x16x32_bf16 v[24:27], a[132:135], a[168:171], v[24:27]\n\t"
"ds_read_b128 a[164:167], v224 offset:19872\n\t"
"v_mfma_f32_16x16x32_bf16 v[40:43], a[136:139], a[152:155], v[40:43]\n\t"
"ds_read_b128 a[228:231], v254 offset:39168\n\t"
"v_mfma_f32_16x16x32_bf16 v[40:43], a[140:143], a[168:171], v[40:43]\n\t"
"buffer_load_dwordx4 a[104:107], v252, s[8:11], s52 offen offset:2048 sc1\n\t"
"v_mfma_f32_16x16x32_bf16 v[44:47], a[136:139], a[156:159], v[44:47]\n\t"
"ds_read_b128 a[152:155], v224 offset:18080\n\t"
"v_mfma_f32_16x16x32_bf16 v[44:47], a[140:143], a[172:175], v[44:47]\n\t"
"buffer_load_dwordx4 a[96:99], v252, s[8:11], s52 offen offset:0 sc1\n\t"
"v_mfma_f32_16x16x32_bf16 v[28:31], a[128:131], a[156:159], v[28:31]\n\t"
"ds_read_b128 a[168:171], v224 offset:20128\n\t"
"v_mfma_f32_16x16x32_bf16 v[28:31], a[132:135], a[172:175], v[28:31]\n\t"
"buffer_load_dwordx4 a[100:103], v252, s[8:11], s52 offen offset:1024 sc1\n\t"
"v_mfma_f32_16x16x32_bf16 v[12:15], a[120:123], a[156:159], v[12:15]\n\t"
"ds_read_b128 a[156:159], v224 offset:18336\n\t"
"v_mfma_f32_16x16x32_bf16 v[12:15], a[124:127], a[172:175], v[12:15]\n\t"
"v_mfma_f32_4x4x4_16b_bf16 v[200:203], a[220:221], a[178:179], 0\n\t"
"ds_read_b64_tr_b16 a[178:179], v250 offset:3456\n\t"
"v_mfma_f32_4x4x4_16b_bf16 v[204:207], a[220:221], a[186:187], 0\n\t"
"buffer_load_dwordx4 a[136:139], v235, s[8:11], s52 offen offset:2048 sc1\n\t"
"v_mfma_f32_4x4x4_16b_bf16 v[216:219], a[220:221], a[194:195], 0\n\t"
"ds_read_b64_tr_b16 a[186:187], v251 offset:3456\n\t"
"v_mfma_f32_4x4x4_16b_bf16 v[220:223], a[220:221], a[202:203], 0\n\t"
"ds_read_b64_tr_b16 a[194:195], v248 offset:3456\n\t"
"v_mfma_f32_4x4x4_16b_bf16 v[192:195], a[222:223], a[176:177], v[200:203]\n\t"
"ds_read_b128 a[172:175], v224 offset:20384\n\t"
"v_mfma_f32_4x4x4_16b_bf16 v[196:199], a[222:223], a[184:185], v[204:207]\n\t"
"ds_read_b64_tr_b16 a[202:203], v249 offset:3456\n\t"
"v_mfma_f32_4x4x4_16b_bf16 v[200:203], a[222:223], a[180:181], v[200:203]\n\t"
"ds_read_b64_tr_b16 a[176:177], v248 offset:1152\n\t"
"v_mfma_f32_4x4x4_16b_bf16 v[204:207], a[222:223], a[188:189], v[204:207]\n\t"
"ds_read_b64_tr_b16 a[184:185], v249 offset:1152\n\t"
"v_mfma_f32_4x4x4_16b_bf16 v[208:211], a[222:223], a[192:193], v[216:219]\n\t"
"ds_read_b64_tr_b16 a[180:181], v248 offset:5760\n\t"
"v_mfma_f32_4x4x4_16b_bf16 v[212:215], a[222:223], a[200:201], v[220:223]\n\t"
"ds_read_b64_tr_b16 a[188:189], v249 offset:5760\n\t"
"v_mfma_f32_4x4x4_16b_bf16 v[216:219], a[222:223], a[196:197], v[216:219]\n\t"
"ds_read_b64_tr_b16 a[192:193], v250 offset:1152\n\t"
"v_mfma_f32_4x4x4_16b_bf16 v[220:223], a[222:223], a[204:205], v[220:223]\n\t"
"s_waitcnt vmcnt(25) lgkmcnt(9)\n\t"
"buffer_load_dwordx4 a[140:143], v235, s[8:11], s52 offen offset:3072 sc1\n\t"
"v_mfma_f32_16x16x32_bf16 v[48:51], a[0:3], a[144:147], v[48:51]\n\t"
"v_cvt_pk_bf16_f32 v193, -v193, -v197\n\t"
"v_cvt_pk_bf16_f32 v194, -v194, -v198\n\t"
"v_mfma_f32_16x16x32_bf16 v[48:51], a[4:7], a[160:163], v[48:51]\n\t"
"ds_write_b128 v255, a[228:231] offset:20736\n\t"
"v_mfma_f32_16x16x32_bf16 v[64:67], a[8:11], a[144:147], v[64:67]\n\t"
"v_cvt_pk_bf16_f32 v202, v202, v206\n\t"
"v_cvt_pk_bf16_f32 v203, v203, v207\n\t"
"v_mfma_f32_16x16x32_bf16 v[64:67], a[12:15], a[160:163], v[64:67]\n\t"
"buffer_load_dwordx4 a[132:135], v235, s[8:11], s52 offen offset:1024 sc1\n\t"
"v_mfma_f32_16x16x32_bf16 v[80:83], a[16:19], a[144:147], v[80:83]\n\t"
"v_cvt_pk_bf16_f32 v195, -v195, -v199\n\t"
"v_cvt_pk_bf16_f32 v210, -v210, -v214\n\t"
"v_mfma_f32_16x16x32_bf16 v[80:83], a[20:23], a[160:163], v[80:83]\n\t"
"buffer_load_dwordx4 a[128:131], v235, s[8:11], s52 offen offset:0 sc1\n\t"
"v_mfma_f32_16x16x32_bf16 v[84:87], a[16:19], a[148:151], v[84:87]\n\t"
"ds_write_addtid_b32 v195 offset:9344\n\t"
"v_cvt_pk_bf16_f32 v217, v217, v221\n\t"
"v_mfma_f32_16x16x32_bf16 v[84:87], a[20:23], a[164:167], v[84:87]\n\t"
"s_nop 0\n\t"
"ds_read_b128 a[160:163], v224 offset:18592\n\t"
"ds_write_addtid_b32 v202 offset:5152\n\t"
"v_mfma_f32_16x16x32_bf16 v[68:71], a[8:11], a[148:151], v[68:71]\n\t"
"ds_read_b128 a[144:147], v224 offset:16544\n\t"
"v_cvt_pk_bf16_f32 v219, v219, v223\n\t"
"v_mfma_f32_16x16x32_bf16 v[68:71], a[12:15], a[164:167], v[68:71]\n\t"
"buffer_load_dwordx4 a[120:123], v252, s[8:11], s51 offen offset:2048 sc1\n\t"
"v_mfma_f32_16x16x32_bf16 v[52:55], a[0:3], a[148:151], v[52:55]\n\t"
"ds_write_addtid_b32 v203 offset:13472\n\t"
"v_cvt_pk_bf16_f32 v201, v201, v205\n\t"
"v_mfma_f32_16x16x32_bf16 v[52:55], a[4:7], a[164:167], v[52:55]\n\t"
"ds_read_b128 a[164:167], v224 offset:18848\n\t"
"v_cvt_pk_bf16_f32 v209, -v209, -v213\n\t"
"v_mfma_f32_16x16x32_bf16 v[56:59], a[0:3], a[152:155], v[56:59]\n\t"
"ds_read_b128 a[148:151], v224 offset:16800\n\t"
"v_mfma_f32_16x16x32_bf16 v[56:59], a[4:7], a[168:171], v[56:59]\n\t"
"buffer_load_dwordx4 a[124:127], v252, s[8:11], s51 offen offset:3072 sc1\n\t"
"v_mfma_f32_16x16x32_bf16 v[72:75], a[8:11], a[152:155], v[72:75]\n\t"
"ds_write_addtid_b32 v194 offset:1024\n\t"
"v_cvt_pk_bf16_f32 v211, -v211, -v215\n\t"
"v_mfma_f32_16x16x32_bf16 v[72:75], a[12:15], a[168:171], v[72:75]\n\t"
"ds_write_addtid_b32 v209 offset:10368\n\t"
"v_cvt_pk_bf16_f32 v208, -v208, -v212\n\t"
"v_mfma_f32_16x16x32_bf16 v[88:91], a[16:19], a[152:155], v[88:91]\n\t"
"ds_write_addtid_b32 v217 offset:14496\n\t"
"v_cvt_pk_bf16_f32 v192, -v192, -v196\n\t"
"v_mfma_f32_16x16x32_bf16 v[88:91], a[20:23], a[168:171], v[88:91]\n\t"
"s_waitcnt lgkmcnt(7)\n\t"
"ds_write_addtid_b32 v201 offset:12448\n\t"
"v_cvt_pk_bf16_f32 v218, v218, v222\n\t"
"v_mfma_f32_16x16x32_bf16 v[92:95], a[16:19], a[156:159], v[92:95]\n\t"
"s_add_u32 m0, s26, 124544\n\t"
"ds_read_b128 a[152:155], v224 offset:17056\n\t"
"v_cvt_pk_bf16_f32 v200, v200, v204\n\t"
"v_mfma_f32_16x16x32_bf16 v[92:95], a[20:23], a[172:175], v[92:95]\n\t"
"buffer_load_dwordx4 v236, s[12:15], s22 offen offset:128 nt lds\n\t"
"s_lshl_b32 m0, s73, 8\n\t"
"v_mfma_f32_16x16x32_bf16 v[76:79], a[8:11], a[156:159], v[76:79]\n\t"
"s_nop 0\n\t"
"ds_write_addtid_b32 v192 offset:0\n\t"
"ds_write_addtid_b32 v193 offset:8320\n\t"
"v_mfma_f32_16x16x32_bf16 v[76:79], a[12:15], a[172:175], v[76:79]\n\t"
"s_nop 0\n\t"
"ds_write_addtid_b32 v208 offset:2048\n\t"
"ds_write_addtid_b32 v210 offset:3072\n\t"
"v_mfma_f32_16x16x32_bf16 v[60:63], a[0:3], a[156:159], v[60:63]\n\t"
"s_nop 0\n\t"
"ds_write_addtid_b32 v200 offset:4128\n\t"
"ds_write_addtid_b32 v211 offset:11392\n\t"
"s_add_u32 m0, s26, 124544\n\t"
"v_mfma_f32_16x16x32_bf16 v[60:63], a[4:7], a[172:175], v[60:63]\n\t"
"s_waitcnt vmcnt(25) lgkmcnt(11)\n\t"
"buffer_load_dwordx4 v237, s[12:15], s22 offen offset:1152 nt lds\n\t"
"s_lshl_b32 m0, s73, 8\n\t"
"v_mfma_f32_16x16x32_bf16 v[0:3], a[24:27], a[144:147], v[0:3]\n\t"
"ds_write_addtid_b32 v219 offset:15520\n\t"
"v_cvt_pk_bf16_f32 v216, v216, v220\n\t"
"s_add_u32 m0, s26, 124544\n\t"
"v_mfma_f32_16x16x32_bf16 v[0:3], a[28:31], a[160:163], v[0:3]\n\t"
"buffer_load_dwordx4 v238, s[12:15], s22 offen offset:2176 nt lds\n\t"
"s_lshl_b32 m0, s73, 8\n\t"
"v_mfma_f32_16x16x32_bf16 v[16:19], a[32:35], a[144:147], v[16:19]\n\t"
"s_nop 0\n\t"
"ds_write_addtid_b32 v216 offset:6176\n\t"
"ds_write_addtid_b32 v218 offset:7200\n\t"
"v_mfma_f32_16x16x32_bf16 v[16:19], a[36:39], a[160:163], v[16:19]\n\t"
"s_add_u32 m0, s26, 124544\n\t"
"ds_read_b128 a[168:171], v224 offset:19104\n\t"
"ds_read_b128 a[156:159], v224 offset:17312\n\t"
"v_mfma_f32_16x16x32_bf16 v[32:35], a[40:43], a[144:147], v[32:35]\n\t"
"ds_read_b128 a[172:175], v224 offset:19360\n\t"
"v_mfma_f32_16x16x32_bf16 v[32:35], a[44:47], a[160:163], v[32:35]\n\t"
"buffer_load_dwordx4 v239, s[12:15], s22 offen offset:3200 nt lds\n\t"
"v_mfma_f32_16x16x32_bf16 v[36:39], a[40:43], a[148:151], v[36:39]\n\t"
"ds_read_b64_tr_b16 a[200:201], v251 offset:1152\n\t"
"v_mfma_f32_16x16x32_bf16 v[36:39], a[44:47], a[164:167], v[36:39]\n\t"
"ds_read_b64_tr_b16 a[196:197], v250 offset:5760\n\t"
"v_mfma_f32_16x16x32_bf16 v[20:23], a[32:35], a[148:151], v[20:23]\n\t"
"ds_read_b64_tr_b16 a[204:205], v251 offset:5760\n\t"
"v_mfma_f32_16x16x32_bf16 v[20:23], a[36:39], a[164:167], v[20:23]\n\t"
"s_mov_b64 exec, s[36:37]\n\t"
"buffer_load_dwordx4 v240, s[12:15], s22 offen offset:3712 nt lds\n\t"
"s_mov_b64 exec, -1\n\t"
"v_mfma_f32_16x16x32_bf16 v[4:7], a[24:27], a[148:151], v[4:7]\n\t"
"s_lshl_b32 m0, s73, 8\n\t"
"v_mfma_f32_16x16x32_bf16 v[4:7], a[28:31], a[164:167], v[4:7]\n\t"
"s_waitcnt lgkmcnt(0)\n\t"
"s_barrier\n\t"
"v_mfma_f32_16x16x32_bf16 v[8:11], a[24:27], a[152:155], v[8:11]\n\t"
"ds_read_b128 a[144:147], v224 offset:1024\n\t"
"v_mfma_f32_16x16x32_bf16 v[8:11], a[28:31], a[168:171], v[8:11]\n\t"
"ds_read_b128 a[160:163], v224 offset:3072\n\t"
"v_add_u32 v248, 33280, v227\n\t"
"v_mfma_f32_16x16x32_bf16 v[24:27], a[32:35], a[152:155], v[24:27]\n\t"
"ds_read_b128 a[148:151], v224 offset:1280\n\t"
"v_xor_b32 v249, 32, v248\n\t"
"v_mfma_f32_16x16x32_bf16 v[24:27], a[36:39], a[168:171], v[24:27]\n\t"
"ds_read_b128 a[164:167], v224 offset:3328\n\t"
"v_xor_b32 v250, 64, v248\n\t"
"v_mfma_f32_16x16x32_bf16 v[40:43], a[40:43], a[152:155], v[40:43]\n\t"
"ds_read_b128 a[152:155], v224 offset:1536\n\t"
"v_xor_b32 v251, 96, v248\n\t"
"v_mfma_f32_16x16x32_bf16 v[40:43], a[44:47], a[168:171], v[40:43]\n\t"
"ds_read_b128 a[168:171], v224 offset:3584\n\t"
"v_mfma_f32_16x16x32_bf16 v[44:47], a[40:43], a[156:159], v[44:47]\n\t"
"ds_read_b64_tr_b16 a[182:183], v249 offset:41984\n\t"
"v_mfma_f32_16x16x32_bf16 v[44:47], a[44:47], a[172:175], v[44:47]\n\t"
"ds_read_b64_tr_b16 a[190:191], v251 offset:41984\n\t"
"v_mfma_f32_16x16x32_bf16 v[28:31], a[32:35], a[156:159], v[28:31]\n\t"
"ds_read_b64_tr_b16 a[198:199], v249 offset:21248\n\t"
"v_mfma_f32_16x16x32_bf16 v[28:31], a[36:39], a[172:175], v[28:31]\n\t"
"ds_read_b64_tr_b16 a[206:207], v251 offset:21248\n\t"
"v_mfma_f32_16x16x32_bf16 v[12:15], a[24:27], a[156:159], v[12:15]\n\t"
"ds_read_b128 a[156:159], v224 offset:1792\n\t"
"v_mfma_f32_16x16x32_bf16 v[12:15], a[28:31], a[172:175], v[12:15]\n\t"
"ds_read_b128 a[172:175], v224 offset:3840\n\t"
"v_mfma_f32_4x4x4_16b_bf16 v[200:203], a[220:221], a[178:179], 0\n\t"
"ds_read_b64_tr_b16 a[178:179], v248 offset:41984\n\t"
"v_mfma_f32_4x4x4_16b_bf16 v[204:207], a[220:221], a[186:187], 0\n\t"
"ds_read_b64_tr_b16 a[186:187], v250 offset:41984\n\t"
"v_mfma_f32_4x4x4_16b_bf16 v[216:219], a[220:221], a[194:195], 0\n\t"
"ds_read_b64_tr_b16 a[194:195], v248 offset:21248\n\t"
"v_mfma_f32_4x4x4_16b_bf16 v[220:223], a[220:221], a[202:203], 0\n\t"
"ds_read_b64_tr_b16 a[202:203], v250 offset:21248\n\t"
"v_mfma_f32_4x4x4_16b_bf16 v[192:195], a[222:223], a[176:177], v[200:203]\n\t"
"ds_read_b64_tr_b16 a[176:177], v248 offset:41472\n\t"
"v_mfma_f32_4x4x4_16b_bf16 v[196:199], a[222:223], a[184:185], v[204:207]\n\t"
".p2align 3\n\t"
"ds_read_b64_tr_b16 a[184:185], v250 offset:41472\n\t"
"v_pk_add_f32 v[0:1], v[0:1], v[48:49]\n\t"
"v_pk_add_f32 v[2:3], v[2:3], v[50:51]\n\t"
"v_pk_fma_f32 v[48:49], v[48:49], s[38:39], v[0:1]\n\t"
"v_pk_fma_f32 v[50:51], v[50:51], s[38:39], v[2:3]\n\t"
"v_pk_add_f32 v[16:17], v[16:17], v[64:65]\n\t"
"v_pk_add_f32 v[18:19], v[18:19], v[66:67]\n\t"
"v_pk_fma_f32 v[64:65], v[64:65], s[38:39], v[16:17]\n\t"
"v_pk_fma_f32 v[66:67], v[66:67], s[38:39], v[18:19]\n\t"
"v_pk_add_f32 v[32:33], v[32:33], v[80:81]\n\t"
"v_pk_add_f32 v[34:35], v[34:35], v[82:83]\n\t"
"v_pk_fma_f32 v[80:81], v[80:81], s[38:39], v[32:33]\n\t"
"v_pk_fma_f32 v[82:83], v[82:83], s[38:39], v[34:35]\n\t"
"v_pk_add_f32 v[36:37], v[36:37], v[84:85]\n\t"
"v_pk_add_f32 v[38:39], v[38:39], v[86:87]\n\t"
"v_pk_fma_f32 v[84:85], v[84:85], s[38:39], v[36:37]\n\t"
"v_pk_fma_f32 v[86:87], v[86:87], s[38:39], v[38:39]\n\t"
"v_pk_add_f32 v[20:21], v[20:21], v[68:69]\n\t"
"v_pk_add_f32 v[22:23], v[22:23], v[70:71]\n\t"
"v_pk_fma_f32 v[68:69], v[68:69], s[38:39], v[20:21]\n\t"
"v_pk_fma_f32 v[70:71], v[70:71], s[38:39], v[22:23]\n\t"
"v_pk_add_f32 v[4:5], v[4:5], v[52:53]\n\t"
"v_pk_add_f32 v[6:7], v[6:7], v[54:55]\n\t"
"v_pk_fma_f32 v[52:53], v[52:53], s[38:39], v[4:5]\n\t"
"v_pk_fma_f32 v[54:55], v[54:55], s[38:39], v[6:7]\n\t"
"v_pk_add_f32 v[8:9], v[8:9], v[56:57]\n\t"
"v_pk_add_f32 v[10:11], v[10:11], v[58:59]\n\t"
"v_pk_fma_f32 v[56:57], v[56:57], s[38:39], v[8:9]\n\t"
"v_pk_fma_f32 v[58:59], v[58:59], s[38:39], v[10:11]\n\t"
"v_pk_add_f32 v[24:25], v[24:25], v[72:73]\n\t"
"v_pk_add_f32 v[26:27], v[26:27], v[74:75]\n\t"
"v_pk_fma_f32 v[72:73], v[72:73], s[38:39], v[24:25]\n\t"
"v_pk_fma_f32 v[74:75], v[74:75], s[38:39], v[26:27]\n\t"
"v_pk_add_f32 v[40:41], v[40:41], v[88:89]\n\t"
"v_pk_add_f32 v[42:43], v[42:43], v[90:91]\n\t"
"v_pk_fma_f32 v[88:89], v[88:89], s[38:39], v[40:41]\n\t"
"v_pk_fma_f32 v[90:91], v[90:91], s[38:39], v[42:43]\n\t"
"v_pk_add_f32 v[44:45], v[44:45], v[92:93]\n\t"
"v_pk_add_f32 v[46:47], v[46:47], v[94:95]\n\t"
"v_pk_fma_f32 v[92:93], v[92:93], s[38:39], v[44:45]\n\t"
"v_pk_fma_f32 v[94:95], v[94:95], s[38:39], v[46:47]\n\t"
"v_pk_add_f32 v[28:29], v[28:29], v[76:77]\n\t"
"v_pk_add_f32 v[30:31], v[30:31], v[78:79]\n\t"
"v_pk_fma_f32 v[76:77], v[76:77], s[38:39], v[28:29]\n\t"
"v_pk_fma_f32 v[78:79], v[78:79], s[38:39], v[30:31]\n\t"
"v_pk_add_f32 v[12:13], v[12:13], v[60:61]\n\t"
"v_pk_add_f32 v[14:15], v[14:15], v[62:63]\n\t"
"v_pk_fma_f32 v[60:61], v[60:61], s[38:39], v[12:13]\n\t"
"v_pk_fma_f32 v[62:63], v[62:63], s[38:39], v[14:15]\n\t"
"v_mfma_f32_4x4x4_16b_bf16 v[200:203], a[222:223], a[180:181], v[200:203]\n\t"
"v_cvt_pk_bf16_f32 v0, v0, v1\n\t"
"v_mfma_f32_4x4x4_16b_bf16 v[204:207], a[222:223], a[188:189], v[204:207]\n\t"
"v_cvt_pk_bf16_f32 v4, v4, v5\n\t"
"v_mfma_f32_4x4x4_16b_bf16 v[208:211], a[222:223], a[192:193], v[216:219]\n\t"
"v_cvt_pk_bf16_f32 v5, v6, v7\n\t"
"v_mfma_f32_4x4x4_16b_bf16 v[212:215], a[222:223], a[200:201], v[220:223]\n\t"
"v_cvt_pk_bf16_f32 v1, v2, v3\n\t"
"v_mfma_f32_4x4x4_16b_bf16 v[216:219], a[222:223], a[196:197], v[216:219]\n\t"
"ds_write2st64_b64 v228, v[0:1], v[4:5] offset0:40 offset1:53\n\t"
"v_cvt_pk_bf16_f32 v16, v16, v17\n\t"
"v_mfma_f32_4x4x4_16b_bf16 v[220:223], a[222:223], a[204:205], v[220:223]\n\t"
"s_waitcnt vmcnt(23) lgkmcnt(5)\n\t"
"v_cvt_pk_bf16_f32 v17, v18, v19\n\t"
"v_cvt_pk_bf16_f32 v20, v20, v21\n\t"
"v_cvt_pk_bf16_f32 v32, v32, v33\n\t"
"v_mfma_f32_16x16x32_bf16 v[144:147], a[48:51], a[144:147], v[144:147]\n\t"
"v_cvt_pk_bf16_f32 v21, v22, v23\n\t"
"v_cvt_pk_bf16_f32 v36, v36, v37\n\t"
"v_mfma_f32_16x16x32_bf16 v[144:147], a[52:55], a[160:163], v[144:147]\n\t"
"ds_write2st64_b64 v246, v[16:17], v[20:21] offset0:40 offset1:53\n\t"
"v_mfma_f32_16x16x32_bf16 v[160:163], a[56:59], a[144:147], v[160:163]\n\t"
"v_cvt_pk_bf16_f32 v37, v38, v39\n\t"
"v_cvt_pk_bf16_f32 v8, v8, v9\n\t"
"v_mfma_f32_16x16x32_bf16 v[160:163], a[60:63], a[160:163], v[160:163]\n\t"
"buffer_load_dwordx4 a[16:19], v252, s[84:87], s69 offen offset:0 sc1\n\t"
"v_mfma_f32_16x16x32_bf16 v[176:179], a[64:67], a[144:147], v[176:179]\n\t"
"v_cvt_pk_bf16_f32 v202, v202, v206\n\t"
"v_cvt_pk_bf16_f32 v33, v34, v35\n\t"
"v_mfma_f32_16x16x32_bf16 v[176:179], a[68:71], a[160:163], v[176:179]\n\t"
"ds_read_b128 a[144:147], v224 offset:0\n\t"
"v_cvt_pk_bf16_f32 v12, v12, v13\n\t"
"v_mfma_f32_16x16x32_bf16 v[180:183], a[64:67], a[148:151], v[180:183]\n\t"
"v_cvt_pk_bf16_f32 v209, -v209, -v213\n\t"
"v_cvt_pk_bf16_f32 v24, v24, v25\n\t"
"v_mfma_f32_16x16x32_bf16 v[180:183], a[68:71], a[164:167], v[180:183]\n\t"
"ds_read_b128 a[160:163], v224 offset:2048\n\t"
"ds_write2st64_b64 v247, v[32:33], v[36:37] offset0:40 offset1:53\n\t"
"v_mfma_f32_16x16x32_bf16 v[164:167], a[56:59], a[148:151], v[164:167]\n\t"
"v_cvt_pk_bf16_f32 v203, v203, v207\n\t"
"v_cvt_pk_bf16_f32 v44, v44, v45\n\t"
"v_mfma_f32_16x16x32_bf16 v[164:167], a[60:63], a[164:167], v[164:167]\n\t"
"v_cvt_pk_bf16_f32 v9, v10, v11\n\t"
"v_cvt_pk_bf16_f32 v13, v14, v15\n\t"
"v_mfma_f32_16x16x32_bf16 v[148:151], a[48:51], a[148:151], v[148:151]\n\t"
"v_cvt_pk_bf16_f32 v193, -v193, -v197\n\t"
"v_cvt_pk_bf16_f32 v194, -v194, -v198\n\t"
"v_mfma_f32_16x16x32_bf16 v[148:151], a[52:55], a[164:167], v[148:151]\n\t"
"s_nop 0\n\t"
"ds_read_b128 a[148:151], v224 offset:256\n\t"
"ds_read_b128 a[164:167], v224 offset:2304\n\t"
"ds_write_addtid_b32 v209 offset:26912\n\t"
"ds_write2st64_b64 v228, v[8:9], v[12:13] offset0:66 offset1:79\n\t"
"v_mfma_f32_16x16x32_bf16 v[152:155], a[48:51], a[152:155], v[152:155]\n\t"
"v_cvt_pk_bf16_f32 v210, -v210, -v214\n\t"
"v_cvt_pk_bf16_f32 v219, v219, v223\n\t"
"v_mfma_f32_16x16x32_bf16 v[152:155], a[52:55], a[168:171], v[152:155]\n\t"
"v_cvt_pk_bf16_f32 v28, v28, v29\n\t"
"v_cvt_pk_bf16_f32 v40, v40, v41\n\t"
"v_mfma_f32_16x16x32_bf16 v[168:171], a[56:59], a[152:155], v[168:171]\n\t"
"v_cvt_pk_bf16_f32 v25, v26, v27\n\t"
"v_cvt_pk_bf16_f32 v29, v30, v31\n\t"
"v_mfma_f32_16x16x32_bf16 v[168:171], a[60:63], a[168:171], v[168:171]\n\t"
"ds_write2st64_b64 v246, v[24:25], v[28:29] offset0:66 offset1:79\n\t"
"v_mfma_f32_16x16x32_bf16 v[184:187], a[64:67], a[152:155], v[184:187]\n\t"
"v_cvt_pk_bf16_f32 v195, -v195, -v199\n\t"
"v_cvt_pk_bf16_f32 v200, v200, v204\n\t"
"v_mfma_f32_16x16x32_bf16 v[184:187], a[68:71], a[168:171], v[184:187]\n\t"
"v_cvt_pk_bf16_f32 v201, v201, v205\n\t"
"v_cvt_pk_bf16_f32 v45, v46, v47\n\t"
"v_mfma_f32_16x16x32_bf16 v[188:191], a[64:67], a[156:159], v[188:191]\n\t"
"v_cvt_pk_bf16_f32 v211, -v211, -v215\n\t"
"v_cvt_pk_bf16_f32 v41, v42, v43\n\t"
"v_mfma_f32_16x16x32_bf16 v[188:191], a[68:71], a[172:175], v[188:191]\n\t"
"ds_write_addtid_b32 v203 offset:30016\n\t"
"ds_write2st64_b64 v247, v[40:41], v[44:45] offset0:66 offset1:79\n\t"
"v_mfma_f32_16x16x32_bf16 v[172:175], a[56:59], a[156:159], v[172:175]\n\t"
"v_cvt_pk_bf16_f32 v192, -v192, -v196\n\t"
"v_cvt_pk_bf16_f32 v218, v218, v222\n\t"
"v_mfma_f32_16x16x32_bf16 v[172:175], a[60:63], a[172:175], v[172:175]\n\t"
"ds_write_addtid_b32 v218 offset:23744\n\t"
"v_cvt_pk_bf16_f32 v208, -v208, -v212\n\t"
"v_mfma_f32_16x16x32_bf16 v[156:159], a[48:51], a[156:159], v[156:159]\n\t"
"v_cvt_pk_bf16_f32 v216, v216, v220\n\t"
"v_cvt_pk_bf16_f32 v217, v217, v221\n\t"
"v_mfma_f32_16x16x32_bf16 v[156:159], a[52:55], a[172:175], v[156:159]\n\t"
"s_waitcnt vmcnt(18) lgkmcnt(4)\n\t"
"ds_write_addtid_b32 v193 offset:24864\n\t"
"ds_write_addtid_b32 v200 offset:20672\n\t"
"ds_write_addtid_b32 v211 offset:27936\n\t"
"ds_write_addtid_b32 v216 offset:22720\n\t"
"ds_write_addtid_b32 v219 offset:32064\n\t"
"v_mfma_f32_16x16x32_bf16 v[96:99], a[72:75], a[144:147], v[96:99]\n\t"
"s_nop 0\n\t"
"ds_write_addtid_b32 v195 offset:25888\n\t"
"ds_write_addtid_b32 v210 offset:19616\n\t"
"v_mfma_f32_16x16x32_bf16 v[96:99], a[76:79], a[160:163], v[96:99]\n\t"
"s_nop 0\n\t"
"ds_write_addtid_b32 v194 offset:17568\n\t"
"ds_write_addtid_b32 v202 offset:21696\n\t"
"ds_write_addtid_b32 v208 offset:18592\n\t"
"ds_write_addtid_b32 v217 offset:31040\n\t"
"v_mfma_f32_16x16x32_bf16 v[112:115], a[80:83], a[144:147], v[112:115]\n\t"
"s_nop 0\n\t"
"ds_write_addtid_b32 v192 offset:16544\n\t"
"ds_write_addtid_b32 v201 offset:28992\n\t"
"v_mfma_f32_16x16x32_bf16 v[112:115], a[84:87], a[160:163], v[112:115]\n\t"
"s_nop 0\n\t"
"ds_read_b128 a[168:171], v224 offset:2560\n\t"
"ds_read_b128 a[172:175], v224 offset:2816\n\t"
"v_mfma_f32_16x16x32_bf16 v[128:131], a[88:91], a[144:147], v[128:131]\n\t"
"s_nop 0\n\t"
"ds_read_b128 a[152:155], v224 offset:512\n\t"
"ds_read_b64_tr_b16 a[180:181], v249 offset:41472\n\t"
"v_mfma_f32_16x16x32_bf16 v[128:131], a[92:95], a[160:163], v[128:131]\n\t"
"s_nop 0\n\t"
"ds_read_b128 a[156:159], v224 offset:768\n\t"
"ds_read_b64_tr_b16 a[188:189], v251 offset:41472\n\t"
"v_mfma_f32_16x16x32_bf16 v[132:135], a[88:91], a[148:151], v[132:135]\n\t"
"ds_read_b64_tr_b16 a[192:193], v248 offset:20736\n\t"
"v_mfma_f32_16x16x32_bf16 v[132:135], a[92:95], a[164:167], v[132:135]\n\t"
"ds_read_b64_tr_b16 a[200:201], v250 offset:20736\n\t"
"v_mfma_f32_16x16x32_bf16 v[116:119], a[80:83], a[148:151], v[116:119]\n\t"
"s_nop 0\n\t"
"ds_read_b64_tr_b16 a[196:197], v249 offset:20736\n\t"
"ds_read_b64_tr_b16 a[204:205], v251 offset:20736\n\t"
"v_mfma_f32_16x16x32_bf16 v[116:119], a[84:87], a[164:167], v[116:119]\n\t"
"v_mfma_f32_16x16x32_bf16 v[100:103], a[72:75], a[148:151], v[100:103]\n\t"
"s_waitcnt lgkmcnt(5)\n\t"
"v_mfma_f32_16x16x32_bf16 v[100:103], a[76:79], a[164:167], v[100:103]\n\t"
"s_barrier\n\t"
"s_cmp_eq_u32 s21, 1\n\t"
"s_cbranch_scc1 Ltail_%=\n\t"
"v_mfma_f32_16x16x32_bf16 v[104:107], a[72:75], a[152:155], v[104:107]\n\t"
"ds_read_b128 a[148:151], v224 offset:17824\n\t"
"v_mfma_f32_16x16x32_bf16 v[104:107], a[76:79], a[168:171], v[104:107]\n\t"
"ds_read_b128 a[144:147], v224 offset:17568\n\t"
"v_mfma_f32_16x16x32_bf16 v[120:123], a[80:83], a[152:155], v[120:123]\n\t"
"ds_read_b128 a[160:163], v224 offset:19616\n\t"
"v_mfma_f32_16x16x32_bf16 v[120:123], a[84:87], a[168:171], v[120:123]\n\t"
"ds_read_b128 a[164:167], v224 offset:19872\n\t"
"v_mfma_f32_16x16x32_bf16 v[136:139], a[88:91], a[152:155], v[136:139]\n\t"
"v_mfma_f32_16x16x32_bf16 v[136:139], a[92:95], a[168:171], v[136:139]\n\t"
"buffer_load_dwordx4 a[20:23], v252, s[84:87], s69 offen offset:1024 sc1\n\t"
"v_mfma_f32_16x16x32_bf16 v[140:143], a[88:91], a[156:159], v[140:143]\n\t"
"ds_read_b128 a[152:155], v224 offset:18080\n\t"
"v_mfma_f32_16x16x32_bf16 v[140:143], a[92:95], a[172:175], v[140:143]\n\t"
"buffer_load_dwordx4 a[12:15], v235, s[84:87], s69 offen offset:3072 sc1\n\t"
"v_mfma_f32_16x16x32_bf16 v[124:127], a[80:83], a[156:159], v[124:127]\n\t"
"ds_read_b128 v[8:11], v229 offset:20480\n\t"
"v_mfma_f32_16x16x32_bf16 v[124:127], a[84:87], a[172:175], v[124:127]\n\t"
"ds_read_b128 a[168:171], v224 offset:20128\n\t"
"v_mfma_f32_16x16x32_bf16 v[108:111], a[72:75], a[156:159], v[108:111]\n\t"
"ds_read_b128 a[156:159], v224 offset:18336\n\t"
"v_mfma_f32_16x16x32_bf16 v[108:111], a[76:79], a[172:175], v[108:111]\n\t"
"buffer_load_dwordx4 a[8:11], v235, s[84:87], s69 offen offset:2048 sc1\n\t"
"v_mfma_f32_16x16x32_bf16 v[192:195], a[224:227], a[176:179], 0\n\t"
"s_nop 0\n\t"
"ds_read_b64_tr_b16 a[176:177], v248 offset:0\n\t"
"ds_read_b64_tr_b16 a[178:179], v248 offset:512\n\t"
"v_mfma_f32_16x16x32_bf16 v[196:199], a[224:227], a[180:183], 0\n\t"
"s_nop 0\n\t"
"ds_read_b64_tr_b16 a[180:181], v249 offset:0\n\t"
"ds_read_b64_tr_b16 a[182:183], v249 offset:512\n\t"
"v_mfma_f32_16x16x32_bf16 v[200:203], a[224:227], a[184:187], 0\n\t"
"s_waitcnt lgkmcnt(8)\n\t"
"buffer_load_dwordx4 a[0:3], v235, s[84:87], s69 offen offset:0 sc1\n\t"
"v_mfma_f32_16x16x32_bf16 v[204:207], a[224:227], a[188:191], 0\n\t"
"s_nop 0\n\t"
"ds_read_b64_tr_b16 a[184:185], v250 offset:0\n\t"
"ds_read_b64_tr_b16 a[186:187], v250 offset:512\n\t"
"v_mfma_f32_16x16x32_bf16 v[208:211], a[224:227], a[192:195], 0\n\t"
"s_nop 0\n\t"
"ds_read_b64_tr_b16 a[188:189], v251 offset:0\n\t"
"ds_read_b64_tr_b16 a[190:191], v251 offset:512\n\t"
"v_mfma_f32_16x16x32_bf16 v[212:215], a[224:227], a[196:199], 0\n\t"
"s_nop 0\n\t"
"ds_read_b64_tr_b16 a[192:193], v248 offset:1152\n\t"
"ds_read_b64_tr_b16 a[194:195], v248 offset:1664\n\t"
"v_mfma_f32_16x16x32_bf16 v[216:219], a[224:227], a[200:203], 0\n\t"
"s_waitcnt lgkmcnt(12)\n\t"
"buffer_store_dwordx4 v[8:11], v232, s[16:19], s24 offen nt\n\t"
"v_mfma_f32_16x16x32_bf16 v[220:223], a[224:227], a[204:207], 0\n\t"
"s_waitcnt vmcnt(17) lgkmcnt(11)\n\t"
"ds_read_b128 a[172:175], v224 offset:20384\n\t"
"v_cvt_pk_bf16_f32 v192, v192, v196\n\t"
"v_mfma_f32_16x16x32_bf16 v[144:147], a[96:99], a[144:147], v[144:147]\n\t"
"ds_write_addtid_b32 v192 offset:0\n\t"
"v_cvt_pk_bf16_f32 v194, v194, v198\n\t"
"v_mfma_f32_16x16x32_bf16 v[144:147], a[100:103], a[160:163], v[144:147]\n\t"
"buffer_load_dwordx4 a[4:7], v235, s[84:87], s69 offen offset:1024 sc1\n\t"
"v_mfma_f32_16x16x32_bf16 v[160:163], a[104:107], a[144:147], v[160:163]\n\t"
"ds_read_b64_tr_b16 a[196:197], v249 offset:1152\n\t"
"v_cvt_pk_bf16_f32 v201, v201, v205\n\t"
"v_mfma_f32_16x16x32_bf16 v[160:163], a[108:111], a[160:163], v[160:163]\n\t"
"ds_write_addtid_b32 v201 offset:10368\n\t"
"v_cvt_pk_bf16_f32 v208, v208, v212\n\t"
"v_mfma_f32_16x16x32_bf16 v[176:179], a[112:115], a[144:147], v[176:179]\n\t"
"ds_read_b128 a[144:147], v224 offset:16544\n\t"
"v_cvt_pk_bf16_f32 v211, v211, v215\n\t"
"v_mfma_f32_16x16x32_bf16 v[176:179], a[116:119], a[160:163], v[176:179]\n\t"
"buffer_load_dwordx4 a[40:43], v252, s[84:87], s54 offen offset:0 sc1\n\t"
"v_mfma_f32_16x16x32_bf16 v[180:183], a[112:115], a[148:151], v[180:183]\n\t"
"s_nop 0\n\t"
"ds_read_b128 a[160:163], v224 offset:18592\n\t"
"ds_read_b128 v[24:27], v230 offset:20480\n\t"
"v_mfma_f32_16x16x32_bf16 v[180:183], a[116:119], a[164:167], v[180:183]\n\t"
"ds_write_addtid_b32 v208 offset:1024\n\t"
"v_cvt_pk_bf16_f32 v200, v200, v204\n\t"
"v_mfma_f32_16x16x32_bf16 v[164:167], a[104:107], a[148:151], v[164:167]\n\t"
"ds_read_b128 v[12:15], v229 offset:33792\n\t"
"v_cvt_pk_bf16_f32 v195, v195, v199\n\t"
"v_mfma_f32_16x16x32_bf16 v[164:167], a[108:111], a[164:167], v[164:167]\n\t"
"buffer_load_dwordx4 a[44:47], v252, s[84:87], s54 offen offset:1024 sc1\n\t"
"v_mfma_f32_16x16x32_bf16 v[148:151], a[96:99], a[148:151], v[148:151]\n\t"
"s_nop 0\n\t"
"ds_write_addtid_b32 v194 offset:4128\n\t"
"ds_write_addtid_b32 v211 offset:13472\n\t"
"v_mfma_f32_16x16x32_bf16 v[148:151], a[100:103], a[164:167], v[148:151]\n\t"
"ds_read_b128 a[164:167], v224 offset:18848\n\t"
"v_cvt_pk_bf16_f32 v216, v216, v220\n\t"
"v_mfma_f32_16x16x32_bf16 v[152:155], a[96:99], a[152:155], v[152:155]\n\t"
"ds_write_addtid_b32 v200 offset:2048\n\t"
"v_cvt_pk_bf16_f32 v217, v217, v221\n\t"
"v_mfma_f32_16x16x32_bf16 v[152:155], a[100:103], a[168:171], v[152:155]\n\t"
"ds_write_addtid_b32 v217 offset:11392\n\t"
"buffer_load_dwordx4 a[36:39], v235, s[84:87], s54 offen offset:3072 sc1\n\t"
"v_mfma_f32_16x16x32_bf16 v[168:171], a[104:107], a[152:155], v[168:171]\n\t"
"ds_read_b128 a[148:151], v224 offset:16800\n\t"
"v_cvt_pk_bf16_f32 v219, v219, v223\n\t"
"v_mfma_f32_16x16x32_bf16 v[168:171], a[108:111], a[168:171], v[168:171]\n\t"
"ds_write_addtid_b32 v216 offset:3072\n\t"
"v_cvt_pk_bf16_f32 v203, v203, v207\n\t"
"v_mfma_f32_16x16x32_bf16 v[184:187], a[112:115], a[152:155], v[184:187]\n\t"
"ds_write_addtid_b32 v203 offset:14496\n\t"
"v_cvt_pk_bf16_f32 v218, v218, v222\n\t"
"v_mfma_f32_16x16x32_bf16 v[184:187], a[116:119], a[168:171], v[184:187]\n\t"
"s_waitcnt lgkmcnt(9)\n\t"
"ds_read_b128 v[28:31], v230 offset:33792\n\t"
"buffer_store_dwordx4 v[24:27], v233, s[16:19], s24 offen nt\n\t"
"v_mfma_f32_16x16x32_bf16 v[188:191], a[112:115], a[156:159], v[188:191]\n\t"
"ds_write_addtid_b32 v219 offset:15520\n\t"
"v_cvt_pk_bf16_f32 v202, v202, v206\n\t"
"v_mfma_f32_16x16x32_bf16 v[188:191], a[116:119], a[172:175], v[188:191]\n\t"
"ds_read_b128 v[44:47], v231 offset:33792\n\t"
"v_cvt_pk_bf16_f32 v210, v210, v214\n\t"
"v_mfma_f32_16x16x32_bf16 v[172:175], a[104:107], a[156:159], v[172:175]\n\t"
"ds_write_addtid_b32 v210 offset:5152\n\t"
"v_cvt_pk_bf16_f32 v193, v193, v197\n\t"
"v_mfma_f32_16x16x32_bf16 v[172:175], a[108:111], a[172:175], v[172:175]\n\t"
"buffer_load_dwordx4 a[32:35], v235, s[84:87], s54 offen offset:2048 sc1\n\t"
"v_mfma_f32_16x16x32_bf16 v[156:159], a[96:99], a[156:159], v[156:159]\n\t"
"s_nop 0\n\t"
"ds_write_addtid_b32 v218 offset:7200\n\t"
"ds_read_b128 v[40:43], v231 offset:20480\n\t"
"v_mfma_f32_16x16x32_bf16 v[156:159], a[100:103], a[172:175], v[156:159]\n\t"
"s_waitcnt vmcnt(17) lgkmcnt(9)\n\t"
"buffer_load_dwordx4 a[24:27], v235, s[84:87], s54 offen offset:0 sc1\n\t"
"v_mfma_f32_16x16x32_bf16 v[96:99], a[120:123], a[144:147], v[96:99]\n\t"
"s_nop 0\n\t"
"ds_write_addtid_b32 v193 offset:8320\n\t"
"ds_write_addtid_b32 v195 offset:12448\n\t"
"v_mfma_f32_16x16x32_bf16 v[96:99], a[124:127], a[160:163], v[96:99]\n\t"
"ds_write_addtid_b32 v202 offset:6176\n\t"
"v_cvt_pk_bf16_f32 v209, v209, v213\n\t"
"v_mfma_f32_16x16x32_bf16 v[112:115], a[128:131], a[144:147], v[112:115]\n\t"
"s_nop 0\n\t"
"ds_read_b128 a[172:175], v224 offset:19360\n\t"
"ds_write_addtid_b32 v209 offset:9344\n\t"
"v_mfma_f32_16x16x32_bf16 v[112:115], a[132:135], a[160:163], v[112:115]\n\t"
"buffer_load_dwordx4 a[28:31], v235, s[84:87], s54 offen offset:1024 sc1\n\t"
"v_mfma_f32_16x16x32_bf16 v[128:131], a[136:139], a[144:147], v[128:131]\n\t"
"s_nop 0\n\t"
"ds_read_b128 a[156:159], v224 offset:17312\n\t"
"ds_read_b64_tr_b16 a[198:199], v249 offset:1664\n\t"
"v_mfma_f32_16x16x32_bf16 v[128:131], a[140:143], a[160:163], v[128:131]\n\t"
"s_waitcnt lgkmcnt(10)\n\t"
"ds_read_b128 a[152:155], v224 offset:17056\n\t"
"ds_read_b128 a[168:171], v224 offset:19104\n\t"
"v_mfma_f32_16x16x32_bf16 v[132:135], a[136:139], a[148:151], v[132:135]\n\t"
"buffer_load_dwordx4 a[64:67], v252, s[84:87], 0 offen offset:0 sc1\n\t"
"v_mfma_f32_16x16x32_bf16 v[132:135], a[140:143], a[164:167], v[132:135]\n\t"
"s_nop 0\n\t"
"ds_read_b64_tr_b16 a[200:201], v250 offset:1152\n\t"
"ds_read_b64_tr_b16 a[202:203], v250 offset:1664\n\t"
"v_mfma_f32_16x16x32_bf16 v[116:119], a[128:131], a[148:151], v[116:119]\n\t"
"s_nop 0\n\t"
"ds_read_b64_tr_b16 a[204:205], v251 offset:1152\n\t"
"ds_read_b64_tr_b16 a[206:207], v251 offset:1664\n\t"
"v_mfma_f32_16x16x32_bf16 v[116:119], a[132:135], a[164:167], v[116:119]\n\t"
"s_waitcnt lgkmcnt(13)\n\t"
"buffer_store_dwordx4 v[40:43], v234, s[16:19], s24 offen nt\n\t"
"v_mfma_f32_16x16x32_bf16 v[100:103], a[120:123], a[148:151], v[100:103]\n\t"
"v_mfma_f32_16x16x32_bf16 v[100:103], a[124:127], a[164:167], v[100:103]\n\t"
"buffer_load_dwordx4 a[68:71], v252, s[84:87], 0 offen offset:1024 sc1\n\t"
"s_waitcnt vmcnt(17) lgkmcnt(10)\n\t"
"s_waitcnt lgkmcnt(4)\n\t"
"s_barrier\n\t"
"v_mfma_f32_16x16x32_bf16 v[104:107], a[120:123], a[152:155], v[104:107]\n\t"
"ds_read_b128 a[144:147], v224 offset:0\n\t"
"v_mfma_f32_16x16x32_bf16 v[104:107], a[124:127], a[168:171], v[104:107]\n\t"
"ds_read_b128 a[148:151], v224 offset:256\n\t"
"v_add_u32 v248, 122368, v227\n\t"
"v_mfma_f32_16x16x32_bf16 v[120:123], a[128:131], a[152:155], v[120:123]\n\t"
"ds_read_b128 a[160:163], v224 offset:2048\n\t"
"v_mfma_f32_16x16x32_bf16 v[120:123], a[132:135], a[168:171], v[120:123]\n\t"
"ds_read_b128 a[164:167], v224 offset:2304\n\t"
"v_mfma_f32_16x16x32_bf16 v[136:139], a[136:139], a[152:155], v[136:139]\n\t"
"v_mfma_f32_16x16x32_bf16 v[136:139], a[140:143], a[168:171], v[136:139]\n\t"
"ds_read_b128 a[168:171], v224 offset:2560\n\t"
"v_xor_b32 v249, 32, v248\n\t"
"v_mfma_f32_16x16x32_bf16 v[140:143], a[136:139], a[156:159], v[140:143]\n\t"
"v_mfma_f32_16x16x32_bf16 v[140:143], a[140:143], a[172:175], v[140:143]\n\t"
"buffer_load_dwordx4 a[60:63], v235, s[84:87], 0 offen offset:3072 sc1\n\t"
"v_mfma_f32_16x16x32_bf16 v[124:127], a[128:131], a[156:159], v[124:127]\n\t"
"ds_read_b128 a[152:155], v224 offset:512\n\t"
"v_mfma_f32_16x16x32_bf16 v[124:127], a[132:135], a[172:175], v[124:127]\n\t"
"v_xor_b32 v250, 64, v248\n\t"
"v_xor_b32 v251, 96, v248\n\t"
"v_mfma_f32_16x16x32_bf16 v[108:111], a[120:123], a[156:159], v[108:111]\n\t"
"ds_read_b128 a[156:159], v224 offset:768\n\t"
"v_mfma_f32_16x16x32_bf16 v[108:111], a[124:127], a[172:175], v[108:111]\n\t"
"buffer_load_dwordx4 a[56:59], v235, s[84:87], 0 offen offset:2048 sc1\n\t"
"v_mfma_f32_16x16x32_bf16 v[192:195], a[224:227], a[176:179], 0\n\t"
"s_nop 0\n\t"
"ds_read_b64_tr_b16 a[176:177], v248 offset:20736\n\t"
"ds_read_b64_tr_b16 a[178:179], v248 offset:21248\n\t"
"v_mfma_f32_16x16x32_bf16 v[196:199], a[224:227], a[180:183], 0\n\t"
"s_nop 0\n\t"
"ds_read_b64_tr_b16 a[180:181], v249 offset:20736\n\t"
"ds_read_b64_tr_b16 a[182:183], v249 offset:21248\n\t"
"v_mfma_f32_16x16x32_bf16 v[200:203], a[224:227], a[184:187], 0\n\t"
"s_nop 0\n\t"
"ds_read_b64_tr_b16 a[184:185], v250 offset:20736\n\t"
"ds_read_b64_tr_b16 a[186:187], v250 offset:21248\n\t"
"v_mfma_f32_16x16x32_bf16 v[204:207], a[224:227], a[188:191], 0\n\t"
"buffer_load_dwordx4 a[48:51], v235, s[84:87], 0 offen offset:0 sc1\n\t"
"buffer_load_dwordx4 a[52:55], v235, s[84:87], 0 offen offset:1024 sc1\n\t"
"v_mfma_f32_16x16x32_bf16 v[208:211], a[224:227], a[192:195], 0\n\t"
"s_nop 0\n\t"
"ds_read_b128 a[172:175], v224 offset:2816\n\t"
"ds_read_b64_tr_b16 a[188:189], v251 offset:20736\n\t"
"v_mfma_f32_16x16x32_bf16 v[212:215], a[224:227], a[196:199], 0\n\t"
"s_waitcnt lgkmcnt(10)\n\t"
"ds_read_b64_tr_b16 a[190:191], v251 offset:21248\n\t"
"ds_read_b64_tr_b16 a[192:193], v248 offset:0\n\t"
"v_mfma_f32_16x16x32_bf16 v[216:219], a[224:227], a[200:203], 0\n\t"
".p2align 3\n\t"
"buffer_store_dwordx4 v[12:15], v232, s[16:19], s27 offen nt\n\t"
"v_pk_add_f32 v[96:97], v[96:97], v[144:145]\n\t"
"v_pk_add_f32 v[98:99], v[98:99], v[146:147]\n\t"
"v_pk_fma_f32 v[144:145], v[144:145], s[38:39], v[96:97]\n\t"
"v_pk_fma_f32 v[146:147], v[146:147], s[38:39], v[98:99]\n\t"
"v_pk_add_f32 v[112:113], v[112:113], v[160:161]\n\t"
"v_pk_add_f32 v[114:115], v[114:115], v[162:163]\n\t"
"v_pk_fma_f32 v[160:161], v[160:161], s[38:39], v[112:113]\n\t"
"v_pk_fma_f32 v[162:163], v[162:163], s[38:39], v[114:115]\n\t"
"v_pk_add_f32 v[128:129], v[128:129], v[176:177]\n\t"
"v_pk_add_f32 v[130:131], v[130:131], v[178:179]\n\t"
"v_pk_fma_f32 v[176:177], v[176:177], s[38:39], v[128:129]\n\t"
"v_pk_fma_f32 v[178:179], v[178:179], s[38:39], v[130:131]\n\t"
"v_pk_add_f32 v[132:133], v[132:133], v[180:181]\n\t"
"v_pk_add_f32 v[134:135], v[134:135], v[182:183]\n\t"
"v_pk_fma_f32 v[180:181], v[180:181], s[38:39], v[132:133]\n\t"
"v_pk_fma_f32 v[182:183], v[182:183], s[38:39], v[134:135]\n\t"
"v_pk_add_f32 v[116:117], v[116:117], v[164:165]\n\t"
"v_pk_add_f32 v[118:119], v[118:119], v[166:167]\n\t"
"v_pk_fma_f32 v[164:165], v[164:165], s[38:39], v[116:117]\n\t"
"v_pk_fma_f32 v[166:167], v[166:167], s[38:39], v[118:119]\n\t"
"v_pk_add_f32 v[100:101], v[100:101], v[148:149]\n\t"
"v_pk_add_f32 v[102:103], v[102:103], v[150:151]\n\t"
"v_pk_fma_f32 v[148:149], v[148:149], s[38:39], v[100:101]\n\t"
"v_pk_fma_f32 v[150:151], v[150:151], s[38:39], v[102:103]\n\t"
"v_pk_add_f32 v[104:105], v[104:105], v[152:153]\n\t"
"v_pk_add_f32 v[106:107], v[106:107], v[154:155]\n\t"
"v_pk_fma_f32 v[152:153], v[152:153], s[38:39], v[104:105]\n\t"
"v_pk_fma_f32 v[154:155], v[154:155], s[38:39], v[106:107]\n\t"
"v_pk_add_f32 v[120:121], v[120:121], v[168:169]\n\t"
"v_pk_add_f32 v[122:123], v[122:123], v[170:171]\n\t"
"v_pk_fma_f32 v[168:169], v[168:169], s[38:39], v[120:121]\n\t"
"v_pk_fma_f32 v[170:171], v[170:171], s[38:39], v[122:123]\n\t"
"v_pk_add_f32 v[136:137], v[136:137], v[184:185]\n\t"
"v_pk_add_f32 v[138:139], v[138:139], v[186:187]\n\t"
"v_pk_fma_f32 v[184:185], v[184:185], s[38:39], v[136:137]\n\t"
"v_pk_fma_f32 v[186:187], v[186:187], s[38:39], v[138:139]\n\t"
"v_pk_add_f32 v[140:141], v[140:141], v[188:189]\n\t"
"v_pk_add_f32 v[142:143], v[142:143], v[190:191]\n\t"
"v_pk_fma_f32 v[188:189], v[188:189], s[38:39], v[140:141]\n\t"
"v_pk_fma_f32 v[190:191], v[190:191], s[38:39], v[142:143]\n\t"
"v_pk_add_f32 v[124:125], v[124:125], v[172:173]\n\t"
"v_pk_add_f32 v[126:127], v[126:127], v[174:175]\n\t"
"v_pk_fma_f32 v[172:173], v[172:173], s[38:39], v[124:125]\n\t"
"v_pk_fma_f32 v[174:175], v[174:175], s[38:39], v[126:127]\n\t"
"v_pk_add_f32 v[108:109], v[108:109], v[156:157]\n\t"
"v_pk_add_f32 v[110:111], v[110:111], v[158:159]\n\t"
"v_pk_fma_f32 v[156:157], v[156:157], s[38:39], v[108:109]\n\t"
"v_pk_fma_f32 v[158:159], v[158:159], s[38:39], v[110:111]\n\t"
"v_cvt_pk_bf16_f32 v100, v100, v101\n\t"
"v_mfma_f32_16x16x32_bf16 v[220:223], a[224:227], a[204:207], 0\n\t"
"s_waitcnt vmcnt(15) lgkmcnt(11)\n\t"
"ds_read_b64_tr_b16 a[194:195], v248 offset:512\n\t"
"v_cvt_pk_bf16_f32 v96, v96, v97\n\t"
"v_cvt_pk_bf16_f32 v97, v98, v99\n\t"
"v_mfma_f32_16x16x32_bf16 v[48:51], a[0:3], a[144:147], v[48:51]\n\t"
"v_cvt_pk_bf16_f32 v101, v102, v103\n\t"
"v_cvt_pk_bf16_f32 v112, v112, v113\n\t"
"v_mfma_f32_16x16x32_bf16 v[48:51], a[4:7], a[160:163], v[48:51]\n\t"
"ds_read_b64_tr_b16 a[196:197], v249 offset:0\n\t"
"buffer_load_dwordx4 a[88:91], v252, s[84:87], s51 offen offset:0 sc1\n\t"
"buffer_load_dwordx4 a[92:95], v252, s[84:87], s51 offen offset:1024 sc1\n\t"
"buffer_load_dwordx4 a[84:87], v235, s[84:87], s51 offen offset:3072 sc1\n\t"
"buffer_load_dwordx4 a[80:83], v235, s[84:87], s51 offen offset:2048 sc1\n\t"
"ds_write2st64_b64 v228, v[96:97], v[100:101] offset0:40 offset1:53\n\t"
"v_cvt_pk_bf16_f32 v132, v132, v133\n\t"
"v_mfma_f32_16x16x32_bf16 v[64:67], a[8:11], a[144:147], v[64:67]\n\t"
"v_cvt_pk_bf16_f32 v113, v114, v115\n\t"
"v_cvt_pk_bf16_f32 v116, v116, v117\n\t"
"v_mfma_f32_16x16x32_bf16 v[64:67], a[12:15], a[160:163], v[64:67]\n\t"
"v_cvt_pk_bf16_f32 v128, v128, v129\n\t"
"v_cvt_pk_bf16_f32 v124, v124, v125\n\t"
"v_mfma_f32_16x16x32_bf16 v[80:83], a[16:19], a[144:147], v[80:83]\n\t"
"v_cvt_pk_bf16_f32 v210, v210, v214\n\t"
"v_cvt_pk_bf16_f32 v117, v118, v119\n\t"
"v_mfma_f32_16x16x32_bf16 v[80:83], a[20:23], a[160:163], v[80:83]\n\t"
"s_nop 0\n\t"
"ds_read_b128 a[144:147], v224 offset:1024\n\t"
"ds_read_b128 a[160:163], v224 offset:3072\n\t"
"ds_write_addtid_b32 v210 offset:21696\n\t"
"buffer_load_dwordx4 a[72:75], v235, s[84:87], s51 offen offset:0 sc1\n\t"
"ds_write2st64_b64 v246, v[112:113], v[116:117] offset0:40 offset1:53\n\t"
"v_cvt_pk_bf16_f32 v129, v130, v131\n\t"
"v_mfma_f32_16x16x32_bf16 v[84:87], a[16:19], a[148:151], v[84:87]\n\t"
"v_cvt_pk_bf16_f32 v133, v134, v135\n\t"
"v_cvt_pk_bf16_f32 v120, v120, v121\n\t"
"v_mfma_f32_16x16x32_bf16 v[84:87], a[20:23], a[164:167], v[84:87]\n\t"
"v_cvt_pk_bf16_f32 v200, v200, v204\n\t"
"v_cvt_pk_bf16_f32 v211, v211, v215\n\t"
"v_cvt_pk_bf16_f32 v216, v216, v220\n\t"
"v_mfma_f32_16x16x32_bf16 v[68:71], a[8:11], a[148:151], v[68:71]\n\t"
"v_cvt_pk_bf16_f32 v193, v193, v197\n\t"
"v_cvt_pk_bf16_f32 v140, v140, v141\n\t"
"v_mfma_f32_16x16x32_bf16 v[68:71], a[12:15], a[164:167], v[68:71]\n\t"
"ds_write_addtid_b32 v216 offset:19616\n\t"
"buffer_load_dwordx4 a[76:79], v235, s[84:87], s51 offen offset:1024 sc1\n\t"
"ds_write2st64_b64 v247, v[128:129], v[132:133] offset0:40 offset1:53\n\t"
"v_cvt_pk_bf16_f32 v209, v209, v213\n\t"
"v_cvt_pk_bf16_f32 v136, v136, v137\n\t"
"v_mfma_f32_16x16x32_bf16 v[52:55], a[0:3], a[148:151], v[52:55]\n\t"
"v_cvt_pk_bf16_f32 v104, v104, v105\n\t"
"v_cvt_pk_bf16_f32 v108, v108, v109\n\t"
"v_mfma_f32_16x16x32_bf16 v[52:55], a[4:7], a[164:167], v[52:55]\n\t"
"v_cvt_pk_bf16_f32 v192, v192, v196\n\t"
"v_cvt_pk_bf16_f32 v195, v195, v199\n\t"
"v_cvt_pk_bf16_f32 v105, v106, v107\n\t"
"v_cvt_pk_bf16_f32 v109, v110, v111\n\t"
"v_mfma_f32_16x16x32_bf16 v[56:59], a[0:3], a[152:155], v[56:59]\n\t"
"v_cvt_pk_bf16_f32 v219, v219, v223\n\t"
"v_cvt_pk_bf16_f32 v121, v122, v123\n\t"
"v_mfma_f32_16x16x32_bf16 v[56:59], a[4:7], a[168:171], v[56:59]\n\t"
"s_nop 0\n\t"
".p2align 3\n\t"
"ds_read_b128 a[148:151], v224 offset:1280\n\t"
"ds_read_b128 a[164:167], v224 offset:3328\n\t"
"ds_write_addtid_b32 v192 offset:16544\n\t"
"ds_write_addtid_b32 v193 offset:24864\n\t"
"ds_write_addtid_b32 v200 offset:18592\n\t"
"ds_write_addtid_b32 v209 offset:25888\n\t"
"ds_write_addtid_b32 v219 offset:32064\n\t"
"ds_write2st64_b64 v228, v[104:105], v[108:109] offset0:66 offset1:79\n\t"
"v_mfma_f32_16x16x32_bf16 v[72:75], a[8:11], a[152:155], v[72:75]\n\t"
"v_cvt_pk_bf16_f32 v125, v126, v127\n\t"
"v_cvt_pk_bf16_f32 v141, v142, v143\n\t"
"v_mfma_f32_16x16x32_bf16 v[72:75], a[12:15], a[168:171], v[72:75]\n\t"
"buffer_store_dwordx4 v[28:31], v233, s[16:19], s27 offen nt\n\t"
"v_mfma_f32_16x16x32_bf16 v[88:91], a[16:19], a[152:155], v[88:91]\n\t"
"v_cvt_pk_bf16_f32 v202, v202, v206\n\t"
"v_cvt_pk_bf16_f32 v137, v138, v139\n\t"
"v_mfma_f32_16x16x32_bf16 v[88:91], a[20:23], a[168:171], v[88:91]\n\t"
"s_waitcnt lgkmcnt(10)\n\t"
"ds_write_addtid_b32 v211 offset:30016\n\t"
"ds_write2st64_b64 v246, v[120:121], v[124:125] offset0:66 offset1:79\n\t"
"v_mfma_f32_16x16x32_bf16 v[92:95], a[16:19], a[156:159], v[92:95]\n\t"
"v_cvt_pk_bf16_f32 v194, v194, v198\n\t"
"v_cvt_pk_bf16_f32 v201, v201, v205\n\t"
"v_mfma_f32_16x16x32_bf16 v[92:95], a[20:23], a[172:175], v[92:95]\n\t"
"buffer_load_dwordx4 a[112:115], v252, s[84:87], s81 offen offset:0 sc1\n\t"
"v_mfma_f32_16x16x32_bf16 v[76:79], a[8:11], a[156:159], v[76:79]\n\t"
"v_cvt_pk_bf16_f32 v217, v217, v221\n\t"
"v_cvt_pk_bf16_f32 v218, v218, v222\n\t"
"v_mfma_f32_16x16x32_bf16 v[76:79], a[12:15], a[172:175], v[76:79]\n\t"
"ds_write_addtid_b32 v202 offset:22720\n\t"
"ds_write2st64_b64 v247, v[136:137], v[140:141] offset0:66 offset1:79\n\t"
"v_mfma_f32_16x16x32_bf16 v[60:63], a[0:3], a[156:159], v[60:63]\n\t"
"buffer_load_dwordx4 a[116:119], v252, s[84:87], s81 offen offset:1024 sc1\n\t"
"v_mfma_f32_16x16x32_bf16 v[60:63], a[4:7], a[172:175], v[60:63]\n\t"
"s_waitcnt vmcnt(17) lgkmcnt(4)\n\t"
"v_cvt_pk_bf16_f32 v203, v203, v207\n\t"
"v_cvt_pk_bf16_f32 v208, v208, v212\n\t"
"v_mfma_f32_16x16x32_bf16 v[48:51], a[24:27], a[144:147], v[48:51]\n\t"
"buffer_load_dwordx4 a[108:111], v235, s[84:87], s81 offen offset:3072 sc1\n\t"
"v_mfma_f32_16x16x32_bf16 v[48:51], a[28:31], a[160:163], v[48:51]\n\t"
"s_nop 0\n\t"
"ds_read_b128 a[156:159], v224 offset:1792\n\t"
"ds_write_addtid_b32 v194 offset:20672\n\t"
"ds_write_addtid_b32 v201 offset:26912\n\t"
"ds_write_addtid_b32 v203 offset:31040\n\t"
"ds_write_addtid_b32 v217 offset:27936\n\t"
"ds_write_addtid_b32 v218 offset:23744\n\t"
"v_mfma_f32_16x16x32_bf16 v[64:67], a[32:35], a[144:147], v[64:67]\n\t"
"s_nop 0\n\t"
"ds_write_addtid_b32 v195 offset:28992\n\t"
"ds_write_addtid_b32 v208 offset:17568\n\t"
"v_mfma_f32_16x16x32_bf16 v[64:67], a[36:39], a[160:163], v[64:67]\n\t"
"buffer_load_dwordx4 a[104:107], v235, s[84:87], s81 offen offset:2048 sc1\n\t"
"v_mfma_f32_16x16x32_bf16 v[80:83], a[40:43], a[144:147], v[80:83]\n\t"
"s_nop 0\n\t"
"ds_read_b128 a[152:155], v224 offset:1536\n\t"
"ds_read_b128 a[168:171], v224 offset:3584\n\t"
"v_mfma_f32_16x16x32_bf16 v[80:83], a[44:47], a[160:163], v[80:83]\n\t"
"s_nop 0\n\t"
"ds_read_b128 a[172:175], v224 offset:3840\n\t"
"ds_read_b64_tr_b16 a[198:199], v249 offset:512\n\t"
"v_mfma_f32_16x16x32_bf16 v[84:87], a[40:43], a[148:151], v[84:87]\n\t"
"buffer_load_dwordx4 a[96:99], v235, s[84:87], s81 offen offset:0 sc1\n\t"
"v_mfma_f32_16x16x32_bf16 v[84:87], a[44:47], a[164:167], v[84:87]\n\t"
"s_nop 0\n\t"
"ds_read_b64_tr_b16 a[200:201], v250 offset:0\n\t"
"ds_read_b64_tr_b16 a[202:203], v250 offset:512\n\t"
"v_mfma_f32_16x16x32_bf16 v[68:71], a[32:35], a[148:151], v[68:71]\n\t"
"s_nop 0\n\t"
"ds_read_b64_tr_b16 a[204:205], v251 offset:0\n\t"
"ds_read_b64_tr_b16 a[206:207], v251 offset:512\n\t"
"v_mfma_f32_16x16x32_bf16 v[68:71], a[36:39], a[164:167], v[68:71]\n\t"
"buffer_store_dwordx4 v[44:47], v234, s[16:19], s27 offen nt\n\t"
"v_mfma_f32_16x16x32_bf16 v[52:55], a[24:27], a[148:151], v[52:55]\n\t"
"s_waitcnt lgkmcnt(5)\n\t"
"v_mfma_f32_16x16x32_bf16 v[52:55], a[28:31], a[164:167], v[52:55]\n\t"
"buffer_load_dwordx4 a[100:103], v235, s[84:87], s81 offen offset:1024 sc1\n\t"
"s_barrier\n\t"
"v_mfma_f32_16x16x32_bf16 v[56:59], a[24:27], a[152:155], v[56:59]\n\t"
"ds_read_b128 a[144:147], v224 offset:16544\n\t"
"v_add_u32 v248, 54016, v227\n\t"
"v_mfma_f32_16x16x32_bf16 v[56:59], a[28:31], a[168:171], v[56:59]\n\t"
"buffer_load_dwordx4 a[136:139], v252, s[84:87], s66 offen offset:0 sc1\n\t"
"v_mfma_f32_16x16x32_bf16 v[72:75], a[32:35], a[152:155], v[72:75]\n\t"
"s_nop 0\n\t"
"ds_read_b128 a[160:163], v224 offset:18592\n\t"
"ds_read_b128 a[148:151], v224 offset:16800\n\t"
"v_mfma_f32_16x16x32_bf16 v[72:75], a[36:39], a[168:171], v[72:75]\n\t"
"ds_read_b128 a[164:167], v224 offset:18848\n\t"
"v_xor_b32 v249, 32, v248\n\t"
"v_mfma_f32_16x16x32_bf16 v[88:91], a[40:43], a[152:155], v[88:91]\n\t"
"ds_read_b128 v[108:111], v229 offset:33792\n\t"
"v_xor_b32 v250, 64, v248\n\t"
"v_mfma_f32_16x16x32_bf16 v[88:91], a[44:47], a[168:171], v[88:91]\n\t"
"buffer_load_dwordx4 a[140:143], v252, s[84:87], s66 offen offset:1024 sc1\n\t"
"v_mfma_f32_16x16x32_bf16 v[92:95], a[40:43], a[156:159], v[92:95]\n\t"
"ds_read_b128 a[236:239], v231 offset:33792\n\t"
"v_xor_b32 v251, 96, v248\n\t"
"v_mfma_f32_16x16x32_bf16 v[92:95], a[44:47], a[172:175], v[92:95]\n\t"
"ds_read_b128 v[104:107], v229 offset:20480\n\t"
"v_mfma_f32_16x16x32_bf16 v[76:79], a[32:35], a[156:159], v[76:79]\n\t"
"s_nop 0\n\t"
"ds_read_b128 v[120:123], v230 offset:20480\n\t"
"ds_read_b128 a[232:235], v230 offset:33792\n\t"
"v_mfma_f32_16x16x32_bf16 v[76:79], a[36:39], a[172:175], v[76:79]\n\t"
"buffer_load_dwordx4 a[132:135], v235, s[84:87], s66 offen offset:3072 sc1\n\t"
"v_mfma_f32_16x16x32_bf16 v[60:63], a[24:27], a[156:159], v[60:63]\n\t"
"s_nop 0\n\t"
"ds_read_b128 a[168:171], v224 offset:19104\n\t"
"ds_read_b128 a[156:159], v224 offset:17312\n\t"
"v_mfma_f32_16x16x32_bf16 v[60:63], a[28:31], a[172:175], v[60:63]\n\t"
"s_nop 0\n\t"
"ds_read_b128 a[172:175], v224 offset:19360\n\t"
"ds_read_b128 v[136:139], v231 offset:20480\n\t"
"v_mfma_f32_16x16x32_bf16 v[192:195], a[216:219], a[176:179], 0\n\t"
"buffer_load_dwordx4 a[128:131], v235, s[84:87], s66 offen offset:2048 sc1\n\t"
"v_mfma_f32_16x16x32_bf16 v[196:199], a[216:219], a[180:183], 0\n\t"
"s_nop 0\n\t"
"ds_read_b64_tr_b16 a[176:177], v248 offset:21888\n\t"
"ds_read_b64_tr_b16 a[178:179], v248 offset:22400\n\t"
"v_mfma_f32_16x16x32_bf16 v[200:203], a[216:219], a[184:187], 0\n\t"
"s_nop 0\n\t"
"ds_read_b64_tr_b16 a[180:181], v249 offset:21888\n\t"
"ds_read_b64_tr_b16 a[182:183], v249 offset:22400\n\t"
"v_mfma_f32_16x16x32_bf16 v[204:207], a[216:219], a[188:191], 0\n\t"
"s_waitcnt lgkmcnt(10)\n\t"
"buffer_store_dwordx4 v[104:107], v232, s[16:19], s20 offen nt\n\t"
"v_mfma_f32_16x16x32_bf16 v[208:211], a[216:219], a[192:195], 0\n\t"
"s_nop 0\n\t"
"ds_read_b128 a[152:155], v224 offset:17056\n\t"
"ds_read_b64_tr_b16 a[184:185], v250 offset:21888\n\t"
"v_mfma_f32_16x16x32_bf16 v[212:215], a[216:219], a[196:199], 0\n\t"
"s_waitcnt lgkmcnt(6)\n\t"
"s_barrier\n\t"
"v_mfma_f32_16x16x32_bf16 v[216:219], a[216:219], a[200:203], 0\n\t"
"buffer_load_dwordx4 a[120:123], v235, s[84:87], s66 offen offset:0 sc1\n\t"
"v_mfma_f32_16x16x32_bf16 v[220:223], a[216:219], a[204:207], 0\n\t"
"s_waitcnt vmcnt(21)\n\t"
"ds_read_b64_tr_b16 a[186:187], v250 offset:22400\n\t"
"v_cvt_pk_bf16_f32 v193, v193, v197\n\t"
"v_mfma_f32_16x16x32_bf16 v[48:51], a[48:51], a[144:147], v[48:51]\n\t"
"ds_read_b64_tr_b16 a[188:189], v251 offset:21888\n\t"
"v_cvt_pk_bf16_f32 v195, v195, v199\n\t"
"v_mfma_f32_16x16x32_bf16 v[48:51], a[52:55], a[160:163], v[48:51]\n\t"
"ds_read_b64_tr_b16 a[190:191], v251 offset:22400\n\t"
"v_cvt_pk_bf16_f32 v203, v203, v207\n\t"
"v_mfma_f32_16x16x32_bf16 v[64:67], a[56:59], a[144:147], v[64:67]\n\t"
"ds_write_addtid_b32 v195 offset:12448\n\t"
"v_cvt_pk_bf16_f32 v202, v202, v206\n\t"
"v_mfma_f32_16x16x32_bf16 v[64:67], a[60:63], a[160:163], v[64:67]\n\t"
"buffer_store_dwordx4 v[120:123], v233, s[16:19], s20 offen nt\n\t"
"v_mfma_f32_16x16x32_bf16 v[80:83], a[64:67], a[144:147], v[80:83]\n\t"
"ds_write_addtid_b32 v203 offset:14496\n\t"
"v_cvt_pk_bf16_f32 v200, v200, v204\n\t"
"v_mfma_f32_16x16x32_bf16 v[80:83], a[68:71], a[160:163], v[80:83]\n\t"
"ds_write_addtid_b32 v193 offset:8320\n\t"
"v_cvt_pk_bf16_f32 v210, v210, v214\n\t"
"v_mfma_f32_16x16x32_bf16 v[84:87], a[64:67], a[148:151], v[84:87]\n\t"
"ds_read_b128 a[144:147], v224 offset:17568\n\t"
"v_cvt_pk_bf16_f32 v194, v194, v198\n\t"
"v_mfma_f32_16x16x32_bf16 v[84:87], a[68:71], a[164:167], v[84:87]\n\t"
"buffer_load_dwordx4 a[124:127], v235, s[84:87], s66 offen offset:1024 sc1\n\t"
"v_mfma_f32_16x16x32_bf16 v[68:71], a[56:59], a[148:151], v[68:71]\n\t"
"ds_read_b128 a[160:163], v224 offset:19616\n\t"
"v_cvt_pk_bf16_f32 v218, v218, v222\n\t"
"v_mfma_f32_16x16x32_bf16 v[68:71], a[60:63], a[164:167], v[68:71]\n\t"
"ds_write_addtid_b32 v218 offset:7200\n\t"
"v_cvt_pk_bf16_f32 v209, v209, v213\n\t"
"v_mfma_f32_16x16x32_bf16 v[52:55], a[48:51], a[148:151], v[52:55]\n\t"
"ds_read_b128 a[148:151], v224 offset:17824\n\t"
"v_cvt_pk_bf16_f32 v211, v211, v215\n\t"
"v_mfma_f32_16x16x32_bf16 v[52:55], a[52:55], a[164:167], v[52:55]\n\t"
"s_waitcnt lgkmcnt(7)\n\t"
"ds_read_b128 a[164:167], v224 offset:19872\n\t"
"ds_read_b64_tr_b16 a[192:193], v248 offset:1152\n\t"
"v_mfma_f32_16x16x32_bf16 v[56:59], a[48:51], a[152:155], v[56:59]\n\t"
"s_nop 0\n\t"
"ds_write_addtid_b32 v202 offset:6176\n\t"
"ds_read_b64_tr_b16 a[194:195], v248 offset:1664\n\t"
"v_mfma_f32_16x16x32_bf16 v[56:59], a[52:55], a[168:171], v[56:59]\n\t"
"buffer_store_dwordx4 v[136:139], v234, s[16:19], s20 offen nt\n\t"
"v_mfma_f32_16x16x32_bf16 v[72:75], a[56:59], a[152:155], v[72:75]\n\t"
"ds_write_addtid_b32 v194 offset:4128\n\t"
"v_cvt_pk_bf16_f32 v217, v217, v221\n\t"
"v_mfma_f32_16x16x32_bf16 v[72:75], a[60:63], a[168:171], v[72:75]\n\t"
"ds_write_addtid_b32 v217 offset:11392\n\t"
"v_cvt_pk_bf16_f32 v208, v208, v212\n\t"
"v_mfma_f32_16x16x32_bf16 v[88:91], a[64:67], a[152:155], v[88:91]\n\t"
"s_nop 0\n\t"
"ds_write_addtid_b32 v209 offset:9344\n\t"
"ds_write_addtid_b32 v210 offset:5152\n\t"
"s_add_u32 m0, s26, 103936\n\t"
"v_mfma_f32_16x16x32_bf16 v[88:91], a[68:71], a[168:171], v[88:91]\n\t"
"buffer_load_dwordx4 v236, s[12:15], s22 offen offset:0 nt lds\n\t"
"v_mfma_f32_16x16x32_bf16 v[92:95], a[64:67], a[156:159], v[92:95]\n\t"
"ds_read_b64_tr_b16 a[196:197], v249 offset:1152\n\t"
"v_cvt_pk_bf16_f32 v219, v219, v223\n\t"
"v_mfma_f32_16x16x32_bf16 v[92:95], a[68:71], a[172:175], v[92:95]\n\t"
"buffer_load_dwordx4 v237, s[12:15], s22 offen offset:1024 nt lds\n\t"
"s_lshl_b32 m0, s73, 8\n\t"
"v_mfma_f32_16x16x32_bf16 v[76:79], a[56:59], a[156:159], v[76:79]\n\t"
"ds_write_addtid_b32 v211 offset:13472\n\t"
"v_cvt_pk_bf16_f32 v192, v192, v196\n\t"
"v_mfma_f32_16x16x32_bf16 v[76:79], a[60:63], a[172:175], v[76:79]\n\t"
"ds_write_addtid_b32 v208 offset:1024\n\t"
"v_cvt_pk_bf16_f32 v216, v216, v220\n\t"
"v_mfma_f32_16x16x32_bf16 v[60:63], a[48:51], a[156:159], v[60:63]\n\t"
"ds_write_addtid_b32 v219 offset:15520\n\t"
"v_cvt_pk_bf16_f32 v201, v201, v205\n\t"
"s_add_u32 m0, s26, 103936\n\t"
"v_mfma_f32_16x16x32_bf16 v[60:63], a[52:55], a[172:175], v[60:63]\n\t"
"s_waitcnt vmcnt(19) lgkmcnt(8)\n\t"
"buffer_load_dwordx4 v238, s[12:15], s22 offen offset:2048 nt lds\n\t"
"s_lshl_b32 m0, s73, 8\n\t"
"v_mfma_f32_16x16x32_bf16 v[144:147], a[72:75], a[144:147], v[144:147]\n\t"
"s_nop 0\n\t"
"ds_write_addtid_b32 v192 offset:0\n\t"
"ds_write_addtid_b32 v200 offset:2048\n\t"
"v_mfma_f32_16x16x32_bf16 v[144:147], a[76:79], a[160:163], v[144:147]\n\t"
"s_nop 0\n\t"
"ds_write_addtid_b32 v201 offset:10368\n\t"
"ds_write_addtid_b32 v216 offset:3072\n\t"
"v_mfma_f32_16x16x32_bf16 v[160:163], a[80:83], a[144:147], v[160:163]\n\t"
"s_add_u32 m0, s26, 103936\n\t"
"ds_read_b128 a[152:155], v224 offset:18080\n\t"
"ds_read_b128 a[172:175], v224 offset:20384\n\t"
"v_mfma_f32_16x16x32_bf16 v[160:163], a[84:87], a[160:163], v[160:163]\n\t"
"buffer_load_dwordx4 v239, s[12:15], s22 offen offset:3072 nt lds\n\t"
"v_mfma_f32_16x16x32_bf16 v[176:179], a[88:91], a[144:147], v[176:179]\n\t"
"s_nop 0\n\t"
"ds_read_b128 a[168:171], v224 offset:20128\n\t"
"ds_read_b128 a[156:159], v224 offset:18336\n\t"
"v_mfma_f32_16x16x32_bf16 v[176:179], a[92:95], a[160:163], v[176:179]\n\t"
"buffer_store_dwordx4 v[108:111], v232, s[16:19], s25 offen nt\n\t"
"v_mfma_f32_16x16x32_bf16 v[180:183], a[88:91], a[148:151], v[180:183]\n\t"
"s_nop 0\n\t"
"ds_read_b64_tr_b16 a[198:199], v249 offset:1664\n\t"
"ds_read_b64_tr_b16 a[200:201], v250 offset:1152\n\t"
"v_mfma_f32_16x16x32_bf16 v[180:183], a[92:95], a[164:167], v[180:183]\n\t"
"s_nop 0\n\t"
"ds_read_b64_tr_b16 a[202:203], v250 offset:1664\n\t"
"ds_read_b64_tr_b16 a[204:205], v251 offset:1152\n\t"
"v_mfma_f32_16x16x32_bf16 v[164:167], a[80:83], a[148:151], v[164:167]\n\t"
"ds_read_b64_tr_b16 a[206:207], v251 offset:1664\n\t"
"v_mfma_f32_16x16x32_bf16 v[164:167], a[84:87], a[164:167], v[164:167]\n\t"
"s_mov_b64 exec, s[36:37]\n\t"
"buffer_load_dwordx4 v240, s[12:15], s22 offen offset:3584 nt lds\n\t"
"s_mov_b64 exec, -1\n\t"
"v_mfma_f32_16x16x32_bf16 v[148:151], a[72:75], a[148:151], v[148:151]\n\t"
"s_waitcnt lgkmcnt(5)\n\t"
"v_mfma_f32_16x16x32_bf16 v[148:151], a[76:79], a[164:167], v[148:151]\n\t"
"s_lshl_b32 m0, s73, 8\n\t"
"s_barrier\n\t"
"v_mfma_f32_16x16x32_bf16 v[152:155], a[72:75], a[152:155], v[152:155]\n\t"
"ds_read_b128 a[144:147], v224 offset:0\n\t"
"v_mfma_f32_16x16x32_bf16 v[152:155], a[76:79], a[168:171], v[152:155]\n\t"
"ds_read_b128 a[160:163], v224 offset:2048\n\t"
"v_add_u32 v248, 54016, v226\n\t"
"v_mfma_f32_16x16x32_bf16 v[168:171], a[80:83], a[152:155], v[168:171]\n\t"
"ds_read_b128 a[148:151], v224 offset:256\n\t"
"v_mfma_f32_16x16x32_bf16 v[168:171], a[84:87], a[168:171], v[168:171]\n\t"
"ds_read_b128 a[164:167], v224 offset:2304\n\t"
"v_mfma_f32_16x16x32_bf16 v[184:187], a[88:91], a[152:155], v[184:187]\n\t"
"ds_read_b128 a[228:231], v254 offset:18432\n\t"
"v_xor_b32 v249, 32, v248\n\t"
"v_mfma_f32_16x16x32_bf16 v[184:187], a[92:95], a[168:171], v[184:187]\n\t"
"ds_read_b128 a[168:171], v224 offset:2560\n\t"
"v_mfma_f32_16x16x32_bf16 v[188:191], a[88:91], a[156:159], v[188:191]\n\t"
"v_xor_b32 v250, 64, v248\n\t"
"v_mfma_f32_16x16x32_bf16 v[188:191], a[92:95], a[172:175], v[188:191]\n\t"
"v_mfma_f32_16x16x32_bf16 v[172:175], a[80:83], a[156:159], v[172:175]\n\t"
"v_mfma_f32_16x16x32_bf16 v[172:175], a[84:87], a[172:175], v[172:175]\n\t"
"ds_read_b128 a[152:155], v224 offset:512\n\t"
"v_mfma_f32_16x16x32_bf16 v[156:159], a[72:75], a[156:159], v[156:159]\n\t"
"ds_read_b128 a[156:159], v224 offset:768\n\t"
"v_mfma_f32_16x16x32_bf16 v[156:159], a[76:79], a[172:175], v[156:159]\n\t"
"v_xor_b32 v251, 96, v248\n\t"
"v_mfma_f32_16x16x32_bf16 v[192:195], a[224:227], a[176:179], 0\n\t"
"s_waitcnt lgkmcnt(3)\n\t"
"ds_write_b128 v255, a[228:231] offset:0\n\t"
"v_mfma_f32_16x16x32_bf16 v[196:199], a[224:227], a[180:183], 0\n\t"
"v_mfma_f32_16x16x32_bf16 v[200:203], a[224:227], a[184:187], 0\n\t"
"v_mfma_f32_16x16x32_bf16 v[204:207], a[224:227], a[188:191], 0\n\t"
"v_mfma_f32_16x16x32_bf16 v[208:211], a[224:227], a[192:195], 0\n\t"
"ds_read_b64_tr_b16 a[178:179], v248 offset:128\n\t"
"v_mfma_f32_16x16x32_bf16 v[212:215], a[224:227], a[196:199], 0\n\t"
"ds_read_b128 a[172:175], v224 offset:2816\n\t"
"v_mfma_f32_16x16x32_bf16 v[216:219], a[224:227], a[200:203], 0\n\t"
"ds_read_b64_tr_b16 a[176:177], v248 offset:0\n\t"
"v_mfma_f32_16x16x32_bf16 v[220:223], a[224:227], a[204:207], 0\n\t"
"ds_read_b64_tr_b16 a[180:181], v249 offset:256\n\t"
"v_cvt_pk_bf16_f32 v192, v192, v196\n\t"
"v_mfma_f32_16x16x32_bf16 v[0:3], a[0:3], a[144:147], 0\n\t"
"ds_write_addtid_b32 v192 offset:16544\n\t"
"v_cvt_pk_bf16_f32 v193, v193, v197\n\t"
"v_mfma_f32_16x16x32_bf16 v[0:3], a[4:7], a[160:163], v[0:3]\n\t"
"ds_read_b64_tr_b16 a[186:187], v249 offset:128\n\t"
"v_cvt_pk_bf16_f32 v201, v201, v205\n\t"
"v_mfma_f32_16x16x32_bf16 v[16:19], a[8:11], a[144:147], 0\n\t"
"ds_write_addtid_b32 v201 offset:26912\n\t"
"v_mfma_f32_16x16x32_bf16 v[16:19], a[12:15], a[160:163], v[16:19]\n\t"
"ds_read_b64_tr_b16 a[184:185], v249 offset:0\n\t"
"v_cvt_pk_bf16_f32 v210, v210, v214\n\t"
"v_mfma_f32_16x16x32_bf16 v[32:35], a[16:19], a[144:147], 0\n\t"
"ds_read_b128 a[144:147], v224 offset:1024\n\t"
"v_cvt_pk_bf16_f32 v200, v200, v204\n\t"
"v_mfma_f32_16x16x32_bf16 v[32:35], a[20:23], a[160:163], v[32:35]\n\t"
"ds_read_b64_tr_b16 a[188:189], v248 offset:256\n\t"
"v_cvt_pk_bf16_f32 v209, v209, v213\n\t"
"v_mfma_f32_16x16x32_bf16 v[36:39], a[16:19], a[148:151], 0\n\t"
"ds_write_addtid_b32 v210 offset:21696\n\t"
"v_cvt_pk_bf16_f32 v194, v194, v198\n\t"
"v_mfma_f32_16x16x32_bf16 v[36:39], a[20:23], a[164:167], v[36:39]\n\t"
"ds_read_b128 a[160:163], v224 offset:3072\n\t"
"v_cvt_pk_bf16_f32 v217, v217, v221\n\t"
"v_mfma_f32_16x16x32_bf16 v[20:23], a[8:11], a[148:151], 0\n\t"
"ds_write_addtid_b32 v194 offset:20672\n\t"
"v_mfma_f32_16x16x32_bf16 v[20:23], a[12:15], a[164:167], v[20:23]\n\t"
"ds_read_b64_tr_b16 a[194:195], v250 offset:128\n\t"
"v_cvt_pk_bf16_f32 v218, v218, v222\n\t"
"v_mfma_f32_16x16x32_bf16 v[4:7], a[0:3], a[148:151], 0\n\t"
"ds_read_b128 a[148:151], v224 offset:1280\n\t"
"v_cvt_pk_bf16_f32 v211, v211, v215\n\t"
"v_mfma_f32_16x16x32_bf16 v[4:7], a[4:7], a[164:167], v[4:7]\n\t"
"s_waitcnt lgkmcnt(9)\n\t"
"ds_read_b128 a[164:167], v224 offset:3328\n\t"
"v_cvt_pk_bf16_f32 v202, v202, v206\n\t"
"v_mfma_f32_16x16x32_bf16 v[8:11], a[0:3], a[152:155], 0\n\t"
"ds_write_addtid_b32 v211 offset:30016\n\t"
"v_cvt_pk_bf16_f32 v219, v219, v223\n\t"
"v_mfma_f32_16x16x32_bf16 v[8:11], a[4:7], a[168:171], v[8:11]\n\t"
"ds_write_addtid_b32 v202 offset:22720\n\t"
"v_cvt_pk_bf16_f32 v216, v216, v220\n\t"
"v_mfma_f32_16x16x32_bf16 v[24:27], a[8:11], a[152:155], 0\n\t"
"ds_write_addtid_b32 v217 offset:27936\n\t"
"v_mfma_f32_16x16x32_bf16 v[24:27], a[12:15], a[168:171], v[24:27]\n\t"
"ds_write_addtid_b32 v200 offset:18592\n\t"
"v_cvt_pk_bf16_f32 v208, v208, v212\n\t"
"v_mfma_f32_16x16x32_bf16 v[40:43], a[16:19], a[152:155], 0\n\t"
"ds_write_addtid_b32 v219 offset:32064\n\t"
"v_mfma_f32_16x16x32_bf16 v[40:43], a[20:23], a[168:171], v[40:43]\n\t"
"ds_write_addtid_b32 v218 offset:23744\n\t"
"v_mfma_f32_16x16x32_bf16 v[44:47], a[16:19], a[156:159], 0\n\t"
"ds_read_b128 a[168:171], v224 offset:3584\n\t"
"v_cvt_pk_bf16_f32 v203, v203, v207\n\t"
"v_mfma_f32_16x16x32_bf16 v[44:47], a[20:23], a[172:175], v[44:47]\n\t"
"ds_write_addtid_b32 v193 offset:24864\n\t"
"v_mfma_f32_16x16x32_bf16 v[28:31], a[8:11], a[156:159], 0\n\t"
"ds_write_addtid_b32 v209 offset:25888\n\t"
"v_mfma_f32_16x16x32_bf16 v[28:31], a[12:15], a[172:175], v[28:31]\n\t"
"ds_write_addtid_b32 v208 offset:17568\n\t"
"v_cvt_pk_bf16_f32 v195, v195, v199\n\t"
"v_mfma_f32_16x16x32_bf16 v[12:15], a[0:3], a[156:159], 0\n\t"
"ds_read_b64_tr_b16 a[192:193], v250 offset:0\n\t"
"v_mfma_f32_16x16x32_bf16 v[12:15], a[4:7], a[172:175], v[12:15]\n\t"
"s_waitcnt lgkmcnt(9)\n\t"
"ds_write_addtid_b32 v195 offset:28992\n\t"
"v_mfma_f32_16x16x32_bf16 v[0:3], a[24:27], a[144:147], v[0:3]\n\t"
"ds_write_addtid_b32 v203 offset:31040\n\t"
"v_mfma_f32_16x16x32_bf16 v[0:3], a[28:31], a[160:163], v[0:3]\n\t"
"ds_read_b128 a[152:155], v224 offset:1536\n\t"
"v_mfma_f32_16x16x32_bf16 v[16:19], a[32:35], a[144:147], v[16:19]\n\t"
"ds_write_addtid_b32 v216 offset:19616\n\t"
"v_mfma_f32_16x16x32_bf16 v[16:19], a[36:39], a[160:163], v[16:19]\n\t"
"ds_read_b128 a[172:175], v224 offset:3840\n\t"
"v_mfma_f32_16x16x32_bf16 v[32:35], a[40:43], a[144:147], v[32:35]\n\t"
"ds_read_b64_tr_b16 a[196:197], v251 offset:256\n\t"
"v_mfma_f32_16x16x32_bf16 v[32:35], a[44:47], a[160:163], v[32:35]\n\t"
"ds_read_b128 a[156:159], v224 offset:1792\n\t"
"v_mfma_f32_16x16x32_bf16 v[36:39], a[40:43], a[148:151], v[36:39]\n\t"
"ds_read_b64_tr_b16 a[202:203], v251 offset:128\n\t"
"v_mfma_f32_16x16x32_bf16 v[36:39], a[44:47], a[164:167], v[36:39]\n\t"
"ds_read_b64_tr_b16 a[200:201], v251 offset:0\n\t"
"v_mfma_f32_16x16x32_bf16 v[20:23], a[32:35], a[148:151], v[20:23]\n\t"
"ds_read_b64_tr_b16 a[204:205], v250 offset:256\n\t"
"v_mfma_f32_16x16x32_bf16 v[20:23], a[36:39], a[164:167], v[20:23]\n\t"
"v_mfma_f32_16x16x32_bf16 v[4:7], a[24:27], a[148:151], v[4:7]\n\t"
"v_mfma_f32_16x16x32_bf16 v[4:7], a[28:31], a[164:167], v[4:7]\n\t"
"s_waitcnt lgkmcnt(0)\n\t"
"s_barrier\n\t"
"v_mfma_f32_16x16x32_bf16 v[8:11], a[24:27], a[152:155], v[8:11]\n\t"
"ds_read_b128 a[144:147], v224 offset:16544\n\t"
"v_mfma_f32_16x16x32_bf16 v[8:11], a[28:31], a[168:171], v[8:11]\n\t"
"ds_read_b128 a[160:163], v224 offset:18592\n\t"
"v_mfma_f32_16x16x32_bf16 v[24:27], a[32:35], a[152:155], v[24:27]\n\t"
"ds_read_b128 a[148:151], v224 offset:16800\n\t"
"v_mfma_f32_16x16x32_bf16 v[24:27], a[36:39], a[168:171], v[24:27]\n\t"
"ds_read_b128 a[164:167], v224 offset:18848\n\t"
"v_swap_b32 v236, v241\n\t"
"v_mfma_f32_16x16x32_bf16 v[40:43], a[40:43], a[152:155], v[40:43]\n\t"
"v_mfma_f32_16x16x32_bf16 v[40:43], a[44:47], a[168:171], v[40:43]\n\t"
"ds_write_b128 v254, a[244:247] offset:20736\n\t"
"v_mfma_f32_16x16x32_bf16 v[44:47], a[40:43], a[156:159], v[44:47]\n\t"
"v_swap_b32 v237, v242\n\t"
"v_mfma_f32_16x16x32_bf16 v[44:47], a[44:47], a[172:175], v[44:47]\n\t"
"v_swap_b32 v238, v243\n\t"
"v_mfma_f32_16x16x32_bf16 v[28:31], a[32:35], a[156:159], v[28:31]\n\t"
"v_mfma_f32_16x16x32_bf16 v[28:31], a[36:39], a[172:175], v[28:31]\n\t"
"v_swap_b32 v239, v244\n\t"
"v_add_u32 v248, 101632, v227\n\t"
"v_mfma_f32_16x16x32_bf16 v[12:15], a[24:27], a[156:159], v[12:15]\n\t"
"v_mfma_f32_16x16x32_bf16 v[12:15], a[28:31], a[172:175], v[12:15]\n\t"
"v_swap_b32 v240, v245\n\t"
"v_mfma_f32_4x4x4_16b_bf16 v[200:203], a[220:221], a[178:179], 0\n\t"
"ds_read_b128 a[168:171], v224 offset:19104\n\t"
"v_mfma_f32_4x4x4_16b_bf16 v[204:207], a[220:221], a[186:187], 0\n\t"
"v_xor_b32 v249, 32, v248\n\t"
"v_mfma_f32_4x4x4_16b_bf16 v[216:219], a[220:221], a[194:195], 0\n\t"
"v_mfma_f32_4x4x4_16b_bf16 v[220:223], a[220:221], a[202:203], 0\n\t"
"v_add_u32 v225, v225, v253\n\t"
"v_mfma_f32_4x4x4_16b_bf16 v[192:195], a[222:223], a[176:177], v[200:203]\n\t"
"v_sub_u32 v253, 0, v253\n\t"
"v_mfma_f32_4x4x4_16b_bf16 v[196:199], a[222:223], a[184:185], v[204:207]\n\t"
"v_accvgpr_mov_b32 a228, a208\n\t"
"v_mfma_f32_4x4x4_16b_bf16 v[200:203], a[222:223], a[180:181], v[200:203]\n\t"
"ds_read_b128 a[152:155], v224 offset:17056\n\t"
"v_mfma_f32_4x4x4_16b_bf16 v[204:207], a[222:223], a[188:189], v[204:207]\n\t"
"v_mfma_f32_4x4x4_16b_bf16 v[208:211], a[222:223], a[192:193], v[216:219]\n\t"
"ds_read_b128 a[156:159], v224 offset:17312\n\t"
"v_mfma_f32_4x4x4_16b_bf16 v[212:215], a[222:223], a[200:201], v[220:223]\n\t"
"ds_read_b128 a[172:175], v224 offset:19360\n\t"
"v_mfma_f32_4x4x4_16b_bf16 v[216:219], a[222:223], a[196:197], v[216:219]\n\t"
"v_accvgpr_mov_b32 a208, a209\n\t"
"v_mfma_f32_4x4x4_16b_bf16 v[220:223], a[222:223], a[204:205], v[220:223]\n\t"
"s_waitcnt vmcnt(15) lgkmcnt(3)\n\t"
"buffer_load_dwordx4 a[16:19], v252, s[84:87], s57 offen offset:0 sc1\n\t"
"v_mfma_f32_16x16x32_bf16 v[144:147], a[96:99], a[144:147], v[144:147]\n\t"
"v_cvt_pk_bf16_f32 v192, -v192, -v196\n\t"
"v_cvt_pk_bf16_f32 v194, -v194, -v198\n\t"
"v_mfma_f32_16x16x32_bf16 v[144:147], a[100:103], a[160:163], v[144:147]\n\t"
"buffer_load_dwordx4 a[20:23], v252, s[84:87], s57 offen offset:1024 sc1\n\t"
"v_mfma_f32_16x16x32_bf16 v[160:163], a[104:107], a[144:147], v[160:163]\n\t"
"ds_write_addtid_b32 v194 offset:1024\n\t"
"v_cvt_pk_bf16_f32 v201, v201, v205\n\t"
"v_mfma_f32_16x16x32_bf16 v[160:163], a[108:111], a[160:163], v[160:163]\n\t"
"buffer_load_dwordx4 a[12:15], v235, s[84:87], s57 offen offset:3072 sc1\n\t"
"v_mfma_f32_16x16x32_bf16 v[176:179], a[112:115], a[144:147], v[176:179]\n\t"
"ds_read_b128 a[144:147], v224 offset:17568\n\t"
"v_cvt_pk_bf16_f32 v203, v203, v207\n\t"
"v_mfma_f32_16x16x32_bf16 v[176:179], a[116:119], a[160:163], v[176:179]\n\t"
"buffer_load_dwordx4 a[8:11], v235, s[84:87], s57 offen offset:2048 sc1\n\t"
"v_mfma_f32_16x16x32_bf16 v[180:183], a[112:115], a[148:151], v[180:183]\n\t"
"ds_read_b128 a[160:163], v224 offset:19616\n\t"
"v_cvt_pk_bf16_f32 v219, v219, v223\n\t"
"v_mfma_f32_16x16x32_bf16 v[180:183], a[116:119], a[164:167], v[180:183]\n\t"
"buffer_load_dwordx4 a[0:3], v235, s[84:87], s57 offen offset:0 sc1\n\t"
"v_mfma_f32_16x16x32_bf16 v[164:167], a[104:107], a[148:151], v[164:167]\n\t"
"ds_write_addtid_b32 v203 offset:13472\n\t"
"v_cvt_pk_bf16_f32 v211, -v211, -v215\n\t"
"v_mfma_f32_16x16x32_bf16 v[164:167], a[108:111], a[164:167], v[164:167]\n\t"
"buffer_load_dwordx4 a[4:7], v235, s[84:87], s57 offen offset:1024 sc1\n\t"
"v_mfma_f32_16x16x32_bf16 v[148:151], a[96:99], a[148:151], v[148:151]\n\t"
"ds_read_b128 a[148:151], v224 offset:17824\n\t"
"v_cvt_pk_bf16_f32 v216, v216, v220\n\t"
"v_mfma_f32_16x16x32_bf16 v[148:151], a[100:103], a[164:167], v[148:151]\n\t"
"s_waitcnt lgkmcnt(5)\n\t"
"buffer_load_dwordx4 a[40:43], v252, s[84:87], s63 offen offset:0 sc1\n\t"
"v_mfma_f32_16x16x32_bf16 v[152:155], a[96:99], a[152:155], v[152:155]\n\t"
"ds_read_b128 a[164:167], v224 offset:19872\n\t"
"v_cvt_pk_bf16_f32 v218, v218, v222\n\t"
"v_mfma_f32_16x16x32_bf16 v[152:155], a[100:103], a[168:171], v[152:155]\n\t"
"buffer_load_dwordx4 a[44:47], v252, s[84:87], s63 offen offset:1024 sc1\n\t"
"v_mfma_f32_16x16x32_bf16 v[168:171], a[104:107], a[152:155], v[168:171]\n\t"
"ds_write_addtid_b32 v216 offset:10368\n\t"
"v_cvt_pk_bf16_f32 v208, -v208, -v212\n\t"
"v_mfma_f32_16x16x32_bf16 v[168:171], a[108:111], a[168:171], v[168:171]\n\t"
"ds_write_addtid_b32 v208 offset:2048\n\t"
"v_cvt_pk_bf16_f32 v193, -v193, -v197\n\t"
"v_mfma_f32_16x16x32_bf16 v[184:187], a[112:115], a[152:155], v[184:187]\n\t"
"ds_write_addtid_b32 v192 offset:0\n\t"
"v_cvt_pk_bf16_f32 v210, -v210, -v214\n\t"
"v_mfma_f32_16x16x32_bf16 v[184:187], a[116:119], a[168:171], v[184:187]\n\t"
"buffer_load_dwordx4 a[36:39], v235, s[84:87], s63 offen offset:3072 sc1\n\t"
"v_mfma_f32_16x16x32_bf16 v[188:191], a[112:115], a[156:159], v[188:191]\n\t"
"s_nop 0\n\t"
"ds_write_addtid_b32 v210 offset:3072\n\t"
"ds_write_addtid_b32 v219 offset:15520\n\t"
"v_mfma_f32_16x16x32_bf16 v[188:191], a[116:119], a[172:175], v[188:191]\n\t"
"ds_write_addtid_b32 v211 offset:7200\n\t"
"v_cvt_pk_bf16_f32 v209, -v209, -v213\n\t"
"v_mfma_f32_16x16x32_bf16 v[172:175], a[104:107], a[156:159], v[172:175]\n\t"
"ds_write_addtid_b32 v209 offset:6176\n\t"
"v_cvt_pk_bf16_f32 v200, v200, v204\n\t"
"v_mfma_f32_16x16x32_bf16 v[172:175], a[108:111], a[172:175], v[172:175]\n\t"
"ds_read_b128 a[152:155], v224 offset:18080\n\t"
"v_cvt_pk_bf16_f32 v217, v217, v221\n\t"
"v_mfma_f32_16x16x32_bf16 v[156:159], a[96:99], a[156:159], v[156:159]\n\t"
"ds_write_addtid_b32 v201 offset:12448\n\t"
"v_cvt_pk_bf16_f32 v195, -v195, -v199\n\t"
"v_mfma_f32_16x16x32_bf16 v[156:159], a[100:103], a[172:175], v[156:159]\n\t"
"s_waitcnt vmcnt(16) lgkmcnt(9)\n\t"
"ds_write_addtid_b32 v217 offset:14496\n\t"
"ds_write_addtid_b32 v218 offset:11392\n\t"
"v_mfma_f32_16x16x32_bf16 v[144:147], a[120:123], a[144:147], v[144:147]\n\t"
"s_nop 0\n\t"
"ds_write_addtid_b32 v193 offset:4128\n\t"
"ds_write_addtid_b32 v195 offset:5152\n\t"
"v_mfma_f32_16x16x32_bf16 v[144:147], a[124:127], a[160:163], v[144:147]\n\t"
"ds_write_addtid_b32 v200 offset:8320\n\t"
"v_cvt_pk_bf16_f32 v202, v202, v206\n\t"
"v_mfma_f32_16x16x32_bf16 v[160:163], a[128:131], a[144:147], v[160:163]\n\t"
"ds_write_addtid_b32 v202 offset:9344\n\t"
"v_mfma_f32_16x16x32_bf16 v[160:163], a[132:135], a[160:163], v[160:163]\n\t"
"buffer_load_dwordx4 a[32:35], v235, s[84:87], s63 offen offset:2048 sc1\n\t"
"v_mfma_f32_16x16x32_bf16 v[176:179], a[136:139], a[144:147], v[176:179]\n\t"
"ds_read_b128 a[172:175], v224 offset:20384\n\t"
"v_xor_b32 v250, 64, v248\n\t"
"v_mfma_f32_16x16x32_bf16 v[176:179], a[140:143], a[160:163], v[176:179]\n\t"
"s_nop 0\n\t"
"ds_read_b128 a[168:171], v224 offset:20128\n\t"
"ds_read_b128 a[156:159], v224 offset:18336\n\t"
"v_mfma_f32_16x16x32_bf16 v[180:183], a[136:139], a[148:151], v[180:183]\n\t"
"v_accvgpr_mov_b32 a209, a228\n\t"
"v_xor_b32 v251, 96, v248\n\t"
"v_mfma_f32_16x16x32_bf16 v[180:183], a[140:143], a[164:167], v[180:183]\n\t"
"buffer_load_dwordx4 a[24:27], v235, s[84:87], s63 offen offset:0 sc1\n\t"
"v_mfma_f32_16x16x32_bf16 v[164:167], a[128:131], a[148:151], v[164:167]\n\t"
"v_mfma_f32_16x16x32_bf16 v[164:167], a[132:135], a[164:167], v[164:167]\n\t"
"buffer_load_dwordx4 a[28:31], v235, s[84:87], s63 offen offset:1024 sc1\n\t"
"v_mfma_f32_16x16x32_bf16 v[148:151], a[120:123], a[148:151], v[148:151]\n\t"
"v_mfma_f32_16x16x32_bf16 v[148:151], a[124:127], a[164:167], v[148:151]\n\t"
"s_waitcnt vmcnt(12) lgkmcnt(4)\n\t"
"s_waitcnt lgkmcnt(0)\n\t"
"s_barrier\n\t"
"v_mfma_f32_16x16x32_bf16 v[152:155], a[120:123], a[152:155], v[152:155]\n\t"
"s_add_u32 m0, s26, 56192\n\t"
"ds_read_b128 a[144:147], v224 offset:0\n\t"
"ds_read_b64_tr_b16 a[176:177], v248 offset:0\n\t"
"v_mfma_f32_16x16x32_bf16 v[152:155], a[124:127], a[168:171], v[152:155]\n\t"
"buffer_load_dwordx4 v236, s[12:15], s22 offen offset:128 nt lds\n\t"
"v_mfma_f32_16x16x32_bf16 v[168:171], a[128:131], a[152:155], v[168:171]\n\t"
"s_nop 0\n\t"
"ds_read_b128 a[160:163], v224 offset:2048\n\t"
"ds_read_b128 a[148:151], v224 offset:256\n\t"
"v_mfma_f32_16x16x32_bf16 v[168:171], a[132:135], a[168:171], v[168:171]\n\t"
"s_nop 0\n\t"
"ds_read_b128 a[164:167], v224 offset:2304\n\t"
"ds_read_b64_tr_b16 a[178:179], v248 offset:512\n\t"
"v_mfma_f32_16x16x32_bf16 v[184:187], a[136:139], a[152:155], v[184:187]\n\t"
"ds_read_b64_tr_b16 a[180:181], v249 offset:0\n\t"
"v_mfma_f32_16x16x32_bf16 v[184:187], a[140:143], a[168:171], v[184:187]\n\t"
"buffer_store_dwordx4 a[232:235], v233, s[16:19], s25 offen nt\n\t"
"v_mfma_f32_16x16x32_bf16 v[188:191], a[136:139], a[156:159], v[188:191]\n\t"
"s_nop 0\n\t"
"ds_read_b128 a[152:155], v224 offset:512\n\t"
"ds_read_b64_tr_b16 a[182:183], v249 offset:512\n\t"
"v_mfma_f32_16x16x32_bf16 v[188:191], a[140:143], a[172:175], v[188:191]\n\t"
"ds_read_b64_tr_b16 a[184:185], v250 offset:0\n\t"
"v_mfma_f32_16x16x32_bf16 v[172:175], a[128:131], a[156:159], v[172:175]\n\t"
"s_nop 0\n\t"
"ds_read_b64_tr_b16 a[186:187], v250 offset:512\n\t"
"ds_read_b64_tr_b16 a[188:189], v251 offset:0\n\t"
"v_mfma_f32_16x16x32_bf16 v[172:175], a[132:135], a[172:175], v[172:175]\n\t"
"buffer_load_dwordx4 v237, s[12:15], s22 offen offset:1152 nt lds\n\t"
"v_mfma_f32_16x16x32_bf16 v[156:159], a[120:123], a[156:159], v[156:159]\n\t"
"s_nop 0\n\t"
"ds_read_b128 a[156:159], v224 offset:768\n\t"
"ds_read_b64_tr_b16 a[190:191], v251 offset:512\n\t"
"v_mfma_f32_16x16x32_bf16 v[156:159], a[124:127], a[172:175], v[156:159]\n\t"
"s_waitcnt vmcnt(9) lgkmcnt(11)\n\t"
".p2align 3\n\t"
"ds_read_b64_tr_b16 a[192:193], v248 offset:1152\n\t"
"ds_read_b64_tr_b16 a[194:195], v248 offset:1664\n\t"
"v_pk_add_f32 v[144:145], v[48:49], v[144:145] neg_lo:[0,1] neg_hi:[0,1]\n\t"
"v_pk_add_f32 v[146:147], v[50:51], v[146:147] neg_lo:[0,1] neg_hi:[0,1]\n\t"
"v_pk_fma_f32 v[48:49], v[144:145], s[90:91], v[48:49] neg_lo:[1,0,0] neg_hi:[1,0,0]\n\t"
"v_pk_fma_f32 v[50:51], v[146:147], s[90:91], v[50:51] neg_lo:[1,0,0] neg_hi:[1,0,0]\n\t"
"v_pk_add_f32 v[160:161], v[64:65], v[160:161] neg_lo:[0,1] neg_hi:[0,1]\n\t"
"v_pk_add_f32 v[162:163], v[66:67], v[162:163] neg_lo:[0,1] neg_hi:[0,1]\n\t"
"v_pk_fma_f32 v[64:65], v[160:161], s[90:91], v[64:65] neg_lo:[1,0,0] neg_hi:[1,0,0]\n\t"
"v_pk_fma_f32 v[66:67], v[162:163], s[90:91], v[66:67] neg_lo:[1,0,0] neg_hi:[1,0,0]\n\t"
"v_pk_add_f32 v[176:177], v[80:81], v[176:177] neg_lo:[0,1] neg_hi:[0,1]\n\t"
"v_pk_add_f32 v[178:179], v[82:83], v[178:179] neg_lo:[0,1] neg_hi:[0,1]\n\t"
"v_pk_fma_f32 v[80:81], v[176:177], s[90:91], v[80:81] neg_lo:[1,0,0] neg_hi:[1,0,0]\n\t"
"v_pk_fma_f32 v[82:83], v[178:179], s[90:91], v[82:83] neg_lo:[1,0,0] neg_hi:[1,0,0]\n\t"
"v_pk_add_f32 v[180:181], v[84:85], v[180:181] neg_lo:[0,1] neg_hi:[0,1]\n\t"
"v_pk_add_f32 v[182:183], v[86:87], v[182:183] neg_lo:[0,1] neg_hi:[0,1]\n\t"
"v_pk_fma_f32 v[84:85], v[180:181], s[90:91], v[84:85] neg_lo:[1,0,0] neg_hi:[1,0,0]\n\t"
"v_pk_fma_f32 v[86:87], v[182:183], s[90:91], v[86:87] neg_lo:[1,0,0] neg_hi:[1,0,0]\n\t"
"v_pk_add_f32 v[164:165], v[68:69], v[164:165] neg_lo:[0,1] neg_hi:[0,1]\n\t"
"v_pk_add_f32 v[166:167], v[70:71], v[166:167] neg_lo:[0,1] neg_hi:[0,1]\n\t"
"v_pk_fma_f32 v[68:69], v[164:165], s[90:91], v[68:69] neg_lo:[1,0,0] neg_hi:[1,0,0]\n\t"
"v_pk_fma_f32 v[70:71], v[166:167], s[90:91], v[70:71] neg_lo:[1,0,0] neg_hi:[1,0,0]\n\t"
"v_pk_add_f32 v[148:149], v[52:53], v[148:149] neg_lo:[0,1] neg_hi:[0,1]\n\t"
"v_pk_add_f32 v[150:151], v[54:55], v[150:151] neg_lo:[0,1] neg_hi:[0,1]\n\t"
"v_pk_fma_f32 v[52:53], v[148:149], s[90:91], v[52:53] neg_lo:[1,0,0] neg_hi:[1,0,0]\n\t"
"v_pk_fma_f32 v[54:55], v[150:151], s[90:91], v[54:55] neg_lo:[1,0,0] neg_hi:[1,0,0]\n\t"
"v_pk_add_f32 v[152:153], v[56:57], v[152:153] neg_lo:[0,1] neg_hi:[0,1]\n\t"
"v_pk_add_f32 v[154:155], v[58:59], v[154:155] neg_lo:[0,1] neg_hi:[0,1]\n\t"
"v_pk_fma_f32 v[56:57], v[152:153], s[90:91], v[56:57] neg_lo:[1,0,0] neg_hi:[1,0,0]\n\t"
"v_pk_fma_f32 v[58:59], v[154:155], s[90:91], v[58:59] neg_lo:[1,0,0] neg_hi:[1,0,0]\n\t"
"v_pk_add_f32 v[168:169], v[72:73], v[168:169] neg_lo:[0,1] neg_hi:[0,1]\n\t"
"v_pk_add_f32 v[170:171], v[74:75], v[170:171] neg_lo:[0,1] neg_hi:[0,1]\n\t"
"v_pk_fma_f32 v[72:73], v[168:169], s[90:91], v[72:73] neg_lo:[1,0,0] neg_hi:[1,0,0]\n\t"
"v_pk_fma_f32 v[74:75], v[170:171], s[90:91], v[74:75] neg_lo:[1,0,0] neg_hi:[1,0,0]\n\t"
"v_pk_add_f32 v[184:185], v[88:89], v[184:185] neg_lo:[0,1] neg_hi:[0,1]\n\t"
"v_pk_add_f32 v[186:187], v[90:91], v[186:187] neg_lo:[0,1] neg_hi:[0,1]\n\t"
"v_pk_fma_f32 v[88:89], v[184:185], s[90:91], v[88:89] neg_lo:[1,0,0] neg_hi:[1,0,0]\n\t"
"v_pk_fma_f32 v[90:91], v[186:187], s[90:91], v[90:91] neg_lo:[1,0,0] neg_hi:[1,0,0]\n\t"
"v_pk_add_f32 v[188:189], v[92:93], v[188:189] neg_lo:[0,1] neg_hi:[0,1]\n\t"
"v_pk_add_f32 v[190:191], v[94:95], v[190:191] neg_lo:[0,1] neg_hi:[0,1]\n\t"
"v_pk_fma_f32 v[92:93], v[188:189], s[90:91], v[92:93] neg_lo:[1,0,0] neg_hi:[1,0,0]\n\t"
"v_pk_fma_f32 v[94:95], v[190:191], s[90:91], v[94:95] neg_lo:[1,0,0] neg_hi:[1,0,0]\n\t"
"v_pk_add_f32 v[172:173], v[76:77], v[172:173] neg_lo:[0,1] neg_hi:[0,1]\n\t"
"v_pk_add_f32 v[174:175], v[78:79], v[174:175] neg_lo:[0,1] neg_hi:[0,1]\n\t"
"v_pk_fma_f32 v[76:77], v[172:173], s[90:91], v[76:77] neg_lo:[1,0,0] neg_hi:[1,0,0]\n\t"
"v_pk_fma_f32 v[78:79], v[174:175], s[90:91], v[78:79] neg_lo:[1,0,0] neg_hi:[1,0,0]\n\t"
"v_pk_add_f32 v[156:157], v[60:61], v[156:157] neg_lo:[0,1] neg_hi:[0,1]\n\t"
"v_pk_add_f32 v[158:159], v[62:63], v[158:159] neg_lo:[0,1] neg_hi:[0,1]\n\t"
"v_pk_fma_f32 v[60:61], v[156:157], s[90:91], v[60:61] neg_lo:[1,0,0] neg_hi:[1,0,0]\n\t"
"v_pk_fma_f32 v[62:63], v[158:159], s[90:91], v[62:63] neg_lo:[1,0,0] neg_hi:[1,0,0]\n\t"
"v_mfma_f32_16x16x32_bf16 v[48:51], a[0:3], a[144:147], v[48:51]\n\t"
"s_nop 0\n\t"
"ds_read_b64_tr_b16 a[196:197], v249 offset:1152\n\t"
"ds_read_b64_tr_b16 a[198:199], v249 offset:1664\n\t"
"v_mfma_f32_16x16x32_bf16 v[48:51], a[4:7], a[160:163], v[48:51]\n\t"
"buffer_load_dwordx4 v238, s[12:15], s22 offen offset:2176 nt lds\n\t"
"v_mfma_f32_16x16x32_bf16 v[64:67], a[8:11], a[144:147], v[64:67]\n\t"
"ds_read_b64_tr_b16 a[200:201], v250 offset:1152\n\t"
"v_mfma_f32_16x16x32_bf16 v[64:67], a[12:15], a[160:163], v[64:67]\n\t"
"s_nop 0\n\t"
"ds_read_b128 a[168:171], v224 offset:2560\n\t"
"ds_read_b64_tr_b16 a[202:203], v250 offset:1664\n\t"
"v_mfma_f32_16x16x32_bf16 v[80:83], a[16:19], a[144:147], v[80:83]\n\t"
"s_nop 0\n\t"
"ds_read_b64_tr_b16 a[204:205], v251 offset:1152\n\t"
"ds_read_b64_tr_b16 a[206:207], v251 offset:1664\n\t"
"v_mfma_f32_16x16x32_bf16 v[80:83], a[20:23], a[160:163], v[80:83]\n\t"
"s_waitcnt lgkmcnt(13)\n\t"
"buffer_store_dwordx4 a[236:239], v234, s[16:19], s25 offen nt\n\t"
"v_mfma_f32_16x16x32_bf16 v[192:195], a[216:219], a[176:179], 0\n\t"
"ds_read_b64_tr_b16 a[176:177], v248 offset:42624\n\t"
"v_mfma_f32_16x16x32_bf16 v[196:199], a[216:219], a[180:183], 0\n\t"
"s_waitcnt lgkmcnt(12)\n\t"
"ds_read_b64_tr_b16 a[178:179], v248 offset:43136\n\t"
"ds_read_b64_tr_b16 a[180:181], v249 offset:42624\n\t"
"v_mfma_f32_16x16x32_bf16 v[200:203], a[216:219], a[184:187], 0\n\t"
"s_waitcnt lgkmcnt(12)\n\t"
"buffer_load_dwordx4 v239, s[12:15], s22 offen offset:3200 nt lds\n\t"
"v_mfma_f32_16x16x32_bf16 v[204:207], a[216:219], a[188:191], 0\n\t"
"s_waitcnt lgkmcnt(10)\n\t"
"ds_read_b128 a[172:175], v224 offset:2816\n\t"
"v_mfma_f32_16x16x32_bf16 v[208:211], a[216:219], a[192:195], 0\n\t"
"s_waitcnt lgkmcnt(9)\n\t"
"ds_read_b128 a[160:163], v224 offset:3072\n\t"
"v_mfma_f32_16x16x32_bf16 v[212:215], a[216:219], a[196:199], 0\n\t"
"s_waitcnt lgkmcnt(7)\n\t"
"ds_read_b64_tr_b16 a[182:183], v249 offset:43136\n\t"
"ds_read_b64_tr_b16 a[184:185], v250 offset:42624\n\t"
"v_mfma_f32_16x16x32_bf16 v[216:219], a[216:219], a[200:203], 0\n\t"
"s_waitcnt lgkmcnt(7)\n\t"
"ds_read_b64_tr_b16 a[186:187], v250 offset:43136\n\t"
"v_mfma_f32_16x16x32_bf16 v[220:223], a[216:219], a[204:207], 0\n\t"
"s_mov_b64 exec, s[36:37]\n\t"
"buffer_load_dwordx4 v240, s[12:15], s22 offen offset:3712 nt lds\n\t"
"s_mov_b64 exec, -1\n\t"
"v_mfma_f32_16x16x32_bf16 v[84:87], a[16:19], a[148:151], v[84:87]\n\t"
"s_lshl_b32 m0, s73, 8\n\t"
"ds_read_b64_tr_b16 a[188:189], v251 offset:42624\n\t"
"v_cvt_pk_bf16_f32 v192, v192, v196\n\t"
"v_mfma_f32_16x16x32_bf16 v[84:87], a[20:23], a[164:167], v[84:87]\n\t"
"ds_read_b128 a[144:147], v224 offset:1024\n\t"
"v_cvt_pk_bf16_f32 v202, v202, v206\n\t"
"v_mfma_f32_16x16x32_bf16 v[68:71], a[8:11], a[148:151], v[68:71]\n\t"
"ds_write_addtid_b32 v202 offset:22720\n\t"
"v_cvt_pk_bf16_f32 v195, v195, v199\n\t"
"v_mfma_f32_16x16x32_bf16 v[68:71], a[12:15], a[164:167], v[68:71]\n\t"
"ds_write_addtid_b32 v195 offset:28992\n\t"
"v_cvt_pk_bf16_f32 v200, v200, v204\n\t"
"v_mfma_f32_16x16x32_bf16 v[52:55], a[0:3], a[148:151], v[52:55]\n\t"
"ds_read_b64_tr_b16 a[190:191], v251 offset:43136\n\t"
"v_cvt_pk_bf16_f32 v201, v201, v205\n\t"
"v_mfma_f32_16x16x32_bf16 v[52:55], a[4:7], a[164:167], v[52:55]\n\t"
"ds_read_b128 a[164:167], v224 offset:3328\n\t"
"v_cvt_pk_bf16_f32 v208, -v208, -v212\n\t"
"v_mfma_f32_16x16x32_bf16 v[56:59], a[0:3], a[152:155], v[56:59]\n\t"
"ds_read_b128 a[148:151], v224 offset:1280\n\t"
"v_cvt_pk_bf16_f32 v194, v194, v198\n\t"
"v_mfma_f32_16x16x32_bf16 v[56:59], a[4:7], a[168:171], v[56:59]\n\t"
"s_nop 0\n\t"
"ds_write_addtid_b32 v192 offset:16544\n\t"
"ds_write_addtid_b32 v200 offset:18592\n\t"
"v_mfma_f32_16x16x32_bf16 v[72:75], a[8:11], a[152:155], v[72:75]\n\t"
"ds_read_b64_tr_b16 a[192:193], v248 offset:21888\n\t"
"v_cvt_pk_bf16_f32 v210, -v210, -v214\n\t"
"v_mfma_f32_16x16x32_bf16 v[72:75], a[12:15], a[168:171], v[72:75]\n\t"
"ds_write_addtid_b32 v194 offset:20672\n\t"
"v_cvt_pk_bf16_f32 v209, -v209, -v213\n\t"
"v_mfma_f32_16x16x32_bf16 v[88:91], a[16:19], a[152:155], v[88:91]\n\t"
"ds_write_addtid_b32 v209 offset:25888\n\t"
"v_cvt_pk_bf16_f32 v193, v193, v197\n\t"
"v_mfma_f32_16x16x32_bf16 v[88:91], a[20:23], a[168:171], v[88:91]\n\t"
"s_waitcnt lgkmcnt(10)\n\t"
"ds_write_addtid_b32 v210 offset:21696\n\t"
"v_cvt_pk_bf16_f32 v218, -v218, -v222\n\t"
"v_mfma_f32_16x16x32_bf16 v[92:95], a[16:19], a[156:159], v[92:95]\n\t"
"ds_read_b64_tr_b16 a[194:195], v248 offset:22400\n\t"
"v_cvt_pk_bf16_f32 v216, -v216, -v220\n\t"
"v_mfma_f32_16x16x32_bf16 v[92:95], a[20:23], a[172:175], v[92:95]\n\t"
"ds_write_addtid_b32 v218 offset:23744\n\t"
"v_cvt_pk_bf16_f32 v211, -v211, -v215\n\t"
"v_mfma_f32_16x16x32_bf16 v[76:79], a[8:11], a[156:159], v[76:79]\n\t"
"ds_write_addtid_b32 v216 offset:19616\n\t"
"v_cvt_pk_bf16_f32 v219, -v219, -v223\n\t"
"v_mfma_f32_16x16x32_bf16 v[76:79], a[12:15], a[172:175], v[76:79]\n\t"
"ds_read_b128 a[152:155], v224 offset:1536\n\t"
"v_mfma_f32_16x16x32_bf16 v[60:63], a[0:3], a[156:159], v[60:63]\n\t"
"ds_write_addtid_b32 v193 offset:24864\n\t"
"v_cvt_pk_bf16_f32 v217, -v217, -v221\n\t"
"v_mfma_f32_16x16x32_bf16 v[60:63], a[4:7], a[172:175], v[60:63]\n\t"
"s_waitcnt vmcnt(7) lgkmcnt(9)\n\t"
"ds_write_addtid_b32 v219 offset:32064\n\t"
"ds_read_b64_tr_b16 a[196:197], v249 offset:21888\n\t"
"v_mfma_f32_16x16x32_bf16 v[144:147], a[24:27], a[144:147], v[144:147]\n\t"
"ds_write_addtid_b32 v208 offset:17568\n\t"
"v_cvt_pk_bf16_f32 v203, v203, v207\n\t"
"v_mfma_f32_16x16x32_bf16 v[144:147], a[28:31], a[160:163], v[144:147]\n\t"
"s_nop 0\n\t"
"ds_write_addtid_b32 v203 offset:31040\n\t"
"ds_write_addtid_b32 v217 offset:27936\n\t"
"v_mfma_f32_16x16x32_bf16 v[160:163], a[32:35], a[144:147], v[160:163]\n\t"
"s_nop 0\n\t"
"ds_write_addtid_b32 v201 offset:26912\n\t"
"ds_write_addtid_b32 v211 offset:30016\n\t"
"v_mfma_f32_16x16x32_bf16 v[160:163], a[36:39], a[160:163], v[160:163]\n\t"
"ds_read_b128 a[156:159], v224 offset:1792\n\t"
"v_mfma_f32_16x16x32_bf16 v[176:179], a[40:43], a[144:147], v[176:179]\n\t"
"s_nop 0\n\t"
"ds_read_b128 a[168:171], v224 offset:3584\n\t"
"ds_read_b128 a[172:175], v224 offset:3840\n\t"
"v_mfma_f32_16x16x32_bf16 v[176:179], a[44:47], a[160:163], v[176:179]\n\t"
"ds_read_b64_tr_b16 a[198:199], v249 offset:22400\n\t"
"v_mfma_f32_16x16x32_bf16 v[180:183], a[40:43], a[148:151], v[180:183]\n\t"
"ds_read_b64_tr_b16 a[200:201], v250 offset:21888\n\t"
"v_mfma_f32_16x16x32_bf16 v[180:183], a[44:47], a[164:167], v[180:183]\n\t"
"ds_read_b64_tr_b16 a[202:203], v250 offset:22400\n\t"
"v_mfma_f32_16x16x32_bf16 v[164:167], a[32:35], a[148:151], v[164:167]\n\t"
"s_nop 0\n\t"
"ds_read_b64_tr_b16 a[204:205], v251 offset:21888\n\t"
"ds_read_b64_tr_b16 a[206:207], v251 offset:22400\n\t"
"v_mfma_f32_16x16x32_bf16 v[164:167], a[36:39], a[164:167], v[164:167]\n\t"
"v_mfma_f32_16x16x32_bf16 v[148:151], a[24:27], a[148:151], v[148:151]\n\t"
"s_waitcnt lgkmcnt(5)\n\t"
"v_mfma_f32_16x16x32_bf16 v[148:151], a[28:31], a[164:167], v[148:151]\n\t"
"s_barrier\n\t"
"v_mfma_f32_16x16x32_bf16 v[152:155], a[24:27], a[152:155], v[152:155]\n\t"
"ds_read_b128 a[144:147], v224 offset:16544\n\t"
"v_mfma_f32_16x16x32_bf16 v[152:155], a[28:31], a[168:171], v[152:155]\n\t"
"ds_read_b128 a[160:163], v224 offset:18592\n\t"
"v_add_u32 v248, 33280, v226\n\t"
"v_mfma_f32_16x16x32_bf16 v[168:171], a[32:35], a[152:155], v[168:171]\n\t"
"ds_read_b128 a[148:151], v224 offset:16800\n\t"
"v_mfma_f32_16x16x32_bf16 v[168:171], a[36:39], a[168:171], v[168:171]\n\t"
"ds_read_b128 a[164:167], v224 offset:18848\n\t"
"v_xor_b32 v249, 32, v248\n\t"
"v_mfma_f32_16x16x32_bf16 v[184:187], a[40:43], a[152:155], v[184:187]\n\t"
"v_xor_b32 v250, 64, v248\n\t"
"v_mfma_f32_16x16x32_bf16 v[184:187], a[44:47], a[168:171], v[184:187]\n\t"
"v_mfma_f32_16x16x32_bf16 v[188:191], a[40:43], a[156:159], v[188:191]\n\t"
"v_mfma_f32_16x16x32_bf16 v[188:191], a[44:47], a[172:175], v[188:191]\n\t"
"ds_read_b128 a[152:155], v224 offset:17056\n\t"
"v_mfma_f32_16x16x32_bf16 v[172:175], a[32:35], a[156:159], v[172:175]\n\t"
"v_mfma_f32_16x16x32_bf16 v[172:175], a[36:39], a[172:175], v[172:175]\n\t"
"v_mfma_f32_16x16x32_bf16 v[156:159], a[24:27], a[156:159], v[156:159]\n\t"
"ds_read_b128 a[168:171], v224 offset:19104\n\t"
"v_xor_b32 v251, 96, v248\n\t"
"v_mfma_f32_16x16x32_bf16 v[156:159], a[28:31], a[172:175], v[156:159]\n\t"
"ds_read_b128 a[172:175], v224 offset:19360\n\t"
"v_mfma_f32_16x16x32_bf16 v[192:195], a[216:219], a[176:179], 0\n\t"
"ds_read_b64_tr_b16 a[178:179], v248 offset:128\n\t"
"v_mfma_f32_16x16x32_bf16 v[196:199], a[216:219], a[180:183], 0\n\t"
"ds_read_b64_tr_b16 a[176:177], v248 offset:0\n\t"
"v_mfma_f32_16x16x32_bf16 v[200:203], a[216:219], a[184:187], 0\n\t"
"ds_read_b64_tr_b16 a[180:181], v249 offset:256\n\t"
"v_mfma_f32_16x16x32_bf16 v[204:207], a[216:219], a[188:191], 0\n\t"
"ds_read_b64_tr_b16 a[186:187], v249 offset:128\n\t"
"v_mfma_f32_16x16x32_bf16 v[208:211], a[216:219], a[192:195], 0\n\t"
"s_waitcnt lgkmcnt(7)\n\t"
"ds_read_b64_tr_b16 a[184:185], v249 offset:0\n\t"
"v_mfma_f32_16x16x32_bf16 v[212:215], a[216:219], a[196:199], 0\n\t"
"ds_read_b64_tr_b16 a[188:189], v248 offset:256\n\t"
"v_mfma_f32_16x16x32_bf16 v[216:219], a[216:219], a[200:203], 0\n\t"
"ds_read_b64_tr_b16 a[194:195], v250 offset:128\n\t"
"v_mfma_f32_16x16x32_bf16 v[220:223], a[216:219], a[204:207], 0\n\t"
"s_xor_b32 s24, s24, 384\n\t"
"ds_read_b128 a[156:159], v224 offset:17312\n\t"
"ds_read_b64_tr_b16 a[192:193], v250 offset:0\n\t"
"v_mfma_f32_16x16x32_bf16 v[0:3], a[48:51], a[144:147], v[0:3]\n\t"
"v_cvt_pk_bf16_f32 v194, -v194, -v198\n\t"
"v_cvt_pk_bf16_f32 v195, -v195, -v199\n\t"
"v_mfma_f32_16x16x32_bf16 v[0:3], a[52:55], a[160:163], v[0:3]\n\t"
"ds_write_addtid_b32 v194 offset:4128\n\t"
"v_mfma_f32_16x16x32_bf16 v[16:19], a[56:59], a[144:147], v[16:19]\n\t"
"ds_read_b64_tr_b16 a[196:197], v251 offset:256\n\t"
"v_cvt_pk_bf16_f32 v201, -v201, -v205\n\t"
"v_mfma_f32_16x16x32_bf16 v[16:19], a[60:63], a[160:163], v[16:19]\n\t"
"v_cvt_pk_bf16_f32 v208, -v208, -v212\n\t"
"v_cvt_pk_bf16_f32 v209, -v209, -v213\n\t"
"v_mfma_f32_16x16x32_bf16 v[32:35], a[64:67], a[144:147], v[32:35]\n\t"
"ds_write_addtid_b32 v201 offset:10368\n\t"
"v_cvt_pk_bf16_f32 v203, -v203, -v207\n\t"
"v_mfma_f32_16x16x32_bf16 v[32:35], a[68:71], a[160:163], v[32:35]\n\t"
"ds_read_b64_tr_b16 a[202:203], v251 offset:128\n\t"
"v_cvt_pk_bf16_f32 v200, -v200, -v204\n\t"
"v_mfma_f32_16x16x32_bf16 v[36:39], a[64:67], a[148:151], v[36:39]\n\t"
"ds_read_b128 a[160:163], v224 offset:19616\n\t"
"v_cvt_pk_bf16_f32 v210, -v210, -v214\n\t"
"v_mfma_f32_16x16x32_bf16 v[36:39], a[68:71], a[164:167], v[36:39]\n\t"
"ds_read_b128 a[144:147], v224 offset:17568\n\t"
"v_cvt_pk_bf16_f32 v211, -v211, -v215\n\t"
"v_mfma_f32_16x16x32_bf16 v[20:23], a[56:59], a[148:151], v[20:23]\n\t"
"ds_write_addtid_b32 v200 offset:2048\n\t"
"v_mfma_f32_16x16x32_bf16 v[20:23], a[60:63], a[164:167], v[20:23]\n\t"
"ds_write_addtid_b32 v208 offset:1024\n\t"
"v_cvt_pk_bf16_f32 v216, -v216, -v220\n\t"
"v_mfma_f32_16x16x32_bf16 v[4:7], a[48:51], a[148:151], v[4:7]\n\t"
"ds_read_b128 a[148:151], v224 offset:17824\n\t"
"v_cvt_pk_bf16_f32 v219, -v219, -v223\n\t"
"v_mfma_f32_16x16x32_bf16 v[4:7], a[52:55], a[164:167], v[4:7]\n\t"
"s_waitcnt lgkmcnt(8)\n\t"
"ds_write_addtid_b32 v210 offset:5152\n\t"
"v_mfma_f32_16x16x32_bf16 v[8:11], a[48:51], a[152:155], v[8:11]\n\t"
"ds_write_addtid_b32 v216 offset:3072\n\t"
"v_cvt_pk_bf16_f32 v218, -v218, -v222\n\t"
"v_mfma_f32_16x16x32_bf16 v[8:11], a[52:55], a[168:171], v[8:11]\n\t"
"ds_write_addtid_b32 v218 offset:7200\n\t"
"v_cvt_pk_bf16_f32 v217, -v217, -v221\n\t"
"v_mfma_f32_16x16x32_bf16 v[24:27], a[56:59], a[152:155], v[24:27]\n\t"
"ds_read_b128 a[164:167], v224 offset:19872\n\t"
"v_mfma_f32_16x16x32_bf16 v[24:27], a[60:63], a[168:171], v[24:27]\n\t"
"ds_write_addtid_b32 v203 offset:14496\n\t"
"v_cvt_pk_bf16_f32 v193, -v193, -v197\n\t"
"v_mfma_f32_16x16x32_bf16 v[40:43], a[64:67], a[152:155], v[40:43]\n\t"
"ds_write_addtid_b32 v219 offset:15520\n\t"
"v_cvt_pk_bf16_f32 v202, -v202, -v206\n\t"
"v_mfma_f32_16x16x32_bf16 v[40:43], a[68:71], a[168:171], v[40:43]\n\t"
"ds_write_addtid_b32 v209 offset:9344\n\t"
"v_cvt_pk_bf16_f32 v192, -v192, -v196\n\t"
"v_mfma_f32_16x16x32_bf16 v[44:47], a[64:67], a[156:159], v[44:47]\n\t"
"ds_read_b128 a[168:171], v224 offset:20128\n\t"
"v_mfma_f32_16x16x32_bf16 v[44:47], a[68:71], a[172:175], v[44:47]\n\t"
"buffer_load_dwordx4 a[64:67], v252, s[84:87], s42 offen offset:0 sc1\n\t"
"v_mfma_f32_16x16x32_bf16 v[28:31], a[56:59], a[156:159], v[28:31]\n\t"
"ds_write_addtid_b32 v217 offset:11392\n\t"
"v_mfma_f32_16x16x32_bf16 v[28:31], a[60:63], a[172:175], v[28:31]\n\t"
"buffer_load_dwordx4 a[68:71], v252, s[84:87], s42 offen offset:1024 sc1\n\t"
"v_mfma_f32_16x16x32_bf16 v[12:15], a[48:51], a[156:159], v[12:15]\n\t"
"s_xor_b32 s20, s20, 384\n\t"
"ds_write_addtid_b32 v193 offset:8320\n\t"
"ds_write_addtid_b32 v195 offset:12448\n\t"
"v_mfma_f32_16x16x32_bf16 v[12:15], a[52:55], a[172:175], v[12:15]\n\t"
"s_waitcnt lgkmcnt(8)\n\t"
"buffer_load_dwordx4 a[60:63], v235, s[84:87], s42 offen offset:3072 sc1\n\t"
"v_mfma_f32_16x16x32_bf16 v[96:99], a[72:75], a[144:147], v[0:3]\n\t"
"ds_write_addtid_b32 v202 offset:6176\n\t"
"v_mfma_f32_16x16x32_bf16 v[96:99], a[76:79], a[160:163], v[96:99]\n\t"
"buffer_load_dwordx4 a[56:59], v235, s[84:87], s42 offen offset:2048 sc1\n\t"
"v_mfma_f32_16x16x32_bf16 v[112:115], a[80:83], a[144:147], v[16:19]\n\t"
"s_xor_b32 s27, s27, 384\n\t"
"ds_write_addtid_b32 v192 offset:0\n\t"
"ds_write_addtid_b32 v211 offset:13472\n\t"
"v_mfma_f32_16x16x32_bf16 v[112:115], a[84:87], a[160:163], v[112:115]\n\t"
"buffer_load_dwordx4 a[48:51], v235, s[84:87], s42 offen offset:0 sc1\n\t"
"v_mfma_f32_16x16x32_bf16 v[128:131], a[88:91], a[144:147], v[32:35]\n\t"
"s_xor_b32 s25, s25, 384\n\t"
"ds_read_b128 a[156:159], v224 offset:18336\n\t"
"ds_read_b128 a[172:175], v224 offset:20384\n\t"
"v_mfma_f32_16x16x32_bf16 v[128:131], a[92:95], a[160:163], v[128:131]\n\t"
"s_waitcnt lgkmcnt(8)\n\t"
"ds_read_b128 a[152:155], v224 offset:18080\n\t"
"v_mfma_f32_16x16x32_bf16 v[132:135], a[88:91], a[148:151], v[36:39]\n\t"
"ds_read_b64_tr_b16 a[200:201], v251 offset:0\n\t"
"v_mfma_f32_16x16x32_bf16 v[132:135], a[92:95], a[164:167], v[132:135]\n\t"
"buffer_load_dwordx4 a[52:55], v235, s[84:87], s42 offen offset:1024 sc1\n\t"
"v_mfma_f32_16x16x32_bf16 v[116:119], a[80:83], a[148:151], v[20:23]\n\t"
"ds_read_b64_tr_b16 a[204:205], v250 offset:256\n\t"
"v_mfma_f32_16x16x32_bf16 v[116:119], a[84:87], a[164:167], v[116:119]\n\t"
"v_mfma_f32_16x16x32_bf16 v[100:103], a[72:75], a[148:151], v[4:7]\n\t"
"v_mfma_f32_16x16x32_bf16 v[100:103], a[76:79], a[164:167], v[100:103]\n\t"
"s_waitcnt lgkmcnt(0)\n\t"
"s_barrier\n\t"
"v_mfma_f32_16x16x32_bf16 v[104:107], a[72:75], a[152:155], v[8:11]\n\t"
"ds_read_b128 a[160:163], v224 offset:2048\n\t"
"v_mfma_f32_16x16x32_bf16 v[104:107], a[76:79], a[168:171], v[104:107]\n\t"
"ds_read_b128 a[144:147], v224 offset:0\n\t"
"v_mfma_f32_16x16x32_bf16 v[120:123], a[80:83], a[152:155], v[24:27]\n\t"
"ds_read_b128 a[148:151], v224 offset:256\n\t"
"v_mfma_f32_16x16x32_bf16 v[120:123], a[84:87], a[168:171], v[120:123]\n\t"
"ds_read_b128 a[164:167], v224 offset:2304\n\t"
"v_mfma_f32_16x16x32_bf16 v[136:139], a[88:91], a[152:155], v[40:43]\n\t"
"v_mfma_f32_16x16x32_bf16 v[136:139], a[92:95], a[168:171], v[136:139]\n\t"
"ds_write_b128 v254, a[240:243] offset:0\n\t"
"v_mfma_f32_16x16x32_bf16 v[140:143], a[88:91], a[156:159], v[44:47]\n\t"
"v_mfma_f32_16x16x32_bf16 v[140:143], a[92:95], a[172:175], v[140:143]\n\t"
"buffer_load_dwordx4 a[88:91], v252, s[84:87], s48 offen offset:0 sc1\n\t"
"v_mfma_f32_16x16x32_bf16 v[124:127], a[80:83], a[156:159], v[28:31]\n\t"
"v_mfma_f32_16x16x32_bf16 v[124:127], a[84:87], a[172:175], v[124:127]\n\t"
"buffer_load_dwordx4 a[92:95], v252, s[84:87], s48 offen offset:1024 sc1\n\t"
"v_mfma_f32_16x16x32_bf16 v[108:111], a[72:75], a[156:159], v[12:15]\n\t"
"ds_read_b128 a[168:171], v224 offset:2560\n\t"
"v_mfma_f32_16x16x32_bf16 v[108:111], a[76:79], a[172:175], v[108:111]\n\t"
"buffer_load_dwordx4 a[84:87], v235, s[84:87], s48 offen offset:3072 sc1\n\t"
"v_mfma_f32_4x4x4_16b_bf16 v[200:203], a[220:221], a[178:179], 0\n\t"
"ds_read_b64_tr_b16 a[178:179], v248 offset:41600\n\t"
"v_mfma_f32_4x4x4_16b_bf16 v[204:207], a[220:221], a[186:187], 0\n\t"
"buffer_load_dwordx4 a[80:83], v235, s[84:87], s48 offen offset:2048 sc1\n\t"
"v_mfma_f32_4x4x4_16b_bf16 v[216:219], a[220:221], a[194:195], 0\n\t"
"ds_read_b64_tr_b16 a[186:187], v249 offset:41600\n\t"
"v_mfma_f32_4x4x4_16b_bf16 v[220:223], a[220:221], a[202:203], 0\n\t"
"buffer_load_dwordx4 a[72:75], v235, s[84:87], s48 offen offset:0 sc1\n\t"
"v_mfma_f32_4x4x4_16b_bf16 v[192:195], a[222:223], a[176:177], v[200:203]\n\t"
"ds_read_b128 a[152:155], v224 offset:512\n\t"
"v_mfma_f32_4x4x4_16b_bf16 v[196:199], a[222:223], a[184:185], v[204:207]\n\t"
"ds_read_b64_tr_b16 a[194:195], v250 offset:41600\n\t"
"v_mfma_f32_4x4x4_16b_bf16 v[200:203], a[222:223], a[180:181], v[200:203]\n\t"
"ds_read_b64_tr_b16 a[202:203], v251 offset:41600\n\t"
"v_mfma_f32_4x4x4_16b_bf16 v[204:207], a[222:223], a[188:189], v[204:207]\n\t"
"ds_read_b128 a[156:159], v224 offset:768\n\t"
"v_mfma_f32_4x4x4_16b_bf16 v[208:211], a[222:223], a[192:193], v[216:219]\n\t"
"ds_read_b64_tr_b16 a[176:177], v248 offset:41472\n\t"
"v_mfma_f32_4x4x4_16b_bf16 v[212:215], a[222:223], a[200:201], v[220:223]\n\t"
"ds_read_b64_tr_b16 a[184:185], v249 offset:41472\n\t"
"v_mfma_f32_4x4x4_16b_bf16 v[216:219], a[222:223], a[196:197], v[216:219]\n\t"
"ds_read_b64_tr_b16 a[180:181], v249 offset:41728\n\t"
"v_mfma_f32_4x4x4_16b_bf16 v[220:223], a[222:223], a[204:205], v[220:223]\n\t"
"s_waitcnt lgkmcnt(8)\n\t"
"buffer_load_dwordx4 a[76:79], v235, s[84:87], s48 offen offset:1024 sc1\n\t"
"v_mfma_f32_16x16x32_bf16 v[96:99], a[96:99], a[144:147], v[96:99]\n\t"
"ds_read_b128 a[172:175], v224 offset:2816\n\t"
"v_cvt_pk_bf16_f32 v195, -v195, -v199\n\t"
"v_mfma_f32_16x16x32_bf16 v[96:99], a[100:103], a[160:163], v[96:99]\n\t"
"ds_read_b64_tr_b16 a[188:189], v248 offset:41728\n\t"
"v_cvt_pk_bf16_f32 v193, -v193, -v197\n\t"
"v_mfma_f32_16x16x32_bf16 v[112:115], a[104:107], a[144:147], v[112:115]\n\t"
"ds_write_addtid_b32 v195 offset:21696\n\t"
"v_cvt_pk_bf16_f32 v203, v203, v207\n\t"
"v_mfma_f32_16x16x32_bf16 v[112:115], a[108:111], a[160:163], v[112:115]\n\t"
"ds_write_addtid_b32 v203 offset:30016\n\t"
"v_cvt_pk_bf16_f32 v202, v202, v206\n\t"
"v_mfma_f32_16x16x32_bf16 v[128:131], a[112:115], a[144:147], v[128:131]\n\t"
"ds_read_b64_tr_b16 a[192:193], v250 offset:41472\n\t"
"v_cvt_pk_bf16_f32 v211, -v211, -v215\n\t"
"v_mfma_f32_16x16x32_bf16 v[128:131], a[116:119], a[160:163], v[128:131]\n\t"
"ds_write_addtid_b32 v202 offset:25888\n\t"
"v_cvt_pk_bf16_f32 v192, -v192, -v196\n\t"
"v_mfma_f32_16x16x32_bf16 v[132:135], a[112:115], a[148:151], v[132:135]\n\t"
"ds_read_b128 a[144:147], v224 offset:1024\n\t"
"v_cvt_pk_bf16_f32 v216, v216, v220\n\t"
"v_mfma_f32_16x16x32_bf16 v[132:135], a[116:119], a[164:167], v[132:135]\n\t"
"ds_read_b128 a[160:163], v224 offset:3072\n\t"
"v_cvt_pk_bf16_f32 v210, -v210, -v214\n\t"
"v_mfma_f32_16x16x32_bf16 v[116:119], a[104:107], a[148:151], v[116:119]\n\t"
"ds_write_addtid_b32 v193 offset:20672\n\t"
"v_mfma_f32_16x16x32_bf16 v[116:119], a[108:111], a[164:167], v[116:119]\n\t"
"ds_write_addtid_b32 v216 offset:26912\n\t"
"v_cvt_pk_bf16_f32 v209, -v209, -v213\n\t"
"v_mfma_f32_16x16x32_bf16 v[100:103], a[96:99], a[148:151], v[100:103]\n\t"
"ds_write_addtid_b32 v209 offset:22720\n\t"
"v_cvt_pk_bf16_f32 v208, -v208, -v212\n\t"
"v_mfma_f32_16x16x32_bf16 v[100:103], a[100:103], a[164:167], v[100:103]\n\t"
"s_waitcnt lgkmcnt(9)\n\t"
"ds_read_b128 a[164:167], v224 offset:3328\n\t"
"v_cvt_pk_bf16_f32 v201, v201, v205\n\t"
"v_mfma_f32_16x16x32_bf16 v[104:107], a[96:99], a[152:155], v[104:107]\n\t"
"ds_write_addtid_b32 v210 offset:19616\n\t"
"v_cvt_pk_bf16_f32 v217, v217, v221\n\t"
"v_mfma_f32_16x16x32_bf16 v[104:107], a[100:103], a[168:171], v[104:107]\n\t"
"ds_read_b128 a[148:151], v224 offset:1280\n\t"
"v_cvt_pk_bf16_f32 v219, v219, v223\n\t"
"v_mfma_f32_16x16x32_bf16 v[120:123], a[104:107], a[152:155], v[120:123]\n\t"
"ds_write_addtid_b32 v211 offset:23744\n\t"
"v_mfma_f32_16x16x32_bf16 v[120:123], a[108:111], a[168:171], v[120:123]\n\t"
"ds_write_addtid_b32 v219 offset:32064\n\t"
"v_cvt_pk_bf16_f32 v200, v200, v204\n\t"
"v_mfma_f32_16x16x32_bf16 v[136:139], a[112:115], a[152:155], v[136:139]\n\t"
"ds_write_addtid_b32 v192 offset:16544\n\t"
"v_cvt_pk_bf16_f32 v194, -v194, -v198\n\t"
"v_mfma_f32_16x16x32_bf16 v[136:139], a[116:119], a[168:171], v[136:139]\n\t"
"ds_write_addtid_b32 v200 offset:24864\n\t"
"v_cvt_pk_bf16_f32 v218, v218, v222\n\t"
"v_mfma_f32_16x16x32_bf16 v[140:143], a[112:115], a[156:159], v[140:143]\n\t"
"ds_write_addtid_b32 v217 offset:31040\n\t"
"v_mfma_f32_16x16x32_bf16 v[140:143], a[116:119], a[172:175], v[140:143]\n\t"
"buffer_load_dwordx4 a[112:115], v252, s[84:87], s75 offen offset:0 sc1\n\t"
"v_mfma_f32_16x16x32_bf16 v[124:127], a[104:107], a[156:159], v[124:127]\n\t"
"ds_write_addtid_b32 v201 offset:28992\n\t"
"v_mfma_f32_16x16x32_bf16 v[124:127], a[108:111], a[172:175], v[124:127]\n\t"
"buffer_load_dwordx4 a[116:119], v252, s[84:87], s75 offen offset:1024 sc1\n\t"
"v_mfma_f32_16x16x32_bf16 v[108:111], a[96:99], a[156:159], v[108:111]\n\t"
"s_nop 0\n\t"
"ds_read_b128 a[168:171], v224 offset:3584\n\t"
"ds_write_addtid_b32 v218 offset:27936\n\t"
"v_mfma_f32_16x16x32_bf16 v[108:111], a[100:103], a[172:175], v[108:111]\n\t"
"s_waitcnt lgkmcnt(8)\n\t"
"buffer_load_dwordx4 a[108:111], v235, s[84:87], s75 offen offset:3072 sc1\n\t"
"v_mfma_f32_16x16x32_bf16 v[96:99], a[120:123], a[144:147], v[96:99]\n\t"
"ds_write_addtid_b32 v194 offset:17568\n\t"
"v_mfma_f32_16x16x32_bf16 v[96:99], a[124:127], a[160:163], v[96:99]\n\t"
"buffer_load_dwordx4 a[104:107], v235, s[84:87], s75 offen offset:2048 sc1\n\t"
"v_mfma_f32_16x16x32_bf16 v[112:115], a[128:131], a[144:147], v[112:115]\n\t"
"ds_write_addtid_b32 v208 offset:18592\n\t"
"v_mfma_f32_16x16x32_bf16 v[112:115], a[132:135], a[160:163], v[112:115]\n\t"
"buffer_load_dwordx4 a[96:99], v235, s[84:87], s75 offen offset:0 sc1\n\t"
"v_mfma_f32_16x16x32_bf16 v[128:131], a[136:139], a[144:147], v[128:131]\n\t"
"s_nop 0\n\t"
"ds_read_b128 a[152:155], v224 offset:1536\n\t"
"ds_read_b128 a[172:175], v224 offset:3840\n\t"
"v_mfma_f32_16x16x32_bf16 v[128:131], a[140:143], a[160:163], v[128:131]\n\t"
"ds_read_b128 a[156:159], v224 offset:1792\n\t"
"v_mfma_f32_16x16x32_bf16 v[132:135], a[136:139], a[148:151], v[132:135]\n\t"
"ds_read_b64_tr_b16 a[200:201], v251 offset:41472\n\t"
"v_mfma_f32_16x16x32_bf16 v[132:135], a[140:143], a[164:167], v[132:135]\n\t"
"ds_read_b64_tr_b16 a[196:197], v251 offset:41728\n\t"
"v_mfma_f32_16x16x32_bf16 v[116:119], a[128:131], a[148:151], v[116:119]\n\t"
"ds_read_b64_tr_b16 a[204:205], v250 offset:41728\n\t"
"v_mfma_f32_16x16x32_bf16 v[116:119], a[132:135], a[164:167], v[116:119]\n\t"
"buffer_load_dwordx4 a[100:103], v235, s[84:87], s75 offen offset:1024 sc1\n\t"
"v_mfma_f32_16x16x32_bf16 v[100:103], a[120:123], a[148:151], v[100:103]\n\t"
"v_mfma_f32_16x16x32_bf16 v[100:103], a[124:127], a[164:167], v[100:103]\n\t"
"s_waitcnt lgkmcnt(0)\n\t"
"s_barrier\n\t"
"v_mfma_f32_16x16x32_bf16 v[104:107], a[120:123], a[152:155], v[104:107]\n\t"
"ds_read_b128 a[160:163], v224 offset:18592\n\t"
"v_add_u32 v248, 101632, v226\n\t"
"v_mfma_f32_16x16x32_bf16 v[104:107], a[124:127], a[168:171], v[104:107]\n\t"
"ds_read_b128 a[144:147], v224 offset:16544\n\t"
"v_mfma_f32_16x16x32_bf16 v[120:123], a[128:131], a[152:155], v[120:123]\n\t"
"ds_read_b128 a[148:151], v224 offset:16800\n\t"
"v_mfma_f32_16x16x32_bf16 v[120:123], a[132:135], a[168:171], v[120:123]\n\t"
"ds_read_b128 a[164:167], v224 offset:18848\n\t"
"v_mfma_f32_16x16x32_bf16 v[136:139], a[136:139], a[152:155], v[136:139]\n\t"
"v_mfma_f32_16x16x32_bf16 v[136:139], a[140:143], a[168:171], v[136:139]\n\t"
"v_xor_b32 v249, 32, v248\n\t"
"v_mfma_f32_16x16x32_bf16 v[140:143], a[136:139], a[156:159], v[140:143]\n\t"
"ds_read_b128 a[168:171], v224 offset:19104\n\t"
"v_xor_b32 v250, 64, v248\n\t"
"v_mfma_f32_16x16x32_bf16 v[140:143], a[140:143], a[172:175], v[140:143]\n\t"
"buffer_load_dwordx4 a[136:139], v252, s[84:87], s78 offen offset:0 sc1\n\t"
"v_mfma_f32_16x16x32_bf16 v[124:127], a[128:131], a[156:159], v[124:127]\n\t"
"v_mfma_f32_16x16x32_bf16 v[124:127], a[132:135], a[172:175], v[124:127]\n\t"
"buffer_load_dwordx4 a[140:143], v252, s[84:87], s78 offen offset:1024 sc1\n\t"
"v_mfma_f32_16x16x32_bf16 v[108:111], a[120:123], a[156:159], v[108:111]\n\t"
"ds_read_b128 a[152:155], v224 offset:17056\n\t"
"v_xor_b32 v251, 96, v248\n\t"
"v_mfma_f32_16x16x32_bf16 v[108:111], a[124:127], a[172:175], v[108:111]\n\t"
"buffer_load_dwordx4 a[132:135], v235, s[84:87], s78 offen offset:3072 sc1\n\t"
"v_mfma_f32_4x4x4_16b_bf16 v[200:203], a[220:221], a[178:179], 0\n\t"
"ds_read_b64_tr_b16 a[178:179], v248 offset:20864\n\t"
"v_mfma_f32_4x4x4_16b_bf16 v[204:207], a[220:221], a[186:187], 0\n\t"
"v_mfma_f32_4x4x4_16b_bf16 v[216:219], a[220:221], a[194:195], 0\n\t"
"ds_read_b128 a[172:175], v224 offset:19360\n\t"
"v_mfma_f32_4x4x4_16b_bf16 v[220:223], a[220:221], a[202:203], 0\n\t"
".p2align 3\n\t"
"buffer_load_dwordx4 a[128:131], v235, s[84:87], s78 offen offset:2048 sc1\n\t"
"v_pk_fma_f32 v[0:1], v[96:97], s[90:91], v[0:1] neg_lo:[1,0,0] neg_hi:[1,0,0]\n\t"
"v_pk_fma_f32 v[2:3], v[98:99], s[90:91], v[2:3] neg_lo:[1,0,0] neg_hi:[1,0,0]\n\t"
"v_pk_fma_f32 v[16:17], v[112:113], s[90:91], v[16:17] neg_lo:[1,0,0] neg_hi:[1,0,0]\n\t"
"v_pk_fma_f32 v[18:19], v[114:115], s[90:91], v[18:19] neg_lo:[1,0,0] neg_hi:[1,0,0]\n\t"
"v_pk_fma_f32 v[32:33], v[128:129], s[90:91], v[32:33] neg_lo:[1,0,0] neg_hi:[1,0,0]\n\t"
"v_pk_fma_f32 v[34:35], v[130:131], s[90:91], v[34:35] neg_lo:[1,0,0] neg_hi:[1,0,0]\n\t"
"v_pk_fma_f32 v[36:37], v[132:133], s[90:91], v[36:37] neg_lo:[1,0,0] neg_hi:[1,0,0]\n\t"
"v_pk_fma_f32 v[38:39], v[134:135], s[90:91], v[38:39] neg_lo:[1,0,0] neg_hi:[1,0,0]\n\t"
"v_pk_fma_f32 v[20:21], v[116:117], s[90:91], v[20:21] neg_lo:[1,0,0] neg_hi:[1,0,0]\n\t"
"v_pk_fma_f32 v[22:23], v[118:119], s[90:91], v[22:23] neg_lo:[1,0,0] neg_hi:[1,0,0]\n\t"
"v_pk_fma_f32 v[4:5], v[100:101], s[90:91], v[4:5] neg_lo:[1,0,0] neg_hi:[1,0,0]\n\t"
"v_pk_fma_f32 v[6:7], v[102:103], s[90:91], v[6:7] neg_lo:[1,0,0] neg_hi:[1,0,0]\n\t"
"buffer_load_dwordx4 a[120:123], v235, s[84:87], s78 offen offset:0 sc1\n\t"
"v_pk_fma_f32 v[8:9], v[104:105], s[90:91], v[8:9] neg_lo:[1,0,0] neg_hi:[1,0,0]\n\t"
"v_pk_fma_f32 v[10:11], v[106:107], s[90:91], v[10:11] neg_lo:[1,0,0] neg_hi:[1,0,0]\n\t"
"v_pk_fma_f32 v[24:25], v[120:121], s[90:91], v[24:25] neg_lo:[1,0,0] neg_hi:[1,0,0]\n\t"
"v_pk_fma_f32 v[26:27], v[122:123], s[90:91], v[26:27] neg_lo:[1,0,0] neg_hi:[1,0,0]\n\t"
"v_pk_fma_f32 v[40:41], v[136:137], s[90:91], v[40:41] neg_lo:[1,0,0] neg_hi:[1,0,0]\n\t"
"v_pk_fma_f32 v[42:43], v[138:139], s[90:91], v[42:43] neg_lo:[1,0,0] neg_hi:[1,0,0]\n\t"
"v_pk_fma_f32 v[44:45], v[140:141], s[90:91], v[44:45] neg_lo:[1,0,0] neg_hi:[1,0,0]\n\t"
"v_pk_fma_f32 v[46:47], v[142:143], s[90:91], v[46:47] neg_lo:[1,0,0] neg_hi:[1,0,0]\n\t"
"v_pk_fma_f32 v[28:29], v[124:125], s[90:91], v[28:29] neg_lo:[1,0,0] neg_hi:[1,0,0]\n\t"
"v_pk_fma_f32 v[30:31], v[126:127], s[90:91], v[30:31] neg_lo:[1,0,0] neg_hi:[1,0,0]\n\t"
"v_pk_fma_f32 v[12:13], v[108:109], s[90:91], v[12:13] neg_lo:[1,0,0] neg_hi:[1,0,0]\n\t"
"v_pk_fma_f32 v[14:15], v[110:111], s[90:91], v[14:15] neg_lo:[1,0,0] neg_hi:[1,0,0]\n\t"
"v_mfma_f32_4x4x4_16b_bf16 v[192:195], a[222:223], a[176:177], v[200:203]\n\t"
"v_mfma_f32_4x4x4_16b_bf16 v[196:199], a[222:223], a[184:185], v[204:207]\n\t"
"ds_read_b64_tr_b16 a[186:187], v249 offset:20864\n\t"
"v_mfma_f32_4x4x4_16b_bf16 v[200:203], a[222:223], a[180:181], v[200:203]\n\t"
"ds_read_b64_tr_b16 a[194:195], v250 offset:20864\n\t"
"v_mfma_f32_4x4x4_16b_bf16 v[204:207], a[222:223], a[188:189], v[204:207]\n\t"
"ds_read_b64_tr_b16 a[202:203], v251 offset:20864\n\t"
"v_mfma_f32_4x4x4_16b_bf16 v[208:211], a[222:223], a[192:193], v[216:219]\n\t"
"v_mfma_f32_4x4x4_16b_bf16 v[212:215], a[222:223], a[200:201], v[220:223]\n\t"
"ds_read_b64_tr_b16 a[176:177], v248 offset:20736\n\t"
"v_mfma_f32_4x4x4_16b_bf16 v[216:219], a[222:223], a[196:197], v[216:219]\n\t"
"ds_read_b64_tr_b16 a[184:185], v249 offset:20736\n\t"
"v_mfma_f32_4x4x4_16b_bf16 v[220:223], a[222:223], a[204:205], v[220:223]\n\t"
"s_waitcnt vmcnt(17) lgkmcnt(6)\n\t"
"buffer_load_dwordx4 a[124:127], v235, s[84:87], s78 offen offset:1024 sc1\n\t"
"v_mfma_f32_16x16x32_bf16 v[48:51], a[48:51], a[144:147], v[48:51]\n\t"
"ds_read_b64_tr_b16 a[180:181], v249 offset:20992\n\t"
"v_cvt_pk_bf16_f32 v192, -v192, -v196\n\t"
"v_mfma_f32_16x16x32_bf16 v[48:51], a[52:55], a[160:163], v[48:51]\n\t"
"ds_read_b128 a[156:159], v224 offset:17312\n\t"
"v_mfma_f32_16x16x32_bf16 v[64:67], a[56:59], a[144:147], v[64:67]\n\t"
"ds_read_b64_tr_b16 a[188:189], v248 offset:20992\n\t"
"v_cvt_pk_bf16_f32 v194, -v194, -v198\n\t"
"v_mfma_f32_16x16x32_bf16 v[64:67], a[60:63], a[160:163], v[64:67]\n\t"
"v_cvt_pk_bf16_f32 v193, -v193, -v197\n\t"
"v_mfma_f32_16x16x32_bf16 v[80:83], a[64:67], a[144:147], v[80:83]\n\t"
"ds_write_addtid_b32 v192 offset:0\n\t"
"v_cvt_pk_bf16_f32 v200, v200, v204\n\t"
"v_mfma_f32_16x16x32_bf16 v[80:83], a[68:71], a[160:163], v[80:83]\n\t"
"ds_read_b128 a[160:163], v224 offset:19616\n\t"
"v_mfma_f32_16x16x32_bf16 v[84:87], a[64:67], a[148:151], v[84:87]\n\t"
"ds_read_b128 a[144:147], v224 offset:17568\n\t"
"v_cvt_pk_bf16_f32 v216, v216, v220\n\t"
"v_mfma_f32_16x16x32_bf16 v[84:87], a[68:71], a[164:167], v[84:87]\n\t"
"ds_write_addtid_b32 v193 offset:4128\n\t"
"v_cvt_pk_bf16_f32 v201, v201, v205\n\t"
"v_mfma_f32_16x16x32_bf16 v[68:71], a[56:59], a[148:151], v[68:71]\n\t"
"v_cvt_pk_bf16_f32 v202, v202, v206\n\t"
"v_cvt_pk_bf16_f32 v203, v203, v207\n\t"
"v_mfma_f32_16x16x32_bf16 v[68:71], a[60:63], a[164:167], v[68:71]\n\t"
"ds_write_addtid_b32 v203 offset:13472\n\t"
"v_cvt_pk_bf16_f32 v209, -v209, -v213\n\t"
"v_mfma_f32_16x16x32_bf16 v[52:55], a[48:51], a[148:151], v[52:55]\n\t"
"ds_write_addtid_b32 v200 offset:8320\n\t"
"v_cvt_pk_bf16_f32 v218, v218, v222\n\t"
"v_mfma_f32_16x16x32_bf16 v[52:55], a[52:55], a[164:167], v[52:55]\n\t"
"ds_write_addtid_b32 v201 offset:12448\n\t"
"v_mfma_f32_16x16x32_bf16 v[56:59], a[48:51], a[152:155], v[56:59]\n\t"
"ds_read_b128 a[148:151], v224 offset:17824\n\t"
"v_cvt_pk_bf16_f32 v211, -v211, -v215\n\t"
"v_mfma_f32_16x16x32_bf16 v[56:59], a[52:55], a[168:171], v[56:59]\n\t"
"ds_read_b64_tr_b16 a[192:193], v250 offset:20736\n\t"
"v_cvt_pk_bf16_f32 v219, v219, v223\n\t"
"v_mfma_f32_16x16x32_bf16 v[72:75], a[56:59], a[152:155], v[72:75]\n\t"
"ds_read_b128 a[164:167], v224 offset:19872\n\t"
"v_mfma_f32_16x16x32_bf16 v[72:75], a[60:63], a[168:171], v[72:75]\n\t"
"ds_write_addtid_b32 v218 offset:11392\n\t"
"v_mfma_f32_16x16x32_bf16 v[88:91], a[64:67], a[152:155], v[88:91]\n\t"
"ds_write_addtid_b32 v202 offset:9344\n\t"
"v_mfma_f32_16x16x32_bf16 v[88:91], a[68:71], a[168:171], v[88:91]\n\t"
"s_waitcnt lgkmcnt(8)\n\t"
"ds_write_addtid_b32 v219 offset:15520\n\t"
"v_cvt_pk_bf16_f32 v195, -v195, -v199\n\t"
"v_mfma_f32_16x16x32_bf16 v[92:95], a[64:67], a[156:159], v[92:95]\n\t"
"ds_write_addtid_b32 v216 offset:10368\n\t"
"v_cvt_pk_bf16_f32 v208, -v208, -v212\n\t"
"v_mfma_f32_16x16x32_bf16 v[92:95], a[68:71], a[172:175], v[92:95]\n\t"
"ds_write_addtid_b32 v211 offset:7200\n\t"
"v_cvt_pk_bf16_f32 v210, -v210, -v214\n\t"
"v_mfma_f32_16x16x32_bf16 v[76:79], a[56:59], a[156:159], v[76:79]\n\t"
"ds_write_addtid_b32 v209 offset:6176\n\t"
"v_cvt_pk_bf16_f32 v217, v217, v221\n\t"
"v_mfma_f32_16x16x32_bf16 v[76:79], a[60:63], a[172:175], v[76:79]\n\t"
"ds_read_b128 a[152:155], v224 offset:18080\n\t"
"v_mfma_f32_16x16x32_bf16 v[60:63], a[48:51], a[156:159], v[60:63]\n\t"
"ds_write_addtid_b32 v208 offset:2048\n\t"
"v_mfma_f32_16x16x32_bf16 v[60:63], a[52:55], a[172:175], v[60:63]\n\t"
"s_waitcnt vmcnt(12) lgkmcnt(9)\n\t"
"ds_write_addtid_b32 v210 offset:3072\n\t"
"v_mfma_f32_16x16x32_bf16 v[144:147], a[72:75], a[144:147], v[144:147]\n\t"
"ds_write_addtid_b32 v217 offset:14496\n\t"
"v_mfma_f32_16x16x32_bf16 v[144:147], a[76:79], a[160:163], v[144:147]\n\t"
"ds_write_addtid_b32 v195 offset:5152\n\t"
"v_mfma_f32_16x16x32_bf16 v[160:163], a[80:83], a[144:147], v[160:163]\n\t"
"ds_write_addtid_b32 v194 offset:1024\n\t"
"v_mfma_f32_16x16x32_bf16 v[160:163], a[84:87], a[160:163], v[160:163]\n\t"
"ds_read_b128 a[156:159], v224 offset:18336\n\t"
"v_mfma_f32_16x16x32_bf16 v[176:179], a[88:91], a[144:147], v[176:179]\n\t"
"ds_read_b128 a[168:171], v224 offset:20128\n\t"
"v_mfma_f32_16x16x32_bf16 v[176:179], a[92:95], a[160:163], v[176:179]\n\t"
"s_waitcnt lgkmcnt(9)\n\t"
"ds_read_b128 a[172:175], v224 offset:20384\n\t"
"v_mfma_f32_16x16x32_bf16 v[180:183], a[88:91], a[148:151], v[180:183]\n\t"
"ds_read_b64_tr_b16 a[200:201], v251 offset:20736\n\t"
"v_mfma_f32_16x16x32_bf16 v[180:183], a[92:95], a[164:167], v[180:183]\n\t"
"ds_read_b64_tr_b16 a[196:197], v251 offset:20992\n\t"
"v_mfma_f32_16x16x32_bf16 v[164:167], a[80:83], a[148:151], v[164:167]\n\t"
"ds_read_b64_tr_b16 a[204:205], v250 offset:20992\n\t"
"v_mfma_f32_16x16x32_bf16 v[164:167], a[84:87], a[164:167], v[164:167]\n\t"
"v_mfma_f32_16x16x32_bf16 v[148:151], a[72:75], a[148:151], v[148:151]\n\t"
"s_waitcnt lgkmcnt(3)\n\t"
"v_mfma_f32_16x16x32_bf16 v[148:151], a[76:79], a[164:167], v[148:151]\n\t"
"s_barrier\n\t"
"v_mfma_f32_16x16x32_bf16 v[152:155], a[72:75], a[152:155], v[152:155]\n\t"
"s_add_u32 m0, s26, 35584\n\t"
"ds_read_b128 a[144:147], v224 offset:0\n\t"
"ds_read_b128 a[148:151], v224 offset:256\n\t"
"v_mfma_f32_16x16x32_bf16 v[152:155], a[76:79], a[168:171], v[152:155]\n\t"
"buffer_load_dwordx4 v236, s[12:15], s22 offen offset:0 nt lds\n\t"
"v_mfma_f32_16x16x32_bf16 v[168:171], a[80:83], a[152:155], v[168:171]\n\t"
"ds_read_b128 a[160:163], v224 offset:2048\n\t"
"v_mfma_f32_16x16x32_bf16 v[168:171], a[84:87], a[168:171], v[168:171]\n\t"
"ds_read_b128 a[164:167], v224 offset:2304\n\t"
"v_mfma_f32_16x16x32_bf16 v[184:187], a[88:91], a[152:155], v[184:187]\n\t"
"v_mfma_f32_16x16x32_bf16 v[184:187], a[92:95], a[168:171], v[184:187]\n\t"
"buffer_load_dwordx4 v237, s[12:15], s22 offen offset:1024 nt lds\n\t"
"v_mfma_f32_16x16x32_bf16 v[188:191], a[88:91], a[156:159], v[188:191]\n\t"
"v_mfma_f32_16x16x32_bf16 v[188:191], a[92:95], a[172:175], v[188:191]\n\t"
"buffer_load_dwordx4 v238, s[12:15], s22 offen offset:2048 nt lds\n\t"
"v_mfma_f32_16x16x32_bf16 v[172:175], a[80:83], a[156:159], v[172:175]\n\t"
"v_mfma_f32_16x16x32_bf16 v[172:175], a[84:87], a[172:175], v[172:175]\n\t"
"buffer_load_dwordx4 v239, s[12:15], s22 offen offset:3072 nt lds\n\t"
"v_mfma_f32_16x16x32_bf16 v[156:159], a[72:75], a[156:159], v[156:159]\n\t"
"ds_read_b128 a[168:171], v224 offset:2560\n\t"
"v_mfma_f32_16x16x32_bf16 v[156:159], a[76:79], a[172:175], v[156:159]\n\t"
"s_waitcnt lgkmcnt(3)\n\t"
"s_mov_b64 exec, s[36:37]\n\t"
"buffer_load_dwordx4 v240, s[12:15], s22 offen offset:3584 nt lds\n\t"
"s_mov_b64 exec, -1\n\t"
"v_mfma_f32_4x4x4_16b_bf16 v[200:203], a[252:253], a[178:179], 0\n\t"
"s_lshl_b32 m0, s73, 8\n\t"
"v_mfma_f32_4x4x4_16b_bf16 v[204:207], a[252:253], a[186:187], 0\n\t"
"ds_read_b128 a[156:159], v224 offset:768\n\t"
"v_mfma_f32_4x4x4_16b_bf16 v[216:219], a[252:253], a[194:195], 0\n\t"
"v_mfma_f32_4x4x4_16b_bf16 v[220:223], a[252:253], a[202:203], 0\n\t"
"ds_read_b128 a[152:155], v224 offset:512\n\t"
"v_mfma_f32_4x4x4_16b_bf16 v[192:195], a[254:255], a[176:177], v[200:203]\n\t"
"ds_read_b64_tr_b16 a[178:179], v248 offset:128\n\t"
"v_mfma_f32_4x4x4_16b_bf16 v[196:199], a[254:255], a[184:185], v[204:207]\n\t"
"ds_read_b64_tr_b16 a[186:187], v249 offset:128\n\t"
"v_mfma_f32_4x4x4_16b_bf16 v[200:203], a[254:255], a[180:181], v[200:203]\n\t"
"ds_read_b64_tr_b16 a[194:195], v250 offset:128\n\t"
"v_mfma_f32_4x4x4_16b_bf16 v[204:207], a[254:255], a[188:189], v[204:207]\n\t"
"ds_read_b64_tr_b16 a[202:203], v251 offset:128\n\t"
"v_mfma_f32_4x4x4_16b_bf16 v[208:211], a[254:255], a[192:193], v[216:219]\n\t"
"ds_read_b128 a[172:175], v224 offset:2816\n\t"
"v_mfma_f32_4x4x4_16b_bf16 v[212:215], a[254:255], a[200:201], v[220:223]\n\t"
"ds_read_b64_tr_b16 a[176:177], v248 offset:0\n\t"
"v_mfma_f32_4x4x4_16b_bf16 v[216:219], a[254:255], a[196:197], v[216:219]\n\t"
"ds_read_b64_tr_b16 a[184:185], v249 offset:0\n\t"
"v_mfma_f32_4x4x4_16b_bf16 v[220:223], a[254:255], a[204:205], v[220:223]\n\t"
"s_waitcnt vmcnt(11) lgkmcnt(8)\n\t"
"ds_read_b64_tr_b16 a[180:181], v249 offset:256\n\t"
"v_mfma_f32_16x16x32_bf16 v[48:51], a[96:99], a[144:147], v[48:51]\n\t"
"ds_read_b64_tr_b16 a[188:189], v248 offset:256\n\t"
"v_mfma_f32_16x16x32_bf16 v[48:51], a[100:103], a[160:163], v[48:51]\n\t"
"ds_read_b64_tr_b16 a[192:193], v250 offset:0\n\t"
"v_mfma_f32_16x16x32_bf16 v[64:67], a[104:107], a[144:147], v[64:67]\n\t"
"ds_read_b64_tr_b16 a[200:201], v251 offset:0\n\t"
"v_cvt_pk_bf16_f32 v203, v203, v207\n\t"
"v_mfma_f32_16x16x32_bf16 v[64:67], a[108:111], a[160:163], v[64:67]\n\t"
"ds_read_b64_tr_b16 a[196:197], v251 offset:256\n\t"
"v_mfma_f32_16x16x32_bf16 v[80:83], a[112:115], a[144:147], v[80:83]\n\t"
"ds_read_b128 a[144:147], v224 offset:1024\n\t"
"v_mfma_f32_16x16x32_bf16 v[80:83], a[116:119], a[160:163], v[80:83]\n\t"
"ds_read_b128 a[160:163], v224 offset:3072\n\t"
"v_cvt_pk_bf16_f32 v210, -v210, -v214\n\t"
"v_mfma_f32_16x16x32_bf16 v[84:87], a[112:115], a[148:151], v[84:87]\n\t"
"ds_write_addtid_b32 v203 offset:30016\n\t"
"v_cvt_pk_bf16_f32 v218, v218, v222\n\t"
"v_mfma_f32_16x16x32_bf16 v[84:87], a[116:119], a[164:167], v[84:87]\n\t"
"ds_write_addtid_b32 v218 offset:27936\n\t"
"v_cvt_pk_bf16_f32 v208, -v208, -v212\n\t"
"v_mfma_f32_16x16x32_bf16 v[68:71], a[104:107], a[148:151], v[68:71]\n\t"
"ds_write_addtid_b32 v208 offset:18592\n\t"
"v_cvt_pk_bf16_f32 v211, -v211, -v215\n\t"
"v_mfma_f32_16x16x32_bf16 v[68:71], a[108:111], a[164:167], v[68:71]\n\t"
"ds_write_addtid_b32 v211 offset:23744\n\t"
"v_cvt_pk_bf16_f32 v219, v219, v223\n\t"
"v_mfma_f32_16x16x32_bf16 v[52:55], a[96:99], a[148:151], v[52:55]\n\t"
"ds_write_addtid_b32 v219 offset:32064\n\t"
"v_cvt_pk_bf16_f32 v209, -v209, -v213\n\t"
"v_mfma_f32_16x16x32_bf16 v[52:55], a[100:103], a[164:167], v[52:55]\n\t"
"s_waitcnt lgkmcnt(9)\n\t"
"ds_read_b128 a[148:151], v224 offset:1280\n\t"
"v_cvt_pk_bf16_f32 v202, v202, v206\n\t"
"v_mfma_f32_16x16x32_bf16 v[56:59], a[96:99], a[152:155], v[56:59]\n\t"
"ds_read_b64_tr_b16 a[204:205], v250 offset:256\n\t"
"v_cvt_pk_bf16_f32 v200, v200, v204\n\t"
"v_mfma_f32_16x16x32_bf16 v[56:59], a[100:103], a[168:171], v[56:59]\n\t"
"ds_read_b128 a[164:167], v224 offset:3328\n\t"
"v_cvt_pk_bf16_f32 v216, v216, v220\n\t"
"v_mfma_f32_16x16x32_bf16 v[72:75], a[104:107], a[152:155], v[72:75]\n\t"
"v_cvt_pk_bf16_f32 v192, -v192, -v196\n\t"
"v_mfma_f32_16x16x32_bf16 v[72:75], a[108:111], a[168:171], v[72:75]\n\t"
"ds_write_addtid_b32 v216 offset:26912\n\t"
"v_cvt_pk_bf16_f32 v193, -v193, -v197\n\t"
"v_mfma_f32_16x16x32_bf16 v[88:91], a[112:115], a[152:155], v[88:91]\n\t"
"ds_write_addtid_b32 v193 offset:20672\n\t"
"v_cvt_pk_bf16_f32 v194, -v194, -v198\n\t"
"v_mfma_f32_16x16x32_bf16 v[88:91], a[116:119], a[168:171], v[88:91]\n\t"
"ds_write_addtid_b32 v210 offset:19616\n\t"
"v_cvt_pk_bf16_f32 v201, v201, v205\n\t"
"v_mfma_f32_16x16x32_bf16 v[92:95], a[112:115], a[156:159], v[92:95]\n\t"
"ds_write_addtid_b32 v200 offset:24864\n\t"
"v_mfma_f32_16x16x32_bf16 v[92:95], a[116:119], a[172:175], v[92:95]\n\t"
"ds_write_addtid_b32 v192 offset:16544\n\t"
"v_cvt_pk_bf16_f32 v217, v217, v221\n\t"
"v_mfma_f32_16x16x32_bf16 v[76:79], a[104:107], a[156:159], v[76:79]\n\t"
"ds_write_addtid_b32 v217 offset:31040\n\t"
"v_mfma_f32_16x16x32_bf16 v[76:79], a[108:111], a[172:175], v[76:79]\n\t"
"ds_write_addtid_b32 v194 offset:17568\n\t"
"v_mfma_f32_16x16x32_bf16 v[60:63], a[96:99], a[156:159], v[60:63]\n\t"
"ds_write_addtid_b32 v202 offset:25888\n\t"
"v_cvt_pk_bf16_f32 v195, -v195, -v199\n\t"
"v_mfma_f32_16x16x32_bf16 v[60:63], a[100:103], a[172:175], v[60:63]\n\t"
"s_waitcnt vmcnt(5) lgkmcnt(8)\n\t"
"ds_write_addtid_b32 v209 offset:22720\n\t"
"v_mfma_f32_16x16x32_bf16 v[144:147], a[120:123], a[144:147], v[144:147]\n\t"
"ds_write_addtid_b32 v201 offset:28992\n\t"
"v_mfma_f32_16x16x32_bf16 v[144:147], a[124:127], a[160:163], v[144:147]\n\t"
"ds_write_addtid_b32 v195 offset:21696\n\t"
"v_mfma_f32_16x16x32_bf16 v[160:163], a[128:131], a[144:147], v[160:163]\n\t"
"ds_read_b128 a[168:171], v224 offset:3584\n\t"
"v_mfma_f32_16x16x32_bf16 v[160:163], a[132:135], a[160:163], v[160:163]\n\t"
"ds_read_b128 a[152:155], v224 offset:1536\n\t"
"v_mfma_f32_16x16x32_bf16 v[176:179], a[136:139], a[144:147], v[176:179]\n\t"
"ds_read_b128 a[172:175], v224 offset:3840\n\t"
"v_mfma_f32_16x16x32_bf16 v[176:179], a[140:143], a[160:163], v[176:179]\n\t"
"ds_read_b128 a[156:159], v224 offset:1792\n\t"
"v_mfma_f32_16x16x32_bf16 v[180:183], a[136:139], a[148:151], v[180:183]\n\t"
"v_mfma_f32_16x16x32_bf16 v[180:183], a[140:143], a[164:167], v[180:183]\n\t"
"v_mfma_f32_16x16x32_bf16 v[164:167], a[128:131], a[148:151], v[164:167]\n\t"
"v_mfma_f32_16x16x32_bf16 v[164:167], a[132:135], a[164:167], v[164:167]\n\t"
"v_mfma_f32_16x16x32_bf16 v[148:151], a[120:123], a[148:151], v[148:151]\n\t"
"s_waitcnt lgkmcnt(0)\n\t"
"v_mfma_f32_16x16x32_bf16 v[148:151], a[124:127], a[164:167], v[148:151]\n\t"
"s_barrier\n\t"
"v_mfma_f32_16x16x32_bf16 v[152:155], a[120:123], a[152:155], v[152:155]\n\t"
"ds_read_b128 a[160:163], v224 offset:18592\n\t"
"v_mfma_f32_16x16x32_bf16 v[152:155], a[124:127], a[168:171], v[152:155]\n\t"
"ds_read_b128 a[144:147], v224 offset:16544\n\t"
"v_mfma_f32_16x16x32_bf16 v[168:171], a[128:131], a[152:155], v[168:171]\n\t"
"ds_read_b128 a[148:151], v224 offset:16800\n\t"
"v_mfma_f32_16x16x32_bf16 v[168:171], a[132:135], a[168:171], v[168:171]\n\t"
"ds_read_b128 a[164:167], v224 offset:18848\n\t"
"v_mfma_f32_16x16x32_bf16 v[184:187], a[136:139], a[152:155], v[184:187]\n\t"
"v_mfma_f32_16x16x32_bf16 v[184:187], a[140:143], a[168:171], v[184:187]\n\t"
"ds_read_b128 a[168:171], v224 offset:19104\n\t"
"v_mfma_f32_16x16x32_bf16 v[188:191], a[136:139], a[156:159], v[188:191]\n\t"
"v_mfma_f32_16x16x32_bf16 v[188:191], a[140:143], a[172:175], v[188:191]\n\t"
"ds_read_b128 a[152:155], v224 offset:17056\n\t"
"v_mfma_f32_16x16x32_bf16 v[172:175], a[128:131], a[156:159], v[172:175]\n\t"
"v_mfma_f32_16x16x32_bf16 v[172:175], a[132:135], a[172:175], v[172:175]\n\t"
"v_mfma_f32_16x16x32_bf16 v[156:159], a[120:123], a[156:159], v[156:159]\n\t"
"ds_read_b128 a[156:159], v224 offset:17312\n\t"
"v_mfma_f32_16x16x32_bf16 v[156:159], a[124:127], a[172:175], v[156:159]\n\t"
"ds_read_b128 a[172:175], v224 offset:19360\n\t"
"v_mfma_f32_4x4x4_16b_bf16 v[200:203], a[252:253], a[178:179], 0\n\t"
"ds_read_b64_tr_b16 a[178:179], v248 offset:41600\n\t"
"v_mfma_f32_4x4x4_16b_bf16 v[204:207], a[252:253], a[186:187], 0\n\t"
"ds_read_b64_tr_b16 a[186:187], v249 offset:41600\n\t"
"v_mfma_f32_4x4x4_16b_bf16 v[216:219], a[252:253], a[194:195], 0\n\t"
"ds_read_b64_tr_b16 a[194:195], v250 offset:41600\n\t"
"v_mfma_f32_4x4x4_16b_bf16 v[220:223], a[252:253], a[202:203], 0\n\t"
".p2align 3\n\t"
"ds_read_b64_tr_b16 a[202:203], v251 offset:41600\n\t"
"v_pk_fma_f32 v[48:49], v[144:145], s[90:91], v[48:49]\n\t"
"v_pk_fma_f32 v[50:51], v[146:147], s[90:91], v[50:51]\n\t"
"v_pk_add_f32 v[144:145], v[48:49], v[144:145] neg_lo:[0,1] neg_hi:[0,1]\n\t"
"v_pk_add_f32 v[146:147], v[50:51], v[146:147] neg_lo:[0,1] neg_hi:[0,1]\n\t"
"v_pk_fma_f32 v[64:65], v[160:161], s[90:91], v[64:65]\n\t"
"v_pk_fma_f32 v[66:67], v[162:163], s[90:91], v[66:67]\n\t"
"v_pk_add_f32 v[160:161], v[64:65], v[160:161] neg_lo:[0,1] neg_hi:[0,1]\n\t"
"v_pk_add_f32 v[162:163], v[66:67], v[162:163] neg_lo:[0,1] neg_hi:[0,1]\n\t"
"v_pk_fma_f32 v[80:81], v[176:177], s[90:91], v[80:81]\n\t"
"v_pk_fma_f32 v[82:83], v[178:179], s[90:91], v[82:83]\n\t"
"v_pk_add_f32 v[176:177], v[80:81], v[176:177] neg_lo:[0,1] neg_hi:[0,1]\n\t"
"v_pk_add_f32 v[178:179], v[82:83], v[178:179] neg_lo:[0,1] neg_hi:[0,1]\n\t"
"v_pk_fma_f32 v[84:85], v[180:181], s[90:91], v[84:85]\n\t"
"v_pk_fma_f32 v[86:87], v[182:183], s[90:91], v[86:87]\n\t"
"v_pk_add_f32 v[180:181], v[84:85], v[180:181] neg_lo:[0,1] neg_hi:[0,1]\n\t"
"v_pk_add_f32 v[182:183], v[86:87], v[182:183] neg_lo:[0,1] neg_hi:[0,1]\n\t"
"v_pk_fma_f32 v[68:69], v[164:165], s[90:91], v[68:69]\n\t"
"v_pk_fma_f32 v[70:71], v[166:167], s[90:91], v[70:71]\n\t"
"v_pk_add_f32 v[164:165], v[68:69], v[164:165] neg_lo:[0,1] neg_hi:[0,1]\n\t"
"v_pk_add_f32 v[166:167], v[70:71], v[166:167] neg_lo:[0,1] neg_hi:[0,1]\n\t"
"v_pk_fma_f32 v[52:53], v[148:149], s[90:91], v[52:53]\n\t"
"v_pk_fma_f32 v[54:55], v[150:151], s[90:91], v[54:55]\n\t"
"v_pk_add_f32 v[148:149], v[52:53], v[148:149] neg_lo:[0,1] neg_hi:[0,1]\n\t"
"v_pk_add_f32 v[150:151], v[54:55], v[150:151] neg_lo:[0,1] neg_hi:[0,1]\n\t"
"v_pk_fma_f32 v[56:57], v[152:153], s[90:91], v[56:57]\n\t"
"v_pk_fma_f32 v[58:59], v[154:155], s[90:91], v[58:59]\n\t"
"v_pk_add_f32 v[152:153], v[56:57], v[152:153] neg_lo:[0,1] neg_hi:[0,1]\n\t"
"v_pk_add_f32 v[154:155], v[58:59], v[154:155] neg_lo:[0,1] neg_hi:[0,1]\n\t"
"v_pk_fma_f32 v[72:73], v[168:169], s[90:91], v[72:73]\n\t"
"v_pk_fma_f32 v[74:75], v[170:171], s[90:91], v[74:75]\n\t"
"v_pk_add_f32 v[168:169], v[72:73], v[168:169] neg_lo:[0,1] neg_hi:[0,1]\n\t"
"v_pk_add_f32 v[170:171], v[74:75], v[170:171] neg_lo:[0,1] neg_hi:[0,1]\n\t"
"v_pk_fma_f32 v[88:89], v[184:185], s[90:91], v[88:89]\n\t"
"v_pk_fma_f32 v[90:91], v[186:187], s[90:91], v[90:91]\n\t"
"v_pk_add_f32 v[184:185], v[88:89], v[184:185] neg_lo:[0,1] neg_hi:[0,1]\n\t"
"v_pk_add_f32 v[186:187], v[90:91], v[186:187] neg_lo:[0,1] neg_hi:[0,1]\n\t"
"v_pk_fma_f32 v[92:93], v[188:189], s[90:91], v[92:93]\n\t"
"v_pk_fma_f32 v[94:95], v[190:191], s[90:91], v[94:95]\n\t"
"v_pk_add_f32 v[188:189], v[92:93], v[188:189] neg_lo:[0,1] neg_hi:[0,1]\n\t"
"v_pk_add_f32 v[190:191], v[94:95], v[190:191] neg_lo:[0,1] neg_hi:[0,1]\n\t"
"v_pk_fma_f32 v[76:77], v[172:173], s[90:91], v[76:77]\n\t"
"v_pk_fma_f32 v[78:79], v[174:175], s[90:91], v[78:79]\n\t"
"v_pk_add_f32 v[172:173], v[76:77], v[172:173] neg_lo:[0,1] neg_hi:[0,1]\n\t"
"v_pk_add_f32 v[174:175], v[78:79], v[174:175] neg_lo:[0,1] neg_hi:[0,1]\n\t"
"v_pk_fma_f32 v[60:61], v[156:157], s[90:91], v[60:61]\n\t"
"v_pk_fma_f32 v[62:63], v[158:159], s[90:91], v[62:63]\n\t"
"v_pk_add_f32 v[156:157], v[60:61], v[156:157] neg_lo:[0,1] neg_hi:[0,1]\n\t"
"v_pk_add_f32 v[158:159], v[62:63], v[158:159] neg_lo:[0,1] neg_hi:[0,1]\n\t"
"v_mfma_f32_4x4x4_16b_bf16 v[192:195], a[254:255], a[176:177], v[200:203]\n\t"
"v_cvt_pk_bf16_f32 v52, v52, v53\n\t"
"v_cvt_pk_bf16_f32 v68, v68, v69\n\t"
"v_cvt_pk_bf16_f32 v69, v70, v71\n\t"
"v_mfma_f32_4x4x4_16b_bf16 v[196:199], a[254:255], a[184:185], v[204:207]\n\t"
"v_cvt_pk_bf16_f32 v48, v48, v49\n\t"
"v_cvt_pk_bf16_f32 v53, v54, v55\n\t"
"v_mfma_f32_4x4x4_16b_bf16 v[200:203], a[254:255], a[180:181], v[200:203]\n\t"
"v_cvt_pk_bf16_f32 v49, v50, v51\n\t"
"v_mfma_f32_4x4x4_16b_bf16 v[204:207], a[254:255], a[188:189], v[204:207]\n\t"
"v_cvt_pk_bf16_f32 v64, v64, v65\n\t"
"v_mfma_f32_4x4x4_16b_bf16 v[208:211], a[254:255], a[192:193], v[216:219]\n\t"
"ds_write2st64_b64 v228, v[48:49], v[52:53] offset0:0 offset1:13\n\t"
"v_cvt_pk_bf16_f32 v76, v76, v77\n\t"
"v_mfma_f32_4x4x4_16b_bf16 v[212:215], a[254:255], a[200:201], v[220:223]\n\t"
"v_cvt_pk_bf16_f32 v84, v84, v85\n\t"
"v_mfma_f32_4x4x4_16b_bf16 v[216:219], a[254:255], a[196:197], v[216:219]\n\t"
"v_cvt_pk_bf16_f32 v65, v66, v67\n\t"
"v_mfma_f32_4x4x4_16b_bf16 v[220:223], a[254:255], a[204:205], v[220:223]\n\t"
"s_waitcnt lgkmcnt(3)\n\t"
"ds_write2st64_b64 v246, v[64:65], v[68:69] offset0:0 offset1:13\n\t"
"v_mfma_f32_16x16x32_bf16 v[0:3], a[0:3], a[144:147], v[0:3]\n\t"
"v_cvt_pk_bf16_f32 v80, v80, v81\n\t"
"v_cvt_pk_bf16_f32 v81, v82, v83\n\t"
"v_mfma_f32_16x16x32_bf16 v[0:3], a[4:7], a[160:163], v[0:3]\n\t"
"v_cvt_pk_bf16_f32 v85, v86, v87\n\t"
"v_cvt_pk_bf16_f32 v72, v72, v73\n\t"
"v_mfma_f32_16x16x32_bf16 v[16:19], a[8:11], a[144:147], v[16:19]\n\t"
"v_cvt_pk_bf16_f32 v60, v60, v61\n\t"
"v_cvt_pk_bf16_f32 v92, v92, v93\n\t"
"v_mfma_f32_16x16x32_bf16 v[16:19], a[12:15], a[160:163], v[16:19]\n\t"
"ds_write2st64_b64 v247, v[80:81], v[84:85] offset0:0 offset1:13\n\t"
"v_mfma_f32_16x16x32_bf16 v[32:35], a[16:19], a[144:147], v[32:35]\n\t"
"v_cvt_pk_bf16_f32 v209, -v209, -v213\n\t"
"v_cvt_pk_bf16_f32 v56, v56, v57\n\t"
"v_mfma_f32_16x16x32_bf16 v[32:35], a[20:23], a[160:163], v[32:35]\n\t"
"v_cvt_pk_bf16_f32 v208, -v208, -v212\n\t"
"v_cvt_pk_bf16_f32 v73, v74, v75\n\t"
"v_mfma_f32_16x16x32_bf16 v[36:39], a[16:19], a[148:151], v[36:39]\n\t"
"v_cvt_pk_bf16_f32 v210, -v210, -v214\n\t"
"v_cvt_pk_bf16_f32 v77, v78, v79\n\t"
"v_mfma_f32_16x16x32_bf16 v[36:39], a[20:23], a[164:167], v[36:39]\n\t"
"s_nop 0\n\t"
"ds_read_b128 a[144:147], v224 offset:17568\n\t"
"ds_read_b128 a[160:163], v224 offset:19616\n\t"
"ds_read_b64_tr_b16 a[176:177], v248 offset:41472\n\t"
"ds_write2st64_b64 v246, v[72:73], v[76:77] offset0:26 offset1:39\n\t"
"v_mfma_f32_16x16x32_bf16 v[20:23], a[8:11], a[148:151], v[20:23]\n\t"
"v_cvt_pk_bf16_f32 v203, v203, v207\n\t"
"v_cvt_pk_bf16_f32 v93, v94, v95\n\t"
"v_mfma_f32_16x16x32_bf16 v[20:23], a[12:15], a[164:167], v[20:23]\n\t"
"v_cvt_pk_bf16_f32 v201, v201, v205\n\t"
"v_cvt_pk_bf16_f32 v211, -v211, -v215\n\t"
"v_mfma_f32_16x16x32_bf16 v[4:7], a[0:3], a[148:151], v[4:7]\n\t"
"v_cvt_pk_bf16_f32 v219, v219, v223\n\t"
"v_cvt_pk_bf16_f32 v57, v58, v59\n\t"
"v_mfma_f32_16x16x32_bf16 v[4:7], a[4:7], a[164:167], v[4:7]\n\t"
"ds_read_b128 a[164:167], v224 offset:19872\n\t"
"v_cvt_pk_bf16_f32 v61, v62, v63\n\t"
"v_mfma_f32_16x16x32_bf16 v[8:11], a[0:3], a[152:155], v[8:11]\n\t"
"v_cvt_pk_bf16_f32 v194, -v194, -v198\n\t"
"v_cvt_pk_bf16_f32 v202, v202, v206\n\t"
"v_mfma_f32_16x16x32_bf16 v[8:11], a[4:7], a[168:171], v[8:11]\n\t"
"ds_read_b128 a[148:151], v224 offset:17824\n\t"
"ds_write2st64_b64 v228, v[56:57], v[60:61] offset0:26 offset1:39\n\t"
"v_mfma_f32_16x16x32_bf16 v[24:27], a[8:11], a[152:155], v[24:27]\n\t"
"v_cvt_pk_bf16_f32 v192, -v192, -v196\n\t"
"v_cvt_pk_bf16_f32 v195, -v195, -v199\n\t"
"v_mfma_f32_16x16x32_bf16 v[24:27], a[12:15], a[168:171], v[24:27]\n\t"
"v_cvt_pk_bf16_f32 v216, v216, v220\n\t"
"v_cvt_pk_bf16_f32 v88, v88, v89\n\t"
"v_mfma_f32_16x16x32_bf16 v[40:43], a[16:19], a[152:155], v[40:43]\n\t"
"v_cvt_pk_bf16_f32 v217, v217, v221\n\t"
"v_cvt_pk_bf16_f32 v89, v90, v91\n\t"
"v_mfma_f32_16x16x32_bf16 v[40:43], a[20:23], a[168:171], v[40:43]\n\t"
"ds_write_addtid_b32 v195 offset:5152\n\t"
"ds_write2st64_b64 v247, v[88:89], v[92:93] offset0:26 offset1:39\n\t"
"v_mfma_f32_16x16x32_bf16 v[44:47], a[16:19], a[156:159], v[44:47]\n\t"
"buffer_load_dwordx4 a[16:19], v235, s[8:11], s59 offen offset:0 sc1\n\t"
"v_mfma_f32_16x16x32_bf16 v[44:47], a[20:23], a[172:175], v[44:47]\n\t"
"v_cvt_pk_bf16_f32 v193, -v193, -v197\n\t"
"v_cvt_pk_bf16_f32 v200, v200, v204\n\t"
"v_mfma_f32_16x16x32_bf16 v[28:31], a[8:11], a[156:159], v[28:31]\n\t"
"buffer_load_dwordx4 a[20:23], v235, s[8:11], s59 offen offset:1024 sc1\n\t"
"v_mfma_f32_16x16x32_bf16 v[28:31], a[12:15], a[172:175], v[28:31]\n\t"
"s_nop 0\n\t"
"ds_read_b128 a[168:171], v224 offset:20128\n\t"
"ds_write_addtid_b32 v192 offset:0\n\t"
"ds_write_addtid_b32 v193 offset:4128\n\t"
"ds_write_addtid_b32 v200 offset:8320\n\t"
"ds_write_addtid_b32 v201 offset:12448\n\t"
"ds_write_addtid_b32 v211 offset:7200\n\t"
"v_mfma_f32_16x16x32_bf16 v[12:15], a[0:3], a[156:159], v[12:15]\n\t"
"s_nop 0\n\t"
"ds_write_addtid_b32 v216 offset:10368\n\t"
"ds_read_b64_tr_b16 a[184:185], v249 offset:41472\n\t"
"v_mfma_f32_16x16x32_bf16 v[12:15], a[4:7], a[172:175], v[12:15]\n\t"
"s_waitcnt lgkmcnt(10)\n\t"
"ds_write_addtid_b32 v210 offset:3072\n\t"
"buffer_load_dwordx4 a[12:15], v252, s[8:11], s58 offen offset:3072 sc1\n\t"
"v_mfma_f32_16x16x32_bf16 v[96:99], a[24:27], a[144:147], v[96:99]\n\t"
"ds_write_addtid_b32 v208 offset:2048\n\t"
"v_cvt_pk_bf16_f32 v218, v218, v222\n\t"
"v_mfma_f32_16x16x32_bf16 v[96:99], a[28:31], a[160:163], v[96:99]\n\t"
"s_nop 0\n\t"
"ds_write_addtid_b32 v194 offset:1024\n\t"
"ds_write_addtid_b32 v203 offset:13472\n\t"
"ds_write_addtid_b32 v209 offset:6176\n\t"
"ds_write_addtid_b32 v217 offset:14496\n\t"
"ds_write_addtid_b32 v218 offset:11392\n\t"
"v_mfma_f32_16x16x32_bf16 v[112:115], a[32:35], a[144:147], v[112:115]\n\t"
"s_nop 0\n\t"
"ds_write_addtid_b32 v202 offset:9344\n\t"
"ds_write_addtid_b32 v219 offset:15520\n\t"
"v_mfma_f32_16x16x32_bf16 v[112:115], a[36:39], a[160:163], v[112:115]\n\t"
"ds_read_b128 a[172:175], v224 offset:20384\n\t"
"buffer_load_dwordx4 a[8:11], v252, s[8:11], s58 offen offset:2048 sc1\n\t"
"v_mfma_f32_16x16x32_bf16 v[128:131], a[40:43], a[144:147], v[128:131]\n\t"
"s_nop 0\n\t"
"ds_read_b128 a[152:155], v224 offset:18080\n\t"
"ds_read_b128 a[156:159], v224 offset:18336\n\t"
"v_mfma_f32_16x16x32_bf16 v[128:131], a[44:47], a[160:163], v[128:131]\n\t"
"buffer_load_dwordx4 a[0:3], v252, s[8:11], s58 offen offset:0 sc1\n\t"
"v_mfma_f32_16x16x32_bf16 v[132:135], a[40:43], a[148:151], v[132:135]\n\t"
"s_nop 0\n\t"
"ds_read_b64_tr_b16 a[180:181], v249 offset:41728\n\t"
"ds_read_b64_tr_b16 a[188:189], v248 offset:41728\n\t"
"v_mfma_f32_16x16x32_bf16 v[132:135], a[44:47], a[164:167], v[132:135]\n\t"
"s_nop 0\n\t"
"ds_read_b64_tr_b16 a[192:193], v250 offset:41472\n\t"
"ds_read_b64_tr_b16 a[200:201], v251 offset:41472\n\t"
"v_mfma_f32_16x16x32_bf16 v[116:119], a[32:35], a[148:151], v[116:119]\n\t"
"s_nop 0\n\t"
"ds_read_b64_tr_b16 a[196:197], v251 offset:41728\n\t"
"ds_read_b64_tr_b16 a[204:205], v250 offset:41728\n\t"
"v_mfma_f32_16x16x32_bf16 v[116:119], a[36:39], a[164:167], v[116:119]\n\t"
"buffer_load_dwordx4 a[4:7], v252, s[8:11], s58 offen offset:1024 sc1\n\t"
"v_mfma_f32_16x16x32_bf16 v[100:103], a[24:27], a[148:151], v[100:103]\n\t"
"s_waitcnt lgkmcnt(6)\n\t"
"v_mfma_f32_16x16x32_bf16 v[100:103], a[28:31], a[164:167], v[100:103]\n\t"
"s_barrier\n\t"
"v_mfma_f32_16x16x32_bf16 v[104:107], a[24:27], a[152:155], v[104:107]\n\t"
"ds_read_b128 a[144:147], v224 offset:0\n\t"
"v_cvt_pk_bf16_f32 v144, v144, v145\n\t"
"v_mfma_f32_16x16x32_bf16 v[104:107], a[28:31], a[168:171], v[104:107]\n\t"
"ds_read_b128 a[148:151], v224 offset:256\n\t"
"v_cvt_pk_bf16_f32 v184, v184, v185\n\t"
"v_mfma_f32_16x16x32_bf16 v[120:123], a[32:35], a[152:155], v[120:123]\n\t"
"ds_read_b128 a[160:163], v224 offset:2048\n\t"
"v_cvt_pk_bf16_f32 v148, v148, v149\n\t"
"v_mfma_f32_16x16x32_bf16 v[120:123], a[36:39], a[168:171], v[120:123]\n\t"
"ds_read_b128 a[164:167], v224 offset:2304\n\t"
"v_cvt_pk_bf16_f32 v152, v152, v153\n\t"
"v_mfma_f32_16x16x32_bf16 v[136:139], a[40:43], a[152:155], v[136:139]\n\t"
"ds_read_b128 v[56:59], v229 offset:0\n\t"
"v_cvt_pk_bf16_f32 v145, v146, v147\n\t"
"v_mfma_f32_16x16x32_bf16 v[136:139], a[44:47], a[168:171], v[136:139]\n\t"
"ds_read_b128 a[152:155], v224 offset:512\n\t"
"v_cvt_pk_bf16_f32 v168, v168, v169\n\t"
"v_mfma_f32_16x16x32_bf16 v[140:143], a[40:43], a[156:159], v[140:143]\n\t"
"ds_read_b128 v[76:79], v230 offset:13312\n\t"
"v_cvt_pk_bf16_f32 v164, v164, v165\n\t"
"v_mfma_f32_16x16x32_bf16 v[140:143], a[44:47], a[172:175], v[140:143]\n\t"
"buffer_load_dwordx4 a[40:43], v235, s[8:11], s58 offen offset:2048 sc1\n\t"
"buffer_load_dwordx4 a[44:47], v235, s[8:11], s58 offen offset:3072 sc1\n\t"
"v_mfma_f32_16x16x32_bf16 v[124:127], a[32:35], a[156:159], v[124:127]\n\t"
"ds_read_b128 v[88:91], v231 offset:0\n\t"
"v_cvt_pk_bf16_f32 v153, v154, v155\n\t"
"v_mfma_f32_16x16x32_bf16 v[124:127], a[36:39], a[172:175], v[124:127]\n\t"
"buffer_load_dwordx4 a[36:39], v235, s[8:11], s58 offen offset:1024 sc1\n\t"
"v_mfma_f32_16x16x32_bf16 v[108:111], a[24:27], a[156:159], v[108:111]\n\t"
"ds_read_b128 v[72:75], v230 offset:0\n\t"
"v_cvt_pk_bf16_f32 v160, v160, v161\n\t"
"v_mfma_f32_16x16x32_bf16 v[108:111], a[28:31], a[172:175], v[108:111]\n\t"
"s_waitcnt lgkmcnt(7)\n\t"
"buffer_load_dwordx4 a[32:35], v235, s[8:11], s58 offen offset:0 sc1\n\t"
"buffer_load_dwordx4 a[24:27], v252, s[8:11], s57 offen offset:2048 sc1\n\t"
"v_mfma_f32_4x4x4_16b_bf16 v[200:203], a[252:253], a[178:179], 0\n\t"
"ds_read_b128 v[60:63], v229 offset:13312\n\t"
"v_mfma_f32_4x4x4_16b_bf16 v[204:207], a[252:253], a[186:187], 0\n\t"
"buffer_load_dwordx4 a[28:31], v252, s[8:11], s57 offen offset:3072 sc1\n\t"
"v_mfma_f32_4x4x4_16b_bf16 v[216:219], a[252:253], a[194:195], 0\n\t"
"ds_read_b128 a[168:171], v224 offset:2560\n\t"
"v_cvt_pk_bf16_f32 v156, v156, v157\n\t"
"v_cvt_pk_bf16_f32 v188, v188, v189\n\t"
"v_mfma_f32_4x4x4_16b_bf16 v[220:223], a[252:253], a[202:203], 0\n\t"
"ds_read_b128 v[92:95], v231 offset:13312\n\t"
"v_mfma_f32_4x4x4_16b_bf16 v[192:195], a[254:255], a[176:177], v[200:203]\n\t"
"s_waitcnt lgkmcnt(5)\n\t"
"buffer_store_dwordx4 v[56:59], v232, s[16:19], s24 offen nt\n\t"
"v_mfma_f32_4x4x4_16b_bf16 v[196:199], a[254:255], a[184:185], v[204:207]\n\t"
"v_cvt_pk_bf16_f32 v189, v190, v191\n\t"
"v_mfma_f32_4x4x4_16b_bf16 v[200:203], a[254:255], a[180:181], v[200:203]\n\t"
"v_cvt_pk_bf16_f32 v176, v176, v177\n\t"
"v_mfma_f32_4x4x4_16b_bf16 v[204:207], a[254:255], a[188:189], v[204:207]\n\t"
"ds_read_b128 a[172:175], v224 offset:2816\n\t"
"v_mfma_f32_4x4x4_16b_bf16 v[208:211], a[254:255], a[192:193], v[216:219]\n\t"
"ds_read_b128 a[156:159], v224 offset:768\n\t"
"v_cvt_pk_bf16_f32 v180, v180, v181\n\t"
"v_mfma_f32_4x4x4_16b_bf16 v[212:215], a[254:255], a[200:201], v[220:223]\n\t"
"s_waitcnt lgkmcnt(5)\n\t"
"buffer_store_dwordx4 v[72:75], v233, s[16:19], s24 offen nt\n\t"
"v_mfma_f32_4x4x4_16b_bf16 v[216:219], a[254:255], a[196:197], v[216:219]\n\t"
"v_cvt_pk_bf16_f32 v149, v150, v151\n\t"
"v_mfma_f32_4x4x4_16b_bf16 v[220:223], a[254:255], a[204:205], v[220:223]\n\t"
"s_waitcnt lgkmcnt(2)\n\t"
"s_barrier\n\t"
"v_mfma_f32_16x16x32_bf16 v[0:3], a[48:51], a[144:147], v[0:3]\n\t"
"ds_write2st64_b64 v228, v[144:145], v[148:149] offset0:0 offset1:13\n\t"
"v_mfma_f32_16x16x32_bf16 v[0:3], a[52:55], a[160:163], v[0:3]\n\t"
"v_cvt_pk_bf16_f32 v161, v162, v163\n\t"
"v_cvt_pk_bf16_f32 v185, v186, v187\n\t"
"v_mfma_f32_16x16x32_bf16 v[16:19], a[56:59], a[144:147], v[16:19]\n\t"
"v_cvt_pk_bf16_f32 v165, v166, v167\n\t"
"v_cvt_pk_bf16_f32 v172, v172, v173\n\t"
"v_mfma_f32_16x16x32_bf16 v[16:19], a[60:63], a[160:163], v[16:19]\n\t"
"ds_write2st64_b64 v246, v[160:161], v[164:165] offset0:0 offset1:13\n\t"
"v_mfma_f32_16x16x32_bf16 v[32:35], a[64:67], a[144:147], v[32:35]\n\t"
"v_cvt_pk_bf16_f32 v201, v201, v205\n\t"
"v_cvt_pk_bf16_f32 v181, v182, v183\n\t"
"v_mfma_f32_16x16x32_bf16 v[32:35], a[68:71], a[160:163], v[32:35]\n\t"
"buffer_store_dwordx4 v[88:91], v234, s[16:19], s24 offen nt\n\t"
"v_mfma_f32_16x16x32_bf16 v[36:39], a[64:67], a[148:151], v[36:39]\n\t"
"v_cvt_pk_bf16_f32 v195, -v195, -v199\n\t"
"v_cvt_pk_bf16_f32 v177, v178, v179\n\t"
"v_mfma_f32_16x16x32_bf16 v[36:39], a[68:71], a[164:167], v[36:39]\n\t"
"s_nop 0\n\t"
"ds_read_b128 a[144:147], v224 offset:1024\n\t"
"ds_read_b128 a[160:163], v224 offset:3072\n\t"
"ds_write_addtid_b32 v201 offset:28992\n\t"
"ds_write2st64_b64 v247, v[184:185], v[188:189] offset0:26 offset1:39\n\t"
"v_mfma_f32_16x16x32_bf16 v[20:23], a[56:59], a[148:151], v[20:23]\n\t"
"v_cvt_pk_bf16_f32 v209, -v209, -v213\n\t"
"v_cvt_pk_bf16_f32 v157, v158, v159\n\t"
"v_mfma_f32_16x16x32_bf16 v[20:23], a[60:63], a[164:167], v[20:23]\n\t"
"v_cvt_pk_bf16_f32 v208, -v208, -v212\n\t"
"v_cvt_pk_bf16_f32 v216, v216, v220\n\t"
"v_mfma_f32_16x16x32_bf16 v[4:7], a[48:51], a[148:151], v[4:7]\n\t"
"v_cvt_pk_bf16_f32 v192, -v192, -v196\n\t"
"v_cvt_pk_bf16_f32 v217, v217, v221\n\t"
"v_mfma_f32_16x16x32_bf16 v[4:7], a[52:55], a[164:167], v[4:7]\n\t"
"s_nop 0\n\t"
"ds_read_b128 a[148:151], v224 offset:1280\n\t"
"ds_read_b128 a[164:167], v224 offset:3328\n\t"
"ds_write_addtid_b32 v209 offset:22720\n\t"
"ds_write2st64_b64 v247, v[176:177], v[180:181] offset0:0 offset1:13\n\t"
"v_mfma_f32_16x16x32_bf16 v[8:11], a[48:51], a[152:155], v[8:11]\n\t"
"v_cvt_pk_bf16_f32 v169, v170, v171\n\t"
"v_cvt_pk_bf16_f32 v173, v174, v175\n\t"
"v_mfma_f32_16x16x32_bf16 v[8:11], a[52:55], a[168:171], v[8:11]\n\t"
"buffer_store_dwordx4 v[60:63], v232, s[16:19], s27 offen nt\n\t"
"v_mfma_f32_16x16x32_bf16 v[24:27], a[56:59], a[152:155], v[24:27]\n\t"
"ds_write2st64_b64 v228, v[152:153], v[156:157] offset0:26 offset1:39\n\t"
"v_mfma_f32_16x16x32_bf16 v[24:27], a[60:63], a[168:171], v[24:27]\n\t"
"v_cvt_pk_bf16_f32 v193, -v193, -v197\n\t"
"v_cvt_pk_bf16_f32 v210, -v210, -v214\n\t"
"v_mfma_f32_16x16x32_bf16 v[40:43], a[64:67], a[152:155], v[40:43]\n\t"
"v_cvt_pk_bf16_f32 v194, -v194, -v198\n\t"
"v_cvt_pk_bf16_f32 v203, v203, v207\n\t"
"v_mfma_f32_16x16x32_bf16 v[40:43], a[68:71], a[168:171], v[40:43]\n\t"
"s_waitcnt lgkmcnt(5)\n\t"
"ds_write2st64_b64 v246, v[168:169], v[172:173] offset0:26 offset1:39\n\t"
"v_mfma_f32_16x16x32_bf16 v[44:47], a[64:67], a[156:159], v[44:47]\n\t"
"buffer_load_dwordx4 a[64:67], v235, s[8:11], s65 offen offset:0 sc1\n\t"
"v_mfma_f32_16x16x32_bf16 v[44:47], a[68:71], a[172:175], v[44:47]\n\t"
"v_cvt_pk_bf16_f32 v211, -v211, -v215\n\t"
"v_cvt_pk_bf16_f32 v219, v219, v223\n\t"
"v_mfma_f32_16x16x32_bf16 v[28:31], a[56:59], a[156:159], v[28:31]\n\t"
"buffer_load_dwordx4 a[68:71], v235, s[8:11], s65 offen offset:1024 sc1\n\t"
"v_mfma_f32_16x16x32_bf16 v[28:31], a[60:63], a[172:175], v[28:31]\n\t"
"ds_write_addtid_b32 v210 offset:19616\n\t"
"v_cvt_pk_bf16_f32 v200, v200, v204\n\t"
"v_mfma_f32_16x16x32_bf16 v[12:15], a[48:51], a[156:159], v[12:15]\n\t"
"ds_write_addtid_b32 v211 offset:23744\n\t"
"v_cvt_pk_bf16_f32 v218, v218, v222\n\t"
"v_mfma_f32_16x16x32_bf16 v[12:15], a[52:55], a[172:175], v[12:15]\n\t"
"ds_write_addtid_b32 v217 offset:31040\n\t"
"buffer_store_dwordx4 v[76:79], v233, s[16:19], s27 offen nt\n\t"
"v_mfma_f32_16x16x32_bf16 v[96:99], a[72:75], a[144:147], v[96:99]\n\t"
"ds_write_addtid_b32 v216 offset:26912\n\t"
"v_cvt_pk_bf16_f32 v202, v202, v206\n\t"
"v_mfma_f32_16x16x32_bf16 v[96:99], a[76:79], a[160:163], v[96:99]\n\t"
"s_nop 0\n\t"
".p2align 3\n\t"
"ds_write_addtid_b32 v192 offset:16544\n\t"
"ds_write_addtid_b32 v193 offset:20672\n\t"
"ds_write_addtid_b32 v194 offset:17568\n\t"
"ds_write_addtid_b32 v200 offset:24864\n\t"
"ds_write_addtid_b32 v202 offset:25888\n\t"
"ds_write_addtid_b32 v203 offset:30016\n\t"
"ds_write_addtid_b32 v218 offset:27936\n\t"
"ds_write_addtid_b32 v219 offset:32064\n\t"
"v_mfma_f32_16x16x32_bf16 v[112:115], a[80:83], a[144:147], v[112:115]\n\t"
"s_nop 0\n\t"
"ds_write_addtid_b32 v195 offset:21696\n\t"
"ds_write_addtid_b32 v208 offset:18592\n\t"
"v_mfma_f32_16x16x32_bf16 v[112:115], a[84:87], a[160:163], v[112:115]\n\t"
"ds_read_b128 a[168:171], v224 offset:3584\n\t"
"buffer_store_dwordx4 v[92:95], v234, s[16:19], s27 offen nt\n\t"
"v_mfma_f32_16x16x32_bf16 v[128:131], a[88:91], a[144:147], v[128:131]\n\t"
"s_nop 0\n\t"
"ds_read_b128 a[152:155], v224 offset:1536\n\t"
"ds_read_b128 a[156:159], v224 offset:1792\n\t"
"v_mfma_f32_16x16x32_bf16 v[128:131], a[92:95], a[160:163], v[128:131]\n\t"
"s_waitcnt lgkmcnt(15)\n\t"
"ds_read_b128 a[172:175], v224 offset:3840\n\t"
"v_accvgpr_read_b32 v248, a208\n\t"
"v_mfma_f32_16x16x32_bf16 v[132:135], a[88:91], a[148:151], v[132:135]\n\t"
"buffer_load_dwordx4 a[60:63], v252, s[8:11], s64 offen offset:3072 sc1\n\t"
"v_mfma_f32_16x16x32_bf16 v[132:135], a[92:95], a[164:167], v[132:135]\n\t"
"buffer_load_dwordx4 a[56:59], v252, s[8:11], s64 offen offset:2048 sc1\n\t"
"buffer_load_dwordx4 a[48:51], v252, s[8:11], s64 offen offset:0 sc1\n\t"
"v_mfma_f32_16x16x32_bf16 v[116:119], a[80:83], a[148:151], v[116:119]\n\t"
"v_add_u32 v248, 101632, v248\n\t"
"v_xor_b32 v249, 32, v248\n\t"
"v_mfma_f32_16x16x32_bf16 v[116:119], a[84:87], a[164:167], v[116:119]\n\t"
"buffer_load_dwordx4 a[52:55], v252, s[8:11], s64 offen offset:1024 sc1\n\t"
"v_mfma_f32_16x16x32_bf16 v[100:103], a[72:75], a[148:151], v[100:103]\n\t"
"v_xor_b32 v250, 64, v248\n\t"
"v_xor_b32 v251, 96, v248\n\t"
"v_mfma_f32_16x16x32_bf16 v[100:103], a[76:79], a[164:167], v[100:103]\n\t"
"s_waitcnt lgkmcnt(0)\n\t"
"s_barrier\n\t"
"v_mfma_f32_16x16x32_bf16 v[104:107], a[72:75], a[152:155], v[104:107]\n\t"
"s_nop 0\n\t"
"ds_read_b128 a[144:147], v224 offset:16544\n\t"
"ds_read_b64_tr_b16 a[176:177], v248 offset:20736\n\t"
"v_mfma_f32_16x16x32_bf16 v[104:107], a[76:79], a[168:171], v[104:107]\n\t"
"s_nop 0\n\t"
"ds_read_b64_tr_b16 a[178:179], v249 offset:20736\n\t"
"ds_read_b64_tr_b16 a[180:181], v248 offset:21248\n\t"
"v_mfma_f32_16x16x32_bf16 v[120:123], a[80:83], a[152:155], v[120:123]\n\t"
"s_nop 0\n\t"
"ds_read_b128 a[160:163], v224 offset:18592\n\t"
"ds_read_b128 a[148:151], v224 offset:16800\n\t"
"v_mfma_f32_16x16x32_bf16 v[120:123], a[84:87], a[168:171], v[120:123]\n\t"
"s_nop 0\n\t"
"ds_read_b128 a[164:167], v224 offset:18848\n\t"
"ds_read_b64_tr_b16 a[182:183], v249 offset:21248\n\t"
"v_mfma_f32_16x16x32_bf16 v[136:139], a[88:91], a[152:155], v[136:139]\n\t"
"s_nop 0\n\t"
"ds_read_b64_tr_b16 a[184:185], v250 offset:23040\n\t"
"ds_read_b64_tr_b16 a[186:187], v251 offset:23040\n\t"
"v_mfma_f32_16x16x32_bf16 v[136:139], a[92:95], a[168:171], v[136:139]\n\t"
"s_nop 0\n\t"
"ds_read_b64_tr_b16 a[188:189], v250 offset:23552\n\t"
"ds_read_b128 v[152:155], v229 offset:0\n\t"
"v_mfma_f32_16x16x32_bf16 v[140:143], a[88:91], a[156:159], v[140:143]\n\t"
"s_nop 0\n\t"
"ds_read_b128 a[168:171], v224 offset:19104\n\t"
"ds_read_b64_tr_b16 a[190:191], v251 offset:23552\n\t"
"v_mfma_f32_16x16x32_bf16 v[140:143], a[92:95], a[172:175], v[140:143]\n\t"
"buffer_load_dwordx4 a[88:91], v235, s[8:11], s64 offen offset:2048 sc1\n\t"
"v_mfma_f32_16x16x32_bf16 v[124:127], a[80:83], a[156:159], v[124:127]\n\t"
"s_nop 0\n\t"
"ds_read_b128 a[152:155], v224 offset:17056\n\t"
"ds_read_b64_tr_b16 a[192:193], v250 offset:20736\n\t"
"v_mfma_f32_16x16x32_bf16 v[124:127], a[84:87], a[172:175], v[124:127]\n\t"
"buffer_load_dwordx4 a[92:95], v235, s[8:11], s64 offen offset:3072 sc1\n\t"
"v_mfma_f32_16x16x32_bf16 v[108:111], a[72:75], a[156:159], v[108:111]\n\t"
"s_nop 0\n\t"
"ds_read_b64_tr_b16 a[194:195], v251 offset:20736\n\t"
"ds_read_b64_tr_b16 a[196:197], v250 offset:21248\n\t"
"v_mfma_f32_16x16x32_bf16 v[108:111], a[76:79], a[172:175], v[108:111]\n\t"
"s_waitcnt lgkmcnt(10)\n\t"
"buffer_load_dwordx4 a[84:87], v235, s[8:11], s64 offen offset:1024 sc1\n\t"
"v_mfma_f32_4x4x4_16b_bf16 v[192:195], a[210:211], a[176:177], 0\n\t"
"ds_read_b64_tr_b16 a[198:199], v251 offset:21248\n\t"
"v_mfma_f32_4x4x4_16b_bf16 v[196:199], a[210:211], a[178:179], 0\n\t"
"ds_read_b128 v[168:171], v230 offset:0\n\t"
"v_mfma_f32_4x4x4_16b_bf16 v[200:203], a[210:211], a[180:181], 0\n\t"
"ds_read_b64_tr_b16 a[200:201], v248 offset:23040\n\t"
"v_mfma_f32_4x4x4_16b_bf16 v[204:207], a[210:211], a[182:183], 0\n\t"
"s_waitcnt lgkmcnt(9)\n\t"
"buffer_store_dwordx4 v[152:155], v232, s[16:19], s20 offen nt\n\t"
"v_mfma_f32_16x16x32_bf16 v[0:3], a[96:99], a[144:147], v[0:3]\n\t"
"s_nop 0\n\t"
"ds_read_b64_tr_b16 a[202:203], v249 offset:23040\n\t"
"ds_read_b128 v[184:187], v231 offset:0\n\t"
"v_mfma_f32_16x16x32_bf16 v[0:3], a[100:103], a[160:163], v[0:3]\n\t"
"s_nop 0\n\t"
"ds_read_b128 a[156:159], v224 offset:17312\n\t"
"ds_read_b128 a[172:175], v224 offset:19360\n\t"
"v_mfma_f32_16x16x32_bf16 v[16:19], a[104:107], a[144:147], v[16:19]\n\t"
"buffer_load_dwordx4 a[80:83], v235, s[8:11], s64 offen offset:0 sc1\n\t"
"v_mfma_f32_16x16x32_bf16 v[16:19], a[108:111], a[160:163], v[16:19]\n\t"
"ds_read_b64_tr_b16 a[204:205], v248 offset:23552\n\t"
"v_cvt_pk_bf16_f32 v195, v195, v199\n\t"
"v_mfma_f32_16x16x32_bf16 v[32:35], a[112:115], a[144:147], v[32:35]\n\t"
"v_cvt_pk_bf16_f32 v196, v192, v196\n\t"
"v_cvt_pk_bf16_f32 v194, v194, v198\n\t"
"v_mfma_f32_16x16x32_bf16 v[32:35], a[116:119], a[160:163], v[32:35]\n\t"
"buffer_load_dwordx4 a[72:75], v252, s[8:11], s63 offen offset:2048 sc1\n\t"
"v_mfma_f32_16x16x32_bf16 v[36:39], a[112:115], a[148:151], v[36:39]\n\t"
"ds_read_b64_tr_b16 a[206:207], v249 offset:23552\n\t"
"v_cvt_pk_bf16_f32 v197, v193, v197\n\t"
"v_mfma_f32_16x16x32_bf16 v[36:39], a[116:119], a[164:167], v[36:39]\n\t"
"s_waitcnt lgkmcnt(7)\n\t"
"buffer_load_dwordx4 a[76:79], v252, s[8:11], s63 offen offset:3072 sc1\n\t"
"v_mfma_f32_4x4x4_16b_bf16 v[208:211], a[210:211], a[184:185], 0\n\t"
"ds_read_b128 v[156:159], v229 offset:13312\n\t"
"v_mfma_f32_4x4x4_16b_bf16 v[212:215], a[210:211], a[186:187], 0\n\t"
"ds_read_b128 a[144:147], v224 offset:17568\n\t"
"v_mfma_f32_4x4x4_16b_bf16 v[216:219], a[210:211], a[188:189], 0\n\t"
"v_cvt_pk_bf16_f32 v192, v202, v206\n\t"
"v_mfma_f32_4x4x4_16b_bf16 v[220:223], a[210:211], a[190:191], 0\n\t"
"buffer_store_dwordx4 v[168:171], v233, s[16:19], s20 offen nt\n\t"
"v_mfma_f32_16x16x32_bf16 v[20:23], a[104:107], a[148:151], v[20:23]\n\t"
"ds_read_b128 a[160:163], v224 offset:19616\n\t"
"v_cvt_pk_bf16_f32 v199, v201, v205\n\t"
"v_mfma_f32_16x16x32_bf16 v[20:23], a[108:111], a[164:167], v[20:23]\n\t"
"ds_write_addtid_b32 v199 offset:9344\n\t"
"v_cvt_pk_bf16_f32 v198, v200, v204\n\t"
"v_mfma_f32_16x16x32_bf16 v[4:7], a[96:99], a[148:151], v[4:7]\n\t"
"ds_write_addtid_b32 v197 offset:1024\n\t"
"v_cvt_pk_bf16_f32 v193, v203, v207\n\t"
"v_mfma_f32_16x16x32_bf16 v[4:7], a[100:103], a[164:167], v[4:7]\n\t"
"ds_read_b128 v[172:175], v230 offset:13312\n\t"
"s_waitcnt lgkmcnt(7)\n\t"
"buffer_store_dwordx4 v[184:187], v234, s[16:19], s20 offen nt\n\t"
"v_cvt_pk_bf16_f32 v210, v210, v214\n\t"
"v_mfma_f32_16x16x32_bf16 v[8:11], a[96:99], a[152:155], v[8:11]\n\t"
"ds_read_b128 a[164:167], v224 offset:19872\n\t"
"v_cvt_pk_bf16_f32 v211, v211, v215\n\t"
"v_mfma_f32_16x16x32_bf16 v[8:11], a[100:103], a[168:171], v[8:11]\n\t"
"ds_write_addtid_b32 v198 offset:8320\n\t"
"v_cvt_pk_bf16_f32 v214, v216, v220\n\t"
"v_mfma_f32_16x16x32_bf16 v[24:27], a[104:107], a[152:155], v[24:27]\n\t"
"ds_write_addtid_b32 v196 offset:0\n\t"
"v_cvt_pk_bf16_f32 v212, v208, v212\n\t"
"v_mfma_f32_16x16x32_bf16 v[24:27], a[108:111], a[168:171], v[24:27]\n\t"
"ds_read_b128 a[148:151], v224 offset:17824\n\t"
"v_cvt_pk_bf16_f32 v215, v217, v221\n\t"
"v_mfma_f32_4x4x4_16b_bf16 v[200:203], a[210:211], a[192:193], 0\n\t"
"v_cvt_pk_bf16_f32 v208, v218, v222\n\t"
"v_mfma_f32_4x4x4_16b_bf16 v[204:207], a[210:211], a[194:195], 0\n\t"
"v_cvt_pk_bf16_f32 v213, v209, v213\n\t"
"v_mfma_f32_4x4x4_16b_bf16 v[196:199], a[210:211], a[196:197], 0\n\t"
"v_cvt_pk_bf16_f32 v209, v219, v223\n\t"
"v_mfma_f32_4x4x4_16b_bf16 v[216:219], a[210:211], a[198:199], 0\n\t"
"s_waitcnt lgkmcnt(6)\n\t"
"buffer_store_dwordx4 v[156:159], v232, s[16:19], s25 offen nt\n\t"
"v_mfma_f32_16x16x32_bf16 v[40:43], a[112:115], a[152:155], v[40:43]\n\t"
"s_nop 0\n\t"
"ds_write_addtid_b32 v214 offset:12448\n\t"
"ds_write_addtid_b32 v215 offset:13472\n\t"
"v_mfma_f32_16x16x32_bf16 v[40:43], a[116:119], a[168:171], v[40:43]\n\t"
"s_nop 0\n\t"
"ds_read_b128 a[168:171], v224 offset:20128\n\t"
"ds_read_b128 v[188:191], v231 offset:13312\n\t"
"v_mfma_f32_16x16x32_bf16 v[44:47], a[112:115], a[156:159], v[44:47]\n\t"
"buffer_load_dwordx4 a[112:115], v235, s[8:11], s47 offen offset:0 sc1\n\t"
"v_mfma_f32_16x16x32_bf16 v[44:47], a[116:119], a[172:175], v[44:47]\n\t"
"ds_write_addtid_b32 v212 offset:4128\n\t"
"v_cvt_pk_bf16_f32 v202, v202, v206\n\t"
"v_cvt_pk_bf16_f32 v203, v203, v207\n\t"
"v_mfma_f32_16x16x32_bf16 v[28:31], a[104:107], a[156:159], v[28:31]\n\t"
"v_cvt_pk_bf16_f32 v204, v200, v204\n\t"
"v_cvt_pk_bf16_f32 v205, v201, v205\n\t"
"v_mfma_f32_16x16x32_bf16 v[28:31], a[108:111], a[172:175], v[28:31]\n\t"
"ds_read_b128 a[152:155], v224 offset:18080\n\t"
"s_waitcnt lgkmcnt(6)\n\t"
"buffer_store_dwordx4 v[172:175], v233, s[16:19], s25 offen nt\n\t"
"v_mfma_f32_16x16x32_bf16 v[12:15], a[96:99], a[156:159], v[12:15]\n\t"
"ds_write_addtid_b32 v204 offset:2048\n\t"
"v_cvt_pk_bf16_f32 v206, v196, v216\n\t"
"v_mfma_f32_16x16x32_bf16 v[12:15], a[100:103], a[172:175], v[12:15]\n\t"
"ds_write_addtid_b32 v213 offset:5152\n\t"
"v_cvt_pk_bf16_f32 v200, v198, v218\n\t"
"v_mfma_f32_4x4x4_16b_bf16 v[220:223], a[210:211], a[200:201], 0\n\t"
"buffer_load_dwordx4 a[116:119], v235, s[8:11], s47 offen offset:1024 sc1\n\t"
"buffer_load_dwordx4 a[108:111], v252, s[8:11], s46 offen offset:3072 sc1\n\t"
"buffer_load_dwordx4 a[104:107], v252, s[8:11], s46 offen offset:2048 sc1\n\t"
"v_cvt_pk_bf16_f32 v207, v197, v217\n\t"
"v_mfma_f32_4x4x4_16b_bf16 v[212:215], a[210:211], a[202:203], 0\n\t"
"v_cvt_pk_bf16_f32 v201, v199, v219\n\t"
"v_mfma_f32_4x4x4_16b_bf16 v[196:199], a[210:211], a[204:205], 0\n\t"
"ds_write_addtid_b32 v205 offset:3072\n\t"
"v_mfma_f32_4x4x4_16b_bf16 v[216:219], a[210:211], a[206:207], 0\n\t"
"s_waitcnt lgkmcnt(5)\n\t"
"buffer_store_dwordx4 v[188:191], v234, s[16:19], s25 offen nt\n\t"
"v_mfma_f32_16x16x32_bf16 v[96:99], a[120:123], a[144:147], v[96:99]\n\t"
"s_nop 0\n\t"
"ds_read_b128 a[156:159], v224 offset:18336\n\t"
"ds_read_b128 a[172:175], v224 offset:20384\n\t"
"v_mfma_f32_16x16x32_bf16 v[96:99], a[124:127], a[160:163], v[96:99]\n\t"
"s_nop 0\n\t"
"ds_write_addtid_b32 v206 offset:10368\n\t"
"ds_write_addtid_b32 v207 offset:11392\n\t"
"v_mfma_f32_16x16x32_bf16 v[112:115], a[128:131], a[144:147], v[112:115]\n\t"
"buffer_load_dwordx4 a[96:99], v252, s[8:11], s46 offen offset:0 sc1\n\t"
"v_mfma_f32_16x16x32_bf16 v[112:115], a[132:135], a[160:163], v[112:115]\n\t"
"v_cvt_pk_bf16_f32 v222, v222, v214\n\t"
"v_cvt_pk_bf16_f32 v223, v223, v215\n\t"
"v_mfma_f32_16x16x32_bf16 v[128:131], a[136:139], a[144:147], v[128:131]\n\t"
"v_cvt_pk_bf16_f32 v212, v220, v212\n\t"
"v_cvt_pk_bf16_f32 v213, v221, v213\n\t"
"v_mfma_f32_16x16x32_bf16 v[128:131], a[140:143], a[160:163], v[128:131]\n\t"
"s_nop 0\n\t"
"ds_write_addtid_b32 v212 offset:6176\n\t"
"ds_write_addtid_b32 v213 offset:7200\n\t"
"v_cvt_pk_bf16_f32 v214, v196, v216\n\t"
"v_cvt_pk_bf16_f32 v215, v197, v217\n\t"
"v_mfma_f32_16x16x32_bf16 v[132:135], a[136:139], a[148:151], v[132:135]\n\t"
"s_nop 0\n\t"
"ds_write_addtid_b32 v214 offset:14496\n\t"
"ds_write_addtid_b32 v215 offset:15520\n\t"
"v_mfma_f32_16x16x32_bf16 v[132:135], a[140:143], a[164:167], v[132:135]\n\t"
"buffer_load_dwordx4 a[100:103], v252, s[8:11], s46 offen offset:1024 sc1\n\t"
"v_mfma_f32_16x16x32_bf16 v[116:119], a[128:131], a[148:151], v[116:119]\n\t"
"v_accvgpr_read_b32 v224, a213\n\t"
"v_cvt_pk_bf16_f32 v220, v198, v218\n\t"
"v_mfma_f32_16x16x32_bf16 v[116:119], a[132:135], a[164:167], v[116:119]\n\t"
"v_cvt_pk_bf16_f32 v221, v199, v219\n\t"
"v_mfma_f32_16x16x32_bf16 v[100:103], a[120:123], a[148:151], v[100:103]\n\t"
"s_waitcnt lgkmcnt(0)\n\t"
"v_mfma_f32_16x16x32_bf16 v[100:103], a[124:127], a[164:167], v[100:103]\n\t"
"s_barrier\n\t"
"s_add_u32 s71, s71, 1\n\t"
"v_mov_b32 v215, s71\n\t"
"s_mov_b64 exec, 1\n\t"
"buffer_store_dword v215, off, s[8:11], s70 sc1\n\t"
"s_mov_b64 exec, -1\n\t"
"s_sub_u32 s21, s21, 1\n\t"
"s_xor_b64 s[84:85], s[84:85], s[88:89]\n\t"
"s_branch Lunit_%=\n\t"
"Lfb_%=:\n\t"
"v_mbcnt_lo_u32_b32 v162, -1, 0\n\t"
"v_mbcnt_hi_u32_b32 v162, -1, v162\n\t"
"v_bfe_u32 v161, v162, 1, 2\n\t"
"v_lshlrev_b32 v163, 4, v162\n\t"
"v_and_or_b32 v161, v163, 16, v161\n\t"
"v_mul_u32_u24 v161, 0xc0, v161\n\t"
"v_lshrrev_b32 v163, 5, v162\n\t"
"v_mul_u32_u24 v163, 12, v163\n\t"
"v_bfe_u32 v164, v162, 3, 2\n\t"
"v_add3_u32 v161, v161, v163, v164\n\t"
"v_lshlrev_b32 v161, 1, v161\n\t"
"v_lshlrev_b32 v162, 1, v162\n\t"
"v_mov_b32 v152, 0\n\t"
"v_mov_b32 v153, 0\n\t"
"v_mov_b32 v154, 0\n\t"
"v_mov_b32 v155, 0\n\t"
"v_mov_b32 v156, 0\n\t"
"v_mov_b32 v157, 0\n\t"
"v_mov_b32 v158, 0\n\t"
"v_mov_b32 v159, 0\n\t"
"v_mov_b32 v160, 0\n\t"
"s_mov_b32 s67, 0\n\t"
"Lfb0_%=:\n\t"
"s_mov_b32 s69, 0\n\t"
"Lfb1_%=:\n\t"
"s_lshr_b32 s60, s69, 5\n\t"
"s_mul_i32 s56, s60, 12288\n\t"
"s_bfe_u32 s61, s69, 0x20001\n\t"
"s_mul_i32 s61, s61, 1536\n\t"
"s_add_u32 s56, s56, s61\n\t"
"s_bitcmp1_b32 s69, 0\n\t"
"s_cselect_b32 s61, 48, 0\n\t"
"s_add_u32 s56, s56, s61\n\t"
"s_bfe_u32 s62, s69, 0x20003\n\t"
"s_mul_i32 s61, s62, 96\n\t"
"s_add_u32 s56, s56, s61\n\t"
"s_lshl3_add_u32 s56, s67, s56\n\t"
"s_lshr_b32 s60, s69, 5\n\t"
"s_bfe_u32 s62, s69, 0x20003\n\t"
"s_and_b32 s61, s60, 1\n\t"
"s_lshl1_add_u32 s61, s67, s61\n\t"
"s_mul_i32 s62, s62, 288\n\t"
"s_add_u32 s61, s61, s62\n\t"
"s_lshr_b32 s60, s60, 1\n\t"
"s_mul_i32 s62, s60, 96\n\t"
"s_add_u32 s61, s61, s62\n\t"
"s_lshl_b32 s57, s61, 10\n\t"
"s_and_b32 s61, s69, 7\n\t"
"s_lshl_b32 s61, s61, 7\n\t"
"s_add_u32 s57, s57, s61\n\t"
"s_add_u32 s60, s56, 0\n\t"
"buffer_load_short_d16_hi v152, v161, s[52:55], s60 offen\n\t"
"s_add_u32 s60, s56, 73728\n\t"
"buffer_load_short_d16_hi v153, v161, s[52:55], s60 offen\n\t"
"s_add_u32 s60, s56, 147456\n\t"
"buffer_load_short_d16_hi v154, v161, s[52:55], s60 offen\n\t"
"s_add_u32 s60, s56, 221184\n\t"
"buffer_load_short_d16_hi v155, v161, s[52:55], s60 offen\n\t"
"s_add_u32 s60, s56, 294912\n\t"
"buffer_load_short_d16_hi v156, v161, s[52:55], s60 offen\n\t"
"s_add_u32 s60, s56, 368640\n\t"
"buffer_load_short_d16_hi v157, v161, s[52:55], s60 offen\n\t"
"s_add_u32 s60, s56, 442368\n\t"
"buffer_load_short_d16_hi v158, v161, s[52:55], s60 offen\n\t"
"s_add_u32 s60, s56, 516096\n\t"
"buffer_load_short_d16_hi v159, v161, s[52:55], s60 offen\n\t"
"s_add_u32 s60, s56, 589824\n\t"
"buffer_load_short_d16_hi v160, v161, s[52:55], s60 offen\n\t"
"s_waitcnt vmcnt(2)\n\t"
"v_add_f32 v168, v152, v158\n\t"
"s_waitcnt vmcnt(1)\n\t"
"v_add_f32 v169, v153, v159\n\t"
"s_waitcnt vmcnt(0)\n\t"
"v_add_f32 v170, v154, v160\n\t"
"v_add_f32 v171, v168, v155\n\t"
"v_sub_f32 v174, v168, v155\n\t"
"v_add_f32 v172, v169, v156\n\t"
"v_sub_f32 v175, v169, v156\n\t"
"v_add_f32 v173, v170, v157\n\t"
"v_sub_f32 v176, v170, v157\n\t"
"v_add_f32 v177, v152, v154\n\t"
"v_add_f32 v178, v171, v173\n\t"
"v_add_f32 v179, v174, v176\n\t"
"v_add_f32 v180, v158, v160\n\t"
"v_add_f32 v182, v177, v153\n\t"
"v_sub_f32 v183, v177, v153\n\t"
"v_add_f32 v184, v178, v172\n\t"
"v_sub_f32 v185, v178, v172\n\t"
"v_add_f32 v186, v179, v175\n\t"
"v_sub_f32 v187, v179, v175\n\t"
"v_add_f32 v188, v180, v159\n\t"
"v_sub_f32 v189, v180, v159\n\t"
"v_mul_f32 v191, 0.5, v182\n\t"
"v_mul_f32 v192, 0.5, v183\n\t"
"v_mul_f32 v194, 0.5, v171\n\t"
"v_mul_f32 v195, 0x3e800000, v184\n\t"
"v_mul_f32 v196, 0x3e800000, v185\n\t"
"v_mul_f32 v197, -0.5, v173\n\t"
"v_mul_f32 v199, 0.5, v186\n\t"
"v_mul_f32 v200, 0.5, v187\n\t"
"v_mul_f32 v203, -0.5, v188\n\t"
"v_mul_f32 v204, -0.5, v189\n\t"
"v_cvt_pk_bf16_f32 v206, v152, v191\n\t"
"v_cvt_pk_bf16_f32 v207, v192, -v154\n\t"
"v_cvt_pk_bf16_f32 v208, v194, v195\n\t"
"v_cvt_pk_bf16_f32 v209, v196, v197\n\t"
"v_cvt_pk_bf16_f32 v210, v174, v199\n\t"
"v_cvt_pk_bf16_f32 v211, v200, -v176\n\t"
"v_cvt_pk_bf16_f32 v212, -v158, v203\n\t"
"v_cvt_pk_bf16_f32 v213, v204, v160\n\t"
"s_add_u32 s60, s57, 0\n\t"
"buffer_store_short v206, v162, s[8:11], s60 offen sc1\n\t"
"s_add_u32 s60, s57, 6144\n\t"
"buffer_store_short_d16_hi v206, v162, s[8:11], s60 offen sc1\n\t"
"s_add_u32 s60, s57, 12288\n\t"
"buffer_store_short v207, v162, s[8:11], s60 offen sc1\n\t"
"s_add_u32 s60, s57, 18432\n\t"
"buffer_store_short_d16_hi v207, v162, s[8:11], s60 offen sc1\n\t"
"s_add_u32 s60, s57, 24576\n\t"
"buffer_store_short v208, v162, s[8:11], s60 offen sc1\n\t"
"s_add_u32 s60, s57, 30720\n\t"
"buffer_store_short_d16_hi v208, v162, s[8:11], s60 offen sc1\n\t"
"s_add_u32 s60, s57, 36864\n\t"
"buffer_store_short v209, v162, s[8:11], s60 offen sc1\n\t"
"s_add_u32 s60, s57, 43008\n\t"
"buffer_store_short_d16_hi v209, v162, s[8:11], s60 offen sc1\n\t"
"s_add_u32 s60, s57, 49152\n\t"
"buffer_store_short v210, v162, s[8:11], s60 offen sc1\n\t"
"s_add_u32 s60, s57, 55296\n\t"
"buffer_store_short_d16_hi v210, v162, s[8:11], s60 offen sc1\n\t"
"s_add_u32 s60, s57, 61440\n\t"
"buffer_store_short v211, v162, s[8:11], s60 offen sc1\n\t"
"s_add_u32 s60, s57, 67584\n\t"
"buffer_store_short_d16_hi v211, v162, s[8:11], s60 offen sc1\n\t"
"s_add_u32 s60, s57, 73728\n\t"
"buffer_store_short v212, v162, s[8:11], s60 offen sc1\n\t"
"s_add_u32 s60, s57, 79872\n\t"
"buffer_store_short_d16_hi v212, v162, s[8:11], s60 offen sc1\n\t"
"s_add_u32 s60, s57, 86016\n\t"
"buffer_store_short v213, v162, s[8:11], s60 offen sc1\n\t"
"s_add_u32 s60, s57, 92160\n\t"
"buffer_store_short_d16_hi v213, v162, s[8:11], s60 offen sc1\n\t"
"s_waitcnt vmcnt(0)\n\t"
"s_add_u32 s69, s69, 1\n\t"
"s_cmp_lt_u32 s69, 192\n\t"
"s_cbranch_scc1 Lfb1_%=\n\t"
"s_add_u32 s67, s67, 1\n\t"
"s_cmp_lt_u32 s67, 3\n\t"
"s_cbranch_scc1 Lfb0_%=\n\t"
"s_branch Lgo_%=\n\t"
"Ltail_%=:\n\t"
"v_mfma_f32_16x16x32_bf16 v[104:107], a[72:75], a[152:155], v[104:107]\n\t"
"s_nop 0\n\t"
"ds_read_b128 a[144:147], v224 offset:17568\n\t"
"ds_read_b128 a[164:167], v224 offset:19872\n\t"
"v_mfma_f32_16x16x32_bf16 v[104:107], a[76:79], a[168:171], v[104:107]\n\t"
"buffer_load_dwordx4 a[20:23], v252, s[84:87], s69 offen offset:1024 sc1\n\t"
"v_mfma_f32_16x16x32_bf16 v[120:123], a[80:83], a[152:155], v[120:123]\n\t"
"s_nop 0\n\t"
"ds_read_b128 a[160:163], v224 offset:19616\n\t"
"ds_read_b128 a[148:151], v224 offset:17824\n\t"
"v_mfma_f32_16x16x32_bf16 v[120:123], a[84:87], a[168:171], v[120:123]\n\t"
"buffer_load_dwordx4 a[12:15], v235, s[84:87], s69 offen offset:3072 sc1\n\t"
"v_mfma_f32_16x16x32_bf16 v[136:139], a[88:91], a[152:155], v[136:139]\n\t"
"ds_read_b128 a[152:155], v224 offset:18080\n\t"
"v_add_u32 v248, 33280, v226\n\t"
"v_mfma_f32_16x16x32_bf16 v[136:139], a[92:95], a[168:171], v[136:139]\n\t"
"buffer_load_dwordx4 a[8:11], v235, s[84:87], s69 offen offset:2048 sc1\n\t"
"v_mfma_f32_16x16x32_bf16 v[140:143], a[88:91], a[156:159], v[140:143]\n\t"
"ds_read_b128 v[8:11], v229 offset:20480\n\t"
"v_xor_b32 v249, 32, v248\n\t"
"v_mfma_f32_16x16x32_bf16 v[140:143], a[92:95], a[172:175], v[140:143]\n\t"
"buffer_load_dwordx4 a[0:3], v235, s[84:87], s69 offen offset:0 sc1\n\t"
"v_mfma_f32_16x16x32_bf16 v[124:127], a[80:83], a[156:159], v[124:127]\n\t"
"ds_read_b128 a[168:171], v224 offset:20128\n\t"
"v_xor_b32 v250, 64, v248\n\t"
"v_mfma_f32_16x16x32_bf16 v[124:127], a[84:87], a[172:175], v[124:127]\n\t"
"buffer_load_dwordx4 a[4:7], v235, s[84:87], s69 offen offset:1024 sc1\n\t"
"v_mfma_f32_16x16x32_bf16 v[108:111], a[72:75], a[156:159], v[108:111]\n\t"
"ds_read_b128 a[156:159], v224 offset:18336\n\t"
"v_xor_b32 v251, 96, v248\n\t"
"v_mfma_f32_16x16x32_bf16 v[108:111], a[76:79], a[172:175], v[108:111]\n\t"
"buffer_load_dwordx4 a[40:43], v252, s[84:87], s54 offen offset:0 sc1\n\t"
"v_mfma_f32_16x16x32_bf16 v[192:195], a[224:227], a[176:179], 0\n\t"
"s_nop 0\n\t"
"ds_read_b64_tr_b16 a[178:179], v248 offset:20864\n\t"
"ds_read_b64_tr_b16 a[176:177], v248 offset:20736\n\t"
"v_mfma_f32_16x16x32_bf16 v[196:199], a[224:227], a[180:183], 0\n\t"
"ds_read_b128 a[172:175], v224 offset:20384\n\t"
"v_mfma_f32_16x16x32_bf16 v[200:203], a[224:227], a[184:187], 0\n\t"
"s_waitcnt lgkmcnt(6)\n\t"
"ds_read_b64_tr_b16 a[180:181], v249 offset:20992\n\t"
"ds_read_b64_tr_b16 a[186:187], v249 offset:20864\n\t"
"v_mfma_f32_16x16x32_bf16 v[204:207], a[224:227], a[188:191], 0\n\t"
"s_nop 0\n\t"
"ds_read_b64_tr_b16 a[184:185], v249 offset:20736\n\t"
"ds_read_b64_tr_b16 a[188:189], v248 offset:20992\n\t"
"v_mfma_f32_16x16x32_bf16 v[208:211], a[224:227], a[192:195], 0\n\t"
"s_nop 0\n\t"
"ds_read_b64_tr_b16 a[194:195], v250 offset:20864\n\t"
"ds_read_b64_tr_b16 a[192:193], v250 offset:20736\n\t"
"v_mfma_f32_16x16x32_bf16 v[212:215], a[224:227], a[196:199], 0\n\t"
"buffer_load_dwordx4 a[44:47], v252, s[84:87], s54 offen offset:1024 sc1\n\t"
"v_mfma_f32_16x16x32_bf16 v[216:219], a[224:227], a[200:203], 0\n\t"
"s_nop 0\n\t"
"ds_read_b64_tr_b16 a[196:197], v251 offset:20992\n\t"
"ds_read_b128 v[24:27], v230 offset:20480\n\t"
"v_mfma_f32_16x16x32_bf16 v[220:223], a[224:227], a[204:207], 0\n\t"
"s_waitcnt vmcnt(19) lgkmcnt(12)\n\t"
"ds_read_b64_tr_b16 a[202:203], v251 offset:20864\n\t"
"v_cvt_pk_bf16_f32 v195, v195, v199\n\t"
"v_mfma_f32_16x16x32_bf16 v[144:147], a[96:99], a[144:147], v[144:147]\n\t"
"ds_write_addtid_b32 v195 offset:12448\n\t"
"v_cvt_pk_bf16_f32 v192, v192, v196\n\t"
"v_mfma_f32_16x16x32_bf16 v[144:147], a[100:103], a[160:163], v[144:147]\n\t"
"buffer_store_dwordx4 v[8:11], v232, s[16:19], s24 offen nt\n\t"
"v_mfma_f32_16x16x32_bf16 v[160:163], a[104:107], a[144:147], v[160:163]\n\t"
"ds_read_b128 v[40:43], v231 offset:20480\n\t"
"v_cvt_pk_bf16_f32 v203, v203, v207\n\t"
"v_mfma_f32_16x16x32_bf16 v[160:163], a[108:111], a[160:163], v[160:163]\n\t"
"ds_write_addtid_b32 v192 offset:0\n\t"
"v_cvt_pk_bf16_f32 v208, v208, v212\n\t"
"v_mfma_f32_16x16x32_bf16 v[176:179], a[112:115], a[144:147], v[176:179]\n\t"
"ds_read_b128 a[144:147], v224 offset:16544\n\t"
"v_cvt_pk_bf16_f32 v193, v193, v197\n\t"
"v_mfma_f32_16x16x32_bf16 v[176:179], a[116:119], a[160:163], v[176:179]\n\t"
"buffer_load_dwordx4 a[36:39], v235, s[84:87], s54 offen offset:3072 sc1\n\t"
"v_mfma_f32_16x16x32_bf16 v[180:183], a[112:115], a[148:151], v[180:183]\n\t"
"ds_write_addtid_b32 v203 offset:14496\n\t"
"v_cvt_pk_bf16_f32 v216, v216, v220\n\t"
"v_mfma_f32_16x16x32_bf16 v[180:183], a[116:119], a[164:167], v[180:183]\n\t"
"ds_read_b128 v[12:15], v229 offset:33792\n\t"
"v_cvt_pk_bf16_f32 v200, v200, v204\n\t"
"v_mfma_f32_16x16x32_bf16 v[164:167], a[104:107], a[148:151], v[164:167]\n\t"
"ds_read_b128 a[160:163], v224 offset:18592\n\t"
"v_cvt_pk_bf16_f32 v217, v217, v221\n\t"
"v_mfma_f32_16x16x32_bf16 v[164:167], a[108:111], a[164:167], v[164:167]\n\t"
"s_waitcnt lgkmcnt(7)\n\t"
"buffer_store_dwordx4 v[24:27], v233, s[16:19], s24 offen nt\n\t"
"v_mfma_f32_16x16x32_bf16 v[148:151], a[96:99], a[148:151], v[148:151]\n\t"
"ds_read_b128 a[148:151], v224 offset:16800\n\t"
"v_cvt_pk_bf16_f32 v194, v194, v198\n\t"
"v_mfma_f32_16x16x32_bf16 v[148:151], a[100:103], a[164:167], v[148:151]\n\t"
"ds_write_addtid_b32 v208 offset:1024\n\t"
"v_cvt_pk_bf16_f32 v219, v219, v223\n\t"
"v_mfma_f32_16x16x32_bf16 v[152:155], a[96:99], a[152:155], v[152:155]\n\t"
"ds_read_b128 a[164:167], v224 offset:18848\n\t"
"v_cvt_pk_bf16_f32 v209, v209, v213\n\t"
"v_mfma_f32_16x16x32_bf16 v[152:155], a[100:103], a[168:171], v[152:155]\n\t"
"s_waitcnt lgkmcnt(7)\n\t"
"buffer_store_dwordx4 v[40:43], v234, s[16:19], s24 offen nt\n\t"
"v_mfma_f32_16x16x32_bf16 v[168:171], a[104:107], a[152:155], v[168:171]\n\t"
"ds_read_b128 v[28:31], v230 offset:33792\n\t"
"v_cvt_pk_bf16_f32 v211, v211, v215\n\t"
"v_mfma_f32_16x16x32_bf16 v[168:171], a[108:111], a[168:171], v[168:171]\n\t"
"s_nop 0\n\t"
"ds_write_addtid_b32 v193 offset:8320\n\t"
"ds_write_addtid_b32 v217 offset:11392\n\t"
"v_mfma_f32_16x16x32_bf16 v[184:187], a[112:115], a[152:155], v[184:187]\n\t"
"ds_write_addtid_b32 v219 offset:15520\n\t"
"v_cvt_pk_bf16_f32 v210, v210, v214\n\t"
"v_mfma_f32_16x16x32_bf16 v[184:187], a[116:119], a[168:171], v[184:187]\n\t"
"s_waitcnt lgkmcnt(8)\n\t"
"buffer_store_dwordx4 v[12:15], v232, s[16:19], s27 offen nt\n\t"
"v_mfma_f32_16x16x32_bf16 v[188:191], a[112:115], a[156:159], v[188:191]\n\t"
"s_nop 0\n\t"
"ds_write_addtid_b32 v209 offset:9344\n\t"
"ds_read_b128 v[44:47], v231 offset:33792\n\t"
"v_mfma_f32_16x16x32_bf16 v[188:191], a[116:119], a[172:175], v[188:191]\n\t"
"ds_write_addtid_b32 v211 offset:13472\n\t"
"v_cvt_pk_bf16_f32 v218, v218, v222\n\t"
"v_mfma_f32_16x16x32_bf16 v[172:175], a[104:107], a[156:159], v[172:175]\n\t"
"ds_write_addtid_b32 v200 offset:2048\n\t"
"v_cvt_pk_bf16_f32 v202, v202, v206\n\t"
"v_mfma_f32_16x16x32_bf16 v[172:175], a[108:111], a[172:175], v[172:175]\n\t"
"buffer_load_dwordx4 a[32:35], v235, s[84:87], s54 offen offset:2048 sc1\n\t"
"buffer_load_dwordx4 a[24:27], v235, s[84:87], s54 offen offset:0 sc1\n\t"
"v_mfma_f32_16x16x32_bf16 v[156:159], a[96:99], a[156:159], v[156:159]\n\t"
"ds_write_addtid_b32 v210 offset:5152\n\t"
"v_cvt_pk_bf16_f32 v201, v201, v205\n\t"
"v_mfma_f32_16x16x32_bf16 v[156:159], a[100:103], a[172:175], v[156:159]\n\t"
"s_waitcnt vmcnt(20) lgkmcnt(9)\n\t"
"buffer_load_dwordx4 a[28:31], v235, s[84:87], s54 offen offset:1024 sc1\n\t"
"v_mfma_f32_16x16x32_bf16 v[96:99], a[120:123], a[144:147], v[96:99]\n\t"
"s_nop 0\n\t"
"ds_write_addtid_b32 v194 offset:4128\n\t"
"ds_write_addtid_b32 v218 offset:7200\n\t"
"v_mfma_f32_16x16x32_bf16 v[96:99], a[124:127], a[160:163], v[96:99]\n\t"
"s_nop 0\n\t"
"ds_write_addtid_b32 v201 offset:10368\n\t"
"ds_write_addtid_b32 v202 offset:6176\n\t"
"v_mfma_f32_16x16x32_bf16 v[112:115], a[128:131], a[144:147], v[112:115]\n\t"
"s_nop 0\n\t"
"ds_write_addtid_b32 v216 offset:3072\n\t"
"ds_read_b64_tr_b16 a[200:201], v251 offset:20736\n\t"
"v_mfma_f32_16x16x32_bf16 v[112:115], a[132:135], a[160:163], v[112:115]\n\t"
"s_waitcnt lgkmcnt(11)\n\t"
"buffer_store_dwordx4 v[28:31], v233, s[16:19], s27 offen nt\n\t"
"v_mfma_f32_16x16x32_bf16 v[128:131], a[136:139], a[144:147], v[128:131]\n\t"
"s_nop 0\n\t"
"ds_read_b128 a[168:171], v224 offset:19104\n\t"
"ds_read_b128 a[172:175], v224 offset:19360\n\t"
"v_mfma_f32_16x16x32_bf16 v[128:131], a[140:143], a[160:163], v[128:131]\n\t"
"s_nop 0\n\t"
"ds_read_b128 a[152:155], v224 offset:17056\n\t"
"ds_read_b128 a[156:159], v224 offset:17312\n\t"
"v_mfma_f32_16x16x32_bf16 v[132:135], a[136:139], a[148:151], v[132:135]\n\t"
"buffer_load_dwordx4 a[64:67], v252, s[84:87], s57 offen offset:0 sc1\n\t"
"v_mfma_f32_16x16x32_bf16 v[132:135], a[140:143], a[164:167], v[132:135]\n\t"
"s_waitcnt lgkmcnt(11)\n\t"
"buffer_store_dwordx4 v[44:47], v234, s[16:19], s27 offen nt\n\t"
"v_mfma_f32_16x16x32_bf16 v[116:119], a[128:131], a[148:151], v[116:119]\n\t"
"ds_read_b64_tr_b16 a[204:205], v250 offset:20992\n\t"
"v_mfma_f32_16x16x32_bf16 v[116:119], a[132:135], a[164:167], v[116:119]\n\t"
"v_mfma_f32_16x16x32_bf16 v[100:103], a[120:123], a[148:151], v[100:103]\n\t"
"buffer_load_dwordx4 a[68:71], v252, s[84:87], s57 offen offset:1024 sc1\n\t"
"v_mfma_f32_16x16x32_bf16 v[100:103], a[124:127], a[164:167], v[100:103]\n\t"
"s_waitcnt vmcnt(20) lgkmcnt(7)\n\t"
"s_waitcnt lgkmcnt(0)\n\t"
"s_barrier\n\t"
"v_mfma_f32_16x16x32_bf16 v[104:107], a[120:123], a[152:155], v[104:107]\n\t"
"ds_read_b128 a[144:147], v224 offset:0\n\t"
"v_mfma_f32_16x16x32_bf16 v[104:107], a[124:127], a[168:171], v[104:107]\n\t"
"ds_read_b128 a[148:151], v224 offset:256\n\t"
"v_mfma_f32_16x16x32_bf16 v[120:123], a[128:131], a[152:155], v[120:123]\n\t"
"ds_read_b128 a[160:163], v224 offset:2048\n\t"
"v_mfma_f32_16x16x32_bf16 v[120:123], a[132:135], a[168:171], v[120:123]\n\t"
"ds_read_b128 a[164:167], v224 offset:2304\n\t"
"v_mfma_f32_16x16x32_bf16 v[136:139], a[136:139], a[152:155], v[136:139]\n\t"
"v_mfma_f32_16x16x32_bf16 v[136:139], a[140:143], a[168:171], v[136:139]\n\t"
"buffer_load_dwordx4 a[60:63], v235, s[84:87], s57 offen offset:3072 sc1\n\t"
"v_mfma_f32_16x16x32_bf16 v[140:143], a[136:139], a[156:159], v[140:143]\n\t"
"ds_read_b128 a[168:171], v224 offset:2560\n\t"
"v_mfma_f32_16x16x32_bf16 v[140:143], a[140:143], a[172:175], v[140:143]\n\t"
"ds_read_b128 a[152:155], v224 offset:512\n\t"
"v_mfma_f32_16x16x32_bf16 v[124:127], a[128:131], a[156:159], v[124:127]\n\t"
"v_mfma_f32_16x16x32_bf16 v[124:127], a[132:135], a[172:175], v[124:127]\n\t"
"v_mfma_f32_16x16x32_bf16 v[108:111], a[120:123], a[156:159], v[108:111]\n\t"
"ds_read_b128 a[156:159], v224 offset:768\n\t"
"v_mfma_f32_16x16x32_bf16 v[108:111], a[124:127], a[172:175], v[108:111]\n\t"
"buffer_load_dwordx4 a[56:59], v235, s[84:87], s57 offen offset:2048 sc1\n\t"
"v_mfma_f32_4x4x4_16b_bf16 v[200:203], a[220:221], a[178:179], 0\n\t"
"ds_read_b64_tr_b16 a[178:179], v248 offset:128\n\t"
"v_mfma_f32_4x4x4_16b_bf16 v[204:207], a[220:221], a[186:187], 0\n\t"
"ds_read_b64_tr_b16 a[186:187], v249 offset:128\n\t"
"v_mfma_f32_4x4x4_16b_bf16 v[216:219], a[220:221], a[194:195], 0\n\t"
"ds_read_b128 a[172:175], v224 offset:2816\n\t"
"v_mfma_f32_4x4x4_16b_bf16 v[220:223], a[220:221], a[202:203], 0\n\t"
"ds_read_b64_tr_b16 a[194:195], v250 offset:128\n\t"
"v_mfma_f32_4x4x4_16b_bf16 v[192:195], a[222:223], a[176:177], v[200:203]\n\t"
"ds_read_b64_tr_b16 a[202:203], v251 offset:128\n\t"
"v_mfma_f32_4x4x4_16b_bf16 v[196:199], a[222:223], a[184:185], v[204:207]\n\t"
"ds_read_b64_tr_b16 a[176:177], v248 offset:0\n\t"
"v_mfma_f32_4x4x4_16b_bf16 v[200:203], a[222:223], a[180:181], v[200:203]\n\t"
".p2align 3\n\t"
"buffer_load_dwordx4 a[48:51], v235, s[84:87], s57 offen offset:0 sc1\n\t"
"v_pk_add_f32 v[96:97], v[96:97], v[144:145]\n\t"
"v_pk_add_f32 v[98:99], v[98:99], v[146:147]\n\t"
"v_pk_fma_f32 v[144:145], v[144:145], s[38:39], v[96:97]\n\t"
"v_pk_fma_f32 v[146:147], v[146:147], s[38:39], v[98:99]\n\t"
"v_pk_add_f32 v[112:113], v[112:113], v[160:161]\n\t"
"v_pk_add_f32 v[114:115], v[114:115], v[162:163]\n\t"
"v_pk_fma_f32 v[160:161], v[160:161], s[38:39], v[112:113]\n\t"
"v_pk_fma_f32 v[162:163], v[162:163], s[38:39], v[114:115]\n\t"
"v_pk_add_f32 v[128:129], v[128:129], v[176:177]\n\t"
"v_pk_add_f32 v[130:131], v[130:131], v[178:179]\n\t"
"v_pk_fma_f32 v[176:177], v[176:177], s[38:39], v[128:129]\n\t"
"v_pk_fma_f32 v[178:179], v[178:179], s[38:39], v[130:131]\n\t"
"v_pk_add_f32 v[132:133], v[132:133], v[180:181]\n\t"
"v_pk_add_f32 v[134:135], v[134:135], v[182:183]\n\t"
"v_pk_fma_f32 v[180:181], v[180:181], s[38:39], v[132:133]\n\t"
"v_pk_fma_f32 v[182:183], v[182:183], s[38:39], v[134:135]\n\t"
"buffer_load_dwordx4 a[52:55], v235, s[84:87], s57 offen offset:1024 sc1\n\t"
"v_pk_add_f32 v[116:117], v[116:117], v[164:165]\n\t"
"v_pk_add_f32 v[118:119], v[118:119], v[166:167]\n\t"
"v_pk_fma_f32 v[164:165], v[164:165], s[38:39], v[116:117]\n\t"
"v_pk_fma_f32 v[166:167], v[166:167], s[38:39], v[118:119]\n\t"
"v_pk_add_f32 v[100:101], v[100:101], v[148:149]\n\t"
"v_pk_add_f32 v[102:103], v[102:103], v[150:151]\n\t"
"v_pk_fma_f32 v[148:149], v[148:149], s[38:39], v[100:101]\n\t"
"v_pk_fma_f32 v[150:151], v[150:151], s[38:39], v[102:103]\n\t"
"v_pk_add_f32 v[104:105], v[104:105], v[152:153]\n\t"
"v_pk_add_f32 v[106:107], v[106:107], v[154:155]\n\t"
"v_pk_fma_f32 v[152:153], v[152:153], s[38:39], v[104:105]\n\t"
"v_pk_fma_f32 v[154:155], v[154:155], s[38:39], v[106:107]\n\t"
"v_pk_add_f32 v[120:121], v[120:121], v[168:169]\n\t"
"v_pk_add_f32 v[122:123], v[122:123], v[170:171]\n\t"
"v_pk_fma_f32 v[168:169], v[168:169], s[38:39], v[120:121]\n\t"
"v_pk_fma_f32 v[170:171], v[170:171], s[38:39], v[122:123]\n\t"
"buffer_load_dwordx4 a[88:91], v252, s[84:87], s63 offen offset:0 sc1\n\t"
"v_pk_add_f32 v[136:137], v[136:137], v[184:185]\n\t"
"v_pk_add_f32 v[138:139], v[138:139], v[186:187]\n\t"
"v_pk_fma_f32 v[184:185], v[184:185], s[38:39], v[136:137]\n\t"
"v_pk_fma_f32 v[186:187], v[186:187], s[38:39], v[138:139]\n\t"
"v_pk_add_f32 v[140:141], v[140:141], v[188:189]\n\t"
"v_pk_add_f32 v[142:143], v[142:143], v[190:191]\n\t"
"v_pk_fma_f32 v[188:189], v[188:189], s[38:39], v[140:141]\n\t"
"v_pk_fma_f32 v[190:191], v[190:191], s[38:39], v[142:143]\n\t"
"v_pk_add_f32 v[124:125], v[124:125], v[172:173]\n\t"
"v_pk_add_f32 v[126:127], v[126:127], v[174:175]\n\t"
"v_pk_fma_f32 v[172:173], v[172:173], s[38:39], v[124:125]\n\t"
"v_pk_fma_f32 v[174:175], v[174:175], s[38:39], v[126:127]\n\t"
"v_pk_add_f32 v[108:109], v[108:109], v[156:157]\n\t"
"v_pk_add_f32 v[110:111], v[110:111], v[158:159]\n\t"
"v_pk_fma_f32 v[156:157], v[156:157], s[38:39], v[108:109]\n\t"
"v_pk_fma_f32 v[158:159], v[158:159], s[38:39], v[110:111]\n\t"
"v_cvt_pk_bf16_f32 v136, v136, v137\n\t"
"v_mfma_f32_4x4x4_16b_bf16 v[204:207], a[222:223], a[188:189], v[204:207]\n\t"
"v_cvt_pk_bf16_f32 v96, v96, v97\n\t"
"v_mfma_f32_4x4x4_16b_bf16 v[208:211], a[222:223], a[192:193], v[216:219]\n\t"
"v_cvt_pk_bf16_f32 v97, v98, v99\n\t"
"v_cvt_pk_bf16_f32 v112, v112, v113\n\t"
"v_mfma_f32_4x4x4_16b_bf16 v[212:215], a[222:223], a[200:201], v[220:223]\n\t"
"v_cvt_pk_bf16_f32 v100, v100, v101\n\t"
"v_cvt_pk_bf16_f32 v101, v102, v103\n\t"
"v_mfma_f32_4x4x4_16b_bf16 v[216:219], a[222:223], a[196:197], v[216:219]\n\t"
"ds_read_b64_tr_b16 a[184:185], v249 offset:0\n\t"
"ds_write2st64_b64 v228, v[96:97], v[100:101] offset0:40 offset1:53\n\t"
"v_mfma_f32_4x4x4_16b_bf16 v[220:223], a[222:223], a[204:205], v[220:223]\n\t"
"s_waitcnt vmcnt(19) lgkmcnt(6)\n\t"
"buffer_load_dwordx4 a[92:95], v252, s[84:87], s63 offen offset:1024 sc1\n\t"
"v_cvt_pk_bf16_f32 v113, v114, v115\n\t"
"v_cvt_pk_bf16_f32 v116, v116, v117\n\t"
"v_mfma_f32_16x16x32_bf16 v[48:51], a[0:3], a[144:147], v[48:51]\n\t"
"v_cvt_pk_bf16_f32 v117, v118, v119\n\t"
"v_cvt_pk_bf16_f32 v140, v140, v141\n\t"
"v_mfma_f32_16x16x32_bf16 v[48:51], a[4:7], a[160:163], v[48:51]\n\t"
"ds_write2st64_b64 v246, v[112:113], v[116:117] offset0:40 offset1:53\n\t"
"v_mfma_f32_16x16x32_bf16 v[64:67], a[8:11], a[144:147], v[64:67]\n\t"
"v_cvt_pk_bf16_f32 v200, v200, v204\n\t"
"v_cvt_pk_bf16_f32 v132, v132, v133\n\t"
"v_mfma_f32_16x16x32_bf16 v[64:67], a[12:15], a[160:163], v[64:67]\n\t"
"v_cvt_pk_bf16_f32 v194, -v194, -v198\n\t"
"v_cvt_pk_bf16_f32 v133, v134, v135\n\t"
"v_mfma_f32_16x16x32_bf16 v[80:83], a[16:19], a[144:147], v[80:83]\n\t"
"v_cvt_pk_bf16_f32 v208, -v208, -v212\n\t"
"v_cvt_pk_bf16_f32 v128, v128, v129\n\t"
"v_mfma_f32_16x16x32_bf16 v[80:83], a[20:23], a[160:163], v[80:83]\n\t"
"ds_read_b128 a[160:163], v224 offset:3072\n\t"
"v_cvt_pk_bf16_f32 v104, v104, v105\n\t"
"v_mfma_f32_16x16x32_bf16 v[84:87], a[16:19], a[148:151], v[84:87]\n\t"
"v_cvt_pk_bf16_f32 v211, -v211, -v215\n\t"
"v_cvt_pk_bf16_f32 v129, v130, v131\n\t"
"v_mfma_f32_16x16x32_bf16 v[84:87], a[20:23], a[164:167], v[84:87]\n\t"
"ds_read_b128 a[144:147], v224 offset:1024\n\t"
"ds_write2st64_b64 v247, v[128:129], v[132:133] offset0:40 offset1:53\n\t"
"v_cvt_pk_bf16_f32 v120, v120, v121\n\t"
"v_mfma_f32_16x16x32_bf16 v[68:71], a[8:11], a[148:151], v[68:71]\n\t"
"v_cvt_pk_bf16_f32 v108, v108, v109\n\t"
"v_cvt_pk_bf16_f32 v124, v124, v125\n\t"
"v_mfma_f32_16x16x32_bf16 v[68:71], a[12:15], a[164:167], v[68:71]\n\t"
"v_cvt_pk_bf16_f32 v218, v218, v222\n\t"
"v_cvt_pk_bf16_f32 v137, v138, v139\n\t"
"v_mfma_f32_16x16x32_bf16 v[52:55], a[0:3], a[148:151], v[52:55]\n\t"
"v_cvt_pk_bf16_f32 v105, v106, v107\n\t"
"v_cvt_pk_bf16_f32 v109, v110, v111\n\t"
"v_mfma_f32_16x16x32_bf16 v[52:55], a[4:7], a[164:167], v[52:55]\n\t"
"s_nop 0\n\t"
"ds_read_b128 a[148:151], v224 offset:1280\n\t"
"ds_read_b128 a[164:167], v224 offset:3328\n\t"
"ds_write_addtid_b32 v194 offset:17568\n\t"
"ds_write_addtid_b32 v211 offset:23744\n\t"
"ds_write2st64_b64 v228, v[104:105], v[108:109] offset0:66 offset1:79\n\t"
"v_mfma_f32_16x16x32_bf16 v[56:59], a[0:3], a[152:155], v[56:59]\n\t"
"v_cvt_pk_bf16_f32 v209, -v209, -v213\n\t"
"v_cvt_pk_bf16_f32 v121, v122, v123\n\t"
"v_mfma_f32_16x16x32_bf16 v[56:59], a[4:7], a[168:171], v[56:59]\n\t"
"buffer_load_dwordx4 a[84:87], v235, s[84:87], s63 offen offset:3072 sc1\n\t"
"v_mfma_f32_16x16x32_bf16 v[72:75], a[8:11], a[152:155], v[72:75]\n\t"
"v_cvt_pk_bf16_f32 v219, v219, v223\n\t"
"v_cvt_pk_bf16_f32 v141, v142, v143\n\t"
"v_mfma_f32_16x16x32_bf16 v[72:75], a[12:15], a[168:171], v[72:75]\n\t"
"ds_write_addtid_b32 v218 offset:27936\n\t"
"ds_write2st64_b64 v247, v[136:137], v[140:141] offset0:66 offset1:79\n\t"
"v_mfma_f32_16x16x32_bf16 v[88:91], a[16:19], a[152:155], v[88:91]\n\t"
"v_cvt_pk_bf16_f32 v192, -v192, -v196\n\t"
"v_cvt_pk_bf16_f32 v125, v126, v127\n\t"
"v_mfma_f32_16x16x32_bf16 v[88:91], a[20:23], a[168:171], v[88:91]\n\t"
"s_waitcnt lgkmcnt(7)\n\t"
"buffer_load_dwordx4 a[80:83], v235, s[84:87], s63 offen offset:2048 sc1\n\t"
"v_mfma_f32_16x16x32_bf16 v[92:95], a[16:19], a[156:159], v[92:95]\n\t"
"v_cvt_pk_bf16_f32 v195, -v195, -v199\n\t"
"v_cvt_pk_bf16_f32 v217, v217, v221\n\t"
"v_mfma_f32_16x16x32_bf16 v[92:95], a[20:23], a[172:175], v[92:95]\n\t"
"ds_write_addtid_b32 v195 offset:21696\n\t"
"ds_write2st64_b64 v246, v[120:121], v[124:125] offset0:66 offset1:79\n\t"
"v_mfma_f32_16x16x32_bf16 v[76:79], a[8:11], a[156:159], v[76:79]\n\t"
"v_cvt_pk_bf16_f32 v201, v201, v205\n\t"
"v_cvt_pk_bf16_f32 v202, v202, v206\n\t"
"v_mfma_f32_16x16x32_bf16 v[76:79], a[12:15], a[172:175], v[76:79]\n\t"
"buffer_load_dwordx4 a[72:75], v235, s[84:87], s63 offen offset:0 sc1\n\t"
"v_mfma_f32_16x16x32_bf16 v[60:63], a[0:3], a[156:159], v[60:63]\n\t"
"v_cvt_pk_bf16_f32 v193, -v193, -v197\n\t"
"v_cvt_pk_bf16_f32 v210, -v210, -v214\n\t"
"v_mfma_f32_16x16x32_bf16 v[60:63], a[4:7], a[172:175], v[60:63]\n\t"
"s_waitcnt vmcnt(13) lgkmcnt(4)\n\t"
"ds_write_addtid_b32 v208 offset:18592\n\t"
"v_cvt_pk_bf16_f32 v216, v216, v220\n\t"
"v_mfma_f32_16x16x32_bf16 v[48:51], a[24:27], a[144:147], v[48:51]\n\t"
"ds_write_addtid_b32 v209 offset:22720\n\t"
"v_cvt_pk_bf16_f32 v203, v203, v207\n\t"
"v_mfma_f32_16x16x32_bf16 v[48:51], a[28:31], a[160:163], v[48:51]\n\t"
"s_nop 0\n\t"
".p2align 3\n\t"
"ds_write_addtid_b32 v193 offset:20672\n\t"
"ds_write_addtid_b32 v200 offset:24864\n\t"
"ds_write_addtid_b32 v201 offset:28992\n\t"
"ds_write_addtid_b32 v202 offset:25888\n\t"
"ds_write_addtid_b32 v203 offset:30016\n\t"
"ds_write_addtid_b32 v210 offset:19616\n\t"
"ds_write_addtid_b32 v216 offset:26912\n\t"
"ds_write_addtid_b32 v217 offset:31040\n\t"
"v_mfma_f32_16x16x32_bf16 v[64:67], a[32:35], a[144:147], v[64:67]\n\t"
"s_nop 0\n\t"
"ds_write_addtid_b32 v192 offset:16544\n\t"
"ds_write_addtid_b32 v219 offset:32064\n\t"
"v_mfma_f32_16x16x32_bf16 v[64:67], a[36:39], a[160:163], v[64:67]\n\t"
"buffer_load_dwordx4 a[76:79], v235, s[84:87], s63 offen offset:1024 sc1\n\t"
"v_mfma_f32_16x16x32_bf16 v[80:83], a[40:43], a[144:147], v[80:83]\n\t"
"s_nop 0\n\t"
"ds_read_b128 a[168:171], v224 offset:3584\n\t"
"ds_read_b128 a[156:159], v224 offset:1792\n\t"
"v_mfma_f32_16x16x32_bf16 v[80:83], a[44:47], a[160:163], v[80:83]\n\t"
"s_nop 0\n\t"
"ds_read_b128 a[152:155], v224 offset:1536\n\t"
"ds_read_b128 a[172:175], v224 offset:3840\n\t"
"v_mfma_f32_16x16x32_bf16 v[84:87], a[40:43], a[148:151], v[84:87]\n\t"
"s_nop 0\n\t"
"ds_read_b64_tr_b16 a[180:181], v249 offset:256\n\t"
"ds_read_b64_tr_b16 a[188:189], v248 offset:256\n\t"
"v_mfma_f32_16x16x32_bf16 v[84:87], a[44:47], a[164:167], v[84:87]\n\t"
"s_nop 0\n\t"
"ds_read_b64_tr_b16 a[192:193], v250 offset:0\n\t"
"ds_read_b64_tr_b16 a[200:201], v251 offset:0\n\t"
"v_mfma_f32_16x16x32_bf16 v[68:71], a[32:35], a[148:151], v[68:71]\n\t"
"s_nop 0\n\t"
"ds_read_b64_tr_b16 a[196:197], v251 offset:256\n\t"
"ds_read_b64_tr_b16 a[204:205], v250 offset:256\n\t"
"v_mfma_f32_16x16x32_bf16 v[68:71], a[36:39], a[164:167], v[68:71]\n\t"
"buffer_load_dwordx4 a[112:115], v252, s[84:87], s42 offen offset:0 sc1\n\t"
"v_mfma_f32_16x16x32_bf16 v[52:55], a[24:27], a[148:151], v[52:55]\n\t"
"v_mfma_f32_16x16x32_bf16 v[52:55], a[28:31], a[164:167], v[52:55]\n\t"
"buffer_load_dwordx4 a[116:119], v252, s[84:87], s42 offen offset:1024 sc1\n\t"
"s_waitcnt lgkmcnt(0)\n\t"
"s_barrier\n\t"
"v_mfma_f32_16x16x32_bf16 v[56:59], a[24:27], a[152:155], v[56:59]\n\t"
"ds_read_b128 a[144:147], v224 offset:16544\n\t"
"v_mfma_f32_16x16x32_bf16 v[56:59], a[28:31], a[168:171], v[56:59]\n\t"
"ds_read_b128 a[160:163], v224 offset:18592\n\t"
"v_mfma_f32_16x16x32_bf16 v[72:75], a[32:35], a[152:155], v[72:75]\n\t"
"ds_read_b128 a[148:151], v224 offset:16800\n\t"
"v_mfma_f32_16x16x32_bf16 v[72:75], a[36:39], a[168:171], v[72:75]\n\t"
"ds_read_b128 a[164:167], v224 offset:18848\n\t"
"v_mfma_f32_16x16x32_bf16 v[88:91], a[40:43], a[152:155], v[88:91]\n\t"
"ds_read_b128 v[104:107], v229 offset:20480\n\t"
"v_mfma_f32_16x16x32_bf16 v[88:91], a[44:47], a[168:171], v[88:91]\n\t"
"v_mfma_f32_16x16x32_bf16 v[92:95], a[40:43], a[156:159], v[92:95]\n\t"
"ds_read_b128 a[168:171], v224 offset:19104\n\t"
"v_mfma_f32_16x16x32_bf16 v[92:95], a[44:47], a[172:175], v[92:95]\n\t"
"buffer_load_dwordx4 a[108:111], v235, s[84:87], s42 offen offset:3072 sc1\n\t"
"v_mfma_f32_16x16x32_bf16 v[76:79], a[32:35], a[156:159], v[76:79]\n\t"
"v_mfma_f32_16x16x32_bf16 v[76:79], a[36:39], a[172:175], v[76:79]\n\t"
"ds_read_b128 a[152:155], v224 offset:17056\n\t"
"v_mfma_f32_16x16x32_bf16 v[60:63], a[24:27], a[156:159], v[60:63]\n\t"
"ds_read_b128 a[156:159], v224 offset:17312\n\t"
"v_mfma_f32_16x16x32_bf16 v[60:63], a[28:31], a[172:175], v[60:63]\n\t"
"s_waitcnt lgkmcnt(3)\n\t"
"buffer_store_dwordx4 v[104:107], v232, s[16:19], s20 offen nt\n\t"
"v_mfma_f32_4x4x4_16b_bf16 v[200:203], a[220:221], a[178:179], 0\n\t"
"ds_read_b64_tr_b16 a[178:179], v248 offset:41600\n\t"
"v_mfma_f32_4x4x4_16b_bf16 v[204:207], a[220:221], a[186:187], 0\n\t"
"ds_read_b64_tr_b16 a[186:187], v249 offset:41600\n\t"
"v_mfma_f32_4x4x4_16b_bf16 v[216:219], a[220:221], a[194:195], 0\n\t"
"ds_read_b64_tr_b16 a[194:195], v250 offset:41600\n\t"
"v_mfma_f32_4x4x4_16b_bf16 v[220:223], a[220:221], a[202:203], 0\n\t"
".p2align 3\n\t"
"buffer_load_dwordx4 a[104:107], v235, s[84:87], s42 offen offset:2048 sc1\n\t"
"v_pk_add_f32 v[144:145], v[48:49], v[144:145] neg_lo:[0,1] neg_hi:[0,1]\n\t"
"v_pk_add_f32 v[146:147], v[50:51], v[146:147] neg_lo:[0,1] neg_hi:[0,1]\n\t"
"v_pk_fma_f32 v[48:49], v[144:145], s[90:91], v[48:49] neg_lo:[1,0,0] neg_hi:[1,0,0]\n\t"
"v_pk_fma_f32 v[50:51], v[146:147], s[90:91], v[50:51] neg_lo:[1,0,0] neg_hi:[1,0,0]\n\t"
"v_pk_add_f32 v[160:161], v[64:65], v[160:161] neg_lo:[0,1] neg_hi:[0,1]\n\t"
"v_pk_add_f32 v[162:163], v[66:67], v[162:163] neg_lo:[0,1] neg_hi:[0,1]\n\t"
"v_pk_fma_f32 v[64:65], v[160:161], s[90:91], v[64:65] neg_lo:[1,0,0] neg_hi:[1,0,0]\n\t"
"v_pk_fma_f32 v[66:67], v[162:163], s[90:91], v[66:67] neg_lo:[1,0,0] neg_hi:[1,0,0]\n\t"
"v_pk_add_f32 v[176:177], v[80:81], v[176:177] neg_lo:[0,1] neg_hi:[0,1]\n\t"
"v_pk_add_f32 v[178:179], v[82:83], v[178:179] neg_lo:[0,1] neg_hi:[0,1]\n\t"
"v_pk_fma_f32 v[80:81], v[176:177], s[90:91], v[80:81] neg_lo:[1,0,0] neg_hi:[1,0,0]\n\t"
"v_pk_fma_f32 v[82:83], v[178:179], s[90:91], v[82:83] neg_lo:[1,0,0] neg_hi:[1,0,0]\n\t"
"v_pk_add_f32 v[180:181], v[84:85], v[180:181] neg_lo:[0,1] neg_hi:[0,1]\n\t"
"v_pk_add_f32 v[182:183], v[86:87], v[182:183] neg_lo:[0,1] neg_hi:[0,1]\n\t"
"v_pk_fma_f32 v[84:85], v[180:181], s[90:91], v[84:85] neg_lo:[1,0,0] neg_hi:[1,0,0]\n\t"
"v_pk_fma_f32 v[86:87], v[182:183], s[90:91], v[86:87] neg_lo:[1,0,0] neg_hi:[1,0,0]\n\t"
"buffer_load_dwordx4 a[96:99], v235, s[84:87], s42 offen offset:0 sc1\n\t"
"v_pk_add_f32 v[164:165], v[68:69], v[164:165] neg_lo:[0,1] neg_hi:[0,1]\n\t"
"v_pk_add_f32 v[166:167], v[70:71], v[166:167] neg_lo:[0,1] neg_hi:[0,1]\n\t"
"v_pk_fma_f32 v[68:69], v[164:165], s[90:91], v[68:69] neg_lo:[1,0,0] neg_hi:[1,0,0]\n\t"
"v_pk_fma_f32 v[70:71], v[166:167], s[90:91], v[70:71] neg_lo:[1,0,0] neg_hi:[1,0,0]\n\t"
"v_pk_add_f32 v[148:149], v[52:53], v[148:149] neg_lo:[0,1] neg_hi:[0,1]\n\t"
"v_pk_add_f32 v[150:151], v[54:55], v[150:151] neg_lo:[0,1] neg_hi:[0,1]\n\t"
"v_pk_fma_f32 v[52:53], v[148:149], s[90:91], v[52:53] neg_lo:[1,0,0] neg_hi:[1,0,0]\n\t"
"v_pk_fma_f32 v[54:55], v[150:151], s[90:91], v[54:55] neg_lo:[1,0,0] neg_hi:[1,0,0]\n\t"
"v_pk_add_f32 v[152:153], v[56:57], v[152:153] neg_lo:[0,1] neg_hi:[0,1]\n\t"
"v_pk_add_f32 v[154:155], v[58:59], v[154:155] neg_lo:[0,1] neg_hi:[0,1]\n\t"
"v_pk_fma_f32 v[56:57], v[152:153], s[90:91], v[56:57] neg_lo:[1,0,0] neg_hi:[1,0,0]\n\t"
"v_pk_fma_f32 v[58:59], v[154:155], s[90:91], v[58:59] neg_lo:[1,0,0] neg_hi:[1,0,0]\n\t"
"v_pk_add_f32 v[168:169], v[72:73], v[168:169] neg_lo:[0,1] neg_hi:[0,1]\n\t"
"v_pk_add_f32 v[170:171], v[74:75], v[170:171] neg_lo:[0,1] neg_hi:[0,1]\n\t"
"v_pk_fma_f32 v[72:73], v[168:169], s[90:91], v[72:73] neg_lo:[1,0,0] neg_hi:[1,0,0]\n\t"
"v_pk_fma_f32 v[74:75], v[170:171], s[90:91], v[74:75] neg_lo:[1,0,0] neg_hi:[1,0,0]\n\t"
"buffer_load_dwordx4 a[100:103], v235, s[84:87], s42 offen offset:1024 sc1\n\t"
"v_pk_add_f32 v[184:185], v[88:89], v[184:185] neg_lo:[0,1] neg_hi:[0,1]\n\t"
"v_pk_add_f32 v[186:187], v[90:91], v[186:187] neg_lo:[0,1] neg_hi:[0,1]\n\t"
"v_pk_fma_f32 v[88:89], v[184:185], s[90:91], v[88:89] neg_lo:[1,0,0] neg_hi:[1,0,0]\n\t"
"v_pk_fma_f32 v[90:91], v[186:187], s[90:91], v[90:91] neg_lo:[1,0,0] neg_hi:[1,0,0]\n\t"
"v_pk_add_f32 v[188:189], v[92:93], v[188:189] neg_lo:[0,1] neg_hi:[0,1]\n\t"
"v_pk_add_f32 v[190:191], v[94:95], v[190:191] neg_lo:[0,1] neg_hi:[0,1]\n\t"
"v_pk_fma_f32 v[92:93], v[188:189], s[90:91], v[92:93] neg_lo:[1,0,0] neg_hi:[1,0,0]\n\t"
"v_pk_fma_f32 v[94:95], v[190:191], s[90:91], v[94:95] neg_lo:[1,0,0] neg_hi:[1,0,0]\n\t"
"v_pk_add_f32 v[172:173], v[76:77], v[172:173] neg_lo:[0,1] neg_hi:[0,1]\n\t"
"v_pk_add_f32 v[174:175], v[78:79], v[174:175] neg_lo:[0,1] neg_hi:[0,1]\n\t"
"v_pk_fma_f32 v[76:77], v[172:173], s[90:91], v[76:77] neg_lo:[1,0,0] neg_hi:[1,0,0]\n\t"
"v_pk_fma_f32 v[78:79], v[174:175], s[90:91], v[78:79] neg_lo:[1,0,0] neg_hi:[1,0,0]\n\t"
"v_pk_add_f32 v[156:157], v[60:61], v[156:157] neg_lo:[0,1] neg_hi:[0,1]\n\t"
"v_pk_add_f32 v[158:159], v[62:63], v[158:159] neg_lo:[0,1] neg_hi:[0,1]\n\t"
"v_pk_fma_f32 v[60:61], v[156:157], s[90:91], v[60:61] neg_lo:[1,0,0] neg_hi:[1,0,0]\n\t"
"v_pk_fma_f32 v[62:63], v[158:159], s[90:91], v[62:63] neg_lo:[1,0,0] neg_hi:[1,0,0]\n\t"
"v_mfma_f32_4x4x4_16b_bf16 v[192:195], a[222:223], a[176:177], v[200:203]\n\t"
"ds_read_b64_tr_b16 a[202:203], v251 offset:41600\n\t"
"v_mfma_f32_4x4x4_16b_bf16 v[196:199], a[222:223], a[184:185], v[204:207]\n\t"
"ds_read_b64_tr_b16 a[176:177], v248 offset:41472\n\t"
"v_mfma_f32_4x4x4_16b_bf16 v[200:203], a[222:223], a[180:181], v[200:203]\n\t"
"ds_read_b64_tr_b16 a[184:185], v249 offset:41472\n\t"
"v_mfma_f32_4x4x4_16b_bf16 v[204:207], a[222:223], a[188:189], v[204:207]\n\t"
"ds_read_b128 a[172:175], v224 offset:19360\n\t"
"v_mfma_f32_4x4x4_16b_bf16 v[208:211], a[222:223], a[192:193], v[216:219]\n\t"
"ds_read_b64_tr_b16 a[180:181], v249 offset:41728\n\t"
"v_mfma_f32_4x4x4_16b_bf16 v[212:215], a[222:223], a[200:201], v[220:223]\n\t"
"ds_read_b128 v[120:123], v230 offset:20480\n\t"
"v_mfma_f32_4x4x4_16b_bf16 v[216:219], a[222:223], a[196:197], v[216:219]\n\t"
"ds_read_b64_tr_b16 a[188:189], v248 offset:41728\n\t"
"v_mfma_f32_4x4x4_16b_bf16 v[220:223], a[222:223], a[204:205], v[220:223]\n\t"
"s_waitcnt vmcnt(13) lgkmcnt(8)\n\t"
"buffer_load_dwordx4 a[136:139], v252, s[84:87], s48 offen offset:0 sc1\n\t"
"v_mfma_f32_16x16x32_bf16 v[48:51], a[48:51], a[144:147], v[48:51]\n\t"
"ds_read_b64_tr_b16 a[192:193], v250 offset:41472\n\t"
"v_cvt_pk_bf16_f32 v193, -v193, -v197\n\t"
"v_mfma_f32_16x16x32_bf16 v[48:51], a[52:55], a[160:163], v[48:51]\n\t"
"buffer_load_dwordx4 a[140:143], v252, s[84:87], s48 offen offset:1024 sc1\n\t"
"v_mfma_f32_16x16x32_bf16 v[64:67], a[56:59], a[144:147], v[64:67]\n\t"
"ds_write_addtid_b32 v193 offset:4128\n\t"
"v_cvt_pk_bf16_f32 v203, v203, v207\n\t"
"v_mfma_f32_16x16x32_bf16 v[64:67], a[60:63], a[160:163], v[64:67]\n\t"
"ds_read_b128 v[108:111], v229 offset:33792\n\t"
"v_cvt_pk_bf16_f32 v192, -v192, -v196\n\t"
"v_mfma_f32_16x16x32_bf16 v[80:83], a[64:67], a[144:147], v[80:83]\n\t"
"ds_write_addtid_b32 v203 offset:13472\n\t"
"v_cvt_pk_bf16_f32 v211, -v211, -v215\n\t"
"v_mfma_f32_16x16x32_bf16 v[80:83], a[68:71], a[160:163], v[80:83]\n\t"
"s_waitcnt lgkmcnt(5)\n\t"
"buffer_store_dwordx4 v[120:123], v233, s[16:19], s20 offen nt\n\t"
"v_mfma_f32_16x16x32_bf16 v[84:87], a[64:67], a[148:151], v[84:87]\n\t"
"ds_read_b128 a[232:235], v230 offset:33792\n\t"
"v_cvt_pk_bf16_f32 v217, v217, v221\n\t"
"v_mfma_f32_16x16x32_bf16 v[84:87], a[68:71], a[164:167], v[84:87]\n\t"
"s_nop 0\n\t"
"ds_read_b128 a[144:147], v224 offset:17568\n\t"
"ds_write_addtid_b32 v192 offset:0\n\t"
"v_mfma_f32_16x16x32_bf16 v[68:71], a[56:59], a[148:151], v[68:71]\n\t"
"ds_read_b128 a[160:163], v224 offset:19616\n\t"
"v_cvt_pk_bf16_f32 v202, v202, v206\n\t"
"v_mfma_f32_16x16x32_bf16 v[68:71], a[60:63], a[164:167], v[68:71]\n\t"
"buffer_load_dwordx4 a[132:135], v235, s[84:87], s48 offen offset:3072 sc1\n\t"
"v_mfma_f32_16x16x32_bf16 v[52:55], a[48:51], a[148:151], v[52:55]\n\t"
"ds_read_b128 v[136:139], v231 offset:20480\n\t"
"v_cvt_pk_bf16_f32 v210, -v210, -v214\n\t"
"v_mfma_f32_16x16x32_bf16 v[52:55], a[52:55], a[164:167], v[52:55]\n\t"
"ds_read_b128 a[148:151], v224 offset:17824\n\t"
"v_cvt_pk_bf16_f32 v208, -v208, -v212\n\t"
"v_mfma_f32_16x16x32_bf16 v[56:59], a[48:51], a[152:155], v[56:59]\n\t"
"ds_write_addtid_b32 v202 offset:9344\n\t"
"v_cvt_pk_bf16_f32 v195, -v195, -v199\n\t"
"v_mfma_f32_16x16x32_bf16 v[56:59], a[52:55], a[168:171], v[56:59]\n\t"
"buffer_load_dwordx4 a[128:131], v235, s[84:87], s48 offen offset:2048 sc1\n\t"
"v_mfma_f32_16x16x32_bf16 v[72:75], a[56:59], a[152:155], v[72:75]\n\t"
"ds_read_b128 a[164:167], v224 offset:19872\n\t"
"v_cvt_pk_bf16_f32 v200, v200, v204\n\t"
"v_mfma_f32_16x16x32_bf16 v[72:75], a[60:63], a[168:171], v[72:75]\n\t"
"ds_write_addtid_b32 v195 offset:5152\n\t"
"v_cvt_pk_bf16_f32 v209, -v209, -v213\n\t"
"v_mfma_f32_16x16x32_bf16 v[88:91], a[64:67], a[152:155], v[88:91]\n\t"
"s_nop 0\n\t"
"ds_read_b128 a[152:155], v224 offset:18080\n\t"
"ds_read_b128 a[236:239], v231 offset:33792\n\t"
"v_mfma_f32_16x16x32_bf16 v[88:91], a[68:71], a[168:171], v[88:91]\n\t"
"ds_write_addtid_b32 v211 offset:7200\n\t"
"v_cvt_pk_bf16_f32 v219, v219, v223\n\t"
"v_mfma_f32_16x16x32_bf16 v[92:95], a[64:67], a[156:159], v[92:95]\n\t"
"ds_write_addtid_b32 v209 offset:6176\n\t"
"v_cvt_pk_bf16_f32 v218, v218, v222\n\t"
"v_mfma_f32_16x16x32_bf16 v[92:95], a[68:71], a[172:175], v[92:95]\n\t"
"s_nop 0\n\t"
"ds_write_addtid_b32 v210 offset:3072\n\t"
"ds_write_addtid_b32 v219 offset:15520\n\t"
"v_mfma_f32_16x16x32_bf16 v[76:79], a[56:59], a[156:159], v[76:79]\n\t"
"ds_write_addtid_b32 v218 offset:11392\n\t"
"v_cvt_pk_bf16_f32 v201, v201, v205\n\t"
"v_mfma_f32_16x16x32_bf16 v[76:79], a[60:63], a[172:175], v[76:79]\n\t"
"ds_write_addtid_b32 v217 offset:14496\n\t"
"v_cvt_pk_bf16_f32 v216, v216, v220\n\t"
"v_mfma_f32_16x16x32_bf16 v[60:63], a[48:51], a[156:159], v[60:63]\n\t"
"ds_write_addtid_b32 v200 offset:8320\n\t"
"v_cvt_pk_bf16_f32 v194, -v194, -v198\n\t"
"v_mfma_f32_16x16x32_bf16 v[60:63], a[52:55], a[172:175], v[60:63]\n\t"
"s_waitcnt vmcnt(12) lgkmcnt(11)\n\t"
"buffer_store_dwordx4 v[136:139], v234, s[16:19], s20 offen nt\n\t"
"v_mfma_f32_16x16x32_bf16 v[144:147], a[72:75], a[144:147], v[144:147]\n\t"
"s_nop 0\n\t"
"ds_write_addtid_b32 v201 offset:12448\n\t"
"ds_write_addtid_b32 v208 offset:2048\n\t"
"v_mfma_f32_16x16x32_bf16 v[144:147], a[76:79], a[160:163], v[144:147]\n\t"
"ds_write_addtid_b32 v194 offset:1024\n\t"
"v_mfma_f32_16x16x32_bf16 v[160:163], a[80:83], a[144:147], v[160:163]\n\t"
"s_nop 0\n\t"
"ds_read_b128 a[156:159], v224 offset:18336\n\t"
"ds_write_addtid_b32 v216 offset:10368\n\t"
"v_mfma_f32_16x16x32_bf16 v[160:163], a[84:87], a[160:163], v[160:163]\n\t"
"buffer_load_dwordx4 a[120:123], v235, s[84:87], s48 offen offset:0 sc1\n\t"
"v_mfma_f32_16x16x32_bf16 v[176:179], a[88:91], a[144:147], v[176:179]\n\t"
"s_nop 0\n\t"
"ds_read_b128 a[168:171], v224 offset:20128\n\t"
"ds_read_b128 a[172:175], v224 offset:20384\n\t"
"v_mfma_f32_16x16x32_bf16 v[176:179], a[92:95], a[160:163], v[176:179]\n\t"
"s_waitcnt lgkmcnt(10)\n\t"
"buffer_load_dwordx4 a[124:127], v235, s[84:87], s48 offen offset:1024 sc1\n\t"
"v_mfma_f32_16x16x32_bf16 v[180:183], a[88:91], a[148:151], v[180:183]\n\t"
"ds_read_b64_tr_b16 a[200:201], v251 offset:41472\n\t"
"v_mfma_f32_16x16x32_bf16 v[180:183], a[92:95], a[164:167], v[180:183]\n\t"
"ds_read_b64_tr_b16 a[196:197], v251 offset:41728\n\t"
"v_mfma_f32_16x16x32_bf16 v[164:167], a[80:83], a[148:151], v[164:167]\n\t"
"ds_read_b64_tr_b16 a[204:205], v250 offset:41728\n\t"
"v_mfma_f32_16x16x32_bf16 v[164:167], a[84:87], a[164:167], v[164:167]\n\t"
"buffer_load_dwordx4 a[16:19], v252, s[84:87], s75 offen offset:0 sc1\n\t"
"v_mfma_f32_16x16x32_bf16 v[148:151], a[72:75], a[148:151], v[148:151]\n\t"
"v_mfma_f32_16x16x32_bf16 v[148:151], a[76:79], a[164:167], v[148:151]\n\t"
"s_waitcnt lgkmcnt(0)\n\t"
"s_barrier\n\t"
"v_mfma_f32_16x16x32_bf16 v[152:155], a[72:75], a[152:155], v[152:155]\n\t"
"ds_read_b128 a[160:163], v224 offset:2048\n\t"
"v_add_u32 v248, 33280, v227\n\t"
"v_mfma_f32_16x16x32_bf16 v[152:155], a[76:79], a[168:171], v[152:155]\n\t"
"ds_read_b128 a[144:147], v224 offset:0\n\t"
"v_xor_b32 v249, 32, v248\n\t"
"v_mfma_f32_16x16x32_bf16 v[168:171], a[80:83], a[152:155], v[168:171]\n\t"
"ds_read_b128 a[148:151], v224 offset:256\n\t"
"v_xor_b32 v250, 64, v248\n\t"
"v_mfma_f32_16x16x32_bf16 v[168:171], a[84:87], a[168:171], v[168:171]\n\t"
"ds_read_b128 a[164:167], v224 offset:2304\n\t"
"v_xor_b32 v251, 96, v248\n\t"
"v_mfma_f32_16x16x32_bf16 v[184:187], a[88:91], a[152:155], v[184:187]\n\t"
"ds_read_b128 a[152:155], v224 offset:512\n\t"
"v_mfma_f32_16x16x32_bf16 v[184:187], a[92:95], a[168:171], v[184:187]\n\t"
"buffer_load_dwordx4 a[20:23], v252, s[84:87], s75 offen offset:1024 sc1\n\t"
"v_mfma_f32_16x16x32_bf16 v[188:191], a[88:91], a[156:159], v[188:191]\n\t"
"ds_read_b64_tr_b16 a[182:183], v249 offset:512\n\t"
"v_mfma_f32_16x16x32_bf16 v[188:191], a[92:95], a[172:175], v[188:191]\n\t"
"buffer_load_dwordx4 a[12:15], v235, s[84:87], s75 offen offset:3072 sc1\n\t"
"v_mfma_f32_16x16x32_bf16 v[172:175], a[80:83], a[156:159], v[172:175]\n\t"
"ds_read_b64_tr_b16 a[190:191], v251 offset:512\n\t"
"v_mfma_f32_16x16x32_bf16 v[172:175], a[84:87], a[172:175], v[172:175]\n\t"
"ds_read_b64_tr_b16 a[198:199], v249 offset:1664\n\t"
"v_mfma_f32_16x16x32_bf16 v[156:159], a[72:75], a[156:159], v[156:159]\n\t"
"ds_read_b64_tr_b16 a[206:207], v251 offset:1664\n\t"
"v_mfma_f32_16x16x32_bf16 v[156:159], a[76:79], a[172:175], v[156:159]\n\t"
"buffer_store_dwordx4 v[108:111], v232, s[16:19], s25 offen nt\n\t"
"v_mfma_f32_4x4x4_16b_bf16 v[200:203], a[220:221], a[178:179], 0\n\t"
"ds_read_b64_tr_b16 a[178:179], v248 offset:512\n\t"
"v_mfma_f32_4x4x4_16b_bf16 v[204:207], a[220:221], a[186:187], 0\n\t"
"ds_read_b64_tr_b16 a[186:187], v250 offset:512\n\t"
"v_mfma_f32_4x4x4_16b_bf16 v[216:219], a[220:221], a[194:195], 0\n\t"
"ds_read_b64_tr_b16 a[194:195], v248 offset:1664\n\t"
"v_mfma_f32_4x4x4_16b_bf16 v[220:223], a[220:221], a[202:203], 0\n\t"
"ds_read_b64_tr_b16 a[202:203], v250 offset:1664\n\t"
"v_mfma_f32_4x4x4_16b_bf16 v[192:195], a[222:223], a[176:177], v[200:203]\n\t"
"ds_read_b64_tr_b16 a[176:177], v248 offset:0\n\t"
"v_mfma_f32_4x4x4_16b_bf16 v[196:199], a[222:223], a[184:185], v[204:207]\n\t"
"ds_read_b128 a[168:171], v224 offset:2560\n\t"
"v_mfma_f32_4x4x4_16b_bf16 v[200:203], a[222:223], a[180:181], v[200:203]\n\t"
"ds_read_b128 a[156:159], v224 offset:768\n\t"
"v_mfma_f32_4x4x4_16b_bf16 v[204:207], a[222:223], a[188:189], v[204:207]\n\t"
"ds_read_b64_tr_b16 a[184:185], v250 offset:0\n\t"
"v_mfma_f32_4x4x4_16b_bf16 v[208:211], a[222:223], a[192:193], v[216:219]\n\t"
"ds_read_b64_tr_b16 a[180:181], v249 offset:0\n\t"
"v_mfma_f32_4x4x4_16b_bf16 v[212:215], a[222:223], a[200:201], v[220:223]\n\t"
"ds_read_b64_tr_b16 a[188:189], v251 offset:0\n\t"
"v_mfma_f32_4x4x4_16b_bf16 v[216:219], a[222:223], a[196:197], v[216:219]\n\t"
"ds_read_b64_tr_b16 a[192:193], v248 offset:1152\n\t"
"v_mfma_f32_4x4x4_16b_bf16 v[220:223], a[222:223], a[204:205], v[220:223]\n\t"
"s_waitcnt vmcnt(12) lgkmcnt(9)\n\t"
"buffer_load_dwordx4 a[8:11], v235, s[84:87], s75 offen offset:2048 sc1\n\t"
"v_mfma_f32_16x16x32_bf16 v[48:51], a[96:99], a[144:147], v[48:51]\n\t"
"ds_read_b64_tr_b16 a[200:201], v250 offset:1152\n\t"
"v_cvt_pk_bf16_f32 v195, -v195, -v199\n\t"
"v_mfma_f32_16x16x32_bf16 v[48:51], a[100:103], a[160:163], v[48:51]\n\t"
"buffer_load_dwordx4 a[0:3], v235, s[84:87], s75 offen offset:0 sc1\n\t"
"v_mfma_f32_16x16x32_bf16 v[64:67], a[104:107], a[144:147], v[64:67]\n\t"
"ds_read_b128 a[172:175], v224 offset:2816\n\t"
"v_cvt_pk_bf16_f32 v200, v200, v204\n\t"
"v_mfma_f32_16x16x32_bf16 v[64:67], a[108:111], a[160:163], v[64:67]\n\t"
"ds_write_addtid_b32 v200 offset:24864\n\t"
"v_cvt_pk_bf16_f32 v201, v201, v205\n\t"
"v_mfma_f32_16x16x32_bf16 v[80:83], a[112:115], a[144:147], v[80:83]\n\t"
"ds_read_b128 a[144:147], v224 offset:1024\n\t"
"v_cvt_pk_bf16_f32 v211, -v211, -v215\n\t"
"v_mfma_f32_16x16x32_bf16 v[80:83], a[116:119], a[160:163], v[80:83]\n\t"
"buffer_store_dwordx4 a[232:235], v233, s[16:19], s25 offen nt\n\t"
"v_mfma_f32_16x16x32_bf16 v[84:87], a[112:115], a[148:151], v[84:87]\n\t"
"ds_write_addtid_b32 v201 offset:28992\n\t"
"v_cvt_pk_bf16_f32 v192, -v192, -v196\n\t"
"v_mfma_f32_16x16x32_bf16 v[84:87], a[116:119], a[164:167], v[84:87]\n\t"
"ds_read_b128 a[160:163], v224 offset:3072\n\t"
"v_cvt_pk_bf16_f32 v219, v219, v223\n\t"
"v_mfma_f32_16x16x32_bf16 v[68:71], a[104:107], a[148:151], v[68:71]\n\t"
"ds_write_addtid_b32 v219 offset:32064\n\t"
"v_cvt_pk_bf16_f32 v208, -v208, -v212\n\t"
"v_mfma_f32_16x16x32_bf16 v[68:71], a[108:111], a[164:167], v[68:71]\n\t"
"buffer_load_dwordx4 a[4:7], v235, s[84:87], s75 offen offset:1024 sc1\n\t"
"v_mfma_f32_16x16x32_bf16 v[52:55], a[96:99], a[148:151], v[52:55]\n\t"
"ds_write_addtid_b32 v195 offset:21696\n\t"
"v_mfma_f32_16x16x32_bf16 v[52:55], a[100:103], a[164:167], v[52:55]\n\t"
"s_waitcnt lgkmcnt(7)\n\t"
"ds_read_b128 a[164:167], v224 offset:3328\n\t"
"v_cvt_pk_bf16_f32 v209, -v209, -v213\n\t"
"v_mfma_f32_16x16x32_bf16 v[56:59], a[96:99], a[152:155], v[56:59]\n\t"
"ds_write_addtid_b32 v209 offset:22720\n\t"
"v_cvt_pk_bf16_f32 v203, v203, v207\n\t"
"v_mfma_f32_16x16x32_bf16 v[56:59], a[100:103], a[168:171], v[56:59]\n\t"
"buffer_load_dwordx4 a[40:43], v252, s[84:87], s78 offen offset:0 sc1\n\t"
"v_mfma_f32_16x16x32_bf16 v[72:75], a[104:107], a[152:155], v[72:75]\n\t"
"ds_read_b128 a[148:151], v224 offset:1280\n\t"
"v_cvt_pk_bf16_f32 v217, v217, v221\n\t"
"v_mfma_f32_16x16x32_bf16 v[72:75], a[108:111], a[168:171], v[72:75]\n\t"
"ds_write_addtid_b32 v208 offset:18592\n\t"
"v_cvt_pk_bf16_f32 v210, -v210, -v214\n\t"
"v_mfma_f32_16x16x32_bf16 v[88:91], a[112:115], a[152:155], v[88:91]\n\t"
"ds_write_addtid_b32 v210 offset:19616\n\t"
"v_cvt_pk_bf16_f32 v194, -v194, -v198\n\t"
"v_mfma_f32_16x16x32_bf16 v[88:91], a[116:119], a[168:171], v[88:91]\n\t"
"s_waitcnt lgkmcnt(7)\n\t"
"ds_write_addtid_b32 v194 offset:17568\n\t"
"v_cvt_pk_bf16_f32 v218, v218, v222\n\t"
"v_mfma_f32_16x16x32_bf16 v[92:95], a[112:115], a[156:159], v[92:95]\n\t"
"ds_write_addtid_b32 v192 offset:16544\n\t"
"v_cvt_pk_bf16_f32 v193, -v193, -v197\n\t"
"v_mfma_f32_16x16x32_bf16 v[92:95], a[116:119], a[172:175], v[92:95]\n\t"
"ds_write_addtid_b32 v218 offset:27936\n\t"
"v_cvt_pk_bf16_f32 v202, v202, v206\n\t"
"v_mfma_f32_16x16x32_bf16 v[76:79], a[104:107], a[156:159], v[76:79]\n\t"
"ds_write_addtid_b32 v217 offset:31040\n\t"
"v_mfma_f32_16x16x32_bf16 v[76:79], a[108:111], a[172:175], v[76:79]\n\t"
"buffer_store_dwordx4 a[236:239], v234, s[16:19], s25 offen nt\n\t"
"v_mfma_f32_16x16x32_bf16 v[60:63], a[96:99], a[156:159], v[60:63]\n\t"
"ds_read_b128 a[156:159], v224 offset:1792\n\t"
"v_cvt_pk_bf16_f32 v216, v216, v220\n\t"
"v_mfma_f32_16x16x32_bf16 v[60:63], a[100:103], a[172:175], v[60:63]\n\t"
"s_waitcnt vmcnt(10) lgkmcnt(8)\n\t"
"ds_write_addtid_b32 v216 offset:26912\n\t"
"v_mfma_f32_16x16x32_bf16 v[144:147], a[120:123], a[144:147], v[144:147]\n\t"
"s_nop 0\n\t"
"ds_write_addtid_b32 v193 offset:20672\n\t"
"ds_write_addtid_b32 v203 offset:30016\n\t"
"v_mfma_f32_16x16x32_bf16 v[144:147], a[124:127], a[160:163], v[144:147]\n\t"
"buffer_load_dwordx4 a[44:47], v252, s[84:87], s78 offen offset:1024 sc1\n\t"
"v_mfma_f32_16x16x32_bf16 v[160:163], a[128:131], a[144:147], v[160:163]\n\t"
"s_nop 0\n\t"
"ds_write_addtid_b32 v202 offset:25888\n\t"
"ds_write_addtid_b32 v211 offset:23744\n\t"
"v_mfma_f32_16x16x32_bf16 v[160:163], a[132:135], a[160:163], v[160:163]\n\t"
"ds_read_b128 a[172:175], v224 offset:3840\n\t"
"v_mfma_f32_16x16x32_bf16 v[176:179], a[136:139], a[144:147], v[176:179]\n\t"
"ds_read_b128 a[152:155], v224 offset:1536\n\t"
"v_mfma_f32_16x16x32_bf16 v[176:179], a[140:143], a[160:163], v[176:179]\n\t"
"s_waitcnt lgkmcnt(9)\n\t"
"ds_read_b128 a[168:171], v224 offset:3584\n\t"
"v_mfma_f32_16x16x32_bf16 v[180:183], a[136:139], a[148:151], v[180:183]\n\t"
"ds_read_b64_tr_b16 a[196:197], v249 offset:1152\n\t"
"v_mfma_f32_16x16x32_bf16 v[180:183], a[140:143], a[164:167], v[180:183]\n\t"
"buffer_load_dwordx4 a[36:39], v235, s[84:87], s78 offen offset:3072 sc1\n\t"
"v_mfma_f32_16x16x32_bf16 v[164:167], a[128:131], a[148:151], v[164:167]\n\t"
"ds_read_b64_tr_b16 a[204:205], v251 offset:1152\n\t"
"v_mfma_f32_16x16x32_bf16 v[164:167], a[132:135], a[164:167], v[164:167]\n\t"
"buffer_load_dwordx4 a[32:35], v235, s[84:87], s78 offen offset:2048 sc1\n\t"
"v_mfma_f32_16x16x32_bf16 v[148:151], a[120:123], a[148:151], v[148:151]\n\t"
"v_mfma_f32_16x16x32_bf16 v[148:151], a[124:127], a[164:167], v[148:151]\n\t"
"s_waitcnt lgkmcnt(0)\n\t"
"s_barrier\n\t"
"v_mfma_f32_16x16x32_bf16 v[152:155], a[120:123], a[152:155], v[152:155]\n\t"
"ds_read_b128 a[148:151], v224 offset:16800\n\t"
"v_mfma_f32_16x16x32_bf16 v[152:155], a[124:127], a[168:171], v[152:155]\n\t"
"ds_read_b128 a[144:147], v224 offset:16544\n\t"
"v_mfma_f32_16x16x32_bf16 v[168:171], a[128:131], a[152:155], v[168:171]\n\t"
"ds_read_b128 a[160:163], v224 offset:18592\n\t"
"v_mfma_f32_16x16x32_bf16 v[168:171], a[132:135], a[168:171], v[168:171]\n\t"
"ds_read_b128 a[164:167], v224 offset:18848\n\t"
"v_mfma_f32_16x16x32_bf16 v[184:187], a[136:139], a[152:155], v[184:187]\n\t"
"v_mfma_f32_16x16x32_bf16 v[184:187], a[140:143], a[168:171], v[184:187]\n\t"
"buffer_load_dwordx4 a[24:27], v235, s[84:87], s78 offen offset:0 sc1\n\t"
"v_mfma_f32_16x16x32_bf16 v[188:191], a[136:139], a[156:159], v[188:191]\n\t"
"ds_read_b128 a[168:171], v224 offset:19104\n\t"
"v_mfma_f32_16x16x32_bf16 v[188:191], a[140:143], a[172:175], v[188:191]\n\t"
"v_mfma_f32_16x16x32_bf16 v[172:175], a[128:131], a[156:159], v[172:175]\n\t"
"ds_read_b128 a[152:155], v224 offset:17056\n\t"
"v_mfma_f32_16x16x32_bf16 v[172:175], a[132:135], a[172:175], v[172:175]\n\t"
"buffer_load_dwordx4 a[28:31], v235, s[84:87], s78 offen offset:1024 sc1\n\t"
"v_mfma_f32_16x16x32_bf16 v[156:159], a[120:123], a[156:159], v[156:159]\n\t"
"ds_read_b128 a[156:159], v224 offset:17312\n\t"
"v_mfma_f32_16x16x32_bf16 v[156:159], a[124:127], a[172:175], v[156:159]\n\t"
"ds_read_b128 a[172:175], v224 offset:19360\n\t"
"v_mfma_f32_16x16x32_bf16 v[192:195], a[224:227], a[176:179], 0\n\t"
"ds_read_b64_tr_b16 a[176:177], v248 offset:42624\n\t"
"v_mfma_f32_16x16x32_bf16 v[196:199], a[224:227], a[180:183], 0\n\t"
"ds_read_b64_tr_b16 a[178:179], v248 offset:43136\n\t"
"v_mfma_f32_16x16x32_bf16 v[200:203], a[224:227], a[184:187], 0\n\t"
"buffer_load_dwordx4 a[64:67], v252, s[84:87], 0 offen offset:0 sc1\n\t"
"v_mfma_f32_16x16x32_bf16 v[204:207], a[224:227], a[188:191], 0\n\t"
"ds_read_b64_tr_b16 a[180:181], v249 offset:42624\n\t"
"v_mfma_f32_16x16x32_bf16 v[208:211], a[224:227], a[192:195], 0\n\t"
"ds_read_b64_tr_b16 a[182:183], v249 offset:43136\n\t"
"v_mfma_f32_16x16x32_bf16 v[212:215], a[224:227], a[196:199], 0\n\t"
"ds_read_b64_tr_b16 a[184:185], v250 offset:42624\n\t"
"v_mfma_f32_16x16x32_bf16 v[216:219], a[224:227], a[200:203], 0\n\t"
"ds_read_b64_tr_b16 a[186:187], v250 offset:43136\n\t"
"v_mfma_f32_16x16x32_bf16 v[220:223], a[224:227], a[204:207], 0\n\t"
"s_waitcnt vmcnt(8) lgkmcnt(8)\n\t"
"ds_read_b64_tr_b16 a[188:189], v251 offset:42624\n\t"
"v_cvt_pk_bf16_f32 v194, v194, v198\n\t"
"v_mfma_f32_16x16x32_bf16 v[48:51], a[0:3], a[144:147], v[48:51]\n\t"
"ds_read_b64_tr_b16 a[190:191], v251 offset:43136\n\t"
"v_mfma_f32_16x16x32_bf16 v[48:51], a[4:7], a[160:163], v[48:51]\n\t"
"buffer_load_dwordx4 a[68:71], v252, s[84:87], 0 offen offset:1024 sc1\n\t"
"v_mfma_f32_16x16x32_bf16 v[64:67], a[8:11], a[144:147], v[64:67]\n\t"
"ds_read_b64_tr_b16 a[192:193], v248 offset:21888\n\t"
"v_cvt_pk_bf16_f32 v202, v202, v206\n\t"
"v_mfma_f32_16x16x32_bf16 v[64:67], a[12:15], a[160:163], v[64:67]\n\t"
"ds_read_b64_tr_b16 a[194:195], v248 offset:22400\n\t"
"v_cvt_pk_bf16_f32 v210, v210, v214\n\t"
"v_mfma_f32_16x16x32_bf16 v[80:83], a[16:19], a[144:147], v[80:83]\n\t"
"ds_read_b128 a[144:147], v224 offset:17568\n\t"
"v_cvt_pk_bf16_f32 v200, v200, v204\n\t"
"v_mfma_f32_16x16x32_bf16 v[80:83], a[20:23], a[160:163], v[80:83]\n\t"
"ds_write_addtid_b32 v202 offset:6176\n\t"
"v_cvt_pk_bf16_f32 v211, v211, v215\n\t"
"v_mfma_f32_16x16x32_bf16 v[84:87], a[16:19], a[148:151], v[84:87]\n\t"
"ds_write_addtid_b32 v200 offset:2048\n\t"
"v_cvt_pk_bf16_f32 v217, v217, v221\n\t"
"v_mfma_f32_16x16x32_bf16 v[84:87], a[20:23], a[164:167], v[84:87]\n\t"
"buffer_load_dwordx4 a[60:63], v235, s[84:87], 0 offen offset:3072 sc1\n\t"
"v_mfma_f32_16x16x32_bf16 v[68:71], a[8:11], a[148:151], v[68:71]\n\t"
"ds_read_b128 a[160:163], v224 offset:19616\n\t"
"v_cvt_pk_bf16_f32 v208, v208, v212\n\t"
"v_mfma_f32_16x16x32_bf16 v[68:71], a[12:15], a[164:167], v[68:71]\n\t"
"ds_write_addtid_b32 v217 offset:11392\n\t"
"v_cvt_pk_bf16_f32 v219, v219, v223\n\t"
"v_mfma_f32_16x16x32_bf16 v[52:55], a[0:3], a[148:151], v[52:55]\n\t"
"ds_write_addtid_b32 v211 offset:13472\n\t"
"v_cvt_pk_bf16_f32 v201, v201, v205\n\t"
"v_mfma_f32_16x16x32_bf16 v[52:55], a[4:7], a[164:167], v[52:55]\n\t"
"ds_read_b128 a[148:151], v224 offset:17824\n\t"
"v_cvt_pk_bf16_f32 v216, v216, v220\n\t"
"v_mfma_f32_16x16x32_bf16 v[56:59], a[0:3], a[152:155], v[56:59]\n\t"
"ds_write_addtid_b32 v201 offset:10368\n\t"
"v_cvt_pk_bf16_f32 v203, v203, v207\n\t"
"v_mfma_f32_16x16x32_bf16 v[56:59], a[4:7], a[168:171], v[56:59]\n\t"
"buffer_load_dwordx4 a[56:59], v235, s[84:87], 0 offen offset:2048 sc1\n\t"
"v_mfma_f32_16x16x32_bf16 v[72:75], a[8:11], a[152:155], v[72:75]\n\t"
"ds_read_b128 a[164:167], v224 offset:19872\n\t"
"v_cvt_pk_bf16_f32 v193, v193, v197\n\t"
"v_mfma_f32_16x16x32_bf16 v[72:75], a[12:15], a[168:171], v[72:75]\n\t"
"ds_read_b64_tr_b16 a[196:197], v249 offset:21888\n\t"
"v_cvt_pk_bf16_f32 v192, v192, v196\n\t"
"v_mfma_f32_16x16x32_bf16 v[88:91], a[16:19], a[152:155], v[88:91]\n\t"
"ds_read_b64_tr_b16 a[198:199], v249 offset:22400\n\t"
"v_mfma_f32_16x16x32_bf16 v[88:91], a[20:23], a[168:171], v[88:91]\n\t"
"s_waitcnt lgkmcnt(8)\n\t"
"ds_write_addtid_b32 v210 offset:5152\n\t"
"v_cvt_pk_bf16_f32 v195, v195, v199\n\t"
"v_mfma_f32_16x16x32_bf16 v[92:95], a[16:19], a[156:159], v[92:95]\n\t"
"ds_write_addtid_b32 v195 offset:12448\n\t"
"v_cvt_pk_bf16_f32 v218, v218, v222\n\t"
"v_mfma_f32_16x16x32_bf16 v[92:95], a[20:23], a[172:175], v[92:95]\n\t"
"buffer_load_dwordx4 a[48:51], v235, s[84:87], 0 offen offset:0 sc1\n\t"
"v_mfma_f32_16x16x32_bf16 v[76:79], a[8:11], a[156:159], v[76:79]\n\t"
"ds_write_addtid_b32 v193 offset:8320\n\t"
"v_cvt_pk_bf16_f32 v209, v209, v213\n\t"
"v_mfma_f32_16x16x32_bf16 v[76:79], a[12:15], a[172:175], v[76:79]\n\t"
"buffer_load_dwordx4 a[52:55], v235, s[84:87], 0 offen offset:1024 sc1\n\t"
"v_mfma_f32_16x16x32_bf16 v[60:63], a[0:3], a[156:159], v[60:63]\n\t"
"ds_read_b128 a[156:159], v224 offset:18336\n\t"
"v_mfma_f32_16x16x32_bf16 v[60:63], a[4:7], a[172:175], v[60:63]\n\t"
"s_waitcnt vmcnt(6) lgkmcnt(7)\n\t"
"ds_write_addtid_b32 v192 offset:0\n\t"
"ds_write_addtid_b32 v203 offset:14496\n\t"
"s_xor_b32 s24, s24, 384\n\t"
"v_mfma_f32_16x16x32_bf16 v[144:147], a[24:27], a[144:147], v[144:147]\n\t"
"s_xor_b32 s20, s20, 384\n\t"
"ds_write_addtid_b32 v208 offset:1024\n\t"
"ds_write_addtid_b32 v216 offset:3072\n\t"
"v_mfma_f32_16x16x32_bf16 v[144:147], a[28:31], a[160:163], v[144:147]\n\t"
"s_xor_b32 s27, s27, 384\n\t"
"ds_write_addtid_b32 v194 offset:4128\n\t"
"ds_write_addtid_b32 v209 offset:9344\n\t"
"v_mfma_f32_16x16x32_bf16 v[160:163], a[32:35], a[144:147], v[160:163]\n\t"
"s_nop 0\n\t"
"ds_write_addtid_b32 v218 offset:7200\n\t"
"ds_write_addtid_b32 v219 offset:15520\n\t"
"v_mfma_f32_16x16x32_bf16 v[160:163], a[36:39], a[160:163], v[160:163]\n\t"
"ds_read_b128 a[168:171], v224 offset:20128\n\t"
"v_mfma_f32_16x16x32_bf16 v[176:179], a[40:43], a[144:147], v[176:179]\n\t"
"s_xor_b32 s25, s25, 384\n\t"
"ds_read_b128 a[152:155], v224 offset:18080\n\t"
"ds_read_b64_tr_b16 a[200:201], v250 offset:21888\n\t"
"v_mfma_f32_16x16x32_bf16 v[176:179], a[44:47], a[160:163], v[176:179]\n\t"
"s_waitcnt lgkmcnt(13)\n\t"
"ds_read_b128 a[172:175], v224 offset:20384\n\t"
"v_mfma_f32_16x16x32_bf16 v[180:183], a[40:43], a[148:151], v[180:183]\n\t"
"ds_read_b64_tr_b16 a[202:203], v250 offset:22400\n\t"
"v_mfma_f32_16x16x32_bf16 v[180:183], a[44:47], a[164:167], v[180:183]\n\t"
"ds_read_b64_tr_b16 a[204:205], v251 offset:21888\n\t"
"v_mfma_f32_16x16x32_bf16 v[164:167], a[32:35], a[148:151], v[164:167]\n\t"
"ds_read_b64_tr_b16 a[206:207], v251 offset:22400\n\t"
"v_mfma_f32_16x16x32_bf16 v[164:167], a[36:39], a[164:167], v[164:167]\n\t"
"buffer_load_dwordx4 a[88:91], v252, s[84:87], s51 offen offset:0 sc1\n\t"
"v_mfma_f32_16x16x32_bf16 v[148:151], a[24:27], a[148:151], v[148:151]\n\t"
"v_mfma_f32_16x16x32_bf16 v[148:151], a[28:31], a[164:167], v[148:151]\n\t"
"s_waitcnt lgkmcnt(0)\n\t"
"s_barrier\n\t"
"v_mfma_f32_16x16x32_bf16 v[152:155], a[24:27], a[152:155], v[152:155]\n\t"
"ds_read_b128 a[144:147], v224 offset:0\n\t"
"v_mfma_f32_16x16x32_bf16 v[152:155], a[28:31], a[168:171], v[152:155]\n\t"
"ds_read_b128 a[160:163], v224 offset:2048\n\t"
"v_mfma_f32_16x16x32_bf16 v[168:171], a[32:35], a[152:155], v[168:171]\n\t"
"ds_read_b128 a[148:151], v224 offset:256\n\t"
"v_mfma_f32_16x16x32_bf16 v[168:171], a[36:39], a[168:171], v[168:171]\n\t"
"ds_read_b128 a[164:167], v224 offset:2304\n\t"
"v_mfma_f32_16x16x32_bf16 v[184:187], a[40:43], a[152:155], v[184:187]\n\t"
"ds_read_b128 a[152:155], v224 offset:512\n\t"
"v_mfma_f32_16x16x32_bf16 v[184:187], a[44:47], a[168:171], v[184:187]\n\t"
"buffer_load_dwordx4 a[92:95], v252, s[84:87], s51 offen offset:1024 sc1\n\t"
"v_mfma_f32_16x16x32_bf16 v[188:191], a[40:43], a[156:159], v[188:191]\n\t"
"v_mfma_f32_16x16x32_bf16 v[188:191], a[44:47], a[172:175], v[188:191]\n\t"
"buffer_load_dwordx4 a[84:87], v235, s[84:87], s51 offen offset:3072 sc1\n\t"
"v_mfma_f32_16x16x32_bf16 v[172:175], a[32:35], a[156:159], v[172:175]\n\t"
"v_mfma_f32_16x16x32_bf16 v[172:175], a[36:39], a[172:175], v[172:175]\n\t"
"buffer_load_dwordx4 a[80:83], v235, s[84:87], s51 offen offset:2048 sc1\n\t"
"v_mfma_f32_16x16x32_bf16 v[156:159], a[24:27], a[156:159], v[156:159]\n\t"
"v_mfma_f32_16x16x32_bf16 v[156:159], a[28:31], a[172:175], v[156:159]\n\t"
"buffer_load_dwordx4 a[72:75], v235, s[84:87], s51 offen offset:0 sc1\n\t"
"v_mfma_f32_16x16x32_bf16 v[192:195], a[224:227], a[176:179], 0\n\t"
"ds_read_b128 a[168:171], v224 offset:2560\n\t"
"v_mfma_f32_16x16x32_bf16 v[196:199], a[224:227], a[180:183], 0\n\t"
"buffer_load_dwordx4 a[76:79], v235, s[84:87], s51 offen offset:1024 sc1\n\t"
"v_mfma_f32_16x16x32_bf16 v[200:203], a[224:227], a[184:187], 0\n\t"
"ds_read_b128 a[172:175], v224 offset:2816\n\t"
"v_mfma_f32_16x16x32_bf16 v[204:207], a[224:227], a[188:191], 0\n\t"
".p2align 3\n\t"
"buffer_load_dwordx4 a[112:115], v252, s[84:87], s81 offen offset:0 sc1\n\t"
"v_pk_fma_f32 v[48:49], v[144:145], s[90:91], v[48:49]\n\t"
"v_pk_fma_f32 v[50:51], v[146:147], s[90:91], v[50:51]\n\t"
"v_pk_add_f32 v[144:145], v[48:49], v[144:145] neg_lo:[0,1] neg_hi:[0,1]\n\t"
"v_pk_add_f32 v[146:147], v[50:51], v[146:147] neg_lo:[0,1] neg_hi:[0,1]\n\t"
"v_pk_fma_f32 v[64:65], v[160:161], s[90:91], v[64:65]\n\t"
"v_pk_fma_f32 v[66:67], v[162:163], s[90:91], v[66:67]\n\t"
"v_pk_add_f32 v[160:161], v[64:65], v[160:161] neg_lo:[0,1] neg_hi:[0,1]\n\t"
"v_pk_add_f32 v[162:163], v[66:67], v[162:163] neg_lo:[0,1] neg_hi:[0,1]\n\t"
"v_pk_fma_f32 v[80:81], v[176:177], s[90:91], v[80:81]\n\t"
"v_pk_fma_f32 v[82:83], v[178:179], s[90:91], v[82:83]\n\t"
"v_pk_add_f32 v[176:177], v[80:81], v[176:177] neg_lo:[0,1] neg_hi:[0,1]\n\t"
"v_pk_add_f32 v[178:179], v[82:83], v[178:179] neg_lo:[0,1] neg_hi:[0,1]\n\t"
"v_pk_fma_f32 v[84:85], v[180:181], s[90:91], v[84:85]\n\t"
"v_pk_fma_f32 v[86:87], v[182:183], s[90:91], v[86:87]\n\t"
"v_pk_add_f32 v[180:181], v[84:85], v[180:181] neg_lo:[0,1] neg_hi:[0,1]\n\t"
"v_pk_add_f32 v[182:183], v[86:87], v[182:183] neg_lo:[0,1] neg_hi:[0,1]\n\t"
"v_pk_fma_f32 v[68:69], v[164:165], s[90:91], v[68:69]\n\t"
"v_pk_fma_f32 v[70:71], v[166:167], s[90:91], v[70:71]\n\t"
"v_pk_add_f32 v[164:165], v[68:69], v[164:165] neg_lo:[0,1] neg_hi:[0,1]\n\t"
"v_pk_add_f32 v[166:167], v[70:71], v[166:167] neg_lo:[0,1] neg_hi:[0,1]\n\t"
"v_pk_fma_f32 v[52:53], v[148:149], s[90:91], v[52:53]\n\t"
"v_pk_fma_f32 v[54:55], v[150:151], s[90:91], v[54:55]\n\t"
"v_pk_add_f32 v[148:149], v[52:53], v[148:149] neg_lo:[0,1] neg_hi:[0,1]\n\t"
"v_pk_add_f32 v[150:151], v[54:55], v[150:151] neg_lo:[0,1] neg_hi:[0,1]\n\t"
"v_pk_fma_f32 v[56:57], v[152:153], s[90:91], v[56:57]\n\t"
"v_pk_fma_f32 v[58:59], v[154:155], s[90:91], v[58:59]\n\t"
"v_pk_add_f32 v[152:153], v[56:57], v[152:153] neg_lo:[0,1] neg_hi:[0,1]\n\t"
"v_pk_add_f32 v[154:155], v[58:59], v[154:155] neg_lo:[0,1] neg_hi:[0,1]\n\t"
"v_pk_fma_f32 v[72:73], v[168:169], s[90:91], v[72:73]\n\t"
"v_pk_fma_f32 v[74:75], v[170:171], s[90:91], v[74:75]\n\t"
"v_pk_add_f32 v[168:169], v[72:73], v[168:169] neg_lo:[0,1] neg_hi:[0,1]\n\t"
"v_pk_add_f32 v[170:171], v[74:75], v[170:171] neg_lo:[0,1] neg_hi:[0,1]\n\t"
"v_pk_fma_f32 v[88:89], v[184:185], s[90:91], v[88:89]\n\t"
"v_pk_fma_f32 v[90:91], v[186:187], s[90:91], v[90:91]\n\t"
"v_pk_add_f32 v[184:185], v[88:89], v[184:185] neg_lo:[0,1] neg_hi:[0,1]\n\t"
"v_pk_add_f32 v[186:187], v[90:91], v[186:187] neg_lo:[0,1] neg_hi:[0,1]\n\t"
"v_pk_fma_f32 v[92:93], v[188:189], s[90:91], v[92:93]\n\t"
"v_pk_fma_f32 v[94:95], v[190:191], s[90:91], v[94:95]\n\t"
"v_pk_add_f32 v[188:189], v[92:93], v[188:189] neg_lo:[0,1] neg_hi:[0,1]\n\t"
"v_pk_add_f32 v[190:191], v[94:95], v[190:191] neg_lo:[0,1] neg_hi:[0,1]\n\t"
"v_pk_fma_f32 v[76:77], v[172:173], s[90:91], v[76:77]\n\t"
"v_pk_fma_f32 v[78:79], v[174:175], s[90:91], v[78:79]\n\t"
"v_pk_add_f32 v[172:173], v[76:77], v[172:173] neg_lo:[0,1] neg_hi:[0,1]\n\t"
"v_pk_add_f32 v[174:175], v[78:79], v[174:175] neg_lo:[0,1] neg_hi:[0,1]\n\t"
"v_pk_fma_f32 v[60:61], v[156:157], s[90:91], v[60:61]\n\t"
"v_pk_fma_f32 v[62:63], v[158:159], s[90:91], v[62:63]\n\t"
"v_pk_add_f32 v[156:157], v[60:61], v[156:157] neg_lo:[0,1] neg_hi:[0,1]\n\t"
"v_pk_add_f32 v[158:159], v[62:63], v[158:159] neg_lo:[0,1] neg_hi:[0,1]\n\t"
"v_mfma_f32_16x16x32_bf16 v[208:211], a[224:227], a[192:195], 0\n\t"
"v_mfma_f32_16x16x32_bf16 v[212:215], a[224:227], a[196:199], 0\n\t"
"ds_read_b128 a[156:159], v224 offset:768\n\t"
"v_mfma_f32_16x16x32_bf16 v[216:219], a[224:227], a[200:203], 0\n\t"
"v_mfma_f32_16x16x32_bf16 v[220:223], a[224:227], a[204:207], 0\n\t"
"s_waitcnt vmcnt(7) lgkmcnt(3)\n\t"
"buffer_load_dwordx4 a[116:119], v252, s[84:87], s81 offen offset:1024 sc1\n\t"
"v_mfma_f32_16x16x32_bf16 v[48:51], a[48:51], a[144:147], v[48:51]\n\t"
"v_cvt_pk_bf16_f32 v192, v192, v196\n\t"
"v_cvt_pk_bf16_f32 v193, v193, v197\n\t"
"v_mfma_f32_16x16x32_bf16 v[48:51], a[52:55], a[160:163], v[48:51]\n\t"
"ds_write_addtid_b32 v193 offset:24864\n\t"
"v_cvt_pk_bf16_f32 v195, v195, v199\n\t"
"v_mfma_f32_16x16x32_bf16 v[64:67], a[56:59], a[144:147], v[64:67]\n\t"
"ds_write_addtid_b32 v192 offset:16544\n\t"
"v_cvt_pk_bf16_f32 v194, v194, v198\n\t"
"v_mfma_f32_16x16x32_bf16 v[64:67], a[60:63], a[160:163], v[64:67]\n\t"
"ds_write_addtid_b32 v194 offset:20672\n\t"
"v_cvt_pk_bf16_f32 v201, v201, v205\n\t"
"v_mfma_f32_16x16x32_bf16 v[80:83], a[64:67], a[144:147], v[80:83]\n\t"
"ds_read_b128 a[144:147], v224 offset:1024\n\t"
"v_cvt_pk_bf16_f32 v203, v203, v207\n\t"
"v_mfma_f32_16x16x32_bf16 v[80:83], a[68:71], a[160:163], v[80:83]\n\t"
"ds_write_addtid_b32 v195 offset:28992\n\t"
"v_cvt_pk_bf16_f32 v209, v209, v213\n\t"
"v_cvt_pk_bf16_f32 v217, v217, v221\n\t"
"v_mfma_f32_16x16x32_bf16 v[84:87], a[64:67], a[148:151], v[84:87]\n\t"
"ds_write_addtid_b32 v203 offset:31040\n\t"
"v_cvt_pk_bf16_f32 v200, v200, v204\n\t"
"v_mfma_f32_16x16x32_bf16 v[84:87], a[68:71], a[164:167], v[84:87]\n\t"
"ds_write_addtid_b32 v201 offset:26912\n\t"
"buffer_load_dwordx4 a[108:111], v235, s[84:87], s81 offen offset:3072 sc1\n\t"
"v_cvt_pk_bf16_f32 v64, v64, v65\n\t"
"v_mfma_f32_16x16x32_bf16 v[68:71], a[56:59], a[148:151], v[68:71]\n\t"
"ds_read_b128 a[160:163], v224 offset:3072\n\t"
"v_cvt_pk_bf16_f32 v219, v219, v223\n\t"
"v_mfma_f32_16x16x32_bf16 v[68:71], a[60:63], a[164:167], v[68:71]\n\t"
"ds_write_addtid_b32 v200 offset:18592\n\t"
"v_cvt_pk_bf16_f32 v48, v48, v49\n\t"
"v_mfma_f32_16x16x32_bf16 v[52:55], a[48:51], a[148:151], v[52:55]\n\t"
"ds_read_b128 a[148:151], v224 offset:1280\n\t"
"v_cvt_pk_bf16_f32 v65, v66, v67\n\t"
"v_mfma_f32_16x16x32_bf16 v[52:55], a[52:55], a[164:167], v[52:55]\n\t"
"s_waitcnt lgkmcnt(9)\n\t"
"v_cvt_pk_bf16_f32 v211, v211, v215\n\t"
"v_cvt_pk_bf16_f32 v49, v50, v51\n\t"
"v_cvt_pk_bf16_f32 v80, v80, v81\n\t"
"v_mfma_f32_16x16x32_bf16 v[56:59], a[48:51], a[152:155], v[56:59]\n\t"
"ds_write_addtid_b32 v209 offset:25888\n\t"
"v_cvt_pk_bf16_f32 v81, v82, v83\n\t"
"v_mfma_f32_16x16x32_bf16 v[56:59], a[52:55], a[168:171], v[56:59]\n\t"
"ds_read_b128 a[164:167], v224 offset:3328\n\t"
"buffer_load_dwordx4 a[104:107], v235, s[84:87], s81 offen offset:2048 sc1\n\t"
"buffer_load_dwordx4 a[96:99], v235, s[84:87], s81 offen offset:0 sc1\n\t"
"v_mfma_f32_16x16x32_bf16 v[72:75], a[56:59], a[152:155], v[72:75]\n\t"
"v_cvt_pk_bf16_f32 v52, v52, v53\n\t"
"v_cvt_pk_bf16_f32 v53, v54, v55\n\t"
"v_mfma_f32_16x16x32_bf16 v[72:75], a[60:63], a[168:171], v[72:75]\n\t"
"ds_write_addtid_b32 v211 offset:30016\n\t"
"buffer_load_dwordx4 a[100:103], v235, s[84:87], s81 offen offset:1024 sc1\n\t"
"ds_write2st64_b64 v228, v[48:49], v[52:53] offset0:0 offset1:13\n\t"
"v_cvt_pk_bf16_f32 v208, v208, v212\n\t"
"v_mfma_f32_16x16x32_bf16 v[88:91], a[64:67], a[152:155], v[88:91]\n\t"
"v_cvt_pk_bf16_f32 v210, v210, v214\n\t"
"v_cvt_pk_bf16_f32 v216, v216, v220\n\t"
"v_mfma_f32_16x16x32_bf16 v[88:91], a[68:71], a[168:171], v[88:91]\n\t"
"v_cvt_pk_bf16_f32 v218, v218, v222\n\t"
"v_cvt_pk_bf16_f32 v68, v68, v69\n\t"
"v_cvt_pk_bf16_f32 v72, v72, v73\n\t"
"v_mfma_f32_16x16x32_bf16 v[92:95], a[64:67], a[156:159], v[92:95]\n\t"
"v_cvt_pk_bf16_f32 v69, v70, v71\n\t"
"v_cvt_pk_bf16_f32 v56, v56, v57\n\t"
"v_mfma_f32_16x16x32_bf16 v[92:95], a[68:71], a[172:175], v[92:95]\n\t"
"ds_read_b128 a[168:171], v224 offset:3584\n\t"
"ds_write2st64_b64 v246, v[64:65], v[68:69] offset0:0 offset1:13\n\t"
"v_mfma_f32_16x16x32_bf16 v[76:79], a[56:59], a[156:159], v[76:79]\n\t"
"v_cvt_pk_bf16_f32 v202, v202, v206\n\t"
"v_cvt_pk_bf16_f32 v84, v84, v85\n\t"
"v_mfma_f32_16x16x32_bf16 v[76:79], a[60:63], a[172:175], v[76:79]\n\t"
"v_cvt_pk_bf16_f32 v85, v86, v87\n\t"
"s_nop 0\n\t"
"v_cvt_pk_bf16_f32 v92, v92, v93\n\t"
"v_mfma_f32_16x16x32_bf16 v[60:63], a[48:51], a[156:159], v[60:63]\n\t"
"v_cvt_pk_bf16_f32 v88, v88, v89\n\t"
"v_cvt_pk_bf16_f32 v89, v90, v91\n\t"
"v_mfma_f32_16x16x32_bf16 v[60:63], a[52:55], a[172:175], v[60:63]\n\t"
"s_waitcnt vmcnt(6) lgkmcnt(4)\n\t"
".p2align 3\n\t"
"ds_read_b128 a[152:155], v224 offset:1536\n\t"
"ds_read_b128 a[156:159], v224 offset:1792\n\t"
"ds_read_b128 a[172:175], v224 offset:3840\n\t"
"ds_write_addtid_b32 v202 offset:22720\n\t"
"ds_write_addtid_b32 v208 offset:17568\n\t"
"ds_write_addtid_b32 v210 offset:21696\n\t"
"ds_write_addtid_b32 v216 offset:19616\n\t"
"ds_write_addtid_b32 v217 offset:27936\n\t"
"ds_write_addtid_b32 v218 offset:23744\n\t"
"ds_write_addtid_b32 v219 offset:32064\n\t"
"ds_write2st64_b64 v247, v[80:81], v[84:85] offset0:0 offset1:13\n\t"
"v_mfma_f32_16x16x32_bf16 v[144:147], a[72:75], a[144:147], v[144:147]\n\t"
"v_cvt_pk_bf16_f32 v76, v76, v77\n\t"
"v_cvt_pk_bf16_f32 v93, v94, v95\n\t"
"v_mfma_f32_16x16x32_bf16 v[144:147], a[76:79], a[160:163], v[144:147]\n\t"
"v_cvt_pk_bf16_f32 v57, v58, v59\n\t"
"v_cvt_pk_bf16_f32 v73, v74, v75\n\t"
"v_mfma_f32_16x16x32_bf16 v[160:163], a[80:83], a[144:147], v[160:163]\n\t"
"ds_write2st64_b64 v247, v[88:89], v[92:93] offset0:26 offset1:39\n\t"
"v_mfma_f32_16x16x32_bf16 v[160:163], a[84:87], a[160:163], v[160:163]\n\t"
"buffer_load_dwordx4 a[136:139], v252, s[84:87], s66 offen offset:0 sc1\n\t"
"v_cvt_pk_bf16_f32 v60, v60, v61\n\t"
"v_mfma_f32_16x16x32_bf16 v[176:179], a[88:91], a[144:147], v[176:179]\n\t"
"v_cvt_pk_bf16_f32 v61, v62, v63\n\t"
"v_cvt_pk_bf16_f32 v77, v78, v79\n\t"
"v_mfma_f32_16x16x32_bf16 v[176:179], a[92:95], a[160:163], v[176:179]\n\t"
"ds_write2st64_b64 v246, v[72:73], v[76:77] offset0:26 offset1:39\n\t"
"v_mfma_f32_16x16x32_bf16 v[180:183], a[88:91], a[148:151], v[180:183]\n\t"
"buffer_load_dwordx4 a[140:143], v252, s[84:87], s66 offen offset:1024 sc1\n\t"
"v_mfma_f32_16x16x32_bf16 v[180:183], a[92:95], a[164:167], v[180:183]\n\t"
"ds_write2st64_b64 v228, v[56:57], v[60:61] offset0:26 offset1:39\n\t"
"v_mfma_f32_16x16x32_bf16 v[164:167], a[80:83], a[148:151], v[164:167]\n\t"
"buffer_load_dwordx4 a[132:135], v235, s[84:87], s66 offen offset:3072 sc1\n\t"
"v_mfma_f32_16x16x32_bf16 v[164:167], a[84:87], a[164:167], v[164:167]\n\t"
"buffer_load_dwordx4 a[128:131], v235, s[84:87], s66 offen offset:2048 sc1\n\t"
"buffer_load_dwordx4 a[120:123], v235, s[84:87], s66 offen offset:0 sc1\n\t"
"v_mfma_f32_16x16x32_bf16 v[148:151], a[72:75], a[148:151], v[148:151]\n\t"
"buffer_load_dwordx4 a[124:127], v235, s[84:87], s66 offen offset:1024 sc1\n\t"
"v_mfma_f32_16x16x32_bf16 v[148:151], a[76:79], a[164:167], v[148:151]\n\t"
"s_waitcnt lgkmcnt(0)\n\t"
"s_barrier\n\t"
"v_mfma_f32_16x16x32_bf16 v[152:155], a[72:75], a[152:155], v[152:155]\n\t"
"s_nop 0\n\t"
"ds_read_b128 a[144:147], v224 offset:16544\n\t"
"ds_read_b128 a[12:15], v229 offset:13312\n\t"
"v_mfma_f32_16x16x32_bf16 v[152:155], a[76:79], a[168:171], v[152:155]\n\t"
"ds_read_b128 a[16:19], v230 offset:13312\n\t"
"v_mfma_f32_16x16x32_bf16 v[168:171], a[80:83], a[152:155], v[168:171]\n\t"
"s_nop 0\n\t"
"ds_read_b128 a[160:163], v224 offset:18592\n\t"
"ds_read_b128 a[148:151], v224 offset:16800\n\t"
"v_mfma_f32_16x16x32_bf16 v[168:171], a[84:87], a[168:171], v[168:171]\n\t"
"s_nop 0\n\t"
"ds_read_b128 a[164:167], v224 offset:18848\n\t"
"ds_read_b128 a[0:3], v229 offset:0\n\t"
"v_mfma_f32_16x16x32_bf16 v[184:187], a[88:91], a[152:155], v[184:187]\n\t"
"ds_read_b128 a[4:7], v230 offset:0\n\t"
"v_mfma_f32_16x16x32_bf16 v[184:187], a[92:95], a[168:171], v[184:187]\n\t"
"s_nop 0\n\t"
"ds_read_b128 a[8:11], v231 offset:0\n\t"
"ds_read_b128 a[20:23], v231 offset:13312\n\t"
"v_mfma_f32_16x16x32_bf16 v[188:191], a[88:91], a[156:159], v[188:191]\n\t"
"s_waitcnt lgkmcnt(8)\n\t"
"buffer_store_dwordx4 a[12:15], v232, s[16:19], s27 offen nt\n\t"
"v_mfma_f32_16x16x32_bf16 v[188:191], a[92:95], a[172:175], v[188:191]\n\t"
"ds_read_b128 a[152:155], v224 offset:17568\n\t"
"v_mfma_f32_16x16x32_bf16 v[172:175], a[80:83], a[156:159], v[172:175]\n\t"
"ds_read_b128 a[168:171], v224 offset:19616\n\t"
"v_mfma_f32_16x16x32_bf16 v[172:175], a[84:87], a[172:175], v[172:175]\n\t"
"s_waitcnt lgkmcnt(5)\n\t"
"buffer_store_dwordx4 a[0:3], v232, s[16:19], s24 offen nt\n\t"
"v_mfma_f32_16x16x32_bf16 v[156:159], a[72:75], a[156:159], v[156:159]\n\t"
"v_mfma_f32_16x16x32_bf16 v[156:159], a[76:79], a[172:175], v[156:159]\n\t"
"s_waitcnt vmcnt(8)\n\t"
"buffer_store_dwordx4 a[16:19], v233, s[16:19], s27 offen nt\n\t"
"v_mfma_f32_16x16x32_bf16 v[144:147], a[96:99], a[144:147], v[144:147]\n\t"
"ds_read_b128 a[156:159], v224 offset:17824\n\t"
"v_mfma_f32_16x16x32_bf16 v[144:147], a[100:103], a[160:163], v[144:147]\n\t"
"s_waitcnt lgkmcnt(5)\n\t"
"buffer_store_dwordx4 a[4:7], v233, s[16:19], s24 offen nt\n\t"
"v_mfma_f32_16x16x32_bf16 v[160:163], a[104:107], a[144:147], v[160:163]\n\t"
"ds_read_b128 a[172:175], v224 offset:19872\n\t"
"v_mfma_f32_16x16x32_bf16 v[160:163], a[108:111], a[160:163], v[160:163]\n\t"
"s_waitcnt lgkmcnt(4)\n\t"
"buffer_store_dwordx4 a[20:23], v234, s[16:19], s27 offen nt\n\t"
"v_mfma_f32_16x16x32_bf16 v[176:179], a[112:115], a[144:147], v[176:179]\n\t"
"ds_read_b128 a[144:147], v224 offset:17056\n\t"
"v_mfma_f32_16x16x32_bf16 v[176:179], a[116:119], a[160:163], v[176:179]\n\t"
"v_mfma_f32_16x16x32_bf16 v[180:183], a[112:115], a[148:151], v[180:183]\n\t"
"buffer_store_dwordx4 a[8:11], v234, s[16:19], s24 offen nt\n\t"
"v_mfma_f32_16x16x32_bf16 v[180:183], a[116:119], a[164:167], v[180:183]\n\t"
"ds_read_b128 a[160:163], v224 offset:19104\n\t"
"v_mfma_f32_16x16x32_bf16 v[164:167], a[104:107], a[148:151], v[164:167]\n\t"
"v_mfma_f32_16x16x32_bf16 v[164:167], a[108:111], a[164:167], v[164:167]\n\t"
"v_mfma_f32_16x16x32_bf16 v[148:151], a[96:99], a[148:151], v[148:151]\n\t"
"v_mfma_f32_16x16x32_bf16 v[148:151], a[100:103], a[164:167], v[148:151]\n\t"
"s_waitcnt vmcnt(6) lgkmcnt(3)\n\t"
"v_mfma_f32_16x16x32_bf16 v[144:147], a[120:123], a[152:155], v[144:147]\n\t"
"ds_read_b128 a[164:167], v224 offset:19360\n\t"
"v_mfma_f32_16x16x32_bf16 v[144:147], a[124:127], a[168:171], v[144:147]\n\t"
"v_mfma_f32_16x16x32_bf16 v[160:163], a[128:131], a[152:155], v[160:163]\n\t"
"ds_read_b128 a[148:151], v224 offset:17312\n\t"
"v_mfma_f32_16x16x32_bf16 v[160:163], a[132:135], a[168:171], v[160:163]\n\t"
"s_nop 4\n\t"
"v_cvt_pk_bf16_f32 v144, v144, v145\n\t"
"v_mfma_f32_16x16x32_bf16 v[176:179], a[136:139], a[152:155], v[176:179]\n\t"
"v_mfma_f32_16x16x32_bf16 v[176:179], a[140:143], a[168:171], v[176:179]\n\t"
"s_waitcnt lgkmcnt(2)\n\t"
"ds_read_b128 a[168:171], v224 offset:20128\n\t"
"v_mfma_f32_16x16x32_bf16 v[180:183], a[136:139], a[156:159], v[180:183]\n\t"
"v_cvt_pk_bf16_f32 v160, v160, v161\n\t"
"v_mfma_f32_16x16x32_bf16 v[180:183], a[140:143], a[172:175], v[180:183]\n\t"
"ds_read_b128 a[152:155], v224 offset:18080\n\t"
"v_cvt_pk_bf16_f32 v145, v146, v147\n\t"
"v_mfma_f32_16x16x32_bf16 v[164:167], a[128:131], a[156:159], v[164:167]\n\t"
"v_cvt_pk_bf16_f32 v176, v176, v177\n\t"
"v_mfma_f32_16x16x32_bf16 v[164:167], a[132:135], a[172:175], v[164:167]\n\t"
"v_cvt_pk_bf16_f32 v177, v178, v179\n\t"
"v_mfma_f32_16x16x32_bf16 v[148:151], a[120:123], a[156:159], v[148:151]\n\t"
"ds_read_b128 a[156:159], v224 offset:18336\n\t"
"v_mfma_f32_16x16x32_bf16 v[148:151], a[124:127], a[172:175], v[148:151]\n\t"
"ds_read_b128 a[172:175], v224 offset:20384\n\t"
"v_mfma_f32_16x16x32_bf16 v[152:155], a[96:99], a[144:147], v[152:155]\n\t"
"v_cvt_pk_bf16_f32 v180, v180, v181\n\t"
"v_cvt_pk_bf16_f32 v181, v182, v183\n\t"
"v_mfma_f32_16x16x32_bf16 v[152:155], a[100:103], a[160:163], v[152:155]\n\t"
"ds_write2st64_b64 v247, v[176:177], v[180:181] offset0:52 offset1:65\n\t"
"v_mfma_f32_16x16x32_bf16 v[168:171], a[104:107], a[144:147], v[168:171]\n\t"
"v_cvt_pk_bf16_f32 v164, v164, v165\n\t"
"v_mfma_f32_16x16x32_bf16 v[168:171], a[108:111], a[160:163], v[168:171]\n\t"
"v_cvt_pk_bf16_f32 v161, v162, v163\n\t"
"v_cvt_pk_bf16_f32 v165, v166, v167\n\t"
"v_mfma_f32_16x16x32_bf16 v[184:187], a[112:115], a[144:147], v[184:187]\n\t"
"ds_write2st64_b64 v246, v[160:161], v[164:165] offset0:52 offset1:65\n\t"
"v_mfma_f32_16x16x32_bf16 v[184:187], a[116:119], a[160:163], v[184:187]\n\t"
"s_waitcnt lgkmcnt(4)\n\t"
"v_cvt_pk_bf16_f32 v148, v148, v149\n\t"
"v_cvt_pk_bf16_f32 v149, v150, v151\n\t"
"v_mfma_f32_16x16x32_bf16 v[188:191], a[112:115], a[148:151], v[188:191]\n\t"
"v_mfma_f32_16x16x32_bf16 v[188:191], a[116:119], a[164:167], v[188:191]\n\t"
"ds_write2st64_b64 v228, v[144:145], v[148:149] offset0:52 offset1:65\n\t"
"v_mfma_f32_16x16x32_bf16 v[172:175], a[104:107], a[148:151], v[172:175]\n\t"
"v_mfma_f32_16x16x32_bf16 v[172:175], a[108:111], a[164:167], v[172:175]\n\t"
"v_mfma_f32_16x16x32_bf16 v[156:159], a[96:99], a[148:151], v[156:159]\n\t"
"v_mfma_f32_16x16x32_bf16 v[156:159], a[100:103], a[164:167], v[156:159]\n\t"
"s_waitcnt lgkmcnt(0)\n\t"
"s_barrier\n\t"
"v_mfma_f32_16x16x32_bf16 v[152:155], a[120:123], a[152:155], v[152:155]\n\t"
"ds_read_b128 a[28:31], v230 offset:26624\n\t"
"v_mfma_f32_16x16x32_bf16 v[152:155], a[124:127], a[168:171], v[152:155]\n\t"
"ds_read_b128 a[24:27], v229 offset:26624\n\t"
"v_mfma_f32_16x16x32_bf16 v[168:171], a[128:131], a[152:155], v[168:171]\n\t"
"v_mfma_f32_16x16x32_bf16 v[168:171], a[132:135], a[168:171], v[168:171]\n\t"
"ds_read_b128 a[32:35], v231 offset:26624\n\t"
"s_nop 3\n\t"
"v_cvt_pk_bf16_f32 v152, v152, v153\n\t"
"v_mfma_f32_16x16x32_bf16 v[184:187], a[136:139], a[152:155], v[184:187]\n\t"
"v_cvt_pk_bf16_f32 v153, v154, v155\n\t"
"v_mfma_f32_16x16x32_bf16 v[184:187], a[140:143], a[168:171], v[184:187]\n\t"
"v_cvt_pk_bf16_f32 v168, v168, v169\n\t"
"v_cvt_pk_bf16_f32 v169, v170, v171\n\t"
"v_mfma_f32_16x16x32_bf16 v[188:191], a[136:139], a[156:159], v[188:191]\n\t"
"s_waitcnt lgkmcnt(2)\n\t"
"buffer_store_dwordx4 a[28:31], v233, s[16:19], s20 offen nt\n\t"
"v_mfma_f32_16x16x32_bf16 v[188:191], a[140:143], a[172:175], v[188:191]\n\t"
"s_nop 1\n\t"
"v_cvt_pk_bf16_f32 v184, v184, v185\n\t"
"v_mfma_f32_16x16x32_bf16 v[172:175], a[128:131], a[156:159], v[172:175]\n\t"
"v_cvt_pk_bf16_f32 v185, v186, v187\n\t"
"v_mfma_f32_16x16x32_bf16 v[172:175], a[132:135], a[172:175], v[172:175]\n\t"
"s_waitcnt lgkmcnt(1)\n\t"
"buffer_store_dwordx4 a[24:27], v232, s[16:19], s20 offen nt\n\t"
"v_cvt_pk_bf16_f32 v188, v188, v189\n\t"
"v_cvt_pk_bf16_f32 v189, v190, v191\n\t"
"v_mfma_f32_16x16x32_bf16 v[156:159], a[120:123], a[156:159], v[156:159]\n\t"
"ds_write2st64_b64 v247, v[184:185], v[188:189] offset0:78 offset1:91\n\t"
"v_mfma_f32_16x16x32_bf16 v[156:159], a[124:127], a[172:175], v[156:159]\n\t"
"s_waitcnt lgkmcnt(1)\n\t"
"buffer_store_dwordx4 a[32:35], v234, s[16:19], s20 offen nt\n\t"
"s_nop 5\n\t"
"v_cvt_pk_bf16_f32 v156, v156, v157\n\t"
"v_cvt_pk_bf16_f32 v157, v158, v159\n\t"
"ds_write2st64_b64 v228, v[152:153], v[156:157] offset0:78 offset1:91\n\t"
"v_cvt_pk_bf16_f32 v172, v172, v173\n\t"
"v_cvt_pk_bf16_f32 v173, v174, v175\n\t"
"ds_write2st64_b64 v246, v[168:169], v[172:173] offset0:78 offset1:91\n\t"
"s_waitcnt lgkmcnt(0)\n\t"
"s_barrier\n\t"
"ds_read_b128 a[36:39], v229 offset:39936\n\t"
"ds_read_b128 a[40:43], v230 offset:39936\n\t"
"ds_read_b128 a[44:47], v231 offset:39936\n\t"
"s_waitcnt lgkmcnt(2)\n\t"
"buffer_store_dwordx4 a[36:39], v232, s[16:19], s25 offen nt\n\t"
"s_waitcnt lgkmcnt(1)\n\t"
"buffer_store_dwordx4 a[40:43], v233, s[16:19], s25 offen nt\n\t"
"s_waitcnt lgkmcnt(0)\n\t"
"buffer_store_dwordx4 a[44:47], v234, s[16:19], s25 offen nt\n\t"
"s_waitcnt vmcnt(0)\n\t"
"Lwino_end:\n\t"
  : "+{v205}"(tid)
  : "s"(__builtin_amdgcn_kernarg_segment_ptr()), "s"(blockIdx.x), "s"((unsigned)(size_t)(__attribute__((address_space(3))) char*)smem)
  : "v0", "v1", "v2", "v3", "v4", "v5", "v6", "v7", "v8", "v9", "v10", "v11", "v12", "v13", "v14", "v15", "v16", "v17", "v18", "v19", "v20", "v21", "v22", "v23", "v24", "v25", "v26", "v27", "v28", "v29", "v30", "v31", "v32", "v33", "v34", "v35", "v36", "v37", "v38", "v39", "v40", "v41", "v42", "v43", "v44", "v45", "v46", "v47", "v48", "v49", "v50", "v51", "v52", "v53", "v54", "v55", "v56", "v57", "v58", "v59", "v60", "v61", "v62", "v63", "v64", "v65", "v66", "v67", "v68", "v69", "v70", "v71", "v72", "v73", "v74", "v75", "v76", "v77", "v78", "v79", "v80", "v81", "v82", "v83", "v84", "v85", "v86", "v87", "v88", "v89", "v90", "v91", "v92", "v93", "v94", "v95", "v96", "v97", "v98", "v99", "v100", "v101", "v102", "v103", "v104", "v105", "v106", "v107", "v108", "v109", "v110", "v111", "v112", "v113", "v114", "v115", "v116", "v117", "v118", "v119", "v120", "v121", "v122", "v123", "v124", "v125", "v126", "v127", "v128", "v129", "v130", "v131", "v132", "v133", "v134", "v135", "v136", "v137", "v138", "v139", "v140", "v141", "v142", "v143", "v144", "v145", "v146", "v147", "v148", "v149", "v150", "v151", "v152", "v153", "v154", "v155", "v156", "v157", "v158", "v159", "v160", "v161", "v162", "v163", "v164", "v165", "v166", "v167", "v168", "v169", "v170", "v171", "v172", "v173", "v174", "v175", "v176", "v177", "v178", "v179", "v180", "v181", "v182", "v183", "v184", "v185", "v186", "v187", "v188", "v189", "v190", "v191", "v192", "v193", "v194", "v195", "v196", "v197", "v198", "v199", "v200", "v201", "v202", "v203", "v204", "v206", "v207", "v208", "v209", "v210", "v211", "v212", "v213", "v214", "v215", "v216", "v217", "v218", "v219", "v220", "v221", "v222", "v223", "v224", "v225", "v226", "v227", "v228", "v229", "v230", "v231", "v232", "v233", "v234", "v235", "v236", "v237", "v238", "v239", "v240", "v241", "v242", "v243", "v244", "v245", "v246", "v247", "v248", "v249", "v250", "v251", "v252", "v253", "v254", "v255", "a0", "a1", "a2", "a3", "a4", "a5", "a6", "a7", "a8", "a9", "a10", "a11", "a12", "a13", "a14", "a15", "a16", "a17", "a18", "a19", "a20", "a21", "a22", "a23", "a24", "a25", "a26", "a27", "a28", "a29", "a30", "a31", "a32", "a33", "a34", "a35", "a36", "a37", "a38", "a39", "a40", "a41", "a42", "a43", "a44", "a45", "a46", "a47", "a48", "a49", "a50", "a51", "a52", "a53", "a54", "a55", "a56", "a57", "a58", "a59", "a60", "a61", "a62", "a63", "a64", "a65", "a66", "a67", "a68", "a69", "a70", "a71", "a72", "a73", "a74", "a75", "a76", "a77", "a78", "a79", "a80", "a81", "a82", "a83", "a84", "a85", "a86", "a87", "a88", "a89", "a90", "a91", "a92", "a93", "a94", "a95", "a96", "a97", "a98", "a99", "a100", "a101", "a102", "a103", "a104", "a105", "a106", "a107", "a108", "a109", "a110", "a111", "a112", "a113", "a114", "a115", "a116", "a117", "a118", "a119", "a120", "a121", "a122", "a123", "a124", "a125", "a126", "a127", "a128", "a129", "a130", "a131", "a132", "a133", "a134", "a135", "a136", "a137", "a138", "a139", "a140", "a141", "a142", "a143", "a144", "a145", "a146", "a147", "a148", "a149", "a150", "a151", "a152", "a153", "a154", "a155", "a156", "a157", "a158", "a159", "a160", "a161", "a162", "a163", "a164", "a165", "a166", "a167", "a168", "a169", "a170", "a171", "a172", "a173", "a174", "a175", "a176", "a177", "a178", "a179", "a180", "a181", "a182", "a183", "a184", "a185", "a186", "a187", "a188", "a189", "a190", "a191", "a192", "a193", "a194", "a195", "a196", "a197", "a198", "a199", "a200", "a201", "a202", "a203", "a204", "a205", "a206", "a207", "a208", "a209", "a210", "a211", "a212", "a213", "a214", "a215", "a216", "a217", "a218", "a219", "a220", "a221", "a222", "a223", "a224", "a225", "a226", "a227", "a228", "a229", "a230", "a231", "a232", "a233", "a234", "a235", "a236", "a237", "a238", "a239", "a240", "a241", "a242", "a243", "a244", "a245", "a246", "a247", "a248", "a249", "a250", "a251", "a252", "a253", "a254", "a255", "s6", "s7", "s8", "s9", "s10", "s11", "s12", "s13", "s14", "s15", "s16", "s17", "s18", "s19", "s20", "s21", "s22", "s23", "s24", "s25", "s26", "s27", "s28", "s29", "s30", "s31", "s32", "s33", "s34", "s35", "s36", "s37", "s38", "s39", "s40", "s41", "s42", "s43", "s44", "s45", "s46", "s47", "s48", "s49", "s50", "s51", "s52", "s53", "s54", "s55", "s56", "s57", "s58", "s59", "s60", "s61", "s62", "s63", "s64", "s65", "s66", "s67", "s68", "s69", "s70", "s71", "s72", "s73", "s74", "s75", "s76", "s77", "s78", "s79", "s80", "s81", "s82", "s83", "s84", "s85", "s86", "s87", "s88", "s89", "s90", "s91", "scc", "vcc", "memory");
}

extern "C" void kernel_launch(void* const* d_in, const int* in_sizes, int n_in, void* d_out, int out_size, void* d_ws,
                              size_t ws_size, hipStream_t stream) {
  wino_main_kernel<<<256, 256, 0, stream>>>((const char*)d_in[0], (char*)d_ws, (char*)d_out, (const char*)d_in[1]);
}
```

```cpp
#include <hip/hip_runtime.h>
#include <hip/hip_bf16.h>
#include <stdint.h>

#define U_BYTES 1179648

__global__ __launch_bounds__(256, 1) void wino_main_kernel(const char* __restrict__ x, char* __restrict__ Uq, char* __restrict__ out, const char* __restrict__ wt) {
  __shared__ __attribute__((aligned(1024))) char smem[163840];
  unsigned tid = threadIdx.x;
  asm volatile(
"s_load_dwordx8 s[76:83], %1, 0x0\n\t"
"s_mov_b32 s66, %2\n\t"
"s_getpc_b64 s[6:7]\n\t"
"Lwino_pc:\n\t"
"s_bfe_u32 s42, %2, 0x50003\n\t"
"s_lshl_b32 s42, s42, 12\n\t"
"v_lshl_add_u32 v203, v205, 4, s42\n\t"
"v_min_u32 v203, (Lwino_end-Lwino_pc-16)&-16, v203\n\t"
"global_load_dwordx4 v[196:199], v203, s[6:7]\n\t"
"v_and_b32 v204, 63, v205\n\t"
"v_readfirstlane_b32 s73, v205\n\t"
"v_lshrrev_b32 v120, 3, v204\n\t"
"v_and_b32 v121, 7, v204\n\t"
"s_lshr_b32 s73, s73, 6\n\t"
"s_lshr_b32 s56, s66, 5\n\t"
"s_mul_i32 s56, s56, 12288\n\t"
"s_bfe_u32 s61, s66, 0x20001\n\t"
"s_mul_i32 s61, s61, 1536\n\t"
"s_add_u32 s56, s56, s61\n\t"
"s_bitcmp1_b32 s66, 0\n\t"
"s_cselect_b32 s61, 48, 0\n\t"
"s_add_u32 s56, s56, s61\n\t"
"s_bfe_u32 s62, s66, 0x20003\n\t"
"s_mul_i32 s61, s62, 96\n\t"
"s_add_u32 s56, s56, s61\n\t"
"s_cmp_lt_u32 s66, 192\n\t"
"s_cselect_b32 s56, s56, 2.0\n\t"
"v_bfe_u32 v42, v204, 3, 2\n\t"
"v_lshlrev_b32 v43, 2, v204\n\t"
"v_and_or_b32 v42, v43, 16, v42\n\t"
"v_mul_u32_u24 v42, 0x180, v42\n\t"
"v_lshrrev_b32 v44, 5, v204\n\t"
"v_mul_u32_u24 v43, 0x12000, v44\n\t"
"v_and_b32 v45, 3, v204\n\t"
"v_min_u32 v41, 2, v45\n\t"
"v_lshl_add_u32 v41, v41, 4, v42\n\t"
"v_add_u32 v41, v41, v43\n\t"
"v_lshlrev_b32 v43, 4, v204\n\t"
"v_bfe_u32 v45, v204, 1, 2\n\t"
"v_and_or_b32 v45, v43, 16, v45\n\t"
"v_mul_u32_u24 v45, 0x180, v45\n\t"
"v_lshl_add_u32 v44, v44, 3, v120\n\t"
"v_lshl_add_u32 v45, v44, 1, v45\n\t"
"v_mov_b32 v40, 0\n\t"
"s_mul_i32 s61, s73, 147456\n\t"
"s_add_u32 s61, s61, s56\n\t"
"s_lshl3_add_u32 s62, s73, s56\n\t"
"s_add_u32 s62, s62, 589824\n\t"
"s_lshl_b32 s64, s73, 10\n\t"
"s_and_b32 s60, s66, 7\n\t"
"s_lshr_b32 s65, s66, 3\n\t"
"s_mul_i32 s24, s60, 12582912\n\t"
"s_mul_i32 s63, s65, 6144\n\t"
"s_add_u32 s24, s24, s63\n\t"
"s_add_u32 s22, s24, 196224\n\t"
"s_mul_i32 s63, s73, 786432\n\t"
"s_mul_i32 s26, s73, 4608\n\t"
"s_mov_b32 s48, 189696\n\t"
"s_cmp_eq_u32 s65, 0\n\t"
"s_cselect_b64 s[40:41], 0xff, 0\n\t"
"s_cselect_b64 s[46:47], -1, 0\n\t"
"v_mul_u32_u24 v122, 0x300, v120\n\t"
"v_xor_b32 v123, 4, v121\n\t"
"v_add_u32 v122, s63, v122\n\t"
"v_and_b32 v124, 2, v120\n\t"
"v_xor_b32 v124, v124, v123\n\t"
"v_lshl_add_u32 v128, v124, 4, v122\n\t"
"v_cndmask_b32 v241, v128, 2.0, s[40:41]\n\t"
"v_add_u32 v122, 194816, v122\n\t"
"v_add_u32 v124, -1, v120\n\t"
"v_ashrrev_i32 v125, 31, v124\n\t"
"v_sub_u32 v124, v124, v125\n\t"
"v_and_b32 v124, 2, v124\n\t"
"v_xor_b32 v124, v124, v123\n\t"
"v_mad_i32_i24 v125, v125, s48, v122\n\t"
"v_lshl_add_u32 v129, v124, 4, v125\n\t"
"s_lshl_b64 s[40:41], s[40:41], 8\n\t"
"v_cndmask_b32 v242, v129, 2.0, s[40:41]\n\t"
"v_add_u32 v122, 194816, v122\n\t"
"v_add_u32 v124, -2, v120\n\t"
"v_ashrrev_i32 v125, 31, v124\n\t"
"v_sub_u32 v124, v124, v125\n\t"
"v_and_b32 v124, 2, v124\n\t"
"v_and_or_b32 v124, v125, 4, v124\n\t"
"v_xor_b32 v124, v124, v121\n\t"
"v_mad_i32_i24 v125, v125, s48, v122\n\t"
"v_lshl_add_u32 v130, v124, 4, v125\n\t"
"s_lshl_b64 s[40:41], s[40:41], 8\n\t"
"v_cndmask_b32 v243, v130, 2.0, s[40:41]\n\t"
"v_add_u32 v122, 194816, v122\n\t"
"v_add_u32 v124, -3, v120\n\t"
"v_ashrrev_i32 v125, 31, v124\n\t"
"v_sub_u32 v124, v124, v125\n\t"
"v_and_b32 v124, 2, v124\n\t"
"v_xor_b32 v124, v124, v121\n\t"
"v_mad_i32_i24 v125, v125, s48, v122\n\t"
"v_lshl_add_u32 v131, v124, 4, v125\n\t"
"s_lshl_b64 s[40:41], s[40:41], 8\n\t"
"v_cndmask_b32 v244, v131, 2.0, s[40:41]\n\t"
"v_add_u32 v124, 5, v120\n\t"
"v_and_b32 v124, 2, v124\n\t"
"v_xor_b32 v124, v124, v121\n\t"
"v_add_u32 v122, 2560, v122\n\t"
"v_lshl_add_u32 v245, v124, 4, v122\n\t"
"s_mov_b32 s37, -1\n\t"
"s_mov_b32 s36, 0\n\t"
"s_mul_i32 s67, s73, 48\n\t"
"s_add_u32 s67, s67, 118784\n\t"
"s_mov_b32 s10, 0x200000\n\t"
"s_mov_b32 s11, 0x20000\n\t"
"v_min_u32 v189, 47, v204\n\t"
"v_add_u32 v189, s67, v189\n\t"
"v_mul_u32_u24 v189, 16, v189\n\t"
"s_mov_b32 s54, 663552\n\t"
"s_mov_b32 s55, 0x20000\n\t"
"s_mov_b32 s14, 100663296\n\t"
"s_mov_b32 s15, 0x20000\n\t"
"s_add_u32 m0, s64, 0\n\t"
"s_waitcnt lgkmcnt(0)\n\t"
"s_mov_b32 s52, s82\n\t"
"s_and_b32 s53, s83, 0xffff\n\t"
"buffer_load_dwordx4 v41, s[52:55], s61 offen lds\n\t"
"buffer_load_short_d16_hi v40, v45, s[52:55], s62 offen\n\t"
"s_mov_b32 s8, s78\n\t"
"s_and_b32 s9, s79, 0xffff\n\t"
"buffer_load_dword v188, v189, s[8:11], 0 offen sc1\n\t"
"s_mov_b32 s12, s76\n\t"
"s_and_b32 s13, s77, 0xffff\n\t"
"s_add_u32 m0, s26, 103936\n\t"
"s_nop 0\n\t"
"buffer_load_dwordx4 v241, s[12:15], s22 offen offset:0 nt lds\n\t"
"buffer_load_dwordx4 v242, s[12:15], s22 offen offset:1024 nt lds\n\t"
"buffer_load_dwordx4 v243, s[12:15], s22 offen offset:2048 nt lds\n\t"
"buffer_load_dwordx4 v244, s[12:15], s22 offen offset:3072 nt lds\n\t"
"s_mov_b64 exec, s[36:37]\n\t"
"buffer_load_dwordx4 v245, s[12:15], s22 offen offset:3584 nt lds\n\t"
"s_mov_b64 exec, -1\n\t"
"s_mul_i32 s49, s73, 36\n\t"
"s_mul_i32 s43, s73, 576\n\t"
"s_sub_u32 s42, s22, 203520\n\t"
"s_mov_b32 s44, -1\n\t"
"s_mov_b32 s45, 15\n\t"
"v_min_u32 v124, 35, v204\n\t"
"v_add_u32 v124, s49, v124\n\t"
"v_lshrrev_b32 v119, 3, v124\n\t"
"v_and_b32 v124, 7, v124\n\t"
"v_mul_u32_u24 v127, 0x300, v119\n\t"
"v_cmp_gt_u32 vcc, 9, v119\n\t"
"v_add_u32 v119, -9, v119\n\t"
"v_ashrrev_i32 v125, 31, v119\n\t"
"v_add_u32 v127, s42, v127\n\t"
"v_mad_i32_i24 v127, v125, s48, v127\n\t"
"v_and_b32 v125, 9, v125\n\t"
"v_add_u32 v119, v119, v125\n\t"
"v_and_b32 v125, 2, v119\n\t"
"v_xor_b32 v124, v124, v125\n\t"
"v_lshl_add_u32 v133, v124, 4, v127\n\t"
"s_cmp_eq_u32 s60, 0\n\t"
"s_cselect_b64 s[50:51], vcc, 0\n\t"
"v_cmp_eq_u32 vcc, 0, v119\n\t"
"v_cndmask_b32 v246, v133, 2.0, s[50:51]\n\t"
"s_and_b64 vcc, vcc, s[46:47]\n\t"
"v_cndmask_b32 v246, v246, 2.0, vcc\n\t"
"s_add_u32 m0, s43, 101632\n\t"
"s_mov_b64 exec, s[44:45]\n\t"
"buffer_load_dwordx4 v246, s[12:15], 0 offen offset:0 nt lds\n\t"
"s_mov_b64 exec, -1\n\t"
"s_mul_i32 s70, s66, 16\n\t"
"s_add_u32 s70, s70, 1900544\n\t"
"s_lshl_b32 s61, s73, 8\n\t"
"s_or_b32 s61, s61, s66\n\t"
"s_cmp_lt_u32 s61, 192\n\t"
"s_cselect_b32 s70, s70, 2.0\n\t"
"s_lshr_b32 s61, s66, 3\n\t"
"s_lshl2_add_u32 s61, s61, s73\n\t"
"s_lshl_b32 s60, s61, 8\n\t"
"s_cmp_lt_u32 s61, 96\n\t"
"s_cselect_b32 s60, s60, 2.0\n\t"
"v_lshrrev_b32 v118, 1, v204\n\t"
"v_mul_u32_u24 v118, 220928, v118\n\t"
"v_lshl_add_u32 v118, v204, 7, v118\n\t"
"s_mov_b64 exec, 63\n\t"
"buffer_load_dword v118, v118, s[52:55], s60 offen\n\t"
"s_mov_b64 exec, -1\n\t"
"buffer_load_dword v187, off, s[8:11], s70 sc1\n\t"
"v_add_u32 v107, 0, v204\n\t"
"v_mul_u32_u24 v104, 0xab, v107\n\t"
"v_lshrrev_b32 v104, 10, v104\n\t"
"v_mul_u32_u24 v104, 0x120, v104\n\t"
"v_lshl_add_u32 v104, v107, 4, v104\n\t"
"v_add_u32 v107, 64, v204\n\t"
"v_mul_u32_u24 v105, 0xab, v107\n\t"
"v_lshrrev_b32 v105, 10, v105\n\t"
"v_mul_u32_u24 v105, 0x120, v105\n\t"
"v_lshl_add_u32 v105, v107, 4, v105\n\t"
"v_add_u32 v107, 128, v204\n\t"
"v_mul_u32_u24 v106, 0xab, v107\n\t"
"v_lshrrev_b32 v106, 10, v106\n\t"
"v_mul_u32_u24 v106, 0x120, v106\n\t"
"v_lshl_add_u32 v106, v107, 4, v106\n\t"
"s_mul_i32 s74, s73, 20736\n\t"
"s_mul_i32 s72, s73, 96\n\t"
"s_add_u32 s74, s74, 33280\n\t"
"s_add_u32 s75, s74, 12288\n\t"
"s_cmp_eq_u32 s73, 3\n\t"
"s_cselect_b32 s74, 16544, s74\n\t"
"s_cselect_b32 s75, 95488, s75\n\t"
"s_lshr_b32 s60, s66, 5\n\t"
"s_bfe_u32 s62, s66, 0x20003\n\t"
"s_and_b32 s61, s60, 1\n\t"
"s_lshl1_add_u32 s61, s73, s61\n\t"
"s_mul_i32 s62, s62, 288\n\t"
"s_add_u32 s61, s61, s62\n\t"
"s_lshr_b32 s60, s60, 1\n\t"
"s_mul_i32 s62, s60, 96\n\t"
"s_add_u32 s61, s61, s62\n\t"
"s_lshl_b32 s57, s61, 10\n\t"
"s_and_b32 s61, s66, 7\n\t"
"s_lshl_b32 s61, s61, 7\n\t"
"s_add_u32 s57, s57, s61\n\t"
"s_cmp_lt_u32 s73, 3\n\t"
"s_cselect_b32 s67, s66, 0xff\n\t"
"s_cmp_lt_u32 s67, 192\n\t"
"s_cselect_b32 s57, s57, 2.0\n\t"
"s_lshl_b32 s60, s73, 3\n\t"
"s_add_u32 s60, s60, 0\n\t"
"s_lshl_b32 s61, s73, 11\n\t"
"s_add_u32 s61, s61, 4096\n\t"
"v_lshl_add_u32 v46, v121, 6, s60\n\t"
"v_lshl_add_u32 v46, v44, 1, v46\n\t"
"v_lshl_add_u32 v42, v204, 1, s61\n\t"
"v_lshl_add_u32 v43, v204, 4, s61\n\t"
"v_mul_u32_u24 v44, 6144, v120\n\t"
"v_lshl_add_u32 v44, v121, 4, v44\n\t"
"v_mov_b32 v32, 0\n\t"
"v_mov_b32 v33, 0\n\t"
"v_mov_b32 v34, 0\n\t"
"v_mov_b32 v35, 0\n\t"
"v_mov_b32 v36, 0\n\t"
"v_mov_b32 v37, 0\n\t"
"v_mov_b32 v38, 0\n\t"
"v_mov_b32 v39, 0\n\t"
"s_waitcnt vmcnt(9)\n\t"
"s_barrier\n\t"
".p2align 3\n\t"
"ds_read_u16_d16_hi v32, v46 offset:0\n\t"
"ds_read_u16_d16_hi v33, v46 offset:512\n\t"
"ds_read_u16_d16_hi v34, v46 offset:1024\n\t"
"ds_read_u16_d16_hi v35, v46 offset:1536\n\t"
"ds_read_u16_d16_hi v36, v46 offset:2048\n\t"
"ds_read_u16_d16_hi v37, v46 offset:2560\n\t"
"ds_read_u16_d16_hi v38, v46 offset:3072\n\t"
"ds_read_u16_d16_hi v39, v46 offset:3584\n\t"
"s_waitcnt lgkmcnt(1)\n\t"
"v_add_f32 v48, v32, v38\n\t"
"s_waitcnt lgkmcnt(0)\n\t"
"v_add_f32 v49, v33, v39\n\t"
"v_add_f32 v50, v34, v40\n\t"
"v_add_f32 v51, v48, v35\n\t"
"v_sub_f32 v54, v48, v35\n\t"
"v_add_f32 v52, v49, v36\n\t"
"v_sub_f32 v55, v49, v36\n\t"
"v_add_f32 v53, v50, v37\n\t"
"v_sub_f32 v56, v50, v37\n\t"
"v_add_f32 v57, v32, v34\n\t"
"v_add_f32 v58, v51, v53\n\t"
"v_add_f32 v59, v54, v56\n\t"
"v_add_f32 v60, v38, v40\n\t"
"v_add_f32 v62, v57, v33\n\t"
"v_sub_f32 v63, v57, v33\n\t"
"v_add_f32 v64, v58, v52\n\t"
"v_sub_f32 v65, v58, v52\n\t"
"v_add_f32 v66, v59, v55\n\t"
"v_sub_f32 v67, v59, v55\n\t"
"v_add_f32 v68, v60, v39\n\t"
"v_sub_f32 v69, v60, v39\n\t"
"v_mul_f32 v71, 0.5, v62\n\t"
"v_mul_f32 v72, 0.5, v63\n\t"
"v_mul_f32 v74, 0.5, v51\n\t"
"v_mul_f32 v75, 0x3e800000, v64\n\t"
"v_mul_f32 v76, 0x3e800000, v65\n\t"
"v_mul_f32 v77, -0.5, v53\n\t"
"v_mul_f32 v79, 0.5, v66\n\t"
"v_mul_f32 v80, 0.5, v67\n\t"
"v_mul_f32 v83, -0.5, v68\n\t"
"v_mul_f32 v84, -0.5, v69\n\t"
".p2align 3\n\t"
"v_cvt_pk_bf16_f32 v86, v32, v71\n\t"
"v_cvt_pk_bf16_f32 v87, v72, -v34\n\t"
"v_cvt_pk_bf16_f32 v88, v74, v75\n\t"
"v_cvt_pk_bf16_f32 v89, v76, v77\n\t"
"v_cvt_pk_bf16_f32 v90, v54, v79\n\t"
"v_cvt_pk_bf16_f32 v91, v80, -v56\n\t"
"v_cvt_pk_bf16_f32 v92, -v38, v83\n\t"
"v_cvt_pk_bf16_f32 v93, v84, v40\n\t"
"ds_write_b16 v42, v86 offset:0\n\t"
"ds_write_b16_d16_hi v42, v86 offset:128\n\t"
"ds_write_b16 v42, v87 offset:256\n\t"
"ds_write_b16_d16_hi v42, v87 offset:384\n\t"
"ds_write_b16 v42, v88 offset:512\n\t"
"ds_write_b16_d16_hi v42, v88 offset:640\n\t"
"ds_write_b16 v42, v89 offset:768\n\t"
"ds_write_b16_d16_hi v42, v89 offset:896\n\t"
"ds_write_b16 v42, v90 offset:1024\n\t"
"ds_write_b16_d16_hi v42, v90 offset:1152\n\t"
"ds_write_b16 v42, v91 offset:1280\n\t"
"ds_write_b16_d16_hi v42, v91 offset:1408\n\t"
"ds_write_b16 v42, v92 offset:1536\n\t"
"ds_write_b16_d16_hi v42, v92 offset:1664\n\t"
"ds_write_b16 v42, v93 offset:1792\n\t"
"ds_write_b16_d16_hi v42, v93 offset:1920\n\t"
"ds_read_b128 v[96:99], v43 offset:0\n\t"
"ds_read_b128 v[100:103], v43 offset:1024\n\t"
"s_add_u32 s60, s57, 0\n\t"
"s_waitcnt lgkmcnt(1)\n\t"
"buffer_store_dwordx4 v[96:99], v44, s[8:11], s60 offen sc1\n\t"
"s_add_u32 s60, s57, 49152\n\t"
"s_waitcnt lgkmcnt(0)\n\t"
"buffer_store_dwordx4 v[100:103], v44, s[8:11], s60 offen sc1\n\t"
"v_lshrrev_b32 v123, 4, v204\n\t"
"v_bfe_u32 v124, v204, 2, 2\n\t"
"v_and_b32 v125, 3, v204\n\t"
"v_lshlrev_b32 v125, 3, v125\n\t"
"v_and_b32 v138, 49, v204\n\t"
"v_lshl_or_b32 v138, v124, 1, v138\n\t"
"v_and_b32 v139, 2, v204\n\t"
"v_lshl_or_b32 v138, v139, 2, v138\n\t"
"s_add_u32 s63, s26, 1152\n\t"
"v_and_b32 v126, 48, v204\n\t"
"v_and_b32 v127, 6, v204\n\t"
"v_lshl_add_u32 v126, v127, 5, v126\n\t"
"v_and_b32 v127, 8, v204\n\t"
"s_add_u32 m0, s74, 0\n\t"
"s_add_u32 s27, s72, 0\n\t"
"buffer_load_dwordx4 v104, s[52:55], s27 offen lds\n\t"
"v_mul_u32_u24 v134, 516, v127\n\t"
"v_and_b32 v127, 1, v204\n\t"
"v_mul_u32_u24 v127, 8320, v127\n\t"
"v_add3_u32 v224, v134, v126, v127\n\t"
"v_and_b32 v127, 16, v204\n\t"
"v_lshl_add_u32 v127, v127, 1, v125\n\t"
"v_lshl_add_u32 v127, v123, 8, v127\n\t"
"v_add_u32 v126, 4, v204\n\t"
"v_and_b32 v126, 8, v126\n\t"
"v_lshl_add_u32 v127, v126, 3, v127\n\t"
"v_mul_u32_u24 v126, 1152, v124\n\t"
"v_add3_u32 v226, v126, v127, s63\n\t"
"s_add_u32 m0, s74, 1024\n\t"
"s_add_u32 s27, s72, 0\n\t"
"buffer_load_dwordx4 v105, s[52:55], s27 offen lds\n\t"
"v_min_u32 v126, 2, v124\n\t"
"v_cmp_gt_u32 vcc, 48, v204\n\t"
"s_mov_b32 s48, 384\n\t"
"s_cmp_eq_u32 s65, 31\n\t"
"v_cndmask_b32 v126, v126, v123, vcc\n\t"
"v_cndmask_b32 v127, 4, v124, vcc\n\t"
"v_and_b32 v134, 1, v126\n\t"
"v_lshl_add_u32 v134, v134, 6, v125\n\t"
"v_lshl_add_u32 v134, v127, 7, v134\n\t"
"v_and_b32 v127, 2, v127\n\t"
"v_lshl_add_u32 v134, v127, 4, v134\n\t"
"v_mul_u32_u24 v126, 2304, v126\n\t"
"v_add3_u32 v227, v126, v134, s26\n\t"
"v_lshlrev_b32 v126, 14, v204\n\t"
"v_and_b32 v126, 0x8000, v126\n\t"
"v_or_b32 v134, 0x3f803f80, v126\n\t"
"v_and_b32 v126, 1, v204\n\t"
"v_cmp_eq_u32 vcc, 0, v126\n\t"
"s_cselect_b64 s[40:41], 0xff, 0\n\t"
"s_cselect_b64 s[46:47], -1, 0\n\t"
"s_add_u32 m0, s74, 2048\n\t"
"s_add_u32 s27, s72, 0\n\t"
"buffer_load_dwordx4 v106, s[52:55], s27 offen lds\n\t"
"v_cndmask_b32 v135, v134, 0, vcc\n\t"
"v_cndmask_b32 v134, 0, v134, vcc\n\t"
"v_xor_b32 v136, 0x80008000, v134\n\t"
"v_xor_b32 v137, 0x80008000, v135\n\t"
"s_add_u32 m0, s74, 3072\n\t"
"s_add_u32 s27, s72, 12288\n\t"
"buffer_load_dwordx4 v104, s[52:55], s27 offen lds\n\t"
"v_accvgpr_write_b32 a220, v134\n\t"
"v_accvgpr_write_b32 a221, v135\n\t"
"v_accvgpr_write_b32 a222, v136\n\t"
"v_accvgpr_write_b32 a223, v137\n\t"
"v_xad_u32 v236, v128, 64, s48\n\t"
"v_xad_u32 v237, v129, 64, s48\n\t"
"v_cndmask_b32 v237, v237, 2.0, s[40:41]\n\t"
"s_lshl_b64 s[40:41], s[40:41], 8\n\t"
"v_xad_u32 v238, v130, 64, s48\n\t"
"v_cndmask_b32 v238, v238, 2.0, s[40:41]\n\t"
"s_lshl_b64 s[40:41], s[40:41], 8\n\t"
"v_xad_u32 v239, v131, 64, s48\n\t"
"v_cndmask_b32 v239, v239, 2.0, s[40:41]\n\t"
"s_add_u32 m0, s74, 4096\n\t"
"s_add_u32 s27, s72, 12288\n\t"
"buffer_load_dwordx4 v105, s[52:55], s27 offen lds\n\t"
"s_lshl_b64 s[40:41], s[40:41], 40\n\t"
"v_xad_u32 v240, v245, 64, s48\n\t"
"v_cndmask_b32 v240, v240, 2.0, s[40:41]\n\t"
"v_cmp_eq_u32 vcc, 8, v119\n\t"
"v_xad_u32 v247, v133, 64, s48\n\t"
"s_and_b64 vcc, vcc, s[46:47]\n\t"
"v_cndmask_b32 v247, v247, 2.0, s[50:51]\n\t"
"v_cndmask_b32 v247, v247, 2.0, vcc\n\t"
"v_and_b32 v139, 7, v138\n\t"
"v_bfe_u32 v126, v138, 3, 1\n\t"
"v_lshrrev_b32 v127, 4, v138\n\t"
"v_sub_u32 v127, v127, v126\n\t"
"s_add_u32 m0, s74, 5120\n\t"
"s_add_u32 s27, s72, 12288\n\t"
"buffer_load_dwordx4 v106, s[52:55], s27 offen lds\n\t"
"v_mov_b32 v134, 0xbf803f80\n\t"
"v_mov_b32 v135, 0x3f80bf80\n\t"
"v_cmp_eq_u32 vcc, 0, v127\n\t"
"v_add_u32 v126, 4, v126\n\t"
"s_nop 0\n\t"
"v_cndmask_b32 v134, 0, v134, vcc\n\t"
"v_cmp_eq_u32 vcc, 1, v127\n\t"
"s_nop 1\n\t"
"v_cndmask_b32 v134, v134, v135, vcc\n\t"
"v_cmp_eq_u32 vcc, 7, v139\n\t"
"s_nop 1\n\t"
"v_cndmask_b32 v135, 0, v135, vcc\n\t"
"v_cmp_gt_u32 vcc, 48, v138\n\t"
"s_nop 1\n\t"
"v_cndmask_b32 v134, v135, v134, vcc\n\t"
"v_mov_b32 v135, 0\n\t"
"s_add_u32 m0, s74, 6144\n\t"
"s_add_u32 s27, s72, 442368\n\t"
"buffer_load_dwordx4 v104, s[52:55], s27 offen lds\n\t"
"v_cndmask_b32 v136, v126, v139, vcc\n\t"
"v_and_b32 v137, 1, v136\n\t"
"v_lshlrev_b32 v137, 4, v137\n\t"
"v_lshrrev_b32 v136, 1, v136\n\t"
"v_lshlrev_b64 v[134:135], v137, v[134:135]\n\t"
"v_cmp_eq_u32 vcc, 0, v136\n\t"
"s_nop 1\n\t"
"v_cndmask_b32 v140, 0, v134, vcc\n\t"
"v_accvgpr_write_b32 a224, v140\n\t"
"v_cndmask_b32 v141, 0, v135, vcc\n\t"
"v_cmp_eq_u32 vcc, 1, v136\n\t"
"v_and_b32 v140, 0xff7fff7f, v140\n\t"
"v_xor_b32 v148, 0x80008000, v140\n\t"
"s_add_u32 m0, s74, 7168\n\t"
"s_add_u32 s27, s72, 442368\n\t"
"buffer_load_dwordx4 v105, s[52:55], s27 offen lds\n\t"
"v_cndmask_b32 v141, v141, v134, vcc\n\t"
"v_cndmask_b32 v142, 0, v135, vcc\n\t"
"v_cmp_eq_u32 vcc, 2, v136\n\t"
"v_accvgpr_write_b32 a225, v141\n\t"
"v_accvgpr_write_b32 a216, v140\n\t"
"v_cndmask_b32 v142, v142, v134, vcc\n\t"
"v_cndmask_b32 v143, 0, v135, vcc\n\t"
"v_cmp_eq_u32 vcc, 3, v136\n\t"
"v_accvgpr_write_b32 a226, v142\n\t"
"v_and_b32 v141, 0xff7fff7f, v141\n\t"
"v_cndmask_b32 v143, v143, v134, vcc\n\t"
"v_accvgpr_write_b32 a227, v143\n\t"
"v_xor_b32 v149, 0x80008000, v141\n\t"
"s_add_u32 m0, s74, 8192\n\t"
"s_add_u32 s27, s72, 442368\n\t"
"buffer_load_dwordx4 v106, s[52:55], s27 offen lds\n\t"
"v_accvgpr_write_b32 a217, v141\n\t"
"v_and_b32 v142, 0xff7fff7f, v142\n\t"
"v_xor_b32 v150, 0x80008000, v142\n\t"
"v_accvgpr_write_b32 a218, v142\n\t"
"v_and_b32 v143, 0xff7fff7f, v143\n\t"
"v_xor_b32 v151, 0x80008000, v143\n\t"
"v_accvgpr_write_b32 a219, v143\n\t"
"s_mov_b32 s68, 3000\n\t"
"v_and_b32 v107, 51, v204\n\t"
"v_mul_u32_u24 v107, 24, v107\n\t"
"v_and_b32 v47, 8, v204\n\t"
"v_mad_u32_u24 v107, v47, 12, v107\n\t"
"s_add_u32 m0, s74, 9216\n\t"
"s_add_u32 s27, s72, 454656\n\t"
"buffer_load_dwordx4 v104, s[52:55], s27 offen lds\n\t"
"v_and_b32 v47, 4, v204\n\t"
"v_mul_u32_u24 v47, 0x180, v47\n\t"
"v_add_u32 v107, v47, v107\n\t"
"v_add_u32 v47, s75, v107\n\t"
"v_add_u32 v107, s74, v107\n\t"
"v_lshrrev_b32 v112, 4, v204\n\t"
"v_bfe_u32 v113, v204, 2, 2\n\t"
"v_cmp_eq_u32 vcc, v112, v113\n\t"
"v_lshlrev_b32 v112, 4, v204\n\t"
"v_mov_b32 v113, 0x3e80\n\t"
"v_lshlrev_b32 v113, v112, v113\n\t"
"v_cndmask_b32 v113, 0, v113, vcc\n\t"
"s_add_u32 m0, s74, 10240\n\t"
"s_add_u32 s27, s72, 454656\n\t"
"buffer_load_dwordx4 v105, s[52:55], s27 offen lds\n\t"
"v_and_b32 v112, 2, v204\n\t"
"v_cmp_eq_u32 vcc, 0, v112\n\t"
"v_mov_b32 v108, 0\n\t"
"v_mov_b32 v109, 0\n\t"
"v_cndmask_b32 v110, 0, v113, vcc\n\t"
"v_cndmask_b32 v111, v113, 0, vcc\n\t"
"v_xor_b32 v114, 0x80008000, v110\n\t"
"v_xor_b32 v115, 0x80008000, v111\n\t"
"v_mov_b32 v112, 0\n\t"
"v_mov_b32 v113, 0\n\t"
"v_mov_b32 v116, 0\n\t"
"v_mov_b32 v117, 0\n\t"
"s_add_u32 m0, s74, 11264\n\t"
"s_add_u32 s27, s72, 454656\n\t"
"buffer_load_dwordx4 v106, s[52:55], s27 offen lds\n\t"
"s_waitcnt vmcnt(16)\n\t"
"s_waitcnt vmcnt(12)\n\t"
"v_readfirstlane_b32 s71, v187\n\t"
"s_barrier\n\t"
"s_add_u32 s71, s71, 1\n\t"
"v_mov_b32 v215, s71\n\t"
"s_mov_b64 exec, 1\n\t"
"buffer_store_dword v215, off, s[8:11], s70 sc1\n\t"
"s_mov_b64 exec, -1\n\t"
"v_add_u32 v248, 101632, v226\n\t"
"v_xor_b32 v249, 32, v248\n\t"
"v_xor_b32 v250, 64, v248\n\t"
"v_xor_b32 v251, 96, v248\n\t"
"ds_read_b64_tr_b16 a[178:179], v248 offset:128\n\t"
"ds_read_b64_tr_b16 a[186:187], v249 offset:128\n\t"
"s_add_u32 m0, s75, 0\n\t"
"s_add_u32 s27, s72, 221184\n\t"
"buffer_load_dwordx4 v104, s[52:55], s27 offen lds\n\t"
"ds_read_b64_tr_b16 a[194:195], v250 offset:128\n\t"
"ds_read_b64_tr_b16 a[202:203], v251 offset:128\n\t"
"ds_read_b64_tr_b16 a[176:177], v248 offset:0\n\t"
"s_add_u32 m0, s75, 1024\n\t"
"s_add_u32 s27, s72, 221184\n\t"
"buffer_load_dwordx4 v105, s[52:55], s27 offen lds\n\t"
"ds_read_b64_tr_b16 a[184:185], v249 offset:0\n\t"
"ds_read_b64_tr_b16 a[180:181], v249 offset:256\n\t"
"ds_read_b64_tr_b16 a[188:189], v248 offset:256\n\t"
"s_add_u32 m0, s75, 2048\n\t"
"s_add_u32 s27, s72, 221184\n\t"
"buffer_load_dwordx4 v106, s[52:55], s27 offen lds\n\t"
"ds_read_b64_tr_b16 a[192:193], v250 offset:0\n\t"
"ds_read_b64_tr_b16 a[200:201], v251 offset:0\n\t"
"ds_read_b64_tr_b16 a[196:197], v251 offset:256\n\t"
"s_add_u32 m0, s75, 3072\n\t"
"s_add_u32 s27, s72, 233472\n\t"
"buffer_load_dwordx4 v104, s[52:55], s27 offen lds\n\t"
"ds_read_b64_tr_b16 a[204:205], v250 offset:256\n\t"
"s_waitcnt lgkmcnt(11)\n\t"
"v_mfma_f32_4x4x4_16b_bf16 v[8:11], a[220:221], a[178:179], 0\n\t"
"s_waitcnt lgkmcnt(10)\n\t"
"v_mfma_f32_4x4x4_16b_bf16 v[12:15], a[220:221], a[186:187], 0\n\t"
"s_waitcnt lgkmcnt(9)\n\t"
"v_mfma_f32_4x4x4_16b_bf16 v[24:27], a[220:221], a[194:195], 0\n\t"
"s_waitcnt lgkmcnt(8)\n\t"
"v_mfma_f32_4x4x4_16b_bf16 v[28:31], a[220:221], a[202:203], 0\n\t"
"s_waitcnt lgkmcnt(7)\n\t"
"v_mfma_f32_4x4x4_16b_bf16 v[0:3], a[222:223], a[176:177], v[8:11]\n\t"
"s_waitcnt lgkmcnt(6)\n\t"
"v_mfma_f32_4x4x4_16b_bf16 v[4:7], a[222:223], a[184:185], v[12:15]\n\t"
"s_add_u32 m0, s75, 4096\n\t"
"s_add_u32 s27, s72, 233472\n\t"
"buffer_load_dwordx4 v105, s[52:55], s27 offen lds\n\t"
"s_add_u32 m0, s75, 5120\n\t"
"s_add_u32 s27, s72, 233472\n\t"
"buffer_load_dwordx4 v106, s[52:55], s27 offen lds\n\t"
"s_waitcnt lgkmcnt(5)\n\t"
"v_mfma_f32_4x4x4_16b_bf16 v[8:11], a[222:223], a[180:181], v[8:11]\n\t"
"s_waitcnt lgkmcnt(4)\n\t"
"v_mfma_f32_4x4x4_16b_bf16 v[12:15], a[222:223], a[188:189], v[12:15]\n\t"
"s_waitcnt lgkmcnt(3)\n\t"
"v_mfma_f32_4x4x4_16b_bf16 v[16:19], a[222:223], a[192:193], v[24:27]\n\t"
"s_waitcnt lgkmcnt(2)\n\t"
"v_mfma_f32_4x4x4_16b_bf16 v[20:23], a[222:223], a[200:201], v[28:31]\n\t"
"s_waitcnt lgkmcnt(1)\n\t"
"v_mfma_f32_4x4x4_16b_bf16 v[24:27], a[222:223], a[196:197], v[24:27]\n\t"
"s_waitcnt lgkmcnt(0)\n\t"
"v_mfma_f32_4x4x4_16b_bf16 v[28:31], a[222:223], a[204:205], v[28:31]\n\t"
"s_add_u32 m0, s26, 124544\n\t"
"s_nop 0\n\t"
"buffer_load_dwordx4 v241, s[12:15], s22 offen offset:128 nt lds\n\t"
"buffer_load_dwordx4 v242, s[12:15], s22 offen offset:1152 nt lds\n\t"
"v_cvt_pk_bf16_f32 v0, -v0, -v4\n\t"
"v_cvt_pk_bf16_f32 v1, -v1, -v5\n\t"
"v_cvt_pk_bf16_f32 v2, -v2, -v6\n\t"
"v_cvt_pk_bf16_f32 v3, -v3, -v7\n\t"
"s_lshl_b32 m0, s73, 8\n\t"
"s_nop 0\n\t"
".p2align 3\n\t"
"ds_write_addtid_b32 v0 offset:0\n\t"
"ds_write_addtid_b32 v1 offset:4128\n\t"
"ds_write_addtid_b32 v2 offset:1024\n\t"
"ds_write_addtid_b32 v3 offset:5152\n\t"
"v_cvt_pk_bf16_f32 v8, v8, v12\n\t"
"v_cvt_pk_bf16_f32 v9, v9, v13\n\t"
"v_cvt_pk_bf16_f32 v10, v10, v14\n\t"
"v_cvt_pk_bf16_f32 v11, v11, v15\n\t"
"ds_write_addtid_b32 v8 offset:8320\n\t"
"ds_write_addtid_b32 v9 offset:12448\n\t"
"ds_write_addtid_b32 v10 offset:9344\n\t"
"ds_write_addtid_b32 v11 offset:13472\n\t"
"v_cvt_pk_bf16_f32 v16, -v16, -v20\n\t"
"v_cvt_pk_bf16_f32 v17, -v17, -v21\n\t"
"v_cvt_pk_bf16_f32 v18, -v18, -v22\n\t"
"v_cvt_pk_bf16_f32 v19, -v19, -v23\n\t"
"ds_write_addtid_b32 v16 offset:2048\n\t"
"ds_write_addtid_b32 v17 offset:6176\n\t"
"ds_write_addtid_b32 v18 offset:3072\n\t"
"ds_write_addtid_b32 v19 offset:7200\n\t"
"v_cvt_pk_bf16_f32 v24, v24, v28\n\t"
"v_cvt_pk_bf16_f32 v25, v25, v29\n\t"
"v_cvt_pk_bf16_f32 v26, v26, v30\n\t"
"v_cvt_pk_bf16_f32 v27, v27, v31\n\t"
"ds_write_addtid_b32 v24 offset:10368\n\t"
"ds_write_addtid_b32 v25 offset:14496\n\t"
"ds_write_addtid_b32 v26 offset:11392\n\t"
"ds_write_addtid_b32 v27 offset:15520\n\t"
"v_add_u32 v248, 101632, v227\n\t"
"v_xor_b32 v249, 32, v248\n\t"
"v_xor_b32 v250, 64, v248\n\t"
"v_xor_b32 v251, 96, v248\n\t"
"ds_read_b64_tr_b16 a[176:177], v248 offset:0\n\t"
"ds_read_b64_tr_b16 a[178:179], v248 offset:512\n\t"
"ds_read_b64_tr_b16 a[180:181], v249 offset:0\n\t"
"ds_read_b64_tr_b16 a[182:183], v249 offset:512\n\t"
"s_add_u32 m0, s26, 124544\n\t"
"s_nop 0\n\t"
".p2align 3\n\t"
"buffer_load_dwordx4 v243, s[12:15], s22 offen offset:2176 nt lds\n\t"
"ds_read_b64_tr_b16 a[184:185], v250 offset:0\n\t"
"ds_read_b64_tr_b16 a[186:187], v250 offset:512\n\t"
"ds_read_b64_tr_b16 a[188:189], v251 offset:0\n\t"
"ds_read_b64_tr_b16 a[190:191], v251 offset:512\n\t"
"buffer_load_dwordx4 v244, s[12:15], s22 offen offset:3200 nt lds\n\t"
"ds_read_b64_tr_b16 a[192:193], v248 offset:1152\n\t"
"ds_read_b64_tr_b16 a[194:195], v248 offset:1664\n\t"
"ds_read_b64_tr_b16 a[196:197], v249 offset:1152\n\t"
"ds_read_b64_tr_b16 a[198:199], v249 offset:1664\n\t"
"s_mov_b64 exec, s[36:37]\n\t"
"buffer_load_dwordx4 v245, s[12:15], s22 offen offset:3712 nt lds\n\t"
"s_mov_b64 exec, -1\n\t"
"ds_read_b64_tr_b16 a[200:201], v250 offset:1152\n\t"
"ds_read_b64_tr_b16 a[202:203], v250 offset:1664\n\t"
"ds_read_b64_tr_b16 a[204:205], v251 offset:1152\n\t"
"ds_read_b64_tr_b16 a[206:207], v251 offset:1664\n\t"
"s_add_u32 m0, s43, 122240\n\t"
"s_mov_b64 exec, s[44:45]\n\t"
"buffer_load_dwordx4 v246, s[12:15], 0 offen offset:128 nt lds\n\t"
"s_mov_b64 exec, -1\n\t"
"s_waitcnt vmcnt(19)\n\t"
".p2align 3\n\t"
"ds_read_b64_tr_b16 a[48:49], v107 offset:0\n\t"
"ds_read_b64_tr_b16 a[50:51], v107 offset:192\n\t"
"ds_read_b64_tr_b16 a[52:53], v107 offset:3072\n\t"
"ds_read_b64_tr_b16 a[54:55], v107 offset:3264\n\t"
"ds_read_b64_tr_b16 a[56:57], v107 offset:8\n\t"
"ds_read_b64_tr_b16 a[58:59], v107 offset:200\n\t"
"ds_read_b64_tr_b16 a[60:61], v107 offset:3080\n\t"
"ds_read_b64_tr_b16 a[62:63], v107 offset:3272\n\t"
"ds_read_b64_tr_b16 a[64:65], v107 offset:16\n\t"
"ds_read_b64_tr_b16 a[66:67], v107 offset:208\n\t"
"ds_read_b64_tr_b16 a[68:69], v107 offset:3088\n\t"
"ds_read_b64_tr_b16 a[70:71], v107 offset:3280\n\t"
"s_waitcnt vmcnt(13)\n\t"
".p2align 3\n\t"
"ds_read_b64_tr_b16 a[72:73], v107 offset:6144\n\t"
"ds_read_b64_tr_b16 a[74:75], v107 offset:6336\n\t"
"ds_read_b64_tr_b16 a[76:77], v107 offset:9216\n\t"
"ds_read_b64_tr_b16 a[78:79], v107 offset:9408\n\t"
"ds_read_b64_tr_b16 a[80:81], v107 offset:6152\n\t"
"ds_read_b64_tr_b16 a[82:83], v107 offset:6344\n\t"
"ds_read_b64_tr_b16 a[84:85], v107 offset:9224\n\t"
"ds_read_b64_tr_b16 a[86:87], v107 offset:9416\n\t"
"ds_read_b64_tr_b16 a[88:89], v107 offset:6160\n\t"
"ds_read_b64_tr_b16 a[90:91], v107 offset:6352\n\t"
"ds_read_b64_tr_b16 a[92:93], v107 offset:9232\n\t"
"ds_read_b64_tr_b16 a[94:95], v107 offset:9424\n\t"
"s_waitcnt lgkmcnt(15)\n\t"
"v_mfma_f32_16x16x32_bf16 v[0:3], v[110:113], a[48:51], 0\n\t"
"s_waitcnt lgkmcnt(10)\n\t"
"v_mfma_f32_16x16x32_bf16 v[0:3], v[110:113], a[72:75], v[0:3]\n\t"
"v_mfma_f32_16x16x32_bf16 v[8:11], v[108:111], a[48:51], 0\n\t"
"v_mfma_f32_16x16x32_bf16 v[8:11], v[108:111], a[72:75], v[8:11]\n\t"
"s_add_u32 m0, s26, 145152\n\t"
"s_nop 0\n\t"
"buffer_load_dwordx4 v241, s[12:15], s22 offen offset:256 nt lds\n\t"
"v_mfma_f32_16x16x32_bf16 v[16:19], v[110:113], a[52:55], 0\n\t"
"s_waitcnt lgkmcnt(8)\n\t"
"v_mfma_f32_16x16x32_bf16 v[16:19], v[110:113], a[76:79], v[16:19]\n\t"
"v_mfma_f32_16x16x32_bf16 v[24:27], v[108:111], a[52:55], 0\n\t"
"v_mfma_f32_16x16x32_bf16 v[24:27], v[108:111], a[76:79], v[24:27]\n\t"
"buffer_load_dwordx4 v242, s[12:15], s22 offen offset:1280 nt lds\n\t"
"s_waitcnt vmcnt(8)\n\t"
".p2align 3\n\t"
"ds_read_b64_tr_b16 a[0:1], v47 offset:0\n\t"
"ds_read_b64_tr_b16 a[2:3], v47 offset:192\n\t"
"ds_read_b64_tr_b16 a[4:5], v47 offset:3072\n\t"
"ds_read_b64_tr_b16 a[6:7], v47 offset:3264\n\t"
"ds_read_b64_tr_b16 a[8:9], v47 offset:8\n\t"
"ds_read_b64_tr_b16 a[10:11], v47 offset:200\n\t"
"ds_read_b64_tr_b16 a[12:13], v47 offset:3080\n\t"
"ds_read_b64_tr_b16 a[14:15], v47 offset:3272\n\t"
"ds_read_b64_tr_b16 a[16:17], v47 offset:16\n\t"
"ds_read_b64_tr_b16 a[18:19], v47 offset:208\n\t"
"ds_read_b64_tr_b16 a[20:21], v47 offset:3088\n\t"
"ds_read_b64_tr_b16 a[22:23], v47 offset:3280\n\t"
"v_mfma_f32_16x16x32_bf16 v[32:35], v[110:113], a[56:59], 0\n\t"
"s_waitcnt lgkmcnt(15)\n\t"
"v_mfma_f32_16x16x32_bf16 v[32:35], v[110:113], a[80:83], v[32:35]\n\t"
"v_mfma_f32_16x16x32_bf16 v[96:99], v[108:111], a[56:59], 0\n\t"
"v_mfma_f32_16x16x32_bf16 v[96:99], v[108:111], a[80:83], v[96:99]\n\t"
"buffer_load_dwordx4 v243, s[12:15], s22 offen offset:2304 nt lds\n\t"
"v_mfma_f32_16x16x32_bf16 v[120:123], v[110:113], a[60:63], 0\n\t"
"v_mfma_f32_16x16x32_bf16 v[120:123], v[110:113], a[84:87], v[120:123]\n\t"
"v_mfma_f32_16x16x32_bf16 v[128:131], v[108:111], a[60:63], 0\n\t"
"v_mfma_f32_16x16x32_bf16 v[128:131], v[108:111], a[84:87], v[128:131]\n\t"
"buffer_load_dwordx4 v244, s[12:15], s22 offen offset:3328 nt lds\n\t"
"v_mfma_f32_16x16x32_bf16 v[136:139], v[110:113], a[64:67], 0\n\t"
"s_waitcnt lgkmcnt(14)\n\t"
"v_mfma_f32_16x16x32_bf16 v[136:139], v[110:113], a[88:91], v[136:139]\n\t"
"v_mfma_f32_16x16x32_bf16 v[152:155], v[108:111], a[64:67], 0\n\t"
"v_mfma_f32_16x16x32_bf16 v[152:155], v[108:111], a[88:91], v[152:155]\n\t"
"s_mov_b64 exec, s[36:37]\n\t"
"buffer_load_dwordx4 v245, s[12:15], s22 offen offset:3840 nt lds\n\t"
"s_mov_b64 exec, -1\n\t"
"v_mfma_f32_16x16x32_bf16 v[160:163], v[110:113], a[68:71], 0\n\t"
"s_waitcnt lgkmcnt(12)\n\t"
"v_mfma_f32_16x16x32_bf16 v[160:163], v[110:113], a[92:95], v[160:163]\n\t"
"v_mfma_f32_16x16x32_bf16 v[168:171], v[108:111], a[68:71], 0\n\t"
"v_mfma_f32_16x16x32_bf16 v[168:171], v[108:111], a[92:95], v[168:171]\n\t"
"s_add_u32 m0, s43, 142848\n\t"
"s_mov_b64 exec, s[44:45]\n\t"
"buffer_load_dwordx4 v246, s[12:15], 0 offen offset:256 nt lds\n\t"
"s_mov_b64 exec, -1\n\t"
"s_waitcnt lgkmcnt(10)\n\t"
"v_mfma_f32_16x16x32_bf16 v[4:7], v[110:113], a[0:3], v[0:3]\n\t"
"v_mfma_f32_16x16x32_bf16 v[0:3], v[114:117], a[0:3], v[0:3]\n\t"
"v_mfma_f32_16x16x32_bf16 v[12:15], v[108:111], a[0:3], v[8:11]\n\t"
"v_mfma_f32_16x16x32_bf16 v[8:11], v[112:115], a[0:3], v[8:11]\n\t"
"s_waitcnt lgkmcnt(8)\n\t"
"v_mfma_f32_16x16x32_bf16 v[20:23], v[110:113], a[4:7], v[16:19]\n\t"
"v_mfma_f32_16x16x32_bf16 v[16:19], v[114:117], a[4:7], v[16:19]\n\t"
"v_mfma_f32_16x16x32_bf16 v[28:31], v[108:111], a[4:7], v[24:27]\n\t"
"s_nop 0\n\t"
"v_cvt_pk_bf16_f32 v48, v4, v5\n\t"
"v_cvt_pk_bf16_f32 v49, v6, v7\n\t"
"v_mfma_f32_16x16x32_bf16 v[24:27], v[112:115], a[4:7], v[24:27]\n\t"
"v_cvt_pk_bf16_f32 v72, v0, v1\n\t"
"v_cvt_pk_bf16_f32 v73, v2, v3\n\t"
"s_waitcnt lgkmcnt(6)\n\t"
"v_mfma_f32_16x16x32_bf16 v[36:39], v[110:113], a[8:11], v[32:35]\n\t"
"v_cvt_pk_bf16_f32 v50, v12, v13\n\t"
"v_cvt_pk_bf16_f32 v51, v14, v15\n\t"
"v_mfma_f32_16x16x32_bf16 v[32:35], v[114:117], a[8:11], v[32:35]\n\t"
"v_cvt_pk_bf16_f32 v74, v8, v9\n\t"
"v_cvt_pk_bf16_f32 v75, v10, v11\n\t"
"v_mfma_f32_16x16x32_bf16 v[100:103], v[108:111], a[8:11], v[96:99]\n\t"
"v_cvt_pk_bf16_f32 v52, v20, v21\n\t"
"v_cvt_pk_bf16_f32 v53, v22, v23\n\t"
"v_mfma_f32_16x16x32_bf16 v[96:99], v[112:115], a[8:11], v[96:99]\n\t"
"v_cvt_pk_bf16_f32 v76, v16, v17\n\t"
"v_cvt_pk_bf16_f32 v77, v18, v19\n\t"
"s_waitcnt lgkmcnt(4)\n\t"
"v_mfma_f32_16x16x32_bf16 v[124:127], v[110:113], a[12:15], v[120:123]\n\t"
"v_cvt_pk_bf16_f32 v54, v28, v29\n\t"
"v_cvt_pk_bf16_f32 v55, v30, v31\n\t"
"v_mfma_f32_16x16x32_bf16 v[120:123], v[114:117], a[12:15], v[120:123]\n\t"
"v_cvt_pk_bf16_f32 v78, v24, v25\n\t"
"v_cvt_pk_bf16_f32 v79, v26, v27\n\t"
"v_mfma_f32_16x16x32_bf16 v[132:135], v[108:111], a[12:15], v[128:131]\n\t"
"v_cvt_pk_bf16_f32 v56, v36, v37\n\t"
"v_cvt_pk_bf16_f32 v57, v38, v39\n\t"
"v_mfma_f32_16x16x32_bf16 v[128:131], v[112:115], a[12:15], v[128:131]\n\t"
"v_cvt_pk_bf16_f32 v80, v32, v33\n\t"
"v_cvt_pk_bf16_f32 v81, v34, v35\n\t"
"s_waitcnt lgkmcnt(2)\n\t"
"v_mfma_f32_16x16x32_bf16 v[140:143], v[110:113], a[16:19], v[136:139]\n\t"
"v_cvt_pk_bf16_f32 v58, v100, v101\n\t"
"v_cvt_pk_bf16_f32 v59, v102, v103\n\t"
"v_mfma_f32_16x16x32_bf16 v[136:139], v[114:117], a[16:19], v[136:139]\n\t"
"v_cvt_pk_bf16_f32 v82, v96, v97\n\t"
"v_cvt_pk_bf16_f32 v83, v98, v99\n\t"
"v_mfma_f32_16x16x32_bf16 v[156:159], v[108:111], a[16:19], v[152:155]\n\t"
"v_cvt_pk_bf16_f32 v60, v124, v125\n\t"
"v_cvt_pk_bf16_f32 v61, v126, v127\n\t"
"v_mfma_f32_16x16x32_bf16 v[152:155], v[112:115], a[16:19], v[152:155]\n\t"
"v_cvt_pk_bf16_f32 v84, v120, v121\n\t"
"v_cvt_pk_bf16_f32 v85, v122, v123\n\t"
"s_waitcnt lgkmcnt(0)\n\t"
"v_mfma_f32_16x16x32_bf16 v[164:167], v[110:113], a[20:23], v[160:163]\n\t"
"v_cvt_pk_bf16_f32 v62, v132, v133\n\t"
"v_cvt_pk_bf16_f32 v63, v134, v135\n\t"
"v_mfma_f32_16x16x32_bf16 v[160:163], v[114:117], a[20:23], v[160:163]\n\t"
"v_cvt_pk_bf16_f32 v86, v128, v129\n\t"
"v_cvt_pk_bf16_f32 v87, v130, v131\n\t"
"v_mfma_f32_16x16x32_bf16 v[172:175], v[108:111], a[20:23], v[168:171]\n\t"
"v_cvt_pk_bf16_f32 v64, v140, v141\n\t"
"v_cvt_pk_bf16_f32 v65, v142, v143\n\t"
"v_mfma_f32_16x16x32_bf16 v[168:171], v[112:115], a[20:23], v[168:171]\n\t"
".p2align 3\n\t"
"v_cvt_pk_bf16_f32 v88, v136, v137\n\t"
"v_cvt_pk_bf16_f32 v89, v138, v139\n\t"
"v_cvt_pk_bf16_f32 v66, v156, v157\n\t"
"v_cvt_pk_bf16_f32 v67, v158, v159\n\t"
"v_cvt_pk_bf16_f32 v90, v152, v153\n\t"
"v_cvt_pk_bf16_f32 v91, v154, v155\n\t"
"v_cvt_pk_bf16_f32 v68, v164, v165\n\t"
"v_cvt_pk_bf16_f32 v69, v166, v167\n\t"
"v_cvt_pk_bf16_f32 v92, v160, v161\n\t"
"v_cvt_pk_bf16_f32 v93, v162, v163\n\t"
"v_cvt_pk_bf16_f32 v70, v172, v173\n\t"
"v_cvt_pk_bf16_f32 v71, v174, v175\n\t"
"v_cvt_pk_bf16_f32 v94, v168, v169\n\t"
"v_cvt_pk_bf16_f32 v95, v170, v171\n\t"
"s_barrier\n\t"
"v_mfma_f32_16x16x32_bf16 v[192:195], a[216:219], a[176:179], 0\n\t"
"s_add_u32 m0, s43, 53888\n\t"
"ds_read_b128 a[144:147], v224 offset:0\n\t"
"ds_read_b128 a[148:151], v224 offset:256\n\t"
"ds_read_b128 a[152:155], v224 offset:512\n\t"
"v_mfma_f32_16x16x32_bf16 v[196:199], a[216:219], a[180:183], 0\n\t"
"s_mov_b64 exec, s[44:45]\n\t"
"buffer_load_dwordx4 v247, s[12:15], 0 offen offset:128 nt lds\n\t"
"s_mov_b64 exec, -1\n\t"
"v_mfma_f32_16x16x32_bf16 v[200:203], a[216:219], a[184:187], 0\n\t"
"s_add_u32 m0, s26, 56192\n\t"
"ds_read_b128 a[160:163], v224 offset:2048\n\t"
"ds_read_b128 a[164:167], v224 offset:2304\n\t"
"s_mul_i32 s92, s73, 294912\n\t"
"v_mfma_f32_16x16x32_bf16 v[204:207], a[216:219], a[188:191], 0\n\t"
"s_add_u32 s92, s92, s8\n\t"
"s_addc_u32 s93, s9, 0x100000\n\t"
"s_mov_b32 s94, s10\n\t"
"v_mfma_f32_16x16x32_bf16 v[208:211], v[148:151], a[192:195], 0\n\t"
"s_mov_b32 s95, 0x800000\n\t"
"ds_read_b128 a[168:171], v224 offset:2560\n\t"
"ds_read_b128 a[156:159], v224 offset:768\n\t"
"v_mfma_f32_16x16x32_bf16 v[212:215], v[148:151], a[196:199], 0\n\t"
"s_waitcnt lgkmcnt(3)\n\t"
"ds_read_b128 a[172:175], v224 offset:2816\n\t"
"s_add_u32 s96, s92, 4096\n\t"
"s_addc_u32 s97, s93, 0\n\t"
"v_mfma_f32_16x16x32_bf16 v[96:99], v[48:51], a[144:147], 0\n\t"
"buffer_load_dwordx4 v236, s[12:15], s22 offen offset:128 nt lds\n\t"
"v_mfma_f32_16x16x32_bf16 v[96:99], v[52:55], a[160:163], v[96:99]\n\t"
"s_lshl_b32 m0, s73, 8\n\t"
"v_cvt_pk_bf16_f32 v192, v192, v196\n\t"
"v_cvt_pk_bf16_f32 v193, v193, v197\n\t"
"v_cvt_pk_bf16_f32 v194, v194, v198\n\t"
"v_cvt_pk_bf16_f32 v195, v195, v199\n\t"
"s_mov_b64 s[98:99], s[94:95]\n\t"
"v_mfma_f32_16x16x32_bf16 v[112:115], v[56:59], a[144:147], 0\n\t"
"s_mov_b32 s16, s80\n\t"
"ds_write_addtid_b32 v193 offset:24864\n\t"
"ds_write_addtid_b32 v195 offset:28992\n\t"
"v_mfma_f32_16x16x32_bf16 v[112:115], v[60:63], a[160:163], v[112:115]\n\t"
"s_and_b32 s17, s81, 0xffff\n\t"
"ds_write_addtid_b32 v194 offset:20672\n\t"
"v_cvt_pk_bf16_f32 v200, v200, v204\n\t"
"v_mfma_f32_16x16x32_bf16 v[128:131], v[64:67], a[144:147], 0\n\t"
"s_mov_b32 s18, 100663296\n\t"
"ds_read_b128 a[144:147], v224 offset:1024\n\t"
"v_cvt_pk_bf16_f32 v202, v202, v206\n\t"
"v_mfma_f32_16x16x32_bf16 v[128:131], v[68:71], a[160:163], v[128:131]\n\t"
"s_waitcnt lgkmcnt(7)\n\t"
"v_cvt_pk_bf16_f32 v201, v201, v205\n\t"
"v_cvt_pk_bf16_f32 v211, v211, v215\n\t"
"v_mfma_f32_16x16x32_bf16 v[132:135], v[64:67], a[148:151], 0\n\t"
"s_mov_b32 s19, 0x20000\n\t"
"ds_read_b128 a[160:163], v224 offset:3072\n\t"
"ds_write_addtid_b32 v200 offset:18592\n\t"
"v_mfma_f32_16x16x32_bf16 v[132:135], v[68:71], a[164:167], v[132:135]\n\t"
"s_mov_b32 s21, 4\n\t"
"ds_write_addtid_b32 v192 offset:16544\n\t"
"ds_write_addtid_b32 v202 offset:22720\n\t"
"v_mfma_f32_16x16x32_bf16 v[116:119], v[56:59], a[148:151], 0\n\t"
"s_add_u32 m0, s26, 56192\n\t"
"v_cvt_pk_bf16_f32 v203, v203, v207\n\t"
"v_cvt_pk_bf16_f32 v210, v210, v214\n\t"
"s_mov_b32 s38, -2.0\n\t"
"v_mfma_f32_16x16x32_bf16 v[116:119], v[60:63], a[164:167], v[116:119]\n\t"
"buffer_load_dword v190, v189, s[8:11], 0 offen sc1\n\t"
"s_waitcnt vmcnt(9)\n\t"
"s_barrier\n\t"
"v_mfma_f32_16x16x32_bf16 v[216:219], v[148:151], a[200:203], 0\n\t"
"s_mov_b32 s39, -2.0\n\t"
"ds_read_b64_tr_b16 a[176:177], v248 offset:20736\n\t"
"ds_read_b64_tr_b16 a[178:179], v248 offset:21248\n\t"
"ds_read_b64_tr_b16 a[180:181], v249 offset:20736\n\t"
"v_mbcnt_lo_u32_b32 v176, -1, 0\n\t"
"v_mbcnt_hi_u32_b32 v176, -1, v176\n\t"
"v_cvt_pk_bf16_f32 v208, v208, v212\n\t"
"v_mfma_f32_16x16x32_bf16 v[100:103], v[48:51], a[148:151], 0\n\t"
"ds_read_b128 a[148:151], v224 offset:1280\n\t"
"v_bfe_u32 v156, v176, 2, 2\n\t"
"v_mfma_f32_16x16x32_bf16 v[100:103], v[52:55], a[164:167], v[100:103]\n\t"
"s_waitcnt lgkmcnt(14)\n\t"
"v_and_b32 v157, 3, v176\n\t"
"v_lshlrev_b32 v157, 3, v157\n\t"
"v_and_b32 v158, 48, v176\n\t"
"v_mfma_f32_16x16x32_bf16 v[104:107], v[48:51], a[152:155], 0\n\t"
"ds_read_b64_tr_b16 a[182:183], v249 offset:21248\n\t"
"v_lshl_add_u32 v158, v157, 3, v158\n\t"
"v_mfma_f32_16x16x32_bf16 v[104:107], v[52:55], a[168:171], v[104:107]\n\t"
"ds_read_b128 a[164:167], v224 offset:3328\n\t"
"v_and_b32 v159, 8, v176\n\t"
"v_mfma_f32_16x16x32_bf16 v[220:223], v[148:151], a[204:207], 0\n\t"
"ds_read_b64_tr_b16 a[184:185], v250 offset:20736\n\t"
"buffer_load_dwordx4 v237, s[12:15], s22 offen offset:1152 nt lds\n\t"
"v_mul_u32_u24 v166, 516, v159\n\t"
"v_and_b32 v159, 4, v176\n\t"
"v_cvt_pk_bf16_f32 v209, v209, v213\n\t"
"v_mfma_f32_16x16x32_bf16 v[120:123], v[56:59], a[152:155], 0\n\t"
"s_lshl_b32 m0, s73, 8\n\t"
"ds_read_b64_tr_b16 a[186:187], v250 offset:21248\n\t"
"v_mul_u32_u24 v159, 2080, v159\n\t"
"v_mfma_f32_16x16x32_bf16 v[120:123], v[60:63], a[168:171], v[120:123]\n\t"
"ds_read_b64_tr_b16 a[188:189], v251 offset:20736\n\t"
"v_add3_u32 v166, v166, v158, v159\n\t"
"v_accvgpr_write_b32 a212, v224\n\t"
"v_accvgpr_write_b32 a213, v166\n\t"
"v_mfma_f32_16x16x32_bf16 v[136:139], v[64:67], a[152:155], 0\n\t"
"ds_read_b128 a[152:155], v224 offset:1536\n\t"
"v_lshlrev_b32 v158, 15, v176\n\t"
"v_mfma_f32_16x16x32_bf16 v[136:139], v[68:71], a[168:171], v[136:139]\n\t"
"s_waitcnt lgkmcnt(15)\n\t"
"ds_read_b128 a[168:171], v224 offset:3584\n\t"
"v_and_b32 v158, 0x8000, v158\n\t"
"v_mfma_f32_16x16x32_bf16 v[140:143], v[64:67], a[156:159], 0\n\t"
"ds_read_b64_tr_b16 a[190:191], v251 offset:21248\n\t"
"v_or_b32 v167, 0x3f803f80, v158\n\t"
"v_mfma_f32_16x16x32_bf16 v[140:143], v[68:71], a[172:175], v[140:143]\n\t"
"ds_read_b64_tr_b16 a[192:193], v248 offset:21888\n\t"
"v_and_b32 v158, 2, v176\n\t"
"v_mul_u32_u24 v158, 0x4000, v158\n\t"
"v_mfma_f32_16x16x32_bf16 v[124:127], v[56:59], a[156:159], 0\n\t"
"ds_read_b64_tr_b16 a[194:195], v248 offset:22400\n\t"
"v_lshl_or_b32 v158, v158, 16, v158\n\t"
"v_mfma_f32_16x16x32_bf16 v[124:127], v[60:63], a[172:175], v[124:127]\n\t"
"s_nop 0\n\t"
"ds_write_addtid_b32 v201 offset:26912\n\t"
"ds_write_addtid_b32 v203 offset:31040\n\t"
"ds_write_addtid_b32 v208 offset:17568\n\t"
"v_xor_b32 v166, v167, v158\n\t"
"v_accvgpr_write_b32 a210, v166\n\t"
"v_accvgpr_write_b32 a211, v167\n\t"
"s_add_u32 s61, s26, 4294900096\n\t"
"v_lshrrev_b32 v155, 4, v176\n\t"
"v_and_b32 v158, 1, v156\n\t"
"v_mfma_f32_16x16x32_bf16 v[108:111], v[48:51], a[156:159], 0\n\t"
"ds_read_b128 a[156:159], v224 offset:1792\n\t"
"v_cvt_pk_bf16_f32 v219, v219, v223\n\t"
"v_mfma_f32_16x16x32_bf16 v[108:111], v[52:55], a[172:175], v[108:111]\n\t"
"s_waitcnt lgkmcnt(15)\n\t"
"ds_write_addtid_b32 v219 offset:32064\n\t"
"buffer_load_dword v191, v189, s[8:11], 0 offen sc1\n\t"
"v_lshrrev_b32 v159, 1, v156\n\t"
"v_mul_u32_u24 v166, 1152, v159\n\t"
"v_mul_u32_u24 v167, 68352, v158\n\t"
"v_add3_u32 v166, v166, v157, s61\n\t"
"v_add_u32 v166, v166, v167\n\t"
"v_cvt_pk_bf16_f32 v216, v216, v220\n\t"
"v_cvt_pk_bf16_f32 v217, v217, v221\n\t"
"v_cvt_pk_bf16_f32 v218, v218, v222\n\t"
"v_mfma_f32_16x16x32_bf16 v[0:3], v[72:75], a[144:147], v[96:99]\n\t"
"ds_write_addtid_b32 v217 offset:27936\n\t"
"v_xor_b32 v159, v159, v158\n\t"
"v_mfma_f32_16x16x32_bf16 v[0:3], v[76:79], a[160:163], v[0:3]\n\t"
"s_nop 0\n\t"
"ds_write_addtid_b32 v209 offset:25888\n\t"
"ds_write_addtid_b32 v210 offset:21696\n\t"
"ds_write_addtid_b32 v211 offset:30016\n\t"
"ds_write_addtid_b32 v216 offset:19616\n\t"
"v_lshl_add_u32 v166, v159, 6, v166\n\t"
"v_xad_u32 v167, v158, 0, v155\n\t"
"v_mfma_f32_16x16x32_bf16 v[16:19], v[80:83], a[144:147], v[112:115]\n\t"
"ds_write_addtid_b32 v218 offset:23744\n\t"
"v_lshl_add_u32 v168, v167, 7, v166\n\t"
"v_mfma_f32_16x16x32_bf16 v[16:19], v[84:87], a[160:163], v[16:19]\n\t"
"ds_read_b128 a[172:175], v224 offset:3840\n\t"
"v_and_b32 v167, 2, v167\n\t"
"v_mfma_f32_16x16x32_bf16 v[32:35], v[88:91], a[144:147], v[128:131]\n\t"
"ds_read_b64_tr_b16 a[196:197], v249 offset:21888\n\t"
"v_lshl_add_u32 v168, v167, 4, v168\n\t"
"v_mfma_f32_16x16x32_bf16 v[32:35], v[92:95], a[160:163], v[32:35]\n\t"
"s_waitcnt lgkmcnt(15)\n\t"
"ds_read_b64_tr_b16 a[198:199], v249 offset:22400\n\t"
"v_xor_b32 v168, 64, v168\n\t"
"v_mfma_f32_16x16x32_bf16 v[36:39], v[88:91], a[148:151], v[132:135]\n\t"
"ds_read_b64_tr_b16 a[200:201], v250 offset:21888\n\t"
"v_accvgpr_write_b32 a208, v168\n\t"
"v_mfma_f32_16x16x32_bf16 v[36:39], v[92:95], a[164:167], v[36:39]\n\t"
"ds_read_b64_tr_b16 a[202:203], v250 offset:22400\n\t"
"v_xad_u32 v167, v158, 1, v155\n\t"
"v_mfma_f32_16x16x32_bf16 v[20:23], v[80:83], a[148:151], v[116:119]\n\t"
"s_nop 0\n\t"
"ds_read_b64_tr_b16 a[204:205], v251 offset:21888\n\t"
"ds_read_b64_tr_b16 a[206:207], v251 offset:22400\n\t"
"v_mfma_f32_16x16x32_bf16 v[20:23], v[84:87], a[164:167], v[20:23]\n\t"
"v_lshl_add_u32 v168, v167, 7, v166\n\t"
"v_and_b32 v167, 2, v167\n\t"
"v_mfma_f32_16x16x32_bf16 v[4:7], v[72:75], a[148:151], v[100:103]\n\t"
"v_lshl_add_u32 v168, v167, 4, v168\n\t"
"v_accvgpr_write_b32 a209, v168\n\t"
"v_mfma_f32_16x16x32_bf16 v[4:7], v[76:79], a[164:167], v[4:7]\n\t"
"s_waitcnt lgkmcnt(6)\n\t"
"s_waitcnt vmcnt(2)\n\t"
"v_cmp_eq_u32 vcc, v190, v188\n\t"
"s_nop 4\n\t"
"s_cmp_eq_u64 vcc, 0\n\t"
"s_cbranch_scc1 Lgo_%=\n\t"
"s_sub_u32 s68, s68, 1\n\t"
"s_waitcnt vmcnt(0)\n\t"
"Lchk_%=:\n\t"
"v_cmp_eq_u32 vcc, v191, v188\n\t"
"s_nop 4\n\t"
"s_cmp_eq_u64 vcc, 0\n\t"
"s_cbranch_scc1 Lgo_%=\n\t"
"s_sub_u32 s68, s68, 1\n\t"
"s_cmp_eq_u32 s68, 0\n\t"
"s_cbranch_scc1 Lfb_%=\n\t"
"buffer_load_dword v191, v189, s[8:11], 0 offen sc1\n\t"
"s_waitcnt vmcnt(0)\n\t"
"s_branch Lchk_%=\n\t"
"Lgo_%=:\n\t"
"s_barrier\n\t"
"v_mfma_f32_16x16x32_bf16 v[8:11], v[72:75], a[152:155], v[104:107]\n\t"
"s_nop 0\n\t"
"ds_read_b128 a[144:147], v224 offset:16544\n\t"
"ds_read_b128 a[160:163], v224 offset:18592\n\t"
"v_mfma_f32_16x16x32_bf16 v[8:11], v[76:79], a[168:171], v[8:11]\n\t"
"s_mov_b32 s54, 98304\n\t"
"buffer_load_dwordx4 a[96:99], off, s[92:95], s54 offset:0 sc1\n\t"
"buffer_load_dwordx4 a[100:103], off, s[92:95], s54 offset:1024 sc1\n\t"
"v_mfma_f32_16x16x32_bf16 v[24:27], v[80:83], a[152:155], v[120:123]\n\t"
"ds_read_b128 a[148:151], v224 offset:16800\n\t"
"v_mbcnt_lo_u32_b32 v176, -1, 0\n\t"
"v_mfma_f32_16x16x32_bf16 v[24:27], v[84:87], a[168:171], v[24:27]\n\t"
"ds_read_b128 a[164:167], v224 offset:18848\n\t"
"buffer_load_dwordx4 a[104:107], off, s[92:95], s54 offset:2048 sc1\n\t"
"v_mbcnt_hi_u32_b32 v176, -1, v176\n\t"
"v_mfma_f32_16x16x32_bf16 v[40:43], v[88:91], a[152:155], v[136:139]\n\t"
"v_and_b32 v154, 15, v176\n\t"
"v_lshrrev_b32 v155, 4, v176\n\t"
"v_mfma_f32_16x16x32_bf16 v[40:43], v[92:95], a[168:171], v[40:43]\n\t"
"buffer_load_dwordx4 a[108:111], off, s[92:95], s54 offset:3072 sc1\n\t"
"v_mfma_f32_16x16x32_bf16 v[44:47], v[88:91], a[156:159], v[140:143]\n\t"
"ds_read_b128 a[168:171], v224 offset:19104\n\t"
"v_bfe_u32 v156, v176, 2, 2\n\t"
"v_mfma_f32_16x16x32_bf16 v[44:47], v[92:95], a[172:175], v[44:47]\n\t"
"buffer_load_dwordx4 a[112:115], off, s[96:99], s54 offset:0 sc1\n\t"
"v_mfma_f32_16x16x32_bf16 v[28:31], v[80:83], a[156:159], v[124:127]\n\t"
"v_and_b32 v157, 3, v176\n\t"
"v_lshlrev_b32 v157, 3, v157\n\t"
"s_add_u32 s60, s43, 33280\n\t"
"v_mfma_f32_16x16x32_bf16 v[28:31], v[84:87], a[172:175], v[28:31]\n\t"
"buffer_load_dwordx4 a[116:119], off, s[96:99], s54 offset:1024 sc1\n\t"
"v_mfma_f32_16x16x32_bf16 v[12:15], v[72:75], a[156:159], v[108:111]\n\t"
"s_add_u32 s61, s26, 4294899008\n\t"
"ds_read_b128 a[156:159], v224 offset:17312\n\t"
"s_mul_i32 s62, s73, 96\n\t"
"v_mfma_f32_16x16x32_bf16 v[12:15], v[76:79], a[172:175], v[12:15]\n\t"
"s_mov_b32 s66, 172032\n\t"
"buffer_load_dwordx4 a[120:123], off, s[92:95], s66 offset:0 sc1\n\t"
"v_mfma_f32_16x16x32_bf16 v[192:195], a[216:219], a[176:179], 0\n\t"
"s_add_u32 s62, s62, 75008\n\t"
"v_min_u32 v158, 35, v176\n\t"
"v_lshl_add_u32 v254, v158, 4, s60\n\t"
"v_mfma_f32_16x16x32_bf16 v[196:199], a[216:219], a[180:183], 0\n\t"
"buffer_load_dwordx4 a[124:127], off, s[92:95], s66 offset:1024 sc1\n\t"
"v_mfma_f32_16x16x32_bf16 v[200:203], a[216:219], a[184:187], 0\n\t"
"ds_read_b128 a[152:155], v224 offset:17056\n\t"
"v_add_u32 v255, 68352, v254\n\t"
"v_and_b32 v158, 1, v156\n\t"
"v_lshrrev_b32 v156, 3, v176\n\t"
"v_mfma_f32_16x16x32_bf16 v[204:207], a[216:219], a[188:191], 0\n\t"
"buffer_load_dwordx4 a[128:131], off, s[92:95], s66 offset:2048 sc1\n\t"
"v_add_u32 v159, v156, v158\n\t"
".p2align 3\n\t"
"v_pk_fma_f32 v[96:97], v[96:97], s[38:39], v[0:1] neg_lo:[1,0,1] neg_hi:[1,0,1]\n\t"
"v_pk_fma_f32 v[98:99], v[98:99], s[38:39], v[2:3] neg_lo:[1,0,1] neg_hi:[1,0,1]\n\t"
"v_pk_fma_f32 v[112:113], v[112:113], s[38:39], v[16:17] neg_lo:[1,0,1] neg_hi:[1,0,1]\n\t"
"v_pk_fma_f32 v[114:115], v[114:115], s[38:39], v[18:19] neg_lo:[1,0,1] neg_hi:[1,0,1]\n\t"
"v_pk_fma_f32 v[128:129], v[128:129], s[38:39], v[32:33] neg_lo:[1,0,1] neg_hi:[1,0,1]\n\t"
"v_pk_fma_f32 v[130:131], v[130:131], s[38:39], v[34:35] neg_lo:[1,0,1] neg_hi:[1,0,1]\n\t"
"v_pk_fma_f32 v[132:133], v[132:133], s[38:39], v[36:37] neg_lo:[1,0,1] neg_hi:[1,0,1]\n\t"
"v_pk_fma_f32 v[134:135], v[134:135], s[38:39], v[38:39] neg_lo:[1,0,1] neg_hi:[1,0,1]\n\t"
"v_pk_fma_f32 v[116:117], v[116:117], s[38:39], v[20:21] neg_lo:[1,0,1] neg_hi:[1,0,1]\n\t"
"v_pk_fma_f32 v[118:119], v[118:119], s[38:39], v[22:23] neg_lo:[1,0,1] neg_hi:[1,0,1]\n\t"
"v_pk_fma_f32 v[100:101], v[100:101], s[38:39], v[4:5] neg_lo:[1,0,1] neg_hi:[1,0,1]\n\t"
"v_pk_fma_f32 v[102:103], v[102:103], s[38:39], v[6:7] neg_lo:[1,0,1] neg_hi:[1,0,1]\n\t"
"buffer_load_dwordx4 a[132:135], off, s[92:95], s66 offset:3072 sc1\n\t"
"v_pk_fma_f32 v[104:105], v[104:105], s[38:39], v[8:9] neg_lo:[1,0,1] neg_hi:[1,0,1]\n\t"
"v_pk_fma_f32 v[106:107], v[106:107], s[38:39], v[10:11] neg_lo:[1,0,1] neg_hi:[1,0,1]\n\t"
"v_pk_fma_f32 v[120:121], v[120:121], s[38:39], v[24:25] neg_lo:[1,0,1] neg_hi:[1,0,1]\n\t"
"v_pk_fma_f32 v[122:123], v[122:123], s[38:39], v[26:27] neg_lo:[1,0,1] neg_hi:[1,0,1]\n\t"
"v_pk_fma_f32 v[136:137], v[136:137], s[38:39], v[40:41] neg_lo:[1,0,1] neg_hi:[1,0,1]\n\t"
"v_pk_fma_f32 v[138:139], v[138:139], s[38:39], v[42:43] neg_lo:[1,0,1] neg_hi:[1,0,1]\n\t"
"v_pk_fma_f32 v[140:141], v[140:141], s[38:39], v[44:45] neg_lo:[1,0,1] neg_hi:[1,0,1]\n\t"
"v_pk_fma_f32 v[142:143], v[142:143], s[38:39], v[46:47] neg_lo:[1,0,1] neg_hi:[1,0,1]\n\t"
"v_pk_fma_f32 v[124:125], v[124:125], s[38:39], v[28:29] neg_lo:[1,0,1] neg_hi:[1,0,1]\n\t"
"v_pk_fma_f32 v[126:127], v[126:127], s[38:39], v[30:31] neg_lo:[1,0,1] neg_hi:[1,0,1]\n\t"
"v_pk_fma_f32 v[108:109], v[108:109], s[38:39], v[12:13] neg_lo:[1,0,1] neg_hi:[1,0,1]\n\t"
"v_pk_fma_f32 v[110:111], v[110:111], s[38:39], v[14:15] neg_lo:[1,0,1] neg_hi:[1,0,1]\n\t"
"v_mfma_f32_16x16x32_bf16 v[208:211], a[216:219], a[192:195], 0\n\t"
"s_waitcnt lgkmcnt(11)\n\t"
"ds_read_b128 a[172:175], v224 offset:19360\n\t"
"v_lshl_add_u32 v166, v159, 7, v157\n\t"
"v_and_b32 v159, 2, v159\n\t"
"v_mfma_f32_16x16x32_bf16 v[212:215], a[216:219], a[196:199], 0\n\t"
"s_waitcnt lgkmcnt(10)\n\t"
"buffer_load_dwordx4 a[136:139], off, s[96:99], s66 offset:0 sc1\n\t"
"v_lshl_add_u32 v166, v159, 4, v166\n\t"
"v_mfma_f32_16x16x32_bf16 v[216:219], a[216:219], a[200:203], 0\n\t"
"s_waitcnt lgkmcnt(8)\n\t"
"v_xad_u32 v167, v158, 1, v156\n\t"
"v_lshl_add_u32 v168, v167, 7, v157\n\t"
"v_mfma_f32_16x16x32_bf16 v[220:223], a[216:219], a[204:207], 0\n\t"
"s_waitcnt lgkmcnt(6)\n\t"
"v_and_b32 v167, 2, v167\n\t"
"v_lshl_add_u32 v168, v167, 4, v168\n\t"
"v_cvt_pk_bf16_f32 v192, v192, v196\n\t"
"v_cvt_pk_bf16_f32 v193, v193, v197\n\t"
"v_cvt_pk_bf16_f32 v195, v195, v199\n\t"
"v_mfma_f32_16x16x32_bf16 v[0:3], a[48:51], a[144:147], v[0:3]\n\t"
"ds_write_addtid_b32 v193 offset:8320\n\t"
"v_cvt_pk_bf16_f32 v194, v194, v198\n\t"
"v_mfma_f32_16x16x32_bf16 v[0:3], a[52:55], a[160:163], v[0:3]\n\t"
"ds_write_addtid_b32 v194 offset:4128\n\t"
"buffer_load_dwordx4 a[140:143], off, s[96:99], s66 offset:1024 sc1\n\t"
"v_sub_u32 v168, v168, v166\n\t"
"v_cvt_pk_bf16_f32 v201, v201, v205\n\t"
"v_cvt_pk_bf16_f32 v202, v202, v206\n\t"
"v_mfma_f32_16x16x32_bf16 v[16:19], a[56:59], a[144:147], v[16:19]\n\t"
"ds_write_addtid_b32 v201 offset:10368\n\t"
"v_mul_u32_u24 v159, 68288, v158\n\t"
"v_mfma_f32_16x16x32_bf16 v[16:19], a[60:63], a[160:163], v[16:19]\n\t"
"ds_write_addtid_b32 v192 offset:0\n\t"
"s_mov_b32 s69, 196608\n\t"
"buffer_load_dwordx4 a[0:3], off, s[92:95], s69 offset:0 sc1\n\t"
"v_add_u32 v166, v166, v159\n\t"
"v_lshl_add_u32 v168, v158, 7, v168\n\t"
"v_subrev_u32 v253, 64, v168\n\t"
"v_cvt_pk_bf16_f32 v210, v210, v214\n\t"
"v_mfma_f32_16x16x32_bf16 v[32:35], a[64:67], a[144:147], v[32:35]\n\t"
"ds_read_b128 a[144:147], v224 offset:17568\n\t"
"v_add_u32 v225, s61, v166\n\t"
"v_mfma_f32_16x16x32_bf16 v[32:35], a[68:71], a[160:163], v[32:35]\n\t"
"s_waitcnt lgkmcnt(9)\n\t"
"ds_read_b128 a[160:163], v224 offset:19616\n\t"
"v_mul_u32_u24 v158, 400, v154\n\t"
"v_cvt_pk_bf16_f32 v208, v208, v212\n\t"
"v_mfma_f32_16x16x32_bf16 v[36:39], a[64:67], a[148:151], v[36:39]\n\t"
"ds_write_addtid_b32 v195 offset:12448\n\t"
"v_cvt_pk_bf16_f32 v219, v219, v223\n\t"
"v_mfma_f32_16x16x32_bf16 v[36:39], a[68:71], a[164:167], v[36:39]\n\t"
"ds_write_addtid_b32 v219 offset:15520\n\t"
"buffer_load_dwordx4 a[4:7], off, s[92:95], s69 offset:1024 sc1\n\t"
"v_mul_u32_u24 v159, 24, v155\n\t"
"s_barrier\n\t"
"v_mfma_f32_16x16x32_bf16 v[20:23], a[56:59], a[148:151], v[20:23]\n\t"
"ds_read_b64_tr_b16 a[176:177], v248 offset:41472\n\t"
"v_add3_u32 v228, v158, v159, s62\n\t"
"s_mul_i32 s60, s73, 3200\n\t"
"v_mfma_f32_16x16x32_bf16 v[20:23], a[60:63], a[164:167], v[20:23]\n\t"
"ds_read_b64_tr_b16 a[178:179], v248 offset:41984\n\t"
"buffer_load_dwordx4 a[8:11], off, s[92:95], s69 offset:2048 sc1\n\t"
"v_cvt_pk_bf16_f32 v203, v203, v207\n\t"
"v_mfma_f32_16x16x32_bf16 v[4:7], a[48:51], a[148:151], v[4:7]\n\t"
"s_add_u32 s60, s60, 75008\n\t"
"ds_read_b128 a[148:151], v224 offset:17824\n\t"
"ds_read_b64_tr_b16 a[180:181], v249 offset:41472\n\t"
"v_mfma_f32_16x16x32_bf16 v[4:7], a[52:55], a[164:167], v[4:7]\n\t"
"s_waitcnt lgkmcnt(13)\n\t"
"ds_read_b128 a[164:167], v224 offset:19872\n\t"
"v_cvt_pk_bf16_f32 v218, v218, v222\n\t"
"v_mfma_f32_16x16x32_bf16 v[8:11], a[48:51], a[152:155], v[8:11]\n\t"
"s_and_b32 s61, s73, 2\n\t"
"ds_read_b64_tr_b16 a[182:183], v249 offset:41984\n\t"
"v_cvt_pk_bf16_f32 v217, v217, v221\n\t"
"v_mfma_f32_16x16x32_bf16 v[8:11], a[52:55], a[168:171], v[8:11]\n\t"
"ds_read_b64_tr_b16 a[184:185], v250 offset:41472\n\t"
"buffer_load_dwordx4 a[12:15], off, s[92:95], s69 offset:3072 sc1\n\t"
"v_cvt_pk_bf16_f32 v216, v216, v220\n\t"
"v_mfma_f32_16x16x32_bf16 v[24:27], a[56:59], a[152:155], v[24:27]\n\t"
"s_lshl_b32 s61, s61, 7\n\t"
"ds_read_b64_tr_b16 a[186:187], v250 offset:41984\n\t"
"ds_read_b64_tr_b16 a[188:189], v251 offset:41472\n\t"
"v_mfma_f32_16x16x32_bf16 v[24:27], a[60:63], a[168:171], v[24:27]\n\t"
"ds_read_b64_tr_b16 a[190:191], v251 offset:41984\n\t"
"v_cvt_pk_bf16_f32 v211, v211, v215\n\t"
"v_mfma_f32_16x16x32_bf16 v[40:43], a[64:67], a[152:155], v[40:43]\n\t"
"ds_read_b128 a[152:155], v224 offset:18080\n\t"
"v_cvt_pk_bf16_f32 v200, v200, v204\n\t"
"v_mfma_f32_16x16x32_bf16 v[40:43], a[68:71], a[168:171], v[40:43]\n\t"
"s_waitcnt lgkmcnt(15)\n\t"
"ds_read_b128 a[168:171], v224 offset:20128\n\t"
"buffer_load_dwordx4 a[16:19], off, s[96:99], s69 offset:0 sc1\n\t"
"v_mfma_f32_16x16x32_bf16 v[44:47], a[64:67], a[156:159], v[44:47]\n\t"
"s_add_u32 s60, s60, s61\n\t"
"ds_write_addtid_b32 v218 offset:7200\n\t"
"ds_read_b64_tr_b16 a[192:193], v248 offset:42624\n\t"
"v_mfma_f32_16x16x32_bf16 v[44:47], a[68:71], a[172:175], v[44:47]\n\t"
"ds_read_b64_tr_b16 a[194:195], v248 offset:43136\n\t"
"buffer_load_dwordx4 a[20:23], off, s[96:99], s69 offset:1024 sc1\n\t"
"v_mfma_f32_16x16x32_bf16 v[28:31], a[56:59], a[156:159], v[28:31]\n\t"
"ds_write_addtid_b32 v217 offset:11392\n\t"
"v_cvt_pk_bf16_f32 v209, v209, v213\n\t"
"v_mfma_f32_16x16x32_bf16 v[28:31], a[60:63], a[172:175], v[28:31]\n\t"
"s_mul_i32 s61, s73, 393216\n\t"
"ds_write_addtid_b32 v200 offset:2048\n\t"
"ds_write_addtid_b32 v210 offset:5152\n\t"
"v_add_u32 v158, 0, v176\n\t"
"v_mfma_f32_16x16x32_bf16 v[12:15], a[48:51], a[156:159], v[12:15]\n\t"
"ds_read_b128 a[156:159], v224 offset:18336\n\t"
"v_mul_u32_u24 v159, 0xab, v158\n\t"
"v_mfma_f32_16x16x32_bf16 v[12:15], a[52:55], a[172:175], v[12:15]\n\t"
"s_waitcnt lgkmcnt(15)\n\t"
"ds_read_b128 a[172:175], v224 offset:20384\n\t"
"s_mov_b32 s81, 270336\n\t"
"buffer_load_dwordx4 a[24:27], off, s[92:95], s81 offset:0 sc1\n\t"
"v_lshrrev_b32 v159, 12, v159\n\t"
"v_mfma_f32_16x16x32_bf16 v[96:99], a[72:75], a[144:147], v[96:99]\n\t"
"ds_write_addtid_b32 v208 offset:1024\n\t"
"v_add_lshl_u32 v166, v158, v159, 4\n\t"
"v_mfma_f32_16x16x32_bf16 v[96:99], a[76:79], a[160:163], v[96:99]\n\t"
"s_nop 0\n\t"
"ds_write_addtid_b32 v202 offset:6176\n\t"
"ds_write_addtid_b32 v209 offset:9344\n\t"
"ds_write_addtid_b32 v211 offset:13472\n\t"
"ds_write_addtid_b32 v216 offset:3072\n\t"
"buffer_load_dwordx4 a[28:31], off, s[92:95], s81 offset:1024 sc1\n\t"
"v_add_u32 v229, s60, v166\n\t"
"v_mad_u32_u24 v158, v159, 24, v158\n\t"
"v_lshl_add_u32 v232, v158, 4, s61\n\t"
"v_mfma_f32_16x16x32_bf16 v[112:115], a[80:83], a[144:147], v[112:115]\n\t"
"ds_write_addtid_b32 v203 offset:14496\n\t"
"v_add_u32 v158, 64, v176\n\t"
"v_mfma_f32_16x16x32_bf16 v[112:115], a[84:87], a[160:163], v[112:115]\n\t"
"ds_read_b64_tr_b16 a[196:197], v249 offset:42624\n\t"
"v_mul_u32_u24 v159, 0xab, v158\n\t"
"v_lshrrev_b32 v159, 12, v159\n\t"
"v_mfma_f32_16x16x32_bf16 v[128:131], a[88:91], a[144:147], v[128:131]\n\t"
"ds_read_b64_tr_b16 a[198:199], v249 offset:43136\n\t"
"v_add_lshl_u32 v166, v158, v159, 4\n\t"
"v_mfma_f32_16x16x32_bf16 v[128:131], a[92:95], a[160:163], v[128:131]\n\t"
"s_waitcnt lgkmcnt(15)\n\t"
"ds_read_b64_tr_b16 a[200:201], v250 offset:42624\n\t"
"buffer_load_dwordx4 a[32:35], off, s[92:95], s81 offset:2048 sc1\n\t"
"v_add_u32 v230, s60, v166\n\t"
"v_mad_u32_u24 v158, v159, 24, v158\n\t"
"v_mfma_f32_16x16x32_bf16 v[132:135], a[88:91], a[148:151], v[132:135]\n\t"
"ds_read_b64_tr_b16 a[202:203], v250 offset:43136\n\t"
"v_lshl_add_u32 v233, v158, 4, s61\n\t"
"v_mfma_f32_16x16x32_bf16 v[132:135], a[92:95], a[164:167], v[132:135]\n\t"
"ds_read_b64_tr_b16 a[204:205], v251 offset:42624\n\t"
"buffer_load_dwordx4 a[36:39], off, s[92:95], s81 offset:3072 sc1\n\t"
"v_add_u32 v158, 128, v176\n\t"
"v_mfma_f32_16x16x32_bf16 v[116:119], a[80:83], a[148:151], v[116:119]\n\t"
"ds_read_b64_tr_b16 a[206:207], v251 offset:43136\n\t"
"v_mul_u32_u24 v159, 0xab, v158\n\t"
"v_mfma_f32_16x16x32_bf16 v[116:119], a[84:87], a[164:167], v[116:119]\n\t"
"v_lshrrev_b32 v159, 12, v159\n\t"
"v_add_lshl_u32 v166, v158, v159, 4\n\t"
"v_add_u32 v231, s60, v166\n\t"
"v_mfma_f32_16x16x32_bf16 v[100:103], a[72:75], a[148:151], v[100:103]\n\t"
"v_mad_u32_u24 v158, v159, 24, v158\n\t"
"v_lshl_add_u32 v234, v158, 4, s61\n\t"
"v_mfma_f32_16x16x32_bf16 v[100:103], a[76:79], a[164:167], v[100:103]\n\t"
"buffer_load_dwordx4 a[40:43], off, s[96:99], s81 offset:0 sc1\n\t"
"s_waitcnt lgkmcnt(6)\n\t"
"s_add_u32 s71, s71, 1\n\t"
"v_mov_b32 v215, s71\n\t"
"s_mov_b64 exec, 1\n\t"
"buffer_store_dword v215, off, s[8:11], s70 sc1\n\t"
"s_mov_b64 exec, -1\n\t"
"s_barrier\n\t"
"v_mfma_f32_16x16x32_bf16 v[104:107], a[72:75], a[152:155], v[104:107]\n\t"
"ds_read_b128 a[148:151], v224 offset:256\n\t"
"v_add_u32 v248, 122368, v226\n\t"
"v_mfma_f32_16x16x32_bf16 v[104:107], a[76:79], a[168:171], v[104:107]\n\t"
"ds_read_b128 a[144:147], v224 offset:0\n\t"
"v_xor_b32 v249, 32, v248\n\t"
"v_mfma_f32_16x16x32_bf16 v[120:123], a[80:83], a[152:155], v[120:123]\n\t"
"ds_read_b128 a[160:163], v224 offset:2048\n\t"
"v_accvgpr_read_b32 v172, a220\n\t"
"v_mfma_f32_16x16x32_bf16 v[120:123], a[84:87], a[168:171], v[120:123]\n\t"
"ds_read_b128 a[164:167], v224 offset:2304\n\t"
"buffer_load_dwordx4 a[44:47], off, s[96:99], s81 offset:1024 sc1\n\t"
"v_mfma_f32_16x16x32_bf16 v[136:139], a[88:91], a[152:155], v[136:139]\n\t"
"v_and_b32 v172, 0xff7fff7f, v172\n\t"
"v_accvgpr_write_b32 a252, v172\n\t"
"v_mfma_f32_16x16x32_bf16 v[136:139], a[92:95], a[168:171], v[136:139]\n\t"
"s_mov_b32 s57, 122880\n\t"
"buffer_load_dwordx4 a[48:51], off, s[92:95], s57 offset:0 sc1\n\t"
"v_mfma_f32_16x16x32_bf16 v[140:143], a[88:91], a[156:159], v[140:143]\n\t"
"ds_read_b128 a[168:171], v224 offset:2560\n\t"
"v_accvgpr_read_b32 v173, a221\n\t"
"v_mfma_f32_16x16x32_bf16 v[140:143], a[92:95], a[172:175], v[140:143]\n\t"
"ds_read_b128 a[152:155], v224 offset:512\n\t"
"v_and_b32 v173, 0xff7fff7f, v173\n\t"
"v_mfma_f32_16x16x32_bf16 v[124:127], a[80:83], a[156:159], v[124:127]\n\t"
"v_accvgpr_write_b32 a253, v173\n\t"
"v_accvgpr_read_b32 v174, a222\n\t"
"v_mfma_f32_16x16x32_bf16 v[124:127], a[84:87], a[172:175], v[124:127]\n\t"
"buffer_load_dwordx4 a[52:55], off, s[92:95], s57 offset:1024 sc1\n\t"
"v_mfma_f32_16x16x32_bf16 v[108:111], a[72:75], a[156:159], v[108:111]\n\t"
"ds_read_b128 a[156:159], v224 offset:768\n\t"
"v_xor_b32 v250, 64, v248\n\t"
"v_mfma_f32_16x16x32_bf16 v[108:111], a[76:79], a[172:175], v[108:111]\n\t"
"buffer_load_dwordx4 a[56:59], off, s[92:95], s57 offset:2048 sc1\n\t"
"v_mfma_f32_16x16x32_bf16 v[192:195], a[216:219], a[176:179], 0\n\t"
"ds_read_b128 a[172:175], v224 offset:2816\n\t"
"v_and_b32 v174, 0xff7fff7f, v174\n\t"
"v_mfma_f32_16x16x32_bf16 v[196:199], a[216:219], a[180:183], 0\n\t"
"v_accvgpr_write_b32 a254, v174\n\t"
"v_xor_b32 v251, 96, v248\n\t"
"v_mfma_f32_16x16x32_bf16 v[200:203], a[216:219], a[184:187], 0\n\t"
"ds_read_b64_tr_b16 a[178:179], v248 offset:128\n\t"
"v_accvgpr_read_b32 v175, a223\n\t"
"v_mfma_f32_16x16x32_bf16 v[204:207], a[216:219], a[188:191], 0\n\t"
"buffer_load_dwordx4 a[60:63], off, s[92:95], s57 offset:3072 sc1\n\t"
"v_mfma_f32_16x16x32_bf16 v[208:211], a[216:219], a[192:195], 0\n\t"
"s_waitcnt lgkmcnt(13)\n\t"
"ds_read_b64_tr_b16 a[176:177], v248 offset:0\n\t"
"v_and_b32 v175, 0xff7fff7f, v175\n\t"
"v_mfma_f32_16x16x32_bf16 v[212:215], a[216:219], a[196:199], 0\n\t"
"s_waitcnt lgkmcnt(12)\n\t"
"ds_read_b64_tr_b16 a[180:181], v249 offset:256\n\t"
"v_accvgpr_write_b32 a255, v175\n\t"
"v_mfma_f32_16x16x32_bf16 v[216:219], a[216:219], a[200:203], 0\n\t"
"s_waitcnt lgkmcnt(11)\n\t"
"buffer_load_dwordx4 a[64:67], off, s[96:99], s57 offset:0 sc1\n\t"
"v_mfma_f32_16x16x32_bf16 v[220:223], a[216:219], a[204:207], 0\n\t"
"s_waitcnt vmcnt(28) lgkmcnt(8)\n\t"
"v_cvt_pk_bf16_f32 v192, v192, v196\n\t"
"v_cvt_pk_bf16_f32 v194, v194, v198\n\t"
"v_mfma_f32_16x16x32_bf16 v[0:3], a[96:99], a[144:147], v[0:3]\n\t"
"s_mov_b32 s90, 0.5\n\t"
"ds_read_b64_tr_b16 a[186:187], v249 offset:128\n\t"
"ds_read_b64_tr_b16 a[184:185], v249 offset:0\n\t"
"v_mfma_f32_16x16x32_bf16 v[0:3], a[100:103], a[160:163], v[0:3]\n\t"
"s_waitcnt vmcnt(26)\n\t"
"buffer_load_dwordx4 a[68:71], off, s[96:99], s57 offset:1024 sc1\n\t"
"v_mfma_f32_16x16x32_bf16 v[16:19], a[104:107], a[144:147], v[16:19]\n\t"
"s_mov_b32 s91, 0.5\n\t"
"ds_write_addtid_b32 v194 offset:20672\n\t"
"v_cvt_pk_bf16_f32 v202, v202, v206\n\t"
"v_mfma_f32_16x16x32_bf16 v[16:19], a[108:111], a[160:163], v[16:19]\n\t"
"s_waitcnt vmcnt(25)\n\t"
"v_cvt_pk_bf16_f32 v208, v208, v212\n\t"
"v_cvt_pk_bf16_f32 v211, v211, v215\n\t"
"v_mfma_f32_16x16x32_bf16 v[32:35], a[112:115], a[144:147], v[32:35]\n\t"
"s_add_u32 s84, s92, 18432\n\t"
"s_addc_u32 s85, s93, 0\n\t"
"ds_read_b128 a[144:147], v224 offset:1024\n\t"
"v_mfma_f32_16x16x32_bf16 v[32:35], a[116:119], a[160:163], v[32:35]\n\t"
"s_waitcnt lgkmcnt(11)\n\t"
"s_mov_b32 s63, 147456\n\t"
"buffer_load_dwordx4 a[72:75], off, s[92:95], s63 offset:0 sc1\n\t"
"v_mfma_f32_16x16x32_bf16 v[36:39], a[112:115], a[148:151], v[36:39]\n\t"
"s_mov_b32 s41, 16384\n\t"
"ds_read_b128 a[160:163], v224 offset:3072\n\t"
"v_cvt_pk_bf16_f32 v201, v201, v205\n\t"
"v_mfma_f32_16x16x32_bf16 v[36:39], a[116:119], a[164:167], v[36:39]\n\t"
"ds_write_addtid_b32 v202 offset:22720\n\t"
"v_cvt_pk_bf16_f32 v219, v219, v223\n\t"
"v_mfma_f32_16x16x32_bf16 v[20:23], a[104:107], a[148:151], v[20:23]\n\t"
"ds_write_addtid_b32 v208 offset:17568\n\t"
"v_cvt_pk_bf16_f32 v195, v195, v199\n\t"
"v_mfma_f32_16x16x32_bf16 v[20:23], a[108:111], a[164:167], v[20:23]\n\t"
"buffer_load_dwordx4 a[76:79], off, s[92:95], s63 offset:1024 sc1\n\t"
"v_mfma_f32_16x16x32_bf16 v[4:7], a[96:99], a[148:151], v[4:7]\n\t"
"s_mov_b64 s[86:87], s[94:95]\n\t"
"ds_read_b128 a[148:151], v224 offset:1280\n\t"
"v_cvt_pk_bf16_f32 v200, v200, v204\n\t"
"v_mfma_f32_16x16x32_bf16 v[4:7], a[100:103], a[164:167], v[4:7]\n\t"
"s_waitcnt lgkmcnt(13)\n\t"
"ds_write_addtid_b32 v211 offset:30016\n\t"
"v_cvt_pk_bf16_f32 v218, v218, v222\n\t"
"v_mfma_f32_16x16x32_bf16 v[8:11], a[96:99], a[152:155], v[8:11]\n\t"
"ds_read_b128 a[164:167], v224 offset:3328\n\t"
"v_cvt_pk_bf16_f32 v209, v209, v213\n\t"
"v_mfma_f32_16x16x32_bf16 v[8:11], a[100:103], a[168:171], v[8:11]\n\t"
"buffer_load_dwordx4 a[80:83], off, s[92:95], s63 offset:2048 sc1\n\t"
"v_mfma_f32_16x16x32_bf16 v[24:27], a[104:107], a[152:155], v[24:27]\n\t"
"ds_write_addtid_b32 v209 offset:25888\n\t"
"v_cvt_pk_bf16_f32 v216, v216, v220\n\t"
"v_mfma_f32_16x16x32_bf16 v[24:27], a[108:111], a[168:171], v[24:27]\n\t"
"ds_write_addtid_b32 v216 offset:19616\n\t"
"v_cvt_pk_bf16_f32 v203, v203, v207\n\t"
"v_mfma_f32_16x16x32_bf16 v[40:43], a[112:115], a[152:155], v[40:43]\n\t"
"s_xor_b64 s[88:89], s[84:85], s[92:93]\n\t"
"ds_read_b128 a[152:155], v224 offset:1536\n\t"
"v_cvt_pk_bf16_f32 v193, v193, v197\n\t"
"v_mfma_f32_16x16x32_bf16 v[40:43], a[116:119], a[168:171], v[40:43]\n\t"
"s_waitcnt lgkmcnt(15)\n\t"
"buffer_load_dwordx4 a[84:87], off, s[92:95], s63 offset:3072 sc1\n\t"
"v_mfma_f32_16x16x32_bf16 v[44:47], a[112:115], a[156:159], v[44:47]\n\t"
"ds_read_b128 a[168:171], v224 offset:3584\n\t"
"v_cvt_pk_bf16_f32 v210, v210, v214\n\t"
"v_mfma_f32_16x16x32_bf16 v[44:47], a[116:119], a[172:175], v[44:47]\n\t"
"s_add_u32 s28, s84, 4096\n\t"
"s_addc_u32 s29, s85, 0\n\t"
"ds_write_addtid_b32 v218 offset:23744\n\t"
"v_mfma_f32_16x16x32_bf16 v[28:31], a[104:107], a[156:159], v[28:31]\n\t"
"s_mov_b32 s46, 32768\n\t"
"ds_write_addtid_b32 v193 offset:24864\n\t"
"ds_write_addtid_b32 v195 offset:28992\n\t"
"v_mfma_f32_16x16x32_bf16 v[28:31], a[108:111], a[172:175], v[28:31]\n\t"
"buffer_load_dwordx4 a[88:91], off, s[96:99], s63 offset:0 sc1\n\t"
"v_mfma_f32_16x16x32_bf16 v[12:15], a[96:99], a[156:159], v[12:15]\n\t"
"s_mov_b32 s40, 8192\n\t"
"ds_write_addtid_b32 v201 offset:26912\n\t"
"ds_write_addtid_b32 v219 offset:32064\n\t"
"v_mfma_f32_16x16x32_bf16 v[12:15], a[100:103], a[172:175], v[12:15]\n\t"
"s_waitcnt vmcnt(28) lgkmcnt(14)\n\t"
"ds_write_addtid_b32 v203 offset:31040\n\t"
"ds_write_addtid_b32 v210 offset:21696\n\t"
"s_mov_b32 s47, 40960\n\t"
"v_mfma_f32_16x16x32_bf16 v[96:99], a[120:123], a[144:147], v[96:99]\n\t"
"s_mov_b64 s[30:31], s[94:95]\n\t"
"ds_write_addtid_b32 v200 offset:18592\n\t"
"v_cvt_pk_bf16_f32 v217, v217, v221\n\t"
"v_mfma_f32_16x16x32_bf16 v[96:99], a[124:127], a[160:163], v[96:99]\n\t"
"s_waitcnt vmcnt(26)\n\t"
"buffer_load_dwordx4 a[92:95], off, s[96:99], s63 offset:1024 sc1\n\t"
"v_mfma_f32_16x16x32_bf16 v[112:115], a[128:131], a[144:147], v[112:115]\n\t"
"s_add_u32 s25, s24, 0xffeb0000\n\t"
"ds_write_addtid_b32 v192 offset:16544\n\t"
"ds_write_addtid_b32 v217 offset:27936\n\t"
"v_mfma_f32_16x16x32_bf16 v[112:115], a[132:135], a[160:163], v[112:115]\n\t"
"s_waitcnt vmcnt(25)\n\t"
"ds_read_b128 a[156:159], v224 offset:1792\n\t"
"ds_read_b128 a[172:175], v224 offset:3840\n\t"
"s_mov_b32 s49, 57344\n\t"
"v_mfma_f32_16x16x32_bf16 v[128:131], a[136:139], a[144:147], v[128:131]\n\t"
"s_add_u32 m0, s26, 56192\n\t"
"ds_read_b64_tr_b16 a[188:189], v248 offset:256\n\t"
"s_mov_b32 s42, 24576\n\t"
"s_add_u32 s27, s24, 0xffe80000\n\t"
"v_mfma_f32_16x16x32_bf16 v[128:131], a[140:143], a[160:163], v[128:131]\n\t"
"s_waitcnt lgkmcnt(15)\n\t"
"buffer_load_dwordx4 v238, s[12:15], s22 offen offset:2176 nt lds\n\t"
"v_mfma_f32_16x16x32_bf16 v[132:135], a[136:139], a[148:151], v[132:135]\n\t"
"s_mov_b32 s50, 65536\n\t"
"ds_read_b64_tr_b16 a[194:195], v250 offset:128\n\t"
"ds_read_b64_tr_b16 a[192:193], v250 offset:0\n\t"
"v_mfma_f32_16x16x32_bf16 v[132:135], a[140:143], a[164:167], v[132:135]\n\t"
"s_add_u32 s20, s24, 0xffd30000\n\t"
"ds_read_b64_tr_b16 a[196:197], v251 offset:256\n\t"
"ds_read_b64_tr_b16 a[202:203], v251 offset:128\n\t"
"v_mfma_f32_16x16x32_bf16 v[116:119], a[128:131], a[148:151], v[116:119]\n\t"
"s_add_u32 s24, s24, 0xffd00000\n\t"
"ds_read_b64_tr_b16 a[200:201], v251 offset:0\n\t"
"ds_read_b64_tr_b16 a[204:205], v250 offset:256\n\t"
"v_mfma_f32_16x16x32_bf16 v[116:119], a[132:135], a[164:167], v[116:119]\n\t"
"s_mov_b32 s48, 49152\n\t"
"v_mfma_f32_16x16x32_bf16 v[100:103], a[120:123], a[148:151], v[100:103]\n\t"
"v_mfma_f32_16x16x32_bf16 v[100:103], a[124:127], a[164:167], v[100:103]\n\t"
"buffer_load_dwordx4 v239, s[12:15], s22 offen offset:3200 nt lds\n\t"
"s_lshl_b32 m0, s73, 8\n\t"
"s_waitcnt lgkmcnt(7)\n\t"
"s_barrier\n\t"
"v_mfma_f32_16x16x32_bf16 v[104:107], a[120:123], a[152:155], v[104:107]\n\t"
"s_add_u32 m0, s26, 56192\n\t"
"ds_read_b128 a[144:147], v224 offset:16544\n\t"
"v_mfma_f32_16x16x32_bf16 v[104:107], a[124:127], a[168:171], v[104:107]\n\t"
"ds_read_b128 a[148:151], v224 offset:16800\n\t"
"v_mfma_f32_16x16x32_bf16 v[120:123], a[128:131], a[152:155], v[120:123]\n\t"
"ds_read_b128 a[160:163], v224 offset:18592\n\t"
"v_mfma_f32_16x16x32_bf16 v[120:123], a[132:135], a[168:171], v[120:123]\n\t"
"ds_read_b128 a[164:167], v224 offset:18848\n\t"
"v_mfma_f32_16x16x32_bf16 v[136:139], a[136:139], a[152:155], v[136:139]\n\t"
"v_mfma_f32_16x16x32_bf16 v[136:139], a[140:143], a[168:171], v[136:139]\n\t"
"v_mfma_f32_16x16x32_bf16 v[140:143], a[136:139], a[156:159], v[140:143]\n\t"
"ds_read_b128 a[152:155], v224 offset:17056\n\t"
"v_mfma_f32_16x16x32_bf16 v[140:143], a[140:143], a[172:175], v[140:143]\n\t"
"ds_read_b128 a[168:171], v224 offset:19104\n\t"
"v_mfma_f32_16x16x32_bf16 v[124:127], a[128:131], a[156:159], v[124:127]\n\t"
"v_mfma_f32_16x16x32_bf16 v[124:127], a[132:135], a[172:175], v[124:127]\n\t"
"v_mfma_f32_16x16x32_bf16 v[108:111], a[120:123], a[156:159], v[108:111]\n\t"
"ds_read_b128 a[156:159], v224 offset:17312\n\t"
"v_mfma_f32_16x16x32_bf16 v[108:111], a[124:127], a[172:175], v[108:111]\n\t"
"s_waitcnt lgkmcnt(7)\n\t"
"ds_read_b128 a[172:175], v224 offset:19360\n\t"
"v_mfma_f32_4x4x4_16b_bf16 v[200:203], a[252:253], a[178:179], 0\n\t"
"s_mov_b64 exec, s[36:37]\n\t"
"buffer_load_dwordx4 v240, s[12:15], s22 offen offset:3712 nt lds\n\t"
"s_mov_b64 exec, -1\n\t"
"v_mfma_f32_4x4x4_16b_bf16 v[204:207], a[252:253], a[186:187], 0\n\t"
"ds_read_b64_tr_b16 a[178:179], v248 offset:20864\n\t"
"v_mfma_f32_4x4x4_16b_bf16 v[216:219], a[252:253], a[194:195], 0\n\t"
"ds_read_b64_tr_b16 a[186:187], v249 offset:20864\n\t"
"v_mfma_f32_4x4x4_16b_bf16 v[220:223], a[252:253], a[202:203], 0\n\t"
"ds_read_b64_tr_b16 a[194:195], v250 offset:20864\n\t"
"v_mfma_f32_4x4x4_16b_bf16 v[192:195], a[254:255], a[176:177], v[200:203]\n\t"
"s_lshl_b32 m0, s73, 8\n\t"
"v_mfma_f32_4x4x4_16b_bf16 v[196:199], a[254:255], a[184:185], v[204:207]\n\t"
"ds_read_b64_tr_b16 a[202:203], v251 offset:20864\n\t"
"v_mfma_f32_4x4x4_16b_bf16 v[200:203], a[254:255], a[180:181], v[200:203]\n\t"
"ds_read_b64_tr_b16 a[176:177], v248 offset:20736\n\t"
"v_mfma_f32_4x4x4_16b_bf16 v[204:207], a[254:255], a[188:189], v[204:207]\n\t"
"ds_read_b64_tr_b16 a[184:185], v249 offset:20736\n\t"
"v_mfma_f32_4x4x4_16b_bf16 v[208:211], a[254:255], a[192:193], v[216:219]\n\t"
"v_mfma_f32_4x4x4_16b_bf16 v[212:215], a[254:255], a[200:201], v[220:223]\n\t"
"ds_read_b64_tr_b16 a[180:181], v249 offset:20992\n\t"
"v_mfma_f32_4x4x4_16b_bf16 v[216:219], a[254:255], a[196:197], v[216:219]\n\t"
"ds_read_b64_tr_b16 a[188:189], v248 offset:20992\n\t"
"v_mfma_f32_4x4x4_16b_bf16 v[220:223], a[254:255], a[204:205], v[220:223]\n\t"
"s_waitcnt vmcnt(26) lgkmcnt(13)\n\t"
"s_mov_b32 s75, 221184\n\t"
"buffer_load_dwordx4 a[96:99], off, s[92:95], s75 offset:0 sc1\n\t"
"v_mfma_f32_16x16x32_bf16 v[0:3], a[0:3], a[144:147], v[0:3]\n\t"
"ds_read_b64_tr_b16 a[192:193], v250 offset:20736\n\t"
"v_cvt_pk_bf16_f32 v195, -v195, -v199\n\t"
"v_mfma_f32_16x16x32_bf16 v[0:3], a[4:7], a[160:163], v[0:3]\n\t"
"s_waitcnt vmcnt(25)\n\t"
"buffer_load_dwordx4 a[100:103], off, s[92:95], s75 offset:1024 sc1\n\t"
"v_mfma_f32_16x16x32_bf16 v[16:19], a[8:11], a[144:147], v[16:19]\n\t"
"ds_read_b64_tr_b16 a[200:201], v251 offset:20736\n\t"
"v_cvt_pk_bf16_f32 v202, v202, v206\n\t"
"v_mfma_f32_16x16x32_bf16 v[16:19], a[12:15], a[160:163], v[16:19]\n\t"
"s_waitcnt vmcnt(24)\n\t"
"buffer_load_dwordx4 a[104:107], off, s[92:95], s75 offset:2048 sc1\n\t"
"v_mfma_f32_16x16x32_bf16 v[32:35], a[16:19], a[144:147], v[32:35]\n\t"
"ds_read_b128 a[144:147], v224 offset:17568\n\t"
"v_cvt_pk_bf16_f32 v208, -v208, -v212\n\t"
"v_mfma_f32_16x16x32_bf16 v[32:35], a[20:23], a[160:163], v[32:35]\n\t"
"s_waitcnt lgkmcnt(15)\n\t"
"v_cvt_pk_bf16_f32 v201, v201, v205\n\t"
"v_cvt_pk_bf16_f32 v209, -v209, -v213\n\t"
"v_mfma_f32_16x16x32_bf16 v[36:39], a[16:19], a[148:151], v[36:39]\n\t"
"ds_read_b128 a[160:163], v224 offset:19616\n\t"
"v_cvt_pk_bf16_f32 v217, v217, v221\n\t"
"v_mfma_f32_16x16x32_bf16 v[36:39], a[20:23], a[164:167], v[36:39]\n\t"
"buffer_load_dwordx4 a[108:111], off, s[92:95], s75 offset:3072 sc1\n\t"
"v_mfma_f32_16x16x32_bf16 v[20:23], a[8:11], a[148:151], v[20:23]\n\t"
"ds_write_addtid_b32 v217 offset:14496\n\t"
"v_cvt_pk_bf16_f32 v200, v200, v204\n\t"
"v_mfma_f32_16x16x32_bf16 v[20:23], a[12:15], a[164:167], v[20:23]\n\t"
"ds_write_addtid_b32 v208 offset:2048\n\t"
"v_cvt_pk_bf16_f32 v219, v219, v223\n\t"
"v_mfma_f32_16x16x32_bf16 v[4:7], a[0:3], a[148:151], v[4:7]\n\t"
"ds_read_b128 a[148:151], v224 offset:17824\n\t"
"v_cvt_pk_bf16_f32 v216, v216, v220\n\t"
"v_mfma_f32_16x16x32_bf16 v[4:7], a[4:7], a[164:167], v[4:7]\n\t"
"s_waitcnt lgkmcnt(15)\n\t"
"buffer_load_dwordx4 a[112:115], off, s[96:99], s75 offset:0 sc1\n\t"
"v_mfma_f32_16x16x32_bf16 v[8:11], a[0:3], a[152:155], v[8:11]\n\t"
"ds_read_b128 a[164:167], v224 offset:19872\n\t"
"v_cvt_pk_bf16_f32 v211, -v211, -v215\n\t"
"v_mfma_f32_16x16x32_bf16 v[8:11], a[4:7], a[168:171], v[8:11]\n\t"
"ds_write_addtid_b32 v209 offset:6176\n\t"
"v_cvt_pk_bf16_f32 v203, v203, v207\n\t"
"v_mfma_f32_16x16x32_bf16 v[24:27], a[8:11], a[152:155], v[24:27]\n\t"
"ds_write_addtid_b32 v195 offset:5152\n\t"
"v_cvt_pk_bf16_f32 v210, -v210, -v214\n\t"
"v_mfma_f32_16x16x32_bf16 v[24:27], a[12:15], a[168:171], v[24:27]\n\t"
"buffer_load_dwordx4 a[116:119], off, s[96:99], s75 offset:1024 sc1\n\t"
"v_mfma_f32_16x16x32_bf16 v[40:43], a[16:19], a[152:155], v[40:43]\n\t"
"ds_read_b128 a[152:155], v224 offset:18080\n\t"
"v_cvt_pk_bf16_f32 v192, -v192, -v196\n\t"
"v_mfma_f32_16x16x32_bf16 v[40:43], a[20:23], a[168:171], v[40:43]\n\t"
"ds_read_b128 a[168:171], v224 offset:20128\n\t"
"v_cvt_pk_bf16_f32 v218, v218, v222\n\t"
"v_mfma_f32_16x16x32_bf16 v[44:47], a[16:19], a[156:159], v[44:47]\n\t"
"ds_write_addtid_b32 v210 offset:3072\n\t"
"v_cvt_pk_bf16_f32 v194, -v194, -v198\n\t"
"v_mfma_f32_16x16x32_bf16 v[44:47], a[20:23], a[172:175], v[44:47]\n\t"
"s_mov_b32 s78, 245760\n\t"
"buffer_load_dwordx4 a[120:123], off, s[92:95], s78 offset:0 sc1\n\t"
"v_mfma_f32_16x16x32_bf16 v[28:31], a[8:11], a[156:159], v[28:31]\n\t"
"s_mov_b32 s58, 131072\n\t"
"ds_write_addtid_b32 v192 offset:0\n\t"
"ds_write_addtid_b32 v200 offset:8320\n\t"
"v_mfma_f32_16x16x32_bf16 v[28:31], a[12:15], a[172:175], v[28:31]\n\t"
"s_mov_b32 s55, 106496\n\t"
"ds_write_addtid_b32 v194 offset:1024\n\t"
"ds_write_addtid_b32 v219 offset:15520\n\t"
"v_mfma_f32_16x16x32_bf16 v[12:15], a[0:3], a[156:159], v[12:15]\n\t"
"ds_write_addtid_b32 v218 offset:11392\n\t"
"v_cvt_pk_bf16_f32 v193, -v193, -v197\n\t"
"v_mfma_f32_16x16x32_bf16 v[12:15], a[4:7], a[172:175], v[12:15]\n\t"
"s_waitcnt vmcnt(27) lgkmcnt(14)\n\t"
"buffer_load_dwordx4 a[124:127], off, s[92:95], s78 offset:1024 sc1\n\t"
"v_mfma_f32_16x16x32_bf16 v[96:99], a[24:27], a[144:147], v[96:99]\n\t"
"s_mov_b32 s52, 81920\n\t"
"ds_write_addtid_b32 v211 offset:7200\n\t"
"ds_write_addtid_b32 v216 offset:10368\n\t"
"v_mfma_f32_16x16x32_bf16 v[96:99], a[28:31], a[160:163], v[96:99]\n\t"
"s_waitcnt vmcnt(26)\n\t"
"ds_write_addtid_b32 v193 offset:4128\n\t"
"ds_write_addtid_b32 v203 offset:13472\n\t"
"s_mov_b32 s56, 114688\n\t"
"v_mfma_f32_16x16x32_bf16 v[112:115], a[32:35], a[144:147], v[112:115]\n\t"
"s_mov_b32 s59, 139264\n\t"
"ds_write_addtid_b32 v201 offset:12448\n\t"
"ds_write_addtid_b32 v202 offset:9344\n\t"
"v_mfma_f32_16x16x32_bf16 v[112:115], a[36:39], a[160:163], v[112:115]\n\t"
"s_waitcnt vmcnt(23)\n\t"
"buffer_load_dwordx4 a[128:131], off, s[92:95], s78 offset:2048 sc1\n\t"
"v_mfma_f32_16x16x32_bf16 v[128:131], a[40:43], a[144:147], v[128:131]\n\t"
"ds_read_b128 a[172:175], v224 offset:20384\n\t"
"v_mfma_f32_16x16x32_bf16 v[128:131], a[44:47], a[160:163], v[128:131]\n\t"
"s_waitcnt lgkmcnt(15)\n\t"
"buffer_load_dwordx4 a[132:135], off, s[92:95], s78 offset:3072 sc1\n\t"
"v_mfma_f32_16x16x32_bf16 v[132:135], a[40:43], a[148:151], v[132:135]\n\t"
"ds_read_b128 a[156:159], v224 offset:18336\n\t"
"v_mfma_f32_16x16x32_bf16 v[132:135], a[44:47], a[164:167], v[132:135]\n\t"
"buffer_load_dwordx4 a[136:139], off, s[96:99], s78 offset:0 sc1\n\t"
"v_mfma_f32_16x16x32_bf16 v[116:119], a[32:35], a[148:151], v[116:119]\n\t"
"s_mov_b32 s53, 90112\n\t"
"ds_read_b64_tr_b16 a[196:197], v251 offset:20992\n\t"
"ds_read_b64_tr_b16 a[204:205], v250 offset:20992\n\t"
"v_mfma_f32_16x16x32_bf16 v[116:119], a[36:39], a[164:167], v[116:119]\n\t"
"buffer_load_dwordx4 a[140:143], off, s[96:99], s78 offset:1024 sc1\n\t"
"v_mfma_f32_16x16x32_bf16 v[100:103], a[24:27], a[148:151], v[100:103]\n\t"
"s_mov_b32 s51, 73728\n\t"
"v_mfma_f32_16x16x32_bf16 v[100:103], a[28:31], a[164:167], v[100:103]\n\t"
"s_waitcnt lgkmcnt(2)\n\t"
"s_add_u32 s71, s71, 1\n\t"
"v_mov_b32 v215, s71\n\t"
"s_mov_b64 exec, 1\n\t"
"buffer_store_dword v215, off, s[8:11], s70 sc1\n\t"
"s_mov_b64 exec, -1\n\t"
"s_barrier\n\t"
"v_mfma_f32_16x16x32_bf16 v[104:107], a[24:27], a[152:155], v[104:107]\n\t"
"ds_read_b128 a[148:151], v224 offset:256\n\t"
"v_accvgpr_read_b32 v248, a208\n\t"
"v_mfma_f32_16x16x32_bf16 v[104:107], a[28:31], a[168:171], v[104:107]\n\t"
"ds_read_b128 a[144:147], v224 offset:0\n\t"
"v_mfma_f32_16x16x32_bf16 v[120:123], a[32:35], a[152:155], v[120:123]\n\t"
"ds_read_b128 a[160:163], v224 offset:2048\n\t"
"v_mfma_f32_16x16x32_bf16 v[120:123], a[36:39], a[168:171], v[120:123]\n\t"
"ds_read_b128 a[164:167], v224 offset:2304\n\t"
"v_mfma_f32_16x16x32_bf16 v[136:139], a[40:43], a[152:155], v[136:139]\n\t"
"v_mfma_f32_16x16x32_bf16 v[136:139], a[44:47], a[168:171], v[136:139]\n\t"
"buffer_load_dwordx4 a[248:251], v247, s[12:15], 0 offen offset:256 nt\n\t"
"v_mfma_f32_16x16x32_bf16 v[140:143], a[40:43], a[156:159], v[140:143]\n\t"
"v_mfma_f32_16x16x32_bf16 v[140:143], a[44:47], a[172:175], v[140:143]\n\t"
"buffer_load_dwordx4 a[0:3], off, s[96:99], s58 offset:0 sc1\n\t"
"v_mfma_f32_16x16x32_bf16 v[124:127], a[32:35], a[156:159], v[124:127]\n\t"
"ds_read_b128 a[152:155], v224 offset:512\n\t"
"v_mfma_f32_16x16x32_bf16 v[124:127], a[36:39], a[172:175], v[124:127]\n\t"
"v_mfma_f32_16x16x32_bf16 v[108:111], a[24:27], a[156:159], v[108:111]\n\t"
"v_add_u32 v248, 101632, v248\n\t"
"v_mfma_f32_16x16x32_bf16 v[108:111], a[28:31], a[172:175], v[108:111]\n\t"
"s_waitcnt lgkmcnt(5)\n\t"
"buffer_load_dwordx4 a[4:7], off, s[96:99], s58 offset:1024 sc1\n\t"
"v_mfma_f32_4x4x4_16b_bf16 v[200:203], a[252:253], a[178:179], 0\n\t"
"ds_read_b128 a[168:171], v224 offset:2560\n\t"
"v_mfma_f32_4x4x4_16b_bf16 v[204:207], a[252:253], a[186:187], 0\n\t"
"v_mfma_f32_4x4x4_16b_bf16 v[216:219], a[252:253], a[194:195], 0\n\t"
"v_xor_b32 v249, 32, v248\n\t"
"v_mfma_f32_4x4x4_16b_bf16 v[220:223], a[252:253], a[202:203], 0\n\t"
".p2align 3\n\t"
"buffer_load_dwordx4 a[8:11], off, s[96:99], s58 offset:2048 sc1\n\t"
"v_pk_add_f32 v[96:97], v[0:1], v[96:97] neg_lo:[0,1] neg_hi:[0,1]\n\t"
"v_pk_add_f32 v[98:99], v[2:3], v[98:99] neg_lo:[0,1] neg_hi:[0,1]\n\t"
"v_pk_fma_f32 v[0:1], v[96:97], s[90:91], v[0:1] neg_lo:[1,0,0] neg_hi:[1,0,0]\n\t"
"v_pk_fma_f32 v[2:3], v[98:99], s[90:91], v[2:3] neg_lo:[1,0,0] neg_hi:[1,0,0]\n\t"
"v_pk_add_f32 v[112:113], v[16:17], v[112:113] neg_lo:[0,1] neg_hi:[0,1]\n\t"
"v_pk_add_f32 v[114:115], v[18:19], v[114:115] neg_lo:[0,1] neg_hi:[0,1]\n\t"
"v_pk_fma_f32 v[16:17], v[112:113], s[90:91], v[16:17] neg_lo:[1,0,0] neg_hi:[1,0,0]\n\t"
"v_pk_fma_f32 v[18:19], v[114:115], s[90:91], v[18:19] neg_lo:[1,0,0] neg_hi:[1,0,0]\n\t"
"v_pk_add_f32 v[128:129], v[32:33], v[128:129] neg_lo:[0,1] neg_hi:[0,1]\n\t"
"v_pk_add_f32 v[130:131], v[34:35], v[130:131] neg_lo:[0,1] neg_hi:[0,1]\n\t"
"v_pk_fma_f32 v[32:33], v[128:129], s[90:91], v[32:33] neg_lo:[1,0,0] neg_hi:[1,0,0]\n\t"
"v_pk_fma_f32 v[34:35], v[130:131], s[90:91], v[34:35] neg_lo:[1,0,0] neg_hi:[1,0,0]\n\t"
"v_pk_add_f32 v[132:133], v[36:37], v[132:133] neg_lo:[0,1] neg_hi:[0,1]\n\t"
"v_pk_add_f32 v[134:135], v[38:39], v[134:135] neg_lo:[0,1] neg_hi:[0,1]\n\t"
"v_pk_fma_f32 v[36:37], v[132:133], s[90:91], v[36:37] neg_lo:[1,0,0] neg_hi:[1,0,0]\n\t"
"v_pk_fma_f32 v[38:39], v[134:135], s[90:91], v[38:39] neg_lo:[1,0,0] neg_hi:[1,0,0]\n\t"
"v_pk_add_f32 v[116:117], v[20:21], v[116:117] neg_lo:[0,1] neg_hi:[0,1]\n\t"
"v_pk_add_f32 v[118:119], v[22:23], v[118:119] neg_lo:[0,1] neg_hi:[0,1]\n\t"
"v_pk_fma_f32 v[20:21], v[116:117], s[90:91], v[20:21] neg_lo:[1,0,0] neg_hi:[1,0,0]\n\t"
"v_pk_fma_f32 v[22:23], v[118:119], s[90:91], v[22:23] neg_lo:[1,0,0] neg_hi:[1,0,0]\n\t"
"v_pk_add_f32 v[100:101], v[4:5], v[100:101] neg_lo:[0,1] neg_hi:[0,1]\n\t"
"v_pk_add_f32 v[102:103], v[6:7], v[102:103] neg_lo:[0,1] neg_hi:[0,1]\n\t"
"v_pk_fma_f32 v[4:5], v[100:101], s[90:91], v[4:5] neg_lo:[1,0,0] neg_hi:[1,0,0]\n\t"
"v_pk_fma_f32 v[6:7], v[102:103], s[90:91], v[6:7] neg_lo:[1,0,0] neg_hi:[1,0,0]\n\t"
"v_pk_add_f32 v[104:105], v[8:9], v[104:105] neg_lo:[0,1] neg_hi:[0,1]\n\t"
"v_pk_add_f32 v[106:107], v[10:11], v[106:107] neg_lo:[0,1] neg_hi:[0,1]\n\t"
"v_pk_fma_f32 v[8:9], v[104:105], s[90:91], v[8:9] neg_lo:[1,0,0] neg_hi:[1,0,0]\n\t"
"v_pk_fma_f32 v[10:11], v[106:107], s[90:91], v[10:11] neg_lo:[1,0,0] neg_hi:[1,0,0]\n\t"
"v_pk_add_f32 v[120:121], v[24:25], v[120:121] neg_lo:[0,1] neg_hi:[0,1]\n\t"
"v_pk_add_f32 v[122:123], v[26:27], v[122:123] neg_lo:[0,1] neg_hi:[0,1]\n\t"
"v_pk_fma_f32 v[24:25], v[120:121], s[90:91], v[24:25] neg_lo:[1,0,0] neg_hi:[1,0,0]\n\t"
"v_pk_fma_f32 v[26:27], v[122:123], s[90:91], v[26:27] neg_lo:[1,0,0] neg_hi:[1,0,0]\n\t"
"v_pk_add_f32 v[136:137], v[40:41], v[136:137] neg_lo:[0,1] neg_hi:[0,1]\n\t"
"v_pk_add_f32 v[138:139], v[42:43], v[138:139] neg_lo:[0,1] neg_hi:[0,1]\n\t"
"v_pk_fma_f32 v[40:41], v[136:137], s[90:91], v[40:41] neg_lo:[1,0,0] neg_hi:[1,0,0]\n\t"
"v_pk_fma_f32 v[42:43], v[138:139], s[90:91], v[42:43] neg_lo:[1,0,0] neg_hi:[1,0,0]\n\t"
"v_pk_add_f32 v[140:141], v[44:45], v[140:141] neg_lo:[0,1] neg_hi:[0,1]\n\t"
"v_pk_add_f32 v[142:143], v[46:47], v[142:143] neg_lo:[0,1] neg_hi:[0,1]\n\t"
"v_pk_fma_f32 v[44:45], v[140:141], s[90:91], v[44:45] neg_lo:[1,0,0] neg_hi:[1,0,0]\n\t"
"v_pk_fma_f32 v[46:47], v[142:143], s[90:91], v[46:47] neg_lo:[1,0,0] neg_hi:[1,0,0]\n\t"
"v_pk_add_f32 v[124:125], v[28:29], v[124:125] neg_lo:[0,1] neg_hi:[0,1]\n\t"
"v_pk_add_f32 v[126:127], v[30:31], v[126:127] neg_lo:[0,1] neg_hi:[0,1]\n\t"
"v_pk_fma_f32 v[28:29], v[124:125], s[90:91], v[28:29] neg_lo:[1,0,0] neg_hi:[1,0,0]\n\t"
"v_pk_fma_f32 v[30:31], v[126:127], s[90:91], v[30:31] neg_lo:[1,0,0] neg_hi:[1,0,0]\n\t"
"v_pk_add_f32 v[108:109], v[12:13], v[108:109] neg_lo:[0,1] neg_hi:[0,1]\n\t"
"v_pk_add_f32 v[110:111], v[14:15], v[110:111] neg_lo:[0,1] neg_hi:[0,1]\n\t"
"v_pk_fma_f32 v[12:13], v[108:109], s[90:91], v[12:13] neg_lo:[1,0,0] neg_hi:[1,0,0]\n\t"
"v_pk_fma_f32 v[14:15], v[110:111], s[90:91], v[14:15] neg_lo:[1,0,0] neg_hi:[1,0,0]\n\t"
"v_mfma_f32_4x4x4_16b_bf16 v[192:195], a[254:255], a[176:177], v[200:203]\n\t"
"ds_read_b128 a[172:175], v224 offset:2816\n\t"
"v_mfma_f32_4x4x4_16b_bf16 v[196:199], a[254:255], a[184:185], v[204:207]\n\t"
"buffer_load_dwordx4 a[12:15], off, s[96:99], s58 offset:3072 sc1\n\t"
"v_mfma_f32_4x4x4_16b_bf16 v[200:203], a[254:255], a[180:181], v[200:203]\n\t"
"v_mfma_f32_4x4x4_16b_bf16 v[204:207], a[254:255], a[188:189], v[204:207]\n\t"
"v_mfma_f32_4x4x4_16b_bf16 v[208:211], a[254:255], a[192:193], v[216:219]\n\t"
"ds_read_b128 a[156:159], v224 offset:768\n\t"
"v_mfma_f32_4x4x4_16b_bf16 v[212:215], a[254:255], a[200:201], v[220:223]\n\t"
"v_mfma_f32_4x4x4_16b_bf16 v[216:219], a[254:255], a[196:197], v[216:219]\n\t"
"v_mfma_f32_4x4x4_16b_bf16 v[220:223], a[254:255], a[204:205], v[220:223]\n\t"
"s_waitcnt vmcnt(31) lgkmcnt(5)\n\t"
"buffer_load_dwordx4 a[16:19], off, s[92:95], s59 offset:0 sc1\n\t"
"v_mfma_f32_16x16x32_bf16 v[0:3], a[48:51], a[144:147], v[0:3]\n\t"
"v_cvt_pk_bf16_f32 v192, -v192, -v196\n\t"
"v_cvt_pk_bf16_f32 v193, -v193, -v197\n\t"
"v_mfma_f32_16x16x32_bf16 v[0:3], a[52:55], a[160:163], v[0:3]\n\t"
"s_waitcnt vmcnt(30)\n\t"
"ds_write_addtid_b32 v193 offset:20672\n\t"
"v_cvt_pk_bf16_f32 v195, -v195, -v199\n\t"
"v_mfma_f32_16x16x32_bf16 v[16:19], a[56:59], a[144:147], v[16:19]\n\t"
"ds_write_addtid_b32 v195 offset:21696\n\t"
"v_cvt_pk_bf16_f32 v203, v203, v207\n\t"
"v_mfma_f32_16x16x32_bf16 v[16:19], a[60:63], a[160:163], v[16:19]\n\t"
"s_waitcnt vmcnt(28)\n\t"
"buffer_load_dwordx4 a[20:23], off, s[92:95], s59 offset:1024 sc1\n\t"
"v_mfma_f32_16x16x32_bf16 v[32:35], a[64:67], a[144:147], v[32:35]\n\t"
"ds_read_b128 a[144:147], v224 offset:1024\n\t"
"v_xor_b32 v250, 64, v248\n\t"
"v_mfma_f32_16x16x32_bf16 v[32:35], a[68:71], a[160:163], v[32:35]\n\t"
"s_waitcnt lgkmcnt(7)\n\t"
"buffer_load_dwordx4 a[24:27], off, s[96:99], s57 offset:2048 sc1\n\t"
"v_mfma_f32_16x16x32_bf16 v[36:39], a[64:67], a[148:151], v[36:39]\n\t"
"ds_read_b128 a[160:163], v224 offset:3072\n\t"
"v_xor_b32 v251, 96, v248\n\t"
"v_mfma_f32_16x16x32_bf16 v[36:39], a[68:71], a[164:167], v[36:39]\n\t"
"buffer_load_dwordx4 a[28:31], off, s[96:99], s57 offset:3072 sc1\n\t"
"v_mfma_f32_16x16x32_bf16 v[20:23], a[56:59], a[148:151], v[20:23]\n\t"
"ds_write_addtid_b32 v203 offset:30016\n\t"
"v_cvt_pk_bf16_f32 v219, v219, v223\n\t"
"v_mfma_f32_16x16x32_bf16 v[20:23], a[60:63], a[164:167], v[20:23]\n\t"
"v_cvt_pk_bf16_f32 v194, -v194, -v198\n\t"
"v_cvt_pk_bf16_f32 v217, v217, v221\n\t"
"v_mfma_f32_16x16x32_bf16 v[4:7], a[48:51], a[148:151], v[4:7]\n\t"
"ds_read_b128 a[148:151], v224 offset:1280\n\t"
"v_cvt_pk_bf16_f32 v201, v201, v205\n\t"
"v_mfma_f32_16x16x32_bf16 v[4:7], a[52:55], a[164:167], v[4:7]\n\t"
"s_waitcnt lgkmcnt(8)\n\t"
"buffer_load_dwordx4 a[32:35], off, s[92:95], s58 offset:0 sc1\n\t"
"v_mfma_f32_16x16x32_bf16 v[8:11], a[48:51], a[152:155], v[8:11]\n\t"
"ds_read_b128 a[164:167], v224 offset:3328\n\t"
"v_cvt_pk_bf16_f32 v211, -v211, -v215\n\t"
"v_mfma_f32_16x16x32_bf16 v[8:11], a[52:55], a[168:171], v[8:11]\n\t"
"s_mov_b32 s64, 155648\n\t"
"ds_write_addtid_b32 v201 offset:28992\n\t"
"v_cvt_pk_bf16_f32 v200, v200, v204\n\t"
"v_mfma_f32_16x16x32_bf16 v[24:27], a[56:59], a[152:155], v[24:27]\n\t"
"ds_write_addtid_b32 v194 offset:17568\n\t"
"v_cvt_pk_bf16_f32 v210, -v210, -v214\n\t"
"v_mfma_f32_16x16x32_bf16 v[24:27], a[60:63], a[168:171], v[24:27]\n\t"
"buffer_load_dwordx4 a[36:39], off, s[92:95], s58 offset:1024 sc1\n\t"
"v_mfma_f32_16x16x32_bf16 v[40:43], a[64:67], a[152:155], v[40:43]\n\t"
"ds_read_b128 a[152:155], v224 offset:1536\n\t"
"v_cvt_pk_bf16_f32 v208, -v208, -v212\n\t"
"v_mfma_f32_16x16x32_bf16 v[40:43], a[68:71], a[168:171], v[40:43]\n\t"
"s_waitcnt lgkmcnt(10)\n\t"
"ds_read_b128 a[168:171], v224 offset:3584\n\t"
"v_cvt_pk_bf16_f32 v209, -v209, -v213\n\t"
"v_mfma_f32_16x16x32_bf16 v[44:47], a[64:67], a[156:159], v[44:47]\n\t"
"ds_write_addtid_b32 v217 offset:31040\n\t"
"v_cvt_pk_bf16_f32 v202, v202, v206\n\t"
"v_mfma_f32_16x16x32_bf16 v[44:47], a[68:71], a[172:175], v[44:47]\n\t"
"buffer_load_dwordx4 a[40:43], off, s[92:95], s58 offset:2048 sc1\n\t"
"v_mfma_f32_16x16x32_bf16 v[28:31], a[56:59], a[156:159], v[28:31]\n\t"
"ds_write_addtid_b32 v219 offset:32064\n\t"
"v_cvt_pk_bf16_f32 v216, v216, v220\n\t"
"v_mfma_f32_16x16x32_bf16 v[28:31], a[60:63], a[172:175], v[28:31]\n\t"
"s_mov_b32 s68, 188416\n\t"
"ds_write_addtid_b32 v209 offset:22720\n\t"
"ds_write_addtid_b32 v211 offset:23744\n\t"
"v_mfma_f32_16x16x32_bf16 v[12:15], a[48:51], a[156:159], v[12:15]\n\t"
"s_mov_b32 s67, 180224\n\t"
"ds_write_addtid_b32 v192 offset:16544\n\t"
"ds_write_addtid_b32 v210 offset:19616\n\t"
"v_mfma_f32_16x16x32_bf16 v[12:15], a[52:55], a[172:175], v[12:15]\n\t"
"s_waitcnt vmcnt(32) lgkmcnt(13)\n\t"
"buffer_load_dwordx4 a[44:47], off, s[92:95], s58 offset:3072 sc1\n\t"
"v_mfma_f32_16x16x32_bf16 v[96:99], a[72:75], a[144:147], v[96:99]\n\t"
"ds_write_addtid_b32 v200 offset:24864\n\t"
"v_cvt_pk_bf16_f32 v218, v218, v222\n\t"
"v_mfma_f32_16x16x32_bf16 v[96:99], a[76:79], a[160:163], v[96:99]\n\t"
"s_waitcnt vmcnt(31)\n\t"
"ds_write_addtid_b32 v202 offset:25888\n\t"
"ds_write_addtid_b32 v218 offset:27936\n\t"
"s_mov_b32 s74, 212992\n\t"
"v_mfma_f32_16x16x32_bf16 v[112:115], a[80:83], a[144:147], v[112:115]\n\t"
"s_mov_b32 s65, 163840\n\t"
"ds_write_addtid_b32 v208 offset:18592\n\t"
"ds_write_addtid_b32 v216 offset:26912\n\t"
"v_mfma_f32_16x16x32_bf16 v[112:115], a[84:87], a[160:163], v[112:115]\n\t"
"s_waitcnt vmcnt(29)\n\t"
"v_mfma_f32_16x16x32_bf16 v[128:131], a[88:91], a[144:147], v[128:131]\n\t"
"s_add_u32 m0, s43, 33280\n\t"
"v_mfma_f32_16x16x32_bf16 v[128:131], a[92:95], a[160:163], v[128:131]\n\t"
"s_waitcnt lgkmcnt(15)\n\t"
"s_mov_b64 exec, s[44:45]\n\t"
"buffer_load_dwordx4 v247, s[12:15], 0 offen offset:0 nt lds\n\t"
"s_mov_b64 exec, -1\n\t"
"v_mfma_f32_16x16x32_bf16 v[132:135], a[88:91], a[148:151], v[132:135]\n\t"
"s_add_u32 m0, s26, 35584\n\t"
"ds_read_b128 a[156:159], v224 offset:1792\n\t"
"ds_read_b128 a[172:175], v224 offset:3840\n\t"
"s_mov_b32 s72, 204800\n\t"
"v_mfma_f32_16x16x32_bf16 v[132:135], a[92:95], a[164:167], v[132:135]\n\t"
"v_mfma_f32_16x16x32_bf16 v[116:119], a[80:83], a[148:151], v[116:119]\n\t"
"v_mfma_f32_16x16x32_bf16 v[116:119], a[84:87], a[164:167], v[116:119]\n\t"
"buffer_load_dwordx4 v236, s[12:15], s22 offen offset:0 nt lds\n\t"
"v_mfma_f32_16x16x32_bf16 v[100:103], a[72:75], a[148:151], v[100:103]\n\t"
"s_lshl_b32 m0, s73, 8\n\t"
"v_mfma_f32_16x16x32_bf16 v[100:103], a[76:79], a[164:167], v[100:103]\n\t"
"s_waitcnt vmcnt(28)\n\t"
"s_waitcnt lgkmcnt(0)\n\t"
"s_barrier\n\t"
"v_mfma_f32_16x16x32_bf16 v[104:107], a[72:75], a[152:155], v[104:107]\n\t"
"s_add_u32 m0, s26, 35584\n\t"
"ds_read_b128 a[144:147], v224 offset:16544\n\t"
"ds_read_b128 a[164:167], v224 offset:18848\n\t"
"v_mfma_f32_16x16x32_bf16 v[104:107], a[76:79], a[168:171], v[104:107]\n\t"
"ds_read_b128 a[160:163], v224 offset:18592\n\t"
"v_mfma_f32_16x16x32_bf16 v[120:123], a[80:83], a[152:155], v[120:123]\n\t"
"ds_read_b128 a[148:151], v224 offset:16800\n\t"
"v_mfma_f32_16x16x32_bf16 v[120:123], a[84:87], a[168:171], v[120:123]\n\t"
"ds_read_b64_tr_b16 a[176:177], v248 offset:20736\n\t"
"v_mfma_f32_16x16x32_bf16 v[136:139], a[88:91], a[152:155], v[136:139]\n\t"
"ds_read_b64_tr_b16 a[178:179], v249 offset:20736\n\t"
"v_mfma_f32_16x16x32_bf16 v[136:139], a[92:95], a[168:171], v[136:139]\n\t"
"s_nop 0\n\t"
"ds_read_b64_tr_b16 a[180:181], v248 offset:21248\n\t"
"ds_read_b64_tr_b16 a[182:183], v249 offset:21248\n\t"
"v_mfma_f32_16x16x32_bf16 v[140:143], a[88:91], a[156:159], v[140:143]\n\t"
"ds_read_b64_tr_b16 a[184:185], v250 offset:23040\n\t"
"v_mfma_f32_16x16x32_bf16 v[140:143], a[92:95], a[172:175], v[140:143]\n\t"
"buffer_load_dwordx4 v237, s[12:15], s22 offen offset:1024 nt lds\n\t"
"v_mfma_f32_16x16x32_bf16 v[124:127], a[80:83], a[156:159], v[124:127]\n\t"
"ds_read_b128 a[152:155], v224 offset:17056\n\t"
"v_mfma_f32_16x16x32_bf16 v[124:127], a[84:87], a[172:175], v[124:127]\n\t"
"s_waitcnt lgkmcnt(2)\n\t"
"ds_read_b128 a[168:171], v224 offset:19104\n\t"
"v_mfma_f32_4x4x4_16b_bf16 v[192:195], a[210:211], a[176:177], 0\n\t"
"ds_read_b64_tr_b16 a[186:187], v251 offset:23040\n\t"
"v_mfma_f32_4x4x4_16b_bf16 v[196:199], a[210:211], a[178:179], 0\n\t"
"ds_read_b64_tr_b16 a[188:189], v250 offset:23552\n\t"
"v_mfma_f32_4x4x4_16b_bf16 v[200:203], a[210:211], a[180:181], 0\n\t"
"ds_read_b64_tr_b16 a[190:191], v251 offset:23552\n\t"
"v_mfma_f32_4x4x4_16b_bf16 v[204:207], a[210:211], a[182:183], 0\n\t"
"ds_read_b64_tr_b16 a[192:193], v250 offset:20736\n\t"
"v_mfma_f32_16x16x32_bf16 v[108:111], a[72:75], a[156:159], v[108:111]\n\t"
"s_mov_b32 s76, 229376\n\t"
"ds_read_b64_tr_b16 a[194:195], v251 offset:20736\n\t"
"ds_read_b64_tr_b16 a[196:197], v250 offset:21248\n\t"
"v_mfma_f32_16x16x32_bf16 v[108:111], a[76:79], a[172:175], v[108:111]\n\t"
"s_waitcnt vmcnt(27)\n\t"
"buffer_load_dwordx4 v238, s[12:15], s22 offen offset:2048 nt lds\n\t"
"v_mfma_f32_16x16x32_bf16 v[0:3], a[96:99], a[144:147], v[0:3]\n\t"
"ds_read_b128 a[156:159], v224 offset:17312\n\t"
"v_cvt_pk_bf16_f32 v196, v192, v196\n\t"
"v_mfma_f32_16x16x32_bf16 v[0:3], a[100:103], a[160:163], v[0:3]\n\t"
"s_waitcnt vmcnt(26)\n\t"
"ds_read_b64_tr_b16 a[198:199], v251 offset:21248\n\t"
"v_cvt_pk_bf16_f32 v194, v194, v198\n\t"
"v_mfma_f32_16x16x32_bf16 v[16:19], a[104:107], a[144:147], v[16:19]\n\t"
"ds_read_b64_tr_b16 a[200:201], v248 offset:23040\n\t"
"v_cvt_pk_bf16_f32 v195, v195, v199\n\t"
"v_mfma_f32_16x16x32_bf16 v[16:19], a[108:111], a[160:163], v[16:19]\n\t"
"s_waitcnt vmcnt(24)\n\t"
"ds_read_b64_tr_b16 a[202:203], v249 offset:23040\n\t"
"v_cvt_pk_bf16_f32 v192, v202, v206\n\t"
"v_mfma_f32_16x16x32_bf16 v[32:35], a[112:115], a[144:147], v[32:35]\n\t"
"ds_read_b64_tr_b16 a[204:205], v248 offset:23552\n\t"
"v_cvt_pk_bf16_f32 v199, v201, v205\n\t"
"v_mfma_f32_16x16x32_bf16 v[32:35], a[116:119], a[160:163], v[32:35]\n\t"
"s_waitcnt lgkmcnt(8)\n\t"
"ds_read_b128 a[144:147], v224 offset:17568\n\t"
"v_cvt_pk_bf16_f32 v197, v193, v197\n\t"
"v_mfma_f32_4x4x4_16b_bf16 v[208:211], a[210:211], a[184:185], 0\n\t"
"buffer_load_dwordx4 v239, s[12:15], s22 offen offset:3072 nt lds\n\t"
"v_mfma_f32_4x4x4_16b_bf16 v[212:215], a[210:211], a[186:187], 0\n\t"
"ds_read_b128 a[172:175], v224 offset:19360\n\t"
"v_mfma_f32_4x4x4_16b_bf16 v[216:219], a[210:211], a[188:189], 0\n\t"
"s_lshl_b32 m0, s73, 8\n\t"
"ds_read_b64_tr_b16 a[206:207], v249 offset:23552\n\t"
"v_mfma_f32_4x4x4_16b_bf16 v[220:223], a[210:211], a[190:191], 0\n\t"
"ds_write_addtid_b32 v199 offset:9344\n\t"
"v_cvt_pk_bf16_f32 v193, v203, v207\n\t"
"v_mfma_f32_16x16x32_bf16 v[36:39], a[112:115], a[148:151], v[36:39]\n\t"
"ds_write_addtid_b32 v196 offset:0\n\t"
"v_cvt_pk_bf16_f32 v198, v200, v204\n\t"
"v_mfma_f32_16x16x32_bf16 v[36:39], a[116:119], a[164:167], v[36:39]\n\t"
"s_nop 0\n\t"
"ds_write_addtid_b32 v198 offset:8320\n\t"
"ds_write_addtid_b32 v197 offset:1024\n\t"
"v_mfma_f32_16x16x32_bf16 v[20:23], a[104:107], a[148:151], v[20:23]\n\t"
"s_add_u32 m0, s26, 35584\n\t"
"v_cvt_pk_bf16_f32 v213, v209, v213\n\t"
"v_cvt_pk_bf16_f32 v211, v211, v215\n\t"
"v_mfma_f32_16x16x32_bf16 v[20:23], a[108:111], a[164:167], v[20:23]\n\t"
"v_cvt_pk_bf16_f32 v212, v208, v212\n\t"
"v_cvt_pk_bf16_f32 v210, v210, v214\n\t"
"v_mfma_f32_16x16x32_bf16 v[4:7], a[96:99], a[148:151], v[4:7]\n\t"
"ds_read_b128 a[148:151], v224 offset:17824\n\t"
"v_cvt_pk_bf16_f32 v209, v219, v223\n\t"
"v_mfma_f32_16x16x32_bf16 v[4:7], a[100:103], a[164:167], v[4:7]\n\t"
"s_waitcnt lgkmcnt(11)\n\t"
"s_mov_b64 exec, s[36:37]\n\t"
"buffer_load_dwordx4 v240, s[12:15], s22 offen offset:3584 nt lds\n\t"
"s_mov_b64 exec, -1\n\t"
"v_mfma_f32_4x4x4_16b_bf16 v[200:203], a[210:211], a[192:193], 0\n\t"
"ds_read_b128 a[160:163], v224 offset:19616\n\t"
"v_mfma_f32_4x4x4_16b_bf16 v[204:207], a[210:211], a[194:195], 0\n\t"
"v_cvt_pk_bf16_f32 v208, v218, v222\n\t"
"v_cvt_pk_bf16_f32 v215, v217, v221\n\t"
"v_mfma_f32_4x4x4_16b_bf16 v[196:199], a[210:211], a[196:197], 0\n\t"
"s_lshl_b32 m0, s73, 8\n\t"
"v_cvt_pk_bf16_f32 v214, v216, v220\n\t"
"v_mfma_f32_4x4x4_16b_bf16 v[216:219], a[210:211], a[198:199], 0\n\t"
"s_mov_b32 s80, 262144\n\t"
"ds_write_addtid_b32 v212 offset:4128\n\t"
"ds_write_addtid_b32 v213 offset:5152\n\t"
"v_mfma_f32_16x16x32_bf16 v[8:11], a[96:99], a[152:155], v[8:11]\n\t"
"s_nop 0\n\t"
"ds_write_addtid_b32 v214 offset:12448\n\t"
"ds_write_addtid_b32 v215 offset:13472\n\t"
"v_mfma_f32_16x16x32_bf16 v[8:11], a[100:103], a[168:171], v[8:11]\n\t"
"buffer_load_dwordx4 a[48:51], off, s[96:99], s64 offset:0 sc1\n\t"
"v_mfma_f32_16x16x32_bf16 v[24:27], a[104:107], a[152:155], v[24:27]\n\t"
"v_cvt_pk_bf16_f32 v204, v200, v204\n\t"
"v_cvt_pk_bf16_f32 v203, v203, v207\n\t"
"v_mfma_f32_16x16x32_bf16 v[24:27], a[108:111], a[168:171], v[24:27]\n\t"
"buffer_load_dwordx4 a[52:55], off, s[96:99], s64 offset:1024 sc1\n\t"
"v_mfma_f32_16x16x32_bf16 v[40:43], a[112:115], a[152:155], v[40:43]\n\t"
"ds_read_b128 a[164:167], v224 offset:19872\n\t"
"v_cvt_pk_bf16_f32 v202, v202, v206\n\t"
"v_mfma_f32_16x16x32_bf16 v[40:43], a[116:119], a[168:171], v[40:43]\n\t"
"s_waitcnt lgkmcnt(12)\n\t"
"v_cvt_pk_bf16_f32 v200, v198, v218\n\t"
"v_cvt_pk_bf16_f32 v206, v196, v216\n\t"
"v_mfma_f32_16x16x32_bf16 v[44:47], a[112:115], a[156:159], v[44:47]\n\t"
"v_cvt_pk_bf16_f32 v205, v201, v205\n\t"
"v_cvt_pk_bf16_f32 v201, v199, v219\n\t"
"v_mfma_f32_16x16x32_bf16 v[44:47], a[116:119], a[172:175], v[44:47]\n\t"
"s_waitcnt lgkmcnt(11)\n\t"
"buffer_load_dwordx4 a[56:59], off, s[96:99], s64 offset:2048 sc1\n\t"
"v_mfma_f32_4x4x4_16b_bf16 v[220:223], a[210:211], a[200:201], 0\n\t"
"ds_write_addtid_b32 v205 offset:3072\n\t"
"v_cvt_pk_bf16_f32 v207, v197, v217\n\t"
"v_mfma_f32_4x4x4_16b_bf16 v[212:215], a[210:211], a[202:203], 0\n\t"
"buffer_load_dwordx4 a[60:63], off, s[96:99], s64 offset:3072 sc1\n\t"
"v_mfma_f32_4x4x4_16b_bf16 v[196:199], a[210:211], a[204:205], 0\n\t"
"ds_write_addtid_b32 v204 offset:2048\n\t"
"v_mfma_f32_4x4x4_16b_bf16 v[216:219], a[210:211], a[206:207], 0\n\t"
"buffer_load_dwordx4 a[64:67], off, s[92:95], s65 offset:0 sc1\n\t"
"v_mfma_f32_16x16x32_bf16 v[28:31], a[104:107], a[156:159], v[28:31]\n\t"
"s_mov_b32 s79, 253952\n\t"
"ds_write_addtid_b32 v206 offset:10368\n\t"
"ds_write_addtid_b32 v207 offset:11392\n\t"
"v_mfma_f32_16x16x32_bf16 v[28:31], a[108:111], a[172:175], v[28:31]\n\t"
"buffer_load_dwordx4 a[68:71], off, s[92:95], s65 offset:1024 sc1\n\t"
"v_mfma_f32_16x16x32_bf16 v[12:15], a[96:99], a[156:159], v[12:15]\n\t"
"v_cvt_pk_bf16_f32 v213, v221, v213\n\t"
"v_cvt_pk_bf16_f32 v223, v223, v215\n\t"
"v_mfma_f32_16x16x32_bf16 v[12:15], a[100:103], a[172:175], v[12:15]\n\t"
"s_waitcnt vmcnt(30) lgkmcnt(9)\n\t"
"v_cvt_pk_bf16_f32 v212, v220, v212\n\t"
"v_cvt_pk_bf16_f32 v222, v222, v214\n\t"
"v_mfma_f32_16x16x32_bf16 v[96:99], a[120:123], a[144:147], v[96:99]\n\t"
"ds_write_addtid_b32 v212 offset:6176\n\t"
"v_cvt_pk_bf16_f32 v214, v196, v216\n\t"
"v_mfma_f32_16x16x32_bf16 v[96:99], a[124:127], a[160:163], v[96:99]\n\t"
"s_waitcnt vmcnt(28)\n\t"
"ds_write_addtid_b32 v213 offset:7200\n\t"
"buffer_load_dwordx4 a[72:75], off, s[96:99], s63 offset:2048 sc1\n\t"
"v_cvt_pk_bf16_f32 v215, v197, v217\n\t"
"v_mfma_f32_16x16x32_bf16 v[112:115], a[128:131], a[144:147], v[112:115]\n\t"
"s_mov_b32 s82, 278528\n\t"
"ds_write_addtid_b32 v214 offset:14496\n\t"
"ds_write_addtid_b32 v215 offset:15520\n\t"
"v_mfma_f32_16x16x32_bf16 v[112:115], a[132:135], a[160:163], v[112:115]\n\t"
"s_waitcnt vmcnt(27)\n\t"
"buffer_load_dwordx4 a[76:79], off, s[96:99], s63 offset:3072 sc1\n\t"
"buffer_load_dwordx4 a[80:83], off, s[92:95], s64 offset:0 sc1\n\t"
"v_mfma_f32_16x16x32_bf16 v[128:131], a[136:139], a[144:147], v[128:131]\n\t"
"s_mov_b32 s83, 286720\n\t"
"ds_read_b128 a[152:155], v224 offset:18080\n\t"
"ds_read_b128 a[172:175], v224 offset:20384\n\t"
"v_mfma_f32_16x16x32_bf16 v[128:131], a[140:143], a[160:163], v[128:131]\n\t"
"s_waitcnt lgkmcnt(10)\n\t"
"buffer_load_dwordx4 a[84:87], off, s[92:95], s64 offset:1024 sc1\n\t"
"v_mfma_f32_16x16x32_bf16 v[132:135], a[136:139], a[148:151], v[132:135]\n\t"
"s_mov_b32 s77, 237568\n\t"
"ds_read_b128 a[168:171], v224 offset:20128\n\t"
"ds_read_b128 a[156:159], v224 offset:18336\n\t"
"v_mfma_f32_16x16x32_bf16 v[132:135], a[140:143], a[164:167], v[132:135]\n\t"
"buffer_load_dwordx4 a[88:91], off, s[92:95], s64 offset:2048 sc1\n\t"
"buffer_load_dwordx4 a[92:95], off, s[92:95], s64 offset:3072 sc1\n\t"
"v_mfma_f32_16x16x32_bf16 v[116:119], a[128:131], a[148:151], v[116:119]\n\t"
"v_accvgpr_read_b32 v224, a213\n\t"
"v_cvt_pk_bf16_f32 v221, v199, v219\n\t"
"v_mfma_f32_16x16x32_bf16 v[116:119], a[132:135], a[164:167], v[116:119]\n\t"
"buffer_load_dwordx4 a[108:111], off, s[96:99], s46 offset:3072 sc1\n\t"
"v_mfma_f32_16x16x32_bf16 v[100:103], a[120:123], a[148:151], v[100:103]\n\t"
"v_cvt_pk_bf16_f32 v220, v198, v218\n\t"
"v_mfma_f32_16x16x32_bf16 v[100:103], a[124:127], a[164:167], v[100:103]\n\t"
"buffer_load_dwordx4 a[112:115], off, s[92:95], s47 offset:0 sc1\n\t"
"buffer_load_dwordx4 a[116:119], off, s[92:95], s47 offset:1024 sc1\n\t"
"v_add_u32 v246, 8, v228\n\t"
"v_add_u32 v247, 16, v228\n\t"
"s_waitcnt lgkmcnt(0)\n\t"
"s_barrier\n\t"
"Lunit_%=:\n\t"
"v_mfma_f32_16x16x32_bf16 v[104:107], a[120:123], a[152:155], v[104:107]\n\t"
"s_nop 0\n\t"
"ds_read_b128 a[144:147], v224 offset:1024\n\t"
"ds_read_b128 a[160:163], v224 offset:3072\n\t"
"v_mfma_f32_16x16x32_bf16 v[104:107], a[124:127], a[168:171], v[104:107]\n\t"
"buffer_load_dwordx4 a[104:107], off, s[96:99], s46 offset:2048 sc1\n\t"
"v_mfma_f32_16x16x32_bf16 v[120:123], a[128:131], a[152:155], v[120:123]\n\t"
"ds_read_b128 a[148:151], v224 offset:1280\n\t"
"v_mfma_f32_16x16x32_bf16 v[120:123], a[132:135], a[168:171], v[120:123]\n\t"
"ds_read_b128 a[164:167], v224 offset:3328\n\t"
"v_mfma_f32_16x16x32_bf16 v[136:139], a[136:139], a[152:155], v[136:139]\n\t"
"ds_write_addtid_b32 v195 offset:17568\n\t"
"v_mfma_f32_16x16x32_bf16 v[136:139], a[140:143], a[168:171], v[136:139]\n\t"
"buffer_load_dwordx4 a[96:99], off, s[96:99], s46 offset:0 sc1\n\t"
"v_mfma_f32_16x16x32_bf16 v[140:143], a[136:139], a[156:159], v[140:143]\n\t"
"ds_read_b128 a[168:171], v224 offset:3584\n\t"
"v_mfma_f32_16x16x32_bf16 v[140:143], a[140:143], a[172:175], v[140:143]\n\t"
"ds_read_b128 a[152:155], v224 offset:1536\n\t"
"v_mfma_f32_16x16x32_bf16 v[124:127], a[128:131], a[156:159], v[124:127]\n\t"
"ds_write_addtid_b32 v194 offset:16544\n\t"
"v_mfma_f32_16x16x32_bf16 v[124:127], a[132:135], a[172:175], v[124:127]\n\t"
"buffer_load_dwordx4 a[100:103], off, s[96:99], s46 offset:1024 sc1\n\t"
"v_mfma_f32_16x16x32_bf16 v[108:111], a[120:123], a[156:159], v[108:111]\n\t"
"ds_read_b128 a[156:159], v224 offset:1792\n\t"
"v_mfma_f32_16x16x32_bf16 v[108:111], a[124:127], a[172:175], v[108:111]\n\t"
"s_waitcnt vmcnt(34) lgkmcnt(7)\n\t"
"ds_write_addtid_b32 v211 offset:21696\n\t"
"v_mfma_f32_16x16x32_bf16 v[48:51], a[0:3], a[144:147], v[0:3]\n\t"
"ds_read_b128 a[172:175], v224 offset:3840\n\t"
"v_mfma_f32_16x16x32_bf16 v[48:51], a[4:7], a[160:163], v[48:51]\n\t"
"s_waitcnt vmcnt(32) lgkmcnt(7)\n\t"
"buffer_load_dwordx4 a[136:139], off, s[92:95], s46 offset:2048 sc1\n\t"
"v_mfma_f32_16x16x32_bf16 v[64:67], a[8:11], a[144:147], v[16:19]\n\t"
"ds_write_addtid_b32 v203 offset:19616\n\t"
"v_mfma_f32_16x16x32_bf16 v[64:67], a[12:15], a[160:163], v[64:67]\n\t"
"s_waitcnt vmcnt(31) lgkmcnt(7)\n\t"
"ds_write_addtid_b32 v223 offset:23744\n\t"
"v_mfma_f32_16x16x32_bf16 v[80:83], a[16:19], a[144:147], v[32:35]\n\t"
"ds_read_b128 a[144:147], v224 offset:0\n\t"
"v_mfma_f32_16x16x32_bf16 v[80:83], a[20:23], a[160:163], v[80:83]\n\t"
"buffer_load_dwordx4 a[140:143], off, s[92:95], s46 offset:3072 sc1\n\t"
"v_mfma_f32_16x16x32_bf16 v[84:87], a[16:19], a[148:151], v[36:39]\n\t"
"ds_read_b128 a[160:163], v224 offset:2048\n\t"
"v_mfma_f32_16x16x32_bf16 v[84:87], a[20:23], a[164:167], v[84:87]\n\t"
"ds_write_addtid_b32 v210 offset:20672\n\t"
"v_mfma_f32_16x16x32_bf16 v[68:71], a[8:11], a[148:151], v[20:23]\n\t"
"ds_write_addtid_b32 v193 offset:25888\n\t"
"v_mfma_f32_16x16x32_bf16 v[68:71], a[12:15], a[164:167], v[68:71]\n\t"
"buffer_load_dwordx4 a[132:135], off, s[92:95], s46 offset:1024 sc1\n\t"
"v_mfma_f32_16x16x32_bf16 v[52:55], a[0:3], a[148:151], v[4:7]\n\t"
"ds_read_b128 a[148:151], v224 offset:256\n\t"
"v_mfma_f32_16x16x32_bf16 v[52:55], a[4:7], a[164:167], v[52:55]\n\t"
"s_waitcnt lgkmcnt(7)\n\t"
"ds_write_addtid_b32 v200 offset:26912\n\t"
"v_mfma_f32_16x16x32_bf16 v[56:59], a[0:3], a[152:155], v[8:11]\n\t"
"ds_read_b128 a[164:167], v224 offset:2304\n\t"
"v_mfma_f32_16x16x32_bf16 v[56:59], a[4:7], a[168:171], v[56:59]\n\t"
"buffer_load_dwordx4 a[128:131], off, s[92:95], s46 offset:0 sc1\n\t"
"v_mfma_f32_16x16x32_bf16 v[72:75], a[8:11], a[152:155], v[24:27]\n\t"
"ds_write_addtid_b32 v192 offset:24864\n\t"
"v_mfma_f32_16x16x32_bf16 v[72:75], a[12:15], a[168:171], v[72:75]\n\t"
"ds_write_addtid_b32 v221 offset:32064\n\t"
"v_mfma_f32_16x16x32_bf16 v[88:91], a[16:19], a[152:155], v[40:43]\n\t"
"ds_read_b128 a[152:155], v224 offset:512\n\t"
"v_mfma_f32_16x16x32_bf16 v[88:91], a[20:23], a[168:171], v[88:91]\n\t"
"buffer_load_dwordx4 a[120:123], off, s[96:99], s42 offset:2048 sc1\n\t"
"v_mfma_f32_16x16x32_bf16 v[92:95], a[16:19], a[156:159], v[44:47]\n\t"
"v_mfma_f32_16x16x32_bf16 v[92:95], a[20:23], a[172:175], v[92:95]\n\t"
"ds_write_b128 v254, a[248:251] offset:41472\n\t"
"v_mfma_f32_16x16x32_bf16 v[76:79], a[8:11], a[156:159], v[28:31]\n\t"
"v_mfma_f32_16x16x32_bf16 v[76:79], a[12:15], a[172:175], v[76:79]\n\t"
"buffer_load_dwordx4 a[124:127], off, s[96:99], s42 offset:3072 sc1\n\t"
"v_mfma_f32_16x16x32_bf16 v[60:63], a[0:3], a[156:159], v[12:15]\n\t"
"v_mfma_f32_16x16x32_bf16 v[60:63], a[4:7], a[172:175], v[60:63]\n\t"
"s_waitcnt vmcnt(34) lgkmcnt(4)\n\t"
"ds_write_addtid_b32 v202 offset:18592\n\t"
"ds_write_addtid_b32 v201 offset:27936\n\t"
"v_mfma_f32_16x16x32_bf16 v[0:3], a[24:27], a[144:147], v[0:3]\n\t"
"s_nop 0\n\t"
"ds_write_addtid_b32 v209 offset:30016\n\t"
"ds_write_addtid_b32 v222 offset:22720\n\t"
"v_mfma_f32_16x16x32_bf16 v[0:3], a[28:31], a[160:163], v[0:3]\n\t"
"s_waitcnt vmcnt(32) lgkmcnt(6)\n\t"
"ds_write_addtid_b32 v220 offset:31040\n\t"
"v_mfma_f32_16x16x32_bf16 v[16:19], a[32:35], a[144:147], v[16:19]\n\t"
"ds_write_addtid_b32 v208 offset:28992\n\t"
"v_mfma_f32_16x16x32_bf16 v[16:19], a[36:39], a[160:163], v[16:19]\n\t"
"s_waitcnt vmcnt(30) lgkmcnt(7)\n\t"
"buffer_load_dwordx4 a[16:19], off, s[92:95], s50 offset:0 sc1\n\t"
"v_mfma_f32_16x16x32_bf16 v[32:35], a[40:43], a[144:147], v[32:35]\n\t"
"ds_read_b128 a[168:171], v224 offset:2560\n\t"
"v_mfma_f32_16x16x32_bf16 v[32:35], a[44:47], a[160:163], v[32:35]\n\t"
"ds_read_b128 a[156:159], v224 offset:768\n\t"
"v_mfma_f32_16x16x32_bf16 v[36:39], a[40:43], a[148:151], v[36:39]\n\t"
"ds_read_b128 a[172:175], v224 offset:2816\n\t"
"v_mfma_f32_16x16x32_bf16 v[36:39], a[44:47], a[164:167], v[36:39]\n\t"
"buffer_load_dwordx4 a[20:23], off, s[92:95], s50 offset:1024 sc1\n\t"
"v_mfma_f32_16x16x32_bf16 v[20:23], a[32:35], a[148:151], v[20:23]\n\t"
"v_mfma_f32_16x16x32_bf16 v[20:23], a[36:39], a[164:167], v[20:23]\n\t"
"v_mfma_f32_16x16x32_bf16 v[4:7], a[24:27], a[148:151], v[4:7]\n\t"
"buffer_load_dwordx4 a[12:15], off, s[96:99], s49 offset:3072 sc1\n\t"
"v_mfma_f32_16x16x32_bf16 v[4:7], a[28:31], a[164:167], v[4:7]\n\t"
"s_waitcnt vmcnt(27) lgkmcnt(4)\n\t"
"s_waitcnt lgkmcnt(0)\n\t"
"s_barrier\n\t"
"v_mfma_f32_16x16x32_bf16 v[8:11], a[24:27], a[152:155], v[8:11]\n\t"
"s_add_u32 m0, s26, 76800\n\t"
"ds_read_b128 a[148:151], v224 offset:17824\n\t"
"ds_read_b128 a[164:167], v224 offset:19872\n\t"
"v_mfma_f32_16x16x32_bf16 v[8:11], a[28:31], a[168:171], v[8:11]\n\t"
"ds_read_b128 a[144:147], v224 offset:17568\n\t"
"v_mfma_f32_16x16x32_bf16 v[24:27], a[32:35], a[152:155], v[24:27]\n\t"
"ds_read_b128 a[160:163], v224 offset:19616\n\t"
"v_mfma_f32_16x16x32_bf16 v[24:27], a[36:39], a[168:171], v[24:27]\n\t"
"buffer_load_dwordx4 v236, s[12:15], s22 offen offset:256 nt lds\n\t"
"v_mfma_f32_16x16x32_bf16 v[40:43], a[40:43], a[152:155], v[40:43]\n\t"
"s_add_u32 s24, s24, 3145728\n\t"
"ds_read_b64_tr_b16 a[176:177], v248 offset:0\n\t"
"ds_read_b64_tr_b16 a[178:179], v249 offset:0\n\t"
"v_mfma_f32_16x16x32_bf16 v[40:43], a[44:47], a[168:171], v[40:43]\n\t"
"s_add_u32 s20, s20, 3145728\n\t"
"ds_read_b64_tr_b16 a[180:181], v248 offset:512\n\t"
"ds_read_b64_tr_b16 a[182:183], v249 offset:512\n\t"
"v_mfma_f32_16x16x32_bf16 v[44:47], a[40:43], a[156:159], v[44:47]\n\t"
"ds_read_b64_tr_b16 a[184:185], v250 offset:2304\n\t"
"v_mfma_f32_16x16x32_bf16 v[44:47], a[44:47], a[172:175], v[44:47]\n\t"
"buffer_load_dwordx4 v237, s[12:15], s22 offen offset:1280 nt lds\n\t"
"v_mfma_f32_16x16x32_bf16 v[28:31], a[32:35], a[156:159], v[28:31]\n\t"
"ds_read_b128 a[152:155], v224 offset:18080\n\t"
"v_mfma_f32_16x16x32_bf16 v[28:31], a[36:39], a[172:175], v[28:31]\n\t"
"s_waitcnt lgkmcnt(2)\n\t"
"ds_read_b64_tr_b16 a[186:187], v251 offset:2304\n\t"
"v_mfma_f32_4x4x4_16b_bf16 v[192:195], a[210:211], a[176:177], 0\n\t"
"ds_read_b64_tr_b16 a[188:189], v250 offset:2816\n\t"
"v_mfma_f32_4x4x4_16b_bf16 v[196:199], a[210:211], a[178:179], 0\n\t"
"ds_read_b64_tr_b16 a[190:191], v251 offset:2816\n\t"
"v_mfma_f32_4x4x4_16b_bf16 v[200:203], a[210:211], a[180:181], 0\n\t"
"ds_read_b128 a[168:171], v224 offset:20128\n\t"
"v_mfma_f32_4x4x4_16b_bf16 v[204:207], a[210:211], a[182:183], 0\n\t"
"buffer_load_dwordx4 v238, s[12:15], s22 offen offset:2304 nt lds\n\t"
"v_mfma_f32_16x16x32_bf16 v[12:15], a[24:27], a[156:159], v[12:15]\n\t"
"ds_read_b128 a[156:159], v224 offset:18336\n\t"
"v_mfma_f32_16x16x32_bf16 v[12:15], a[28:31], a[172:175], v[12:15]\n\t"
"s_waitcnt vmcnt(28)\n\t"
"buffer_load_dwordx4 a[8:11], off, s[96:99], s49 offset:2048 sc1\n\t"
"v_mfma_f32_16x16x32_bf16 v[144:147], a[48:51], a[144:147], v[96:99]\n\t"
"ds_read_b64_tr_b16 a[192:193], v250 offset:0\n\t"
"v_cvt_pk_bf16_f32 v197, v193, v197\n\t"
"v_mfma_f32_16x16x32_bf16 v[144:147], a[52:55], a[160:163], v[144:147]\n\t"
"s_waitcnt vmcnt(27) lgkmcnt(7)\n\t"
"ds_read_b64_tr_b16 a[194:195], v251 offset:0\n\t"
"v_cvt_pk_bf16_f32 v195, v195, v199\n\t"
"v_mfma_f32_16x16x32_bf16 v[160:163], a[56:59], a[144:147], v[112:115]\n\t"
"ds_read_b64_tr_b16 a[196:197], v250 offset:512\n\t"
"v_cvt_pk_bf16_f32 v196, v192, v196\n\t"
"v_mfma_f32_16x16x32_bf16 v[160:163], a[60:63], a[160:163], v[160:163]\n\t"
"s_waitcnt vmcnt(25) lgkmcnt(7)\n\t"
"buffer_load_dwordx4 v239, s[12:15], s22 offen offset:3328 nt lds\n\t"
"v_mfma_f32_16x16x32_bf16 v[176:179], a[64:67], a[144:147], v[128:131]\n\t"
"ds_read_b128 a[172:175], v224 offset:20384\n\t"
"v_cvt_pk_bf16_f32 v194, v194, v198\n\t"
"v_mfma_f32_16x16x32_bf16 v[176:179], a[68:71], a[160:163], v[176:179]\n\t"
"s_waitcnt lgkmcnt(6)\n\t"
"v_cvt_pk_bf16_f32 v198, v200, v204\n\t"
"v_cvt_pk_bf16_f32 v193, v203, v207\n\t"
"v_mfma_f32_4x4x4_16b_bf16 v[208:211], a[210:211], a[184:185], 0\n\t"
"ds_read_b128 a[160:163], v224 offset:18592\n\t"
"v_mfma_f32_4x4x4_16b_bf16 v[212:215], a[210:211], a[186:187], 0\n\t"
"ds_read_b64_tr_b16 a[198:199], v251 offset:512\n\t"
"v_mfma_f32_4x4x4_16b_bf16 v[216:219], a[210:211], a[188:189], 0\n\t"
"ds_read_b64_tr_b16 a[200:201], v248 offset:2304\n\t"
"v_mfma_f32_4x4x4_16b_bf16 v[220:223], a[210:211], a[190:191], 0\n\t"
"s_mov_b64 exec, s[36:37]\n\t"
"buffer_load_dwordx4 v240, s[12:15], s22 offen offset:3840 nt lds\n\t"
"s_mov_b64 exec, -1\n\t"
"v_mfma_f32_16x16x32_bf16 v[180:183], a[64:67], a[148:151], v[132:135]\n\t"
"s_lshl_b32 m0, s73, 8\n\t"
"ds_read_b128 a[144:147], v224 offset:16544\n\t"
"v_cvt_pk_bf16_f32 v199, v201, v205\n\t"
"v_mfma_f32_16x16x32_bf16 v[180:183], a[68:71], a[164:167], v[180:183]\n\t"
"ds_write_addtid_b32 v198 offset:8320\n\t"
"v_cvt_pk_bf16_f32 v192, v202, v206\n\t"
"v_mfma_f32_16x16x32_bf16 v[164:167], a[56:59], a[148:151], v[116:119]\n\t"
"v_cvt_pk_bf16_f32 v212, v208, v212\n\t"
"v_cvt_pk_bf16_f32 v211, v211, v215\n\t"
"v_mfma_f32_16x16x32_bf16 v[164:167], a[60:63], a[164:167], v[164:167]\n\t"
"v_cvt_pk_bf16_f32 v210, v210, v214\n\t"
"v_cvt_pk_bf16_f32 v213, v209, v213\n\t"
"v_mfma_f32_16x16x32_bf16 v[148:151], a[48:51], a[148:151], v[100:103]\n\t"
"ds_write_addtid_b32 v197 offset:1024\n\t"
"v_cvt_pk_bf16_f32 v214, v216, v220\n\t"
"v_mfma_f32_16x16x32_bf16 v[148:151], a[52:55], a[164:167], v[148:151]\n\t"
"s_waitcnt lgkmcnt(4)\n\t"
"ds_write_addtid_b32 v196 offset:0\n\t"
"v_cvt_pk_bf16_f32 v209, v219, v223\n\t"
"v_mfma_f32_4x4x4_16b_bf16 v[200:203], a[210:211], a[192:193], 0\n\t"
"ds_write_addtid_b32 v199 offset:9344\n\t"
"v_mfma_f32_4x4x4_16b_bf16 v[204:207], a[210:211], a[194:195], 0\n\t"
"v_cvt_pk_bf16_f32 v208, v218, v222\n\t"
"v_mfma_f32_4x4x4_16b_bf16 v[196:199], a[210:211], a[196:197], 0\n\t"
"v_cvt_pk_bf16_f32 v215, v217, v221\n\t"
"v_mfma_f32_4x4x4_16b_bf16 v[216:219], a[210:211], a[198:199], 0\n\t"
"s_add_u32 s27, s27, 3145728\n\t"
"ds_read_b128 a[148:151], v224 offset:16800\n\t"
"ds_read_b64_tr_b16 a[202:203], v249 offset:2304\n\t"
"v_mfma_f32_16x16x32_bf16 v[152:155], a[48:51], a[152:155], v[104:107]\n\t"
"ds_read_b64_tr_b16 a[204:205], v248 offset:2816\n\t"
"v_mfma_f32_16x16x32_bf16 v[152:155], a[52:55], a[168:171], v[152:155]\n\t"
"ds_read_b64_tr_b16 a[206:207], v249 offset:2816\n\t"
"v_mfma_f32_16x16x32_bf16 v[168:171], a[56:59], a[152:155], v[120:123]\n\t"
"v_cvt_pk_bf16_f32 v205, v201, v205\n\t"
"v_cvt_pk_bf16_f32 v203, v203, v207\n\t"
"v_mfma_f32_16x16x32_bf16 v[168:171], a[60:63], a[168:171], v[168:171]\n\t"
"ds_read_b128 a[164:167], v224 offset:18848\n\t"
"v_cvt_pk_bf16_f32 v204, v200, v204\n\t"
"v_mfma_f32_16x16x32_bf16 v[184:187], a[64:67], a[152:155], v[136:139]\n\t"
"ds_read_b128 a[152:155], v224 offset:17056\n\t"
"v_cvt_pk_bf16_f32 v200, v198, v218\n\t"
"v_mfma_f32_16x16x32_bf16 v[184:187], a[68:71], a[168:171], v[184:187]\n\t"
"ds_write_addtid_b32 v212 offset:4128\n\t"
"v_cvt_pk_bf16_f32 v202, v202, v206\n\t"
"v_mfma_f32_16x16x32_bf16 v[188:191], a[64:67], a[156:159], v[140:143]\n\t"
"ds_write_addtid_b32 v214 offset:12448\n\t"
"v_cvt_pk_bf16_f32 v201, v199, v219\n\t"
"v_mfma_f32_16x16x32_bf16 v[188:191], a[68:71], a[172:175], v[188:191]\n\t"
"s_waitcnt lgkmcnt(4)\n\t"
"ds_write_addtid_b32 v215 offset:13472\n\t"
"v_cvt_pk_bf16_f32 v207, v197, v217\n\t"
"v_mfma_f32_4x4x4_16b_bf16 v[220:223], a[210:211], a[200:201], 0\n\t"
"ds_write_addtid_b32 v213 offset:5152\n\t"
"v_mfma_f32_4x4x4_16b_bf16 v[212:215], a[210:211], a[202:203], 0\n\t"
"v_cvt_pk_bf16_f32 v206, v196, v216\n\t"
"v_mfma_f32_4x4x4_16b_bf16 v[196:199], a[210:211], a[204:205], 0\n\t"
"ds_read_b128 a[168:171], v224 offset:19104\n\t"
"v_mfma_f32_4x4x4_16b_bf16 v[216:219], a[210:211], a[206:207], 0\n\t"
"ds_write_addtid_b32 v205 offset:3072\n\t"
"v_mfma_f32_16x16x32_bf16 v[172:175], a[56:59], a[156:159], v[124:127]\n\t"
"ds_write_addtid_b32 v204 offset:2048\n\t"
"v_mfma_f32_16x16x32_bf16 v[172:175], a[60:63], a[172:175], v[172:175]\n\t"
"s_nop 0\n\t"
"ds_write_addtid_b32 v206 offset:10368\n\t"
"ds_write_addtid_b32 v207 offset:11392\n\t"
"v_mfma_f32_16x16x32_bf16 v[156:159], a[48:51], a[156:159], v[108:111]\n\t"
"v_cvt_pk_bf16_f32 v212, v220, v212\n\t"
"v_cvt_pk_bf16_f32 v223, v223, v215\n\t"
"v_mfma_f32_16x16x32_bf16 v[156:159], a[52:55], a[172:175], v[156:159]\n\t"
"s_waitcnt vmcnt(22) lgkmcnt(8)\n\t"
"v_cvt_pk_bf16_f32 v222, v222, v214\n\t"
"v_cvt_pk_bf16_f32 v213, v221, v213\n\t"
"v_mfma_f32_16x16x32_bf16 v[96:99], a[72:75], a[144:147], v[96:99]\n\t"
"ds_write_addtid_b32 v212 offset:6176\n\t"
"v_cvt_pk_bf16_f32 v215, v197, v217\n\t"
"v_mfma_f32_16x16x32_bf16 v[96:99], a[76:79], a[160:163], v[96:99]\n\t"
"ds_write_addtid_b32 v215 offset:15520\n\t"
"v_cvt_pk_bf16_f32 v214, v196, v216\n\t"
"v_mfma_f32_16x16x32_bf16 v[112:115], a[80:83], a[144:147], v[112:115]\n\t"
"s_add_u32 s25, s25, 3145728\n\t"
"ds_write_addtid_b32 v214 offset:14496\n\t"
"ds_write_addtid_b32 v213 offset:7200\n\t"
"v_mfma_f32_16x16x32_bf16 v[112:115], a[84:87], a[160:163], v[112:115]\n\t"
"s_waitcnt vmcnt(21) lgkmcnt(9)\n\t"
"v_mfma_f32_16x16x32_bf16 v[128:131], a[88:91], a[144:147], v[128:131]\n\t"
"v_cvt_pk_bf16_f32 v220, v198, v218\n\t"
"v_mfma_f32_16x16x32_bf16 v[128:131], a[92:95], a[160:163], v[128:131]\n\t"
"ds_read_b128 a[172:175], v224 offset:19360\n\t"
"v_mfma_f32_16x16x32_bf16 v[132:135], a[88:91], a[148:151], v[132:135]\n\t"
"ds_read_b128 a[156:159], v224 offset:17312\n\t"
"v_mfma_f32_16x16x32_bf16 v[132:135], a[92:95], a[164:167], v[132:135]\n\t"
"v_cvt_pk_bf16_f32 v221, v199, v219\n\t"
"v_mfma_f32_16x16x32_bf16 v[116:119], a[80:83], a[148:151], v[116:119]\n\t"
"v_mfma_f32_16x16x32_bf16 v[116:119], a[84:87], a[164:167], v[116:119]\n\t"
"buffer_load_dwordx4 a[0:3], off, s[96:99], s49 offset:0 sc1\n\t"
"v_mfma_f32_16x16x32_bf16 v[100:103], a[72:75], a[148:151], v[100:103]\n\t"
"s_waitcnt lgkmcnt(0)\n\t"
"v_mfma_f32_16x16x32_bf16 v[100:103], a[76:79], a[164:167], v[100:103]\n\t"
"s_barrier\n\t"
"v_mfma_f32_16x16x32_bf16 v[104:107], a[72:75], a[152:155], v[104:107]\n\t"
"s_nop 0\n\t"
"ds_read_b128 a[144:147], v224 offset:1024\n\t"
"ds_read_b128 a[160:163], v224 offset:3072\n\t"
"v_mfma_f32_16x16x32_bf16 v[104:107], a[76:79], a[168:171], v[104:107]\n\t"
"buffer_load_dwordx4 a[4:7], off, s[96:99], s49 offset:1024 sc1\n\t"
"v_mfma_f32_16x16x32_bf16 v[120:123], a[80:83], a[152:155], v[120:123]\n\t"
"ds_read_b128 a[148:151], v224 offset:1280\n\t"
"v_mfma_f32_16x16x32_bf16 v[120:123], a[84:87], a[168:171], v[120:123]\n\t"
"ds_read_b128 a[164:167], v224 offset:3328\n\t"
"v_mfma_f32_16x16x32_bf16 v[136:139], a[88:91], a[152:155], v[136:139]\n\t"
"ds_read_b128 a[152:155], v224 offset:1536\n\t"
"v_mfma_f32_16x16x32_bf16 v[136:139], a[92:95], a[168:171], v[136:139]\n\t"
"buffer_load_dwordx4 a[40:43], off, s[92:95], s49 offset:2048 sc1\n\t"
"v_mfma_f32_16x16x32_bf16 v[140:143], a[88:91], a[156:159], v[140:143]\n\t"
"v_mfma_f32_16x16x32_bf16 v[140:143], a[92:95], a[172:175], v[140:143]\n\t"
"ds_read_b128 a[168:171], v224 offset:3584\n\t"
"v_mfma_f32_16x16x32_bf16 v[124:127], a[80:83], a[156:159], v[124:127]\n\t"
"ds_read_b128 a[248:251], v255 offset:59904\n\t"
"v_mfma_f32_16x16x32_bf16 v[124:127], a[84:87], a[172:175], v[124:127]\n\t"
"buffer_load_dwordx4 a[44:47], off, s[92:95], s49 offset:3072 sc1\n\t"
"v_mfma_f32_16x16x32_bf16 v[108:111], a[72:75], a[156:159], v[108:111]\n\t"
"ds_write_addtid_b32 v222 offset:22720\n\t"
"v_mfma_f32_16x16x32_bf16 v[108:111], a[76:79], a[172:175], v[108:111]\n\t"
"s_waitcnt vmcnt(19) lgkmcnt(6)\n\t"
"ds_read_b128 a[156:159], v224 offset:1792\n\t"
"v_xor_b32 v226, 64, v226\n\t"
"v_mfma_f32_16x16x32_bf16 v[48:51], a[96:99], a[144:147], v[48:51]\n\t"
"ds_read_b128 a[172:175], v224 offset:3840\n\t"
"v_mfma_f32_16x16x32_bf16 v[48:51], a[100:103], a[160:163], v[48:51]\n\t"
"buffer_load_dwordx4 a[36:39], off, s[92:95], s49 offset:1024 sc1\n\t"
"v_mfma_f32_16x16x32_bf16 v[64:67], a[104:107], a[144:147], v[64:67]\n\t"
"ds_write_addtid_b32 v209 offset:30016\n\t"
"v_mfma_f32_16x16x32_bf16 v[64:67], a[108:111], a[160:163], v[64:67]\n\t"
"ds_write_addtid_b32 v192 offset:24864\n\t"
"v_mfma_f32_16x16x32_bf16 v[80:83], a[112:115], a[144:147], v[80:83]\n\t"
"ds_read_b128 a[144:147], v224 offset:0\n\t"
"v_mfma_f32_16x16x32_bf16 v[80:83], a[116:119], a[160:163], v[80:83]\n\t"
"s_waitcnt lgkmcnt(7)\n\t"
"buffer_load_dwordx4 a[32:35], off, s[92:95], s49 offset:0 sc1\n\t"
"v_mfma_f32_16x16x32_bf16 v[84:87], a[112:115], a[148:151], v[84:87]\n\t"
"ds_read_b128 a[160:163], v224 offset:2048\n\t"
"v_mfma_f32_16x16x32_bf16 v[84:87], a[116:119], a[164:167], v[84:87]\n\t"
"ds_write_addtid_b32 v221 offset:32064\n\t"
"v_mfma_f32_16x16x32_bf16 v[68:71], a[104:107], a[148:151], v[68:71]\n\t"
"ds_write_addtid_b32 v203 offset:19616\n\t"
"v_mfma_f32_16x16x32_bf16 v[68:71], a[108:111], a[164:167], v[68:71]\n\t"
"buffer_load_dwordx4 a[24:27], off, s[96:99], s48 offset:2048 sc1\n\t"
"v_mfma_f32_16x16x32_bf16 v[52:55], a[96:99], a[148:151], v[52:55]\n\t"
"ds_read_b128 a[148:151], v224 offset:256\n\t"
"v_mfma_f32_16x16x32_bf16 v[52:55], a[100:103], a[164:167], v[52:55]\n\t"
"ds_write_addtid_b32 v211 offset:21696\n\t"
"v_mfma_f32_16x16x32_bf16 v[56:59], a[96:99], a[152:155], v[56:59]\n\t"
"ds_read_b128 a[164:167], v224 offset:2304\n\t"
"v_mfma_f32_16x16x32_bf16 v[56:59], a[100:103], a[168:171], v[56:59]\n\t"
"buffer_load_dwordx4 a[28:31], off, s[96:99], s48 offset:3072 sc1\n\t"
"v_mfma_f32_16x16x32_bf16 v[72:75], a[104:107], a[152:155], v[72:75]\n\t"
"ds_write_addtid_b32 v208 offset:28992\n\t"
"v_mfma_f32_16x16x32_bf16 v[72:75], a[108:111], a[168:171], v[72:75]\n\t"
"ds_write_addtid_b32 v195 offset:17568\n\t"
"v_mfma_f32_16x16x32_bf16 v[88:91], a[112:115], a[152:155], v[88:91]\n\t"
"ds_read_b128 a[152:155], v224 offset:512\n\t"
"v_mfma_f32_16x16x32_bf16 v[88:91], a[116:119], a[168:171], v[88:91]\n\t"
"s_waitcnt lgkmcnt(7)\n\t"
"buffer_load_dwordx4 a[64:67], off, s[92:95], s77 offset:0 sc1\n\t"
"v_mfma_f32_16x16x32_bf16 v[92:95], a[112:115], a[156:159], v[92:95]\n\t"
"ds_read_b128 a[168:171], v224 offset:2560\n\t"
"v_xor_b32 v227, 64, v227\n\t"
"v_mfma_f32_16x16x32_bf16 v[92:95], a[116:119], a[172:175], v[92:95]\n\t"
"ds_write_addtid_b32 v200 offset:26912\n\t"
"v_mfma_f32_16x16x32_bf16 v[76:79], a[104:107], a[156:159], v[76:79]\n\t"
"ds_write_addtid_b32 v194 offset:16544\n\t"
"v_mfma_f32_16x16x32_bf16 v[76:79], a[108:111], a[172:175], v[76:79]\n\t"
"buffer_load_dwordx4 a[68:71], off, s[92:95], s77 offset:1024 sc1\n\t"
"v_mfma_f32_16x16x32_bf16 v[60:63], a[96:99], a[156:159], v[60:63]\n\t"
"ds_write_addtid_b32 v202 offset:18592\n\t"
"v_mfma_f32_16x16x32_bf16 v[60:63], a[100:103], a[172:175], v[60:63]\n\t"
"s_waitcnt vmcnt(19) lgkmcnt(7)\n\t"
"ds_write_addtid_b32 v210 offset:20672\n\t"
"v_mfma_f32_16x16x32_bf16 v[0:3], a[120:123], a[144:147], v[0:3]\n\t"
"ds_write_addtid_b32 v193 offset:25888\n\t"
"v_mfma_f32_16x16x32_bf16 v[0:3], a[124:127], a[160:163], v[0:3]\n\t"
"ds_write_addtid_b32 v220 offset:31040\n\t"
"v_mfma_f32_16x16x32_bf16 v[16:19], a[128:131], a[144:147], v[16:19]\n\t"
"s_add_u32 s22, s22, 3145728\n\t"
"ds_write_addtid_b32 v201 offset:27936\n\t"
"ds_write_addtid_b32 v223 offset:23744\n\t"
"v_mfma_f32_16x16x32_bf16 v[16:19], a[132:135], a[160:163], v[16:19]\n\t"
"buffer_load_dwordx4 a[60:63], off, s[96:99], s76 offset:3072 sc1\n\t"
"v_mfma_f32_16x16x32_bf16 v[32:35], a[136:139], a[144:147], v[32:35]\n\t"
"ds_read_b128 a[172:175], v224 offset:2816\n\t"
"v_mfma_f32_16x16x32_bf16 v[32:35], a[140:143], a[160:163], v[32:35]\n\t"
"buffer_load_dwordx4 a[56:59], off, s[96:99], s76 offset:2048 sc1\n\t"
"v_mfma_f32_16x16x32_bf16 v[36:39], a[136:139], a[148:151], v[36:39]\n\t"
"s_cmp_eq_u32 s21, 1\n\t"
"s_cselect_b32 s22, 2.0, s22\n\t"
"ds_read_b128 a[156:159], v224 offset:768\n\t"
"v_mfma_f32_16x16x32_bf16 v[36:39], a[140:143], a[164:167], v[36:39]\n\t"
"v_mfma_f32_16x16x32_bf16 v[20:23], a[128:131], a[148:151], v[20:23]\n\t"
"v_mfma_f32_16x16x32_bf16 v[20:23], a[132:135], a[164:167], v[20:23]\n\t"
"buffer_load_dwordx4 a[48:51], off, s[96:99], s76 offset:0 sc1\n\t"
"v_mfma_f32_16x16x32_bf16 v[4:7], a[120:123], a[148:151], v[4:7]\n\t"
"v_mfma_f32_16x16x32_bf16 v[4:7], a[124:127], a[164:167], v[4:7]\n\t"
"s_waitcnt vmcnt(13) lgkmcnt(4)\n\t"
"s_waitcnt lgkmcnt(0)\n\t"
"s_barrier\n\t"
"v_mfma_f32_16x16x32_bf16 v[8:11], a[120:123], a[152:155], v[8:11]\n\t"
"s_nop 0\n\t"
"ds_read_b128 a[144:147], v224 offset:17568\n\t"
"ds_read_b128 a[160:163], v224 offset:19616\n\t"
"v_mfma_f32_16x16x32_bf16 v[8:11], a[124:127], a[168:171], v[8:11]\n\t"
"buffer_load_dwordx4 a[52:55], off, s[96:99], s76 offset:1024 sc1\n\t"
"v_mfma_f32_16x16x32_bf16 v[24:27], a[128:131], a[152:155], v[24:27]\n\t"
"s_nop 0\n\t"
"ds_read_b128 a[148:151], v224 offset:17824\n\t"
"ds_read_b128 a[164:167], v224 offset:19872\n\t"
"v_mfma_f32_16x16x32_bf16 v[24:27], a[132:135], a[168:171], v[24:27]\n\t"
"buffer_load_dwordx4 a[88:91], off, s[92:95], s76 offset:2048 sc1\n\t"
"v_mfma_f32_16x16x32_bf16 v[40:43], a[136:139], a[152:155], v[40:43]\n\t"
"s_nop 0\n\t"
"ds_read_b64_tr_b16 a[176:177], v248 offset:41472\n\t"
"ds_read_b64_tr_b16 a[178:179], v249 offset:41472\n\t"
"v_mfma_f32_16x16x32_bf16 v[40:43], a[140:143], a[168:171], v[40:43]\n\t"
"s_nop 0\n\t"
"ds_read_b64_tr_b16 a[180:181], v248 offset:41984\n\t"
"ds_read_b64_tr_b16 a[182:183], v249 offset:41984\n\t"
"v_mfma_f32_16x16x32_bf16 v[44:47], a[136:139], a[156:159], v[44:47]\n\t"
"s_nop 0\n\t"
"ds_read_b64_tr_b16 a[184:185], v250 offset:43776\n\t"
"ds_read_b64_tr_b16 a[186:187], v251 offset:43776\n\t"
"v_mfma_f32_16x16x32_bf16 v[44:47], a[140:143], a[172:175], v[44:47]\n\t"
"buffer_load_dwordx4 a[92:95], off, s[92:95], s76 offset:3072 sc1\n\t"
"v_mfma_f32_16x16x32_bf16 v[28:31], a[128:131], a[156:159], v[28:31]\n\t"
"s_nop 0\n\t"
"ds_read_b64_tr_b16 a[188:189], v250 offset:44288\n\t"
"ds_read_b64_tr_b16 a[190:191], v251 offset:44288\n\t"
"v_mfma_f32_16x16x32_bf16 v[28:31], a[132:135], a[172:175], v[28:31]\n\t"
"s_waitcnt lgkmcnt(4)\n\t"
"buffer_load_dwordx4 a[84:87], off, s[92:95], s76 offset:1024 sc1\n\t"
"v_mfma_f32_4x4x4_16b_bf16 v[192:195], a[210:211], a[176:177], 0\n\t"
"ds_read_b128 a[152:155], v224 offset:18080\n\t"
"v_mfma_f32_4x4x4_16b_bf16 v[196:199], a[210:211], a[178:179], 0\n\t"
"ds_read_b64_tr_b16 a[192:193], v250 offset:41472\n\t"
"v_mfma_f32_4x4x4_16b_bf16 v[200:203], a[210:211], a[180:181], 0\n\t"
"ds_read_b64_tr_b16 a[194:195], v251 offset:41472\n\t"
"v_mfma_f32_4x4x4_16b_bf16 v[204:207], a[210:211], a[182:183], 0\n\t"
"buffer_load_dwordx4 a[80:83], off, s[92:95], s76 offset:0 sc1\n\t"
"v_mfma_f32_16x16x32_bf16 v[12:15], a[120:123], a[156:159], v[12:15]\n\t"
"s_nop 0\n\t"
"ds_read_b64_tr_b16 a[196:197], v250 offset:41984\n\t"
"ds_read_b64_tr_b16 a[198:199], v251 offset:41984\n\t"
"v_mfma_f32_16x16x32_bf16 v[12:15], a[124:127], a[172:175], v[12:15]\n\t"
"s_waitcnt vmcnt(16)\n\t"
"ds_read_b64_tr_b16 a[200:201], v248 offset:43776\n\t"
"ds_read_b64_tr_b16 a[202:203], v249 offset:43776\n\t"
"v_mfma_f32_16x16x32_bf16 v[144:147], a[0:3], a[144:147], v[144:147]\n\t"
"v_cvt_pk_bf16_f32 v197, v193, v197\n\t"
"v_cvt_pk_bf16_f32 v195, v195, v199\n\t"
"v_mfma_f32_16x16x32_bf16 v[144:147], a[4:7], a[160:163], v[144:147]\n\t"
"buffer_load_dwordx4 a[72:75], off, s[96:99], s75 offset:2048 sc1\n\t"
"v_mfma_f32_16x16x32_bf16 v[160:163], a[8:11], a[144:147], v[160:163]\n\t"
"ds_read_b128 a[168:171], v224 offset:20128\n\t"
"v_cvt_pk_bf16_f32 v194, v194, v198\n\t"
"v_mfma_f32_16x16x32_bf16 v[160:163], a[12:15], a[160:163], v[160:163]\n\t"
"ds_read_b128 a[172:175], v224 offset:20384\n\t"
"v_cvt_pk_bf16_f32 v196, v192, v196\n\t"
"v_mfma_f32_16x16x32_bf16 v[176:179], a[16:19], a[144:147], v[176:179]\n\t"
"v_cvt_pk_bf16_f32 v198, v200, v204\n\t"
"v_cvt_pk_bf16_f32 v193, v203, v207\n\t"
"v_mfma_f32_16x16x32_bf16 v[176:179], a[20:23], a[160:163], v[176:179]\n\t"
"s_waitcnt lgkmcnt(7)\n\t"
"buffer_load_dwordx4 a[76:79], off, s[96:99], s75 offset:3072 sc1\n\t"
"v_mfma_f32_4x4x4_16b_bf16 v[208:211], a[210:211], a[184:185], 0\n\t"
"ds_read_b128 a[156:159], v224 offset:18336\n\t"
"v_mfma_f32_4x4x4_16b_bf16 v[212:215], a[210:211], a[186:187], 0\n\t"
"ds_read_b128 a[144:147], v224 offset:16544\n\t"
"v_mfma_f32_4x4x4_16b_bf16 v[216:219], a[210:211], a[188:189], 0\n\t"
"ds_write_addtid_b32 v198 offset:8320\n\t"
"v_mfma_f32_4x4x4_16b_bf16 v[220:223], a[210:211], a[190:191], 0\n\t"
"ds_write_addtid_b32 v197 offset:1024\n\t"
"v_cvt_pk_bf16_f32 v192, v202, v206\n\t"
"v_mfma_f32_16x16x32_bf16 v[180:183], a[16:19], a[148:151], v[180:183]\n\t"
"ds_write_addtid_b32 v196 offset:0\n\t"
"v_cvt_pk_bf16_f32 v199, v201, v205\n\t"
"v_mfma_f32_16x16x32_bf16 v[180:183], a[20:23], a[164:167], v[180:183]\n\t"
"buffer_load_dwordx4 a[112:115], off, s[92:95], s80 offset:0 sc1\n\t"
"v_mfma_f32_16x16x32_bf16 v[164:167], a[8:11], a[148:151], v[164:167]\n\t"
"ds_read_b128 a[160:163], v224 offset:18592\n\t"
"v_cvt_pk_bf16_f32 v211, v211, v215\n\t"
"v_mfma_f32_16x16x32_bf16 v[164:167], a[12:15], a[164:167], v[164:167]\n\t"
"v_cvt_pk_bf16_f32 v212, v208, v212\n\t"
"v_cvt_pk_bf16_f32 v210, v210, v214\n\t"
"v_mfma_f32_16x16x32_bf16 v[148:151], a[0:3], a[148:151], v[148:151]\n\t"
"ds_read_b64_tr_b16 a[204:205], v248 offset:44288\n\t"
"v_cvt_pk_bf16_f32 v213, v209, v213\n\t"
"v_mfma_f32_16x16x32_bf16 v[148:151], a[4:7], a[164:167], v[148:151]\n\t"
"s_waitcnt lgkmcnt(7)\n\t"
"v_cvt_pk_bf16_f32 v208, v218, v222\n\t"
"v_cvt_pk_bf16_f32 v215, v217, v221\n\t"
"v_mfma_f32_4x4x4_16b_bf16 v[200:203], a[210:211], a[192:193], 0\n\t"
"ds_read_b128 a[148:151], v224 offset:16800\n\t"
"v_mfma_f32_4x4x4_16b_bf16 v[204:207], a[210:211], a[194:195], 0\n\t"
"ds_write_addtid_b32 v199 offset:9344\n\t"
"v_cvt_pk_bf16_f32 v209, v219, v223\n\t"
"v_mfma_f32_4x4x4_16b_bf16 v[196:199], a[210:211], a[196:197], 0\n\t"
"v_cvt_pk_bf16_f32 v214, v216, v220\n\t"
"v_mfma_f32_4x4x4_16b_bf16 v[216:219], a[210:211], a[198:199], 0\n\t"
"buffer_load_dwordx4 a[116:119], off, s[92:95], s80 offset:1024 sc1\n\t"
"v_mfma_f32_16x16x32_bf16 v[152:155], a[0:3], a[152:155], v[152:155]\n\t"
"s_nop 0\n\t"
"ds_read_b128 a[164:167], v224 offset:18848\n\t"
"ds_read_b64_tr_b16 a[206:207], v249 offset:44288\n\t"
"v_mfma_f32_16x16x32_bf16 v[152:155], a[4:7], a[168:171], v[152:155]\n\t"
"buffer_load_dwordx4 a[108:111], off, s[96:99], s79 offset:3072 sc1\n\t"
"v_mfma_f32_16x16x32_bf16 v[168:171], a[8:11], a[152:155], v[168:171]\n\t"
"v_cvt_pk_bf16_f32 v202, v202, v206\n\t"
"v_cvt_pk_bf16_f32 v205, v201, v205\n\t"
"v_mfma_f32_16x16x32_bf16 v[168:171], a[12:15], a[168:171], v[168:171]\n\t"
"ds_write_addtid_b32 v212 offset:4128\n\t"
"v_cvt_pk_bf16_f32 v204, v200, v204\n\t"
"v_mfma_f32_16x16x32_bf16 v[184:187], a[16:19], a[152:155], v[184:187]\n\t"
"ds_read_b128 a[152:155], v224 offset:17056\n\t"
"v_cvt_pk_bf16_f32 v203, v203, v207\n\t"
"v_mfma_f32_16x16x32_bf16 v[184:187], a[20:23], a[168:171], v[184:187]\n\t"
"s_waitcnt lgkmcnt(6)\n\t"
"v_cvt_pk_bf16_f32 v206, v196, v216\n\t"
"v_cvt_pk_bf16_f32 v207, v197, v217\n\t"
"v_mfma_f32_16x16x32_bf16 v[188:191], a[16:19], a[156:159], v[188:191]\n\t"
"ds_write_addtid_b32 v215 offset:13472\n\t"
"v_cvt_pk_bf16_f32 v201, v199, v219\n\t"
"v_mfma_f32_16x16x32_bf16 v[188:191], a[20:23], a[172:175], v[188:191]\n\t"
"s_waitcnt lgkmcnt(3)\n\t"
"ds_write_addtid_b32 v213 offset:5152\n\t"
"v_cvt_pk_bf16_f32 v200, v198, v218\n\t"
"v_mfma_f32_4x4x4_16b_bf16 v[220:223], a[210:211], a[200:201], 0\n\t"
"ds_write_addtid_b32 v214 offset:12448\n\t"
"v_mfma_f32_4x4x4_16b_bf16 v[212:215], a[210:211], a[202:203], 0\n\t"
"ds_write_addtid_b32 v206 offset:10368\n\t"
"v_mfma_f32_4x4x4_16b_bf16 v[196:199], a[210:211], a[204:205], 0\n\t"
"ds_write_addtid_b32 v207 offset:11392\n\t"
"v_mfma_f32_4x4x4_16b_bf16 v[216:219], a[210:211], a[206:207], 0\n\t"
"buffer_load_dwordx4 a[104:107], off, s[96:99], s79 offset:2048 sc1\n\t"
"v_mfma_f32_16x16x32_bf16 v[172:175], a[8:11], a[156:159], v[172:175]\n\t"
"s_nop 0\n\t"
"ds_write_addtid_b32 v204 offset:2048\n\t"
"ds_write_addtid_b32 v205 offset:3072\n\t"
"v_mfma_f32_16x16x32_bf16 v[172:175], a[12:15], a[172:175], v[172:175]\n\t"
"buffer_load_dwordx4 a[96:99], off, s[96:99], s79 offset:0 sc1\n\t"
"v_mfma_f32_16x16x32_bf16 v[156:159], a[0:3], a[156:159], v[156:159]\n\t"
"v_cvt_pk_bf16_f32 v222, v222, v214\n\t"
"v_cvt_pk_bf16_f32 v223, v223, v215\n\t"
"v_mfma_f32_16x16x32_bf16 v[156:159], a[4:7], a[172:175], v[156:159]\n\t"
"s_waitcnt vmcnt(17) lgkmcnt(7)\n\t"
"v_cvt_pk_bf16_f32 v212, v220, v212\n\t"
"v_cvt_pk_bf16_f32 v213, v221, v213\n\t"
"v_mfma_f32_16x16x32_bf16 v[96:99], a[24:27], a[144:147], v[96:99]\n\t"
"ds_write_addtid_b32 v212 offset:6176\n\t"
"v_cvt_pk_bf16_f32 v214, v196, v216\n\t"
"v_mfma_f32_16x16x32_bf16 v[96:99], a[28:31], a[160:163], v[96:99]\n\t"
"ds_write_addtid_b32 v213 offset:7200\n\t"
"v_cvt_pk_bf16_f32 v215, v197, v217\n\t"
"v_mfma_f32_16x16x32_bf16 v[112:115], a[32:35], a[144:147], v[112:115]\n\t"
"s_nop 0\n\t"
"ds_write_addtid_b32 v214 offset:14496\n\t"
"ds_write_addtid_b32 v215 offset:15520\n\t"
"v_mfma_f32_16x16x32_bf16 v[112:115], a[36:39], a[160:163], v[112:115]\n\t"
"buffer_load_dwordx4 a[100:103], off, s[96:99], s79 offset:1024 sc1\n\t"
"v_mfma_f32_16x16x32_bf16 v[128:131], a[40:43], a[144:147], v[128:131]\n\t"
"s_nop 0\n\t"
"ds_read_b128 a[156:159], v224 offset:17312\n\t"
"ds_read_b128 a[172:175], v224 offset:19360\n\t"
"v_mfma_f32_16x16x32_bf16 v[128:131], a[44:47], a[160:163], v[128:131]\n\t"
"buffer_load_dwordx4 a[136:139], off, s[92:95], s79 offset:2048 sc1\n\t"
"v_mfma_f32_16x16x32_bf16 v[132:135], a[40:43], a[148:151], v[132:135]\n\t"
"s_nop 0\n\t"
"ds_read_b128 a[168:171], v224 offset:19104\n\t"
"ds_read_b128 a[244:247], v255 offset:39168\n\t"
"v_mfma_f32_16x16x32_bf16 v[132:135], a[44:47], a[164:167], v[132:135]\n\t"
"buffer_load_dwordx4 a[140:143], off, s[92:95], s79 offset:3072 sc1\n\t"
"v_mfma_f32_16x16x32_bf16 v[116:119], a[32:35], a[148:151], v[116:119]\n\t"
"v_cvt_pk_bf16_f32 v221, v199, v219\n\t"
"v_mfma_f32_16x16x32_bf16 v[116:119], a[36:39], a[164:167], v[116:119]\n\t"
"buffer_load_dwordx4 a[132:135], off, s[92:95], s79 offset:1024 sc1\n\t"
"v_mfma_f32_16x16x32_bf16 v[100:103], a[24:27], a[148:151], v[100:103]\n\t"
"s_waitcnt lgkmcnt(0)\n\t"
"v_cvt_pk_bf16_f32 v220, v198, v218\n\t"
"v_mfma_f32_16x16x32_bf16 v[100:103], a[28:31], a[164:167], v[100:103]\n\t"
"s_barrier\n\t"
"v_mfma_f32_16x16x32_bf16 v[104:107], a[24:27], a[152:155], v[104:107]\n\t"
"s_nop 0\n\t"
"ds_read_b128 a[144:147], v224 offset:1024\n\t"
"ds_read_b128 a[148:151], v224 offset:1280\n\t"
"v_mfma_f32_16x16x32_bf16 v[104:107], a[28:31], a[168:171], v[104:107]\n\t"
"buffer_load_dwordx4 a[128:131], off, s[92:95], s79 offset:0 sc1\n\t"
"v_mfma_f32_16x16x32_bf16 v[120:123], a[32:35], a[152:155], v[120:123]\n\t"
"ds_read_b128 a[160:163], v224 offset:3072\n\t"
"v_add_u32 v248, 101632, v225\n\t"
"v_mfma_f32_16x16x32_bf16 v[120:123], a[36:39], a[168:171], v[120:123]\n\t"
"ds_read_b128 a[164:167], v224 offset:3328\n\t"
"v_mfma_f32_16x16x32_bf16 v[136:139], a[40:43], a[152:155], v[136:139]\n\t"
"ds_read_b128 a[152:155], v224 offset:1536\n\t"
"v_xor_b32 v249, 32, v248\n\t"
"v_mfma_f32_16x16x32_bf16 v[136:139], a[44:47], a[168:171], v[136:139]\n\t"
"buffer_load_dwordx4 a[120:123], off, s[96:99], s78 offset:2048 sc1\n\t"
"v_mfma_f32_16x16x32_bf16 v[140:143], a[40:43], a[156:159], v[140:143]\n\t"
"ds_read_b128 a[240:243], v255 offset:18432\n\t"
"v_xor_b32 v250, 64, v248\n\t"
"v_mfma_f32_16x16x32_bf16 v[140:143], a[44:47], a[172:175], v[140:143]\n\t"
"ds_read_b128 a[168:171], v224 offset:3584\n\t"
"v_xor_b32 v251, 96, v248\n\t"
"v_mfma_f32_16x16x32_bf16 v[124:127], a[32:35], a[156:159], v[124:127]\n\t"
"ds_read_b64_tr_b16 a[178:179], v250 offset:43776\n\t"
"v_mfma_f32_16x16x32_bf16 v[124:127], a[36:39], a[172:175], v[124:127]\n\t"
"buffer_load_dwordx4 a[124:127], off, s[96:99], s78 offset:3072 sc1\n\t"
"v_mfma_f32_16x16x32_bf16 v[108:111], a[24:27], a[156:159], v[108:111]\n\t"
"ds_read_b128 a[156:159], v224 offset:1792\n\t"
"v_mfma_f32_16x16x32_bf16 v[108:111], a[28:31], a[172:175], v[108:111]\n\t"
"s_waitcnt vmcnt(18) lgkmcnt(6)\n\t"
"ds_read_b128 a[172:175], v224 offset:3840\n\t"
"v_mfma_f32_16x16x32_bf16 v[48:51], a[48:51], a[144:147], v[48:51]\n\t"
"ds_read_b64_tr_b16 a[186:187], v251 offset:43776\n\t"
"v_mfma_f32_16x16x32_bf16 v[48:51], a[52:55], a[160:163], v[48:51]\n\t"
"buffer_load_dwordx4 a[16:19], off, s[92:95], s74 offset:0 sc1\n\t"
"v_mfma_f32_16x16x32_bf16 v[64:67], a[56:59], a[144:147], v[64:67]\n\t"
"ds_read_b64_tr_b16 a[194:195], v248 offset:43776\n\t"
"v_mfma_f32_16x16x32_bf16 v[64:67], a[60:63], a[160:163], v[64:67]\n\t"
"s_nop 0\n\t"
"ds_read_b64_tr_b16 a[202:203], v249 offset:43776\n\t"
"ds_read_b64_tr_b16 a[176:177], v248 offset:41472\n\t"
"v_mfma_f32_16x16x32_bf16 v[80:83], a[64:67], a[144:147], v[80:83]\n\t"
"ds_read_b64_tr_b16 a[184:185], v249 offset:41472\n\t"
"v_mfma_f32_16x16x32_bf16 v[80:83], a[68:71], a[160:163], v[80:83]\n\t"
"s_waitcnt lgkmcnt(8)\n\t"
"buffer_load_dwordx4 a[20:23], off, s[92:95], s74 offset:1024 sc1\n\t"
"v_mfma_f32_16x16x32_bf16 v[84:87], a[64:67], a[148:151], v[84:87]\n\t"
"ds_read_b128 a[144:147], v224 offset:0\n\t"
"v_mfma_f32_16x16x32_bf16 v[84:87], a[68:71], a[164:167], v[84:87]\n\t"
"ds_read_b128 a[160:163], v224 offset:2048\n\t"
"v_mfma_f32_16x16x32_bf16 v[68:71], a[56:59], a[148:151], v[68:71]\n\t"
"ds_read_b64_tr_b16 a[180:181], v248 offset:46080\n\t"
"v_mfma_f32_16x16x32_bf16 v[68:71], a[60:63], a[164:167], v[68:71]\n\t"
"buffer_load_dwordx4 a[12:15], off, s[96:99], s72 offset:3072 sc1\n\t"
"v_mfma_f32_16x16x32_bf16 v[52:55], a[48:51], a[148:151], v[52:55]\n\t"
"ds_read_b128 a[148:151], v224 offset:256\n\t"
"v_mfma_f32_16x16x32_bf16 v[52:55], a[52:55], a[164:167], v[52:55]\n\t"
"ds_read_b128 a[164:167], v224 offset:2304\n\t"
"v_mfma_f32_16x16x32_bf16 v[56:59], a[48:51], a[152:155], v[56:59]\n\t"
"s_nop 0\n\t"
"ds_write_addtid_b32 v209 offset:30016\n\t"
"ds_write_addtid_b32 v201 offset:27936\n\t"
"v_mfma_f32_16x16x32_bf16 v[56:59], a[52:55], a[168:171], v[56:59]\n\t"
"buffer_load_dwordx4 a[8:11], off, s[96:99], s72 offset:2048 sc1\n\t"
"v_mfma_f32_16x16x32_bf16 v[72:75], a[56:59], a[152:155], v[72:75]\n\t"
"s_nop 0\n\t"
"ds_write_addtid_b32 v194 offset:16544\n\t"
"ds_write_addtid_b32 v211 offset:21696\n\t"
"v_mfma_f32_16x16x32_bf16 v[72:75], a[60:63], a[168:171], v[72:75]\n\t"
"s_nop 0\n\t"
"ds_write_addtid_b32 v200 offset:26912\n\t"
"ds_write_addtid_b32 v222 offset:22720\n\t"
"v_mfma_f32_16x16x32_bf16 v[88:91], a[64:67], a[152:155], v[88:91]\n\t"
"ds_read_b128 a[152:155], v224 offset:512\n\t"
"v_mfma_f32_16x16x32_bf16 v[88:91], a[68:71], a[168:171], v[88:91]\n\t"
"s_waitcnt lgkmcnt(10)\n\t"
"buffer_load_dwordx4 a[0:3], off, s[96:99], s72 offset:0 sc1\n\t"
"v_mfma_f32_16x16x32_bf16 v[92:95], a[64:67], a[156:159], v[92:95]\n\t"
"ds_read_b128 a[168:171], v224 offset:2560\n\t"
"v_mfma_f32_16x16x32_bf16 v[92:95], a[68:71], a[172:175], v[92:95]\n\t"
"s_nop 0\n\t"
"ds_write_addtid_b32 v202 offset:18592\n\t"
"ds_write_addtid_b32 v203 offset:19616\n\t"
"v_mfma_f32_16x16x32_bf16 v[76:79], a[56:59], a[156:159], v[76:79]\n\t"
"s_nop 0\n\t"
"ds_write_addtid_b32 v192 offset:24864\n\t"
"ds_write_addtid_b32 v210 offset:20672\n\t"
"v_mfma_f32_16x16x32_bf16 v[76:79], a[60:63], a[172:175], v[76:79]\n\t"
"buffer_load_dwordx4 a[4:7], off, s[96:99], s72 offset:1024 sc1\n\t"
"v_mfma_f32_16x16x32_bf16 v[60:63], a[48:51], a[156:159], v[60:63]\n\t"
"ds_read_b128 a[156:159], v224 offset:768\n\t"
"v_mfma_f32_16x16x32_bf16 v[60:63], a[52:55], a[172:175], v[60:63]\n\t"
"s_waitcnt vmcnt(18) lgkmcnt(11)\n\t"
"ds_write_addtid_b32 v195 offset:17568\n\t"
"ds_write_addtid_b32 v193 offset:25888\n\t"
"v_mfma_f32_16x16x32_bf16 v[0:3], a[72:75], a[144:147], v[0:3]\n\t"
"s_nop 0\n\t"
"ds_write_addtid_b32 v208 offset:28992\n\t"
"ds_write_addtid_b32 v221 offset:32064\n\t"
"v_mfma_f32_16x16x32_bf16 v[0:3], a[76:79], a[160:163], v[0:3]\n\t"
"buffer_load_dwordx4 a[40:43], off, s[92:95], s72 offset:2048 sc1\n\t"
"v_mfma_f32_16x16x32_bf16 v[16:19], a[80:83], a[144:147], v[16:19]\n\t"
"s_nop 0\n\t"
"ds_write_addtid_b32 v223 offset:23744\n\t"
"ds_write_addtid_b32 v220 offset:31040\n\t"
"v_mfma_f32_16x16x32_bf16 v[16:19], a[84:87], a[160:163], v[16:19]\n\t"
"ds_read_b128 a[172:175], v224 offset:2816\n\t"
"v_mfma_f32_16x16x32_bf16 v[32:35], a[88:91], a[144:147], v[32:35]\n\t"
"ds_read_b64_tr_b16 a[188:189], v249 offset:46080\n\t"
"v_mfma_f32_16x16x32_bf16 v[32:35], a[92:95], a[160:163], v[32:35]\n\t"
"buffer_load_dwordx4 a[44:47], off, s[92:95], s72 offset:3072 sc1\n\t"
"v_mfma_f32_16x16x32_bf16 v[36:39], a[88:91], a[148:151], v[36:39]\n\t"
"ds_read_b64_tr_b16 a[192:193], v250 offset:41472\n\t"
"v_mfma_f32_16x16x32_bf16 v[36:39], a[92:95], a[164:167], v[36:39]\n\t"
"ds_read_b64_tr_b16 a[200:201], v251 offset:41472\n\t"
"v_mfma_f32_16x16x32_bf16 v[20:23], a[80:83], a[148:151], v[20:23]\n\t"
"s_nop 0\n\t"
"ds_read_b64_tr_b16 a[196:197], v250 offset:46080\n\t"
"ds_read_b64_tr_b16 a[204:205], v251 offset:46080\n\t"
"v_mfma_f32_16x16x32_bf16 v[20:23], a[84:87], a[164:167], v[20:23]\n\t"
"buffer_load_dwordx4 a[36:39], off, s[92:95], s72 offset:1024 sc1\n\t"
"v_mfma_f32_16x16x32_bf16 v[4:7], a[72:75], a[148:151], v[4:7]\n\t"
"s_waitcnt lgkmcnt(5)\n\t"
"v_mfma_f32_16x16x32_bf16 v[4:7], a[76:79], a[164:167], v[4:7]\n\t"
"s_barrier\n\t"
"v_mfma_f32_16x16x32_bf16 v[8:11], a[72:75], a[152:155], v[8:11]\n\t"
"s_nop 0\n\t"
"ds_read_b128 a[144:147], v224 offset:17568\n\t"
"ds_read_b128 a[148:151], v224 offset:17824\n\t"
"v_mfma_f32_16x16x32_bf16 v[8:11], a[76:79], a[168:171], v[8:11]\n\t"
"buffer_load_dwordx4 a[32:35], off, s[92:95], s72 offset:0 sc1\n\t"
"v_mfma_f32_16x16x32_bf16 v[24:27], a[80:83], a[152:155], v[24:27]\n\t"
"ds_read_b128 a[160:163], v224 offset:19616\n\t"
"v_mfma_f32_16x16x32_bf16 v[24:27], a[84:87], a[168:171], v[24:27]\n\t"
"ds_read_b128 a[164:167], v224 offset:19872\n\t"
"v_mfma_f32_16x16x32_bf16 v[40:43], a[88:91], a[152:155], v[40:43]\n\t"
"v_mfma_f32_16x16x32_bf16 v[40:43], a[92:95], a[168:171], v[40:43]\n\t"
"buffer_load_dwordx4 a[24:27], off, s[96:99], s69 offset:2048 sc1\n\t"
"v_mfma_f32_16x16x32_bf16 v[44:47], a[88:91], a[156:159], v[44:47]\n\t"
"ds_read_b128 a[168:171], v224 offset:20128\n\t"
"v_mfma_f32_16x16x32_bf16 v[44:47], a[92:95], a[172:175], v[44:47]\n\t"
"buffer_load_dwordx4 a[28:31], off, s[96:99], s69 offset:3072 sc1\n\t"
"v_mfma_f32_16x16x32_bf16 v[28:31], a[80:83], a[156:159], v[28:31]\n\t"
"ds_read_b128 a[152:155], v224 offset:18080\n\t"
"v_mfma_f32_16x16x32_bf16 v[28:31], a[84:87], a[172:175], v[28:31]\n\t"
"buffer_load_dwordx4 a[64:67], off, s[92:95], s83 offset:0 sc1\n\t"
"v_mfma_f32_16x16x32_bf16 v[12:15], a[72:75], a[156:159], v[12:15]\n\t"
"ds_read_b128 a[156:159], v224 offset:18336\n\t"
"v_mfma_f32_16x16x32_bf16 v[12:15], a[76:79], a[172:175], v[12:15]\n\t"
"s_waitcnt lgkmcnt(5)\n\t"
"buffer_load_dwordx4 a[68:71], off, s[92:95], s83 offset:1024 sc1\n\t"
"v_mfma_f32_4x4x4_16b_bf16 v[200:203], a[220:221], a[178:179], 0\n\t"
"ds_read_b128 a[172:175], v224 offset:20384\n\t"
"v_mfma_f32_4x4x4_16b_bf16 v[204:207], a[220:221], a[186:187], 0\n\t"
"ds_read_b64_tr_b16 a[178:179], v250 offset:44928\n\t"
"v_mfma_f32_4x4x4_16b_bf16 v[216:219], a[220:221], a[194:195], 0\n\t"
"ds_read_b64_tr_b16 a[186:187], v251 offset:44928\n\t"
"v_mfma_f32_4x4x4_16b_bf16 v[220:223], a[220:221], a[202:203], 0\n\t"
"v_mfma_f32_4x4x4_16b_bf16 v[192:195], a[222:223], a[176:177], v[200:203]\n\t"
"ds_read_b64_tr_b16 a[194:195], v248 offset:44928\n\t"
"v_mfma_f32_4x4x4_16b_bf16 v[196:199], a[222:223], a[184:185], v[204:207]\n\t"
"ds_read_b64_tr_b16 a[202:203], v249 offset:44928\n\t"
"v_mfma_f32_4x4x4_16b_bf16 v[200:203], a[222:223], a[180:181], v[200:203]\n\t"
"ds_read_b64_tr_b16 a[176:177], v248 offset:42624\n\t"
"v_mfma_f32_4x4x4_16b_bf16 v[204:207], a[222:223], a[188:189], v[204:207]\n\t"
"ds_read_b64_tr_b16 a[184:185], v249 offset:42624\n\t"
"v_mfma_f32_4x4x4_16b_bf16 v[208:211], a[222:223], a[192:193], v[216:219]\n\t"
"ds_read_b64_tr_b16 a[180:181], v248 offset:47232\n\t"
"v_mfma_f32_4x4x4_16b_bf16 v[212:215], a[222:223], a[200:201], v[220:223]\n\t"
"ds_read_b64_tr_b16 a[188:189], v249 offset:47232\n\t"
"v_mfma_f32_4x4x4_16b_bf16 v[216:219], a[222:223], a[196:197], v[216:219]\n\t"
"ds_read_b64_tr_b16 a[192:193], v250 offset:42624\n\t"
"v_mfma_f32_4x4x4_16b_bf16 v[220:223], a[222:223], a[204:205], v[220:223]\n\t"
"s_waitcnt vmcnt(20) lgkmcnt(8)\n\t"
"buffer_load_dwordx4 a[60:63], off, s[96:99], s82 offset:3072 sc1\n\t"
"v_mfma_f32_16x16x32_bf16 v[144:147], a[96:99], a[144:147], v[144:147]\n\t"
"ds_read_b64_tr_b16 a[200:201], v251 offset:42624\n\t"
"v_cvt_pk_bf16_f32 v193, -v193, -v197\n\t"
"v_mfma_f32_16x16x32_bf16 v[144:147], a[100:103], a[160:163], v[144:147]\n\t"
"ds_read_b64_tr_b16 a[196:197], v250 offset:47232\n\t"
"v_cvt_pk_bf16_f32 v192, -v192, -v196\n\t"
"v_mfma_f32_16x16x32_bf16 v[160:163], a[104:107], a[144:147], v[160:163]\n\t"
"ds_write_addtid_b32 v193 offset:8320\n\t"
"v_cvt_pk_bf16_f32 v203, v203, v207\n\t"
"v_mfma_f32_16x16x32_bf16 v[160:163], a[108:111], a[160:163], v[160:163]\n\t"
"buffer_load_dwordx4 a[56:59], off, s[96:99], s82 offset:2048 sc1\n\t"
"v_mfma_f32_16x16x32_bf16 v[176:179], a[112:115], a[144:147], v[176:179]\n\t"
"ds_read_b128 a[144:147], v224 offset:16544\n\t"
"v_cvt_pk_bf16_f32 v208, -v208, -v212\n\t"
"v_mfma_f32_16x16x32_bf16 v[176:179], a[116:119], a[160:163], v[176:179]\n\t"
"ds_write_addtid_b32 v208 offset:2048\n\t"
"v_cvt_pk_bf16_f32 v211, -v211, -v215\n\t"
"v_mfma_f32_16x16x32_bf16 v[180:183], a[112:115], a[148:151], v[180:183]\n\t"
"ds_read_b128 a[160:163], v224 offset:18592\n\t"
"v_cvt_pk_bf16_f32 v200, v200, v204\n\t"
"v_mfma_f32_16x16x32_bf16 v[180:183], a[116:119], a[164:167], v[180:183]\n\t"
"buffer_load_dwordx4 a[48:51], off, s[96:99], s82 offset:0 sc1\n\t"
"v_mfma_f32_16x16x32_bf16 v[164:167], a[104:107], a[148:151], v[164:167]\n\t"
"ds_write_addtid_b32 v203 offset:13472\n\t"
"v_cvt_pk_bf16_f32 v209, -v209, -v213\n\t"
"v_mfma_f32_16x16x32_bf16 v[164:167], a[108:111], a[164:167], v[164:167]\n\t"
"ds_write_addtid_b32 v209 offset:10368\n\t"
"v_cvt_pk_bf16_f32 v194, -v194, -v198\n\t"
"v_mfma_f32_16x16x32_bf16 v[148:151], a[96:99], a[148:151], v[148:151]\n\t"
"ds_read_b128 a[148:151], v224 offset:16800\n\t"
"v_cvt_pk_bf16_f32 v218, v218, v222\n\t"
"v_mfma_f32_16x16x32_bf16 v[148:151], a[100:103], a[164:167], v[148:151]\n\t"
"buffer_load_dwordx4 a[52:55], off, s[96:99], s82 offset:1024 sc1\n\t"
"v_mfma_f32_16x16x32_bf16 v[152:155], a[96:99], a[152:155], v[152:155]\n\t"
"ds_read_b128 a[164:167], v224 offset:18848\n\t"
"v_cvt_pk_bf16_f32 v217, v217, v221\n\t"
"v_mfma_f32_16x16x32_bf16 v[152:155], a[100:103], a[168:171], v[152:155]\n\t"
"ds_write_addtid_b32 v217 offset:14496\n\t"
"v_cvt_pk_bf16_f32 v219, v219, v223\n\t"
"v_mfma_f32_16x16x32_bf16 v[168:171], a[104:107], a[152:155], v[168:171]\n\t"
"ds_write_addtid_b32 v218 offset:7200\n\t"
"v_mfma_f32_16x16x32_bf16 v[168:171], a[108:111], a[168:171], v[168:171]\n\t"
"buffer_load_dwordx4 a[88:91], off, s[92:95], s82 offset:2048 sc1\n\t"
"v_mfma_f32_16x16x32_bf16 v[184:187], a[112:115], a[152:155], v[184:187]\n\t"
"ds_read_b128 a[152:155], v224 offset:17056\n\t"
"v_cvt_pk_bf16_f32 v216, v216, v220\n\t"
"v_mfma_f32_16x16x32_bf16 v[184:187], a[116:119], a[168:171], v[184:187]\n\t"
"ds_read_b128 a[168:171], v224 offset:19104\n\t"
"v_cvt_pk_bf16_f32 v210, -v210, -v214\n\t"
"v_mfma_f32_16x16x32_bf16 v[188:191], a[112:115], a[156:159], v[188:191]\n\t"
"ds_write_addtid_b32 v216 offset:6176\n\t"
"v_cvt_pk_bf16_f32 v195, -v195, -v199\n\t"
"v_mfma_f32_16x16x32_bf16 v[188:191], a[116:119], a[172:175], v[188:191]\n\t"
"buffer_load_dwordx4 a[92:95], off, s[92:95], s82 offset:3072 sc1\n\t"
"v_mfma_f32_16x16x32_bf16 v[172:175], a[104:107], a[156:159], v[172:175]\n\t"
"ds_write_addtid_b32 v195 offset:9344\n\t"
"v_mfma_f32_16x16x32_bf16 v[172:175], a[108:111], a[172:175], v[172:175]\n\t"
"s_nop 0\n\t"
"ds_write_addtid_b32 v192 offset:0\n\t"
"ds_write_addtid_b32 v200 offset:4128\n\t"
"v_mfma_f32_16x16x32_bf16 v[156:159], a[96:99], a[156:159], v[156:159]\n\t"
"ds_write_addtid_b32 v219 offset:15520\n\t"
"v_cvt_pk_bf16_f32 v202, v202, v206\n\t"
"v_mfma_f32_16x16x32_bf16 v[156:159], a[100:103], a[172:175], v[156:159]\n\t"
"s_waitcnt vmcnt(20) lgkmcnt(8)\n\t"
"buffer_load_dwordx4 a[84:87], off, s[92:95], s82 offset:1024 sc1\n\t"
"v_mfma_f32_16x16x32_bf16 v[96:99], a[120:123], a[144:147], v[96:99]\n\t"
"ds_write_addtid_b32 v211 offset:11392\n\t"
"v_cvt_pk_bf16_f32 v201, v201, v205\n\t"
"v_mfma_f32_16x16x32_bf16 v[96:99], a[124:127], a[160:163], v[96:99]\n\t"
"s_nop 0\n\t"
"ds_write_addtid_b32 v202 offset:5152\n\t"
"ds_write_addtid_b32 v210 offset:3072\n\t"
"v_mfma_f32_16x16x32_bf16 v[112:115], a[128:131], a[144:147], v[112:115]\n\t"
"s_nop 0\n\t"
"ds_write_addtid_b32 v194 offset:1024\n\t"
"ds_write_addtid_b32 v201 offset:12448\n\t"
"v_mfma_f32_16x16x32_bf16 v[112:115], a[132:135], a[160:163], v[112:115]\n\t"
".p2align 3\n\t"
"buffer_load_dwordx4 a[80:83], off, s[92:95], s82 offset:0 sc1\n\t"
"v_pk_fma_f32 v[48:49], v[144:145], s[90:91], v[48:49]\n\t"
"v_pk_fma_f32 v[50:51], v[146:147], s[90:91], v[50:51]\n\t"
"v_pk_add_f32 v[144:145], v[48:49], v[144:145] neg_lo:[0,1] neg_hi:[0,1]\n\t"
"v_pk_add_f32 v[146:147], v[50:51], v[146:147] neg_lo:[0,1] neg_hi:[0,1]\n\t"
"v_pk_fma_f32 v[64:65], v[160:161], s[90:91], v[64:65]\n\t"
"v_pk_fma_f32 v[66:67], v[162:163], s[90:91], v[66:67]\n\t"
"v_pk_add_f32 v[160:161], v[64:65], v[160:161] neg_lo:[0,1] neg_hi:[0,1]\n\t"
"v_pk_add_f32 v[162:163], v[66:67], v[162:163] neg_lo:[0,1] neg_hi:[0,1]\n\t"
"v_pk_fma_f32 v[80:81], v[176:177], s[90:91], v[80:81]\n\t"
"v_pk_fma_f32 v[82:83], v[178:179], s[90:91], v[82:83]\n\t"
"v_pk_add_f32 v[176:177], v[80:81], v[176:177] neg_lo:[0,1] neg_hi:[0,1]\n\t"
"v_pk_add_f32 v[178:179], v[82:83], v[178:179] neg_lo:[0,1] neg_hi:[0,1]\n\t"
"buffer_load_dwordx4 a[72:75], off, s[96:99], s81 offset:2048 sc1\n\t"
"v_pk_fma_f32 v[84:85], v[180:181], s[90:91], v[84:85]\n\t"
"v_pk_fma_f32 v[86:87], v[182:183], s[90:91], v[86:87]\n\t"
"v_pk_add_f32 v[180:181], v[84:85], v[180:181] neg_lo:[0,1] neg_hi:[0,1]\n\t"
"v_pk_add_f32 v[182:183], v[86:87], v[182:183] neg_lo:[0,1] neg_hi:[0,1]\n\t"
"v_pk_fma_f32 v[68:69], v[164:165], s[90:91], v[68:69]\n\t"
"v_pk_fma_f32 v[70:71], v[166:167], s[90:91], v[70:71]\n\t"
"v_pk_add_f32 v[164:165], v[68:69], v[164:165] neg_lo:[0,1] neg_hi:[0,1]\n\t"
"v_pk_add_f32 v[166:167], v[70:71], v[166:167] neg_lo:[0,1] neg_hi:[0,1]\n\t"
"v_pk_fma_f32 v[52:53], v[148:149], s[90:91], v[52:53]\n\t"
"v_pk_fma_f32 v[54:55], v[150:151], s[90:91], v[54:55]\n\t"
"v_pk_add_f32 v[148:149], v[52:53], v[148:149] neg_lo:[0,1] neg_hi:[0,1]\n\t"
"v_pk_add_f32 v[150:151], v[54:55], v[150:151] neg_lo:[0,1] neg_hi:[0,1]\n\t"
"buffer_load_dwordx4 a[76:79], off, s[96:99], s81 offset:3072 sc1\n\t"
"v_pk_fma_f32 v[56:57], v[152:153], s[90:91], v[56:57]\n\t"
"v_pk_fma_f32 v[58:59], v[154:155], s[90:91], v[58:59]\n\t"
"v_pk_add_f32 v[152:153], v[56:57], v[152:153] neg_lo:[0,1] neg_hi:[0,1]\n\t"
"v_pk_add_f32 v[154:155], v[58:59], v[154:155] neg_lo:[0,1] neg_hi:[0,1]\n\t"
"v_pk_fma_f32 v[72:73], v[168:169], s[90:91], v[72:73]\n\t"
"v_pk_fma_f32 v[74:75], v[170:171], s[90:91], v[74:75]\n\t"
"v_pk_add_f32 v[168:169], v[72:73], v[168:169] neg_lo:[0,1] neg_hi:[0,1]\n\t"
"v_pk_add_f32 v[170:171], v[74:75], v[170:171] neg_lo:[0,1] neg_hi:[0,1]\n\t"
"v_pk_fma_f32 v[88:89], v[184:185], s[90:91], v[88:89]\n\t"
"v_pk_fma_f32 v[90:91], v[186:187], s[90:91], v[90:91]\n\t"
"v_pk_add_f32 v[184:185], v[88:89], v[184:185] neg_lo:[0,1] neg_hi:[0,1]\n\t"
"v_pk_add_f32 v[186:187], v[90:91], v[186:187] neg_lo:[0,1] neg_hi:[0,1]\n\t"
"buffer_load_dwordx4 a[112:115], off, s[92:95], s56 offset:0 sc1\n\t"
"v_pk_fma_f32 v[92:93], v[188:189], s[90:91], v[92:93]\n\t"
"v_pk_fma_f32 v[94:95], v[190:191], s[90:91], v[94:95]\n\t"
"v_pk_add_f32 v[188:189], v[92:93], v[188:189] neg_lo:[0,1] neg_hi:[0,1]\n\t"
"v_pk_add_f32 v[190:191], v[94:95], v[190:191] neg_lo:[0,1] neg_hi:[0,1]\n\t"
"v_pk_fma_f32 v[76:77], v[172:173], s[90:91], v[76:77]\n\t"
"v_pk_fma_f32 v[78:79], v[174:175], s[90:91], v[78:79]\n\t"
"v_pk_add_f32 v[172:173], v[76:77], v[172:173] neg_lo:[0,1] neg_hi:[0,1]\n\t"
"v_pk_add_f32 v[174:175], v[78:79], v[174:175] neg_lo:[0,1] neg_hi:[0,1]\n\t"
"v_pk_fma_f32 v[60:61], v[156:157], s[90:91], v[60:61]\n\t"
"v_pk_fma_f32 v[62:63], v[158:159], s[90:91], v[62:63]\n\t"
"v_pk_add_f32 v[156:157], v[60:61], v[156:157] neg_lo:[0,1] neg_hi:[0,1]\n\t"
"v_pk_add_f32 v[158:159], v[62:63], v[158:159] neg_lo:[0,1] neg_hi:[0,1]\n\t"
"v_mfma_f32_16x16x32_bf16 v[128:131], a[136:139], a[144:147], v[128:131]\n\t"
"ds_read_b128 a[172:175], v224 offset:19360\n\t"
"v_mfma_f32_16x16x32_bf16 v[128:131], a[140:143], a[160:163], v[128:131]\n\t"
"buffer_load_dwordx4 a[116:119], off, s[92:95], s56 offset:1024 sc1\n\t"
"v_mfma_f32_16x16x32_bf16 v[132:135], a[136:139], a[148:151], v[132:135]\n\t"
"ds_read_b128 a[156:159], v224 offset:17312\n\t"
"v_accvgpr_read_b32 v224, a212\n\t"
"v_mfma_f32_16x16x32_bf16 v[132:135], a[140:143], a[164:167], v[132:135]\n\t"
"buffer_load_dwordx4 a[108:111], off, s[96:99], s55 offset:3072 sc1\n\t"
"v_mfma_f32_16x16x32_bf16 v[116:119], a[128:131], a[148:151], v[116:119]\n\t"
"ds_read_b64_tr_b16 a[204:205], v251 offset:47232\n\t"
"v_mfma_f32_16x16x32_bf16 v[116:119], a[132:135], a[164:167], v[116:119]\n\t"
"buffer_load_dwordx4 a[104:107], off, s[96:99], s55 offset:2048 sc1\n\t"
"v_mfma_f32_16x16x32_bf16 v[100:103], a[120:123], a[148:151], v[100:103]\n\t"
"v_mfma_f32_16x16x32_bf16 v[100:103], a[124:127], a[164:167], v[100:103]\n\t"
"s_waitcnt lgkmcnt(0)\n\t"
"s_barrier\n\t"
"v_mfma_f32_16x16x32_bf16 v[104:107], a[120:123], a[152:155], v[104:107]\n\t"
"s_add_u32 m0, s26, 145152\n\t"
"ds_read_b128 a[144:147], v224 offset:1024\n\t"
"v_mfma_f32_16x16x32_bf16 v[104:107], a[124:127], a[168:171], v[104:107]\n\t"
"ds_read_b128 a[160:163], v224 offset:3072\n\t"
"v_mfma_f32_16x16x32_bf16 v[120:123], a[128:131], a[152:155], v[120:123]\n\t"
"ds_read_b128 a[148:151], v224 offset:1280\n\t"
"v_mfma_f32_16x16x32_bf16 v[120:123], a[132:135], a[168:171], v[120:123]\n\t"
"ds_read_b128 a[164:167], v224 offset:3328\n\t"
"v_mfma_f32_16x16x32_bf16 v[136:139], a[136:139], a[152:155], v[136:139]\n\t"
"v_mfma_f32_16x16x32_bf16 v[136:139], a[140:143], a[168:171], v[136:139]\n\t"
"buffer_load_dwordx4 v236, s[12:15], s22 offen offset:256 nt lds\n\t"
"v_mfma_f32_16x16x32_bf16 v[140:143], a[136:139], a[156:159], v[140:143]\n\t"
"ds_read_b128 a[152:155], v224 offset:1536\n\t"
"v_mfma_f32_16x16x32_bf16 v[140:143], a[140:143], a[172:175], v[140:143]\n\t"
"ds_read_b128 a[228:231], v254 offset:59904\n\t"
"v_mfma_f32_16x16x32_bf16 v[124:127], a[128:131], a[156:159], v[124:127]\n\t"
"ds_read_b128 a[168:171], v224 offset:3584\n\t"
"v_mfma_f32_16x16x32_bf16 v[124:127], a[132:135], a[172:175], v[124:127]\n\t"
"buffer_load_dwordx4 v237, s[12:15], s22 offen offset:1280 nt lds\n\t"
"v_mfma_f32_16x16x32_bf16 v[108:111], a[120:123], a[156:159], v[108:111]\n\t"
"ds_read_b128 a[156:159], v224 offset:1792\n\t"
"v_mfma_f32_16x16x32_bf16 v[108:111], a[124:127], a[172:175], v[108:111]\n\t"
"buffer_load_dwordx4 a[96:99], off, s[96:99], s55 offset:0 sc1\n\t"
"v_mfma_f32_4x4x4_16b_bf16 v[200:203], a[220:221], a[178:179], 0\n\t"
"ds_read_b128 a[172:175], v224 offset:3840\n\t"
"v_mfma_f32_4x4x4_16b_bf16 v[204:207], a[220:221], a[186:187], 0\n\t"
"ds_read_b64_tr_b16 a[178:179], v250 offset:23040\n\t"
"v_mfma_f32_4x4x4_16b_bf16 v[216:219], a[220:221], a[194:195], 0\n\t"
"ds_read_b64_tr_b16 a[186:187], v251 offset:23040\n\t"
"v_mfma_f32_4x4x4_16b_bf16 v[220:223], a[220:221], a[202:203], 0\n\t"
"buffer_load_dwordx4 v238, s[12:15], s22 offen offset:2304 nt lds\n\t"
"v_mfma_f32_4x4x4_16b_bf16 v[192:195], a[222:223], a[176:177], v[200:203]\n\t"
"ds_read_b64_tr_b16 a[194:195], v248 offset:23040\n\t"
"v_mfma_f32_4x4x4_16b_bf16 v[196:199], a[222:223], a[184:185], v[204:207]\n\t"
"ds_read_b64_tr_b16 a[202:203], v249 offset:23040\n\t"
"v_mfma_f32_4x4x4_16b_bf16 v[200:203], a[222:223], a[180:181], v[200:203]\n\t"
"ds_read_b64_tr_b16 a[176:177], v248 offset:20736\n\t"
"v_mfma_f32_4x4x4_16b_bf16 v[204:207], a[222:223], a[188:189], v[204:207]\n\t"
".p2align 3\n\t"
"ds_read_b64_tr_b16 a[184:185], v249 offset:20736\n\t"
"v_pk_fma_f32 v[0:1], v[96:97], s[90:91], v[0:1]\n\t"
"v_pk_fma_f32 v[2:3], v[98:99], s[90:91], v[2:3]\n\t"
"v_pk_add_f32 v[96:97], v[0:1], v[96:97] neg_lo:[0,1] neg_hi:[0,1]\n\t"
"v_pk_add_f32 v[98:99], v[2:3], v[98:99] neg_lo:[0,1] neg_hi:[0,1]\n\t"
"v_pk_fma_f32 v[16:17], v[112:113], s[90:91], v[16:17]\n\t"
"v_pk_fma_f32 v[18:19], v[114:115], s[90:91], v[18:19]\n\t"
"v_pk_add_f32 v[112:113], v[16:17], v[112:113] neg_lo:[0,1] neg_hi:[0,1]\n\t"
"v_pk_add_f32 v[114:115], v[18:19], v[114:115] neg_lo:[0,1] neg_hi:[0,1]\n\t"
"v_pk_fma_f32 v[32:33], v[128:129], s[90:91], v[32:33]\n\t"
"v_pk_fma_f32 v[34:35], v[130:131], s[90:91], v[34:35]\n\t"
"v_pk_add_f32 v[128:129], v[32:33], v[128:129] neg_lo:[0,1] neg_hi:[0,1]\n\t"
"v_pk_add_f32 v[130:131], v[34:35], v[130:131] neg_lo:[0,1] neg_hi:[0,1]\n\t"
"v_pk_fma_f32 v[36:37], v[132:133], s[90:91], v[36:37]\n\t"
"v_pk_fma_f32 v[38:39], v[134:135], s[90:91], v[38:39]\n\t"
"v_pk_add_f32 v[132:133], v[36:37], v[132:133] neg_lo:[0,1] neg_hi:[0,1]\n\t"
"v_pk_add_f32 v[134:135], v[38:39], v[134:135] neg_lo:[0,1] neg_hi:[0,1]\n\t"
"v_pk_fma_f32 v[20:21], v[116:117], s[90:91], v[20:21]\n\t"
"v_pk_fma_f32 v[22:23], v[118:119], s[90:91], v[22:23]\n\t"
"v_pk_add_f32 v[116:117], v[20:21], v[116:117] neg_lo:[0,1] neg_hi:[0,1]\n\t"
"v_pk_add_f32 v[118:119], v[22:23], v[118:119] neg_lo:[0,1] neg_hi:[0,1]\n\t"
"v_pk_fma_f32 v[4:5], v[100:101], s[90:91], v[4:5]\n\t"
"v_pk_fma_f32 v[6:7], v[102:103], s[90:91], v[6:7]\n\t"
"v_pk_add_f32 v[100:101], v[4:5], v[100:101] neg_lo:[0,1] neg_hi:[0,1]\n\t"
"v_pk_add_f32 v[102:103], v[6:7], v[102:103] neg_lo:[0,1] neg_hi:[0,1]\n\t"
"v_pk_fma_f32 v[8:9], v[104:105], s[90:91], v[8:9]\n\t"
"v_pk_fma_f32 v[10:11], v[106:107], s[90:91], v[10:11]\n\t"
"v_pk_add_f32 v[104:105], v[8:9], v[104:105] neg_lo:[0,1] neg_hi:[0,1]\n\t"
"v_pk_add_f32 v[106:107], v[10:11], v[106:107] neg_lo:[0,1] neg_hi:[0,1]\n\t"
"v_pk_fma_f32 v[24:25], v[120:121], s[90:91], v[24:25]\n\t"
"v_pk_fma_f32 v[26:27], v[122:123], s[90:91], v[26:27]\n\t"
"v_pk_add_f32 v[120:121], v[24:25], v[120:121] neg_lo:[0,1] neg_hi:[0,1]\n\t"
"v_pk_add_f32 v[122:123], v[26:27], v[122:123] neg_lo:[0,1] neg_hi:[0,1]\n\t"
"v_pk_fma_f32 v[40:41], v[136:137], s[90:91], v[40:41]\n\t"
"v_pk_fma_f32 v[42:43], v[138:139], s[90:91], v[42:43]\n\t"
"v_pk_add_f32 v[136:137], v[40:41], v[136:137] neg_lo:[0,1] neg_hi:[0,1]\n\t"
"v_pk_add_f32 v[138:139], v[42:43], v[138:139] neg_lo:[0,1] neg_hi:[0,1]\n\t"
"v_pk_fma_f32 v[44:45], v[140:141], s[90:91], v[44:45]\n\t"
"v_pk_fma_f32 v[46:47], v[142:143], s[90:91], v[46:47]\n\t"
"v_pk_add_f32 v[140:141], v[44:45], v[140:141] neg_lo:[0,1] neg_hi:[0,1]\n\t"
"v_pk_add_f32 v[142:143], v[46:47], v[142:143] neg_lo:[0,1] neg_hi:[0,1]\n\t"
"v_pk_fma_f32 v[28:29], v[124:125], s[90:91], v[28:29]\n\t"
"v_pk_fma_f32 v[30:31], v[126:127], s[90:91], v[30:31]\n\t"
"v_pk_add_f32 v[124:125], v[28:29], v[124:125] neg_lo:[0,1] neg_hi:[0,1]\n\t"
"v_pk_add_f32 v[126:127], v[30:31], v[126:127] neg_lo:[0,1] neg_hi:[0,1]\n\t"
"v_pk_fma_f32 v[12:13], v[108:109], s[90:91], v[12:13]\n\t"
"v_pk_fma_f32 v[14:15], v[110:111], s[90:91], v[14:15]\n\t"
"v_pk_add_f32 v[108:109], v[12:13], v[108:109] neg_lo:[0,1] neg_hi:[0,1]\n\t"
"v_pk_add_f32 v[110:111], v[14:15], v[110:111] neg_lo:[0,1] neg_hi:[0,1]\n\t"
"v_mfma_f32_4x4x4_16b_bf16 v[208:211], a[222:223], a[192:193], v[216:219]\n\t"
"ds_read_b64_tr_b16 a[180:181], v248 offset:25344\n\t"
"v_mfma_f32_4x4x4_16b_bf16 v[212:215], a[222:223], a[200:201], v[220:223]\n\t"
"ds_read_b64_tr_b16 a[188:189], v249 offset:25344\n\t"
"v_mfma_f32_4x4x4_16b_bf16 v[216:219], a[222:223], a[196:197], v[216:219]\n\t"
"ds_read_b64_tr_b16 a[192:193], v250 offset:20736\n\t"
"v_mfma_f32_4x4x4_16b_bf16 v[220:223], a[222:223], a[204:205], v[220:223]\n\t"
"s_waitcnt vmcnt(26) lgkmcnt(8)\n\t"
"buffer_load_dwordx4 v239, s[12:15], s22 offen offset:3328 nt lds\n\t"
"v_mfma_f32_16x16x32_bf16 v[48:51], a[0:3], a[144:147], v[48:51]\n\t"
"v_cvt_pk_bf16_f32 v193, -v193, -v197\n\t"
"v_mfma_f32_16x16x32_bf16 v[48:51], a[4:7], a[160:163], v[48:51]\n\t"
"ds_write_b128 v255, a[228:231] offset:41472\n\t"
"v_mfma_f32_16x16x32_bf16 v[64:67], a[8:11], a[144:147], v[64:67]\n\t"
"v_mfma_f32_16x16x32_bf16 v[64:67], a[12:15], a[160:163], v[64:67]\n\t"
"s_mov_b64 exec, s[36:37]\n\t"
"buffer_load_dwordx4 v240, s[12:15], s22 offen offset:3840 nt lds\n\t"
"s_mov_b64 exec, -1\n\t"
"v_mfma_f32_16x16x32_bf16 v[80:83], a[16:19], a[144:147], v[80:83]\n\t"
"s_lshl_b32 m0, s73, 8\n\t"
"v_cvt_pk_bf16_f32 v211, -v211, -v215\n\t"
"v_mfma_f32_16x16x32_bf16 v[80:83], a[20:23], a[160:163], v[80:83]\n\t"
"ds_read_b128 a[160:163], v224 offset:2048\n\t"
"v_cvt_pk_bf16_f32 v203, v203, v207\n\t"
"v_mfma_f32_16x16x32_bf16 v[84:87], a[16:19], a[148:151], v[84:87]\n\t"
"ds_read_b128 a[144:147], v224 offset:0\n\t"
"v_cvt_pk_bf16_f32 v216, v216, v220\n\t"
"v_mfma_f32_16x16x32_bf16 v[84:87], a[20:23], a[164:167], v[84:87]\n\t"
"buffer_load_dwordx4 a[100:103], off, s[96:99], s55 offset:1024 sc1\n\t"
"v_mfma_f32_16x16x32_bf16 v[68:71], a[8:11], a[148:151], v[68:71]\n\t"
"ds_read_b64_tr_b16 a[200:201], v251 offset:20736\n\t"
"v_cvt_pk_bf16_f32 v192, -v192, -v196\n\t"
"v_mfma_f32_16x16x32_bf16 v[68:71], a[12:15], a[164:167], v[68:71]\n\t"
"v_cvt_pk_bf16_f32 v210, -v210, -v214\n\t"
"v_cvt_pk_bf16_f32 v217, v217, v221\n\t"
"v_mfma_f32_16x16x32_bf16 v[52:55], a[0:3], a[148:151], v[52:55]\n\t"
"ds_read_b128 a[148:151], v224 offset:256\n\t"
"v_mfma_f32_16x16x32_bf16 v[52:55], a[4:7], a[164:167], v[52:55]\n\t"
"ds_read_b128 a[164:167], v224 offset:2304\n\t"
"v_cvt_pk_bf16_f32 v208, -v208, -v212\n\t"
"v_mfma_f32_16x16x32_bf16 v[56:59], a[0:3], a[152:155], v[56:59]\n\t"
"ds_write_addtid_b32 v211 offset:27936\n\t"
"v_cvt_pk_bf16_f32 v201, v201, v205\n\t"
"v_mfma_f32_16x16x32_bf16 v[56:59], a[4:7], a[168:171], v[56:59]\n\t"
"ds_write_addtid_b32 v208 offset:18592\n\t"
"v_cvt_pk_bf16_f32 v195, -v195, -v199\n\t"
"v_mfma_f32_16x16x32_bf16 v[72:75], a[8:11], a[152:155], v[72:75]\n\t"
"ds_write_addtid_b32 v203 offset:30016\n\t"
"v_cvt_pk_bf16_f32 v200, v200, v204\n\t"
"v_mfma_f32_16x16x32_bf16 v[72:75], a[12:15], a[168:171], v[72:75]\n\t"
"ds_write_addtid_b32 v210 offset:19616\n\t"
"v_cvt_pk_bf16_f32 v194, -v194, -v198\n\t"
"v_mfma_f32_16x16x32_bf16 v[88:91], a[16:19], a[152:155], v[88:91]\n\t"
"ds_read_b128 a[152:155], v224 offset:512\n\t"
"v_cvt_pk_bf16_f32 v202, v202, v206\n\t"
"v_mfma_f32_16x16x32_bf16 v[88:91], a[20:23], a[168:171], v[88:91]\n\t"
"ds_write_addtid_b32 v201 offset:28992\n\t"
"v_cvt_pk_bf16_f32 v218, v218, v222\n\t"
"v_mfma_f32_16x16x32_bf16 v[92:95], a[16:19], a[156:159], v[92:95]\n\t"
"ds_write_addtid_b32 v192 offset:16544\n\t"
"v_mfma_f32_16x16x32_bf16 v[92:95], a[20:23], a[172:175], v[92:95]\n\t"
"ds_write_addtid_b32 v195 offset:25888\n\t"
"v_cvt_pk_bf16_f32 v219, v219, v223\n\t"
"v_mfma_f32_16x16x32_bf16 v[76:79], a[8:11], a[156:159], v[76:79]\n\t"
"s_nop 0\n\t"
"ds_write_addtid_b32 v202 offset:21696\n\t"
"ds_write_addtid_b32 v216 offset:22720\n\t"
"v_mfma_f32_16x16x32_bf16 v[76:79], a[12:15], a[172:175], v[76:79]\n\t"
"ds_write_addtid_b32 v217 offset:31040\n\t"
"v_mfma_f32_16x16x32_bf16 v[60:63], a[0:3], a[156:159], v[60:63]\n\t"
"ds_write_addtid_b32 v194 offset:17568\n\t"
"v_cvt_pk_bf16_f32 v209, -v209, -v213\n\t"
"v_mfma_f32_16x16x32_bf16 v[60:63], a[4:7], a[172:175], v[60:63]\n\t"
"s_waitcnt vmcnt(23) lgkmcnt(10)\n\t"
"ds_write_addtid_b32 v218 offset:23744\n\t"
"v_mfma_f32_16x16x32_bf16 v[0:3], a[24:27], a[144:147], v[0:3]\n\t"
"s_nop 0\n\t"
"ds_write_addtid_b32 v193 offset:24864\n\t"
"ds_write_addtid_b32 v219 offset:32064\n\t"
"v_mfma_f32_16x16x32_bf16 v[0:3], a[28:31], a[160:163], v[0:3]\n\t"
"ds_write_addtid_b32 v209 offset:26912\n\t"
"v_mfma_f32_16x16x32_bf16 v[16:19], a[32:35], a[144:147], v[16:19]\n\t"
"ds_write_addtid_b32 v200 offset:20672\n\t"
"v_mfma_f32_16x16x32_bf16 v[16:19], a[36:39], a[160:163], v[16:19]\n\t"
"ds_read_b128 a[172:175], v224 offset:2816\n\t"
"v_mfma_f32_16x16x32_bf16 v[32:35], a[40:43], a[144:147], v[32:35]\n\t"
"ds_read_b128 a[156:159], v224 offset:768\n\t"
"v_mfma_f32_16x16x32_bf16 v[32:35], a[44:47], a[160:163], v[32:35]\n\t"
"v_mfma_f32_16x16x32_bf16 v[36:39], a[40:43], a[148:151], v[36:39]\n\t"
"ds_read_b128 a[168:171], v224 offset:2560\n\t"
"v_mfma_f32_16x16x32_bf16 v[36:39], a[44:47], a[164:167], v[36:39]\n\t"
"ds_read_b64_tr_b16 a[196:197], v250 offset:25344\n\t"
"v_mfma_f32_16x16x32_bf16 v[20:23], a[32:35], a[148:151], v[20:23]\n\t"
"ds_read_b64_tr_b16 a[204:205], v251 offset:25344\n\t"
"v_mfma_f32_16x16x32_bf16 v[20:23], a[36:39], a[164:167], v[20:23]\n\t"
"v_mfma_f32_16x16x32_bf16 v[4:7], a[24:27], a[148:151], v[4:7]\n\t"
"s_waitcnt lgkmcnt(2)\n\t"
"v_mfma_f32_16x16x32_bf16 v[4:7], a[28:31], a[164:167], v[4:7]\n\t"
"s_barrier\n\t"
"v_mfma_f32_16x16x32_bf16 v[8:11], a[24:27], a[152:155], v[8:11]\n\t"
"s_nop 0\n\t"
"ds_read_b128 a[144:147], v224 offset:17568\n\t"
"ds_read_b128 a[148:151], v224 offset:17824\n\t"
"v_mfma_f32_16x16x32_bf16 v[8:11], a[28:31], a[168:171], v[8:11]\n\t"
"buffer_load_dwordx4 a[136:139], off, s[92:95], s55 offset:2048 sc1\n\t"
"v_mfma_f32_16x16x32_bf16 v[24:27], a[32:35], a[152:155], v[24:27]\n\t"
"ds_read_b128 a[160:163], v224 offset:19616\n\t"
"v_mfma_f32_16x16x32_bf16 v[24:27], a[36:39], a[168:171], v[24:27]\n\t"
"ds_read_b128 a[164:167], v224 offset:19872\n\t"
"v_mfma_f32_16x16x32_bf16 v[40:43], a[40:43], a[152:155], v[40:43]\n\t"
"v_mfma_f32_16x16x32_bf16 v[40:43], a[44:47], a[168:171], v[40:43]\n\t"
"buffer_load_dwordx4 a[140:143], off, s[92:95], s55 offset:3072 sc1\n\t"
"v_mfma_f32_16x16x32_bf16 v[44:47], a[40:43], a[156:159], v[44:47]\n\t"
"ds_read_b128 a[152:155], v224 offset:18080\n\t"
"v_mfma_f32_16x16x32_bf16 v[44:47], a[44:47], a[172:175], v[44:47]\n\t"
"buffer_load_dwordx4 a[132:135], off, s[92:95], s55 offset:1024 sc1\n\t"
"v_mfma_f32_16x16x32_bf16 v[28:31], a[32:35], a[156:159], v[28:31]\n\t"
"ds_read_b128 a[168:171], v224 offset:20128\n\t"
"v_mfma_f32_16x16x32_bf16 v[28:31], a[36:39], a[172:175], v[28:31]\n\t"
"v_mfma_f32_16x16x32_bf16 v[12:15], a[24:27], a[156:159], v[12:15]\n\t"
"ds_read_b128 a[156:159], v224 offset:18336\n\t"
"v_mfma_f32_16x16x32_bf16 v[12:15], a[28:31], a[172:175], v[12:15]\n\t"
"s_waitcnt lgkmcnt(5)\n\t"
"buffer_load_dwordx4 a[128:131], off, s[92:95], s55 offset:0 sc1\n\t"
"v_mfma_f32_4x4x4_16b_bf16 v[200:203], a[220:221], a[178:179], 0\n\t"
"ds_read_b128 a[172:175], v224 offset:20384\n\t"
"v_mfma_f32_4x4x4_16b_bf16 v[204:207], a[220:221], a[186:187], 0\n\t"
"ds_read_b64_tr_b16 a[178:179], v250 offset:2304\n\t"
"v_mfma_f32_4x4x4_16b_bf16 v[216:219], a[220:221], a[194:195], 0\n\t"
"ds_read_b64_tr_b16 a[186:187], v251 offset:2304\n\t"
"v_mfma_f32_4x4x4_16b_bf16 v[220:223], a[220:221], a[202:203], 0\n\t"
"v_mfma_f32_4x4x4_16b_bf16 v[192:195], a[222:223], a[176:177], v[200:203]\n\t"
"ds_read_b64_tr_b16 a[194:195], v248 offset:2304\n\t"
"v_mfma_f32_4x4x4_16b_bf16 v[196:199], a[222:223], a[184:185], v[204:207]\n\t"
"ds_read_b64_tr_b16 a[202:203], v249 offset:2304\n\t"
"v_mfma_f32_4x4x4_16b_bf16 v[200:203], a[222:223], a[180:181], v[200:203]\n\t"
"ds_read_b64_tr_b16 a[176:177], v248 offset:0\n\t"
"v_mfma_f32_4x4x4_16b_bf16 v[204:207], a[222:223], a[188:189], v[204:207]\n\t"
"v_mfma_f32_4x4x4_16b_bf16 v[208:211], a[222:223], a[192:193], v[216:219]\n\t"
"ds_read_b64_tr_b16 a[184:185], v249 offset:0\n\t"
"v_mfma_f32_4x4x4_16b_bf16 v[212:215], a[222:223], a[200:201], v[220:223]\n\t"
"ds_read_b64_tr_b16 a[180:181], v248 offset:4608\n\t"
"v_mfma_f32_4x4x4_16b_bf16 v[216:219], a[222:223], a[196:197], v[216:219]\n\t"
"ds_read_b64_tr_b16 a[188:189], v249 offset:4608\n\t"
"v_mfma_f32_4x4x4_16b_bf16 v[220:223], a[222:223], a[204:205], v[220:223]\n\t"
"s_waitcnt vmcnt(21) lgkmcnt(7)\n\t"
"buffer_load_dwordx4 a[120:123], off, s[96:99], s54 offset:2048 sc1\n\t"
"v_mfma_f32_16x16x32_bf16 v[144:147], a[48:51], a[144:147], v[144:147]\n\t"
"ds_read_b64_tr_b16 a[192:193], v250 offset:0\n\t"
"v_cvt_pk_bf16_f32 v194, -v194, -v198\n\t"
"v_mfma_f32_16x16x32_bf16 v[144:147], a[52:55], a[160:163], v[144:147]\n\t"
"buffer_load_dwordx4 a[124:127], off, s[96:99], s54 offset:3072 sc1\n\t"
"v_mfma_f32_16x16x32_bf16 v[160:163], a[56:59], a[144:147], v[160:163]\n\t"
"ds_read_b64_tr_b16 a[200:201], v251 offset:0\n\t"
"v_cvt_pk_bf16_f32 v193, -v193, -v197\n\t"
"v_mfma_f32_16x16x32_bf16 v[160:163], a[60:63], a[160:163], v[160:163]\n\t"
"buffer_load_dwordx4 a[16:19], off, s[92:95], s41 offset:0 sc1\n\t"
"v_mfma_f32_16x16x32_bf16 v[176:179], a[64:67], a[144:147], v[176:179]\n\t"
"ds_read_b128 a[144:147], v224 offset:16544\n\t"
"v_cvt_pk_bf16_f32 v200, v200, v204\n\t"
"v_mfma_f32_16x16x32_bf16 v[176:179], a[68:71], a[160:163], v[176:179]\n\t"
"ds_write_addtid_b32 v194 offset:1024\n\t"
"v_cvt_pk_bf16_f32 v202, v202, v206\n\t"
"v_mfma_f32_16x16x32_bf16 v[180:183], a[64:67], a[148:151], v[180:183]\n\t"
"ds_read_b128 a[160:163], v224 offset:18592\n\t"
"v_cvt_pk_bf16_f32 v216, v216, v220\n\t"
"v_mfma_f32_16x16x32_bf16 v[180:183], a[68:71], a[164:167], v[180:183]\n\t"
"buffer_load_dwordx4 a[20:23], off, s[92:95], s41 offset:1024 sc1\n\t"
"v_mfma_f32_16x16x32_bf16 v[164:167], a[56:59], a[148:151], v[164:167]\n\t"
"ds_write_addtid_b32 v193 offset:8320\n\t"
"v_cvt_pk_bf16_f32 v219, v219, v223\n\t"
"v_mfma_f32_16x16x32_bf16 v[164:167], a[60:63], a[164:167], v[164:167]\n\t"
"ds_write_addtid_b32 v202 offset:5152\n\t"
"v_cvt_pk_bf16_f32 v192, -v192, -v196\n\t"
"v_mfma_f32_16x16x32_bf16 v[148:151], a[48:51], a[148:151], v[148:151]\n\t"
"ds_read_b128 a[148:151], v224 offset:16800\n\t"
"v_cvt_pk_bf16_f32 v211, -v211, -v215\n\t"
"v_mfma_f32_16x16x32_bf16 v[148:151], a[52:55], a[164:167], v[148:151]\n\t"
"buffer_load_dwordx4 a[12:15], off, s[96:99], s40 offset:3072 sc1\n\t"
"v_mfma_f32_16x16x32_bf16 v[152:155], a[48:51], a[152:155], v[152:155]\n\t"
"ds_read_b128 a[164:167], v224 offset:18848\n\t"
"v_cvt_pk_bf16_f32 v209, -v209, -v213\n\t"
"v_mfma_f32_16x16x32_bf16 v[152:155], a[52:55], a[168:171], v[152:155]\n\t"
"ds_write_addtid_b32 v209 offset:10368\n\t"
"v_mfma_f32_16x16x32_bf16 v[168:171], a[56:59], a[152:155], v[168:171]\n\t"
"ds_write_addtid_b32 v216 offset:6176\n\t"
"v_cvt_pk_bf16_f32 v201, v201, v205\n\t"
"v_mfma_f32_16x16x32_bf16 v[168:171], a[60:63], a[168:171], v[168:171]\n\t"
"buffer_load_dwordx4 a[8:11], off, s[96:99], s40 offset:2048 sc1\n\t"
"v_mfma_f32_16x16x32_bf16 v[184:187], a[64:67], a[152:155], v[184:187]\n\t"
"ds_read_b128 a[152:155], v224 offset:17056\n\t"
"v_cvt_pk_bf16_f32 v210, -v210, -v214\n\t"
"v_mfma_f32_16x16x32_bf16 v[184:187], a[68:71], a[168:171], v[184:187]\n\t"
"ds_read_b128 a[168:171], v224 offset:19104\n\t"
"v_cvt_pk_bf16_f32 v217, v217, v221\n\t"
"v_mfma_f32_16x16x32_bf16 v[188:191], a[64:67], a[156:159], v[188:191]\n\t"
"ds_write_addtid_b32 v217 offset:14496\n\t"
"v_cvt_pk_bf16_f32 v208, -v208, -v212\n\t"
"v_mfma_f32_16x16x32_bf16 v[188:191], a[68:71], a[172:175], v[188:191]\n\t"
"buffer_load_dwordx4 a[0:3], off, s[96:99], s40 offset:0 sc1\n\t"
"v_mfma_f32_16x16x32_bf16 v[172:175], a[56:59], a[156:159], v[172:175]\n\t"
"s_nop 0\n\t"
"ds_write_addtid_b32 v200 offset:4128\n\t"
"ds_write_addtid_b32 v208 offset:2048\n\t"
"v_mfma_f32_16x16x32_bf16 v[172:175], a[60:63], a[172:175], v[172:175]\n\t"
"ds_write_addtid_b32 v211 offset:11392\n\t"
"v_cvt_pk_bf16_f32 v195, -v195, -v199\n\t"
"v_mfma_f32_16x16x32_bf16 v[156:159], a[48:51], a[156:159], v[156:159]\n\t"
"ds_write_addtid_b32 v195 offset:9344\n\t"
"v_cvt_pk_bf16_f32 v203, v203, v207\n\t"
"v_mfma_f32_16x16x32_bf16 v[156:159], a[52:55], a[172:175], v[156:159]\n\t"
"s_waitcnt vmcnt(22) lgkmcnt(8)\n\t"
"ds_write_addtid_b32 v201 offset:12448\n\t"
"v_cvt_pk_bf16_f32 v218, v218, v222\n\t"
"v_mfma_f32_16x16x32_bf16 v[96:99], a[72:75], a[144:147], v[96:99]\n\t"
"s_nop 0\n\t"
"ds_write_addtid_b32 v210 offset:3072\n\t"
"ds_write_addtid_b32 v218 offset:7200\n\t"
"v_mfma_f32_16x16x32_bf16 v[96:99], a[76:79], a[160:163], v[96:99]\n\t"
"ds_write_addtid_b32 v192 offset:0\n\t"
"v_mfma_f32_16x16x32_bf16 v[112:115], a[80:83], a[144:147], v[112:115]\n\t"
"s_nop 0\n\t"
"ds_write_addtid_b32 v203 offset:13472\n\t"
"ds_write_addtid_b32 v219 offset:15520\n\t"
"v_mfma_f32_16x16x32_bf16 v[112:115], a[84:87], a[160:163], v[112:115]\n\t"
"buffer_load_dwordx4 a[4:7], off, s[96:99], s40 offset:1024 sc1\n\t"
"v_mfma_f32_16x16x32_bf16 v[128:131], a[88:91], a[144:147], v[128:131]\n\t"
"ds_read_b128 a[156:159], v224 offset:17312\n\t"
"v_mfma_f32_16x16x32_bf16 v[128:131], a[92:95], a[160:163], v[128:131]\n\t"
"buffer_load_dwordx4 a[40:43], off, s[92:95], s40 offset:2048 sc1\n\t"
"v_mfma_f32_16x16x32_bf16 v[132:135], a[88:91], a[148:151], v[132:135]\n\t"
"ds_read_b128 a[172:175], v224 offset:19360\n\t"
"v_mfma_f32_16x16x32_bf16 v[132:135], a[92:95], a[164:167], v[132:135]\n\t"
"ds_read_b64_tr_b16 a[196:197], v250 offset:4608\n\t"
"v_mfma_f32_16x16x32_bf16 v[116:119], a[80:83], a[148:151], v[116:119]\n\t"
"ds_read_b64_tr_b16 a[204:205], v251 offset:4608\n\t"
"v_mfma_f32_16x16x32_bf16 v[116:119], a[84:87], a[164:167], v[116:119]\n\t"
"buffer_load_dwordx4 a[44:47], off, s[92:95], s40 offset:3072 sc1\n\t"
"v_mfma_f32_16x16x32_bf16 v[100:103], a[72:75], a[148:151], v[100:103]\n\t"
"s_waitcnt lgkmcnt(2)\n\t"
"v_mfma_f32_16x16x32_bf16 v[100:103], a[76:79], a[164:167], v[100:103]\n\t"
"buffer_load_dwordx4 a[36:39], off, s[92:95], s40 offset:1024 sc1\n\t"
"s_barrier\n\t"
"v_mfma_f32_16x16x32_bf16 v[104:107], a[72:75], a[152:155], v[104:107]\n\t"
"ds_read_b128 a[144:147], v224 offset:1024\n\t"
"v_mfma_f32_16x16x32_bf16 v[104:107], a[76:79], a[168:171], v[104:107]\n\t"
"ds_read_b128 a[160:163], v224 offset:3072\n\t"
"v_mfma_f32_16x16x32_bf16 v[120:123], a[80:83], a[152:155], v[120:123]\n\t"
"ds_read_b128 a[148:151], v224 offset:1280\n\t"
"v_mfma_f32_16x16x32_bf16 v[120:123], a[84:87], a[168:171], v[120:123]\n\t"
"ds_read_b128 a[164:167], v224 offset:3328\n\t"
"v_mfma_f32_16x16x32_bf16 v[136:139], a[88:91], a[152:155], v[136:139]\n\t"
"v_mfma_f32_16x16x32_bf16 v[136:139], a[92:95], a[168:171], v[136:139]\n\t"
"buffer_load_dwordx4 a[32:35], off, s[92:95], s40 offset:0 sc1\n\t"
"v_mfma_f32_16x16x32_bf16 v[140:143], a[88:91], a[156:159], v[140:143]\n\t"
"ds_read_b128 a[152:155], v224 offset:1536\n\t"
"v_mfma_f32_16x16x32_bf16 v[140:143], a[92:95], a[172:175], v[140:143]\n\t"
"ds_read_b128 a[168:171], v224 offset:3584\n\t"
"v_mfma_f32_16x16x32_bf16 v[124:127], a[80:83], a[156:159], v[124:127]\n\t"
"v_mfma_f32_16x16x32_bf16 v[124:127], a[84:87], a[172:175], v[124:127]\n\t"
"buffer_load_dwordx4 a[24:27], off, s[96:99], 0 offset:2048 sc1\n\t"
"v_mfma_f32_16x16x32_bf16 v[108:111], a[72:75], a[156:159], v[108:111]\n\t"
"ds_read_b128 a[156:159], v224 offset:1792\n\t"
"v_mfma_f32_16x16x32_bf16 v[108:111], a[76:79], a[172:175], v[108:111]\n\t"
"s_waitcnt lgkmcnt(5)\n\t"
"ds_read_b128 a[172:175], v224 offset:3840\n\t"
"v_mfma_f32_4x4x4_16b_bf16 v[200:203], a[220:221], a[178:179], 0\n\t"
"v_mfma_f32_4x4x4_16b_bf16 v[204:207], a[220:221], a[186:187], 0\n\t"
"ds_read_b64_tr_b16 a[178:179], v250 offset:24192\n\t"
"v_mfma_f32_4x4x4_16b_bf16 v[216:219], a[220:221], a[194:195], 0\n\t"
"ds_read_b64_tr_b16 a[186:187], v251 offset:24192\n\t"
"v_mfma_f32_4x4x4_16b_bf16 v[220:223], a[220:221], a[202:203], 0\n\t"
"ds_read_b64_tr_b16 a[194:195], v248 offset:24192\n\t"
"v_mfma_f32_4x4x4_16b_bf16 v[192:195], a[222:223], a[176:177], v[200:203]\n\t"
"ds_read_b64_tr_b16 a[202:203], v249 offset:24192\n\t"
"v_mfma_f32_4x4x4_16b_bf16 v[196:199], a[222:223], a[184:185], v[204:207]\n\t"
"ds_read_b64_tr_b16 a[176:177], v248 offset:21888\n\t"
"v_mfma_f32_4x4x4_16b_bf16 v[200:203], a[222:223], a[180:181], v[200:203]\n\t"
"v_mfma_f32_4x4x4_16b_bf16 v[204:207], a[222:223], a[188:189], v[204:207]\n\t"
"ds_read_b64_tr_b16 a[184:185], v249 offset:21888\n\t"
"v_mfma_f32_4x4x4_16b_bf16 v[208:211], a[222:223], a[192:193], v[216:219]\n\t"
"ds_read_b64_tr_b16 a[180:181], v248 offset:26496\n\t"
"v_mfma_f32_4x4x4_16b_bf16 v[212:215], a[222:223], a[200:201], v[220:223]\n\t"
"ds_read_b64_tr_b16 a[188:189], v249 offset:26496\n\t"
"v_mfma_f32_4x4x4_16b_bf16 v[216:219], a[222:223], a[196:197], v[216:219]\n\t"
"ds_read_b64_tr_b16 a[192:193], v250 offset:21888\n\t"
"v_mfma_f32_4x4x4_16b_bf16 v[220:223], a[222:223], a[204:205], v[220:223]\n\t"
"s_waitcnt vmcnt(17) lgkmcnt(8)\n\t"
"buffer_load_dwordx4 a[28:31], off, s[96:99], 0 offset:3072 sc1\n\t"
"v_mfma_f32_16x16x32_bf16 v[48:51], a[96:99], a[144:147], v[48:51]\n\t"
"ds_read_b64_tr_b16 a[200:201], v251 offset:21888\n\t"
"v_cvt_pk_bf16_f32 v193, -v193, -v197\n\t"
"v_mfma_f32_16x16x32_bf16 v[48:51], a[100:103], a[160:163], v[48:51]\n\t"
"buffer_load_dwordx4 a[64:67], off, s[92:95], s68 offset:0 sc1\n\t"
"v_mfma_f32_16x16x32_bf16 v[64:67], a[104:107], a[144:147], v[64:67]\n\t"
"ds_write_addtid_b32 v193 offset:24864\n\t"
"v_cvt_pk_bf16_f32 v192, -v192, -v196\n\t"
"v_mfma_f32_16x16x32_bf16 v[64:67], a[108:111], a[160:163], v[64:67]\n\t"
"buffer_load_dwordx4 a[68:71], off, s[92:95], s68 offset:1024 sc1\n\t"
"v_mfma_f32_16x16x32_bf16 v[80:83], a[112:115], a[144:147], v[80:83]\n\t"
"ds_read_b128 a[144:147], v224 offset:0\n\t"
"v_cvt_pk_bf16_f32 v208, -v208, -v212\n\t"
"v_mfma_f32_16x16x32_bf16 v[80:83], a[116:119], a[160:163], v[80:83]\n\t"
"ds_write_addtid_b32 v192 offset:16544\n\t"
"v_cvt_pk_bf16_f32 v211, -v211, -v215\n\t"
"v_mfma_f32_16x16x32_bf16 v[84:87], a[112:115], a[148:151], v[84:87]\n\t"
"ds_read_b128 a[160:163], v224 offset:2048\n\t"
"v_cvt_pk_bf16_f32 v195, -v195, -v199\n\t"
"v_mfma_f32_16x16x32_bf16 v[84:87], a[116:119], a[164:167], v[84:87]\n\t"
"buffer_load_dwordx4 a[60:63], off, s[96:99], s67 offset:3072 sc1\n\t"
"v_mfma_f32_16x16x32_bf16 v[68:71], a[104:107], a[148:151], v[68:71]\n\t"
"ds_write_addtid_b32 v195 offset:25888\n\t"
"v_cvt_pk_bf16_f32 v200, v200, v204\n\t"
"v_mfma_f32_16x16x32_bf16 v[68:71], a[108:111], a[164:167], v[68:71]\n\t"
"ds_write_addtid_b32 v200 offset:20672\n\t"
"v_cvt_pk_bf16_f32 v218, v218, v222\n\t"
"v_mfma_f32_16x16x32_bf16 v[52:55], a[96:99], a[148:151], v[52:55]\n\t"
"ds_read_b128 a[148:151], v224 offset:256\n\t"
"v_cvt_pk_bf16_f32 v210, -v210, -v214\n\t"
"v_mfma_f32_16x16x32_bf16 v[52:55], a[100:103], a[164:167], v[52:55]\n\t"
"buffer_load_dwordx4 a[56:59], off, s[96:99], s67 offset:2048 sc1\n\t"
"v_mfma_f32_16x16x32_bf16 v[56:59], a[96:99], a[152:155], v[56:59]\n\t"
"ds_read_b128 a[164:167], v224 offset:2304\n\t"
"v_cvt_pk_bf16_f32 v194, -v194, -v198\n\t"
"v_mfma_f32_16x16x32_bf16 v[56:59], a[100:103], a[168:171], v[56:59]\n\t"
"ds_write_addtid_b32 v218 offset:23744\n\t"
"v_cvt_pk_bf16_f32 v216, v216, v220\n\t"
"v_mfma_f32_16x16x32_bf16 v[72:75], a[104:107], a[152:155], v[72:75]\n\t"
"ds_write_addtid_b32 v210 offset:19616\n\t"
"v_cvt_pk_bf16_f32 v201, v201, v205\n\t"
"v_mfma_f32_16x16x32_bf16 v[72:75], a[108:111], a[168:171], v[72:75]\n\t"
"buffer_load_dwordx4 a[48:51], off, s[96:99], s67 offset:0 sc1\n\t"
"v_mfma_f32_16x16x32_bf16 v[88:91], a[112:115], a[152:155], v[88:91]\n\t"
"ds_read_b128 a[152:155], v224 offset:512\n\t"
"v_cvt_pk_bf16_f32 v217, v217, v221\n\t"
"v_mfma_f32_16x16x32_bf16 v[88:91], a[116:119], a[168:171], v[88:91]\n\t"
"ds_read_b128 a[168:171], v224 offset:2560\n\t"
"v_cvt_pk_bf16_f32 v203, v203, v207\n\t"
"v_mfma_f32_16x16x32_bf16 v[92:95], a[112:115], a[156:159], v[92:95]\n\t"
"ds_write_addtid_b32 v208 offset:18592\n\t"
"v_cvt_pk_bf16_f32 v209, -v209, -v213\n\t"
"v_mfma_f32_16x16x32_bf16 v[92:95], a[116:119], a[172:175], v[92:95]\n\t"
"buffer_load_dwordx4 a[52:55], off, s[96:99], s67 offset:1024 sc1\n\t"
"v_mfma_f32_16x16x32_bf16 v[76:79], a[104:107], a[156:159], v[76:79]\n\t"
"ds_write_addtid_b32 v209 offset:26912\n\t"
"v_cvt_pk_bf16_f32 v202, v202, v206\n\t"
"v_mfma_f32_16x16x32_bf16 v[76:79], a[108:111], a[172:175], v[76:79]\n\t"
"s_nop 0\n\t"
"ds_write_addtid_b32 v202 offset:21696\n\t"
"ds_write_addtid_b32 v217 offset:31040\n\t"
"v_mfma_f32_16x16x32_bf16 v[60:63], a[96:99], a[156:159], v[60:63]\n\t"
"ds_write_addtid_b32 v203 offset:30016\n\t"
"v_cvt_pk_bf16_f32 v219, v219, v223\n\t"
"v_mfma_f32_16x16x32_bf16 v[60:63], a[100:103], a[172:175], v[60:63]\n\t"
"s_waitcnt vmcnt(18) lgkmcnt(8)\n\t"
"buffer_load_dwordx4 a[88:91], off, s[92:95], s67 offset:2048 sc1\n\t"
"v_mfma_f32_16x16x32_bf16 v[0:3], a[120:123], a[144:147], v[0:3]\n\t"
"s_nop 0\n\t"
"ds_write_addtid_b32 v201 offset:28992\n\t"
"ds_write_addtid_b32 v216 offset:22720\n\t"
"v_mfma_f32_16x16x32_bf16 v[0:3], a[124:127], a[160:163], v[0:3]\n\t"
"s_nop 0\n\t"
"ds_write_addtid_b32 v194 offset:17568\n\t"
"ds_write_addtid_b32 v211 offset:27936\n\t"
"v_mfma_f32_16x16x32_bf16 v[16:19], a[128:131], a[144:147], v[16:19]\n\t"
"ds_write_addtid_b32 v219 offset:32064\n\t"
"v_mfma_f32_16x16x32_bf16 v[16:19], a[132:135], a[160:163], v[16:19]\n\t"
"buffer_load_dwordx4 a[92:95], off, s[92:95], s67 offset:3072 sc1\n\t"
"v_mfma_f32_16x16x32_bf16 v[32:35], a[136:139], a[144:147], v[32:35]\n\t"
"ds_read_b128 a[172:175], v224 offset:2816\n\t"
"v_mfma_f32_16x16x32_bf16 v[32:35], a[140:143], a[160:163], v[32:35]\n\t"
"ds_read_b128 a[156:159], v224 offset:768\n\t"
"v_mfma_f32_16x16x32_bf16 v[36:39], a[136:139], a[148:151], v[36:39]\n\t"
"ds_read_b64_tr_b16 a[196:197], v250 offset:26496\n\t"
"v_mfma_f32_16x16x32_bf16 v[36:39], a[140:143], a[164:167], v[36:39]\n\t"
"buffer_load_dwordx4 a[84:87], off, s[92:95], s67 offset:1024 sc1\n\t"
"v_mfma_f32_16x16x32_bf16 v[20:23], a[128:131], a[148:151], v[20:23]\n\t"
"ds_read_b64_tr_b16 a[204:205], v251 offset:26496\n\t"
"v_mfma_f32_16x16x32_bf16 v[20:23], a[132:135], a[164:167], v[20:23]\n\t"
"v_mfma_f32_16x16x32_bf16 v[4:7], a[120:123], a[148:151], v[4:7]\n\t"
"buffer_load_dwordx4 a[80:83], off, s[92:95], s67 offset:0 sc1\n\t"
"v_mfma_f32_16x16x32_bf16 v[4:7], a[124:127], a[164:167], v[4:7]\n\t"
"s_waitcnt lgkmcnt(0)\n\t"
"s_barrier\n\t"
"v_mfma_f32_16x16x32_bf16 v[8:11], a[120:123], a[152:155], v[8:11]\n\t"
"s_nop 0\n\t"
"ds_read_b128 a[144:147], v224 offset:17568\n\t"
"ds_read_b128 a[160:163], v224 offset:19616\n\t"
"v_mfma_f32_16x16x32_bf16 v[8:11], a[124:127], a[168:171], v[8:11]\n\t"
"buffer_load_dwordx4 a[72:75], off, s[96:99], s66 offset:2048 sc1\n\t"
"v_mfma_f32_16x16x32_bf16 v[24:27], a[128:131], a[152:155], v[24:27]\n\t"
"ds_read_b128 a[148:151], v224 offset:17824\n\t"
"v_mfma_f32_16x16x32_bf16 v[24:27], a[132:135], a[168:171], v[24:27]\n\t"
"ds_read_b128 a[164:167], v224 offset:19872\n\t"
"v_mfma_f32_16x16x32_bf16 v[40:43], a[136:139], a[152:155], v[40:43]\n\t"
"ds_read_b128 a[228:231], v254 offset:39168\n\t"
"v_mfma_f32_16x16x32_bf16 v[40:43], a[140:143], a[168:171], v[40:43]\n\t"
"buffer_load_dwordx4 a[76:79], off, s[96:99], s66 offset:3072 sc1\n\t"
"v_mfma_f32_16x16x32_bf16 v[44:47], a[136:139], a[156:159], v[44:47]\n\t"
"ds_read_b128 a[152:155], v224 offset:18080\n\t"
"v_mfma_f32_16x16x32_bf16 v[44:47], a[140:143], a[172:175], v[44:47]\n\t"
"buffer_load_dwordx4 a[112:115], off, s[92:95], s53 offset:0 sc1\n\t"
"v_mfma_f32_16x16x32_bf16 v[28:31], a[128:131], a[156:159], v[28:31]\n\t"
"ds_read_b128 a[168:171], v224 offset:20128\n\t"
"v_mfma_f32_16x16x32_bf16 v[28:31], a[132:135], a[172:175], v[28:31]\n\t"
"v_mfma_f32_16x16x32_bf16 v[12:15], a[120:123], a[156:159], v[12:15]\n\t"
"ds_read_b128 a[156:159], v224 offset:18336\n\t"
"v_mfma_f32_16x16x32_bf16 v[12:15], a[124:127], a[172:175], v[12:15]\n\t"
"buffer_load_dwordx4 a[116:119], off, s[92:95], s53 offset:1024 sc1\n\t"
"v_mfma_f32_4x4x4_16b_bf16 v[200:203], a[220:221], a[178:179], 0\n\t"
"ds_read_b64_tr_b16 a[178:179], v250 offset:3456\n\t"
"v_mfma_f32_4x4x4_16b_bf16 v[204:207], a[220:221], a[186:187], 0\n\t"
"ds_read_b64_tr_b16 a[186:187], v251 offset:3456\n\t"
"v_mfma_f32_4x4x4_16b_bf16 v[216:219], a[220:221], a[194:195], 0\n\t"
"ds_read_b64_tr_b16 a[194:195], v248 offset:3456\n\t"
"v_mfma_f32_4x4x4_16b_bf16 v[220:223], a[220:221], a[202:203], 0\n\t"
"buffer_load_dwordx4 a[108:111], off, s[96:99], s52 offset:3072 sc1\n\t"
"v_mfma_f32_4x4x4_16b_bf16 v[192:195], a[222:223], a[176:177], v[200:203]\n\t"
"s_waitcnt lgkmcnt(5)\n\t"
"ds_write_b128 v255, a[228:231] offset:20736\n\t"
"v_mfma_f32_4x4x4_16b_bf16 v[196:199], a[222:223], a[184:185], v[204:207]\n\t"
"v_mfma_f32_4x4x4_16b_bf16 v[200:203], a[222:223], a[180:181], v[200:203]\n\t"
"v_mfma_f32_4x4x4_16b_bf16 v[204:207], a[222:223], a[188:189], v[204:207]\n\t"
"v_mfma_f32_4x4x4_16b_bf16 v[208:211], a[222:223], a[192:193], v[216:219]\n\t"
"ds_read_b128 a[172:175], v224 offset:20384\n\t"
"v_mfma_f32_4x4x4_16b_bf16 v[212:215], a[222:223], a[200:201], v[220:223]\n\t"
"ds_read_b64_tr_b16 a[202:203], v249 offset:3456\n\t"
"v_mfma_f32_4x4x4_16b_bf16 v[216:219], a[222:223], a[196:197], v[216:219]\n\t"
"ds_read_b64_tr_b16 a[176:177], v248 offset:1152\n\t"
"v_mfma_f32_4x4x4_16b_bf16 v[220:223], a[222:223], a[204:205], v[220:223]\n\t"
"s_waitcnt vmcnt(21) lgkmcnt(5)\n\t"
"buffer_load_dwordx4 a[104:107], off, s[96:99], s52 offset:2048 sc1\n\t"
"v_mfma_f32_16x16x32_bf16 v[48:51], a[0:3], a[144:147], v[48:51]\n\t"
"ds_read_b64_tr_b16 a[184:185], v249 offset:1152\n\t"
"v_cvt_pk_bf16_f32 v192, -v192, -v196\n\t"
"v_mfma_f32_16x16x32_bf16 v[48:51], a[4:7], a[160:163], v[48:51]\n\t"
"ds_write_addtid_b32 v192 offset:0\n\t"
"v_cvt_pk_bf16_f32 v193, -v193, -v197\n\t"
"v_mfma_f32_16x16x32_bf16 v[64:67], a[8:11], a[144:147], v[64:67]\n\t"
"ds_write_addtid_b32 v193 offset:8320\n\t"
"v_cvt_pk_bf16_f32 v195, -v195, -v199\n\t"
"v_mfma_f32_16x16x32_bf16 v[64:67], a[12:15], a[160:163], v[64:67]\n\t"
"buffer_load_dwordx4 a[96:99], off, s[96:99], s52 offset:0 sc1\n\t"
"v_mfma_f32_16x16x32_bf16 v[80:83], a[16:19], a[144:147], v[80:83]\n\t"
"ds_read_b128 a[144:147], v224 offset:16544\n\t"
"v_cvt_pk_bf16_f32 v201, v201, v205\n\t"
"v_mfma_f32_16x16x32_bf16 v[80:83], a[20:23], a[160:163], v[80:83]\n\t"
"ds_write_addtid_b32 v201 offset:12448\n\t"
"v_cvt_pk_bf16_f32 v194, -v194, -v198\n\t"
"v_mfma_f32_16x16x32_bf16 v[84:87], a[16:19], a[148:151], v[84:87]\n\t"
"ds_read_b128 a[160:163], v224 offset:18592\n\t"
"v_cvt_pk_bf16_f32 v209, -v209, -v213\n\t"
"v_mfma_f32_16x16x32_bf16 v[84:87], a[20:23], a[164:167], v[84:87]\n\t"
"buffer_load_dwordx4 a[100:103], off, s[96:99], s52 offset:1024 sc1\n\t"
"v_mfma_f32_16x16x32_bf16 v[68:71], a[8:11], a[148:151], v[68:71]\n\t"
"ds_write_addtid_b32 v195 offset:9344\n\t"
"v_cvt_pk_bf16_f32 v211, -v211, -v215\n\t"
"v_mfma_f32_16x16x32_bf16 v[68:71], a[12:15], a[164:167], v[68:71]\n\t"
"ds_write_addtid_b32 v194 offset:1024\n\t"
"v_cvt_pk_bf16_f32 v210, -v210, -v214\n\t"
"v_mfma_f32_16x16x32_bf16 v[52:55], a[0:3], a[148:151], v[52:55]\n\t"
"ds_read_b128 a[148:151], v224 offset:16800\n\t"
"v_cvt_pk_bf16_f32 v202, v202, v206\n\t"
"v_mfma_f32_16x16x32_bf16 v[52:55], a[4:7], a[164:167], v[52:55]\n\t"
"buffer_load_dwordx4 a[136:139], off, s[92:95], s52 offset:2048 sc1\n\t"
"v_mfma_f32_16x16x32_bf16 v[56:59], a[0:3], a[152:155], v[56:59]\n\t"
"ds_read_b128 a[164:167], v224 offset:18848\n\t"
"v_cvt_pk_bf16_f32 v200, v200, v204\n\t"
"v_mfma_f32_16x16x32_bf16 v[56:59], a[4:7], a[168:171], v[56:59]\n\t"
"ds_write_addtid_b32 v211 offset:11392\n\t"
"v_cvt_pk_bf16_f32 v219, v219, v223\n\t"
"v_mfma_f32_16x16x32_bf16 v[72:75], a[8:11], a[152:155], v[72:75]\n\t"
"ds_write_addtid_b32 v210 offset:3072\n\t"
"v_cvt_pk_bf16_f32 v217, v217, v221\n\t"
"v_mfma_f32_16x16x32_bf16 v[72:75], a[12:15], a[168:171], v[72:75]\n\t"
"ds_write_addtid_b32 v200 offset:4128\n\t"
"buffer_load_dwordx4 a[140:143], off, s[92:95], s52 offset:3072 sc1\n\t"
"v_mfma_f32_16x16x32_bf16 v[88:91], a[16:19], a[152:155], v[88:91]\n\t"
"ds_write_addtid_b32 v202 offset:5152\n\t"
"v_cvt_pk_bf16_f32 v203, v203, v207\n\t"
"v_mfma_f32_16x16x32_bf16 v[88:91], a[20:23], a[168:171], v[88:91]\n\t"
"s_waitcnt lgkmcnt(8)\n\t"
"ds_write_addtid_b32 v209 offset:10368\n\t"
"ds_write_addtid_b32 v217 offset:14496\n\t"
"v_mfma_f32_16x16x32_bf16 v[92:95], a[16:19], a[156:159], v[92:95]\n\t"
"ds_write_addtid_b32 v203 offset:13472\n\t"
"v_cvt_pk_bf16_f32 v216, v216, v220\n\t"
"v_mfma_f32_16x16x32_bf16 v[92:95], a[20:23], a[172:175], v[92:95]\n\t"
"ds_write_addtid_b32 v219 offset:15520\n\t"
"v_cvt_pk_bf16_f32 v218, v218, v222\n\t"
"v_mfma_f32_16x16x32_bf16 v[76:79], a[8:11], a[156:159], v[76:79]\n\t"
"ds_write_addtid_b32 v218 offset:7200\n\t"
"v_cvt_pk_bf16_f32 v208, -v208, -v212\n\t"
"v_mfma_f32_16x16x32_bf16 v[76:79], a[12:15], a[172:175], v[76:79]\n\t"
"s_nop 0\n\t"
"ds_write_addtid_b32 v208 offset:2048\n\t"
"ds_write_addtid_b32 v216 offset:6176\n\t"
"v_mfma_f32_16x16x32_bf16 v[60:63], a[0:3], a[156:159], v[60:63]\n\t"
"s_add_u32 m0, s26, 124544\n\t"
"ds_read_b128 a[152:155], v224 offset:17056\n\t"
"v_mfma_f32_16x16x32_bf16 v[60:63], a[4:7], a[172:175], v[60:63]\n\t"
"s_waitcnt vmcnt(20) lgkmcnt(11)\n\t"
"buffer_load_dwordx4 v236, s[12:15], s22 offen offset:128 nt lds\n\t"
"v_mfma_f32_16x16x32_bf16 v[0:3], a[24:27], a[144:147], v[0:3]\n\t"
"s_nop 0\n\t"
"ds_read_b128 a[156:159], v224 offset:17312\n\t"
"ds_read_b128 a[172:175], v224 offset:19360\n\t"
"v_mfma_f32_16x16x32_bf16 v[0:3], a[28:31], a[160:163], v[0:3]\n\t"
"buffer_load_dwordx4 v237, s[12:15], s22 offen offset:1152 nt lds\n\t"
"v_mfma_f32_16x16x32_bf16 v[16:19], a[32:35], a[144:147], v[16:19]\n\t"
"ds_read_b128 a[168:171], v224 offset:19104\n\t"
"v_mfma_f32_16x16x32_bf16 v[16:19], a[36:39], a[160:163], v[16:19]\n\t"
"buffer_load_dwordx4 v238, s[12:15], s22 offen offset:2176 nt lds\n\t"
"v_mfma_f32_16x16x32_bf16 v[32:35], a[40:43], a[144:147], v[32:35]\n\t"
"s_nop 0\n\t"
"ds_read_b64_tr_b16 a[180:181], v248 offset:5760\n\t"
"ds_read_b64_tr_b16 a[188:189], v249 offset:5760\n\t"
"v_mfma_f32_16x16x32_bf16 v[32:35], a[44:47], a[160:163], v[32:35]\n\t"
"buffer_load_dwordx4 v239, s[12:15], s22 offen offset:3200 nt lds\n\t"
"v_mfma_f32_16x16x32_bf16 v[36:39], a[40:43], a[148:151], v[36:39]\n\t"
"s_nop 0\n\t"
"ds_read_b64_tr_b16 a[192:193], v250 offset:1152\n\t"
"ds_read_b64_tr_b16 a[200:201], v251 offset:1152\n\t"
"v_mfma_f32_16x16x32_bf16 v[36:39], a[44:47], a[164:167], v[36:39]\n\t"
"buffer_load_dwordx4 a[132:135], off, s[92:95], s52 offset:1024 sc1\n\t"
"v_mfma_f32_16x16x32_bf16 v[20:23], a[32:35], a[148:151], v[20:23]\n\t"
"s_nop 0\n\t"
"ds_read_b64_tr_b16 a[196:197], v250 offset:5760\n\t"
"ds_read_b64_tr_b16 a[204:205], v251 offset:5760\n\t"
"v_mfma_f32_16x16x32_bf16 v[20:23], a[36:39], a[164:167], v[20:23]\n\t"
"s_mov_b64 exec, s[36:37]\n\t"
"buffer_load_dwordx4 v240, s[12:15], s22 offen offset:3712 nt lds\n\t"
"s_mov_b64 exec, -1\n\t"
"v_mfma_f32_16x16x32_bf16 v[4:7], a[24:27], a[148:151], v[4:7]\n\t"
"s_lshl_b32 m0, s73, 8\n\t"
"v_mfma_f32_16x16x32_bf16 v[4:7], a[28:31], a[164:167], v[4:7]\n\t"
"s_waitcnt lgkmcnt(0)\n\t"
"s_barrier\n\t"
"v_mfma_f32_16x16x32_bf16 v[8:11], a[24:27], a[152:155], v[8:11]\n\t"
"ds_read_b128 a[160:163], v224 offset:3072\n\t"
"v_add_u32 v248, 33280, v227\n\t"
"v_mfma_f32_16x16x32_bf16 v[8:11], a[28:31], a[168:171], v[8:11]\n\t"
"ds_read_b128 a[144:147], v224 offset:1024\n\t"
"v_xor_b32 v249, 32, v248\n\t"
"v_mfma_f32_16x16x32_bf16 v[24:27], a[32:35], a[152:155], v[24:27]\n\t"
"ds_read_b128 a[148:151], v224 offset:1280\n\t"
"v_xor_b32 v250, 64, v248\n\t"
"v_mfma_f32_16x16x32_bf16 v[24:27], a[36:39], a[168:171], v[24:27]\n\t"
"ds_read_b128 a[164:167], v224 offset:3328\n\t"
"v_xor_b32 v251, 96, v248\n\t"
"v_mfma_f32_16x16x32_bf16 v[40:43], a[40:43], a[152:155], v[40:43]\n\t"
"ds_read_b64_tr_b16 a[182:183], v249 offset:41984\n\t"
"v_mfma_f32_16x16x32_bf16 v[40:43], a[44:47], a[168:171], v[40:43]\n\t"
"ds_read_b128 a[152:155], v224 offset:1536\n\t"
"v_mfma_f32_16x16x32_bf16 v[44:47], a[40:43], a[156:159], v[44:47]\n\t"
"ds_read_b128 a[168:171], v224 offset:3584\n\t"
"v_mfma_f32_16x16x32_bf16 v[44:47], a[44:47], a[172:175], v[44:47]\n\t"
"ds_read_b64_tr_b16 a[190:191], v251 offset:41984\n\t"
"v_mfma_f32_16x16x32_bf16 v[28:31], a[32:35], a[156:159], v[28:31]\n\t"
"ds_read_b64_tr_b16 a[198:199], v249 offset:21248\n\t"
"v_mfma_f32_16x16x32_bf16 v[28:31], a[36:39], a[172:175], v[28:31]\n\t"
"ds_read_b64_tr_b16 a[206:207], v251 offset:21248\n\t"
"v_mfma_f32_16x16x32_bf16 v[12:15], a[24:27], a[156:159], v[12:15]\n\t"
"ds_read_b128 a[156:159], v224 offset:1792\n\t"
"v_mfma_f32_16x16x32_bf16 v[12:15], a[28:31], a[172:175], v[12:15]\n\t"
"ds_read_b128 a[172:175], v224 offset:3840\n\t"
"v_mfma_f32_4x4x4_16b_bf16 v[200:203], a[220:221], a[178:179], 0\n\t"
"ds_read_b64_tr_b16 a[178:179], v248 offset:41984\n\t"
"v_mfma_f32_4x4x4_16b_bf16 v[204:207], a[220:221], a[186:187], 0\n\t"
"ds_read_b64_tr_b16 a[186:187], v250 offset:41984\n\t"
"v_mfma_f32_4x4x4_16b_bf16 v[216:219], a[220:221], a[194:195], 0\n\t"
"ds_read_b64_tr_b16 a[194:195], v248 offset:21248\n\t"
"v_mfma_f32_4x4x4_16b_bf16 v[220:223], a[220:221], a[202:203], 0\n\t"
"ds_read_b64_tr_b16 a[202:203], v250 offset:21248\n\t"
"v_mfma_f32_4x4x4_16b_bf16 v[192:195], a[222:223], a[176:177], v[200:203]\n\t"
"ds_read_b64_tr_b16 a[176:177], v248 offset:41472\n\t"
"v_mfma_f32_4x4x4_16b_bf16 v[196:199], a[222:223], a[184:185], v[204:207]\n\t"
".p2align 3\n\t"
"ds_read_b64_tr_b16 a[184:185], v250 offset:41472\n\t"
"v_pk_add_f32 v[0:1], v[0:1], v[48:49]\n\t"
"v_pk_add_f32 v[2:3], v[2:3], v[50:51]\n\t"
"v_pk_fma_f32 v[48:49], v[48:49], s[38:39], v[0:1]\n\t"
"v_pk_fma_f32 v[50:51], v[50:51], s[38:39], v[2:3]\n\t"
"v_pk_add_f32 v[16:17], v[16:17], v[64:65]\n\t"
"v_pk_add_f32 v[18:19], v[18:19], v[66:67]\n\t"
"v_pk_fma_f32 v[64:65], v[64:65], s[38:39], v[16:17]\n\t"
"v_pk_fma_f32 v[66:67], v[66:67], s[38:39], v[18:19]\n\t"
"v_pk_add_f32 v[32:33], v[32:33], v[80:81]\n\t"
"v_pk_add_f32 v[34:35], v[34:35], v[82:83]\n\t"
"v_pk_fma_f32 v[80:81], v[80:81], s[38:39], v[32:33]\n\t"
"v_pk_fma_f32 v[82:83], v[82:83], s[38:39], v[34:35]\n\t"
"v_pk_add_f32 v[36:37], v[36:37], v[84:85]\n\t"
"v_pk_add_f32 v[38:39], v[38:39], v[86:87]\n\t"
"v_pk_fma_f32 v[84:85], v[84:85], s[38:39], v[36:37]\n\t"
"v_pk_fma_f32 v[86:87], v[86:87], s[38:39], v[38:39]\n\t"
"v_pk_add_f32 v[20:21], v[20:21], v[68:69]\n\t"
"v_pk_add_f32 v[22:23], v[22:23], v[70:71]\n\t"
"v_pk_fma_f32 v[68:69], v[68:69], s[38:39], v[20:21]\n\t"
"v_pk_fma_f32 v[70:71], v[70:71], s[38:39], v[22:23]\n\t"
"v_pk_add_f32 v[4:5], v[4:5], v[52:53]\n\t"
"v_pk_add_f32 v[6:7], v[6:7], v[54:55]\n\t"
"v_pk_fma_f32 v[52:53], v[52:53], s[38:39], v[4:5]\n\t"
"v_pk_fma_f32 v[54:55], v[54:55], s[38:39], v[6:7]\n\t"
"v_pk_add_f32 v[8:9], v[8:9], v[56:57]\n\t"
"v_pk_add_f32 v[10:11], v[10:11], v[58:59]\n\t"
"v_pk_fma_f32 v[56:57], v[56:57], s[38:39], v[8:9]\n\t"
"v_pk_fma_f32 v[58:59], v[58:59], s[38:39], v[10:11]\n\t"
"v_pk_add_f32 v[24:25], v[24:25], v[72:73]\n\t"
"v_pk_add_f32 v[26:27], v[26:27], v[74:75]\n\t"
"v_pk_fma_f32 v[72:73], v[72:73], s[38:39], v[24:25]\n\t"
"v_pk_fma_f32 v[74:75], v[74:75], s[38:39], v[26:27]\n\t"
"v_pk_add_f32 v[40:41], v[40:41], v[88:89]\n\t"
"v_pk_add_f32 v[42:43], v[42:43], v[90:91]\n\t"
"v_pk_fma_f32 v[88:89], v[88:89], s[38:39], v[40:41]\n\t"
"v_pk_fma_f32 v[90:91], v[90:91], s[38:39], v[42:43]\n\t"
"v_pk_add_f32 v[44:45], v[44:45], v[92:93]\n\t"
"v_pk_add_f32 v[46:47], v[46:47], v[94:95]\n\t"
"v_pk_fma_f32 v[92:93], v[92:93], s[38:39], v[44:45]\n\t"
"v_pk_fma_f32 v[94:95], v[94:95], s[38:39], v[46:47]\n\t"
"v_pk_add_f32 v[28:29], v[28:29], v[76:77]\n\t"
"v_pk_add_f32 v[30:31], v[30:31], v[78:79]\n\t"
"v_pk_fma_f32 v[76:77], v[76:77], s[38:39], v[28:29]\n\t"
"v_pk_fma_f32 v[78:79], v[78:79], s[38:39], v[30:31]\n\t"
"v_pk_add_f32 v[12:13], v[12:13], v[60:61]\n\t"
"v_pk_add_f32 v[14:15], v[14:15], v[62:63]\n\t"
"v_pk_fma_f32 v[60:61], v[60:61], s[38:39], v[12:13]\n\t"
"v_pk_fma_f32 v[62:63], v[62:63], s[38:39], v[14:15]\n\t"
"v_cvt_pk_bf16_f32 v0, v0, v1\n\t"
"v_mfma_f32_4x4x4_16b_bf16 v[200:203], a[222:223], a[180:181], v[200:203]\n\t"
"ds_read_b64_tr_b16 a[180:181], v249 offset:41472\n\t"
"v_cvt_pk_bf16_f32 v4, v4, v5\n\t"
"v_mfma_f32_4x4x4_16b_bf16 v[204:207], a[222:223], a[188:189], v[204:207]\n\t"
"v_cvt_pk_bf16_f32 v1, v2, v3\n\t"
"v_mfma_f32_4x4x4_16b_bf16 v[208:211], a[222:223], a[192:193], v[216:219]\n\t"
"v_cvt_pk_bf16_f32 v5, v6, v7\n\t"
"v_mfma_f32_4x4x4_16b_bf16 v[212:215], a[222:223], a[200:201], v[220:223]\n\t"
"ds_write2st64_b64 v228, v[0:1], v[4:5] offset0:40 offset1:53\n\t"
"v_mfma_f32_4x4x4_16b_bf16 v[216:219], a[222:223], a[196:197], v[216:219]\n\t"
"v_cvt_pk_bf16_f32 v16, v16, v17\n\t"
"v_mfma_f32_4x4x4_16b_bf16 v[220:223], a[222:223], a[204:205], v[220:223]\n\t"
"s_waitcnt vmcnt(20) lgkmcnt(6)\n\t"
"v_cvt_pk_bf16_f32 v17, v18, v19\n\t"
"v_cvt_pk_bf16_f32 v8, v8, v9\n\t"
"v_mfma_f32_16x16x32_bf16 v[144:147], a[48:51], a[144:147], v[144:147]\n\t"
"v_cvt_pk_bf16_f32 v192, -v192, -v196\n\t"
"v_cvt_pk_bf16_f32 v20, v20, v21\n\t"
"v_mfma_f32_16x16x32_bf16 v[144:147], a[52:55], a[160:163], v[144:147]\n\t"
"ds_read_b64_tr_b16 a[188:189], v251 offset:41472\n\t"
"v_cvt_pk_bf16_f32 v12, v12, v13\n\t"
"v_mfma_f32_16x16x32_bf16 v[160:163], a[56:59], a[144:147], v[160:163]\n\t"
"v_cvt_pk_bf16_f32 v21, v22, v23\n\t"
"v_cvt_pk_bf16_f32 v32, v32, v33\n\t"
"v_mfma_f32_16x16x32_bf16 v[160:163], a[60:63], a[160:163], v[160:163]\n\t"
"ds_write_addtid_b32 v192 offset:16544\n\t"
"ds_write2st64_b64 v246, v[16:17], v[20:21] offset0:40 offset1:53\n\t"
"v_cvt_pk_bf16_f32 v24, v24, v25\n\t"
"v_mfma_f32_16x16x32_bf16 v[176:179], a[64:67], a[144:147], v[176:179]\n\t"
"v_cvt_pk_bf16_f32 v33, v34, v35\n\t"
"v_cvt_pk_bf16_f32 v36, v36, v37\n\t"
"v_mfma_f32_16x16x32_bf16 v[176:179], a[68:71], a[160:163], v[176:179]\n\t"
"buffer_load_dwordx4 a[128:131], off, s[92:95], s52 offset:0 sc1\n\t"
"v_mfma_f32_16x16x32_bf16 v[180:183], a[64:67], a[148:151], v[180:183]\n\t"
"v_cvt_pk_bf16_f32 v37, v38, v39\n\t"
"v_cvt_pk_bf16_f32 v40, v40, v41\n\t"
"v_mfma_f32_16x16x32_bf16 v[180:183], a[68:71], a[164:167], v[180:183]\n\t"
"s_nop 0\n\t"
"ds_read_b128 a[144:147], v224 offset:0\n\t"
"ds_read_b128 a[160:163], v224 offset:2048\n\t"
"ds_write2st64_b64 v247, v[32:33], v[36:37] offset0:40 offset1:53\n\t"
"v_mfma_f32_16x16x32_bf16 v[164:167], a[56:59], a[148:151], v[164:167]\n\t"
"v_cvt_pk_bf16_f32 v13, v14, v15\n\t"
"v_cvt_pk_bf16_f32 v41, v42, v43\n\t"
"v_mfma_f32_16x16x32_bf16 v[164:167], a[60:63], a[164:167], v[164:167]\n\t"
"v_cvt_pk_bf16_f32 v9, v10, v11\n\t"
"v_cvt_pk_bf16_f32 v25, v26, v27\n\t"
"v_mfma_f32_16x16x32_bf16 v[148:151], a[48:51], a[148:151], v[148:151]\n\t"
"v_cvt_pk_bf16_f32 v209, -v209, -v213\n\t"
"v_cvt_pk_bf16_f32 v216, v216, v220\n\t"
"v_mfma_f32_16x16x32_bf16 v[148:151], a[52:55], a[164:167], v[148:151]\n\t"
"s_nop 0\n\t"
"ds_read_b128 a[148:151], v224 offset:256\n\t"
"ds_read_b128 a[164:167], v224 offset:2304\n\t"
"ds_write2st64_b64 v228, v[8:9], v[12:13] offset0:66 offset1:79\n\t"
"v_mfma_f32_16x16x32_bf16 v[152:155], a[48:51], a[152:155], v[152:155]\n\t"
"v_cvt_pk_bf16_f32 v44, v44, v45\n\t"
"v_cvt_pk_bf16_f32 v45, v46, v47\n\t"
"v_mfma_f32_16x16x32_bf16 v[152:155], a[52:55], a[168:171], v[152:155]\n\t"
"v_cvt_pk_bf16_f32 v200, v200, v204\n\t"
"v_cvt_pk_bf16_f32 v28, v28, v29\n\t"
"v_mfma_f32_16x16x32_bf16 v[168:171], a[56:59], a[152:155], v[168:171]\n\t"
"v_cvt_pk_bf16_f32 v194, -v194, -v198\n\t"
"v_cvt_pk_bf16_f32 v210, -v210, -v214\n\t"
"v_mfma_f32_16x16x32_bf16 v[168:171], a[60:63], a[168:171], v[168:171]\n\t"
"ds_write_addtid_b32 v200 offset:20672\n\t"
"ds_write2st64_b64 v247, v[40:41], v[44:45] offset0:66 offset1:79\n\t"
"v_mfma_f32_16x16x32_bf16 v[184:187], a[64:67], a[152:155], v[184:187]\n\t"
"v_cvt_pk_bf16_f32 v201, v201, v205\n\t"
"v_cvt_pk_bf16_f32 v29, v30, v31\n\t"
"v_mfma_f32_16x16x32_bf16 v[184:187], a[68:71], a[168:171], v[184:187]\n\t"
"v_cvt_pk_bf16_f32 v195, -v195, -v199\n\t"
"v_cvt_pk_bf16_f32 v202, v202, v206\n\t"
"v_mfma_f32_16x16x32_bf16 v[188:191], a[64:67], a[156:159], v[188:191]\n\t"
"v_cvt_pk_bf16_f32 v193, -v193, -v197\n\t"
"v_cvt_pk_bf16_f32 v203, v203, v207\n\t"
"v_mfma_f32_16x16x32_bf16 v[188:191], a[68:71], a[172:175], v[188:191]\n\t"
"ds_write_addtid_b32 v209 offset:26912\n\t"
"ds_write2st64_b64 v246, v[24:25], v[28:29] offset0:66 offset1:79\n\t"
"v_mfma_f32_16x16x32_bf16 v[172:175], a[56:59], a[156:159], v[172:175]\n\t"
"v_cvt_pk_bf16_f32 v218, v218, v222\n\t"
"v_cvt_pk_bf16_f32 v219, v219, v223\n\t"
"v_mfma_f32_16x16x32_bf16 v[172:175], a[60:63], a[172:175], v[172:175]\n\t"
"s_nop 0\n\t"
"ds_write_addtid_b32 v193 offset:24864\n\t"
"ds_write_addtid_b32 v201 offset:28992\n\t"
"ds_write_addtid_b32 v219 offset:32064\n\t"
"v_mfma_f32_16x16x32_bf16 v[156:159], a[48:51], a[156:159], v[156:159]\n\t"
"v_cvt_pk_bf16_f32 v208, -v208, -v212\n\t"
"v_cvt_pk_bf16_f32 v217, v217, v221\n\t"
"v_mfma_f32_16x16x32_bf16 v[156:159], a[52:55], a[172:175], v[156:159]\n\t"
"s_waitcnt vmcnt(15) lgkmcnt(7)\n\t"
"ds_write_addtid_b32 v194 offset:17568\n\t"
"ds_write_addtid_b32 v208 offset:18592\n\t"
"ds_write_addtid_b32 v210 offset:19616\n\t"
"ds_write_addtid_b32 v218 offset:23744\n\t"
"v_mfma_f32_16x16x32_bf16 v[96:99], a[72:75], a[144:147], v[96:99]\n\t"
"ds_write_addtid_b32 v216 offset:22720\n\t"
"v_cvt_pk_bf16_f32 v211, -v211, -v215\n\t"
"v_mfma_f32_16x16x32_bf16 v[96:99], a[76:79], a[160:163], v[96:99]\n\t"
"s_nop 0\n\t"
"ds_write_addtid_b32 v195 offset:25888\n\t"
"ds_write_addtid_b32 v203 offset:30016\n\t"
"ds_write_addtid_b32 v211 offset:27936\n\t"
"v_mfma_f32_16x16x32_bf16 v[112:115], a[80:83], a[144:147], v[112:115]\n\t"
"s_nop 0\n\t"
"ds_write_addtid_b32 v202 offset:21696\n\t"
"ds_write_addtid_b32 v217 offset:31040\n\t"
"v_mfma_f32_16x16x32_bf16 v[112:115], a[84:87], a[160:163], v[112:115]\n\t"
"buffer_load_dwordx4 a[120:123], off, s[96:99], s51 offset:2048 sc1\n\t"
"v_mfma_f32_16x16x32_bf16 v[128:131], a[88:91], a[144:147], v[128:131]\n\t"
"s_nop 0\n\t"
"ds_read_b128 a[152:155], v224 offset:512\n\t"
"ds_read_b128 a[156:159], v224 offset:768\n\t"
"v_mfma_f32_16x16x32_bf16 v[128:131], a[92:95], a[160:163], v[128:131]\n\t"
"buffer_load_dwordx4 a[124:127], off, s[96:99], s51 offset:3072 sc1\n\t"
"v_mfma_f32_16x16x32_bf16 v[132:135], a[88:91], a[148:151], v[132:135]\n\t"
"s_nop 0\n\t"
"ds_read_b128 a[168:171], v224 offset:2560\n\t"
"ds_read_b128 a[172:175], v224 offset:2816\n\t"
"v_mfma_f32_16x16x32_bf16 v[132:135], a[92:95], a[164:167], v[132:135]\n\t"
"s_nop 0\n\t"
"ds_read_b64_tr_b16 a[192:193], v248 offset:20736\n\t"
"ds_read_b64_tr_b16 a[200:201], v250 offset:20736\n\t"
"v_mfma_f32_16x16x32_bf16 v[116:119], a[80:83], a[148:151], v[116:119]\n\t"
"s_nop 0\n\t"
"ds_read_b64_tr_b16 a[196:197], v249 offset:20736\n\t"
"ds_read_b64_tr_b16 a[204:205], v251 offset:20736\n\t"
"v_mfma_f32_16x16x32_bf16 v[116:119], a[84:87], a[164:167], v[116:119]\n\t"
"buffer_load_dwordx4 a[16:19], off, s[28:31], s69 offset:0 sc1\n\t"
"v_mfma_f32_16x16x32_bf16 v[100:103], a[72:75], a[148:151], v[100:103]\n\t"
"s_waitcnt lgkmcnt(4)\n\t"
"v_mfma_f32_16x16x32_bf16 v[100:103], a[76:79], a[164:167], v[100:103]\n\t"
"s_barrier\n\t"
"s_cmp_eq_u32 s21, 1\n\t"
"s_cbranch_scc1 Ltail_%=\n\t"
"v_mfma_f32_16x16x32_bf16 v[104:107], a[72:75], a[152:155], v[104:107]\n\t"
"s_nop 0\n\t"
"ds_read_b128 a[144:147], v224 offset:17568\n\t"
"ds_read_b128 a[160:163], v224 offset:19616\n\t"
"v_mfma_f32_16x16x32_bf16 v[104:107], a[76:79], a[168:171], v[104:107]\n\t"
"buffer_load_dwordx4 a[20:23], off, s[28:31], s69 offset:1024 sc1\n\t"
"v_mfma_f32_16x16x32_bf16 v[120:123], a[80:83], a[152:155], v[120:123]\n\t"
"ds_read_b128 a[148:151], v224 offset:17824\n\t"
"v_mfma_f32_16x16x32_bf16 v[120:123], a[84:87], a[168:171], v[120:123]\n\t"
"ds_read_b128 a[164:167], v224 offset:19872\n\t"
"v_mfma_f32_16x16x32_bf16 v[136:139], a[88:91], a[152:155], v[136:139]\n\t"
"ds_read_b128 a[152:155], v224 offset:18080\n\t"
"v_mfma_f32_16x16x32_bf16 v[136:139], a[92:95], a[168:171], v[136:139]\n\t"
"buffer_load_dwordx4 a[12:15], off, s[84:87], s69 offset:3072 sc1\n\t"
"v_mfma_f32_16x16x32_bf16 v[140:143], a[88:91], a[156:159], v[140:143]\n\t"
"ds_read_b128 v[8:11], v229 offset:20480\n\t"
"v_mfma_f32_16x16x32_bf16 v[140:143], a[92:95], a[172:175], v[140:143]\n\t"
"buffer_load_dwordx4 a[8:11], off, s[84:87], s69 offset:2048 sc1\n\t"
"v_mfma_f32_16x16x32_bf16 v[124:127], a[80:83], a[156:159], v[124:127]\n\t"
"ds_read_b128 a[168:171], v224 offset:20128\n\t"
"v_mfma_f32_16x16x32_bf16 v[124:127], a[84:87], a[172:175], v[124:127]\n\t"
"buffer_load_dwordx4 a[0:3], off, s[84:87], s69 offset:0 sc1\n\t"
"v_mfma_f32_16x16x32_bf16 v[108:111], a[72:75], a[156:159], v[108:111]\n\t"
"ds_read_b128 a[156:159], v224 offset:18336\n\t"
"v_mfma_f32_16x16x32_bf16 v[108:111], a[76:79], a[172:175], v[108:111]\n\t"
"ds_read_b128 a[172:175], v224 offset:20384\n\t"
"v_mfma_f32_16x16x32_bf16 v[192:195], a[224:227], a[176:179], 0\n\t"
"s_nop 0\n\t"
"ds_read_b64_tr_b16 a[176:177], v248 offset:0\n\t"
"ds_read_b64_tr_b16 a[178:179], v248 offset:512\n\t"
"v_mfma_f32_16x16x32_bf16 v[196:199], a[224:227], a[180:183], 0\n\t"
"s_nop 0\n\t"
"ds_read_b64_tr_b16 a[180:181], v249 offset:0\n\t"
"ds_read_b64_tr_b16 a[182:183], v249 offset:512\n\t"
"v_mfma_f32_16x16x32_bf16 v[200:203], a[224:227], a[184:187], 0\n\t"
"buffer_load_dwordx4 a[4:7], off, s[84:87], s69 offset:1024 sc1\n\t"
"v_mfma_f32_16x16x32_bf16 v[204:207], a[224:227], a[188:191], 0\n\t"
"s_waitcnt lgkmcnt(8)\n\t"
"ds_read_b64_tr_b16 a[184:185], v250 offset:0\n\t"
"ds_read_b64_tr_b16 a[186:187], v250 offset:512\n\t"
"v_mfma_f32_16x16x32_bf16 v[208:211], a[224:227], a[192:195], 0\n\t"
"s_nop 0\n\t"
"ds_read_b64_tr_b16 a[188:189], v251 offset:0\n\t"
"ds_read_b64_tr_b16 a[190:191], v251 offset:512\n\t"
"v_mfma_f32_16x16x32_bf16 v[212:215], a[224:227], a[196:199], 0\n\t"
"s_nop 0\n\t"
"ds_read_b64_tr_b16 a[192:193], v248 offset:1152\n\t"
"ds_read_b64_tr_b16 a[194:195], v248 offset:1664\n\t"
"v_mfma_f32_16x16x32_bf16 v[216:219], a[224:227], a[200:203], 0\n\t"
"s_waitcnt lgkmcnt(12)\n\t"
"buffer_store_dwordx4 v[8:11], v232, s[16:19], s24 offen nt\n\t"
"v_mfma_f32_16x16x32_bf16 v[220:223], a[224:227], a[204:207], 0\n\t"
"s_waitcnt vmcnt(18)\n\t"
"ds_read_b64_tr_b16 a[196:197], v249 offset:1152\n\t"
"v_cvt_pk_bf16_f32 v195, v195, v199\n\t"
"v_mfma_f32_16x16x32_bf16 v[144:147], a[96:99], a[144:147], v[144:147]\n\t"
"ds_read_b64_tr_b16 a[198:199], v249 offset:1664\n\t"
"v_cvt_pk_bf16_f32 v194, v194, v198\n\t"
"v_mfma_f32_16x16x32_bf16 v[144:147], a[100:103], a[160:163], v[144:147]\n\t"
"ds_write_addtid_b32 v194 offset:4128\n\t"
"buffer_load_dwordx4 a[40:43], off, s[28:31], s54 offset:0 sc1\n\t"
"v_mfma_f32_16x16x32_bf16 v[160:163], a[104:107], a[144:147], v[160:163]\n\t"
"ds_write_addtid_b32 v195 offset:12448\n\t"
"v_cvt_pk_bf16_f32 v200, v200, v204\n\t"
"v_mfma_f32_16x16x32_bf16 v[160:163], a[108:111], a[160:163], v[160:163]\n\t"
"ds_write_addtid_b32 v200 offset:2048\n\t"
"v_cvt_pk_bf16_f32 v208, v208, v212\n\t"
"v_mfma_f32_16x16x32_bf16 v[176:179], a[112:115], a[144:147], v[176:179]\n\t"
"ds_read_b128 v[24:27], v230 offset:20480\n\t"
"v_cvt_pk_bf16_f32 v209, v209, v213\n\t"
"v_mfma_f32_16x16x32_bf16 v[176:179], a[116:119], a[160:163], v[176:179]\n\t"
"buffer_load_dwordx4 a[44:47], off, s[28:31], s54 offset:1024 sc1\n\t"
"v_mfma_f32_16x16x32_bf16 v[180:183], a[112:115], a[148:151], v[180:183]\n\t"
"ds_read_b128 a[144:147], v224 offset:16544\n\t"
"v_cvt_pk_bf16_f32 v217, v217, v221\n\t"
"v_mfma_f32_16x16x32_bf16 v[180:183], a[116:119], a[164:167], v[180:183]\n\t"
"ds_read_b128 a[160:163], v224 offset:18592\n\t"
"v_cvt_pk_bf16_f32 v202, v202, v206\n\t"
"v_mfma_f32_16x16x32_bf16 v[164:167], a[104:107], a[148:151], v[164:167]\n\t"
"ds_read_b128 v[12:15], v229 offset:33792\n\t"
"v_cvt_pk_bf16_f32 v203, v203, v207\n\t"
"v_mfma_f32_16x16x32_bf16 v[164:167], a[108:111], a[164:167], v[164:167]\n\t"
"buffer_load_dwordx4 a[36:39], off, s[84:87], s54 offset:3072 sc1\n\t"
"v_mfma_f32_16x16x32_bf16 v[148:151], a[96:99], a[148:151], v[148:151]\n\t"
"ds_read_b128 a[148:151], v224 offset:16800\n\t"
"v_cvt_pk_bf16_f32 v193, v193, v197\n\t"
"v_mfma_f32_16x16x32_bf16 v[148:151], a[100:103], a[164:167], v[148:151]\n\t"
"ds_read_b128 v[28:31], v230 offset:33792\n\t"
"v_cvt_pk_bf16_f32 v201, v201, v205\n\t"
"v_mfma_f32_16x16x32_bf16 v[152:155], a[96:99], a[152:155], v[152:155]\n\t"
"ds_read_b128 a[164:167], v224 offset:18848\n\t"
"v_cvt_pk_bf16_f32 v219, v219, v223\n\t"
"v_mfma_f32_16x16x32_bf16 v[152:155], a[100:103], a[168:171], v[152:155]\n\t"
"buffer_load_dwordx4 a[32:35], off, s[84:87], s54 offset:2048 sc1\n\t"
"v_mfma_f32_16x16x32_bf16 v[168:171], a[104:107], a[152:155], v[168:171]\n\t"
"ds_write_addtid_b32 v209 offset:9344\n\t"
"v_cvt_pk_bf16_f32 v211, v211, v215\n\t"
"v_mfma_f32_16x16x32_bf16 v[168:171], a[108:111], a[168:171], v[168:171]\n\t"
"ds_write_addtid_b32 v203 offset:14496\n\t"
"v_cvt_pk_bf16_f32 v210, v210, v214\n\t"
"v_mfma_f32_16x16x32_bf16 v[184:187], a[112:115], a[152:155], v[184:187]\n\t"
"s_nop 0\n\t"
"ds_read_b128 a[152:155], v224 offset:17056\n\t"
"ds_write_addtid_b32 v210 offset:5152\n\t"
"v_mfma_f32_16x16x32_bf16 v[184:187], a[116:119], a[168:171], v[184:187]\n\t"
"s_waitcnt lgkmcnt(8)\n\t"
"buffer_store_dwordx4 v[24:27], v233, s[16:19], s24 offen nt\n\t"
"v_mfma_f32_16x16x32_bf16 v[188:191], a[112:115], a[156:159], v[188:191]\n\t"
"ds_read_b128 v[44:47], v231 offset:33792\n\t"
"v_cvt_pk_bf16_f32 v218, v218, v222\n\t"
"v_mfma_f32_16x16x32_bf16 v[188:191], a[116:119], a[172:175], v[188:191]\n\t"
"ds_write_addtid_b32 v208 offset:1024\n\t"
"v_cvt_pk_bf16_f32 v192, v192, v196\n\t"
"v_mfma_f32_16x16x32_bf16 v[172:175], a[104:107], a[156:159], v[172:175]\n\t"
"s_nop 0\n\t"
"ds_write_addtid_b32 v201 offset:10368\n\t"
"ds_write_addtid_b32 v218 offset:7200\n\t"
"v_mfma_f32_16x16x32_bf16 v[172:175], a[108:111], a[172:175], v[172:175]\n\t"
"buffer_load_dwordx4 a[24:27], off, s[84:87], s54 offset:0 sc1\n\t"
"v_mfma_f32_16x16x32_bf16 v[156:159], a[96:99], a[156:159], v[156:159]\n\t"
"s_nop 0\n\t"
"ds_write_addtid_b32 v211 offset:13472\n\t"
"ds_read_b128 v[40:43], v231 offset:20480\n\t"
"v_mfma_f32_16x16x32_bf16 v[156:159], a[100:103], a[172:175], v[156:159]\n\t"
"s_waitcnt vmcnt(13) lgkmcnt(10)\n\t"
"ds_write_addtid_b32 v217 offset:11392\n\t"
"buffer_load_dwordx4 a[28:31], off, s[84:87], s54 offset:1024 sc1\n\t"
"v_mfma_f32_16x16x32_bf16 v[96:99], a[120:123], a[144:147], v[96:99]\n\t"
"s_nop 0\n\t"
"ds_write_addtid_b32 v192 offset:0\n\t"
"ds_write_addtid_b32 v202 offset:6176\n\t"
"v_mfma_f32_16x16x32_bf16 v[96:99], a[124:127], a[160:163], v[96:99]\n\t"
"ds_write_addtid_b32 v193 offset:8320\n\t"
"v_cvt_pk_bf16_f32 v216, v216, v220\n\t"
"v_mfma_f32_16x16x32_bf16 v[112:115], a[128:131], a[144:147], v[112:115]\n\t"
"s_nop 0\n\t"
"ds_write_addtid_b32 v216 offset:3072\n\t"
"ds_write_addtid_b32 v219 offset:15520\n\t"
"v_mfma_f32_16x16x32_bf16 v[112:115], a[132:135], a[160:163], v[112:115]\n\t"
"buffer_load_dwordx4 a[64:67], off, s[28:31], 0 offset:0 sc1\n\t"
"v_mfma_f32_16x16x32_bf16 v[128:131], a[136:139], a[144:147], v[128:131]\n\t"
"s_nop 0\n\t"
"ds_read_b128 a[156:159], v224 offset:17312\n\t"
"ds_read_b128 a[172:175], v224 offset:19360\n\t"
"v_mfma_f32_16x16x32_bf16 v[128:131], a[140:143], a[160:163], v[128:131]\n\t"
"ds_read_b128 a[168:171], v224 offset:19104\n\t"
"v_mfma_f32_16x16x32_bf16 v[132:135], a[136:139], a[148:151], v[132:135]\n\t"
"buffer_load_dwordx4 a[68:71], off, s[28:31], 0 offset:1024 sc1\n\t"
"v_mfma_f32_16x16x32_bf16 v[132:135], a[140:143], a[164:167], v[132:135]\n\t"
"s_nop 0\n\t"
"ds_read_b64_tr_b16 a[200:201], v250 offset:1152\n\t"
"ds_read_b64_tr_b16 a[202:203], v250 offset:1664\n\t"
"v_mfma_f32_16x16x32_bf16 v[116:119], a[128:131], a[148:151], v[116:119]\n\t"
"s_nop 0\n\t"
"ds_read_b64_tr_b16 a[204:205], v251 offset:1152\n\t"
"ds_read_b64_tr_b16 a[206:207], v251 offset:1664\n\t"
"v_mfma_f32_16x16x32_bf16 v[116:119], a[132:135], a[164:167], v[116:119]\n\t"
"s_waitcnt lgkmcnt(12)\n\t"
"buffer_store_dwordx4 v[40:43], v234, s[16:19], s24 offen nt\n\t"
"v_mfma_f32_16x16x32_bf16 v[100:103], a[120:123], a[148:151], v[100:103]\n\t"
"v_mfma_f32_16x16x32_bf16 v[100:103], a[124:127], a[164:167], v[100:103]\n\t"
"s_waitcnt lgkmcnt(4)\n\t"
"s_barrier\n\t"
"v_mfma_f32_16x16x32_bf16 v[104:107], a[120:123], a[152:155], v[104:107]\n\t"
"ds_read_b128 a[144:147], v224 offset:0\n\t"
"v_mfma_f32_16x16x32_bf16 v[104:107], a[124:127], a[168:171], v[104:107]\n\t"
"ds_read_b128 a[160:163], v224 offset:2048\n\t"
"v_add_u32 v248, 122368, v227\n\t"
"v_mfma_f32_16x16x32_bf16 v[120:123], a[128:131], a[152:155], v[120:123]\n\t"
"ds_read_b128 a[148:151], v224 offset:256\n\t"
"v_mfma_f32_16x16x32_bf16 v[120:123], a[132:135], a[168:171], v[120:123]\n\t"
"ds_read_b128 a[164:167], v224 offset:2304\n\t"
"v_xor_b32 v249, 32, v248\n\t"
"v_mfma_f32_16x16x32_bf16 v[136:139], a[136:139], a[152:155], v[136:139]\n\t"
"v_xor_b32 v250, 64, v248\n\t"
"v_xor_b32 v251, 96, v248\n\t"
"v_mfma_f32_16x16x32_bf16 v[136:139], a[140:143], a[168:171], v[136:139]\n\t"
"buffer_load_dwordx4 a[60:63], off, s[84:87], 0 offset:3072 sc1\n\t"
"v_mfma_f32_16x16x32_bf16 v[140:143], a[136:139], a[156:159], v[140:143]\n\t"
"ds_read_b128 a[152:155], v224 offset:512\n\t"
"v_mfma_f32_16x16x32_bf16 v[140:143], a[140:143], a[172:175], v[140:143]\n\t"
"ds_read_b128 a[168:171], v224 offset:2560\n\t"
"v_mfma_f32_16x16x32_bf16 v[124:127], a[128:131], a[156:159], v[124:127]\n\t"
"v_mfma_f32_16x16x32_bf16 v[124:127], a[132:135], a[172:175], v[124:127]\n\t"
"buffer_load_dwordx4 a[56:59], off, s[84:87], 0 offset:2048 sc1\n\t"
"v_mfma_f32_16x16x32_bf16 v[108:111], a[120:123], a[156:159], v[108:111]\n\t"
"ds_read_b128 a[156:159], v224 offset:768\n\t"
"v_mfma_f32_16x16x32_bf16 v[108:111], a[124:127], a[172:175], v[108:111]\n\t"
"ds_read_b128 a[172:175], v224 offset:2816\n\t"
"v_mfma_f32_16x16x32_bf16 v[192:195], a[224:227], a[176:179], 0\n\t"
"s_nop 0\n\t"
"ds_read_b64_tr_b16 a[176:177], v248 offset:20736\n\t"
"ds_read_b64_tr_b16 a[178:179], v248 offset:21248\n\t"
"v_mfma_f32_16x16x32_bf16 v[196:199], a[224:227], a[180:183], 0\n\t"
"buffer_load_dwordx4 a[48:51], off, s[84:87], 0 offset:0 sc1\n\t"
"v_mfma_f32_16x16x32_bf16 v[200:203], a[224:227], a[184:187], 0\n\t"
"s_nop 0\n\t"
"ds_read_b64_tr_b16 a[180:181], v249 offset:20736\n\t"
"ds_read_b64_tr_b16 a[182:183], v249 offset:21248\n\t"
"v_mfma_f32_16x16x32_bf16 v[204:207], a[224:227], a[188:191], 0\n\t"
"buffer_load_dwordx4 a[52:55], off, s[84:87], 0 offset:1024 sc1\n\t"
"v_mfma_f32_16x16x32_bf16 v[208:211], a[224:227], a[192:195], 0\n\t"
"s_nop 0\n\t"
"ds_read_b64_tr_b16 a[184:185], v250 offset:20736\n\t"
"ds_read_b64_tr_b16 a[186:187], v250 offset:21248\n\t"
"v_mfma_f32_16x16x32_bf16 v[212:215], a[224:227], a[196:199], 0\n\t"
"s_waitcnt lgkmcnt(8)\n\t"
"ds_read_b64_tr_b16 a[188:189], v251 offset:20736\n\t"
"ds_read_b64_tr_b16 a[190:191], v251 offset:21248\n\t"
"v_mfma_f32_16x16x32_bf16 v[216:219], a[224:227], a[200:203], 0\n\t"
".p2align 3\n\t"
"ds_read_b64_tr_b16 a[192:193], v248 offset:0\n\t"
"buffer_load_dwordx4 a[88:91], off, s[28:31], s51 offset:0 sc1\n\t"
"v_pk_add_f32 v[96:97], v[96:97], v[144:145]\n\t"
"v_pk_add_f32 v[98:99], v[98:99], v[146:147]\n\t"
"v_pk_fma_f32 v[144:145], v[144:145], s[38:39], v[96:97]\n\t"
"v_pk_fma_f32 v[146:147], v[146:147], s[38:39], v[98:99]\n\t"
"v_pk_add_f32 v[112:113], v[112:113], v[160:161]\n\t"
"v_pk_add_f32 v[114:115], v[114:115], v[162:163]\n\t"
"v_pk_fma_f32 v[160:161], v[160:161], s[38:39], v[112:113]\n\t"
"v_pk_fma_f32 v[162:163], v[162:163], s[38:39], v[114:115]\n\t"
"v_pk_add_f32 v[128:129], v[128:129], v[176:177]\n\t"
"v_pk_add_f32 v[130:131], v[130:131], v[178:179]\n\t"
"v_pk_fma_f32 v[176:177], v[176:177], s[38:39], v[128:129]\n\t"
"v_pk_fma_f32 v[178:179], v[178:179], s[38:39], v[130:131]\n\t"
"buffer_load_dwordx4 a[92:95], off, s[28:31], s51 offset:1024 sc1\n\t"
"v_pk_add_f32 v[132:133], v[132:133], v[180:181]\n\t"
"v_pk_add_f32 v[134:135], v[134:135], v[182:183]\n\t"
"v_pk_fma_f32 v[180:181], v[180:181], s[38:39], v[132:133]\n\t"
"v_pk_fma_f32 v[182:183], v[182:183], s[38:39], v[134:135]\n\t"
"v_pk_add_f32 v[116:117], v[116:117], v[164:165]\n\t"
"v_pk_add_f32 v[118:119], v[118:119], v[166:167]\n\t"
"v_pk_fma_f32 v[164:165], v[164:165], s[38:39], v[116:117]\n\t"
"v_pk_fma_f32 v[166:167], v[166:167], s[38:39], v[118:119]\n\t"
"v_pk_add_f32 v[100:101], v[100:101], v[148:149]\n\t"
"v_pk_add_f32 v[102:103], v[102:103], v[150:151]\n\t"
"v_pk_fma_f32 v[148:149], v[148:149], s[38:39], v[100:101]\n\t"
"v_pk_fma_f32 v[150:151], v[150:151], s[38:39], v[102:103]\n\t"
"buffer_load_dwordx4 a[84:87], off, s[84:87], s51 offset:3072 sc1\n\t"
"v_pk_add_f32 v[104:105], v[104:105], v[152:153]\n\t"
"v_pk_add_f32 v[106:107], v[106:107], v[154:155]\n\t"
"v_pk_fma_f32 v[152:153], v[152:153], s[38:39], v[104:105]\n\t"
"v_pk_fma_f32 v[154:155], v[154:155], s[38:39], v[106:107]\n\t"
"v_pk_add_f32 v[120:121], v[120:121], v[168:169]\n\t"
"v_pk_add_f32 v[122:123], v[122:123], v[170:171]\n\t"
"v_pk_fma_f32 v[168:169], v[168:169], s[38:39], v[120:121]\n\t"
"v_pk_fma_f32 v[170:171], v[170:171], s[38:39], v[122:123]\n\t"
"v_pk_add_f32 v[136:137], v[136:137], v[184:185]\n\t"
"v_pk_add_f32 v[138:139], v[138:139], v[186:187]\n\t"
"v_pk_fma_f32 v[184:185], v[184:185], s[38:39], v[136:137]\n\t"
"v_pk_fma_f32 v[186:187], v[186:187], s[38:39], v[138:139]\n\t"
"buffer_load_dwordx4 a[80:83], off, s[84:87], s51 offset:2048 sc1\n\t"
"v_pk_add_f32 v[140:141], v[140:141], v[188:189]\n\t"
"v_pk_add_f32 v[142:143], v[142:143], v[190:191]\n\t"
"v_pk_fma_f32 v[188:189], v[188:189], s[38:39], v[140:141]\n\t"
"v_pk_fma_f32 v[190:191], v[190:191], s[38:39], v[142:143]\n\t"
"v_pk_add_f32 v[124:125], v[124:125], v[172:173]\n\t"
"v_pk_add_f32 v[126:127], v[126:127], v[174:175]\n\t"
"v_pk_fma_f32 v[172:173], v[172:173], s[38:39], v[124:125]\n\t"
"v_pk_fma_f32 v[174:175], v[174:175], s[38:39], v[126:127]\n\t"
"v_pk_add_f32 v[108:109], v[108:109], v[156:157]\n\t"
"v_pk_add_f32 v[110:111], v[110:111], v[158:159]\n\t"
"v_pk_fma_f32 v[156:157], v[156:157], s[38:39], v[108:109]\n\t"
"v_pk_fma_f32 v[158:159], v[158:159], s[38:39], v[110:111]\n\t"
"v_cvt_pk_bf16_f32 v96, v96, v97\n\t"
"v_cvt_pk_bf16_f32 v97, v98, v99\n\t"
"v_cvt_pk_bf16_f32 v100, v100, v101\n\t"
"v_cvt_pk_bf16_f32 v124, v124, v125\n\t"
"v_mfma_f32_16x16x32_bf16 v[220:223], a[224:227], a[204:207], 0\n\t"
"s_waitcnt vmcnt(19)\n\t"
"ds_read_b64_tr_b16 a[194:195], v248 offset:512\n\t"
"buffer_store_dwordx4 v[12:15], v232, s[16:19], s27 offen nt\n\t"
"v_cvt_pk_bf16_f32 v195, v195, v199\n\t"
"v_cvt_pk_bf16_f32 v116, v116, v117\n\t"
"v_mfma_f32_16x16x32_bf16 v[48:51], a[0:3], a[144:147], v[48:51]\n\t"
"v_cvt_pk_bf16_f32 v192, v192, v196\n\t"
"v_cvt_pk_bf16_f32 v101, v102, v103\n\t"
"v_mfma_f32_16x16x32_bf16 v[48:51], a[4:7], a[160:163], v[48:51]\n\t"
"ds_write_addtid_b32 v195 offset:28992\n\t"
"ds_write2st64_b64 v228, v[96:97], v[100:101] offset0:40 offset1:53\n\t"
"v_cvt_pk_bf16_f32 v194, v194, v198\n\t"
"v_cvt_pk_bf16_f32 v128, v128, v129\n\t"
"v_mfma_f32_16x16x32_bf16 v[64:67], a[8:11], a[144:147], v[64:67]\n\t"
"v_cvt_pk_bf16_f32 v117, v118, v119\n\t"
"v_cvt_pk_bf16_f32 v129, v130, v131\n\t"
"v_mfma_f32_16x16x32_bf16 v[64:67], a[12:15], a[160:163], v[64:67]\n\t"
"buffer_load_dwordx4 a[72:75], off, s[84:87], s51 offset:0 sc1\n\t"
"v_cvt_pk_bf16_f32 v112, v112, v113\n\t"
"v_cvt_pk_bf16_f32 v108, v108, v109\n\t"
"v_mfma_f32_16x16x32_bf16 v[80:83], a[16:19], a[144:147], v[80:83]\n\t"
"v_cvt_pk_bf16_f32 v210, v210, v214\n\t"
"v_cvt_pk_bf16_f32 v113, v114, v115\n\t"
"v_mfma_f32_16x16x32_bf16 v[80:83], a[20:23], a[160:163], v[80:83]\n\t"
"s_nop 0\n\t"
"ds_read_b128 a[144:147], v224 offset:1024\n\t"
"ds_read_b128 a[160:163], v224 offset:3072\n\t"
"ds_write2st64_b64 v246, v[112:113], v[116:117] offset0:40 offset1:53\n\t"
"v_cvt_pk_bf16_f32 v132, v132, v133\n\t"
"v_cvt_pk_bf16_f32 v120, v120, v121\n\t"
"v_mfma_f32_16x16x32_bf16 v[84:87], a[16:19], a[148:151], v[84:87]\n\t"
"v_cvt_pk_bf16_f32 v104, v104, v105\n\t"
"v_cvt_pk_bf16_f32 v136, v136, v137\n\t"
"v_mfma_f32_16x16x32_bf16 v[84:87], a[20:23], a[164:167], v[84:87]\n\t"
"buffer_load_dwordx4 a[76:79], off, s[84:87], s51 offset:1024 sc1\n\t"
"v_cvt_pk_bf16_f32 v201, v201, v205\n\t"
"v_cvt_pk_bf16_f32 v125, v126, v127\n\t"
"v_mfma_f32_16x16x32_bf16 v[68:71], a[8:11], a[148:151], v[68:71]\n\t"
"v_cvt_pk_bf16_f32 v133, v134, v135\n\t"
"v_cvt_pk_bf16_f32 v109, v110, v111\n\t"
"v_mfma_f32_16x16x32_bf16 v[68:71], a[12:15], a[164:167], v[68:71]\n\t"
"ds_write_addtid_b32 v194 offset:20672\n\t"
"ds_write2st64_b64 v247, v[128:129], v[132:133] offset0:40 offset1:53\n\t"
"v_mfma_f32_16x16x32_bf16 v[52:55], a[0:3], a[148:151], v[52:55]\n\t"
"v_cvt_pk_bf16_f32 v105, v106, v107\n\t"
"v_cvt_pk_bf16_f32 v140, v140, v141\n\t"
"v_mfma_f32_16x16x32_bf16 v[52:55], a[4:7], a[164:167], v[52:55]\n\t"
"buffer_load_dwordx4 a[112:115], off, s[28:31], s81 offset:0 sc1\n\t"
"v_mfma_f32_16x16x32_bf16 v[56:59], a[0:3], a[152:155], v[56:59]\n\t"
"v_cvt_pk_bf16_f32 v203, v203, v207\n\t"
"v_cvt_pk_bf16_f32 v208, v208, v212\n\t"
"v_mfma_f32_16x16x32_bf16 v[56:59], a[4:7], a[168:171], v[56:59]\n\t"
"s_nop 0\n\t"
"ds_read_b128 a[148:151], v224 offset:1280\n\t"
"ds_read_b128 a[164:167], v224 offset:3328\n\t"
"ds_write2st64_b64 v228, v[104:105], v[108:109] offset0:66 offset1:79\n\t"
"v_mfma_f32_16x16x32_bf16 v[72:75], a[8:11], a[152:155], v[72:75]\n\t"
"v_cvt_pk_bf16_f32 v202, v202, v206\n\t"
"v_cvt_pk_bf16_f32 v217, v217, v221\n\t"
"v_mfma_f32_16x16x32_bf16 v[72:75], a[12:15], a[168:171], v[72:75]\n\t"
"buffer_store_dwordx4 v[28:31], v233, s[16:19], s27 offen nt\n\t"
"v_mfma_f32_16x16x32_bf16 v[88:91], a[16:19], a[152:155], v[88:91]\n\t"
"v_cvt_pk_bf16_f32 v211, v211, v215\n\t"
"v_cvt_pk_bf16_f32 v121, v122, v123\n\t"
"v_mfma_f32_16x16x32_bf16 v[88:91], a[20:23], a[168:171], v[88:91]\n\t"
"s_waitcnt lgkmcnt(5)\n\t"
"ds_read_b128 a[152:155], v224 offset:1536\n\t"
"ds_read_b128 a[168:171], v224 offset:3584\n\t"
"ds_write2st64_b64 v246, v[120:121], v[124:125] offset0:66 offset1:79\n\t"
"v_mfma_f32_16x16x32_bf16 v[92:95], a[16:19], a[156:159], v[92:95]\n\t"
"v_cvt_pk_bf16_f32 v216, v216, v220\n\t"
"v_cvt_pk_bf16_f32 v137, v138, v139\n\t"
"v_mfma_f32_16x16x32_bf16 v[92:95], a[20:23], a[172:175], v[92:95]\n\t"
"buffer_load_dwordx4 a[116:119], off, s[28:31], s81 offset:1024 sc1\n\t"
"v_mfma_f32_16x16x32_bf16 v[76:79], a[8:11], a[156:159], v[76:79]\n\t"
"v_cvt_pk_bf16_f32 v200, v200, v204\n\t"
"v_cvt_pk_bf16_f32 v141, v142, v143\n\t"
"v_mfma_f32_16x16x32_bf16 v[76:79], a[12:15], a[172:175], v[76:79]\n\t"
"s_nop 0\n\t"
"ds_write_addtid_b32 v192 offset:16544\n\t"
"ds_write_addtid_b32 v200 offset:18592\n\t"
"ds_write_addtid_b32 v201 offset:26912\n\t"
"ds_write_addtid_b32 v203 offset:31040\n\t"
"ds_write_addtid_b32 v216 offset:19616\n\t"
"ds_write2st64_b64 v247, v[136:137], v[140:141] offset0:66 offset1:79\n\t"
"v_mfma_f32_16x16x32_bf16 v[60:63], a[0:3], a[156:159], v[60:63]\n\t"
"v_cvt_pk_bf16_f32 v193, v193, v197\n\t"
"v_cvt_pk_bf16_f32 v218, v218, v222\n\t"
"v_mfma_f32_16x16x32_bf16 v[60:63], a[4:7], a[172:175], v[60:63]\n\t"
"s_waitcnt vmcnt(17) lgkmcnt(9)\n\t"
"buffer_load_dwordx4 a[108:111], off, s[84:87], s81 offset:3072 sc1\n\t"
"v_mfma_f32_16x16x32_bf16 v[48:51], a[24:27], a[144:147], v[48:51]\n\t"
"v_cvt_pk_bf16_f32 v209, v209, v213\n\t"
"v_cvt_pk_bf16_f32 v219, v219, v223\n\t"
"v_mfma_f32_16x16x32_bf16 v[48:51], a[28:31], a[160:163], v[48:51]\n\t"
"s_nop 0\n\t"
"ds_write_addtid_b32 v193 offset:24864\n\t"
"ds_write_addtid_b32 v202 offset:22720\n\t"
"ds_write_addtid_b32 v209 offset:25888\n\t"
"ds_write_addtid_b32 v211 offset:30016\n\t"
"ds_write_addtid_b32 v217 offset:27936\n\t"
"ds_write_addtid_b32 v218 offset:23744\n\t"
"ds_write_addtid_b32 v219 offset:32064\n\t"
"v_mfma_f32_16x16x32_bf16 v[64:67], a[32:35], a[144:147], v[64:67]\n\t"
"s_nop 0\n\t"
"ds_write_addtid_b32 v208 offset:17568\n\t"
"ds_write_addtid_b32 v210 offset:21696\n\t"
"v_mfma_f32_16x16x32_bf16 v[64:67], a[36:39], a[160:163], v[64:67]\n\t"
"buffer_load_dwordx4 a[104:107], off, s[84:87], s81 offset:2048 sc1\n\t"
"v_mfma_f32_16x16x32_bf16 v[80:83], a[40:43], a[144:147], v[80:83]\n\t"
"s_nop 0\n\t"
"ds_read_b128 a[156:159], v224 offset:1792\n\t"
"ds_read_b128 a[172:175], v224 offset:3840\n\t"
"v_mfma_f32_16x16x32_bf16 v[80:83], a[44:47], a[160:163], v[80:83]\n\t"
"buffer_load_dwordx4 a[96:99], off, s[84:87], s81 offset:0 sc1\n\t"
"v_mfma_f32_16x16x32_bf16 v[84:87], a[40:43], a[148:151], v[84:87]\n\t"
"s_nop 0\n\t"
"ds_read_b64_tr_b16 a[196:197], v249 offset:0\n\t"
"ds_read_b64_tr_b16 a[198:199], v249 offset:512\n\t"
"v_mfma_f32_16x16x32_bf16 v[84:87], a[44:47], a[164:167], v[84:87]\n\t"
"s_nop 0\n\t"
"ds_read_b64_tr_b16 a[200:201], v250 offset:0\n\t"
"ds_read_b64_tr_b16 a[202:203], v250 offset:512\n\t"
"v_mfma_f32_16x16x32_bf16 v[68:71], a[32:35], a[148:151], v[68:71]\n\t"
"s_nop 0\n\t"
"ds_read_b64_tr_b16 a[204:205], v251 offset:0\n\t"
"ds_read_b64_tr_b16 a[206:207], v251 offset:512\n\t"
"v_mfma_f32_16x16x32_bf16 v[68:71], a[36:39], a[164:167], v[68:71]\n\t"
"buffer_store_dwordx4 v[44:47], v234, s[16:19], s27 offen nt\n\t"
"v_mfma_f32_16x16x32_bf16 v[52:55], a[24:27], a[148:151], v[52:55]\n\t"
"s_waitcnt lgkmcnt(6)\n\t"
"v_mfma_f32_16x16x32_bf16 v[52:55], a[28:31], a[164:167], v[52:55]\n\t"
"buffer_load_dwordx4 a[100:103], off, s[84:87], s81 offset:1024 sc1\n\t"
"s_barrier\n\t"
"v_mfma_f32_16x16x32_bf16 v[56:59], a[24:27], a[152:155], v[56:59]\n\t"
"s_nop 0\n\t"
"ds_read_b128 a[144:147], v224 offset:16544\n\t"
"ds_read_b128 a[164:167], v224 offset:18848\n\t"
"v_mfma_f32_16x16x32_bf16 v[56:59], a[28:31], a[168:171], v[56:59]\n\t"
"buffer_load_dwordx4 a[136:139], off, s[28:31], s66 offset:0 sc1\n\t"
"v_mfma_f32_16x16x32_bf16 v[72:75], a[32:35], a[152:155], v[72:75]\n\t"
"s_nop 0\n\t"
"ds_read_b128 a[160:163], v224 offset:18592\n\t"
"ds_read_b128 a[148:151], v224 offset:16800\n\t"
"v_mfma_f32_16x16x32_bf16 v[72:75], a[36:39], a[168:171], v[72:75]\n\t"
"buffer_load_dwordx4 a[140:143], off, s[28:31], s66 offset:1024 sc1\n\t"
"v_mfma_f32_16x16x32_bf16 v[88:91], a[40:43], a[152:155], v[88:91]\n\t"
"ds_read_b128 v[108:111], v229 offset:33792\n\t"
"v_add_u32 v248, 54016, v227\n\t"
"v_mfma_f32_16x16x32_bf16 v[88:91], a[44:47], a[168:171], v[88:91]\n\t"
"ds_read_b128 v[104:107], v229 offset:20480\n\t"
"v_xor_b32 v249, 32, v248\n\t"
"v_mfma_f32_16x16x32_bf16 v[92:95], a[40:43], a[156:159], v[92:95]\n\t"
"s_nop 0\n\t"
"ds_read_b128 v[136:139], v231 offset:20480\n\t"
"ds_read_b128 a[232:235], v230 offset:33792\n\t"
"v_mfma_f32_16x16x32_bf16 v[92:95], a[44:47], a[172:175], v[92:95]\n\t"
"buffer_load_dwordx4 a[132:135], off, s[84:87], s66 offset:3072 sc1\n\t"
"v_mfma_f32_16x16x32_bf16 v[76:79], a[32:35], a[156:159], v[76:79]\n\t"
"ds_read_b128 a[236:239], v231 offset:33792\n\t"
"v_xor_b32 v250, 64, v248\n\t"
"v_mfma_f32_16x16x32_bf16 v[76:79], a[36:39], a[172:175], v[76:79]\n\t"
"ds_read_b128 a[152:155], v224 offset:17056\n\t"
"v_xor_b32 v251, 96, v248\n\t"
"v_mfma_f32_16x16x32_bf16 v[60:63], a[24:27], a[156:159], v[60:63]\n\t"
"ds_read_b128 v[120:123], v230 offset:20480\n\t"
"v_mfma_f32_16x16x32_bf16 v[60:63], a[28:31], a[172:175], v[60:63]\n\t"
"buffer_load_dwordx4 a[128:131], off, s[84:87], s66 offset:2048 sc1\n\t"
"v_mfma_f32_16x16x32_bf16 v[192:195], a[216:219], a[176:179], 0\n\t"
"s_nop 0\n\t"
"ds_read_b128 a[168:171], v224 offset:19104\n\t"
"ds_read_b64_tr_b16 a[176:177], v248 offset:21888\n\t"
"v_mfma_f32_16x16x32_bf16 v[196:199], a[216:219], a[180:183], 0\n\t"
"ds_read_b64_tr_b16 a[178:179], v248 offset:22400\n\t"
"v_mfma_f32_16x16x32_bf16 v[200:203], a[216:219], a[184:187], 0\n\t"
"s_waitcnt lgkmcnt(8)\n\t"
"buffer_store_dwordx4 v[104:107], v232, s[16:19], s20 offen nt\n\t"
"v_mfma_f32_16x16x32_bf16 v[204:207], a[216:219], a[188:191], 0\n\t"
"s_nop 0\n\t"
"ds_read_b64_tr_b16 a[180:181], v249 offset:21888\n\t"
"ds_read_b64_tr_b16 a[182:183], v249 offset:22400\n\t"
"v_mfma_f32_16x16x32_bf16 v[208:211], a[216:219], a[192:195], 0\n\t"
"s_nop 0\n\t"
"ds_read_b64_tr_b16 a[184:185], v250 offset:21888\n\t"
"ds_read_b64_tr_b16 a[186:187], v250 offset:22400\n\t"
"v_mfma_f32_16x16x32_bf16 v[212:215], a[216:219], a[196:199], 0\n\t"
"s_waitcnt lgkmcnt(7)\n\t"
"s_barrier\n\t"
"v_mfma_f32_16x16x32_bf16 v[216:219], a[216:219], a[200:203], 0\n\t"
"buffer_load_dwordx4 a[120:123], off, s[84:87], s66 offset:0 sc1\n\t"
"v_mfma_f32_16x16x32_bf16 v[220:223], a[216:219], a[204:207], 0\n\t"
"s_waitcnt vmcnt(21)\n\t"
"ds_read_b128 a[156:159], v224 offset:17312\n\t"
"v_cvt_pk_bf16_f32 v195, v195, v199\n\t"
"v_mfma_f32_16x16x32_bf16 v[48:51], a[48:51], a[144:147], v[48:51]\n\t"
"ds_read_b128 a[172:175], v224 offset:19360\n\t"
"v_cvt_pk_bf16_f32 v194, v194, v198\n\t"
"v_mfma_f32_16x16x32_bf16 v[48:51], a[52:55], a[160:163], v[48:51]\n\t"
"ds_read_b64_tr_b16 a[188:189], v251 offset:21888\n\t"
"v_cvt_pk_bf16_f32 v200, v200, v204\n\t"
"v_mfma_f32_16x16x32_bf16 v[64:67], a[56:59], a[144:147], v[64:67]\n\t"
"ds_read_b64_tr_b16 a[190:191], v251 offset:22400\n\t"
"v_cvt_pk_bf16_f32 v203, v203, v207\n\t"
"v_mfma_f32_16x16x32_bf16 v[64:67], a[60:63], a[160:163], v[64:67]\n\t"
"buffer_load_dwordx4 a[124:127], off, s[84:87], s66 offset:1024 sc1\n\t"
"v_mfma_f32_16x16x32_bf16 v[80:83], a[64:67], a[144:147], v[80:83]\n\t"
"ds_read_b128 a[144:147], v224 offset:17568\n\t"
"v_cvt_pk_bf16_f32 v201, v201, v205\n\t"
"v_mfma_f32_16x16x32_bf16 v[80:83], a[68:71], a[160:163], v[80:83]\n\t"
"ds_read_b128 a[160:163], v224 offset:19616\n\t"
"v_cvt_pk_bf16_f32 v208, v208, v212\n\t"
"v_mfma_f32_16x16x32_bf16 v[84:87], a[64:67], a[148:151], v[84:87]\n\t"
"ds_write_addtid_b32 v194 offset:4128\n\t"
"v_cvt_pk_bf16_f32 v219, v219, v223\n\t"
"v_mfma_f32_16x16x32_bf16 v[84:87], a[68:71], a[164:167], v[84:87]\n\t"
"ds_write_addtid_b32 v203 offset:14496\n\t"
"buffer_store_dwordx4 v[120:123], v233, s[16:19], s20 offen nt\n\t"
"v_mfma_f32_16x16x32_bf16 v[68:71], a[56:59], a[148:151], v[68:71]\n\t"
"ds_write_addtid_b32 v195 offset:12448\n\t"
"v_cvt_pk_bf16_f32 v216, v216, v220\n\t"
"v_mfma_f32_16x16x32_bf16 v[68:71], a[60:63], a[164:167], v[68:71]\n\t"
"ds_write_addtid_b32 v208 offset:1024\n\t"
"v_cvt_pk_bf16_f32 v192, v192, v196\n\t"
"v_mfma_f32_16x16x32_bf16 v[52:55], a[48:51], a[148:151], v[52:55]\n\t"
"ds_read_b128 a[148:151], v224 offset:17824\n\t"
"v_cvt_pk_bf16_f32 v210, v210, v214\n\t"
"v_mfma_f32_16x16x32_bf16 v[52:55], a[52:55], a[164:167], v[52:55]\n\t"
"s_waitcnt lgkmcnt(8)\n\t"
"ds_write_addtid_b32 v216 offset:3072\n\t"
"v_cvt_pk_bf16_f32 v202, v202, v206\n\t"
"v_mfma_f32_16x16x32_bf16 v[56:59], a[48:51], a[152:155], v[56:59]\n\t"
"ds_read_b128 a[164:167], v224 offset:19872\n\t"
"v_cvt_pk_bf16_f32 v209, v209, v213\n\t"
"v_mfma_f32_16x16x32_bf16 v[56:59], a[52:55], a[168:171], v[56:59]\n\t"
"ds_write_addtid_b32 v210 offset:5152\n\t"
"v_cvt_pk_bf16_f32 v217, v217, v221\n\t"
"v_mfma_f32_16x16x32_bf16 v[72:75], a[56:59], a[152:155], v[72:75]\n\t"
"s_nop 0\n\t"
"ds_write_addtid_b32 v201 offset:10368\n\t"
"ds_write_addtid_b32 v217 offset:11392\n\t"
"v_mfma_f32_16x16x32_bf16 v[72:75], a[60:63], a[168:171], v[72:75]\n\t"
"buffer_store_dwordx4 v[136:139], v234, s[16:19], s20 offen nt\n\t"
"v_mfma_f32_16x16x32_bf16 v[88:91], a[64:67], a[152:155], v[88:91]\n\t"
"s_nop 0\n\t"
"ds_write_addtid_b32 v202 offset:6176\n\t"
"ds_write_addtid_b32 v209 offset:9344\n\t"
"v_mfma_f32_16x16x32_bf16 v[88:91], a[68:71], a[168:171], v[88:91]\n\t"
"s_nop 0\n\t"
"ds_write_addtid_b32 v200 offset:2048\n\t"
"ds_write_addtid_b32 v219 offset:15520\n\t"
"v_mfma_f32_16x16x32_bf16 v[92:95], a[64:67], a[156:159], v[92:95]\n\t"
"s_add_u32 m0, s26, 103936\n\t"
"ds_read_b128 a[152:155], v224 offset:18080\n\t"
"v_cvt_pk_bf16_f32 v193, v193, v197\n\t"
"v_mfma_f32_16x16x32_bf16 v[92:95], a[68:71], a[172:175], v[92:95]\n\t"
"buffer_load_dwordx4 v236, s[12:15], s22 offen offset:0 nt lds\n\t"
"v_mfma_f32_16x16x32_bf16 v[76:79], a[56:59], a[156:159], v[76:79]\n\t"
"ds_read_b128 a[168:171], v224 offset:20128\n\t"
"v_cvt_pk_bf16_f32 v218, v218, v222\n\t"
"v_mfma_f32_16x16x32_bf16 v[76:79], a[60:63], a[172:175], v[76:79]\n\t"
"buffer_load_dwordx4 v237, s[12:15], s22 offen offset:1024 nt lds\n\t"
"v_mfma_f32_16x16x32_bf16 v[60:63], a[48:51], a[156:159], v[60:63]\n\t"
"s_lshl_b32 m0, s73, 8\n\t"
"ds_read_b128 a[156:159], v224 offset:18336\n\t"
"v_cvt_pk_bf16_f32 v211, v211, v215\n\t"
"v_mfma_f32_16x16x32_bf16 v[60:63], a[52:55], a[172:175], v[60:63]\n\t"
"s_waitcnt vmcnt(19) lgkmcnt(9)\n\t"
"ds_write_addtid_b32 v192 offset:0\n\t"
"ds_write_addtid_b32 v193 offset:8320\n\t"
"v_mfma_f32_16x16x32_bf16 v[144:147], a[72:75], a[144:147], v[144:147]\n\t"
"s_nop 0\n\t"
"ds_write_addtid_b32 v211 offset:13472\n\t"
"ds_write_addtid_b32 v218 offset:7200\n\t"
"v_mfma_f32_16x16x32_bf16 v[144:147], a[76:79], a[160:163], v[144:147]\n\t"
"s_add_u32 m0, s26, 103936\n\t"
"ds_read_b128 a[172:175], v224 offset:20384\n\t"
"v_mfma_f32_16x16x32_bf16 v[160:163], a[80:83], a[144:147], v[160:163]\n\t"
"buffer_load_dwordx4 v238, s[12:15], s22 offen offset:2048 nt lds\n\t"
"v_mfma_f32_16x16x32_bf16 v[160:163], a[84:87], a[160:163], v[160:163]\n\t"
"s_nop 0\n\t"
"ds_read_b64_tr_b16 a[192:193], v248 offset:1152\n\t"
"ds_read_b64_tr_b16 a[194:195], v248 offset:1664\n\t"
"v_mfma_f32_16x16x32_bf16 v[176:179], a[88:91], a[144:147], v[176:179]\n\t"
"s_nop 0\n\t"
"ds_read_b64_tr_b16 a[196:197], v249 offset:1152\n\t"
"ds_read_b64_tr_b16 a[198:199], v249 offset:1664\n\t"
"v_mfma_f32_16x16x32_bf16 v[176:179], a[92:95], a[160:163], v[176:179]\n\t"
"buffer_load_dwordx4 v239, s[12:15], s22 offen offset:3072 nt lds\n\t"
"v_mfma_f32_16x16x32_bf16 v[180:183], a[88:91], a[148:151], v[180:183]\n\t"
"s_nop 0\n\t"
"ds_read_b64_tr_b16 a[200:201], v250 offset:1152\n\t"
"ds_read_b64_tr_b16 a[202:203], v250 offset:1664\n\t"
"v_mfma_f32_16x16x32_bf16 v[180:183], a[92:95], a[164:167], v[180:183]\n\t"
"s_mov_b64 exec, s[36:37]\n\t"
"buffer_load_dwordx4 v240, s[12:15], s22 offen offset:3584 nt lds\n\t"
"s_mov_b64 exec, -1\n\t"
"v_mfma_f32_16x16x32_bf16 v[164:167], a[80:83], a[148:151], v[164:167]\n\t"
"s_lshl_b32 m0, s73, 8\n\t"
"ds_read_b64_tr_b16 a[204:205], v251 offset:1152\n\t"
"ds_read_b64_tr_b16 a[206:207], v251 offset:1664\n\t"
"v_mfma_f32_16x16x32_bf16 v[164:167], a[84:87], a[164:167], v[164:167]\n\t"
"buffer_store_dwordx4 v[108:111], v232, s[16:19], s25 offen nt\n\t"
"v_mfma_f32_16x16x32_bf16 v[148:151], a[72:75], a[148:151], v[148:151]\n\t"
"s_waitcnt lgkmcnt(8)\n\t"
"v_mfma_f32_16x16x32_bf16 v[148:151], a[76:79], a[164:167], v[148:151]\n\t"
"s_barrier\n\t"
"v_mfma_f32_16x16x32_bf16 v[152:155], a[72:75], a[152:155], v[152:155]\n\t"
"ds_read_b128 a[148:151], v224 offset:256\n\t"
"v_add_u32 v248, 54016, v226\n\t"
"v_mfma_f32_16x16x32_bf16 v[152:155], a[76:79], a[168:171], v[152:155]\n\t"
"ds_read_b128 a[144:147], v224 offset:0\n\t"
"v_mfma_f32_16x16x32_bf16 v[168:171], a[80:83], a[152:155], v[168:171]\n\t"
"ds_read_b128 a[160:163], v224 offset:2048\n\t"
"v_mfma_f32_16x16x32_bf16 v[168:171], a[84:87], a[168:171], v[168:171]\n\t"
"ds_read_b128 a[164:167], v224 offset:2304\n\t"
"v_mfma_f32_16x16x32_bf16 v[184:187], a[88:91], a[152:155], v[184:187]\n\t"
"v_xor_b32 v249, 32, v248\n\t"
"v_mfma_f32_16x16x32_bf16 v[184:187], a[92:95], a[168:171], v[184:187]\n\t"
"ds_read_b128 a[228:231], v254 offset:18432\n\t"
"v_mfma_f32_16x16x32_bf16 v[188:191], a[88:91], a[156:159], v[188:191]\n\t"
"ds_read_b128 a[168:171], v224 offset:2560\n\t"
"v_mfma_f32_16x16x32_bf16 v[188:191], a[92:95], a[172:175], v[188:191]\n\t"
"ds_read_b128 a[152:155], v224 offset:512\n\t"
"v_mfma_f32_16x16x32_bf16 v[172:175], a[80:83], a[156:159], v[172:175]\n\t"
"v_mfma_f32_16x16x32_bf16 v[172:175], a[84:87], a[172:175], v[172:175]\n\t"
"v_xor_b32 v250, 64, v248\n\t"
"v_mfma_f32_16x16x32_bf16 v[156:159], a[72:75], a[156:159], v[156:159]\n\t"
"ds_read_b128 a[156:159], v224 offset:768\n\t"
"v_xor_b32 v251, 96, v248\n\t"
"v_mfma_f32_16x16x32_bf16 v[156:159], a[76:79], a[172:175], v[156:159]\n\t"
"ds_read_b128 a[172:175], v224 offset:2816\n\t"
"v_mfma_f32_16x16x32_bf16 v[192:195], a[224:227], a[176:179], 0\n\t"
"ds_read_b64_tr_b16 a[178:179], v248 offset:128\n\t"
"v_mfma_f32_16x16x32_bf16 v[196:199], a[224:227], a[180:183], 0\n\t"
"ds_read_b64_tr_b16 a[176:177], v248 offset:0\n\t"
"v_mfma_f32_16x16x32_bf16 v[200:203], a[224:227], a[184:187], 0\n\t"
"ds_read_b64_tr_b16 a[180:181], v249 offset:256\n\t"
"v_mfma_f32_16x16x32_bf16 v[204:207], a[224:227], a[188:191], 0\n\t"
"s_waitcnt lgkmcnt(7)\n\t"
"ds_read_b64_tr_b16 a[186:187], v249 offset:128\n\t"
"v_mfma_f32_16x16x32_bf16 v[208:211], a[224:227], a[192:195], 0\n\t"
"ds_read_b64_tr_b16 a[184:185], v249 offset:0\n\t"
"v_mfma_f32_16x16x32_bf16 v[212:215], a[224:227], a[196:199], 0\n\t"
"ds_read_b64_tr_b16 a[188:189], v248 offset:256\n\t"
"v_mfma_f32_16x16x32_bf16 v[216:219], a[224:227], a[200:203], 0\n\t"
"v_mfma_f32_16x16x32_bf16 v[220:223], a[224:227], a[204:207], 0\n\t"
"ds_write_b128 v255, a[228:231] offset:0\n\t"
"v_mfma_f32_16x16x32_bf16 v[0:3], a[0:3], a[144:147], 0\n\t"
"v_cvt_pk_bf16_f32 v192, v192, v196\n\t"
"v_mfma_f32_16x16x32_bf16 v[0:3], a[4:7], a[160:163], v[0:3]\n\t"
"v_cvt_pk_bf16_f32 v194, v194, v198\n\t"
"v_mfma_f32_16x16x32_bf16 v[16:19], a[8:11], a[144:147], 0\n\t"
"v_mfma_f32_16x16x32_bf16 v[16:19], a[12:15], a[160:163], v[16:19]\n\t"
"ds_read_b64_tr_b16 a[194:195], v250 offset:128\n\t"
"v_cvt_pk_bf16_f32 v209, v209, v213\n\t"
"v_mfma_f32_16x16x32_bf16 v[32:35], a[16:19], a[144:147], 0\n\t"
"ds_read_b128 a[144:147], v224 offset:1024\n\t"
"v_mfma_f32_16x16x32_bf16 v[32:35], a[20:23], a[160:163], v[32:35]\n\t"
"ds_read_b128 a[160:163], v224 offset:3072\n\t"
"v_cvt_pk_bf16_f32 v218, v218, v222\n\t"
"v_mfma_f32_16x16x32_bf16 v[36:39], a[16:19], a[148:151], 0\n\t"
"ds_write_addtid_b32 v192 offset:16544\n\t"
"v_mfma_f32_16x16x32_bf16 v[36:39], a[20:23], a[164:167], v[36:39]\n\t"
"ds_write_addtid_b32 v209 offset:25888\n\t"
"v_cvt_pk_bf16_f32 v200, v200, v204\n\t"
"v_mfma_f32_16x16x32_bf16 v[20:23], a[8:11], a[148:151], 0\n\t"
"ds_write_addtid_b32 v194 offset:20672\n\t"
"v_cvt_pk_bf16_f32 v219, v219, v223\n\t"
"v_mfma_f32_16x16x32_bf16 v[20:23], a[12:15], a[164:167], v[20:23]\n\t"
"ds_write_addtid_b32 v218 offset:23744\n\t"
"v_cvt_pk_bf16_f32 v201, v201, v205\n\t"
"v_mfma_f32_16x16x32_bf16 v[4:7], a[0:3], a[148:151], 0\n\t"
"ds_read_b128 a[148:151], v224 offset:1280\n\t"
"v_mfma_f32_16x16x32_bf16 v[4:7], a[4:7], a[164:167], v[4:7]\n\t"
"s_waitcnt lgkmcnt(8)\n\t"
"ds_write_addtid_b32 v201 offset:26912\n\t"
"v_cvt_pk_bf16_f32 v211, v211, v215\n\t"
"v_mfma_f32_16x16x32_bf16 v[8:11], a[0:3], a[152:155], 0\n\t"
"ds_read_b128 a[164:167], v224 offset:3328\n\t"
"v_mfma_f32_16x16x32_bf16 v[8:11], a[4:7], a[168:171], v[8:11]\n\t"
"ds_write_addtid_b32 v200 offset:18592\n\t"
"v_cvt_pk_bf16_f32 v217, v217, v221\n\t"
"v_mfma_f32_16x16x32_bf16 v[24:27], a[8:11], a[152:155], 0\n\t"
"ds_write_addtid_b32 v211 offset:30016\n\t"
"v_cvt_pk_bf16_f32 v203, v203, v207\n\t"
"v_mfma_f32_16x16x32_bf16 v[24:27], a[12:15], a[168:171], v[24:27]\n\t"
"ds_write_addtid_b32 v217 offset:27936\n\t"
"v_cvt_pk_bf16_f32 v202, v202, v206\n\t"
"v_mfma_f32_16x16x32_bf16 v[40:43], a[16:19], a[152:155], 0\n\t"
"ds_read_b128 a[152:155], v224 offset:1536\n\t"
"v_cvt_pk_bf16_f32 v193, v193, v197\n\t"
"v_mfma_f32_16x16x32_bf16 v[40:43], a[20:23], a[168:171], v[40:43]\n\t"
"ds_read_b128 a[168:171], v224 offset:3584\n\t"
"v_mfma_f32_16x16x32_bf16 v[44:47], a[16:19], a[156:159], 0\n\t"
"ds_write_addtid_b32 v193 offset:24864\n\t"
"v_cvt_pk_bf16_f32 v195, v195, v199\n\t"
"v_mfma_f32_16x16x32_bf16 v[44:47], a[20:23], a[172:175], v[44:47]\n\t"
"ds_write_addtid_b32 v203 offset:31040\n\t"
"v_cvt_pk_bf16_f32 v208, v208, v212\n\t"
"v_mfma_f32_16x16x32_bf16 v[28:31], a[8:11], a[156:159], 0\n\t"
"ds_write_addtid_b32 v219 offset:32064\n\t"
"v_mfma_f32_16x16x32_bf16 v[28:31], a[12:15], a[172:175], v[28:31]\n\t"
"ds_write_addtid_b32 v202 offset:22720\n\t"
"v_cvt_pk_bf16_f32 v216, v216, v220\n\t"
"v_mfma_f32_16x16x32_bf16 v[12:15], a[0:3], a[156:159], 0\n\t"
"ds_read_b128 a[156:159], v224 offset:1792\n\t"
"v_mfma_f32_16x16x32_bf16 v[12:15], a[4:7], a[172:175], v[12:15]\n\t"
"s_waitcnt lgkmcnt(9)\n\t"
"ds_write_addtid_b32 v195 offset:28992\n\t"
"v_cvt_pk_bf16_f32 v210, v210, v214\n\t"
"v_mfma_f32_16x16x32_bf16 v[0:3], a[24:27], a[144:147], v[0:3]\n\t"
"ds_write_addtid_b32 v216 offset:19616\n\t"
"v_mfma_f32_16x16x32_bf16 v[0:3], a[28:31], a[160:163], v[0:3]\n\t"
"ds_write_addtid_b32 v208 offset:17568\n\t"
"v_mfma_f32_16x16x32_bf16 v[16:19], a[32:35], a[144:147], v[16:19]\n\t"
"ds_write_addtid_b32 v210 offset:21696\n\t"
"v_mfma_f32_16x16x32_bf16 v[16:19], a[36:39], a[160:163], v[16:19]\n\t"
"ds_read_b128 a[172:175], v224 offset:3840\n\t"
"v_mfma_f32_16x16x32_bf16 v[32:35], a[40:43], a[144:147], v[32:35]\n\t"
"ds_read_b64_tr_b16 a[192:193], v250 offset:0\n\t"
"v_mfma_f32_16x16x32_bf16 v[32:35], a[44:47], a[160:163], v[32:35]\n\t"
"ds_read_b64_tr_b16 a[196:197], v251 offset:256\n\t"
"v_mfma_f32_16x16x32_bf16 v[36:39], a[40:43], a[148:151], v[36:39]\n\t"
"ds_read_b64_tr_b16 a[202:203], v251 offset:128\n\t"
"v_mfma_f32_16x16x32_bf16 v[36:39], a[44:47], a[164:167], v[36:39]\n\t"
"ds_read_b64_tr_b16 a[200:201], v251 offset:0\n\t"
"v_mfma_f32_16x16x32_bf16 v[20:23], a[32:35], a[148:151], v[20:23]\n\t"
"ds_read_b64_tr_b16 a[204:205], v250 offset:256\n\t"
"v_mfma_f32_16x16x32_bf16 v[20:23], a[36:39], a[164:167], v[20:23]\n\t"
"v_mfma_f32_16x16x32_bf16 v[4:7], a[24:27], a[148:151], v[4:7]\n\t"
"v_mfma_f32_16x16x32_bf16 v[4:7], a[28:31], a[164:167], v[4:7]\n\t"
"s_waitcnt lgkmcnt(0)\n\t"
"s_barrier\n\t"
"v_mfma_f32_16x16x32_bf16 v[8:11], a[24:27], a[152:155], v[8:11]\n\t"
"ds_read_b128 a[144:147], v224 offset:16544\n\t"
"v_mfma_f32_16x16x32_bf16 v[8:11], a[28:31], a[168:171], v[8:11]\n\t"
"ds_read_b128 a[160:163], v224 offset:18592\n\t"
"v_mfma_f32_16x16x32_bf16 v[24:27], a[32:35], a[152:155], v[24:27]\n\t"
"ds_read_b128 a[148:151], v224 offset:16800\n\t"
"v_swap_b32 v236, v241\n\t"
"v_mfma_f32_16x16x32_bf16 v[24:27], a[36:39], a[168:171], v[24:27]\n\t"
"ds_read_b128 a[164:167], v224 offset:18848\n\t"
"v_swap_b32 v237, v242\n\t"
"v_mfma_f32_16x16x32_bf16 v[40:43], a[40:43], a[152:155], v[40:43]\n\t"
"v_mfma_f32_16x16x32_bf16 v[40:43], a[44:47], a[168:171], v[40:43]\n\t"
"v_swap_b32 v238, v243\n\t"
"v_mfma_f32_16x16x32_bf16 v[44:47], a[40:43], a[156:159], v[44:47]\n\t"
"v_mfma_f32_16x16x32_bf16 v[44:47], a[44:47], a[172:175], v[44:47]\n\t"
"ds_write_b128 v254, a[244:247] offset:20736\n\t"
"v_mfma_f32_16x16x32_bf16 v[28:31], a[32:35], a[156:159], v[28:31]\n\t"
"v_swap_b32 v239, v244\n\t"
"v_mfma_f32_16x16x32_bf16 v[28:31], a[36:39], a[172:175], v[28:31]\n\t"
"v_mfma_f32_16x16x32_bf16 v[12:15], a[24:27], a[156:159], v[12:15]\n\t"
"v_swap_b32 v240, v245\n\t"
"v_mfma_f32_16x16x32_bf16 v[12:15], a[28:31], a[172:175], v[12:15]\n\t"
"ds_read_b128 a[172:175], v224 offset:19360\n\t"
"v_mfma_f32_4x4x4_16b_bf16 v[200:203], a[220:221], a[178:179], 0\n\t"
"v_add_u32 v248, 101632, v227\n\t"
"v_mfma_f32_4x4x4_16b_bf16 v[204:207], a[220:221], a[186:187], 0\n\t"
"v_mfma_f32_4x4x4_16b_bf16 v[216:219], a[220:221], a[194:195], 0\n\t"
"v_mfma_f32_4x4x4_16b_bf16 v[220:223], a[220:221], a[202:203], 0\n\t"
"ds_read_b128 a[152:155], v224 offset:17056\n\t"
"v_mfma_f32_4x4x4_16b_bf16 v[192:195], a[222:223], a[176:177], v[200:203]\n\t"
"v_add_u32 v225, v225, v253\n\t"
"v_mfma_f32_4x4x4_16b_bf16 v[196:199], a[222:223], a[184:185], v[204:207]\n\t"
"v_mfma_f32_4x4x4_16b_bf16 v[200:203], a[222:223], a[180:181], v[200:203]\n\t"
"ds_read_b128 a[168:171], v224 offset:19104\n\t"
"v_mfma_f32_4x4x4_16b_bf16 v[204:207], a[222:223], a[188:189], v[204:207]\n\t"
"v_mfma_f32_4x4x4_16b_bf16 v[208:211], a[222:223], a[192:193], v[216:219]\n\t"
"v_sub_u32 v253, 0, v253\n\t"
"v_mfma_f32_4x4x4_16b_bf16 v[212:215], a[222:223], a[200:201], v[220:223]\n\t"
"v_mfma_f32_4x4x4_16b_bf16 v[216:219], a[222:223], a[196:197], v[216:219]\n\t"
"ds_read_b128 a[156:159], v224 offset:17312\n\t"
"v_mfma_f32_4x4x4_16b_bf16 v[220:223], a[222:223], a[204:205], v[220:223]\n\t"
"s_waitcnt vmcnt(15) lgkmcnt(3)\n\t"
"buffer_load_dwordx4 a[16:19], off, s[28:31], s57 offset:0 sc1\n\t"
"v_mfma_f32_16x16x32_bf16 v[144:147], a[96:99], a[144:147], v[144:147]\n\t"
"v_cvt_pk_bf16_f32 v193, -v193, -v197\n\t"
"v_cvt_pk_bf16_f32 v195, -v195, -v199\n\t"
"v_mfma_f32_16x16x32_bf16 v[144:147], a[100:103], a[160:163], v[144:147]\n\t"
"buffer_load_dwordx4 a[20:23], off, s[28:31], s57 offset:1024 sc1\n\t"
"v_mfma_f32_16x16x32_bf16 v[160:163], a[104:107], a[144:147], v[160:163]\n\t"
"ds_write_addtid_b32 v195 offset:5152\n\t"
"v_cvt_pk_bf16_f32 v202, v202, v206\n\t"
"v_mfma_f32_16x16x32_bf16 v[160:163], a[108:111], a[160:163], v[160:163]\n\t"
"buffer_load_dwordx4 a[12:15], off, s[84:87], s57 offset:3072 sc1\n\t"
"v_mfma_f32_16x16x32_bf16 v[176:179], a[112:115], a[144:147], v[176:179]\n\t"
"ds_read_b128 a[144:147], v224 offset:17568\n\t"
"v_cvt_pk_bf16_f32 v194, -v194, -v198\n\t"
"v_mfma_f32_16x16x32_bf16 v[176:179], a[116:119], a[160:163], v[176:179]\n\t"
"ds_write_addtid_b32 v194 offset:1024\n\t"
"v_cvt_pk_bf16_f32 v201, v201, v205\n\t"
"v_mfma_f32_16x16x32_bf16 v[180:183], a[112:115], a[148:151], v[180:183]\n\t"
"ds_read_b128 a[160:163], v224 offset:19616\n\t"
"v_cvt_pk_bf16_f32 v209, -v209, -v213\n\t"
"v_mfma_f32_16x16x32_bf16 v[180:183], a[116:119], a[164:167], v[180:183]\n\t"
"buffer_load_dwordx4 a[8:11], off, s[84:87], s57 offset:2048 sc1\n\t"
"v_mfma_f32_16x16x32_bf16 v[164:167], a[104:107], a[148:151], v[164:167]\n\t"
"ds_write_addtid_b32 v201 offset:12448\n\t"
"v_cvt_pk_bf16_f32 v200, v200, v204\n\t"
"v_mfma_f32_16x16x32_bf16 v[164:167], a[108:111], a[164:167], v[164:167]\n\t"
"ds_write_addtid_b32 v193 offset:4128\n\t"
"v_cvt_pk_bf16_f32 v219, v219, v223\n\t"
"v_mfma_f32_16x16x32_bf16 v[148:151], a[96:99], a[148:151], v[148:151]\n\t"
"ds_read_b128 a[148:151], v224 offset:17824\n\t"
"v_cvt_pk_bf16_f32 v203, v203, v207\n\t"
"v_mfma_f32_16x16x32_bf16 v[148:151], a[100:103], a[164:167], v[148:151]\n\t"
"s_waitcnt lgkmcnt(7)\n\t"
"buffer_load_dwordx4 a[0:3], off, s[84:87], s57 offset:0 sc1\n\t"
"v_mfma_f32_16x16x32_bf16 v[152:155], a[96:99], a[152:155], v[152:155]\n\t"
"ds_read_b128 a[164:167], v224 offset:19872\n\t"
"v_cvt_pk_bf16_f32 v216, v216, v220\n\t"
"v_mfma_f32_16x16x32_bf16 v[152:155], a[100:103], a[168:171], v[152:155]\n\t"
"ds_write_addtid_b32 v203 offset:13472\n\t"
"v_cvt_pk_bf16_f32 v218, v218, v222\n\t"
"v_mfma_f32_16x16x32_bf16 v[168:171], a[104:107], a[152:155], v[168:171]\n\t"
"ds_write_addtid_b32 v202 offset:9344\n\t"
"v_cvt_pk_bf16_f32 v208, -v208, -v212\n\t"
"v_mfma_f32_16x16x32_bf16 v[168:171], a[108:111], a[168:171], v[168:171]\n\t"
"buffer_load_dwordx4 a[4:7], off, s[84:87], s57 offset:1024 sc1\n\t"
"v_mfma_f32_16x16x32_bf16 v[184:187], a[112:115], a[152:155], v[184:187]\n\t"
"ds_read_b128 a[152:155], v224 offset:18080\n\t"
"v_cvt_pk_bf16_f32 v217, v217, v221\n\t"
"v_mfma_f32_16x16x32_bf16 v[184:187], a[116:119], a[168:171], v[184:187]\n\t"
"ds_read_b128 a[168:171], v224 offset:20128\n\t"
"v_cvt_pk_bf16_f32 v211, -v211, -v215\n\t"
"v_mfma_f32_16x16x32_bf16 v[188:191], a[112:115], a[156:159], v[188:191]\n\t"
"ds_write_addtid_b32 v211 offset:7200\n\t"
"v_cvt_pk_bf16_f32 v192, -v192, -v196\n\t"
"v_mfma_f32_16x16x32_bf16 v[188:191], a[116:119], a[172:175], v[188:191]\n\t"
"buffer_load_dwordx4 a[40:43], off, s[28:31], s63 offset:0 sc1\n\t"
"v_mfma_f32_16x16x32_bf16 v[172:175], a[104:107], a[156:159], v[172:175]\n\t"
"ds_write_addtid_b32 v200 offset:8320\n\t"
"v_accvgpr_mov_b32 a228, a208\n\t"
"v_mfma_f32_16x16x32_bf16 v[172:175], a[108:111], a[172:175], v[172:175]\n\t"
"s_nop 0\n\t"
"ds_write_addtid_b32 v192 offset:0\n\t"
"ds_write_addtid_b32 v208 offset:2048\n\t"
"v_mfma_f32_16x16x32_bf16 v[156:159], a[96:99], a[156:159], v[156:159]\n\t"
"ds_write_addtid_b32 v217 offset:14496\n\t"
"v_cvt_pk_bf16_f32 v210, -v210, -v214\n\t"
"v_mfma_f32_16x16x32_bf16 v[156:159], a[100:103], a[172:175], v[156:159]\n\t"
"s_waitcnt vmcnt(15) lgkmcnt(8)\n\t"
"buffer_load_dwordx4 a[44:47], off, s[28:31], s63 offset:1024 sc1\n\t"
"v_mfma_f32_16x16x32_bf16 v[144:147], a[120:123], a[144:147], v[144:147]\n\t"
"s_nop 0\n\t"
"ds_write_addtid_b32 v210 offset:3072\n\t"
"ds_write_addtid_b32 v218 offset:11392\n\t"
"v_mfma_f32_16x16x32_bf16 v[144:147], a[124:127], a[160:163], v[144:147]\n\t"
"ds_write_addtid_b32 v219 offset:15520\n\t"
"v_accvgpr_mov_b32 a208, a209\n\t"
"v_mfma_f32_16x16x32_bf16 v[160:163], a[128:131], a[144:147], v[160:163]\n\t"
"s_nop 0\n\t"
"ds_write_addtid_b32 v209 offset:6176\n\t"
"ds_write_addtid_b32 v216 offset:10368\n\t"
"v_mfma_f32_16x16x32_bf16 v[160:163], a[132:135], a[160:163], v[160:163]\n\t"
"buffer_load_dwordx4 a[36:39], off, s[84:87], s63 offset:3072 sc1\n\t"
"v_mfma_f32_16x16x32_bf16 v[176:179], a[136:139], a[144:147], v[176:179]\n\t"
"ds_read_b128 a[172:175], v224 offset:20384\n\t"
"v_mfma_f32_16x16x32_bf16 v[176:179], a[140:143], a[160:163], v[176:179]\n\t"
"buffer_load_dwordx4 a[32:35], off, s[84:87], s63 offset:2048 sc1\n\t"
"v_mfma_f32_16x16x32_bf16 v[180:183], a[136:139], a[148:151], v[180:183]\n\t"
"ds_read_b128 a[156:159], v224 offset:18336\n\t"
"v_xor_b32 v249, 32, v248\n\t"
"v_mfma_f32_16x16x32_bf16 v[180:183], a[140:143], a[164:167], v[180:183]\n\t"
"buffer_load_dwordx4 a[24:27], off, s[84:87], s63 offset:0 sc1\n\t"
"v_mfma_f32_16x16x32_bf16 v[164:167], a[128:131], a[148:151], v[164:167]\n\t"
"v_accvgpr_mov_b32 a209, a228\n\t"
"v_mfma_f32_16x16x32_bf16 v[164:167], a[132:135], a[164:167], v[164:167]\n\t"
"buffer_load_dwordx4 a[28:31], off, s[84:87], s63 offset:1024 sc1\n\t"
"v_mfma_f32_16x16x32_bf16 v[148:151], a[120:123], a[148:151], v[148:151]\n\t"
"v_xor_b32 v250, 64, v248\n\t"
"v_xor_b32 v251, 96, v248\n\t"
"v_mfma_f32_16x16x32_bf16 v[148:151], a[124:127], a[164:167], v[148:151]\n\t"
"s_waitcnt vmcnt(13) lgkmcnt(4)\n\t"
"s_waitcnt lgkmcnt(0)\n\t"
"s_barrier\n\t"
"v_mfma_f32_16x16x32_bf16 v[152:155], a[120:123], a[152:155], v[152:155]\n\t"
"s_add_u32 m0, s26, 56192\n\t"
"ds_read_b128 a[144:147], v224 offset:0\n\t"
"ds_read_b128 a[160:163], v224 offset:2048\n\t"
"v_mfma_f32_16x16x32_bf16 v[152:155], a[124:127], a[168:171], v[152:155]\n\t"
"buffer_load_dwordx4 v236, s[12:15], s22 offen offset:128 nt lds\n\t"
"v_mfma_f32_16x16x32_bf16 v[168:171], a[128:131], a[152:155], v[168:171]\n\t"
"s_nop 0\n\t"
"ds_read_b128 a[148:151], v224 offset:256\n\t"
"ds_read_b64_tr_b16 a[176:177], v248 offset:0\n\t"
"v_mfma_f32_16x16x32_bf16 v[168:171], a[132:135], a[168:171], v[168:171]\n\t"
"ds_read_b128 a[164:167], v224 offset:2304\n\t"
"v_mfma_f32_16x16x32_bf16 v[184:187], a[136:139], a[152:155], v[184:187]\n\t"
"ds_read_b128 a[152:155], v224 offset:512\n\t"
"v_mfma_f32_16x16x32_bf16 v[184:187], a[140:143], a[168:171], v[184:187]\n\t"
"buffer_store_dwordx4 a[232:235], v233, s[16:19], s25 offen nt\n\t"
"v_mfma_f32_16x16x32_bf16 v[188:191], a[136:139], a[156:159], v[188:191]\n\t"
"s_nop 0\n\t"
"ds_read_b128 a[168:171], v224 offset:2560\n\t"
"ds_read_b64_tr_b16 a[178:179], v248 offset:512\n\t"
"v_mfma_f32_16x16x32_bf16 v[188:191], a[140:143], a[172:175], v[188:191]\n\t"
"ds_read_b64_tr_b16 a[180:181], v249 offset:0\n\t"
"v_mfma_f32_16x16x32_bf16 v[172:175], a[128:131], a[156:159], v[172:175]\n\t"
"buffer_load_dwordx4 v237, s[12:15], s22 offen offset:1152 nt lds\n\t"
"v_mfma_f32_16x16x32_bf16 v[172:175], a[132:135], a[172:175], v[172:175]\n\t"
"s_nop 0\n\t"
"ds_read_b64_tr_b16 a[182:183], v249 offset:512\n\t"
"ds_read_b64_tr_b16 a[184:185], v250 offset:0\n\t"
"v_mfma_f32_16x16x32_bf16 v[156:159], a[120:123], a[156:159], v[156:159]\n\t"
"s_nop 0\n\t"
"ds_read_b128 a[156:159], v224 offset:768\n\t"
"ds_read_b64_tr_b16 a[186:187], v250 offset:512\n\t"
"v_mfma_f32_16x16x32_bf16 v[156:159], a[124:127], a[172:175], v[156:159]\n\t"
"s_waitcnt vmcnt(9) lgkmcnt(11)\n\t"
".p2align 3\n\t"
"buffer_store_dwordx4 a[236:239], v234, s[16:19], s25 offen nt\n\t"
"v_pk_add_f32 v[144:145], v[48:49], v[144:145] neg_lo:[0,1] neg_hi:[0,1]\n\t"
"v_pk_add_f32 v[146:147], v[50:51], v[146:147] neg_lo:[0,1] neg_hi:[0,1]\n\t"
"v_pk_fma_f32 v[48:49], v[144:145], s[90:91], v[48:49] neg_lo:[1,0,0] neg_hi:[1,0,0]\n\t"
"v_pk_fma_f32 v[50:51], v[146:147], s[90:91], v[50:51] neg_lo:[1,0,0] neg_hi:[1,0,0]\n\t"
"v_pk_add_f32 v[160:161], v[64:65], v[160:161] neg_lo:[0,1] neg_hi:[0,1]\n\t"
"v_pk_add_f32 v[162:163], v[66:67], v[162:163] neg_lo:[0,1] neg_hi:[0,1]\n\t"
"v_pk_fma_f32 v[64:65], v[160:161], s[90:91], v[64:65] neg_lo:[1,0,0] neg_hi:[1,0,0]\n\t"
"v_pk_fma_f32 v[66:67], v[162:163], s[90:91], v[66:67] neg_lo:[1,0,0] neg_hi:[1,0,0]\n\t"
"v_pk_add_f32 v[176:177], v[80:81], v[176:177] neg_lo:[0,1] neg_hi:[0,1]\n\t"
"v_pk_add_f32 v[178:179], v[82:83], v[178:179] neg_lo:[0,1] neg_hi:[0,1]\n\t"
"v_pk_fma_f32 v[80:81], v[176:177], s[90:91], v[80:81] neg_lo:[1,0,0] neg_hi:[1,0,0]\n\t"
"v_pk_fma_f32 v[82:83], v[178:179], s[90:91], v[82:83] neg_lo:[1,0,0] neg_hi:[1,0,0]\n\t"
"v_pk_add_f32 v[180:181], v[84:85], v[180:181] neg_lo:[0,1] neg_hi:[0,1]\n\t"
"v_pk_add_f32 v[182:183], v[86:87], v[182:183] neg_lo:[0,1] neg_hi:[0,1]\n\t"
"v_pk_fma_f32 v[84:85], v[180:181], s[90:91], v[84:85] neg_lo:[1,0,0] neg_hi:[1,0,0]\n\t"
"v_pk_fma_f32 v[86:87], v[182:183], s[90:91], v[86:87] neg_lo:[1,0,0] neg_hi:[1,0,0]\n\t"
"v_pk_add_f32 v[164:165], v[68:69], v[164:165] neg_lo:[0,1] neg_hi:[0,1]\n\t"
"v_pk_add_f32 v[166:167], v[70:71], v[166:167] neg_lo:[0,1] neg_hi:[0,1]\n\t"
"v_pk_fma_f32 v[68:69], v[164:165], s[90:91], v[68:69] neg_lo:[1,0,0] neg_hi:[1,0,0]\n\t"
"v_pk_fma_f32 v[70:71], v[166:167], s[90:91], v[70:71] neg_lo:[1,0,0] neg_hi:[1,0,0]\n\t"
"v_pk_add_f32 v[148:149], v[52:53], v[148:149] neg_lo:[0,1] neg_hi:[0,1]\n\t"
"v_pk_add_f32 v[150:151], v[54:55], v[150:151] neg_lo:[0,1] neg_hi:[0,1]\n\t"
"v_pk_fma_f32 v[52:53], v[148:149], s[90:91], v[52:53] neg_lo:[1,0,0] neg_hi:[1,0,0]\n\t"
"v_pk_fma_f32 v[54:55], v[150:151], s[90:91], v[54:55] neg_lo:[1,0,0] neg_hi:[1,0,0]\n\t"
"v_pk_add_f32 v[152:153], v[56:57], v[152:153] neg_lo:[0,1] neg_hi:[0,1]\n\t"
"v_pk_add_f32 v[154:155], v[58:59], v[154:155] neg_lo:[0,1] neg_hi:[0,1]\n\t"
"v_pk_fma_f32 v[56:57], v[152:153], s[90:91], v[56:57] neg_lo:[1,0,0] neg_hi:[1,0,0]\n\t"
"v_pk_fma_f32 v[58:59], v[154:155], s[90:91], v[58:59] neg_lo:[1,0,0] neg_hi:[1,0,0]\n\t"
"v_pk_add_f32 v[168:169], v[72:73], v[168:169] neg_lo:[0,1] neg_hi:[0,1]\n\t"
"v_pk_add_f32 v[170:171], v[74:75], v[170:171] neg_lo:[0,1] neg_hi:[0,1]\n\t"
"v_pk_fma_f32 v[72:73], v[168:169], s[90:91], v[72:73] neg_lo:[1,0,0] neg_hi:[1,0,0]\n\t"
"v_pk_fma_f32 v[74:75], v[170:171], s[90:91], v[74:75] neg_lo:[1,0,0] neg_hi:[1,0,0]\n\t"
"v_pk_add_f32 v[184:185], v[88:89], v[184:185] neg_lo:[0,1] neg_hi:[0,1]\n\t"
"v_pk_add_f32 v[186:187], v[90:91], v[186:187] neg_lo:[0,1] neg_hi:[0,1]\n\t"
"v_pk_fma_f32 v[88:89], v[184:185], s[90:91], v[88:89] neg_lo:[1,0,0] neg_hi:[1,0,0]\n\t"
"v_pk_fma_f32 v[90:91], v[186:187], s[90:91], v[90:91] neg_lo:[1,0,0] neg_hi:[1,0,0]\n\t"
"v_pk_add_f32 v[188:189], v[92:93], v[188:189] neg_lo:[0,1] neg_hi:[0,1]\n\t"
"v_pk_add_f32 v[190:191], v[94:95], v[190:191] neg_lo:[0,1] neg_hi:[0,1]\n\t"
"v_pk_fma_f32 v[92:93], v[188:189], s[90:91], v[92:93] neg_lo:[1,0,0] neg_hi:[1,0,0]\n\t"
"v_pk_fma_f32 v[94:95], v[190:191], s[90:91], v[94:95] neg_lo:[1,0,0] neg_hi:[1,0,0]\n\t"
"v_pk_add_f32 v[172:173], v[76:77], v[172:173] neg_lo:[0,1] neg_hi:[0,1]\n\t"
"v_pk_add_f32 v[174:175], v[78:79], v[174:175] neg_lo:[0,1] neg_hi:[0,1]\n\t"
"v_pk_fma_f32 v[76:77], v[172:173], s[90:91], v[76:77] neg_lo:[1,0,0] neg_hi:[1,0,0]\n\t"
"v_pk_fma_f32 v[78:79], v[174:175], s[90:91], v[78:79] neg_lo:[1,0,0] neg_hi:[1,0,0]\n\t"
"v_pk_add_f32 v[156:157], v[60:61], v[156:157] neg_lo:[0,1] neg_hi:[0,1]\n\t"
"v_pk_add_f32 v[158:159], v[62:63], v[158:159] neg_lo:[0,1] neg_hi:[0,1]\n\t"
"v_pk_fma_f32 v[60:61], v[156:157], s[90:91], v[60:61] neg_lo:[1,0,0] neg_hi:[1,0,0]\n\t"
"v_pk_fma_f32 v[62:63], v[158:159], s[90:91], v[62:63] neg_lo:[1,0,0] neg_hi:[1,0,0]\n\t"
"v_mfma_f32_16x16x32_bf16 v[48:51], a[0:3], a[144:147], v[48:51]\n\t"
"s_nop 0\n\t"
"ds_read_b128 a[172:175], v224 offset:2816\n\t"
"ds_read_b64_tr_b16 a[188:189], v251 offset:0\n\t"
"v_mfma_f32_16x16x32_bf16 v[48:51], a[4:7], a[160:163], v[48:51]\n\t"
"s_nop 0\n\t"
"ds_read_b64_tr_b16 a[190:191], v251 offset:512\n\t"
"ds_read_b64_tr_b16 a[192:193], v248 offset:1152\n\t"
"v_mfma_f32_16x16x32_bf16 v[64:67], a[8:11], a[144:147], v[64:67]\n\t"
"ds_read_b64_tr_b16 a[194:195], v248 offset:1664\n\t"
"v_mfma_f32_16x16x32_bf16 v[64:67], a[12:15], a[160:163], v[64:67]\n\t"
"buffer_load_dwordx4 v238, s[12:15], s22 offen offset:2176 nt lds\n\t"
"v_mfma_f32_16x16x32_bf16 v[80:83], a[16:19], a[144:147], v[80:83]\n\t"
"s_nop 0\n\t"
"ds_read_b64_tr_b16 a[196:197], v249 offset:1152\n\t"
"ds_read_b64_tr_b16 a[198:199], v249 offset:1664\n\t"
"v_mfma_f32_16x16x32_bf16 v[80:83], a[20:23], a[160:163], v[80:83]\n\t"
"s_waitcnt lgkmcnt(11)\n\t"
"ds_read_b64_tr_b16 a[200:201], v250 offset:1152\n\t"
"ds_read_b64_tr_b16 a[202:203], v250 offset:1664\n\t"
"v_mfma_f32_16x16x32_bf16 v[192:195], a[216:219], a[176:179], 0\n\t"
"s_waitcnt lgkmcnt(12)\n\t"
"ds_read_b64_tr_b16 a[204:205], v251 offset:1152\n\t"
"v_mfma_f32_16x16x32_bf16 v[196:199], a[216:219], a[180:183], 0\n\t"
"s_waitcnt lgkmcnt(10)\n\t"
"buffer_load_dwordx4 v239, s[12:15], s22 offen offset:3200 nt lds\n\t"
"v_mfma_f32_16x16x32_bf16 v[200:203], a[216:219], a[184:187], 0\n\t"
"s_waitcnt lgkmcnt(7)\n\t"
"ds_read_b128 a[160:163], v224 offset:3072\n\t"
"ds_read_b64_tr_b16 a[206:207], v251 offset:1664\n\t"
"v_mfma_f32_16x16x32_bf16 v[204:207], a[216:219], a[188:191], 0\n\t"
"s_waitcnt lgkmcnt(7)\n\t"
"ds_read_b64_tr_b16 a[176:177], v248 offset:42624\n\t"
"v_mfma_f32_16x16x32_bf16 v[208:211], a[216:219], a[192:195], 0\n\t"
"s_waitcnt lgkmcnt(6)\n\t"
"ds_read_b64_tr_b16 a[178:179], v248 offset:43136\n\t"
"v_mfma_f32_16x16x32_bf16 v[212:215], a[216:219], a[196:199], 0\n\t"
"s_waitcnt lgkmcnt(5)\n\t"
"ds_read_b64_tr_b16 a[180:181], v249 offset:42624\n\t"
"v_mfma_f32_16x16x32_bf16 v[216:219], a[216:219], a[200:203], 0\n\t"
"s_waitcnt lgkmcnt(3)\n\t"
"ds_read_b64_tr_b16 a[182:183], v249 offset:43136\n\t"
"ds_read_b64_tr_b16 a[184:185], v250 offset:42624\n\t"
"v_mfma_f32_16x16x32_bf16 v[220:223], a[216:219], a[204:207], 0\n\t"
"s_mov_b64 exec, s[36:37]\n\t"
"buffer_load_dwordx4 v240, s[12:15], s22 offen offset:3712 nt lds\n\t"
"s_mov_b64 exec, -1\n\t"
"v_mfma_f32_16x16x32_bf16 v[84:87], a[16:19], a[148:151], v[84:87]\n\t"
"s_lshl_b32 m0, s73, 8\n\t"
"ds_read_b128 a[144:147], v224 offset:1024\n\t"
"v_cvt_pk_bf16_f32 v195, v195, v199\n\t"
"v_mfma_f32_16x16x32_bf16 v[84:87], a[20:23], a[164:167], v[84:87]\n\t"
"ds_read_b64_tr_b16 a[186:187], v250 offset:43136\n\t"
"v_cvt_pk_bf16_f32 v202, v202, v206\n\t"
"v_mfma_f32_16x16x32_bf16 v[68:71], a[8:11], a[148:151], v[68:71]\n\t"
"ds_read_b64_tr_b16 a[188:189], v251 offset:42624\n\t"
"v_cvt_pk_bf16_f32 v194, v194, v198\n\t"
"v_mfma_f32_16x16x32_bf16 v[68:71], a[12:15], a[164:167], v[68:71]\n\t"
"ds_read_b64_tr_b16 a[190:191], v251 offset:43136\n\t"
"v_cvt_pk_bf16_f32 v209, -v209, -v213\n\t"
"v_mfma_f32_16x16x32_bf16 v[52:55], a[0:3], a[148:151], v[52:55]\n\t"
"ds_read_b128 a[148:151], v224 offset:1280\n\t"
"v_cvt_pk_bf16_f32 v193, v193, v197\n\t"
"v_mfma_f32_16x16x32_bf16 v[52:55], a[4:7], a[164:167], v[52:55]\n\t"
"ds_read_b64_tr_b16 a[192:193], v248 offset:21888\n\t"
"v_cvt_pk_bf16_f32 v192, v192, v196\n\t"
"v_mfma_f32_16x16x32_bf16 v[56:59], a[0:3], a[152:155], v[56:59]\n\t"
"ds_read_b64_tr_b16 a[194:195], v248 offset:22400\n\t"
"v_cvt_pk_bf16_f32 v219, -v219, -v223\n\t"
"v_mfma_f32_16x16x32_bf16 v[56:59], a[4:7], a[168:171], v[56:59]\n\t"
"ds_read_b128 a[164:167], v224 offset:3328\n\t"
"v_cvt_pk_bf16_f32 v211, -v211, -v215\n\t"
"v_mfma_f32_16x16x32_bf16 v[72:75], a[8:11], a[152:155], v[72:75]\n\t"
"ds_write_addtid_b32 v193 offset:24864\n\t"
"v_cvt_pk_bf16_f32 v218, -v218, -v222\n\t"
"v_mfma_f32_16x16x32_bf16 v[72:75], a[12:15], a[168:171], v[72:75]\n\t"
"ds_write_addtid_b32 v218 offset:23744\n\t"
"v_cvt_pk_bf16_f32 v200, v200, v204\n\t"
"v_mfma_f32_16x16x32_bf16 v[88:91], a[16:19], a[152:155], v[88:91]\n\t"
"ds_read_b128 a[152:155], v224 offset:1536\n\t"
"v_cvt_pk_bf16_f32 v216, -v216, -v220\n\t"
"v_mfma_f32_16x16x32_bf16 v[88:91], a[20:23], a[168:171], v[88:91]\n\t"
"ds_read_b128 a[168:171], v224 offset:3584\n\t"
"v_cvt_pk_bf16_f32 v210, -v210, -v214\n\t"
"v_mfma_f32_16x16x32_bf16 v[92:95], a[16:19], a[156:159], v[92:95]\n\t"
"ds_write_addtid_b32 v209 offset:25888\n\t"
"v_cvt_pk_bf16_f32 v208, -v208, -v212\n\t"
"v_mfma_f32_16x16x32_bf16 v[92:95], a[20:23], a[172:175], v[92:95]\n\t"
"ds_write_addtid_b32 v216 offset:19616\n\t"
"v_cvt_pk_bf16_f32 v217, -v217, -v221\n\t"
"v_mfma_f32_16x16x32_bf16 v[76:79], a[8:11], a[156:159], v[76:79]\n\t"
"s_nop 0\n\t"
"ds_write_addtid_b32 v195 offset:28992\n\t"
"ds_write_addtid_b32 v200 offset:18592\n\t"
"v_mfma_f32_16x16x32_bf16 v[76:79], a[12:15], a[172:175], v[76:79]\n\t"
"s_nop 0\n\t"
"ds_write_addtid_b32 v211 offset:30016\n\t"
"ds_write_addtid_b32 v219 offset:32064\n\t"
"v_mfma_f32_16x16x32_bf16 v[60:63], a[0:3], a[156:159], v[60:63]\n\t"
"ds_write_addtid_b32 v208 offset:17568\n\t"
"v_cvt_pk_bf16_f32 v201, v201, v205\n\t"
"v_mfma_f32_16x16x32_bf16 v[60:63], a[4:7], a[172:175], v[60:63]\n\t"
"s_waitcnt vmcnt(7) lgkmcnt(11)\n\t"
"ds_write_addtid_b32 v194 offset:20672\n\t"
"ds_write_addtid_b32 v210 offset:21696\n\t"
"v_mfma_f32_16x16x32_bf16 v[144:147], a[24:27], a[144:147], v[144:147]\n\t"
"s_nop 0\n\t"
"ds_write_addtid_b32 v192 offset:16544\n\t"
"ds_write_addtid_b32 v217 offset:27936\n\t"
"v_mfma_f32_16x16x32_bf16 v[144:147], a[28:31], a[160:163], v[144:147]\n\t"
"ds_write_addtid_b32 v201 offset:26912\n\t"
"v_cvt_pk_bf16_f32 v203, v203, v207\n\t"
"v_mfma_f32_16x16x32_bf16 v[160:163], a[32:35], a[144:147], v[160:163]\n\t"
"s_nop 0\n\t"
"ds_write_addtid_b32 v202 offset:22720\n\t"
"ds_write_addtid_b32 v203 offset:31040\n\t"
"v_mfma_f32_16x16x32_bf16 v[160:163], a[36:39], a[160:163], v[160:163]\n\t"
"ds_read_b128 a[172:175], v224 offset:3840\n\t"
"v_mfma_f32_16x16x32_bf16 v[176:179], a[40:43], a[144:147], v[176:179]\n\t"
"ds_read_b128 a[156:159], v224 offset:1792\n\t"
"v_mfma_f32_16x16x32_bf16 v[176:179], a[44:47], a[160:163], v[176:179]\n\t"
"ds_read_b64_tr_b16 a[196:197], v249 offset:21888\n\t"
"v_mfma_f32_16x16x32_bf16 v[180:183], a[40:43], a[148:151], v[180:183]\n\t"
"ds_read_b64_tr_b16 a[198:199], v249 offset:22400\n\t"
"v_mfma_f32_16x16x32_bf16 v[180:183], a[44:47], a[164:167], v[180:183]\n\t"
"s_nop 0\n\t"
"ds_read_b64_tr_b16 a[200:201], v250 offset:21888\n\t"
"ds_read_b64_tr_b16 a[202:203], v250 offset:22400\n\t"
"v_mfma_f32_16x16x32_bf16 v[164:167], a[32:35], a[148:151], v[164:167]\n\t"
"s_nop 0\n\t"
"ds_read_b64_tr_b16 a[204:205], v251 offset:21888\n\t"
"ds_read_b64_tr_b16 a[206:207], v251 offset:22400\n\t"
"v_mfma_f32_16x16x32_bf16 v[164:167], a[36:39], a[164:167], v[164:167]\n\t"
"v_mfma_f32_16x16x32_bf16 v[148:151], a[24:27], a[148:151], v[148:151]\n\t"
"s_waitcnt lgkmcnt(6)\n\t"
"v_mfma_f32_16x16x32_bf16 v[148:151], a[28:31], a[164:167], v[148:151]\n\t"
"s_barrier\n\t"
"v_mfma_f32_16x16x32_bf16 v[152:155], a[24:27], a[152:155], v[152:155]\n\t"
"ds_read_b128 a[144:147], v224 offset:16544\n\t"
"v_add_u32 v248, 33280, v226\n\t"
"v_mfma_f32_16x16x32_bf16 v[152:155], a[28:31], a[168:171], v[152:155]\n\t"
"ds_read_b128 a[148:151], v224 offset:16800\n\t"
"v_mfma_f32_16x16x32_bf16 v[168:171], a[32:35], a[152:155], v[168:171]\n\t"
"ds_read_b128 a[160:163], v224 offset:18592\n\t"
"v_mfma_f32_16x16x32_bf16 v[168:171], a[36:39], a[168:171], v[168:171]\n\t"
"ds_read_b128 a[164:167], v224 offset:18848\n\t"
"v_mfma_f32_16x16x32_bf16 v[184:187], a[40:43], a[152:155], v[184:187]\n\t"
"v_mfma_f32_16x16x32_bf16 v[184:187], a[44:47], a[168:171], v[184:187]\n\t"
"v_xor_b32 v249, 32, v248\n\t"
"v_mfma_f32_16x16x32_bf16 v[188:191], a[40:43], a[156:159], v[188:191]\n\t"
"ds_read_b128 a[152:155], v224 offset:17056\n\t"
"v_mfma_f32_16x16x32_bf16 v[188:191], a[44:47], a[172:175], v[188:191]\n\t"
"ds_read_b128 a[168:171], v224 offset:19104\n\t"
"v_mfma_f32_16x16x32_bf16 v[172:175], a[32:35], a[156:159], v[172:175]\n\t"
"v_mfma_f32_16x16x32_bf16 v[172:175], a[36:39], a[172:175], v[172:175]\n\t"
"v_mfma_f32_16x16x32_bf16 v[156:159], a[24:27], a[156:159], v[156:159]\n\t"
"ds_read_b128 a[156:159], v224 offset:17312\n\t"
"v_xor_b32 v250, 64, v248\n\t"
"v_mfma_f32_16x16x32_bf16 v[156:159], a[28:31], a[172:175], v[156:159]\n\t"
"ds_read_b128 a[172:175], v224 offset:19360\n\t"
"v_xor_b32 v251, 96, v248\n\t"
"v_mfma_f32_16x16x32_bf16 v[192:195], a[216:219], a[176:179], 0\n\t"
"ds_read_b64_tr_b16 a[178:179], v248 offset:128\n\t"
"v_mfma_f32_16x16x32_bf16 v[196:199], a[216:219], a[180:183], 0\n\t"
"v_mfma_f32_16x16x32_bf16 v[200:203], a[216:219], a[184:187], 0\n\t"
"ds_read_b64_tr_b16 a[176:177], v248 offset:0\n\t"
"v_mfma_f32_16x16x32_bf16 v[204:207], a[216:219], a[188:191], 0\n\t"
"ds_read_b64_tr_b16 a[180:181], v249 offset:256\n\t"
"v_mfma_f32_16x16x32_bf16 v[208:211], a[216:219], a[192:195], 0\n\t"
"s_waitcnt lgkmcnt(6)\n\t"
"ds_read_b64_tr_b16 a[186:187], v249 offset:128\n\t"
"v_mfma_f32_16x16x32_bf16 v[212:215], a[216:219], a[196:199], 0\n\t"
"v_mfma_f32_16x16x32_bf16 v[216:219], a[216:219], a[200:203], 0\n\t"
"ds_read_b64_tr_b16 a[184:185], v249 offset:0\n\t"
"v_mfma_f32_16x16x32_bf16 v[220:223], a[216:219], a[204:207], 0\n\t"
"ds_read_b64_tr_b16 a[188:189], v248 offset:256\n\t"
"v_mfma_f32_16x16x32_bf16 v[0:3], a[48:51], a[144:147], v[0:3]\n\t"
"ds_read_b64_tr_b16 a[194:195], v250 offset:128\n\t"
"v_cvt_pk_bf16_f32 v193, -v193, -v197\n\t"
"v_mfma_f32_16x16x32_bf16 v[0:3], a[52:55], a[160:163], v[0:3]\n\t"
"v_cvt_pk_bf16_f32 v203, -v203, -v207\n\t"
"v_mfma_f32_16x16x32_bf16 v[16:19], a[56:59], a[144:147], v[16:19]\n\t"
"ds_read_b64_tr_b16 a[192:193], v250 offset:0\n\t"
"v_cvt_pk_bf16_f32 v200, -v200, -v204\n\t"
"v_mfma_f32_16x16x32_bf16 v[16:19], a[60:63], a[160:163], v[16:19]\n\t"
"ds_read_b64_tr_b16 a[196:197], v251 offset:256\n\t"
"v_cvt_pk_bf16_f32 v194, -v194, -v198\n\t"
"v_mfma_f32_16x16x32_bf16 v[32:35], a[64:67], a[144:147], v[32:35]\n\t"
"ds_read_b128 a[144:147], v224 offset:17568\n\t"
"v_cvt_pk_bf16_f32 v195, -v195, -v199\n\t"
"v_mfma_f32_16x16x32_bf16 v[32:35], a[68:71], a[160:163], v[32:35]\n\t"
"v_cvt_pk_bf16_f32 v211, -v211, -v215\n\t"
"v_cvt_pk_bf16_f32 v216, -v216, -v220\n\t"
"v_mfma_f32_16x16x32_bf16 v[36:39], a[64:67], a[148:151], v[36:39]\n\t"
"ds_read_b128 a[160:163], v224 offset:19616\n\t"
"v_cvt_pk_bf16_f32 v217, -v217, -v221\n\t"
"v_mfma_f32_16x16x32_bf16 v[36:39], a[68:71], a[164:167], v[36:39]\n\t"
"ds_write_addtid_b32 v211 offset:13472\n\t"
"v_cvt_pk_bf16_f32 v219, -v219, -v223\n\t"
"v_mfma_f32_16x16x32_bf16 v[20:23], a[56:59], a[148:151], v[20:23]\n\t"
"ds_write_addtid_b32 v200 offset:2048\n\t"
"v_cvt_pk_bf16_f32 v202, -v202, -v206\n\t"
"v_mfma_f32_16x16x32_bf16 v[20:23], a[60:63], a[164:167], v[20:23]\n\t"
"ds_write_addtid_b32 v219 offset:15520\n\t"
"v_mfma_f32_16x16x32_bf16 v[4:7], a[48:51], a[148:151], v[4:7]\n\t"
"ds_read_b128 a[148:151], v224 offset:17824\n\t"
"v_cvt_pk_bf16_f32 v218, -v218, -v222\n\t"
"v_mfma_f32_16x16x32_bf16 v[4:7], a[52:55], a[164:167], v[4:7]\n\t"
"s_waitcnt lgkmcnt(8)\n\t"
"ds_write_addtid_b32 v218 offset:7200\n\t"
"v_cvt_pk_bf16_f32 v208, -v208, -v212\n\t"
"v_mfma_f32_16x16x32_bf16 v[8:11], a[48:51], a[152:155], v[8:11]\n\t"
"ds_read_b128 a[164:167], v224 offset:19872\n\t"
"v_cvt_pk_bf16_f32 v192, -v192, -v196\n\t"
"v_mfma_f32_16x16x32_bf16 v[8:11], a[52:55], a[168:171], v[8:11]\n\t"
"ds_write_addtid_b32 v195 offset:12448\n\t"
"v_mfma_f32_16x16x32_bf16 v[24:27], a[56:59], a[152:155], v[24:27]\n\t"
"ds_write_addtid_b32 v203 offset:14496\n\t"
"v_mfma_f32_16x16x32_bf16 v[24:27], a[60:63], a[168:171], v[24:27]\n\t"
"ds_write_addtid_b32 v202 offset:6176\n\t"
"v_cvt_pk_bf16_f32 v209, -v209, -v213\n\t"
"v_mfma_f32_16x16x32_bf16 v[40:43], a[64:67], a[152:155], v[40:43]\n\t"
"ds_read_b128 a[152:155], v224 offset:18080\n\t"
"v_cvt_pk_bf16_f32 v210, -v210, -v214\n\t"
"v_mfma_f32_16x16x32_bf16 v[40:43], a[68:71], a[168:171], v[40:43]\n\t"
"ds_write_addtid_b32 v217 offset:11392\n\t"
"v_mfma_f32_16x16x32_bf16 v[44:47], a[64:67], a[156:159], v[44:47]\n\t"
"ds_read_b128 a[168:171], v224 offset:20128\n\t"
"v_cvt_pk_bf16_f32 v201, -v201, -v205\n\t"
"v_mfma_f32_16x16x32_bf16 v[44:47], a[68:71], a[172:175], v[44:47]\n\t"
"buffer_load_dwordx4 a[64:67], off, s[28:31], s42 offset:0 sc1\n\t"
"v_mfma_f32_16x16x32_bf16 v[28:31], a[56:59], a[156:159], v[28:31]\n\t"
"s_xor_b32 s24, s24, 384\n\t"
"ds_write_addtid_b32 v193 offset:8320\n\t"
"ds_write_addtid_b32 v210 offset:5152\n\t"
"v_mfma_f32_16x16x32_bf16 v[28:31], a[60:63], a[172:175], v[28:31]\n\t"
"buffer_load_dwordx4 a[68:71], off, s[28:31], s42 offset:1024 sc1\n\t"
"v_mfma_f32_16x16x32_bf16 v[12:15], a[48:51], a[156:159], v[12:15]\n\t"
"s_xor_b32 s20, s20, 384\n\t"
"ds_read_b128 a[156:159], v224 offset:18336\n\t"
"ds_write_addtid_b32 v192 offset:0\n\t"
"v_mfma_f32_16x16x32_bf16 v[12:15], a[52:55], a[172:175], v[12:15]\n\t"
"s_waitcnt lgkmcnt(9)\n\t"
"buffer_load_dwordx4 a[60:63], off, s[84:87], s42 offset:3072 sc1\n\t"
"v_mfma_f32_16x16x32_bf16 v[96:99], a[72:75], a[144:147], v[0:3]\n\t"
"s_xor_b32 s27, s27, 384\n\t"
"ds_write_addtid_b32 v208 offset:1024\n\t"
"ds_write_addtid_b32 v209 offset:9344\n\t"
"v_mfma_f32_16x16x32_bf16 v[96:99], a[76:79], a[160:163], v[96:99]\n\t"
"ds_write_addtid_b32 v201 offset:10368\n\t"
"v_mfma_f32_16x16x32_bf16 v[112:115], a[80:83], a[144:147], v[16:19]\n\t"
"s_xor_b32 s25, s25, 384\n\t"
"ds_write_addtid_b32 v194 offset:4128\n\t"
"ds_write_addtid_b32 v216 offset:3072\n\t"
"v_mfma_f32_16x16x32_bf16 v[112:115], a[84:87], a[160:163], v[112:115]\n\t"
"buffer_load_dwordx4 a[56:59], off, s[84:87], s42 offset:2048 sc1\n\t"
"v_mfma_f32_16x16x32_bf16 v[128:131], a[88:91], a[144:147], v[32:35]\n\t"
"ds_read_b128 a[172:175], v224 offset:20384\n\t"
"v_mfma_f32_16x16x32_bf16 v[128:131], a[92:95], a[160:163], v[128:131]\n\t"
"buffer_load_dwordx4 a[48:51], off, s[84:87], s42 offset:0 sc1\n\t"
"v_mfma_f32_16x16x32_bf16 v[132:135], a[88:91], a[148:151], v[36:39]\n\t"
"ds_read_b64_tr_b16 a[202:203], v251 offset:128\n\t"
"v_mfma_f32_16x16x32_bf16 v[132:135], a[92:95], a[164:167], v[132:135]\n\t"
"ds_read_b64_tr_b16 a[200:201], v251 offset:0\n\t"
"v_mfma_f32_16x16x32_bf16 v[116:119], a[80:83], a[148:151], v[20:23]\n\t"
"ds_read_b64_tr_b16 a[204:205], v250 offset:256\n\t"
"v_mfma_f32_16x16x32_bf16 v[116:119], a[84:87], a[164:167], v[116:119]\n\t"
"buffer_load_dwordx4 a[52:55], off, s[84:87], s42 offset:1024 sc1\n\t"
"v_mfma_f32_16x16x32_bf16 v[100:103], a[72:75], a[148:151], v[4:7]\n\t"
"v_mfma_f32_16x16x32_bf16 v[100:103], a[76:79], a[164:167], v[100:103]\n\t"
"s_waitcnt lgkmcnt(0)\n\t"
"s_barrier\n\t"
"v_mfma_f32_16x16x32_bf16 v[104:107], a[72:75], a[152:155], v[8:11]\n\t"
"ds_read_b128 a[148:151], v224 offset:256\n\t"
"v_mfma_f32_16x16x32_bf16 v[104:107], a[76:79], a[168:171], v[104:107]\n\t"
"ds_read_b128 a[144:147], v224 offset:0\n\t"
"v_mfma_f32_16x16x32_bf16 v[120:123], a[80:83], a[152:155], v[24:27]\n\t"
"ds_read_b128 a[160:163], v224 offset:2048\n\t"
"v_mfma_f32_16x16x32_bf16 v[120:123], a[84:87], a[168:171], v[120:123]\n\t"
"ds_read_b128 a[164:167], v224 offset:2304\n\t"
"v_mfma_f32_16x16x32_bf16 v[136:139], a[88:91], a[152:155], v[40:43]\n\t"
"v_mfma_f32_16x16x32_bf16 v[136:139], a[92:95], a[168:171], v[136:139]\n\t"
"ds_write_b128 v254, a[240:243] offset:0\n\t"
"v_mfma_f32_16x16x32_bf16 v[140:143], a[88:91], a[156:159], v[44:47]\n\t"
"v_mfma_f32_16x16x32_bf16 v[140:143], a[92:95], a[172:175], v[140:143]\n\t"
"buffer_load_dwordx4 a[88:91], off, s[28:31], s48 offset:0 sc1\n\t"
"v_mfma_f32_16x16x32_bf16 v[124:127], a[80:83], a[156:159], v[28:31]\n\t"
"v_mfma_f32_16x16x32_bf16 v[124:127], a[84:87], a[172:175], v[124:127]\n\t"
"buffer_load_dwordx4 a[92:95], off, s[28:31], s48 offset:1024 sc1\n\t"
"v_mfma_f32_16x16x32_bf16 v[108:111], a[72:75], a[156:159], v[12:15]\n\t"
"ds_read_b128 a[168:171], v224 offset:2560\n\t"
"v_mfma_f32_16x16x32_bf16 v[108:111], a[76:79], a[172:175], v[108:111]\n\t"
"buffer_load_dwordx4 a[84:87], off, s[84:87], s48 offset:3072 sc1\n\t"
"v_mfma_f32_4x4x4_16b_bf16 v[200:203], a[220:221], a[178:179], 0\n\t"
"ds_read_b128 a[152:155], v224 offset:512\n\t"
"v_mfma_f32_4x4x4_16b_bf16 v[204:207], a[220:221], a[186:187], 0\n\t"
"ds_read_b64_tr_b16 a[178:179], v248 offset:41600\n\t"
"v_mfma_f32_4x4x4_16b_bf16 v[216:219], a[220:221], a[194:195], 0\n\t"
"ds_read_b128 a[172:175], v224 offset:2816\n\t"
"v_mfma_f32_4x4x4_16b_bf16 v[220:223], a[220:221], a[202:203], 0\n\t"
"v_mfma_f32_4x4x4_16b_bf16 v[192:195], a[222:223], a[176:177], v[200:203]\n\t"
"ds_read_b128 a[156:159], v224 offset:768\n\t"
"v_mfma_f32_4x4x4_16b_bf16 v[196:199], a[222:223], a[184:185], v[204:207]\n\t"
"ds_read_b64_tr_b16 a[186:187], v249 offset:41600\n\t"
"v_mfma_f32_4x4x4_16b_bf16 v[200:203], a[222:223], a[180:181], v[200:203]\n\t"
"ds_read_b64_tr_b16 a[194:195], v250 offset:41600\n\t"
"v_mfma_f32_4x4x4_16b_bf16 v[204:207], a[222:223], a[188:189], v[204:207]\n\t"
"ds_read_b64_tr_b16 a[202:203], v251 offset:41600\n\t"
"v_mfma_f32_4x4x4_16b_bf16 v[208:211], a[222:223], a[192:193], v[216:219]\n\t"
"ds_read_b64_tr_b16 a[176:177], v248 offset:41472\n\t"
"v_mfma_f32_4x4x4_16b_bf16 v[212:215], a[222:223], a[200:201], v[220:223]\n\t"
"ds_read_b64_tr_b16 a[184:185], v249 offset:41472\n\t"
"v_mfma_f32_4x4x4_16b_bf16 v[216:219], a[222:223], a[196:197], v[216:219]\n\t"
"ds_read_b64_tr_b16 a[180:181], v249 offset:41728\n\t"
"v_mfma_f32_4x4x4_16b_bf16 v[220:223], a[222:223], a[204:205], v[220:223]\n\t"
"s_waitcnt lgkmcnt(8)\n\t"
"buffer_load_dwordx4 a[80:83], off, s[84:87], s48 offset:2048 sc1\n\t"
"v_mfma_f32_16x16x32_bf16 v[96:99], a[96:99], a[144:147], v[96:99]\n\t"
"ds_read_b64_tr_b16 a[188:189], v248 offset:41728\n\t"
"v_cvt_pk_bf16_f32 v192, -v192, -v196\n\t"
"v_mfma_f32_16x16x32_bf16 v[96:99], a[100:103], a[160:163], v[96:99]\n\t"
"ds_read_b64_tr_b16 a[192:193], v250 offset:41472\n\t"
"v_cvt_pk_bf16_f32 v195, -v195, -v199\n\t"
"v_mfma_f32_16x16x32_bf16 v[112:115], a[104:107], a[144:147], v[112:115]\n\t"
"ds_read_b64_tr_b16 a[200:201], v251 offset:41472\n\t"
"v_cvt_pk_bf16_f32 v202, v202, v206\n\t"
"v_mfma_f32_16x16x32_bf16 v[112:115], a[108:111], a[160:163], v[112:115]\n\t"
"buffer_load_dwordx4 a[72:75], off, s[84:87], s48 offset:0 sc1\n\t"
"v_mfma_f32_16x16x32_bf16 v[128:131], a[112:115], a[144:147], v[128:131]\n\t"
"ds_read_b128 a[144:147], v224 offset:1024\n\t"
"v_cvt_pk_bf16_f32 v210, -v210, -v214\n\t"
"v_mfma_f32_16x16x32_bf16 v[128:131], a[116:119], a[160:163], v[128:131]\n\t"
"ds_read_b64_tr_b16 a[196:197], v251 offset:41728\n\t"
"v_cvt_pk_bf16_f32 v193, -v193, -v197\n\t"
"v_mfma_f32_16x16x32_bf16 v[132:135], a[112:115], a[148:151], v[132:135]\n\t"
"ds_read_b128 a[160:163], v224 offset:3072\n\t"
"v_mfma_f32_16x16x32_bf16 v[132:135], a[116:119], a[164:167], v[132:135]\n\t"
"buffer_load_dwordx4 a[76:79], off, s[84:87], s48 offset:1024 sc1\n\t"
"v_mfma_f32_16x16x32_bf16 v[116:119], a[104:107], a[148:151], v[116:119]\n\t"
"ds_write_addtid_b32 v210 offset:19616\n\t"
"v_mfma_f32_16x16x32_bf16 v[116:119], a[108:111], a[164:167], v[116:119]\n\t"
"ds_write_addtid_b32 v195 offset:21696\n\t"
"v_cvt_pk_bf16_f32 v216, v216, v220\n\t"
"v_mfma_f32_16x16x32_bf16 v[100:103], a[96:99], a[148:151], v[100:103]\n\t"
"ds_read_b128 a[148:151], v224 offset:1280\n\t"
"v_cvt_pk_bf16_f32 v194, -v194, -v198\n\t"
"v_mfma_f32_16x16x32_bf16 v[100:103], a[100:103], a[164:167], v[100:103]\n\t"
"ds_write_addtid_b32 v193 offset:20672\n\t"
"v_cvt_pk_bf16_f32 v217, v217, v221\n\t"
"v_mfma_f32_16x16x32_bf16 v[104:107], a[96:99], a[152:155], v[104:107]\n\t"
"ds_read_b128 a[164:167], v224 offset:3328\n\t"
"v_cvt_pk_bf16_f32 v218, v218, v222\n\t"
"v_mfma_f32_16x16x32_bf16 v[104:107], a[100:103], a[168:171], v[104:107]\n\t"
"ds_write_addtid_b32 v192 offset:16544\n\t"
"v_cvt_pk_bf16_f32 v201, v201, v205\n\t"
"v_mfma_f32_16x16x32_bf16 v[120:123], a[104:107], a[152:155], v[120:123]\n\t"
"ds_write_addtid_b32 v217 offset:31040\n\t"
"v_cvt_pk_bf16_f32 v211, -v211, -v215\n\t"
"v_mfma_f32_16x16x32_bf16 v[120:123], a[108:111], a[168:171], v[120:123]\n\t"
"ds_write_addtid_b32 v216 offset:26912\n\t"
"v_cvt_pk_bf16_f32 v208, -v208, -v212\n\t"
"v_mfma_f32_16x16x32_bf16 v[136:139], a[112:115], a[152:155], v[136:139]\n\t"
"ds_read_b128 a[152:155], v224 offset:1536\n\t"
"v_mfma_f32_16x16x32_bf16 v[136:139], a[116:119], a[168:171], v[136:139]\n\t"
"s_waitcnt lgkmcnt(9)\n\t"
"ds_read_b128 a[168:171], v224 offset:3584\n\t"
"v_mfma_f32_16x16x32_bf16 v[140:143], a[112:115], a[156:159], v[140:143]\n\t"
"ds_write_addtid_b32 v194 offset:17568\n\t"
"v_cvt_pk_bf16_f32 v203, v203, v207\n\t"
"v_mfma_f32_16x16x32_bf16 v[140:143], a[116:119], a[172:175], v[140:143]\n\t"
"buffer_load_dwordx4 a[112:115], off, s[28:31], s75 offset:0 sc1\n\t"
"v_mfma_f32_16x16x32_bf16 v[124:127], a[104:107], a[156:159], v[124:127]\n\t"
"ds_write_addtid_b32 v218 offset:27936\n\t"
"v_cvt_pk_bf16_f32 v219, v219, v223\n\t"
"v_mfma_f32_16x16x32_bf16 v[124:127], a[108:111], a[172:175], v[124:127]\n\t"
"s_nop 0\n\t"
"ds_write_addtid_b32 v201 offset:28992\n\t"
"ds_write_addtid_b32 v202 offset:25888\n\t"
"v_mfma_f32_16x16x32_bf16 v[108:111], a[96:99], a[156:159], v[108:111]\n\t"
"ds_write_addtid_b32 v208 offset:18592\n\t"
"v_cvt_pk_bf16_f32 v209, -v209, -v213\n\t"
"v_mfma_f32_16x16x32_bf16 v[108:111], a[100:103], a[172:175], v[108:111]\n\t"
"buffer_load_dwordx4 a[116:119], off, s[28:31], s75 offset:1024 sc1\n\t"
"v_mfma_f32_16x16x32_bf16 v[96:99], a[120:123], a[144:147], v[96:99]\n\t"
"s_nop 0\n\t"
"ds_write_addtid_b32 v203 offset:30016\n\t"
"ds_write_addtid_b32 v219 offset:32064\n\t"
"v_mfma_f32_16x16x32_bf16 v[96:99], a[124:127], a[160:163], v[96:99]\n\t"
"ds_write_addtid_b32 v209 offset:22720\n\t"
"v_cvt_pk_bf16_f32 v200, v200, v204\n\t"
"v_mfma_f32_16x16x32_bf16 v[112:115], a[128:131], a[144:147], v[112:115]\n\t"
"s_nop 0\n\t"
"ds_write_addtid_b32 v200 offset:24864\n\t"
"ds_write_addtid_b32 v211 offset:23744\n\t"
"v_mfma_f32_16x16x32_bf16 v[112:115], a[132:135], a[160:163], v[112:115]\n\t"
"buffer_load_dwordx4 a[108:111], off, s[84:87], s75 offset:3072 sc1\n\t"
"v_mfma_f32_16x16x32_bf16 v[128:131], a[136:139], a[144:147], v[128:131]\n\t"
"ds_read_b128 a[172:175], v224 offset:3840\n\t"
"v_mfma_f32_16x16x32_bf16 v[128:131], a[140:143], a[160:163], v[128:131]\n\t"
"s_waitcnt lgkmcnt(10)\n\t"
"buffer_load_dwordx4 a[104:107], off, s[84:87], s75 offset:2048 sc1\n\t"
"v_mfma_f32_16x16x32_bf16 v[132:135], a[136:139], a[148:151], v[132:135]\n\t"
"ds_read_b128 a[156:159], v224 offset:1792\n\t"
"v_mfma_f32_16x16x32_bf16 v[132:135], a[140:143], a[164:167], v[132:135]\n\t"
"buffer_load_dwordx4 a[96:99], off, s[84:87], s75 offset:0 sc1\n\t"
"v_mfma_f32_16x16x32_bf16 v[116:119], a[128:131], a[148:151], v[116:119]\n\t"
"ds_read_b64_tr_b16 a[204:205], v250 offset:41728\n\t"
"v_mfma_f32_16x16x32_bf16 v[116:119], a[132:135], a[164:167], v[116:119]\n\t"
"buffer_load_dwordx4 a[100:103], off, s[84:87], s75 offset:1024 sc1\n\t"
"v_mfma_f32_16x16x32_bf16 v[100:103], a[120:123], a[148:151], v[100:103]\n\t"
"v_mfma_f32_16x16x32_bf16 v[100:103], a[124:127], a[164:167], v[100:103]\n\t"
"s_waitcnt lgkmcnt(0)\n\t"
"s_barrier\n\t"
"v_mfma_f32_16x16x32_bf16 v[104:107], a[120:123], a[152:155], v[104:107]\n\t"
"ds_read_b128 a[144:147], v224 offset:16544\n\t"
"v_add_u32 v248, 101632, v226\n\t"
"v_mfma_f32_16x16x32_bf16 v[104:107], a[124:127], a[168:171], v[104:107]\n\t"
"ds_read_b128 a[148:151], v224 offset:16800\n\t"
"v_mfma_f32_16x16x32_bf16 v[120:123], a[128:131], a[152:155], v[120:123]\n\t"
"ds_read_b128 a[160:163], v224 offset:18592\n\t"
"v_mfma_f32_16x16x32_bf16 v[120:123], a[132:135], a[168:171], v[120:123]\n\t"
"ds_read_b128 a[164:167], v224 offset:18848\n\t"
"v_mfma_f32_16x16x32_bf16 v[136:139], a[136:139], a[152:155], v[136:139]\n\t"
"v_xor_b32 v249, 32, v248\n\t"
"v_mfma_f32_16x16x32_bf16 v[136:139], a[140:143], a[168:171], v[136:139]\n\t"
"v_mfma_f32_16x16x32_bf16 v[140:143], a[136:139], a[156:159], v[140:143]\n\t"
"ds_read_b128 a[168:171], v224 offset:19104\n\t"
"v_xor_b32 v250, 64, v248\n\t"
"v_mfma_f32_16x16x32_bf16 v[140:143], a[140:143], a[172:175], v[140:143]\n\t"
"buffer_load_dwordx4 a[136:139], off, s[28:31], s78 offset:0 sc1\n\t"
"v_mfma_f32_16x16x32_bf16 v[124:127], a[128:131], a[156:159], v[124:127]\n\t"
"ds_read_b128 a[152:155], v224 offset:17056\n\t"
"v_mfma_f32_16x16x32_bf16 v[124:127], a[132:135], a[172:175], v[124:127]\n\t"
"v_mfma_f32_16x16x32_bf16 v[108:111], a[120:123], a[156:159], v[108:111]\n\t"
"ds_read_b128 a[156:159], v224 offset:17312\n\t"
"v_xor_b32 v251, 96, v248\n\t"
"v_mfma_f32_16x16x32_bf16 v[108:111], a[124:127], a[172:175], v[108:111]\n\t"
"buffer_load_dwordx4 a[140:143], off, s[28:31], s78 offset:1024 sc1\n\t"
"v_mfma_f32_4x4x4_16b_bf16 v[200:203], a[220:221], a[178:179], 0\n\t"
"ds_read_b64_tr_b16 a[178:179], v248 offset:20864\n\t"
"v_mfma_f32_4x4x4_16b_bf16 v[204:207], a[220:221], a[186:187], 0\n\t"
"v_mfma_f32_4x4x4_16b_bf16 v[216:219], a[220:221], a[194:195], 0\n\t"
"ds_read_b64_tr_b16 a[186:187], v249 offset:20864\n\t"
"v_mfma_f32_4x4x4_16b_bf16 v[220:223], a[220:221], a[202:203], 0\n\t"
".p2align 3\n\t"
"ds_read_b128 a[172:175], v224 offset:19360\n\t"
"v_pk_fma_f32 v[0:1], v[96:97], s[90:91], v[0:1] neg_lo:[1,0,0] neg_hi:[1,0,0]\n\t"
"v_pk_fma_f32 v[2:3], v[98:99], s[90:91], v[2:3] neg_lo:[1,0,0] neg_hi:[1,0,0]\n\t"
"v_pk_fma_f32 v[16:17], v[112:113], s[90:91], v[16:17] neg_lo:[1,0,0] neg_hi:[1,0,0]\n\t"
"v_pk_fma_f32 v[18:19], v[114:115], s[90:91], v[18:19] neg_lo:[1,0,0] neg_hi:[1,0,0]\n\t"
"v_pk_fma_f32 v[32:33], v[128:129], s[90:91], v[32:33] neg_lo:[1,0,0] neg_hi:[1,0,0]\n\t"
"v_pk_fma_f32 v[34:35], v[130:131], s[90:91], v[34:35] neg_lo:[1,0,0] neg_hi:[1,0,0]\n\t"
"v_pk_fma_f32 v[36:37], v[132:133], s[90:91], v[36:37] neg_lo:[1,0,0] neg_hi:[1,0,0]\n\t"
"v_pk_fma_f32 v[38:39], v[134:135], s[90:91], v[38:39] neg_lo:[1,0,0] neg_hi:[1,0,0]\n\t"
"v_pk_fma_f32 v[20:21], v[116:117], s[90:91], v[20:21] neg_lo:[1,0,0] neg_hi:[1,0,0]\n\t"
"v_pk_fma_f32 v[22:23], v[118:119], s[90:91], v[22:23] neg_lo:[1,0,0] neg_hi:[1,0,0]\n\t"
"v_pk_fma_f32 v[4:5], v[100:101], s[90:91], v[4:5] neg_lo:[1,0,0] neg_hi:[1,0,0]\n\t"
"v_pk_fma_f32 v[6:7], v[102:103], s[90:91], v[6:7] neg_lo:[1,0,0] neg_hi:[1,0,0]\n\t"
"v_pk_fma_f32 v[8:9], v[104:105], s[90:91], v[8:9] neg_lo:[1,0,0] neg_hi:[1,0,0]\n\t"
"v_pk_fma_f32 v[10:11], v[106:107], s[90:91], v[10:11] neg_lo:[1,0,0] neg_hi:[1,0,0]\n\t"
"v_pk_fma_f32 v[24:25], v[120:121], s[90:91], v[24:25] neg_lo:[1,0,0] neg_hi:[1,0,0]\n\t"
"v_pk_fma_f32 v[26:27], v[122:123], s[90:91], v[26:27] neg_lo:[1,0,0] neg_hi:[1,0,0]\n\t"
"v_pk_fma_f32 v[40:41], v[136:137], s[90:91], v[40:41] neg_lo:[1,0,0] neg_hi:[1,0,0]\n\t"
"v_pk_fma_f32 v[42:43], v[138:139], s[90:91], v[42:43] neg_lo:[1,0,0] neg_hi:[1,0,0]\n\t"
"v_pk_fma_f32 v[44:45], v[140:141], s[90:91], v[44:45] neg_lo:[1,0,0] neg_hi:[1,0,0]\n\t"
"v_pk_fma_f32 v[46:47], v[142:143], s[90:91], v[46:47] neg_lo:[1,0,0] neg_hi:[1,0,0]\n\t"
"v_pk_fma_f32 v[28:29], v[124:125], s[90:91], v[28:29] neg_lo:[1,0,0] neg_hi:[1,0,0]\n\t"
"v_pk_fma_f32 v[30:31], v[126:127], s[90:91], v[30:31] neg_lo:[1,0,0] neg_hi:[1,0,0]\n\t"
"v_pk_fma_f32 v[12:13], v[108:109], s[90:91], v[12:13] neg_lo:[1,0,0] neg_hi:[1,0,0]\n\t"
"v_pk_fma_f32 v[14:15], v[110:111], s[90:91], v[14:15] neg_lo:[1,0,0] neg_hi:[1,0,0]\n\t"
"v_mfma_f32_4x4x4_16b_bf16 v[192:195], a[222:223], a[176:177], v[200:203]\n\t"
"ds_read_b64_tr_b16 a[194:195], v250 offset:20864\n\t"
"v_mfma_f32_4x4x4_16b_bf16 v[196:199], a[222:223], a[184:185], v[204:207]\n\t"
"ds_read_b64_tr_b16 a[202:203], v251 offset:20864\n\t"
"v_mfma_f32_4x4x4_16b_bf16 v[200:203], a[222:223], a[180:181], v[200:203]\n\t"
"ds_read_b64_tr_b16 a[176:177], v248 offset:20736\n\t"
"v_mfma_f32_4x4x4_16b_bf16 v[204:207], a[222:223], a[188:189], v[204:207]\n\t"
"v_mfma_f32_4x4x4_16b_bf16 v[208:211], a[222:223], a[192:193], v[216:219]\n\t"
"ds_read_b64_tr_b16 a[184:185], v249 offset:20736\n\t"
"v_mfma_f32_4x4x4_16b_bf16 v[212:215], a[222:223], a[200:201], v[220:223]\n\t"
"ds_read_b64_tr_b16 a[180:181], v249 offset:20992\n\t"
"v_mfma_f32_4x4x4_16b_bf16 v[216:219], a[222:223], a[196:197], v[216:219]\n\t"
"ds_read_b64_tr_b16 a[188:189], v248 offset:20992\n\t"
"v_mfma_f32_4x4x4_16b_bf16 v[220:223], a[222:223], a[204:205], v[220:223]\n\t"
"s_waitcnt vmcnt(14) lgkmcnt(8)\n\t"
"buffer_load_dwordx4 a[132:135], off, s[84:87], s78 offset:3072 sc1\n\t"
"v_mfma_f32_16x16x32_bf16 v[48:51], a[48:51], a[144:147], v[48:51]\n\t"
"ds_read_b64_tr_b16 a[192:193], v250 offset:20736\n\t"
"v_cvt_pk_bf16_f32 v192, -v192, -v196\n\t"
"v_mfma_f32_16x16x32_bf16 v[48:51], a[52:55], a[160:163], v[48:51]\n\t"
"ds_read_b64_tr_b16 a[200:201], v251 offset:20736\n\t"
"v_cvt_pk_bf16_f32 v194, -v194, -v198\n\t"
"v_mfma_f32_16x16x32_bf16 v[64:67], a[56:59], a[144:147], v[64:67]\n\t"
"ds_write_addtid_b32 v194 offset:1024\n\t"
"v_cvt_pk_bf16_f32 v202, v202, v206\n\t"
"v_mfma_f32_16x16x32_bf16 v[64:67], a[60:63], a[160:163], v[64:67]\n\t"
"buffer_load_dwordx4 a[128:131], off, s[84:87], s78 offset:2048 sc1\n\t"
"v_mfma_f32_16x16x32_bf16 v[80:83], a[64:67], a[144:147], v[80:83]\n\t"
"ds_read_b128 a[144:147], v224 offset:17568\n\t"
"v_cvt_pk_bf16_f32 v209, -v209, -v213\n\t"
"v_mfma_f32_16x16x32_bf16 v[80:83], a[68:71], a[160:163], v[80:83]\n\t"
"ds_write_addtid_b32 v202 offset:9344\n\t"
"v_cvt_pk_bf16_f32 v203, v203, v207\n\t"
"v_mfma_f32_16x16x32_bf16 v[84:87], a[64:67], a[148:151], v[84:87]\n\t"
"ds_read_b128 a[160:163], v224 offset:19616\n\t"
"v_cvt_pk_bf16_f32 v193, -v193, -v197\n\t"
"v_mfma_f32_16x16x32_bf16 v[84:87], a[68:71], a[164:167], v[84:87]\n\t"
"buffer_load_dwordx4 a[120:123], off, s[84:87], s78 offset:0 sc1\n\t"
"v_mfma_f32_16x16x32_bf16 v[68:71], a[56:59], a[148:151], v[68:71]\n\t"
"ds_write_addtid_b32 v193 offset:4128\n\t"
"v_cvt_pk_bf16_f32 v195, -v195, -v199\n\t"
"v_mfma_f32_16x16x32_bf16 v[68:71], a[60:63], a[164:167], v[68:71]\n\t"
"ds_write_addtid_b32 v203 offset:13472\n\t"
"v_cvt_pk_bf16_f32 v217, v217, v221\n\t"
"v_mfma_f32_16x16x32_bf16 v[52:55], a[48:51], a[148:151], v[52:55]\n\t"
"ds_read_b128 a[148:151], v224 offset:17824\n\t"
"v_cvt_pk_bf16_f32 v216, v216, v220\n\t"
"v_mfma_f32_16x16x32_bf16 v[52:55], a[52:55], a[164:167], v[52:55]\n\t"
"buffer_load_dwordx4 a[124:127], off, s[84:87], s78 offset:1024 sc1\n\t"
"v_mfma_f32_16x16x32_bf16 v[56:59], a[48:51], a[152:155], v[56:59]\n\t"
"ds_read_b128 a[164:167], v224 offset:19872\n\t"
"v_cvt_pk_bf16_f32 v219, v219, v223\n\t"
"v_mfma_f32_16x16x32_bf16 v[56:59], a[52:55], a[168:171], v[56:59]\n\t"
"ds_write_addtid_b32 v216 offset:10368\n\t"
"v_mfma_f32_16x16x32_bf16 v[72:75], a[56:59], a[152:155], v[72:75]\n\t"
"ds_write_addtid_b32 v209 offset:6176\n\t"
"v_mfma_f32_16x16x32_bf16 v[72:75], a[60:63], a[168:171], v[72:75]\n\t"
"ds_write_addtid_b32 v195 offset:5152\n\t"
"v_cvt_pk_bf16_f32 v200, v200, v204\n\t"
"v_mfma_f32_16x16x32_bf16 v[88:91], a[64:67], a[152:155], v[88:91]\n\t"
"ds_read_b128 a[152:155], v224 offset:18080\n\t"
"v_cvt_pk_bf16_f32 v218, v218, v222\n\t"
"v_mfma_f32_16x16x32_bf16 v[88:91], a[68:71], a[168:171], v[88:91]\n\t"
"s_waitcnt lgkmcnt(8)\n\t"
"ds_read_b128 a[168:171], v224 offset:20128\n\t"
"v_cvt_pk_bf16_f32 v210, -v210, -v214\n\t"
"v_mfma_f32_16x16x32_bf16 v[92:95], a[64:67], a[156:159], v[92:95]\n\t"
"ds_write_addtid_b32 v219 offset:15520\n\t"
"v_cvt_pk_bf16_f32 v208, -v208, -v212\n\t"
"v_mfma_f32_16x16x32_bf16 v[92:95], a[68:71], a[172:175], v[92:95]\n\t"
"ds_write_addtid_b32 v208 offset:2048\n\t"
"v_cvt_pk_bf16_f32 v201, v201, v205\n\t"
"v_mfma_f32_16x16x32_bf16 v[76:79], a[56:59], a[156:159], v[76:79]\n\t"
"ds_write_addtid_b32 v200 offset:8320\n\t"
"v_cvt_pk_bf16_f32 v211, -v211, -v215\n\t"
"v_mfma_f32_16x16x32_bf16 v[76:79], a[60:63], a[172:175], v[76:79]\n\t"
"ds_write_addtid_b32 v211 offset:7200\n\t"
"v_mfma_f32_16x16x32_bf16 v[60:63], a[48:51], a[156:159], v[60:63]\n\t"
"ds_write_addtid_b32 v201 offset:12448\n\t"
"v_mfma_f32_16x16x32_bf16 v[60:63], a[52:55], a[172:175], v[60:63]\n\t"
"s_waitcnt vmcnt(12) lgkmcnt(9)\n\t"
"ds_write_addtid_b32 v210 offset:3072\n\t"
"v_mfma_f32_16x16x32_bf16 v[144:147], a[72:75], a[144:147], v[144:147]\n\t"
"ds_write_addtid_b32 v217 offset:14496\n\t"
"v_mfma_f32_16x16x32_bf16 v[144:147], a[76:79], a[160:163], v[144:147]\n\t"
"ds_write_addtid_b32 v192 offset:0\n\t"
"v_mfma_f32_16x16x32_bf16 v[160:163], a[80:83], a[144:147], v[160:163]\n\t"
"ds_write_addtid_b32 v218 offset:11392\n\t"
"v_mfma_f32_16x16x32_bf16 v[160:163], a[84:87], a[160:163], v[160:163]\n\t"
"ds_read_b128 a[156:159], v224 offset:18336\n\t"
"v_mfma_f32_16x16x32_bf16 v[176:179], a[88:91], a[144:147], v[176:179]\n\t"
"v_mfma_f32_16x16x32_bf16 v[176:179], a[92:95], a[160:163], v[176:179]\n\t"
"ds_read_b128 a[172:175], v224 offset:20384\n\t"
"v_mfma_f32_16x16x32_bf16 v[180:183], a[88:91], a[148:151], v[180:183]\n\t"
"v_mfma_f32_16x16x32_bf16 v[180:183], a[92:95], a[164:167], v[180:183]\n\t"
"ds_read_b64_tr_b16 a[196:197], v251 offset:20992\n\t"
"v_mfma_f32_16x16x32_bf16 v[164:167], a[80:83], a[148:151], v[164:167]\n\t"
"ds_read_b64_tr_b16 a[204:205], v250 offset:20992\n\t"
"v_mfma_f32_16x16x32_bf16 v[164:167], a[84:87], a[164:167], v[164:167]\n\t"
"v_mfma_f32_16x16x32_bf16 v[148:151], a[72:75], a[148:151], v[148:151]\n\t"
"s_waitcnt lgkmcnt(2)\n\t"
"v_mfma_f32_16x16x32_bf16 v[148:151], a[76:79], a[164:167], v[148:151]\n\t"
"s_barrier\n\t"
"v_mfma_f32_16x16x32_bf16 v[152:155], a[72:75], a[152:155], v[152:155]\n\t"
"s_add_u32 m0, s26, 35584\n\t"
"ds_read_b128 a[144:147], v224 offset:0\n\t"
"ds_read_b128 a[148:151], v224 offset:256\n\t"
"v_mfma_f32_16x16x32_bf16 v[152:155], a[76:79], a[168:171], v[152:155]\n\t"
"buffer_load_dwordx4 v236, s[12:15], s22 offen offset:0 nt lds\n\t"
"v_mfma_f32_16x16x32_bf16 v[168:171], a[80:83], a[152:155], v[168:171]\n\t"
"ds_read_b128 a[160:163], v224 offset:2048\n\t"
"v_mfma_f32_16x16x32_bf16 v[168:171], a[84:87], a[168:171], v[168:171]\n\t"
"ds_read_b128 a[164:167], v224 offset:2304\n\t"
"v_mfma_f32_16x16x32_bf16 v[184:187], a[88:91], a[152:155], v[184:187]\n\t"
"v_mfma_f32_16x16x32_bf16 v[184:187], a[92:95], a[168:171], v[184:187]\n\t"
"buffer_load_dwordx4 v237, s[12:15], s22 offen offset:1024 nt lds\n\t"
"v_mfma_f32_16x16x32_bf16 v[188:191], a[88:91], a[156:159], v[188:191]\n\t"
"ds_read_b128 a[168:171], v224 offset:2560\n\t"
"v_mfma_f32_16x16x32_bf16 v[188:191], a[92:95], a[172:175], v[188:191]\n\t"
"ds_read_b128 a[152:155], v224 offset:512\n\t"
"v_mfma_f32_16x16x32_bf16 v[172:175], a[80:83], a[156:159], v[172:175]\n\t"
"v_mfma_f32_16x16x32_bf16 v[172:175], a[84:87], a[172:175], v[172:175]\n\t"
"buffer_load_dwordx4 v238, s[12:15], s22 offen offset:2048 nt lds\n\t"
"v_mfma_f32_16x16x32_bf16 v[156:159], a[72:75], a[156:159], v[156:159]\n\t"
"ds_read_b128 a[156:159], v224 offset:768\n\t"
"v_mfma_f32_16x16x32_bf16 v[156:159], a[76:79], a[172:175], v[156:159]\n\t"
"s_waitcnt lgkmcnt(5)\n\t"
"ds_read_b128 a[172:175], v224 offset:2816\n\t"
"v_mfma_f32_4x4x4_16b_bf16 v[200:203], a[252:253], a[178:179], 0\n\t"
"v_mfma_f32_4x4x4_16b_bf16 v[204:207], a[252:253], a[186:187], 0\n\t"
"buffer_load_dwordx4 v239, s[12:15], s22 offen offset:3072 nt lds\n\t"
"v_mfma_f32_4x4x4_16b_bf16 v[216:219], a[252:253], a[194:195], 0\n\t"
"ds_read_b64_tr_b16 a[178:179], v248 offset:128\n\t"
"v_mfma_f32_4x4x4_16b_bf16 v[220:223], a[252:253], a[202:203], 0\n\t"
"ds_read_b64_tr_b16 a[186:187], v249 offset:128\n\t"
"v_mfma_f32_4x4x4_16b_bf16 v[192:195], a[254:255], a[176:177], v[200:203]\n\t"
"ds_read_b64_tr_b16 a[194:195], v250 offset:128\n\t"
"v_mfma_f32_4x4x4_16b_bf16 v[196:199], a[254:255], a[184:185], v[204:207]\n\t"
"s_mov_b64 exec, s[36:37]\n\t"
"buffer_load_dwordx4 v240, s[12:15], s22 offen offset:3584 nt lds\n\t"
"s_mov_b64 exec, -1\n\t"
"v_mfma_f32_4x4x4_16b_bf16 v[200:203], a[254:255], a[180:181], v[200:203]\n\t"
"ds_read_b64_tr_b16 a[202:203], v251 offset:128\n\t"
"v_mfma_f32_4x4x4_16b_bf16 v[204:207], a[254:255], a[188:189], v[204:207]\n\t"
"ds_read_b64_tr_b16 a[176:177], v248 offset:0\n\t"
"v_mfma_f32_4x4x4_16b_bf16 v[208:211], a[254:255], a[192:193], v[216:219]\n\t"
"ds_read_b64_tr_b16 a[184:185], v249 offset:0\n\t"
"v_mfma_f32_4x4x4_16b_bf16 v[212:215], a[254:255], a[200:201], v[220:223]\n\t"
"s_lshl_b32 m0, s73, 8\n\t"
"v_mfma_f32_4x4x4_16b_bf16 v[216:219], a[254:255], a[196:197], v[216:219]\n\t"
"ds_read_b64_tr_b16 a[180:181], v249 offset:256\n\t"
"v_mfma_f32_4x4x4_16b_bf16 v[220:223], a[254:255], a[204:205], v[220:223]\n\t"
"s_waitcnt vmcnt(11) lgkmcnt(7)\n\t"
"ds_read_b64_tr_b16 a[188:189], v248 offset:256\n\t"
"v_mfma_f32_16x16x32_bf16 v[48:51], a[96:99], a[144:147], v[48:51]\n\t"
"ds_read_b64_tr_b16 a[192:193], v250 offset:0\n\t"
"v_mfma_f32_16x16x32_bf16 v[48:51], a[100:103], a[160:163], v[48:51]\n\t"
"v_cvt_pk_bf16_f32 v192, -v192, -v196\n\t"
"v_mfma_f32_16x16x32_bf16 v[64:67], a[104:107], a[144:147], v[64:67]\n\t"
"ds_write_addtid_b32 v192 offset:16544\n\t"
"v_cvt_pk_bf16_f32 v202, v202, v206\n\t"
"v_mfma_f32_16x16x32_bf16 v[64:67], a[108:111], a[160:163], v[64:67]\n\t"
"ds_write_addtid_b32 v202 offset:25888\n\t"
"v_mfma_f32_16x16x32_bf16 v[80:83], a[112:115], a[144:147], v[80:83]\n\t"
"ds_read_b128 a[144:147], v224 offset:1024\n\t"
"v_cvt_pk_bf16_f32 v201, v201, v205\n\t"
"v_mfma_f32_16x16x32_bf16 v[80:83], a[116:119], a[160:163], v[80:83]\n\t"
"v_cvt_pk_bf16_f32 v195, -v195, -v199\n\t"
"v_cvt_pk_bf16_f32 v208, -v208, -v212\n\t"
"v_mfma_f32_16x16x32_bf16 v[84:87], a[112:115], a[148:151], v[84:87]\n\t"
"ds_read_b128 a[160:163], v224 offset:3072\n\t"
"v_cvt_pk_bf16_f32 v193, -v193, -v197\n\t"
"v_mfma_f32_16x16x32_bf16 v[84:87], a[116:119], a[164:167], v[84:87]\n\t"
"ds_write_addtid_b32 v193 offset:20672\n\t"
"v_cvt_pk_bf16_f32 v219, v219, v223\n\t"
"v_mfma_f32_16x16x32_bf16 v[68:71], a[104:107], a[148:151], v[68:71]\n\t"
"ds_write_addtid_b32 v195 offset:21696\n\t"
"v_cvt_pk_bf16_f32 v216, v216, v220\n\t"
"v_mfma_f32_16x16x32_bf16 v[68:71], a[108:111], a[164:167], v[68:71]\n\t"
"v_mfma_f32_16x16x32_bf16 v[52:55], a[96:99], a[148:151], v[52:55]\n\t"
"ds_read_b128 a[148:151], v224 offset:1280\n\t"
"v_mfma_f32_16x16x32_bf16 v[52:55], a[100:103], a[164:167], v[52:55]\n\t"
"ds_read_b128 a[164:167], v224 offset:3328\n\t"
"v_mfma_f32_16x16x32_bf16 v[56:59], a[96:99], a[152:155], v[56:59]\n\t"
"ds_write_addtid_b32 v201 offset:28992\n\t"
"v_cvt_pk_bf16_f32 v218, v218, v222\n\t"
"v_mfma_f32_16x16x32_bf16 v[56:59], a[100:103], a[168:171], v[56:59]\n\t"
"ds_write_addtid_b32 v208 offset:18592\n\t"
"v_cvt_pk_bf16_f32 v210, -v210, -v214\n\t"
"v_mfma_f32_16x16x32_bf16 v[72:75], a[104:107], a[152:155], v[72:75]\n\t"
"ds_write_addtid_b32 v210 offset:19616\n\t"
"v_cvt_pk_bf16_f32 v209, -v209, -v213\n\t"
"v_mfma_f32_16x16x32_bf16 v[72:75], a[108:111], a[168:171], v[72:75]\n\t"
"ds_write_addtid_b32 v216 offset:26912\n\t"
"v_mfma_f32_16x16x32_bf16 v[88:91], a[112:115], a[152:155], v[88:91]\n\t"
"ds_read_b128 a[152:155], v224 offset:1536\n\t"
"v_cvt_pk_bf16_f32 v217, v217, v221\n\t"
"v_mfma_f32_16x16x32_bf16 v[88:91], a[116:119], a[168:171], v[88:91]\n\t"
"ds_read_b128 a[168:171], v224 offset:3584\n\t"
"v_cvt_pk_bf16_f32 v194, -v194, -v198\n\t"
"v_mfma_f32_16x16x32_bf16 v[92:95], a[112:115], a[156:159], v[92:95]\n\t"
"ds_write_addtid_b32 v218 offset:27936\n\t"
"v_cvt_pk_bf16_f32 v203, v203, v207\n\t"
"v_mfma_f32_16x16x32_bf16 v[92:95], a[116:119], a[172:175], v[92:95]\n\t"
"ds_write_addtid_b32 v209 offset:22720\n\t"
"v_cvt_pk_bf16_f32 v200, v200, v204\n\t"
"v_mfma_f32_16x16x32_bf16 v[76:79], a[104:107], a[156:159], v[76:79]\n\t"
"ds_write_addtid_b32 v219 offset:32064\n\t"
"v_cvt_pk_bf16_f32 v211, -v211, -v215\n\t"
"v_mfma_f32_16x16x32_bf16 v[76:79], a[108:111], a[172:175], v[76:79]\n\t"
"ds_write_addtid_b32 v200 offset:24864\n\t"
"v_mfma_f32_16x16x32_bf16 v[60:63], a[96:99], a[156:159], v[60:63]\n\t"
"ds_read_b128 a[156:159], v224 offset:1792\n\t"
"v_mfma_f32_16x16x32_bf16 v[60:63], a[100:103], a[172:175], v[60:63]\n\t"
"s_waitcnt vmcnt(5) lgkmcnt(9)\n\t"
"ds_write_addtid_b32 v203 offset:30016\n\t"
"v_mfma_f32_16x16x32_bf16 v[144:147], a[120:123], a[144:147], v[144:147]\n\t"
"ds_write_addtid_b32 v211 offset:23744\n\t"
"v_mfma_f32_16x16x32_bf16 v[144:147], a[124:127], a[160:163], v[144:147]\n\t"
"ds_write_addtid_b32 v194 offset:17568\n\t"
"v_mfma_f32_16x16x32_bf16 v[160:163], a[128:131], a[144:147], v[160:163]\n\t"
"ds_write_addtid_b32 v217 offset:31040\n\t"
"v_mfma_f32_16x16x32_bf16 v[160:163], a[132:135], a[160:163], v[160:163]\n\t"
"ds_read_b128 a[172:175], v224 offset:3840\n\t"
"v_mfma_f32_16x16x32_bf16 v[176:179], a[136:139], a[144:147], v[176:179]\n\t"
"v_mfma_f32_16x16x32_bf16 v[176:179], a[140:143], a[160:163], v[176:179]\n\t"
"ds_read_b64_tr_b16 a[200:201], v251 offset:0\n\t"
"v_mfma_f32_16x16x32_bf16 v[180:183], a[136:139], a[148:151], v[180:183]\n\t"
"v_mfma_f32_16x16x32_bf16 v[180:183], a[140:143], a[164:167], v[180:183]\n\t"
"ds_read_b64_tr_b16 a[196:197], v251 offset:256\n\t"
"v_mfma_f32_16x16x32_bf16 v[164:167], a[128:131], a[148:151], v[164:167]\n\t"
"ds_read_b64_tr_b16 a[204:205], v250 offset:256\n\t"
"v_mfma_f32_16x16x32_bf16 v[164:167], a[132:135], a[164:167], v[164:167]\n\t"
"v_mfma_f32_16x16x32_bf16 v[148:151], a[120:123], a[148:151], v[148:151]\n\t"
"s_waitcnt lgkmcnt(3)\n\t"
"v_mfma_f32_16x16x32_bf16 v[148:151], a[124:127], a[164:167], v[148:151]\n\t"
"s_barrier\n\t"
"v_mfma_f32_16x16x32_bf16 v[152:155], a[120:123], a[152:155], v[152:155]\n\t"
"ds_read_b128 a[144:147], v224 offset:16544\n\t"
"v_mfma_f32_16x16x32_bf16 v[152:155], a[124:127], a[168:171], v[152:155]\n\t"
"ds_read_b128 a[148:151], v224 offset:16800\n\t"
"v_mfma_f32_16x16x32_bf16 v[168:171], a[128:131], a[152:155], v[168:171]\n\t"
"ds_read_b128 a[160:163], v224 offset:18592\n\t"
"v_mfma_f32_16x16x32_bf16 v[168:171], a[132:135], a[168:171], v[168:171]\n\t"
"ds_read_b128 a[164:167], v224 offset:18848\n\t"
"v_mfma_f32_16x16x32_bf16 v[184:187], a[136:139], a[152:155], v[184:187]\n\t"
"v_mfma_f32_16x16x32_bf16 v[184:187], a[140:143], a[168:171], v[184:187]\n\t"
"v_mfma_f32_16x16x32_bf16 v[188:191], a[136:139], a[156:159], v[188:191]\n\t"
"ds_read_b128 a[152:155], v224 offset:17056\n\t"
"v_mfma_f32_16x16x32_bf16 v[188:191], a[140:143], a[172:175], v[188:191]\n\t"
"ds_read_b128 a[168:171], v224 offset:19104\n\t"
"v_mfma_f32_16x16x32_bf16 v[172:175], a[128:131], a[156:159], v[172:175]\n\t"
"v_mfma_f32_16x16x32_bf16 v[172:175], a[132:135], a[172:175], v[172:175]\n\t"
"v_mfma_f32_16x16x32_bf16 v[156:159], a[120:123], a[156:159], v[156:159]\n\t"
"ds_read_b128 a[156:159], v224 offset:17312\n\t"
"v_mfma_f32_16x16x32_bf16 v[156:159], a[124:127], a[172:175], v[156:159]\n\t"
"s_waitcnt lgkmcnt(5)\n\t"
"ds_read_b128 a[172:175], v224 offset:19360\n\t"
"v_mfma_f32_4x4x4_16b_bf16 v[200:203], a[252:253], a[178:179], 0\n\t"
"ds_read_b64_tr_b16 a[178:179], v248 offset:41600\n\t"
"v_mfma_f32_4x4x4_16b_bf16 v[204:207], a[252:253], a[186:187], 0\n\t"
"ds_read_b64_tr_b16 a[186:187], v249 offset:41600\n\t"
"v_mfma_f32_4x4x4_16b_bf16 v[216:219], a[252:253], a[194:195], 0\n\t"
"ds_read_b64_tr_b16 a[194:195], v250 offset:41600\n\t"
"v_mfma_f32_4x4x4_16b_bf16 v[220:223], a[252:253], a[202:203], 0\n\t"
".p2align 3\n\t"
"ds_read_b64_tr_b16 a[202:203], v251 offset:41600\n\t"
"v_pk_fma_f32 v[48:49], v[144:145], s[90:91], v[48:49]\n\t"
"v_pk_fma_f32 v[50:51], v[146:147], s[90:91], v[50:51]\n\t"
"v_pk_add_f32 v[144:145], v[48:49], v[144:145] neg_lo:[0,1] neg_hi:[0,1]\n\t"
"v_pk_add_f32 v[146:147], v[50:51], v[146:147] neg_lo:[0,1] neg_hi:[0,1]\n\t"
"v_pk_fma_f32 v[64:65], v[160:161], s[90:91], v[64:65]\n\t"
"v_pk_fma_f32 v[66:67], v[162:163], s[90:91], v[66:67]\n\t"
"v_pk_add_f32 v[160:161], v[64:65], v[160:161] neg_lo:[0,1] neg_hi:[0,1]\n\t"
"v_pk_add_f32 v[162:163], v[66:67], v[162:163] neg_lo:[0,1] neg_hi:[0,1]\n\t"
"v_pk_fma_f32 v[80:81], v[176:177], s[90:91], v[80:81]\n\t"
"v_pk_fma_f32 v[82:83], v[178:179], s[90:91], v[82:83]\n\t"
"v_pk_add_f32 v[176:177], v[80:81], v[176:177] neg_lo:[0,1] neg_hi:[0,1]\n\t"
"v_pk_add_f32 v[178:179], v[82:83], v[178:179] neg_lo:[0,1] neg_hi:[0,1]\n\t"
"v_pk_fma_f32 v[84:85], v[180:181], s[90:91], v[84:85]\n\t"
"v_pk_fma_f32 v[86:87], v[182:183], s[90:91], v[86:87]\n\t"
"v_pk_add_f32 v[180:181], v[84:85], v[180:181] neg_lo:[0,1] neg_hi:[0,1]\n\t"
"v_pk_add_f32 v[182:183], v[86:87], v[182:183] neg_lo:[0,1] neg_hi:[0,1]\n\t"
"v_pk_fma_f32 v[68:69], v[164:165], s[90:91], v[68:69]\n\t"
"v_pk_fma_f32 v[70:71], v[166:167], s[90:91], v[70:71]\n\t"
"v_pk_add_f32 v[164:165], v[68:69], v[164:165] neg_lo:[0,1] neg_hi:[0,1]\n\t"
"v_pk_add_f32 v[166:167], v[70:71], v[166:167] neg_lo:[0,1] neg_hi:[0,1]\n\t"
"v_pk_fma_f32 v[52:53], v[148:149], s[90:91], v[52:53]\n\t"
"v_pk_fma_f32 v[54:55], v[150:151], s[90:91], v[54:55]\n\t"
"v_pk_add_f32 v[148:149], v[52:53], v[148:149] neg_lo:[0,1] neg_hi:[0,1]\n\t"
"v_pk_add_f32 v[150:151], v[54:55], v[150:151] neg_lo:[0,1] neg_hi:[0,1]\n\t"
"v_pk_fma_f32 v[56:57], v[152:153], s[90:91], v[56:57]\n\t"
"v_pk_fma_f32 v[58:59], v[154:155], s[90:91], v[58:59]\n\t"
"v_pk_add_f32 v[152:153], v[56:57], v[152:153] neg_lo:[0,1] neg_hi:[0,1]\n\t"
"v_pk_add_f32 v[154:155], v[58:59], v[154:155] neg_lo:[0,1] neg_hi:[0,1]\n\t"
"v_pk_fma_f32 v[72:73], v[168:169], s[90:91], v[72:73]\n\t"
"v_pk_fma_f32 v[74:75], v[170:171], s[90:91], v[74:75]\n\t"
"v_pk_add_f32 v[168:169], v[72:73], v[168:169] neg_lo:[0,1] neg_hi:[0,1]\n\t"
"v_pk_add_f32 v[170:171], v[74:75], v[170:171] neg_lo:[0,1] neg_hi:[0,1]\n\t"
"v_pk_fma_f32 v[88:89], v[184:185], s[90:91], v[88:89]\n\t"
"v_pk_fma_f32 v[90:91], v[186:187], s[90:91], v[90:91]\n\t"
"v_pk_add_f32 v[184:185], v[88:89], v[184:185] neg_lo:[0,1] neg_hi:[0,1]\n\t"
"v_pk_add_f32 v[186:187], v[90:91], v[186:187] neg_lo:[0,1] neg_hi:[0,1]\n\t"
"v_pk_fma_f32 v[92:93], v[188:189], s[90:91], v[92:93]\n\t"
"v_pk_fma_f32 v[94:95], v[190:191], s[90:91], v[94:95]\n\t"
"v_pk_add_f32 v[188:189], v[92:93], v[188:189] neg_lo:[0,1] neg_hi:[0,1]\n\t"
"v_pk_add_f32 v[190:191], v[94:95], v[190:191] neg_lo:[0,1] neg_hi:[0,1]\n\t"
"v_pk_fma_f32 v[76:77], v[172:173], s[90:91], v[76:77]\n\t"
"v_pk_fma_f32 v[78:79], v[174:175], s[90:91], v[78:79]\n\t"
"v_pk_add_f32 v[172:173], v[76:77], v[172:173] neg_lo:[0,1] neg_hi:[0,1]\n\t"
"v_pk_add_f32 v[174:175], v[78:79], v[174:175] neg_lo:[0,1] neg_hi:[0,1]\n\t"
"v_pk_fma_f32 v[60:61], v[156:157], s[90:91], v[60:61]\n\t"
"v_pk_fma_f32 v[62:63], v[158:159], s[90:91], v[62:63]\n\t"
"v_pk_add_f32 v[156:157], v[60:61], v[156:157] neg_lo:[0,1] neg_hi:[0,1]\n\t"
"v_pk_add_f32 v[158:159], v[62:63], v[158:159] neg_lo:[0,1] neg_hi:[0,1]\n\t"
"v_cvt_pk_bf16_f32 v48, v48, v49\n\t"
"v_cvt_pk_bf16_f32 v64, v64, v65\n\t"
"v_mfma_f32_4x4x4_16b_bf16 v[192:195], a[254:255], a[176:177], v[200:203]\n\t"
"ds_read_b64_tr_b16 a[176:177], v248 offset:41472\n\t"
"v_cvt_pk_bf16_f32 v52, v52, v53\n\t"
"v_cvt_pk_bf16_f32 v53, v54, v55\n\t"
"v_mfma_f32_4x4x4_16b_bf16 v[196:199], a[254:255], a[184:185], v[204:207]\n\t"
"ds_read_b64_tr_b16 a[184:185], v249 offset:41472\n\t"
"v_mfma_f32_4x4x4_16b_bf16 v[200:203], a[254:255], a[180:181], v[200:203]\n\t"
"ds_read_b64_tr_b16 a[180:181], v249 offset:41728\n\t"
"v_cvt_pk_bf16_f32 v49, v50, v51\n\t"
"v_mfma_f32_4x4x4_16b_bf16 v[204:207], a[254:255], a[188:189], v[204:207]\n\t"
"ds_read_b64_tr_b16 a[188:189], v248 offset:41728\n\t"
"v_mfma_f32_4x4x4_16b_bf16 v[208:211], a[254:255], a[192:193], v[216:219]\n\t"
"v_cvt_pk_bf16_f32 v68, v68, v69\n\t"
"v_mfma_f32_4x4x4_16b_bf16 v[212:215], a[254:255], a[200:201], v[220:223]\n\t"
"ds_write2st64_b64 v228, v[48:49], v[52:53] offset0:0 offset1:13\n\t"
"v_cvt_pk_bf16_f32 v84, v84, v85\n\t"
"v_mfma_f32_4x4x4_16b_bf16 v[216:219], a[254:255], a[196:197], v[216:219]\n\t"
"v_cvt_pk_bf16_f32 v56, v56, v57\n\t"
"v_mfma_f32_4x4x4_16b_bf16 v[220:223], a[254:255], a[204:205], v[220:223]\n\t"
"s_waitcnt lgkmcnt(7)\n\t"
"v_cvt_pk_bf16_f32 v65, v66, v67\n\t"
"v_cvt_pk_bf16_f32 v80, v80, v81\n\t"
"v_mfma_f32_16x16x32_bf16 v[0:3], a[0:3], a[144:147], v[0:3]\n\t"
"v_cvt_pk_bf16_f32 v69, v70, v71\n\t"
"v_cvt_pk_bf16_f32 v92, v92, v93\n\t"
"v_mfma_f32_16x16x32_bf16 v[0:3], a[4:7], a[160:163], v[0:3]\n\t"
"ds_read_b64_tr_b16 a[192:193], v250 offset:41472\n\t"
"ds_write2st64_b64 v246, v[64:65], v[68:69] offset0:0 offset1:13\n\t"
"v_mfma_f32_16x16x32_bf16 v[16:19], a[8:11], a[144:147], v[16:19]\n\t"
"v_cvt_pk_bf16_f32 v81, v82, v83\n\t"
"v_cvt_pk_bf16_f32 v60, v60, v61\n\t"
"v_mfma_f32_16x16x32_bf16 v[16:19], a[12:15], a[160:163], v[16:19]\n\t"
"v_cvt_pk_bf16_f32 v85, v86, v87\n\t"
"v_cvt_pk_bf16_f32 v93, v94, v95\n\t"
"v_mfma_f32_16x16x32_bf16 v[32:35], a[16:19], a[144:147], v[32:35]\n\t"
"v_cvt_pk_bf16_f32 v193, -v193, -v197\n\t"
"v_cvt_pk_bf16_f32 v202, v202, v206\n\t"
"v_mfma_f32_16x16x32_bf16 v[32:35], a[20:23], a[160:163], v[32:35]\n\t"
"s_nop 0\n\t"
"ds_read_b128 a[144:147], v224 offset:17568\n\t"
"ds_read_b128 a[160:163], v224 offset:19616\n\t"
"ds_write2st64_b64 v247, v[80:81], v[84:85] offset0:0 offset1:13\n\t"
"v_mfma_f32_16x16x32_bf16 v[36:39], a[16:19], a[148:151], v[36:39]\n\t"
"v_cvt_pk_bf16_f32 v194, -v194, -v198\n\t"
"v_cvt_pk_bf16_f32 v219, v219, v223\n\t"
"v_mfma_f32_16x16x32_bf16 v[36:39], a[20:23], a[164:167], v[36:39]\n\t"
"v_cvt_pk_bf16_f32 v57, v58, v59\n\t"
"v_cvt_pk_bf16_f32 v76, v76, v77\n\t"
"v_mfma_f32_16x16x32_bf16 v[20:23], a[8:11], a[148:151], v[20:23]\n\t"
"v_cvt_pk_bf16_f32 v209, -v209, -v213\n\t"
"v_cvt_pk_bf16_f32 v61, v62, v63\n\t"
"v_mfma_f32_16x16x32_bf16 v[20:23], a[12:15], a[164:167], v[20:23]\n\t"
"ds_write_addtid_b32 v202 offset:9344\n\t"
"ds_write2st64_b64 v228, v[56:57], v[60:61] offset0:26 offset1:39\n\t"
"v_mfma_f32_16x16x32_bf16 v[4:7], a[0:3], a[148:151], v[4:7]\n\t"
"v_cvt_pk_bf16_f32 v201, v201, v205\n\t"
"v_cvt_pk_bf16_f32 v88, v88, v89\n\t"
"v_mfma_f32_16x16x32_bf16 v[4:7], a[4:7], a[164:167], v[4:7]\n\t"
"v_cvt_pk_bf16_f32 v77, v78, v79\n\t"
"v_cvt_pk_bf16_f32 v89, v90, v91\n\t"
"v_mfma_f32_16x16x32_bf16 v[8:11], a[0:3], a[152:155], v[8:11]\n\t"
"v_cvt_pk_bf16_f32 v72, v72, v73\n\t"
"v_cvt_pk_bf16_f32 v73, v74, v75\n\t"
"v_mfma_f32_16x16x32_bf16 v[8:11], a[4:7], a[168:171], v[8:11]\n\t"
"s_nop 0\n\t"
"ds_read_b128 a[148:151], v224 offset:17824\n\t"
"ds_read_b128 a[164:167], v224 offset:19872\n\t"
"ds_write2st64_b64 v247, v[88:89], v[92:93] offset0:26 offset1:39\n\t"
"v_mfma_f32_16x16x32_bf16 v[24:27], a[8:11], a[152:155], v[24:27]\n\t"
"v_cvt_pk_bf16_f32 v216, v216, v220\n\t"
"v_cvt_pk_bf16_f32 v218, v218, v222\n\t"
"v_mfma_f32_16x16x32_bf16 v[24:27], a[12:15], a[168:171], v[24:27]\n\t"
"v_cvt_pk_bf16_f32 v195, -v195, -v199\n\t"
"v_cvt_pk_bf16_f32 v217, v217, v221\n\t"
"v_mfma_f32_16x16x32_bf16 v[40:43], a[16:19], a[152:155], v[40:43]\n\t"
"v_cvt_pk_bf16_f32 v203, v203, v207\n\t"
"v_cvt_pk_bf16_f32 v210, -v210, -v214\n\t"
"v_mfma_f32_16x16x32_bf16 v[40:43], a[20:23], a[168:171], v[40:43]\n\t"
"ds_read_b128 a[152:155], v224 offset:18080\n\t"
"ds_write2st64_b64 v246, v[72:73], v[76:77] offset0:26 offset1:39\n\t"
"v_mfma_f32_16x16x32_bf16 v[44:47], a[16:19], a[156:159], v[44:47]\n\t"
"buffer_load_dwordx4 a[16:19], off, s[92:95], s59 offset:0 sc1\n\t"
"v_mfma_f32_16x16x32_bf16 v[44:47], a[20:23], a[172:175], v[44:47]\n\t"
"v_cvt_pk_bf16_f32 v192, -v192, -v196\n\t"
"v_cvt_pk_bf16_f32 v211, -v211, -v215\n\t"
"v_mfma_f32_16x16x32_bf16 v[28:31], a[8:11], a[156:159], v[28:31]\n\t"
"buffer_load_dwordx4 a[20:23], off, s[92:95], s59 offset:1024 sc1\n\t"
"v_mfma_f32_16x16x32_bf16 v[28:31], a[12:15], a[172:175], v[28:31]\n\t"
"s_nop 0\n\t"
"ds_write_addtid_b32 v193 offset:4128\n\t"
"ds_write_addtid_b32 v194 offset:1024\n\t"
"ds_write_addtid_b32 v210 offset:3072\n\t"
"ds_write_addtid_b32 v211 offset:7200\n\t"
"ds_write_addtid_b32 v216 offset:10368\n\t"
"ds_write_addtid_b32 v217 offset:14496\n\t"
"v_mfma_f32_16x16x32_bf16 v[12:15], a[0:3], a[156:159], v[12:15]\n\t"
"ds_write_addtid_b32 v219 offset:15520\n\t"
"v_cvt_pk_bf16_f32 v200, v200, v204\n\t"
"v_mfma_f32_16x16x32_bf16 v[12:15], a[4:7], a[172:175], v[12:15]\n\t"
"s_waitcnt lgkmcnt(9)\n\t"
"ds_write_addtid_b32 v203 offset:13472\n\t"
"v_cvt_pk_bf16_f32 v208, -v208, -v212\n\t"
"v_mfma_f32_16x16x32_bf16 v[96:99], a[24:27], a[144:147], v[96:99]\n\t"
"s_nop 0\n\t"
"ds_write_addtid_b32 v192 offset:0\n\t"
"ds_write_addtid_b32 v195 offset:5152\n\t"
"v_mfma_f32_16x16x32_bf16 v[96:99], a[28:31], a[160:163], v[96:99]\n\t"
"s_nop 0\n\t"
"ds_write_addtid_b32 v201 offset:12448\n\t"
"ds_write_addtid_b32 v208 offset:2048\n\t"
"ds_write_addtid_b32 v218 offset:11392\n\t"
"v_mfma_f32_16x16x32_bf16 v[112:115], a[32:35], a[144:147], v[112:115]\n\t"
"s_nop 0\n\t"
"ds_write_addtid_b32 v200 offset:8320\n\t"
"ds_write_addtid_b32 v209 offset:6176\n\t"
"v_mfma_f32_16x16x32_bf16 v[112:115], a[36:39], a[160:163], v[112:115]\n\t"
"buffer_load_dwordx4 a[12:15], off, s[96:99], s58 offset:3072 sc1\n\t"
"v_mfma_f32_16x16x32_bf16 v[128:131], a[40:43], a[144:147], v[128:131]\n\t"
"s_nop 0\n\t"
"ds_read_b128 a[156:159], v224 offset:18336\n\t"
"ds_read_b128 a[172:175], v224 offset:20384\n\t"
"v_mfma_f32_16x16x32_bf16 v[128:131], a[44:47], a[160:163], v[128:131]\n\t"
"buffer_load_dwordx4 a[8:11], off, s[96:99], s58 offset:2048 sc1\n\t"
"v_mfma_f32_16x16x32_bf16 v[132:135], a[40:43], a[148:151], v[132:135]\n\t"
"s_nop 0\n\t"
"ds_read_b128 a[168:171], v224 offset:20128\n\t"
"ds_read_b64_tr_b16 a[200:201], v251 offset:41472\n\t"
"v_mfma_f32_16x16x32_bf16 v[132:135], a[44:47], a[164:167], v[132:135]\n\t"
"buffer_load_dwordx4 a[0:3], off, s[96:99], s58 offset:0 sc1\n\t"
"v_mfma_f32_16x16x32_bf16 v[116:119], a[32:35], a[148:151], v[116:119]\n\t"
"s_nop 0\n\t"
"ds_read_b64_tr_b16 a[196:197], v251 offset:41728\n\t"
"ds_read_b64_tr_b16 a[204:205], v250 offset:41728\n\t"
"v_mfma_f32_16x16x32_bf16 v[116:119], a[36:39], a[164:167], v[116:119]\n\t"
"buffer_load_dwordx4 a[4:7], off, s[96:99], s58 offset:1024 sc1\n\t"
"v_mfma_f32_16x16x32_bf16 v[100:103], a[24:27], a[148:151], v[100:103]\n\t"
"s_waitcnt lgkmcnt(3)\n\t"
"v_mfma_f32_16x16x32_bf16 v[100:103], a[28:31], a[164:167], v[100:103]\n\t"
"s_barrier\n\t"
"v_mfma_f32_16x16x32_bf16 v[104:107], a[24:27], a[152:155], v[104:107]\n\t"
"ds_read_b128 a[144:147], v224 offset:0\n\t"
"v_cvt_pk_bf16_f32 v168, v168, v169\n\t"
"v_mfma_f32_16x16x32_bf16 v[104:107], a[28:31], a[168:171], v[104:107]\n\t"
"ds_read_b128 a[160:163], v224 offset:2048\n\t"
"v_cvt_pk_bf16_f32 v184, v184, v185\n\t"
"v_mfma_f32_16x16x32_bf16 v[120:123], a[32:35], a[152:155], v[120:123]\n\t"
"ds_read_b128 a[148:151], v224 offset:256\n\t"
"v_cvt_pk_bf16_f32 v176, v176, v177\n\t"
"v_mfma_f32_16x16x32_bf16 v[120:123], a[36:39], a[168:171], v[120:123]\n\t"
"ds_read_b128 a[164:167], v224 offset:2304\n\t"
"v_cvt_pk_bf16_f32 v156, v156, v157\n\t"
"v_mfma_f32_16x16x32_bf16 v[136:139], a[40:43], a[152:155], v[136:139]\n\t"
"v_cvt_pk_bf16_f32 v148, v148, v149\n\t"
"v_cvt_pk_bf16_f32 v169, v170, v171\n\t"
"v_mfma_f32_16x16x32_bf16 v[136:139], a[44:47], a[168:171], v[136:139]\n\t"
"ds_read_b128 v[72:75], v230 offset:0\n\t"
"v_cvt_pk_bf16_f32 v144, v144, v145\n\t"
"v_mfma_f32_16x16x32_bf16 v[140:143], a[40:43], a[156:159], v[140:143]\n\t"
"ds_read_b128 a[152:155], v224 offset:512\n\t"
"v_cvt_pk_bf16_f32 v164, v164, v165\n\t"
"v_mfma_f32_16x16x32_bf16 v[140:143], a[44:47], a[172:175], v[140:143]\n\t"
"buffer_load_dwordx4 a[40:43], off, s[92:95], s58 offset:2048 sc1\n\t"
"v_mfma_f32_16x16x32_bf16 v[124:127], a[32:35], a[156:159], v[124:127]\n\t"
"ds_read_b128 v[92:95], v231 offset:13312\n\t"
"v_cvt_pk_bf16_f32 v152, v152, v153\n\t"
"v_mfma_f32_16x16x32_bf16 v[124:127], a[36:39], a[172:175], v[124:127]\n\t"
"buffer_load_dwordx4 a[44:47], off, s[92:95], s58 offset:3072 sc1\n\t"
"v_mfma_f32_16x16x32_bf16 v[108:111], a[24:27], a[156:159], v[108:111]\n\t"
"ds_read_b128 a[168:171], v224 offset:2560\n\t"
"v_cvt_pk_bf16_f32 v172, v172, v173\n\t"
"v_mfma_f32_16x16x32_bf16 v[108:111], a[28:31], a[172:175], v[108:111]\n\t"
"s_waitcnt lgkmcnt(6)\n\t"
"v_cvt_pk_bf16_f32 v145, v146, v147\n\t"
"v_cvt_pk_bf16_f32 v180, v180, v181\n\t"
"v_mfma_f32_4x4x4_16b_bf16 v[200:203], a[252:253], a[178:179], 0\n\t"
"ds_read_b128 v[56:59], v229 offset:0\n\t"
"buffer_load_dwordx4 a[36:39], off, s[92:95], s58 offset:1024 sc1\n\t"
"v_mfma_f32_4x4x4_16b_bf16 v[204:207], a[252:253], a[186:187], 0\n\t"
"v_cvt_pk_bf16_f32 v185, v186, v187\n\t"
"v_mfma_f32_4x4x4_16b_bf16 v[216:219], a[252:253], a[194:195], 0\n\t"
"ds_read_b128 v[88:91], v231 offset:0\n\t"
"v_mfma_f32_4x4x4_16b_bf16 v[220:223], a[252:253], a[202:203], 0\n\t"
"buffer_load_dwordx4 a[32:35], off, s[92:95], s58 offset:0 sc1\n\t"
"v_mfma_f32_4x4x4_16b_bf16 v[192:195], a[254:255], a[176:177], v[200:203]\n\t"
"ds_read_b128 v[60:63], v229 offset:13312\n\t"
"v_mfma_f32_4x4x4_16b_bf16 v[196:199], a[254:255], a[184:185], v[204:207]\n\t"
"ds_read_b128 v[76:79], v230 offset:13312\n\t"
"v_mfma_f32_4x4x4_16b_bf16 v[200:203], a[254:255], a[180:181], v[200:203]\n\t"
"buffer_load_dwordx4 a[24:27], off, s[96:99], s57 offset:2048 sc1\n\t"
"v_mfma_f32_4x4x4_16b_bf16 v[204:207], a[254:255], a[188:189], v[204:207]\n\t"
"v_cvt_pk_bf16_f32 v149, v150, v151\n\t"
"v_mfma_f32_4x4x4_16b_bf16 v[208:211], a[254:255], a[192:193], v[216:219]\n\t"
"ds_read_b128 a[172:175], v224 offset:2816\n\t"
"v_mfma_f32_4x4x4_16b_bf16 v[212:215], a[254:255], a[200:201], v[220:223]\n\t"
"ds_read_b128 a[156:159], v224 offset:768\n\t"
"s_waitcnt lgkmcnt(5)\n\t"
"buffer_store_dwordx4 v[56:59], v232, s[16:19], s24 offen nt\n\t"
"v_mfma_f32_4x4x4_16b_bf16 v[216:219], a[254:255], a[196:197], v[216:219]\n\t"
"v_cvt_pk_bf16_f32 v173, v174, v175\n\t"
"v_mfma_f32_4x4x4_16b_bf16 v[220:223], a[254:255], a[204:205], v[220:223]\n\t"
"s_waitcnt lgkmcnt(2)\n\t"
"s_barrier\n\t"
"v_mfma_f32_16x16x32_bf16 v[0:3], a[48:51], a[144:147], v[0:3]\n\t"
"ds_write2st64_b64 v228, v[144:145], v[148:149] offset0:0 offset1:13\n\t"
"v_mfma_f32_16x16x32_bf16 v[0:3], a[52:55], a[160:163], v[0:3]\n\t"
"buffer_store_dwordx4 v[72:75], v233, s[16:19], s24 offen nt\n\t"
"v_mfma_f32_16x16x32_bf16 v[16:19], a[56:59], a[144:147], v[16:19]\n\t"
"v_cvt_pk_bf16_f32 v160, v160, v161\n\t"
"v_cvt_pk_bf16_f32 v165, v166, v167\n\t"
"v_mfma_f32_16x16x32_bf16 v[16:19], a[60:63], a[160:163], v[16:19]\n\t"
"ds_write2st64_b64 v246, v[168:169], v[172:173] offset0:26 offset1:39\n\t"
"v_mfma_f32_16x16x32_bf16 v[32:35], a[64:67], a[144:147], v[32:35]\n\t"
"v_cvt_pk_bf16_f32 v161, v162, v163\n\t"
"v_cvt_pk_bf16_f32 v153, v154, v155\n\t"
"v_mfma_f32_16x16x32_bf16 v[32:35], a[68:71], a[160:163], v[32:35]\n\t"
"buffer_load_dwordx4 a[28:31], off, s[96:99], s57 offset:3072 sc1\n\t"
"v_mfma_f32_16x16x32_bf16 v[36:39], a[64:67], a[148:151], v[36:39]\n\t"
"v_cvt_pk_bf16_f32 v200, v200, v204\n\t"
"v_cvt_pk_bf16_f32 v188, v188, v189\n\t"
"v_mfma_f32_16x16x32_bf16 v[36:39], a[68:71], a[164:167], v[36:39]\n\t"
"s_nop 0\n\t"
"ds_read_b128 a[144:147], v224 offset:1024\n\t"
"ds_read_b128 a[160:163], v224 offset:3072\n\t"
"ds_write2st64_b64 v246, v[160:161], v[164:165] offset0:0 offset1:13\n\t"
"v_mfma_f32_16x16x32_bf16 v[20:23], a[56:59], a[148:151], v[20:23]\n\t"
"v_cvt_pk_bf16_f32 v177, v178, v179\n\t"
"v_cvt_pk_bf16_f32 v181, v182, v183\n\t"
"v_mfma_f32_16x16x32_bf16 v[20:23], a[60:63], a[164:167], v[20:23]\n\t"
"buffer_store_dwordx4 v[88:91], v234, s[16:19], s24 offen nt\n\t"
"v_mfma_f32_16x16x32_bf16 v[4:7], a[48:51], a[148:151], v[4:7]\n\t"
"v_cvt_pk_bf16_f32 v193, -v193, -v197\n\t"
"v_cvt_pk_bf16_f32 v202, v202, v206\n\t"
"v_mfma_f32_16x16x32_bf16 v[4:7], a[52:55], a[164:167], v[4:7]\n\t"
"s_nop 0\n\t"
"ds_read_b128 a[148:151], v224 offset:1280\n\t"
"ds_read_b128 a[164:167], v224 offset:3328\n\t"
"ds_write2st64_b64 v247, v[176:177], v[180:181] offset0:0 offset1:13\n\t"
"v_cvt_pk_bf16_f32 v195, -v195, -v199\n\t"
"v_mfma_f32_16x16x32_bf16 v[8:11], a[48:51], a[152:155], v[8:11]\n\t"
"v_cvt_pk_bf16_f32 v201, v201, v205\n\t"
"v_cvt_pk_bf16_f32 v208, -v208, -v212\n\t"
"v_mfma_f32_16x16x32_bf16 v[8:11], a[52:55], a[168:171], v[8:11]\n\t"
"buffer_store_dwordx4 v[60:63], v232, s[16:19], s27 offen nt\n\t"
"v_mfma_f32_16x16x32_bf16 v[24:27], a[56:59], a[152:155], v[24:27]\n\t"
"v_cvt_pk_bf16_f32 v210, -v210, -v214\n\t"
"v_cvt_pk_bf16_f32 v157, v158, v159\n\t"
"v_mfma_f32_16x16x32_bf16 v[24:27], a[60:63], a[168:171], v[24:27]\n\t"
"ds_write_addtid_b32 v208 offset:18592\n\t"
"ds_write2st64_b64 v228, v[152:153], v[156:157] offset0:26 offset1:39\n\t"
"v_mfma_f32_16x16x32_bf16 v[40:43], a[64:67], a[152:155], v[40:43]\n\t"
"v_cvt_pk_bf16_f32 v211, -v211, -v215\n\t"
"v_cvt_pk_bf16_f32 v216, v216, v220\n\t"
"v_mfma_f32_16x16x32_bf16 v[40:43], a[68:71], a[168:171], v[40:43]\n\t"
"s_waitcnt lgkmcnt(5)\n\t"
"v_cvt_pk_bf16_f32 v203, v203, v207\n\t"
"v_cvt_pk_bf16_f32 v219, v219, v223\n\t"
"v_mfma_f32_16x16x32_bf16 v[44:47], a[64:67], a[156:159], v[44:47]\n\t"
"v_cvt_pk_bf16_f32 v192, -v192, -v196\n\t"
"v_cvt_pk_bf16_f32 v189, v190, v191\n\t"
"v_mfma_f32_16x16x32_bf16 v[44:47], a[68:71], a[172:175], v[44:47]\n\t"
"s_nop 0\n\t"
"ds_write_addtid_b32 v192 offset:16544\n\t"
"ds_write_addtid_b32 v202 offset:25888\n\t"
"ds_write_addtid_b32 v203 offset:30016\n\t"
"ds_write_addtid_b32 v216 offset:26912\n\t"
"ds_write2st64_b64 v247, v[184:185], v[188:189] offset0:26 offset1:39\n\t"
"v_mfma_f32_16x16x32_bf16 v[28:31], a[56:59], a[156:159], v[28:31]\n\t"
"v_cvt_pk_bf16_f32 v209, -v209, -v213\n\t"
"v_cvt_pk_bf16_f32 v217, v217, v221\n\t"
"v_mfma_f32_16x16x32_bf16 v[28:31], a[60:63], a[172:175], v[28:31]\n\t"
"buffer_store_dwordx4 v[76:79], v233, s[16:19], s27 offen nt\n\t"
"v_mfma_f32_16x16x32_bf16 v[12:15], a[48:51], a[156:159], v[12:15]\n\t"
"v_cvt_pk_bf16_f32 v194, -v194, -v198\n\t"
"v_cvt_pk_bf16_f32 v218, v218, v222\n\t"
"v_mfma_f32_16x16x32_bf16 v[12:15], a[52:55], a[172:175], v[12:15]\n\t"
"s_nop 0\n\t"
"ds_write_addtid_b32 v193 offset:20672\n\t"
"ds_write_addtid_b32 v194 offset:17568\n\t"
"ds_write_addtid_b32 v195 offset:21696\n\t"
"ds_write_addtid_b32 v211 offset:23744\n\t"
"ds_write_addtid_b32 v217 offset:31040\n\t"
"ds_write_addtid_b32 v218 offset:27936\n\t"
"ds_write_addtid_b32 v219 offset:32064\n\t"
"v_mfma_f32_16x16x32_bf16 v[96:99], a[72:75], a[144:147], v[96:99]\n\t"
"ds_write_addtid_b32 v209 offset:22720\n\t"
"v_accvgpr_read_b32 v248, a208\n\t"
"v_mfma_f32_16x16x32_bf16 v[96:99], a[76:79], a[160:163], v[96:99]\n\t"
"ds_write_addtid_b32 v201 offset:28992\n\t"
"buffer_load_dwordx4 a[64:67], off, s[92:95], s65 offset:0 sc1\n\t"
"buffer_load_dwordx4 a[68:71], off, s[92:95], s65 offset:1024 sc1\n\t"
"v_mfma_f32_16x16x32_bf16 v[112:115], a[80:83], a[144:147], v[112:115]\n\t"
"s_nop 0\n\t"
"ds_write_addtid_b32 v200 offset:24864\n\t"
"ds_write_addtid_b32 v210 offset:19616\n\t"
"v_mfma_f32_16x16x32_bf16 v[112:115], a[84:87], a[160:163], v[112:115]\n\t"
"buffer_load_dwordx4 a[60:63], off, s[96:99], s64 offset:3072 sc1\n\t"
"v_mfma_f32_16x16x32_bf16 v[128:131], a[88:91], a[144:147], v[128:131]\n\t"
"s_nop 0\n\t"
"ds_read_b128 a[152:155], v224 offset:1536\n\t"
"ds_read_b128 a[156:159], v224 offset:1792\n\t"
"v_mfma_f32_16x16x32_bf16 v[128:131], a[92:95], a[160:163], v[128:131]\n\t"
"s_waitcnt lgkmcnt(13)\n\t"
"buffer_load_dwordx4 a[56:59], off, s[96:99], s64 offset:2048 sc1\n\t"
"buffer_load_dwordx4 a[48:51], off, s[96:99], s64 offset:0 sc1\n\t"
"v_mfma_f32_16x16x32_bf16 v[132:135], a[88:91], a[148:151], v[132:135]\n\t"
"s_nop 0\n\t"
"ds_read_b128 a[168:171], v224 offset:3584\n\t"
"ds_read_b128 a[172:175], v224 offset:3840\n\t"
"v_mfma_f32_16x16x32_bf16 v[132:135], a[92:95], a[164:167], v[132:135]\n\t"
"buffer_store_dwordx4 v[92:95], v234, s[16:19], s27 offen nt\n\t"
"v_mfma_f32_16x16x32_bf16 v[116:119], a[80:83], a[148:151], v[116:119]\n\t"
"v_add_u32 v248, 101632, v248\n\t"
"v_xor_b32 v249, 32, v248\n\t"
"v_mfma_f32_16x16x32_bf16 v[116:119], a[84:87], a[164:167], v[116:119]\n\t"
"v_xor_b32 v250, 64, v248\n\t"
"v_xor_b32 v251, 96, v248\n\t"
"v_mfma_f32_16x16x32_bf16 v[100:103], a[72:75], a[148:151], v[100:103]\n\t"
"buffer_load_dwordx4 a[52:55], off, s[96:99], s64 offset:1024 sc1\n\t"
"v_mfma_f32_16x16x32_bf16 v[100:103], a[76:79], a[164:167], v[100:103]\n\t"
"s_waitcnt lgkmcnt(0)\n\t"
"s_barrier\n\t"
"v_mfma_f32_16x16x32_bf16 v[104:107], a[72:75], a[152:155], v[104:107]\n\t"
"s_nop 0\n\t"
"ds_read_b128 a[144:147], v224 offset:16544\n\t"
"ds_read_b128 a[160:163], v224 offset:18592\n\t"
"v_mfma_f32_16x16x32_bf16 v[104:107], a[76:79], a[168:171], v[104:107]\n\t"
"s_nop 0\n\t"
"ds_read_b128 a[148:151], v224 offset:16800\n\t"
"ds_read_b128 a[164:167], v224 offset:18848\n\t"
"v_mfma_f32_16x16x32_bf16 v[120:123], a[80:83], a[152:155], v[120:123]\n\t"
"ds_read_b64_tr_b16 a[176:177], v248 offset:20736\n\t"
"v_mfma_f32_16x16x32_bf16 v[120:123], a[84:87], a[168:171], v[120:123]\n\t"
"ds_read_b64_tr_b16 a[178:179], v249 offset:20736\n\t"
"v_mfma_f32_16x16x32_bf16 v[136:139], a[88:91], a[152:155], v[136:139]\n\t"
"s_nop 0\n\t"
"ds_read_b64_tr_b16 a[180:181], v248 offset:21248\n\t"
"ds_read_b64_tr_b16 a[182:183], v249 offset:21248\n\t"
"v_mfma_f32_16x16x32_bf16 v[136:139], a[92:95], a[168:171], v[136:139]\n\t"
"s_nop 0\n\t"
"ds_read_b64_tr_b16 a[184:185], v250 offset:23040\n\t"
"ds_read_b64_tr_b16 a[186:187], v251 offset:23040\n\t"
"v_mfma_f32_16x16x32_bf16 v[140:143], a[88:91], a[156:159], v[140:143]\n\t"
"ds_read_b64_tr_b16 a[188:189], v250 offset:23552\n\t"
"v_mfma_f32_16x16x32_bf16 v[140:143], a[92:95], a[172:175], v[140:143]\n\t"
"s_nop 0\n\t"
"ds_read_b64_tr_b16 a[190:191], v251 offset:23552\n\t"
"ds_read_b64_tr_b16 a[192:193], v250 offset:20736\n\t"
"v_mfma_f32_16x16x32_bf16 v[124:127], a[80:83], a[156:159], v[124:127]\n\t"
"s_nop 0\n\t"
"ds_read_b64_tr_b16 a[194:195], v251 offset:20736\n\t"
"ds_read_b64_tr_b16 a[196:197], v250 offset:21248\n\t"
"v_mfma_f32_16x16x32_bf16 v[124:127], a[84:87], a[172:175], v[124:127]\n\t"
"s_waitcnt lgkmcnt(7)\n\t"
"ds_read_b64_tr_b16 a[198:199], v251 offset:21248\n\t"
"ds_read_b64_tr_b16 a[200:201], v248 offset:23040\n\t"
"v_mfma_f32_4x4x4_16b_bf16 v[192:195], a[210:211], a[176:177], 0\n\t"
"ds_read_b64_tr_b16 a[202:203], v249 offset:23040\n\t"
"v_mfma_f32_4x4x4_16b_bf16 v[196:199], a[210:211], a[178:179], 0\n\t"
"ds_read_b128 v[152:155], v229 offset:0\n\t"
"v_mfma_f32_4x4x4_16b_bf16 v[200:203], a[210:211], a[180:181], 0\n\t"
"ds_read_b128 a[168:171], v224 offset:19104\n\t"
"v_mfma_f32_4x4x4_16b_bf16 v[204:207], a[210:211], a[182:183], 0\n\t"
"buffer_load_dwordx4 a[88:91], off, s[92:95], s64 offset:2048 sc1\n\t"
"v_mfma_f32_16x16x32_bf16 v[108:111], a[72:75], a[156:159], v[108:111]\n\t"
"s_nop 0\n\t"
"ds_read_b128 a[156:159], v224 offset:17312\n\t"
"ds_read_b64_tr_b16 a[204:205], v248 offset:23552\n\t"
"v_mfma_f32_16x16x32_bf16 v[108:111], a[76:79], a[172:175], v[108:111]\n\t"
"buffer_load_dwordx4 a[92:95], off, s[92:95], s64 offset:3072 sc1\n\t"
"v_mfma_f32_16x16x32_bf16 v[0:3], a[96:99], a[144:147], v[0:3]\n\t"
"ds_read_b128 v[168:171], v230 offset:0\n\t"
"v_cvt_pk_bf16_f32 v194, v194, v198\n\t"
"v_mfma_f32_16x16x32_bf16 v[0:3], a[100:103], a[160:163], v[0:3]\n\t"
"v_cvt_pk_bf16_f32 v196, v192, v196\n\t"
"v_cvt_pk_bf16_f32 v195, v195, v199\n\t"
"v_mfma_f32_16x16x32_bf16 v[16:19], a[104:107], a[144:147], v[16:19]\n\t"
"ds_read_b128 a[172:175], v224 offset:19360\n\t"
"v_cvt_pk_bf16_f32 v199, v201, v205\n\t"
"v_mfma_f32_16x16x32_bf16 v[16:19], a[108:111], a[160:163], v[16:19]\n\t"
"s_waitcnt lgkmcnt(5)\n\t"
"buffer_store_dwordx4 v[152:155], v232, s[16:19], s20 offen nt\n\t"
"v_mfma_f32_16x16x32_bf16 v[32:35], a[112:115], a[144:147], v[32:35]\n\t"
"ds_read_b128 a[152:155], v224 offset:17056\n\t"
"v_cvt_pk_bf16_f32 v198, v200, v204\n\t"
"v_mfma_f32_16x16x32_bf16 v[32:35], a[116:119], a[160:163], v[32:35]\n\t"
"ds_read_b128 v[184:187], v231 offset:0\n\t"
"v_cvt_pk_bf16_f32 v197, v193, v197\n\t"
"v_mfma_f32_4x4x4_16b_bf16 v[208:211], a[210:211], a[184:185], 0\n\t"
"ds_write_addtid_b32 v198 offset:8320\n\t"
"v_mfma_f32_4x4x4_16b_bf16 v[212:215], a[210:211], a[186:187], 0\n\t"
"s_waitcnt lgkmcnt(4)\n\t"
"buffer_store_dwordx4 v[168:171], v233, s[16:19], s20 offen nt\n\t"
"v_mfma_f32_4x4x4_16b_bf16 v[216:219], a[210:211], a[188:189], 0\n\t"
"ds_read_b128 v[156:159], v229 offset:13312\n\t"
"v_mfma_f32_4x4x4_16b_bf16 v[220:223], a[210:211], a[190:191], 0\n\t"
"ds_read_b128 a[144:147], v224 offset:17568\n\t"
"v_cvt_pk_bf16_f32 v193, v203, v207\n\t"
"v_mfma_f32_16x16x32_bf16 v[36:39], a[112:115], a[148:151], v[36:39]\n\t"
"ds_write_addtid_b32 v196 offset:0\n\t"
"v_cvt_pk_bf16_f32 v192, v202, v206\n\t"
"v_mfma_f32_16x16x32_bf16 v[36:39], a[116:119], a[164:167], v[36:39]\n\t"
"buffer_load_dwordx4 a[84:87], off, s[92:95], s64 offset:1024 sc1\n\t"
"v_mfma_f32_16x16x32_bf16 v[20:23], a[104:107], a[148:151], v[20:23]\n\t"
"ds_write_addtid_b32 v199 offset:9344\n\t"
"v_cvt_pk_bf16_f32 v212, v208, v212\n\t"
"v_mfma_f32_16x16x32_bf16 v[20:23], a[108:111], a[164:167], v[20:23]\n\t"
"ds_read_b128 v[172:175], v230 offset:13312\n\t"
"v_cvt_pk_bf16_f32 v210, v210, v214\n\t"
"v_mfma_f32_16x16x32_bf16 v[4:7], a[96:99], a[148:151], v[4:7]\n\t"
"v_cvt_pk_bf16_f32 v208, v218, v222\n\t"
"v_cvt_pk_bf16_f32 v213, v209, v213\n\t"
"v_mfma_f32_16x16x32_bf16 v[4:7], a[100:103], a[164:167], v[4:7]\n\t"
"s_waitcnt lgkmcnt(6)\n\t"
"buffer_store_dwordx4 v[184:187], v234, s[16:19], s20 offen nt\n\t"
"v_cvt_pk_bf16_f32 v211, v211, v215\n\t"
"v_mfma_f32_4x4x4_16b_bf16 v[200:203], a[210:211], a[192:193], 0\n\t"
"ds_read_b128 a[148:151], v224 offset:17824\n\t"
"v_cvt_pk_bf16_f32 v215, v217, v221\n\t"
"v_mfma_f32_4x4x4_16b_bf16 v[204:207], a[210:211], a[194:195], 0\n\t"
"ds_write_addtid_b32 v197 offset:1024\n\t"
"v_cvt_pk_bf16_f32 v214, v216, v220\n\t"
"v_mfma_f32_4x4x4_16b_bf16 v[196:199], a[210:211], a[196:197], 0\n\t"
"v_cvt_pk_bf16_f32 v209, v219, v223\n\t"
"v_mfma_f32_4x4x4_16b_bf16 v[216:219], a[210:211], a[198:199], 0\n\t"
"buffer_load_dwordx4 a[80:83], off, s[92:95], s64 offset:0 sc1\n\t"
"v_mfma_f32_16x16x32_bf16 v[8:11], a[96:99], a[152:155], v[8:11]\n\t"
"s_nop 0\n\t"
"ds_read_b128 a[160:163], v224 offset:19616\n\t"
"ds_write_addtid_b32 v215 offset:13472\n\t"
"v_mfma_f32_16x16x32_bf16 v[8:11], a[100:103], a[168:171], v[8:11]\n\t"
"s_nop 0\n\t"
"ds_write_addtid_b32 v212 offset:4128\n\t"
"ds_read_b64_tr_b16 a[206:207], v249 offset:23552\n\t"
"v_mfma_f32_16x16x32_bf16 v[24:27], a[104:107], a[152:155], v[24:27]\n\t"
"v_cvt_pk_bf16_f32 v204, v200, v204\n\t"
"v_cvt_pk_bf16_f32 v202, v202, v206\n\t"
"v_mfma_f32_16x16x32_bf16 v[24:27], a[108:111], a[168:171], v[24:27]\n\t"
"s_waitcnt lgkmcnt(6)\n\t"
"buffer_store_dwordx4 v[156:159], v232, s[16:19], s25 offen nt\n\t"
"v_cvt_pk_bf16_f32 v203, v203, v207\n\t"
"v_mfma_f32_16x16x32_bf16 v[40:43], a[112:115], a[152:155], v[40:43]\n\t"
"ds_read_b128 v[188:191], v231 offset:13312\n\t"
"v_cvt_pk_bf16_f32 v206, v196, v216\n\t"
"v_mfma_f32_16x16x32_bf16 v[40:43], a[116:119], a[168:171], v[40:43]\n\t"
"ds_read_b128 a[164:167], v224 offset:19872\n\t"
"v_cvt_pk_bf16_f32 v207, v197, v217\n\t"
"v_mfma_f32_16x16x32_bf16 v[44:47], a[112:115], a[156:159], v[44:47]\n\t"
"v_cvt_pk_bf16_f32 v200, v198, v218\n\t"
"v_cvt_pk_bf16_f32 v205, v201, v205\n\t"
"v_mfma_f32_16x16x32_bf16 v[44:47], a[116:119], a[172:175], v[44:47]\n\t"
"s_waitcnt lgkmcnt(2)\n\t"
"ds_write_addtid_b32 v213 offset:5152\n\t"
"buffer_load_dwordx4 a[72:75], off, s[96:99], s63 offset:2048 sc1\n\t"
"v_mfma_f32_4x4x4_16b_bf16 v[220:223], a[210:211], a[200:201], 0\n\t"
"ds_write_addtid_b32 v214 offset:12448\n\t"
"v_mfma_f32_4x4x4_16b_bf16 v[212:215], a[210:211], a[202:203], 0\n\t"
"v_cvt_pk_bf16_f32 v201, v199, v219\n\t"
"v_mfma_f32_4x4x4_16b_bf16 v[196:199], a[210:211], a[204:205], 0\n\t"
"buffer_store_dwordx4 v[172:175], v233, s[16:19], s25 offen nt\n\t"
"v_mfma_f32_4x4x4_16b_bf16 v[216:219], a[210:211], a[206:207], 0\n\t"
"s_nop 0\n\t"
"ds_write_addtid_b32 v205 offset:3072\n\t"
"ds_write_addtid_b32 v207 offset:11392\n\t"
"v_mfma_f32_16x16x32_bf16 v[28:31], a[104:107], a[156:159], v[28:31]\n\t"
"s_nop 0\n\t"
"ds_write_addtid_b32 v204 offset:2048\n\t"
"ds_write_addtid_b32 v206 offset:10368\n\t"
"v_mfma_f32_16x16x32_bf16 v[28:31], a[108:111], a[172:175], v[28:31]\n\t"
"s_waitcnt lgkmcnt(7)\n\t"
"buffer_store_dwordx4 v[188:191], v234, s[16:19], s25 offen nt\n\t"
"v_mfma_f32_16x16x32_bf16 v[12:15], a[96:99], a[156:159], v[12:15]\n\t"
"v_cvt_pk_bf16_f32 v222, v222, v214\n\t"
"v_cvt_pk_bf16_f32 v213, v221, v213\n\t"
"v_mfma_f32_16x16x32_bf16 v[12:15], a[100:103], a[172:175], v[12:15]\n\t"
"v_cvt_pk_bf16_f32 v212, v220, v212\n\t"
"v_cvt_pk_bf16_f32 v223, v223, v215\n\t"
"v_mfma_f32_16x16x32_bf16 v[96:99], a[120:123], a[144:147], v[96:99]\n\t"
"ds_write_addtid_b32 v213 offset:7200\n\t"
"v_cvt_pk_bf16_f32 v214, v196, v216\n\t"
"v_mfma_f32_16x16x32_bf16 v[96:99], a[124:127], a[160:163], v[96:99]\n\t"
"ds_write_addtid_b32 v214 offset:14496\n\t"
"v_cvt_pk_bf16_f32 v215, v197, v217\n\t"
"v_mfma_f32_16x16x32_bf16 v[112:115], a[128:131], a[144:147], v[112:115]\n\t"
"s_nop 0\n\t"
"ds_write_addtid_b32 v212 offset:6176\n\t"
"ds_write_addtid_b32 v215 offset:15520\n\t"
"v_mfma_f32_16x16x32_bf16 v[112:115], a[132:135], a[160:163], v[112:115]\n\t"
"buffer_load_dwordx4 a[76:79], off, s[96:99], s63 offset:3072 sc1\n\t"
"v_mfma_f32_16x16x32_bf16 v[128:131], a[136:139], a[144:147], v[128:131]\n\t"
"ds_read_b128 a[152:155], v224 offset:18080\n\t"
"v_cvt_pk_bf16_f32 v221, v199, v219\n\t"
"v_mfma_f32_16x16x32_bf16 v[128:131], a[140:143], a[160:163], v[128:131]\n\t"
"s_waitcnt lgkmcnt(7)\n\t"
"ds_read_b128 a[172:175], v224 offset:20384\n\t"
"v_mfma_f32_16x16x32_bf16 v[132:135], a[136:139], a[148:151], v[132:135]\n\t"
"s_nop 0\n\t"
"ds_read_b128 a[168:171], v224 offset:20128\n\t"
"ds_read_b128 a[156:159], v224 offset:18336\n\t"
"v_mfma_f32_16x16x32_bf16 v[132:135], a[140:143], a[164:167], v[132:135]\n\t"
"buffer_load_dwordx4 a[112:115], off, s[92:95], s47 offset:0 sc1\n\t"
"v_mfma_f32_16x16x32_bf16 v[116:119], a[128:131], a[148:151], v[116:119]\n\t"
"v_cvt_pk_bf16_f32 v220, v198, v218\n\t"
"v_mfma_f32_16x16x32_bf16 v[116:119], a[132:135], a[164:167], v[116:119]\n\t"
"buffer_load_dwordx4 a[116:119], off, s[92:95], s47 offset:1024 sc1\n\t"
"v_mfma_f32_16x16x32_bf16 v[100:103], a[120:123], a[148:151], v[100:103]\n\t"
"s_waitcnt lgkmcnt(0)\n\t"
"v_accvgpr_read_b32 v224, a213\n\t"
"v_mfma_f32_16x16x32_bf16 v[100:103], a[124:127], a[164:167], v[100:103]\n\t"
"buffer_load_dwordx4 a[108:111], off, s[96:99], s46 offset:3072 sc1\n\t"
"s_barrier\n\t"
"s_add_u32 s71, s71, 1\n\t"
"v_mov_b32 v215, s71\n\t"
"s_mov_b64 exec, 1\n\t"
"buffer_store_dword v215, off, s[8:11], s70 sc1\n\t"
"s_mov_b64 exec, -1\n\t"
"s_sub_u32 s21, s21, 1\n\t"
"s_xor_b64 s[84:85], s[84:85], s[88:89]\n\t"
"s_add_u32 s28, s84, 4096\n\t"
"s_addc_u32 s29, s85, 0\n\t"
"s_branch Lunit_%=\n\t"
"Lfb_%=:\n\t"
"v_mbcnt_lo_u32_b32 v162, -1, 0\n\t"
"v_mbcnt_hi_u32_b32 v162, -1, v162\n\t"
"v_bfe_u32 v161, v162, 1, 2\n\t"
"v_lshlrev_b32 v163, 4, v162\n\t"
"v_and_or_b32 v161, v163, 16, v161\n\t"
"v_mul_u32_u24 v161, 0xc0, v161\n\t"
"v_lshrrev_b32 v163, 5, v162\n\t"
"v_mul_u32_u24 v163, 12, v163\n\t"
"v_bfe_u32 v164, v162, 3, 2\n\t"
"v_add3_u32 v161, v161, v163, v164\n\t"
"v_lshlrev_b32 v161, 1, v161\n\t"
"v_lshlrev_b32 v162, 1, v162\n\t"
"v_mov_b32 v152, 0\n\t"
"v_mov_b32 v153, 0\n\t"
"v_mov_b32 v154, 0\n\t"
"v_mov_b32 v155, 0\n\t"
"v_mov_b32 v156, 0\n\t"
"v_mov_b32 v157, 0\n\t"
"v_mov_b32 v158, 0\n\t"
"v_mov_b32 v159, 0\n\t"
"v_mov_b32 v160, 0\n\t"
"s_mov_b32 s67, 0\n\t"
"Lfb0_%=:\n\t"
"s_mov_b32 s69, 0\n\t"
"Lfb1_%=:\n\t"
"s_lshr_b32 s60, s69, 5\n\t"
"s_mul_i32 s56, s60, 12288\n\t"
"s_bfe_u32 s61, s69, 0x20001\n\t"
"s_mul_i32 s61, s61, 1536\n\t"
"s_add_u32 s56, s56, s61\n\t"
"s_bitcmp1_b32 s69, 0\n\t"
"s_cselect_b32 s61, 48, 0\n\t"
"s_add_u32 s56, s56, s61\n\t"
"s_bfe_u32 s62, s69, 0x20003\n\t"
"s_mul_i32 s61, s62, 96\n\t"
"s_add_u32 s56, s56, s61\n\t"
"s_lshl3_add_u32 s56, s67, s56\n\t"
"s_lshr_b32 s60, s69, 5\n\t"
"s_bfe_u32 s62, s69, 0x20003\n\t"
"s_and_b32 s61, s60, 1\n\t"
"s_lshl1_add_u32 s61, s67, s61\n\t"
"s_mul_i32 s62, s62, 288\n\t"
"s_add_u32 s61, s61, s62\n\t"
"s_lshr_b32 s60, s60, 1\n\t"
"s_mul_i32 s62, s60, 96\n\t"
"s_add_u32 s61, s61, s62\n\t"
"s_lshl_b32 s57, s61, 10\n\t"
"s_and_b32 s61, s69, 7\n\t"
"s_lshl_b32 s61, s61, 7\n\t"
"s_add_u32 s57, s57, s61\n\t"
"s_add_u32 s60, s56, 0\n\t"
"buffer_load_short_d16_hi v152, v161, s[52:55], s60 offen\n\t"
"s_add_u32 s60, s56, 73728\n\t"
"buffer_load_short_d16_hi v153, v161, s[52:55], s60 offen\n\t"
"s_add_u32 s60, s56, 147456\n\t"
"buffer_load_short_d16_hi v154, v161, s[52:55], s60 offen\n\t"
"s_add_u32 s60, s56, 221184\n\t"
"buffer_load_short_d16_hi v155, v161, s[52:55], s60 offen\n\t"
"s_add_u32 s60, s56, 294912\n\t"
"buffer_load_short_d16_hi v156, v161, s[52:55], s60 offen\n\t"
"s_add_u32 s60, s56, 368640\n\t"
"buffer_load_short_d16_hi v157, v161, s[52:55], s60 offen\n\t"
"s_add_u32 s60, s56, 442368\n\t"
"buffer_load_short_d16_hi v158, v161, s[52:55], s60 offen\n\t"
"s_add_u32 s60, s56, 516096\n\t"
"buffer_load_short_d16_hi v159, v161, s[52:55], s60 offen\n\t"
"s_add_u32 s60, s56, 589824\n\t"
"buffer_load_short_d16_hi v160, v161, s[52:55], s60 offen\n\t"
"s_waitcnt vmcnt(2)\n\t"
"v_add_f32 v168, v152, v158\n\t"
"s_waitcnt vmcnt(1)\n\t"
"v_add_f32 v169, v153, v159\n\t"
"s_waitcnt vmcnt(0)\n\t"
"v_add_f32 v170, v154, v160\n\t"
"v_add_f32 v171, v168, v155\n\t"
"v_sub_f32 v174, v168, v155\n\t"
"v_add_f32 v172, v169, v156\n\t"
"v_sub_f32 v175, v169, v156\n\t"
"v_add_f32 v173, v170, v157\n\t"
"v_sub_f32 v176, v170, v157\n\t"
"v_add_f32 v177, v152, v154\n\t"
"v_add_f32 v178, v171, v173\n\t"
"v_add_f32 v179, v174, v176\n\t"
"v_add_f32 v180, v158, v160\n\t"
"v_add_f32 v182, v177, v153\n\t"
"v_sub_f32 v183, v177, v153\n\t"
"v_add_f32 v184, v178, v172\n\t"
"v_sub_f32 v185, v178, v172\n\t"
"v_add_f32 v186, v179, v175\n\t"
"v_sub_f32 v187, v179, v175\n\t"
"v_add_f32 v188, v180, v159\n\t"
"v_sub_f32 v189, v180, v159\n\t"
"v_mul_f32 v191, 0.5, v182\n\t"
"v_mul_f32 v192, 0.5, v183\n\t"
"v_mul_f32 v194, 0.5, v171\n\t"
"v_mul_f32 v195, 0x3e800000, v184\n\t"
"v_mul_f32 v196, 0x3e800000, v185\n\t"
"v_mul_f32 v197, -0.5, v173\n\t"
"v_mul_f32 v199, 0.5, v186\n\t"
"v_mul_f32 v200, 0.5, v187\n\t"
"v_mul_f32 v203, -0.5, v188\n\t"
"v_mul_f32 v204, -0.5, v189\n\t"
"v_cvt_pk_bf16_f32 v206, v152, v191\n\t"
"v_cvt_pk_bf16_f32 v207, v192, -v154\n\t"
"v_cvt_pk_bf16_f32 v208, v194, v195\n\t"
"v_cvt_pk_bf16_f32 v209, v196, v197\n\t"
"v_cvt_pk_bf16_f32 v210, v174, v199\n\t"
"v_cvt_pk_bf16_f32 v211, v200, -v176\n\t"
"v_cvt_pk_bf16_f32 v212, -v158, v203\n\t"
"v_cvt_pk_bf16_f32 v213, v204, v160\n\t"
"s_add_u32 s60, s57, 0\n\t"
"buffer_store_short v206, v162, s[8:11], s60 offen sc1\n\t"
"s_add_u32 s60, s57, 6144\n\t"
"buffer_store_short_d16_hi v206, v162, s[8:11], s60 offen sc1\n\t"
"s_add_u32 s60, s57, 12288\n\t"
"buffer_store_short v207, v162, s[8:11], s60 offen sc1\n\t"
"s_add_u32 s60, s57, 18432\n\t"
"buffer_store_short_d16_hi v207, v162, s[8:11], s60 offen sc1\n\t"
"s_add_u32 s60, s57, 24576\n\t"
"buffer_store_short v208, v162, s[8:11], s60 offen sc1\n\t"
"s_add_u32 s60, s57, 30720\n\t"
"buffer_store_short_d16_hi v208, v162, s[8:11], s60 offen sc1\n\t"
"s_add_u32 s60, s57, 36864\n\t"
"buffer_store_short v209, v162, s[8:11], s60 offen sc1\n\t"
"s_add_u32 s60, s57, 43008\n\t"
"buffer_store_short_d16_hi v209, v162, s[8:11], s60 offen sc1\n\t"
"s_add_u32 s60, s57, 49152\n\t"
"buffer_store_short v210, v162, s[8:11], s60 offen sc1\n\t"
"s_add_u32 s60, s57, 55296\n\t"
"buffer_store_short_d16_hi v210, v162, s[8:11], s60 offen sc1\n\t"
"s_add_u32 s60, s57, 61440\n\t"
"buffer_store_short v211, v162, s[8:11], s60 offen sc1\n\t"
"s_add_u32 s60, s57, 67584\n\t"
"buffer_store_short_d16_hi v211, v162, s[8:11], s60 offen sc1\n\t"
"s_add_u32 s60, s57, 73728\n\t"
"buffer_store_short v212, v162, s[8:11], s60 offen sc1\n\t"
"s_add_u32 s60, s57, 79872\n\t"
"buffer_store_short_d16_hi v212, v162, s[8:11], s60 offen sc1\n\t"
"s_add_u32 s60, s57, 86016\n\t"
"buffer_store_short v213, v162, s[8:11], s60 offen sc1\n\t"
"s_add_u32 s60, s57, 92160\n\t"
"buffer_store_short_d16_hi v213, v162, s[8:11], s60 offen sc1\n\t"
"s_waitcnt vmcnt(0)\n\t"
"s_add_u32 s69, s69, 1\n\t"
"s_cmp_lt_u32 s69, 192\n\t"
"s_cbranch_scc1 Lfb1_%=\n\t"
"s_add_u32 s67, s67, 1\n\t"
"s_cmp_lt_u32 s67, 3\n\t"
"s_cbranch_scc1 Lfb0_%=\n\t"
"s_branch Lgo_%=\n\t"
"Ltail_%=:\n\t"
"v_mfma_f32_16x16x32_bf16 v[104:107], a[72:75], a[152:155], v[104:107]\n\t"
"s_nop 0\n\t"
"ds_read_b128 a[144:147], v224 offset:17568\n\t"
"ds_read_b128 a[160:163], v224 offset:19616\n\t"
"v_mfma_f32_16x16x32_bf16 v[104:107], a[76:79], a[168:171], v[104:107]\n\t"
"buffer_load_dwordx4 a[20:23], off, s[28:31], s69 offset:1024 sc1\n\t"
"v_mfma_f32_16x16x32_bf16 v[120:123], a[80:83], a[152:155], v[120:123]\n\t"
"ds_read_b128 a[148:151], v224 offset:17824\n\t"
"v_add_u32 v248, 33280, v226\n\t"
"v_mfma_f32_16x16x32_bf16 v[120:123], a[84:87], a[168:171], v[120:123]\n\t"
"ds_read_b128 a[164:167], v224 offset:19872\n\t"
"v_xor_b32 v249, 32, v248\n\t"
"v_mfma_f32_16x16x32_bf16 v[136:139], a[88:91], a[152:155], v[136:139]\n\t"
"ds_read_b128 a[152:155], v224 offset:18080\n\t"
"v_mfma_f32_16x16x32_bf16 v[136:139], a[92:95], a[168:171], v[136:139]\n\t"
"buffer_load_dwordx4 a[12:15], off, s[84:87], s69 offset:3072 sc1\n\t"
"v_mfma_f32_16x16x32_bf16 v[140:143], a[88:91], a[156:159], v[140:143]\n\t"
"ds_read_b128 v[8:11], v229 offset:20480\n\t"
"v_mfma_f32_16x16x32_bf16 v[140:143], a[92:95], a[172:175], v[140:143]\n\t"
"buffer_load_dwordx4 a[8:11], off, s[84:87], s69 offset:2048 sc1\n\t"
"v_mfma_f32_16x16x32_bf16 v[124:127], a[80:83], a[156:159], v[124:127]\n\t"
"ds_read_b128 a[168:171], v224 offset:20128\n\t"
"v_xor_b32 v250, 64, v248\n\t"
"v_mfma_f32_16x16x32_bf16 v[124:127], a[84:87], a[172:175], v[124:127]\n\t"
"buffer_load_dwordx4 a[0:3], off, s[84:87], s69 offset:0 sc1\n\t"
"v_mfma_f32_16x16x32_bf16 v[108:111], a[72:75], a[156:159], v[108:111]\n\t"
"ds_read_b128 a[156:159], v224 offset:18336\n\t"
"v_xor_b32 v251, 96, v248\n\t"
"v_mfma_f32_16x16x32_bf16 v[108:111], a[76:79], a[172:175], v[108:111]\n\t"
"buffer_load_dwordx4 a[4:7], off, s[84:87], s69 offset:1024 sc1\n\t"
"v_mfma_f32_16x16x32_bf16 v[192:195], a[224:227], a[176:179], 0\n\t"
"s_nop 0\n\t"
"ds_read_b64_tr_b16 a[178:179], v248 offset:20864\n\t"
"ds_read_b64_tr_b16 a[176:177], v248 offset:20736\n\t"
"v_mfma_f32_16x16x32_bf16 v[196:199], a[224:227], a[180:183], 0\n\t"
"ds_read_b64_tr_b16 a[180:181], v249 offset:20992\n\t"
"v_mfma_f32_16x16x32_bf16 v[200:203], a[224:227], a[184:187], 0\n\t"
"s_nop 0\n\t"
"ds_read_b64_tr_b16 a[186:187], v249 offset:20864\n\t"
"ds_read_b64_tr_b16 a[184:185], v249 offset:20736\n\t"
"v_mfma_f32_16x16x32_bf16 v[204:207], a[224:227], a[188:191], 0\n\t"
"s_waitcnt lgkmcnt(8)\n\t"
"s_waitcnt lgkmcnt(7)\n\t"
"buffer_store_dwordx4 v[8:11], v232, s[16:19], s24 offen nt\n\t"
"v_mfma_f32_16x16x32_bf16 v[208:211], a[224:227], a[192:195], 0\n\t"
"ds_read_b128 v[24:27], v230 offset:20480\n\t"
"v_mfma_f32_16x16x32_bf16 v[212:215], a[224:227], a[196:199], 0\n\t"
"ds_read_b64_tr_b16 a[188:189], v248 offset:20992\n\t"
"v_mfma_f32_16x16x32_bf16 v[216:219], a[224:227], a[200:203], 0\n\t"
"s_nop 0\n\t"
"ds_read_b64_tr_b16 a[194:195], v250 offset:20864\n\t"
"ds_read_b64_tr_b16 a[192:193], v250 offset:20736\n\t"
"v_mfma_f32_16x16x32_bf16 v[220:223], a[224:227], a[204:207], 0\n\t"
"s_waitcnt vmcnt(18) lgkmcnt(10)\n\t"
"buffer_load_dwordx4 a[40:43], off, s[28:31], s54 offset:0 sc1\n\t"
"v_mfma_f32_16x16x32_bf16 v[144:147], a[96:99], a[144:147], v[144:147]\n\t"
"ds_read_b64_tr_b16 a[196:197], v251 offset:20992\n\t"
"v_cvt_pk_bf16_f32 v195, v195, v199\n\t"
"v_mfma_f32_16x16x32_bf16 v[144:147], a[100:103], a[160:163], v[144:147]\n\t"
"ds_read_b128 a[172:175], v224 offset:20384\n\t"
"v_cvt_pk_bf16_f32 v194, v194, v198\n\t"
"v_mfma_f32_16x16x32_bf16 v[160:163], a[104:107], a[144:147], v[160:163]\n\t"
"ds_read_b64_tr_b16 a[202:203], v251 offset:20864\n\t"
"v_cvt_pk_bf16_f32 v200, v200, v204\n\t"
"v_mfma_f32_16x16x32_bf16 v[160:163], a[108:111], a[160:163], v[160:163]\n\t"
"buffer_load_dwordx4 a[44:47], off, s[28:31], s54 offset:1024 sc1\n\t"
"v_mfma_f32_16x16x32_bf16 v[176:179], a[112:115], a[144:147], v[176:179]\n\t"
"ds_read_b128 v[12:15], v229 offset:33792\n\t"
"v_cvt_pk_bf16_f32 v209, v209, v213\n\t"
"v_mfma_f32_16x16x32_bf16 v[176:179], a[116:119], a[160:163], v[176:179]\n\t"
"ds_read_b128 a[144:147], v224 offset:16544\n\t"
"v_cvt_pk_bf16_f32 v193, v193, v197\n\t"
"v_mfma_f32_16x16x32_bf16 v[180:183], a[112:115], a[148:151], v[180:183]\n\t"
"ds_read_b128 a[160:163], v224 offset:18592\n\t"
"v_cvt_pk_bf16_f32 v216, v216, v220\n\t"
"v_mfma_f32_16x16x32_bf16 v[180:183], a[116:119], a[164:167], v[180:183]\n\t"
"s_waitcnt lgkmcnt(8)\n\t"
"buffer_store_dwordx4 v[24:27], v233, s[16:19], s24 offen nt\n\t"
"v_mfma_f32_16x16x32_bf16 v[164:167], a[104:107], a[148:151], v[164:167]\n\t"
"ds_write_addtid_b32 v200 offset:2048\n\t"
"v_cvt_pk_bf16_f32 v217, v217, v221\n\t"
"v_mfma_f32_16x16x32_bf16 v[164:167], a[108:111], a[164:167], v[164:167]\n\t"
"s_nop 0\n\t"
"ds_write_addtid_b32 v193 offset:8320\n\t"
"ds_write_addtid_b32 v209 offset:9344\n\t"
"v_mfma_f32_16x16x32_bf16 v[148:151], a[96:99], a[148:151], v[148:151]\n\t"
"ds_read_b128 v[40:43], v231 offset:20480\n\t"
"v_cvt_pk_bf16_f32 v201, v201, v205\n\t"
"v_mfma_f32_16x16x32_bf16 v[148:151], a[100:103], a[164:167], v[148:151]\n\t"
"buffer_load_dwordx4 a[36:39], off, s[84:87], s54 offset:3072 sc1\n\t"
"v_mfma_f32_16x16x32_bf16 v[152:155], a[96:99], a[152:155], v[152:155]\n\t"
"ds_read_b128 v[28:31], v230 offset:33792\n\t"
"v_cvt_pk_bf16_f32 v192, v192, v196\n\t"
"v_mfma_f32_16x16x32_bf16 v[152:155], a[100:103], a[168:171], v[152:155]\n\t"
"ds_read_b128 a[148:151], v224 offset:16800\n\t"
"v_cvt_pk_bf16_f32 v210, v210, v214\n\t"
"v_mfma_f32_16x16x32_bf16 v[168:171], a[104:107], a[152:155], v[168:171]\n\t"
"ds_read_b128 a[164:167], v224 offset:18848\n\t"
"v_cvt_pk_bf16_f32 v219, v219, v223\n\t"
"v_mfma_f32_16x16x32_bf16 v[168:171], a[108:111], a[168:171], v[168:171]\n\t"
"buffer_load_dwordx4 a[32:35], off, s[84:87], s54 offset:2048 sc1\n\t"
"v_mfma_f32_16x16x32_bf16 v[184:187], a[112:115], a[152:155], v[184:187]\n\t"
"ds_read_b128 a[152:155], v224 offset:17056\n\t"
"v_cvt_pk_bf16_f32 v202, v202, v206\n\t"
"v_mfma_f32_16x16x32_bf16 v[184:187], a[116:119], a[168:171], v[184:187]\n\t"
"s_waitcnt lgkmcnt(8)\n\t"
"ds_read_b128 v[44:47], v231 offset:33792\n\t"
"v_cvt_pk_bf16_f32 v208, v208, v212\n\t"
"v_mfma_f32_16x16x32_bf16 v[188:191], a[112:115], a[156:159], v[188:191]\n\t"
"ds_write_addtid_b32 v195 offset:12448\n\t"
"v_cvt_pk_bf16_f32 v203, v203, v207\n\t"
"v_mfma_f32_16x16x32_bf16 v[188:191], a[116:119], a[172:175], v[188:191]\n\t"
"ds_write_addtid_b32 v194 offset:4128\n\t"
"v_cvt_pk_bf16_f32 v211, v211, v215\n\t"
"v_mfma_f32_16x16x32_bf16 v[172:175], a[104:107], a[156:159], v[172:175]\n\t"
"ds_write_addtid_b32 v201 offset:10368\n\t"
"v_cvt_pk_bf16_f32 v218, v218, v222\n\t"
"v_mfma_f32_16x16x32_bf16 v[172:175], a[108:111], a[172:175], v[172:175]\n\t"
"s_nop 0\n\t"
"ds_write_addtid_b32 v218 offset:7200\n\t"
"ds_write_addtid_b32 v219 offset:15520\n\t"
"v_mfma_f32_16x16x32_bf16 v[156:159], a[96:99], a[156:159], v[156:159]\n\t"
"s_nop 0\n\t"
"ds_write_addtid_b32 v202 offset:6176\n\t"
"ds_write_addtid_b32 v210 offset:5152\n\t"
"v_mfma_f32_16x16x32_bf16 v[156:159], a[100:103], a[172:175], v[156:159]\n\t"
"s_waitcnt vmcnt(12) lgkmcnt(10)\n\t"
"buffer_store_dwordx4 v[40:43], v234, s[16:19], s24 offen nt\n\t"
"v_mfma_f32_16x16x32_bf16 v[96:99], a[120:123], a[144:147], v[96:99]\n\t"
"s_nop 0\n\t"
"ds_write_addtid_b32 v192 offset:0\n\t"
"ds_write_addtid_b32 v211 offset:13472\n\t"
"v_mfma_f32_16x16x32_bf16 v[96:99], a[124:127], a[160:163], v[96:99]\n\t"
"s_nop 0\n\t"
"ds_write_addtid_b32 v203 offset:14496\n\t"
"ds_write_addtid_b32 v216 offset:3072\n\t"
"v_mfma_f32_16x16x32_bf16 v[112:115], a[128:131], a[144:147], v[112:115]\n\t"
"s_nop 0\n\t"
"ds_write_addtid_b32 v208 offset:1024\n\t"
"ds_write_addtid_b32 v217 offset:11392\n\t"
"v_mfma_f32_16x16x32_bf16 v[112:115], a[132:135], a[160:163], v[112:115]\n\t"
"buffer_load_dwordx4 a[24:27], off, s[84:87], s54 offset:0 sc1\n\t"
"v_mfma_f32_16x16x32_bf16 v[128:131], a[136:139], a[144:147], v[128:131]\n\t"
"ds_read_b128 a[172:175], v224 offset:19360\n\t"
"v_mfma_f32_16x16x32_bf16 v[128:131], a[140:143], a[160:163], v[128:131]\n\t"
"s_waitcnt lgkmcnt(12)\n\t"
"ds_read_b128 a[168:171], v224 offset:19104\n\t"
"v_mfma_f32_16x16x32_bf16 v[132:135], a[136:139], a[148:151], v[132:135]\n\t"
"ds_read_b128 a[156:159], v224 offset:17312\n\t"
"v_mfma_f32_16x16x32_bf16 v[132:135], a[140:143], a[164:167], v[132:135]\n\t"
"buffer_load_dwordx4 a[28:31], off, s[84:87], s54 offset:1024 sc1\n\t"
"v_mfma_f32_16x16x32_bf16 v[116:119], a[128:131], a[148:151], v[116:119]\n\t"
"s_nop 0\n\t"
"ds_read_b64_tr_b16 a[200:201], v251 offset:20736\n\t"
"ds_read_b64_tr_b16 a[204:205], v250 offset:20992\n\t"
"v_mfma_f32_16x16x32_bf16 v[116:119], a[132:135], a[164:167], v[116:119]\n\t"
"v_mfma_f32_16x16x32_bf16 v[100:103], a[120:123], a[148:151], v[100:103]\n\t"
"buffer_load_dwordx4 a[64:67], off, s[28:31], s57 offset:0 sc1\n\t"
"v_mfma_f32_16x16x32_bf16 v[100:103], a[124:127], a[164:167], v[100:103]\n\t"
"s_waitcnt lgkmcnt(0)\n\t"
"s_barrier\n\t"
"v_mfma_f32_16x16x32_bf16 v[104:107], a[120:123], a[152:155], v[104:107]\n\t"
"s_nop 0\n\t"
"ds_read_b128 a[144:147], v224 offset:0\n\t"
"ds_read_b128 a[148:151], v224 offset:256\n\t"
"v_mfma_f32_16x16x32_bf16 v[104:107], a[124:127], a[168:171], v[104:107]\n\t"
"buffer_store_dwordx4 v[12:15], v232, s[16:19], s27 offen nt\n\t"
"v_mfma_f32_16x16x32_bf16 v[120:123], a[128:131], a[152:155], v[120:123]\n\t"
"ds_read_b128 a[160:163], v224 offset:2048\n\t"
"v_mfma_f32_16x16x32_bf16 v[120:123], a[132:135], a[168:171], v[120:123]\n\t"
"ds_read_b128 a[164:167], v224 offset:2304\n\t"
"v_mfma_f32_16x16x32_bf16 v[136:139], a[136:139], a[152:155], v[136:139]\n\t"
"v_mfma_f32_16x16x32_bf16 v[136:139], a[140:143], a[168:171], v[136:139]\n\t"
"buffer_load_dwordx4 a[68:71], off, s[28:31], s57 offset:1024 sc1\n\t"
"v_mfma_f32_16x16x32_bf16 v[140:143], a[136:139], a[156:159], v[140:143]\n\t"
"ds_read_b128 a[152:155], v224 offset:512\n\t"
"v_mfma_f32_16x16x32_bf16 v[140:143], a[140:143], a[172:175], v[140:143]\n\t"
"ds_read_b128 a[168:171], v224 offset:2560\n\t"
"v_mfma_f32_16x16x32_bf16 v[124:127], a[128:131], a[156:159], v[124:127]\n\t"
"v_mfma_f32_16x16x32_bf16 v[124:127], a[132:135], a[172:175], v[124:127]\n\t"
"v_mfma_f32_16x16x32_bf16 v[108:111], a[120:123], a[156:159], v[108:111]\n\t"
"ds_read_b128 a[156:159], v224 offset:768\n\t"
"v_mfma_f32_16x16x32_bf16 v[108:111], a[124:127], a[172:175], v[108:111]\n\t"
"buffer_load_dwordx4 a[60:63], off, s[84:87], s57 offset:3072 sc1\n\t"
"v_mfma_f32_4x4x4_16b_bf16 v[200:203], a[220:221], a[178:179], 0\n\t"
"ds_read_b64_tr_b16 a[178:179], v248 offset:128\n\t"
"v_mfma_f32_4x4x4_16b_bf16 v[204:207], a[220:221], a[186:187], 0\n\t"
"ds_read_b64_tr_b16 a[186:187], v249 offset:128\n\t"
"v_mfma_f32_4x4x4_16b_bf16 v[216:219], a[220:221], a[194:195], 0\n\t"
"ds_read_b64_tr_b16 a[194:195], v250 offset:128\n\t"
"buffer_load_dwordx4 a[56:59], off, s[84:87], s57 offset:2048 sc1\n\t"
"v_mfma_f32_4x4x4_16b_bf16 v[220:223], a[220:221], a[202:203], 0\n\t"
"ds_read_b64_tr_b16 a[202:203], v251 offset:128\n\t"
"v_mfma_f32_4x4x4_16b_bf16 v[192:195], a[222:223], a[176:177], v[200:203]\n\t"
"ds_read_b64_tr_b16 a[176:177], v248 offset:0\n\t"
"v_mfma_f32_4x4x4_16b_bf16 v[196:199], a[222:223], a[184:185], v[204:207]\n\t"
"ds_read_b64_tr_b16 a[184:185], v249 offset:0\n\t"
"buffer_load_dwordx4 a[48:51], off, s[84:87], s57 offset:0 sc1\n\t"
"v_mfma_f32_4x4x4_16b_bf16 v[200:203], a[222:223], a[180:181], v[200:203]\n\t"
".p2align 3\n\t"
"ds_read_b64_tr_b16 a[180:181], v249 offset:256\n\t"
"v_pk_add_f32 v[96:97], v[96:97], v[144:145]\n\t"
"v_pk_add_f32 v[98:99], v[98:99], v[146:147]\n\t"
"v_pk_fma_f32 v[144:145], v[144:145], s[38:39], v[96:97]\n\t"
"v_pk_fma_f32 v[146:147], v[146:147], s[38:39], v[98:99]\n\t"
"v_pk_add_f32 v[112:113], v[112:113], v[160:161]\n\t"
"v_pk_add_f32 v[114:115], v[114:115], v[162:163]\n\t"
"v_pk_fma_f32 v[160:161], v[160:161], s[38:39], v[112:113]\n\t"
"v_pk_fma_f32 v[162:163], v[162:163], s[38:39], v[114:115]\n\t"
"v_pk_add_f32 v[128:129], v[128:129], v[176:177]\n\t"
"v_pk_add_f32 v[130:131], v[130:131], v[178:179]\n\t"
"v_pk_fma_f32 v[176:177], v[176:177], s[38:39], v[128:129]\n\t"
"v_pk_fma_f32 v[178:179], v[178:179], s[38:39], v[130:131]\n\t"
"v_pk_add_f32 v[132:133], v[132:133], v[180:181]\n\t"
"v_pk_add_f32 v[134:135], v[134:135], v[182:183]\n\t"
"v_pk_fma_f32 v[180:181], v[180:181], s[38:39], v[132:133]\n\t"
"v_pk_fma_f32 v[182:183], v[182:183], s[38:39], v[134:135]\n\t"
"v_pk_add_f32 v[116:117], v[116:117], v[164:165]\n\t"
"v_pk_add_f32 v[118:119], v[118:119], v[166:167]\n\t"
"v_pk_fma_f32 v[164:165], v[164:165], s[38:39], v[116:117]\n\t"
"v_pk_fma_f32 v[166:167], v[166:167], s[38:39], v[118:119]\n\t"
"v_pk_add_f32 v[100:101], v[100:101], v[148:149]\n\t"
"v_pk_add_f32 v[102:103], v[102:103], v[150:151]\n\t"
"v_pk_fma_f32 v[148:149], v[148:149], s[38:39], v[100:101]\n\t"
"v_pk_fma_f32 v[150:151], v[150:151], s[38:39], v[102:103]\n\t"
"v_pk_add_f32 v[104:105], v[104:105], v[152:153]\n\t"
"v_pk_add_f32 v[106:107], v[106:107], v[154:155]\n\t"
"v_pk_fma_f32 v[152:153], v[152:153], s[38:39], v[104:105]\n\t"
"v_pk_fma_f32 v[154:155], v[154:155], s[38:39], v[106:107]\n\t"
"v_pk_add_f32 v[120:121], v[120:121], v[168:169]\n\t"
"v_pk_add_f32 v[122:123], v[122:123], v[170:171]\n\t"
"v_pk_fma_f32 v[168:169], v[168:169], s[38:39], v[120:121]\n\t"
"v_pk_fma_f32 v[170:171], v[170:171], s[38:39], v[122:123]\n\t"
"v_pk_add_f32 v[136:137], v[136:137], v[184:185]\n\t"
"v_pk_add_f32 v[138:139], v[138:139], v[186:187]\n\t"
"v_pk_fma_f32 v[184:185], v[184:185], s[38:39], v[136:137]\n\t"
"v_pk_fma_f32 v[186:187], v[186:187], s[38:39], v[138:139]\n\t"
"v_pk_add_f32 v[140:141], v[140:141], v[188:189]\n\t"
"v_pk_add_f32 v[142:143], v[142:143], v[190:191]\n\t"
"v_pk_fma_f32 v[188:189], v[188:189], s[38:39], v[140:141]\n\t"
"v_pk_fma_f32 v[190:191], v[190:191], s[38:39], v[142:143]\n\t"
"v_pk_add_f32 v[124:125], v[124:125], v[172:173]\n\t"
"v_pk_add_f32 v[126:127], v[126:127], v[174:175]\n\t"
"v_pk_fma_f32 v[172:173], v[172:173], s[38:39], v[124:125]\n\t"
"v_pk_fma_f32 v[174:175], v[174:175], s[38:39], v[126:127]\n\t"
"v_pk_add_f32 v[108:109], v[108:109], v[156:157]\n\t"
"v_pk_add_f32 v[110:111], v[110:111], v[158:159]\n\t"
"v_pk_fma_f32 v[156:157], v[156:157], s[38:39], v[108:109]\n\t"
"v_pk_fma_f32 v[158:159], v[158:159], s[38:39], v[110:111]\n\t"
"v_cvt_pk_bf16_f32 v100, v100, v101\n\t"
"v_mfma_f32_4x4x4_16b_bf16 v[204:207], a[222:223], a[188:189], v[204:207]\n\t"
"ds_read_b128 a[172:175], v224 offset:2816\n\t"
"v_cvt_pk_bf16_f32 v96, v96, v97\n\t"
"v_mfma_f32_4x4x4_16b_bf16 v[208:211], a[222:223], a[192:193], v[216:219]\n\t"
"ds_read_b64_tr_b16 a[188:189], v248 offset:256\n\t"
"buffer_store_dwordx4 v[28:31], v233, s[16:19], s27 offen nt\n\t"
"v_cvt_pk_bf16_f32 v97, v98, v99\n\t"
"v_cvt_pk_bf16_f32 v101, v102, v103\n\t"
"v_cvt_pk_bf16_f32 v112, v112, v113\n\t"
"v_cvt_pk_bf16_f32 v132, v132, v133\n\t"
"v_cvt_pk_bf16_f32 v108, v108, v109\n\t"
"v_mfma_f32_4x4x4_16b_bf16 v[212:215], a[222:223], a[200:201], v[220:223]\n\t"
"v_cvt_pk_bf16_f32 v113, v114, v115\n\t"
"v_mfma_f32_4x4x4_16b_bf16 v[216:219], a[222:223], a[196:197], v[216:219]\n\t"
"ds_write2st64_b64 v228, v[96:97], v[100:101] offset0:40 offset1:53\n\t"
"v_cvt_pk_bf16_f32 v128, v128, v129\n\t"
"v_mfma_f32_4x4x4_16b_bf16 v[220:223], a[222:223], a[204:205], v[220:223]\n\t"
"s_waitcnt vmcnt(16) lgkmcnt(8)\n\t"
"buffer_load_dwordx4 a[52:55], off, s[84:87], s57 offset:1024 sc1\n\t"
"v_mfma_f32_16x16x32_bf16 v[48:51], a[0:3], a[144:147], v[48:51]\n\t"
"v_cvt_pk_bf16_f32 v116, v116, v117\n\t"
"v_cvt_pk_bf16_f32 v117, v118, v119\n\t"
"v_mfma_f32_16x16x32_bf16 v[48:51], a[4:7], a[160:163], v[48:51]\n\t"
"ds_write2st64_b64 v246, v[112:113], v[116:117] offset0:40 offset1:53\n\t"
"v_cvt_pk_bf16_f32 v129, v130, v131\n\t"
"v_mfma_f32_16x16x32_bf16 v[64:67], a[8:11], a[144:147], v[64:67]\n\t"
"v_cvt_pk_bf16_f32 v124, v124, v125\n\t"
"v_cvt_pk_bf16_f32 v140, v140, v141\n\t"
"v_mfma_f32_16x16x32_bf16 v[64:67], a[12:15], a[160:163], v[64:67]\n\t"
"buffer_load_dwordx4 a[88:91], off, s[28:31], s63 offset:0 sc1\n\t"
"v_mfma_f32_16x16x32_bf16 v[80:83], a[16:19], a[144:147], v[80:83]\n\t"
"v_cvt_pk_bf16_f32 v211, -v211, -v215\n\t"
"v_cvt_pk_bf16_f32 v133, v134, v135\n\t"
"v_mfma_f32_16x16x32_bf16 v[80:83], a[20:23], a[160:163], v[80:83]\n\t"
"s_nop 0\n\t"
"ds_read_b128 a[144:147], v224 offset:1024\n\t"
"ds_read_b128 a[160:163], v224 offset:3072\n\t"
"ds_write2st64_b64 v247, v[128:129], v[132:133] offset0:40 offset1:53\n\t"
"v_mfma_f32_16x16x32_bf16 v[84:87], a[16:19], a[148:151], v[84:87]\n\t"
"v_cvt_pk_bf16_f32 v136, v136, v137\n\t"
"v_cvt_pk_bf16_f32 v137, v138, v139\n\t"
"v_mfma_f32_16x16x32_bf16 v[84:87], a[20:23], a[164:167], v[84:87]\n\t"
"buffer_store_dwordx4 v[44:47], v234, s[16:19], s27 offen nt\n\t"
"v_mfma_f32_16x16x32_bf16 v[68:71], a[8:11], a[148:151], v[68:71]\n\t"
"v_cvt_pk_bf16_f32 v104, v104, v105\n\t"
"v_cvt_pk_bf16_f32 v141, v142, v143\n\t"
"v_mfma_f32_16x16x32_bf16 v[68:71], a[12:15], a[164:167], v[68:71]\n\t"
"ds_write_addtid_b32 v211 offset:23744\n\t"
"ds_write2st64_b64 v247, v[136:137], v[140:141] offset0:66 offset1:79\n\t"
"v_cvt_pk_bf16_f32 v125, v126, v127\n\t"
"v_mfma_f32_16x16x32_bf16 v[52:55], a[0:3], a[148:151], v[52:55]\n\t"
"v_cvt_pk_bf16_f32 v202, v202, v206\n\t"
"v_cvt_pk_bf16_f32 v109, v110, v111\n\t"
"v_mfma_f32_16x16x32_bf16 v[52:55], a[4:7], a[164:167], v[52:55]\n\t"
"buffer_load_dwordx4 a[92:95], off, s[28:31], s63 offset:1024 sc1\n\t"
"v_cvt_pk_bf16_f32 v219, v219, v223\n\t"
"v_mfma_f32_16x16x32_bf16 v[56:59], a[0:3], a[152:155], v[56:59]\n\t"
"v_cvt_pk_bf16_f32 v217, v217, v221\n\t"
"v_cvt_pk_bf16_f32 v105, v106, v107\n\t"
"v_mfma_f32_16x16x32_bf16 v[56:59], a[4:7], a[168:171], v[56:59]\n\t"
"s_nop 0\n\t"
"ds_read_b128 a[148:151], v224 offset:1280\n\t"
"ds_read_b128 a[164:167], v224 offset:3328\n\t"
"ds_write2st64_b64 v228, v[104:105], v[108:109] offset0:66 offset1:79\n\t"
"v_mfma_f32_16x16x32_bf16 v[72:75], a[8:11], a[152:155], v[72:75]\n\t"
"v_cvt_pk_bf16_f32 v216, v216, v220\n\t"
"v_cvt_pk_bf16_f32 v120, v120, v121\n\t"
"v_mfma_f32_16x16x32_bf16 v[72:75], a[12:15], a[168:171], v[72:75]\n\t"
"buffer_load_dwordx4 a[84:87], off, s[84:87], s63 offset:3072 sc1\n\t"
"v_mfma_f32_16x16x32_bf16 v[88:91], a[16:19], a[152:155], v[88:91]\n\t"
"v_cvt_pk_bf16_f32 v218, v218, v222\n\t"
"v_cvt_pk_bf16_f32 v121, v122, v123\n\t"
"v_mfma_f32_16x16x32_bf16 v[88:91], a[20:23], a[168:171], v[88:91]\n\t"
"s_waitcnt lgkmcnt(5)\n\t"
"ds_write_addtid_b32 v216 offset:26912\n\t"
"ds_write2st64_b64 v246, v[120:121], v[124:125] offset0:66 offset1:79\n\t"
"v_mfma_f32_16x16x32_bf16 v[92:95], a[16:19], a[156:159], v[92:95]\n\t"
"v_cvt_pk_bf16_f32 v200, v200, v204\n\t"
"v_cvt_pk_bf16_f32 v209, -v209, -v213\n\t"
"v_mfma_f32_16x16x32_bf16 v[92:95], a[20:23], a[172:175], v[92:95]\n\t"
"v_cvt_pk_bf16_f32 v193, -v193, -v197\n\t"
"v_cvt_pk_bf16_f32 v208, -v208, -v212\n\t"
"v_mfma_f32_16x16x32_bf16 v[76:79], a[8:11], a[156:159], v[76:79]\n\t"
"v_cvt_pk_bf16_f32 v194, -v194, -v198\n\t"
"v_cvt_pk_bf16_f32 v210, -v210, -v214\n\t"
"v_mfma_f32_16x16x32_bf16 v[76:79], a[12:15], a[172:175], v[76:79]\n\t"
"s_nop 0\n\t"
"ds_write_addtid_b32 v193 offset:20672\n\t"
"ds_write_addtid_b32 v194 offset:17568\n\t"
"ds_write_addtid_b32 v200 offset:24864\n\t"
"ds_write_addtid_b32 v202 offset:25888\n\t"
"ds_write_addtid_b32 v210 offset:19616\n\t"
"ds_write_addtid_b32 v217 offset:31040\n\t"
"v_mfma_f32_16x16x32_bf16 v[60:63], a[0:3], a[156:159], v[60:63]\n\t"
"ds_write_addtid_b32 v209 offset:22720\n\t"
"v_cvt_pk_bf16_f32 v203, v203, v207\n\t"
"v_mfma_f32_16x16x32_bf16 v[60:63], a[4:7], a[172:175], v[60:63]\n\t"
"s_waitcnt vmcnt(12) lgkmcnt(9)\n\t"
"v_cvt_pk_bf16_f32 v192, -v192, -v196\n\t"
"v_cvt_pk_bf16_f32 v201, v201, v205\n\t"
"v_mfma_f32_16x16x32_bf16 v[48:51], a[24:27], a[144:147], v[48:51]\n\t"
"ds_write_addtid_b32 v218 offset:27936\n\t"
"v_cvt_pk_bf16_f32 v195, -v195, -v199\n\t"
"v_mfma_f32_16x16x32_bf16 v[48:51], a[28:31], a[160:163], v[48:51]\n\t"
"s_nop 0\n\t"
"ds_write_addtid_b32 v201 offset:28992\n\t"
"ds_write_addtid_b32 v203 offset:30016\n\t"
"ds_write_addtid_b32 v208 offset:18592\n\t"
"ds_write_addtid_b32 v219 offset:32064\n\t"
"v_mfma_f32_16x16x32_bf16 v[64:67], a[32:35], a[144:147], v[64:67]\n\t"
"s_nop 0\n\t"
"ds_write_addtid_b32 v192 offset:16544\n\t"
"ds_write_addtid_b32 v195 offset:21696\n\t"
"v_mfma_f32_16x16x32_bf16 v[64:67], a[36:39], a[160:163], v[64:67]\n\t"
"buffer_load_dwordx4 a[80:83], off, s[84:87], s63 offset:2048 sc1\n\t"
"v_mfma_f32_16x16x32_bf16 v[80:83], a[40:43], a[144:147], v[80:83]\n\t"
"s_nop 0\n\t"
"ds_read_b128 a[168:171], v224 offset:3584\n\t"
"ds_read_b128 a[156:159], v224 offset:1792\n\t"
"v_mfma_f32_16x16x32_bf16 v[80:83], a[44:47], a[160:163], v[80:83]\n\t"
"buffer_load_dwordx4 a[72:75], off, s[84:87], s63 offset:0 sc1\n\t"
"v_mfma_f32_16x16x32_bf16 v[84:87], a[40:43], a[148:151], v[84:87]\n\t"
"s_nop 0\n\t"
"ds_read_b128 a[152:155], v224 offset:1536\n\t"
"ds_read_b128 a[172:175], v224 offset:3840\n\t"
"v_mfma_f32_16x16x32_bf16 v[84:87], a[44:47], a[164:167], v[84:87]\n\t"
"s_nop 0\n\t"
"ds_read_b64_tr_b16 a[192:193], v250 offset:0\n\t"
"ds_read_b64_tr_b16 a[200:201], v251 offset:0\n\t"
"v_mfma_f32_16x16x32_bf16 v[68:71], a[32:35], a[148:151], v[68:71]\n\t"
"s_nop 0\n\t"
"ds_read_b64_tr_b16 a[196:197], v251 offset:256\n\t"
"ds_read_b64_tr_b16 a[204:205], v250 offset:256\n\t"
"v_mfma_f32_16x16x32_bf16 v[68:71], a[36:39], a[164:167], v[68:71]\n\t"
"v_mfma_f32_16x16x32_bf16 v[52:55], a[24:27], a[148:151], v[52:55]\n\t"
"buffer_load_dwordx4 a[76:79], off, s[84:87], s63 offset:1024 sc1\n\t"
"v_mfma_f32_16x16x32_bf16 v[52:55], a[28:31], a[164:167], v[52:55]\n\t"
"s_waitcnt lgkmcnt(0)\n\t"
"s_barrier\n\t"
"v_mfma_f32_16x16x32_bf16 v[56:59], a[24:27], a[152:155], v[56:59]\n\t"
"ds_read_b128 a[144:147], v224 offset:16544\n\t"
"v_mfma_f32_16x16x32_bf16 v[56:59], a[28:31], a[168:171], v[56:59]\n\t"
"ds_read_b128 a[160:163], v224 offset:18592\n\t"
"v_mfma_f32_16x16x32_bf16 v[72:75], a[32:35], a[152:155], v[72:75]\n\t"
"ds_read_b128 a[148:151], v224 offset:16800\n\t"
"v_mfma_f32_16x16x32_bf16 v[72:75], a[36:39], a[168:171], v[72:75]\n\t"
"ds_read_b128 a[164:167], v224 offset:18848\n\t"
"v_mfma_f32_16x16x32_bf16 v[88:91], a[40:43], a[152:155], v[88:91]\n\t"
"v_mfma_f32_16x16x32_bf16 v[88:91], a[44:47], a[168:171], v[88:91]\n\t"
"buffer_load_dwordx4 a[112:115], off, s[28:31], s42 offset:0 sc1\n\t"
"v_mfma_f32_16x16x32_bf16 v[92:95], a[40:43], a[156:159], v[92:95]\n\t"
"ds_read_b128 a[168:171], v224 offset:19104\n\t"
"v_mfma_f32_16x16x32_bf16 v[92:95], a[44:47], a[172:175], v[92:95]\n\t"
"ds_read_b128 v[104:107], v229 offset:20480\n\t"
"v_mfma_f32_16x16x32_bf16 v[76:79], a[32:35], a[156:159], v[76:79]\n\t"
"v_mfma_f32_16x16x32_bf16 v[76:79], a[36:39], a[172:175], v[76:79]\n\t"
"ds_read_b128 a[152:155], v224 offset:17056\n\t"
"v_mfma_f32_16x16x32_bf16 v[60:63], a[24:27], a[156:159], v[60:63]\n\t"
"ds_read_b128 a[156:159], v224 offset:17312\n\t"
"v_mfma_f32_16x16x32_bf16 v[60:63], a[28:31], a[172:175], v[60:63]\n\t"
"buffer_load_dwordx4 a[116:119], off, s[28:31], s42 offset:1024 sc1\n\t"
"v_mfma_f32_4x4x4_16b_bf16 v[200:203], a[220:221], a[178:179], 0\n\t"
"ds_read_b64_tr_b16 a[178:179], v248 offset:41600\n\t"
"v_mfma_f32_4x4x4_16b_bf16 v[204:207], a[220:221], a[186:187], 0\n\t"
"ds_read_b64_tr_b16 a[186:187], v249 offset:41600\n\t"
"v_mfma_f32_4x4x4_16b_bf16 v[216:219], a[220:221], a[194:195], 0\n\t"
"ds_read_b128 a[172:175], v224 offset:19360\n\t"
"v_mfma_f32_4x4x4_16b_bf16 v[220:223], a[220:221], a[202:203], 0\n\t"
"s_waitcnt lgkmcnt(5)\n\t"
".p2align 3\n\t"
"buffer_store_dwordx4 v[104:107], v232, s[16:19], s20 offen nt\n\t"
"v_pk_add_f32 v[144:145], v[48:49], v[144:145] neg_lo:[0,1] neg_hi:[0,1]\n\t"
"v_pk_add_f32 v[146:147], v[50:51], v[146:147] neg_lo:[0,1] neg_hi:[0,1]\n\t"
"v_pk_fma_f32 v[48:49], v[144:145], s[90:91], v[48:49] neg_lo:[1,0,0] neg_hi:[1,0,0]\n\t"
"v_pk_fma_f32 v[50:51], v[146:147], s[90:91], v[50:51] neg_lo:[1,0,0] neg_hi:[1,0,0]\n\t"
"v_pk_add_f32 v[160:161], v[64:65], v[160:161] neg_lo:[0,1] neg_hi:[0,1]\n\t"
"v_pk_add_f32 v[162:163], v[66:67], v[162:163] neg_lo:[0,1] neg_hi:[0,1]\n\t"
"v_pk_fma_f32 v[64:65], v[160:161], s[90:91], v[64:65] neg_lo:[1,0,0] neg_hi:[1,0,0]\n\t"
"v_pk_fma_f32 v[66:67], v[162:163], s[90:91], v[66:67] neg_lo:[1,0,0] neg_hi:[1,0,0]\n\t"
"v_pk_add_f32 v[176:177], v[80:81], v[176:177] neg_lo:[0,1] neg_hi:[0,1]\n\t"
"v_pk_add_f32 v[178:179], v[82:83], v[178:179] neg_lo:[0,1] neg_hi:[0,1]\n\t"
"v_pk_fma_f32 v[80:81], v[176:177], s[90:91], v[80:81] neg_lo:[1,0,0] neg_hi:[1,0,0]\n\t"
"v_pk_fma_f32 v[82:83], v[178:179], s[90:91], v[82:83] neg_lo:[1,0,0] neg_hi:[1,0,0]\n\t"
"v_pk_add_f32 v[180:181], v[84:85], v[180:181] neg_lo:[0,1] neg_hi:[0,1]\n\t"
"v_pk_add_f32 v[182:183], v[86:87], v[182:183] neg_lo:[0,1] neg_hi:[0,1]\n\t"
"v_pk_fma_f32 v[84:85], v[180:181], s[90:91], v[84:85] neg_lo:[1,0,0] neg_hi:[1,0,0]\n\t"
"v_pk_fma_f32 v[86:87], v[182:183], s[90:91], v[86:87] neg_lo:[1,0,0] neg_hi:[1,0,0]\n\t"
"v_pk_add_f32 v[164:165], v[68:69], v[164:165] neg_lo:[0,1] neg_hi:[0,1]\n\t"
"v_pk_add_f32 v[166:167], v[70:71], v[166:167] neg_lo:[0,1] neg_hi:[0,1]\n\t"
"v_pk_fma_f32 v[68:69], v[164:165], s[90:91], v[68:69] neg_lo:[1,0,0] neg_hi:[1,0,0]\n\t"
"v_pk_fma_f32 v[70:71], v[166:167], s[90:91], v[70:71] neg_lo:[1,0,0] neg_hi:[1,0,0]\n\t"
"v_pk_add_f32 v[148:149], v[52:53], v[148:149] neg_lo:[0,1] neg_hi:[0,1]\n\t"
"v_pk_add_f32 v[150:151], v[54:55], v[150:151] neg_lo:[0,1] neg_hi:[0,1]\n\t"
"v_pk_fma_f32 v[52:53], v[148:149], s[90:91], v[52:53] neg_lo:[1,0,0] neg_hi:[1,0,0]\n\t"
"v_pk_fma_f32 v[54:55], v[150:151], s[90:91], v[54:55] neg_lo:[1,0,0] neg_hi:[1,0,0]\n\t"
"v_pk_add_f32 v[152:153], v[56:57], v[152:153] neg_lo:[0,1] neg_hi:[0,1]\n\t"
"v_pk_add_f32 v[154:155], v[58:59], v[154:155] neg_lo:[0,1] neg_hi:[0,1]\n\t"
"v_pk_fma_f32 v[56:57], v[152:153], s[90:91], v[56:57] neg_lo:[1,0,0] neg_hi:[1,0,0]\n\t"
"v_pk_fma_f32 v[58:59], v[154:155], s[90:91], v[58:59] neg_lo:[1,0,0] neg_hi:[1,0,0]\n\t"
"v_pk_add_f32 v[168:169], v[72:73], v[168:169] neg_lo:[0,1] neg_hi:[0,1]\n\t"
"v_pk_add_f32 v[170:171], v[74:75], v[170:171] neg_lo:[0,1] neg_hi:[0,1]\n\t"
"v_pk_fma_f32 v[72:73], v[168:169], s[90:91], v[72:73] neg_lo:[1,0,0] neg_hi:[1,0,0]\n\t"
"v_pk_fma_f32 v[74:75], v[170:171], s[90:91], v[74:75] neg_lo:[1,0,0] neg_hi:[1,0,0]\n\t"
"v_pk_add_f32 v[184:185], v[88:89], v[184:185] neg_lo:[0,1] neg_hi:[0,1]\n\t"
"v_pk_add_f32 v[186:187], v[90:91], v[186:187] neg_lo:[0,1] neg_hi:[0,1]\n\t"
"v_pk_fma_f32 v[88:89], v[184:185], s[90:91], v[88:89] neg_lo:[1,0,0] neg_hi:[1,0,0]\n\t"
"v_pk_fma_f32 v[90:91], v[186:187], s[90:91], v[90:91] neg_lo:[1,0,0] neg_hi:[1,0,0]\n\t"
"v_pk_add_f32 v[188:189], v[92:93], v[188:189] neg_lo:[0,1] neg_hi:[0,1]\n\t"
"v_pk_add_f32 v[190:191], v[94:95], v[190:191] neg_lo:[0,1] neg_hi:[0,1]\n\t"
"v_pk_fma_f32 v[92:93], v[188:189], s[90:91], v[92:93] neg_lo:[1,0,0] neg_hi:[1,0,0]\n\t"
"v_pk_fma_f32 v[94:95], v[190:191], s[90:91], v[94:95] neg_lo:[1,0,0] neg_hi:[1,0,0]\n\t"
"v_pk_add_f32 v[172:173], v[76:77], v[172:173] neg_lo:[0,1] neg_hi:[0,1]\n\t"
"v_pk_add_f32 v[174:175], v[78:79], v[174:175] neg_lo:[0,1] neg_hi:[0,1]\n\t"
"v_pk_fma_f32 v[76:77], v[172:173], s[90:91], v[76:77] neg_lo:[1,0,0] neg_hi:[1,0,0]\n\t"
"v_pk_fma_f32 v[78:79], v[174:175], s[90:91], v[78:79] neg_lo:[1,0,0] neg_hi:[1,0,0]\n\t"
"v_pk_add_f32 v[156:157], v[60:61], v[156:157] neg_lo:[0,1] neg_hi:[0,1]\n\t"
"v_pk_add_f32 v[158:159], v[62:63], v[158:159] neg_lo:[0,1] neg_hi:[0,1]\n\t"
"v_pk_fma_f32 v[60:61], v[156:157], s[90:91], v[60:61] neg_lo:[1,0,0] neg_hi:[1,0,0]\n\t"
"v_pk_fma_f32 v[62:63], v[158:159], s[90:91], v[62:63] neg_lo:[1,0,0] neg_hi:[1,0,0]\n\t"
"v_mfma_f32_4x4x4_16b_bf16 v[192:195], a[222:223], a[176:177], v[200:203]\n\t"
"ds_read_b128 v[120:123], v230 offset:20480\n\t"
"v_mfma_f32_4x4x4_16b_bf16 v[196:199], a[222:223], a[184:185], v[204:207]\n\t"
"ds_read_b64_tr_b16 a[194:195], v250 offset:41600\n\t"
"v_mfma_f32_4x4x4_16b_bf16 v[200:203], a[222:223], a[180:181], v[200:203]\n\t"
"buffer_load_dwordx4 a[108:111], off, s[84:87], s42 offset:3072 sc1\n\t"
"v_mfma_f32_4x4x4_16b_bf16 v[204:207], a[222:223], a[188:189], v[204:207]\n\t"
"ds_read_b64_tr_b16 a[202:203], v251 offset:41600\n\t"
"v_mfma_f32_4x4x4_16b_bf16 v[208:211], a[222:223], a[192:193], v[216:219]\n\t"
"ds_read_b64_tr_b16 a[176:177], v248 offset:41472\n\t"
"v_mfma_f32_4x4x4_16b_bf16 v[212:215], a[222:223], a[200:201], v[220:223]\n\t"
"ds_read_b64_tr_b16 a[184:185], v249 offset:41472\n\t"
"v_mfma_f32_4x4x4_16b_bf16 v[216:219], a[222:223], a[196:197], v[216:219]\n\t"
"ds_read_b64_tr_b16 a[180:181], v249 offset:41728\n\t"
"v_mfma_f32_4x4x4_16b_bf16 v[220:223], a[222:223], a[204:205], v[220:223]\n\t"
"s_waitcnt vmcnt(11) lgkmcnt(7)\n\t"
"buffer_load_dwordx4 a[104:107], off, s[84:87], s42 offset:2048 sc1\n\t"
"v_mfma_f32_16x16x32_bf16 v[48:51], a[48:51], a[144:147], v[48:51]\n\t"
"ds_read_b64_tr_b16 a[188:189], v248 offset:41728\n\t"
"v_cvt_pk_bf16_f32 v195, -v195, -v199\n\t"
"v_mfma_f32_16x16x32_bf16 v[48:51], a[52:55], a[160:163], v[48:51]\n\t"
"buffer_load_dwordx4 a[96:99], off, s[84:87], s42 offset:0 sc1\n\t"
"v_mfma_f32_16x16x32_bf16 v[64:67], a[56:59], a[144:147], v[64:67]\n\t"
"ds_write_addtid_b32 v195 offset:5152\n\t"
"v_cvt_pk_bf16_f32 v194, -v194, -v198\n\t"
"v_mfma_f32_16x16x32_bf16 v[64:67], a[60:63], a[160:163], v[64:67]\n\t"
"ds_write_addtid_b32 v194 offset:1024\n\t"
"v_cvt_pk_bf16_f32 v203, v203, v207\n\t"
"v_mfma_f32_16x16x32_bf16 v[80:83], a[64:67], a[144:147], v[80:83]\n\t"
"ds_read_b128 a[232:235], v230 offset:33792\n\t"
"v_cvt_pk_bf16_f32 v211, -v211, -v215\n\t"
"v_mfma_f32_16x16x32_bf16 v[80:83], a[68:71], a[160:163], v[80:83]\n\t"
"s_waitcnt lgkmcnt(7)\n\t"
"buffer_store_dwordx4 v[120:123], v233, s[16:19], s20 offen nt\n\t"
"v_mfma_f32_16x16x32_bf16 v[84:87], a[64:67], a[148:151], v[84:87]\n\t"
"ds_read_b128 a[144:147], v224 offset:17568\n\t"
"v_cvt_pk_bf16_f32 v201, v201, v205\n\t"
"v_mfma_f32_16x16x32_bf16 v[84:87], a[68:71], a[164:167], v[84:87]\n\t"
"ds_read_b128 v[136:139], v231 offset:20480\n\t"
"v_cvt_pk_bf16_f32 v202, v202, v206\n\t"
"v_mfma_f32_16x16x32_bf16 v[68:71], a[56:59], a[148:151], v[68:71]\n\t"
"ds_read_b128 a[160:163], v224 offset:19616\n\t"
"v_cvt_pk_bf16_f32 v193, -v193, -v197\n\t"
"v_mfma_f32_16x16x32_bf16 v[68:71], a[60:63], a[164:167], v[68:71]\n\t"
"buffer_load_dwordx4 a[100:103], off, s[84:87], s42 offset:1024 sc1\n\t"
"v_mfma_f32_16x16x32_bf16 v[52:55], a[48:51], a[148:151], v[52:55]\n\t"
"ds_read_b128 a[148:151], v224 offset:17824\n\t"
"v_cvt_pk_bf16_f32 v209, -v209, -v213\n\t"
"v_mfma_f32_16x16x32_bf16 v[52:55], a[52:55], a[164:167], v[52:55]\n\t"
"ds_write_addtid_b32 v211 offset:7200\n\t"
"v_cvt_pk_bf16_f32 v200, v200, v204\n\t"
"v_mfma_f32_16x16x32_bf16 v[56:59], a[48:51], a[152:155], v[56:59]\n\t"
"ds_read_b128 a[236:239], v231 offset:33792\n\t"
"v_cvt_pk_bf16_f32 v216, v216, v220\n\t"
"v_mfma_f32_16x16x32_bf16 v[56:59], a[52:55], a[168:171], v[56:59]\n\t"
"buffer_load_dwordx4 a[136:139], off, s[28:31], s48 offset:0 sc1\n\t"
"v_mfma_f32_16x16x32_bf16 v[72:75], a[56:59], a[152:155], v[72:75]\n\t"
"ds_read_b128 a[164:167], v224 offset:19872\n\t"
"v_cvt_pk_bf16_f32 v210, -v210, -v214\n\t"
"v_mfma_f32_16x16x32_bf16 v[72:75], a[60:63], a[168:171], v[72:75]\n\t"
"ds_write_addtid_b32 v193 offset:4128\n\t"
"v_cvt_pk_bf16_f32 v218, v218, v222\n\t"
"v_mfma_f32_16x16x32_bf16 v[88:91], a[64:67], a[152:155], v[88:91]\n\t"
"s_nop 0\n\t"
"ds_write_addtid_b32 v201 offset:12448\n\t"
"ds_write_addtid_b32 v202 offset:9344\n\t"
"v_mfma_f32_16x16x32_bf16 v[88:91], a[68:71], a[168:171], v[88:91]\n\t"
"s_waitcnt lgkmcnt(8)\n\t"
"buffer_store_dwordx4 v[136:139], v234, s[16:19], s20 offen nt\n\t"
"v_mfma_f32_16x16x32_bf16 v[92:95], a[64:67], a[156:159], v[92:95]\n\t"
"ds_read_b128 v[108:111], v229 offset:33792\n\t"
"v_cvt_pk_bf16_f32 v192, -v192, -v196\n\t"
"v_mfma_f32_16x16x32_bf16 v[92:95], a[68:71], a[172:175], v[92:95]\n\t"
"ds_write_addtid_b32 v210 offset:3072\n\t"
"v_cvt_pk_bf16_f32 v219, v219, v223\n\t"
"v_mfma_f32_16x16x32_bf16 v[76:79], a[56:59], a[156:159], v[76:79]\n\t"
"ds_write_addtid_b32 v219 offset:15520\n\t"
"v_cvt_pk_bf16_f32 v208, -v208, -v212\n\t"
"v_mfma_f32_16x16x32_bf16 v[76:79], a[60:63], a[172:175], v[76:79]\n\t"
"ds_write_addtid_b32 v200 offset:8320\n\t"
"buffer_load_dwordx4 a[140:143], off, s[28:31], s48 offset:1024 sc1\n\t"
"v_mfma_f32_16x16x32_bf16 v[60:63], a[48:51], a[156:159], v[60:63]\n\t"
"s_nop 0\n\t"
"ds_write_addtid_b32 v209 offset:6176\n\t"
"ds_write_addtid_b32 v218 offset:11392\n\t"
"v_mfma_f32_16x16x32_bf16 v[60:63], a[52:55], a[172:175], v[60:63]\n\t"
"s_waitcnt vmcnt(11) lgkmcnt(10)\n\t"
"ds_write_addtid_b32 v192 offset:0\n\t"
"ds_write_addtid_b32 v216 offset:10368\n\t"
"v_mfma_f32_16x16x32_bf16 v[144:147], a[72:75], a[144:147], v[144:147]\n\t"
"ds_write_addtid_b32 v203 offset:13472\n\t"
"v_cvt_pk_bf16_f32 v217, v217, v221\n\t"
"v_mfma_f32_16x16x32_bf16 v[144:147], a[76:79], a[160:163], v[144:147]\n\t"
"buffer_load_dwordx4 a[132:135], off, s[84:87], s48 offset:3072 sc1\n\t"
"v_mfma_f32_16x16x32_bf16 v[160:163], a[80:83], a[144:147], v[160:163]\n\t"
"s_nop 0\n\t"
"ds_write_addtid_b32 v208 offset:2048\n\t"
"ds_write_addtid_b32 v217 offset:14496\n\t"
"v_mfma_f32_16x16x32_bf16 v[160:163], a[84:87], a[160:163], v[160:163]\n\t"
"s_nop 0\n\t"
"ds_read_b128 a[152:155], v224 offset:18080\n\t"
"ds_read_b128 a[156:159], v224 offset:18336\n\t"
"v_mfma_f32_16x16x32_bf16 v[176:179], a[88:91], a[144:147], v[176:179]\n\t"
"s_nop 0\n\t"
"ds_read_b128 a[172:175], v224 offset:20384\n\t"
"ds_read_b64_tr_b16 a[192:193], v250 offset:41472\n\t"
"v_mfma_f32_16x16x32_bf16 v[176:179], a[92:95], a[160:163], v[176:179]\n\t"
"s_waitcnt lgkmcnt(13)\n\t"
"buffer_store_dwordx4 v[108:111], v232, s[16:19], s25 offen nt\n\t"
"v_mfma_f32_16x16x32_bf16 v[180:183], a[88:91], a[148:151], v[180:183]\n\t"
"s_nop 0\n\t"
"ds_read_b128 a[168:171], v224 offset:20128\n\t"
"ds_read_b64_tr_b16 a[200:201], v251 offset:41472\n\t"
"v_mfma_f32_16x16x32_bf16 v[180:183], a[92:95], a[164:167], v[180:183]\n\t"
"s_nop 0\n\t"
"ds_read_b64_tr_b16 a[196:197], v251 offset:41728\n\t"
"ds_read_b64_tr_b16 a[204:205], v250 offset:41728\n\t"
"v_mfma_f32_16x16x32_bf16 v[164:167], a[80:83], a[148:151], v[164:167]\n\t"
"buffer_load_dwordx4 a[128:131], off, s[84:87], s48 offset:2048 sc1\n\t"
"v_mfma_f32_16x16x32_bf16 v[164:167], a[84:87], a[164:167], v[164:167]\n\t"
"v_mfma_f32_16x16x32_bf16 v[148:151], a[72:75], a[148:151], v[148:151]\n\t"
"buffer_load_dwordx4 a[120:123], off, s[84:87], s48 offset:0 sc1\n\t"
"v_mfma_f32_16x16x32_bf16 v[148:151], a[76:79], a[164:167], v[148:151]\n\t"
"s_waitcnt lgkmcnt(0)\n\t"
"s_barrier\n\t"
"v_mfma_f32_16x16x32_bf16 v[152:155], a[72:75], a[152:155], v[152:155]\n\t"
"ds_read_b128 a[148:151], v224 offset:256\n\t"
"v_mfma_f32_16x16x32_bf16 v[152:155], a[76:79], a[168:171], v[152:155]\n\t"
"ds_read_b128 a[144:147], v224 offset:0\n\t"
"v_add_u32 v248, 33280, v227\n\t"
"v_mfma_f32_16x16x32_bf16 v[168:171], a[80:83], a[152:155], v[168:171]\n\t"
"ds_read_b128 a[160:163], v224 offset:2048\n\t"
"v_mfma_f32_16x16x32_bf16 v[168:171], a[84:87], a[168:171], v[168:171]\n\t"
"ds_read_b128 a[164:167], v224 offset:2304\n\t"
"v_xor_b32 v249, 32, v248\n\t"
"v_mfma_f32_16x16x32_bf16 v[184:187], a[88:91], a[152:155], v[184:187]\n\t"
"v_xor_b32 v250, 64, v248\n\t"
"v_mfma_f32_16x16x32_bf16 v[184:187], a[92:95], a[168:171], v[184:187]\n\t"
"buffer_load_dwordx4 a[124:127], off, s[84:87], s48 offset:1024 sc1\n\t"
"v_mfma_f32_16x16x32_bf16 v[188:191], a[88:91], a[156:159], v[188:191]\n\t"
"ds_read_b128 a[168:171], v224 offset:2560\n\t"
"v_xor_b32 v251, 96, v248\n\t"
"v_mfma_f32_16x16x32_bf16 v[188:191], a[92:95], a[172:175], v[188:191]\n\t"
"ds_read_b128 a[152:155], v224 offset:512\n\t"
"v_mfma_f32_16x16x32_bf16 v[172:175], a[80:83], a[156:159], v[172:175]\n\t"
"v_mfma_f32_16x16x32_bf16 v[172:175], a[84:87], a[172:175], v[172:175]\n\t"
"ds_read_b64_tr_b16 a[182:183], v249 offset:512\n\t"
"v_mfma_f32_16x16x32_bf16 v[156:159], a[72:75], a[156:159], v[156:159]\n\t"
"ds_read_b128 a[156:159], v224 offset:768\n\t"
"v_mfma_f32_16x16x32_bf16 v[156:159], a[76:79], a[172:175], v[156:159]\n\t"
"buffer_load_dwordx4 a[16:19], off, s[28:31], s75 offset:0 sc1\n\t"
"v_mfma_f32_4x4x4_16b_bf16 v[200:203], a[220:221], a[178:179], 0\n\t"
"ds_read_b64_tr_b16 a[190:191], v251 offset:512\n\t"
"v_mfma_f32_4x4x4_16b_bf16 v[204:207], a[220:221], a[186:187], 0\n\t"
"ds_read_b64_tr_b16 a[198:199], v249 offset:1664\n\t"
"v_mfma_f32_4x4x4_16b_bf16 v[216:219], a[220:221], a[194:195], 0\n\t"
"ds_read_b128 a[172:175], v224 offset:2816\n\t"
"v_mfma_f32_4x4x4_16b_bf16 v[220:223], a[220:221], a[202:203], 0\n\t"
"buffer_store_dwordx4 a[232:235], v233, s[16:19], s25 offen nt\n\t"
"v_mfma_f32_4x4x4_16b_bf16 v[192:195], a[222:223], a[176:177], v[200:203]\n\t"
"ds_read_b64_tr_b16 a[206:207], v251 offset:1664\n\t"
"v_mfma_f32_4x4x4_16b_bf16 v[196:199], a[222:223], a[184:185], v[204:207]\n\t"
"ds_read_b64_tr_b16 a[178:179], v248 offset:512\n\t"
"v_mfma_f32_4x4x4_16b_bf16 v[200:203], a[222:223], a[180:181], v[200:203]\n\t"
"ds_read_b64_tr_b16 a[186:187], v250 offset:512\n\t"
"v_mfma_f32_4x4x4_16b_bf16 v[204:207], a[222:223], a[188:189], v[204:207]\n\t"
"ds_read_b64_tr_b16 a[194:195], v248 offset:1664\n\t"
"v_mfma_f32_4x4x4_16b_bf16 v[208:211], a[222:223], a[192:193], v[216:219]\n\t"
"ds_read_b64_tr_b16 a[202:203], v250 offset:1664\n\t"
"v_mfma_f32_4x4x4_16b_bf16 v[212:215], a[222:223], a[200:201], v[220:223]\n\t"
"ds_read_b64_tr_b16 a[176:177], v248 offset:0\n\t"
"v_mfma_f32_4x4x4_16b_bf16 v[216:219], a[222:223], a[196:197], v[216:219]\n\t"
"ds_read_b64_tr_b16 a[184:185], v250 offset:0\n\t"
"v_mfma_f32_4x4x4_16b_bf16 v[220:223], a[222:223], a[204:205], v[220:223]\n\t"
"s_waitcnt vmcnt(10) lgkmcnt(8)\n\t"
"buffer_load_dwordx4 a[20:23], off, s[28:31], s75 offset:1024 sc1\n\t"
"v_mfma_f32_16x16x32_bf16 v[48:51], a[96:99], a[144:147], v[48:51]\n\t"
"ds_read_b64_tr_b16 a[180:181], v249 offset:0\n\t"
"v_cvt_pk_bf16_f32 v193, -v193, -v197\n\t"
"v_mfma_f32_16x16x32_bf16 v[48:51], a[100:103], a[160:163], v[48:51]\n\t"
"ds_read_b64_tr_b16 a[188:189], v251 offset:0\n\t"
"v_cvt_pk_bf16_f32 v195, -v195, -v199\n\t"
"v_mfma_f32_16x16x32_bf16 v[64:67], a[104:107], a[144:147], v[64:67]\n\t"
"ds_read_b64_tr_b16 a[192:193], v248 offset:1152\n\t"
"v_cvt_pk_bf16_f32 v203, v203, v207\n\t"
"v_mfma_f32_16x16x32_bf16 v[64:67], a[108:111], a[160:163], v[64:67]\n\t"
"buffer_store_dwordx4 a[236:239], v234, s[16:19], s25 offen nt\n\t"
"v_mfma_f32_16x16x32_bf16 v[80:83], a[112:115], a[144:147], v[80:83]\n\t"
"ds_read_b128 a[144:147], v224 offset:1024\n\t"
"v_cvt_pk_bf16_f32 v201, v201, v205\n\t"
"v_mfma_f32_16x16x32_bf16 v[80:83], a[116:119], a[160:163], v[80:83]\n\t"
"ds_write_addtid_b32 v201 offset:28992\n\t"
"v_cvt_pk_bf16_f32 v202, v202, v206\n\t"
"v_mfma_f32_16x16x32_bf16 v[84:87], a[112:115], a[148:151], v[84:87]\n\t"
"ds_read_b128 a[160:163], v224 offset:3072\n\t"
"v_cvt_pk_bf16_f32 v219, v219, v223\n\t"
"v_mfma_f32_16x16x32_bf16 v[84:87], a[116:119], a[164:167], v[84:87]\n\t"
"buffer_load_dwordx4 a[12:15], off, s[84:87], s75 offset:3072 sc1\n\t"
"v_mfma_f32_16x16x32_bf16 v[68:71], a[104:107], a[148:151], v[68:71]\n\t"
"ds_write_addtid_b32 v203 offset:30016\n\t"
"v_mfma_f32_16x16x32_bf16 v[68:71], a[108:111], a[164:167], v[68:71]\n\t"
"ds_write_addtid_b32 v195 offset:21696\n\t"
"v_cvt_pk_bf16_f32 v211, -v211, -v215\n\t"
"v_mfma_f32_16x16x32_bf16 v[52:55], a[96:99], a[148:151], v[52:55]\n\t"
"ds_read_b128 a[148:151], v224 offset:1280\n\t"
"v_cvt_pk_bf16_f32 v209, -v209, -v213\n\t"
"v_mfma_f32_16x16x32_bf16 v[52:55], a[100:103], a[164:167], v[52:55]\n\t"
"ds_write_addtid_b32 v211 offset:23744\n\t"
"v_cvt_pk_bf16_f32 v200, v200, v204\n\t"
"v_mfma_f32_16x16x32_bf16 v[56:59], a[96:99], a[152:155], v[56:59]\n\t"
"ds_read_b128 a[164:167], v224 offset:3328\n\t"
"v_cvt_pk_bf16_f32 v194, -v194, -v198\n\t"
"v_mfma_f32_16x16x32_bf16 v[56:59], a[100:103], a[168:171], v[56:59]\n\t"
"buffer_load_dwordx4 a[8:11], off, s[84:87], s75 offset:2048 sc1\n\t"
"v_mfma_f32_16x16x32_bf16 v[72:75], a[104:107], a[152:155], v[72:75]\n\t"
"s_nop 0\n\t"
"ds_write_addtid_b32 v200 offset:24864\n\t"
"ds_write_addtid_b32 v219 offset:32064\n\t"
"v_mfma_f32_16x16x32_bf16 v[72:75], a[108:111], a[168:171], v[72:75]\n\t"
"ds_write_addtid_b32 v209 offset:22720\n\t"
"v_cvt_pk_bf16_f32 v210, -v210, -v214\n\t"
"v_mfma_f32_16x16x32_bf16 v[88:91], a[112:115], a[152:155], v[88:91]\n\t"
"ds_read_b128 a[152:155], v224 offset:1536\n\t"
"v_cvt_pk_bf16_f32 v217, v217, v221\n\t"
"v_mfma_f32_16x16x32_bf16 v[88:91], a[116:119], a[168:171], v[88:91]\n\t"
"s_waitcnt lgkmcnt(9)\n\t"
"buffer_load_dwordx4 a[0:3], off, s[84:87], s75 offset:0 sc1\n\t"
"v_mfma_f32_16x16x32_bf16 v[92:95], a[112:115], a[156:159], v[92:95]\n\t"
"ds_read_b128 a[168:171], v224 offset:3584\n\t"
"v_cvt_pk_bf16_f32 v192, -v192, -v196\n\t"
"v_mfma_f32_16x16x32_bf16 v[92:95], a[116:119], a[172:175], v[92:95]\n\t"
"ds_write_addtid_b32 v210 offset:19616\n\t"
"v_cvt_pk_bf16_f32 v216, v216, v220\n\t"
"v_mfma_f32_16x16x32_bf16 v[76:79], a[104:107], a[156:159], v[76:79]\n\t"
"ds_write_addtid_b32 v202 offset:25888\n\t"
"v_cvt_pk_bf16_f32 v218, v218, v222\n\t"
"v_mfma_f32_16x16x32_bf16 v[76:79], a[108:111], a[172:175], v[76:79]\n\t"
"ds_write_addtid_b32 v192 offset:16544\n\t"
"v_mfma_f32_16x16x32_bf16 v[60:63], a[96:99], a[156:159], v[60:63]\n\t"
"ds_write_addtid_b32 v216 offset:26912\n\t"
"v_cvt_pk_bf16_f32 v208, -v208, -v212\n\t"
"v_mfma_f32_16x16x32_bf16 v[60:63], a[100:103], a[172:175], v[60:63]\n\t"
"s_waitcnt vmcnt(7) lgkmcnt(9)\n\t"
"ds_write_addtid_b32 v208 offset:18592\n\t"
"ds_write_addtid_b32 v217 offset:31040\n\t"
"v_mfma_f32_16x16x32_bf16 v[144:147], a[120:123], a[144:147], v[144:147]\n\t"
"ds_write_addtid_b32 v194 offset:17568\n\t"
"v_mfma_f32_16x16x32_bf16 v[144:147], a[124:127], a[160:163], v[144:147]\n\t"
"ds_write_addtid_b32 v193 offset:20672\n\t"
"v_mfma_f32_16x16x32_bf16 v[160:163], a[128:131], a[144:147], v[160:163]\n\t"
"ds_write_addtid_b32 v218 offset:27936\n\t"
"v_mfma_f32_16x16x32_bf16 v[160:163], a[132:135], a[160:163], v[160:163]\n\t"
"ds_read_b128 a[156:159], v224 offset:1792\n\t"
"v_mfma_f32_16x16x32_bf16 v[176:179], a[136:139], a[144:147], v[176:179]\n\t"
"ds_read_b128 a[172:175], v224 offset:3840\n\t"
"v_mfma_f32_16x16x32_bf16 v[176:179], a[140:143], a[160:163], v[176:179]\n\t"
"buffer_load_dwordx4 a[4:7], off, s[84:87], s75 offset:1024 sc1\n\t"
"v_mfma_f32_16x16x32_bf16 v[180:183], a[136:139], a[148:151], v[180:183]\n\t"
"ds_read_b64_tr_b16 a[200:201], v250 offset:1152\n\t"
"v_mfma_f32_16x16x32_bf16 v[180:183], a[140:143], a[164:167], v[180:183]\n\t"
"ds_read_b64_tr_b16 a[196:197], v249 offset:1152\n\t"
"v_mfma_f32_16x16x32_bf16 v[164:167], a[128:131], a[148:151], v[164:167]\n\t"
"ds_read_b64_tr_b16 a[204:205], v251 offset:1152\n\t"
"v_mfma_f32_16x16x32_bf16 v[164:167], a[132:135], a[164:167], v[164:167]\n\t"
"buffer_load_dwordx4 a[40:43], off, s[28:31], s78 offset:0 sc1\n\t"
"v_mfma_f32_16x16x32_bf16 v[148:151], a[120:123], a[148:151], v[148:151]\n\t"
"v_mfma_f32_16x16x32_bf16 v[148:151], a[124:127], a[164:167], v[148:151]\n\t"
"s_waitcnt lgkmcnt(0)\n\t"
"s_barrier\n\t"
"v_mfma_f32_16x16x32_bf16 v[152:155], a[120:123], a[152:155], v[152:155]\n\t"
"s_nop 0\n\t"
"ds_read_b128 a[160:163], v224 offset:18592\n\t"
"ds_read_b128 a[164:167], v224 offset:18848\n\t"
"v_mfma_f32_16x16x32_bf16 v[152:155], a[124:127], a[168:171], v[152:155]\n\t"
"ds_read_b128 a[144:147], v224 offset:16544\n\t"
"v_mfma_f32_16x16x32_bf16 v[168:171], a[128:131], a[152:155], v[168:171]\n\t"
"ds_read_b128 a[148:151], v224 offset:16800\n\t"
"v_mfma_f32_16x16x32_bf16 v[168:171], a[132:135], a[168:171], v[168:171]\n\t"
"buffer_load_dwordx4 a[44:47], off, s[28:31], s78 offset:1024 sc1\n\t"
"v_mfma_f32_16x16x32_bf16 v[184:187], a[136:139], a[152:155], v[184:187]\n\t"
"v_mfma_f32_16x16x32_bf16 v[184:187], a[140:143], a[168:171], v[184:187]\n\t"
"ds_read_b128 a[152:155], v224 offset:17056\n\t"
"v_mfma_f32_16x16x32_bf16 v[188:191], a[136:139], a[156:159], v[188:191]\n\t"
"ds_read_b128 a[168:171], v224 offset:19104\n\t"
"v_mfma_f32_16x16x32_bf16 v[188:191], a[140:143], a[172:175], v[188:191]\n\t"
"buffer_load_dwordx4 a[36:39], off, s[84:87], s78 offset:3072 sc1\n\t"
"v_mfma_f32_16x16x32_bf16 v[172:175], a[128:131], a[156:159], v[172:175]\n\t"
"v_mfma_f32_16x16x32_bf16 v[172:175], a[132:135], a[172:175], v[172:175]\n\t"
"buffer_load_dwordx4 a[32:35], off, s[84:87], s78 offset:2048 sc1\n\t"
"v_mfma_f32_16x16x32_bf16 v[156:159], a[120:123], a[156:159], v[156:159]\n\t"
"ds_read_b128 a[156:159], v224 offset:17312\n\t"
"v_mfma_f32_16x16x32_bf16 v[156:159], a[124:127], a[172:175], v[156:159]\n\t"
"ds_read_b128 a[172:175], v224 offset:19360\n\t"
"v_mfma_f32_16x16x32_bf16 v[192:195], a[224:227], a[176:179], 0\n\t"
"ds_read_b64_tr_b16 a[176:177], v248 offset:42624\n\t"
"v_mfma_f32_16x16x32_bf16 v[196:199], a[224:227], a[180:183], 0\n\t"
"buffer_load_dwordx4 a[24:27], off, s[84:87], s78 offset:0 sc1\n\t"
"v_mfma_f32_16x16x32_bf16 v[200:203], a[224:227], a[184:187], 0\n\t"
"ds_read_b64_tr_b16 a[178:179], v248 offset:43136\n\t"
"v_mfma_f32_16x16x32_bf16 v[204:207], a[224:227], a[188:191], 0\n\t"
"ds_read_b64_tr_b16 a[180:181], v249 offset:42624\n\t"
"v_mfma_f32_16x16x32_bf16 v[208:211], a[224:227], a[192:195], 0\n\t"
"ds_read_b64_tr_b16 a[182:183], v249 offset:43136\n\t"
"v_mfma_f32_16x16x32_bf16 v[212:215], a[224:227], a[196:199], 0\n\t"
"buffer_load_dwordx4 a[28:31], off, s[84:87], s78 offset:1024 sc1\n\t"
"v_mfma_f32_16x16x32_bf16 v[216:219], a[224:227], a[200:203], 0\n\t"
"ds_read_b64_tr_b16 a[184:185], v250 offset:42624\n\t"
"v_mfma_f32_16x16x32_bf16 v[220:223], a[224:227], a[204:207], 0\n\t"
"s_waitcnt vmcnt(6) lgkmcnt(7)\n\t"
"ds_read_b64_tr_b16 a[186:187], v250 offset:43136\n\t"
"v_cvt_pk_bf16_f32 v193, v193, v197\n\t"
"v_mfma_f32_16x16x32_bf16 v[48:51], a[0:3], a[144:147], v[48:51]\n\t"
"ds_read_b64_tr_b16 a[188:189], v251 offset:42624\n\t"
"v_mfma_f32_16x16x32_bf16 v[48:51], a[4:7], a[160:163], v[48:51]\n\t"
"ds_read_b64_tr_b16 a[190:191], v251 offset:43136\n\t"
"v_cvt_pk_bf16_f32 v195, v195, v199\n\t"
"v_mfma_f32_16x16x32_bf16 v[64:67], a[8:11], a[144:147], v[64:67]\n\t"
"ds_read_b64_tr_b16 a[192:193], v248 offset:21888\n\t"
"v_cvt_pk_bf16_f32 v194, v194, v198\n\t"
"v_mfma_f32_16x16x32_bf16 v[64:67], a[12:15], a[160:163], v[64:67]\n\t"
"ds_read_b64_tr_b16 a[194:195], v248 offset:22400\n\t"
"v_cvt_pk_bf16_f32 v192, v192, v196\n\t"
"v_mfma_f32_16x16x32_bf16 v[80:83], a[16:19], a[144:147], v[80:83]\n\t"
"ds_read_b128 a[144:147], v224 offset:17568\n\t"
"v_cvt_pk_bf16_f32 v211, v211, v215\n\t"
"v_mfma_f32_16x16x32_bf16 v[80:83], a[20:23], a[160:163], v[80:83]\n\t"
"ds_read_b64_tr_b16 a[196:197], v249 offset:21888\n\t"
"v_mfma_f32_16x16x32_bf16 v[84:87], a[16:19], a[148:151], v[84:87]\n\t"
"ds_write_addtid_b32 v192 offset:0\n\t"
"v_cvt_pk_bf16_f32 v203, v203, v207\n\t"
"v_mfma_f32_16x16x32_bf16 v[84:87], a[20:23], a[164:167], v[84:87]\n\t"
"ds_read_b64_tr_b16 a[198:199], v249 offset:22400\n\t"
"v_cvt_pk_bf16_f32 v209, v209, v213\n\t"
"v_mfma_f32_16x16x32_bf16 v[68:71], a[8:11], a[148:151], v[68:71]\n\t"
"ds_read_b128 a[160:163], v224 offset:19616\n\t"
"v_cvt_pk_bf16_f32 v218, v218, v222\n\t"
"v_mfma_f32_16x16x32_bf16 v[68:71], a[12:15], a[164:167], v[68:71]\n\t"
"ds_write_addtid_b32 v211 offset:13472\n\t"
"v_cvt_pk_bf16_f32 v200, v200, v204\n\t"
"v_mfma_f32_16x16x32_bf16 v[52:55], a[0:3], a[148:151], v[52:55]\n\t"
"ds_write_addtid_b32 v194 offset:4128\n\t"
"v_cvt_pk_bf16_f32 v217, v217, v221\n\t"
"v_mfma_f32_16x16x32_bf16 v[52:55], a[4:7], a[164:167], v[52:55]\n\t"
"ds_write_addtid_b32 v200 offset:2048\n\t"
"v_cvt_pk_bf16_f32 v201, v201, v205\n\t"
"v_mfma_f32_16x16x32_bf16 v[56:59], a[0:3], a[152:155], v[56:59]\n\t"
"ds_read_b128 a[148:151], v224 offset:17824\n\t"
"v_cvt_pk_bf16_f32 v216, v216, v220\n\t"
"v_mfma_f32_16x16x32_bf16 v[56:59], a[4:7], a[168:171], v[56:59]\n\t"
"ds_read_b128 a[164:167], v224 offset:19872\n\t"
"v_cvt_pk_bf16_f32 v210, v210, v214\n\t"
"v_mfma_f32_16x16x32_bf16 v[72:75], a[8:11], a[152:155], v[72:75]\n\t"
"ds_write_addtid_b32 v195 offset:12448\n\t"
"v_cvt_pk_bf16_f32 v208, v208, v212\n\t"
"v_mfma_f32_16x16x32_bf16 v[72:75], a[12:15], a[168:171], v[72:75]\n\t"
"buffer_load_dwordx4 a[64:67], off, s[28:31], 0 offset:0 sc1\n\t"
"v_mfma_f32_16x16x32_bf16 v[88:91], a[16:19], a[152:155], v[88:91]\n\t"
"ds_read_b128 a[152:155], v224 offset:18080\n\t"
"v_mfma_f32_16x16x32_bf16 v[88:91], a[20:23], a[168:171], v[88:91]\n\t"
"s_waitcnt lgkmcnt(8)\n\t"
"ds_read_b128 a[168:171], v224 offset:20128\n\t"
"v_mfma_f32_16x16x32_bf16 v[92:95], a[16:19], a[156:159], v[92:95]\n\t"
"ds_write_addtid_b32 v201 offset:10368\n\t"
"v_cvt_pk_bf16_f32 v219, v219, v223\n\t"
"v_mfma_f32_16x16x32_bf16 v[92:95], a[20:23], a[172:175], v[92:95]\n\t"
"ds_write_addtid_b32 v217 offset:11392\n\t"
"v_cvt_pk_bf16_f32 v202, v202, v206\n\t"
"v_mfma_f32_16x16x32_bf16 v[76:79], a[8:11], a[156:159], v[76:79]\n\t"
"s_xor_b32 s24, s24, 384\n\t"
"ds_write_addtid_b32 v193 offset:8320\n\t"
"ds_write_addtid_b32 v216 offset:3072\n\t"
"v_mfma_f32_16x16x32_bf16 v[76:79], a[12:15], a[172:175], v[76:79]\n\t"
"buffer_load_dwordx4 a[68:71], off, s[28:31], 0 offset:1024 sc1\n\t"
"v_mfma_f32_16x16x32_bf16 v[60:63], a[0:3], a[156:159], v[60:63]\n\t"
"s_xor_b32 s20, s20, 384\n\t"
"ds_read_b128 a[156:159], v224 offset:18336\n\t"
"ds_write_addtid_b32 v218 offset:7200\n\t"
"v_mfma_f32_16x16x32_bf16 v[60:63], a[4:7], a[172:175], v[60:63]\n\t"
"s_waitcnt vmcnt(2) lgkmcnt(9)\n\t"
"buffer_load_dwordx4 a[60:63], off, s[84:87], 0 offset:3072 sc1\n\t"
"v_mfma_f32_16x16x32_bf16 v[144:147], a[24:27], a[144:147], v[144:147]\n\t"
"s_xor_b32 s27, s27, 384\n\t"
"ds_write_addtid_b32 v202 offset:6176\n\t"
"ds_write_addtid_b32 v210 offset:5152\n\t"
"v_mfma_f32_16x16x32_bf16 v[144:147], a[28:31], a[160:163], v[144:147]\n\t"
"s_nop 0\n\t"
"ds_write_addtid_b32 v209 offset:9344\n\t"
"ds_write_addtid_b32 v219 offset:15520\n\t"
"v_mfma_f32_16x16x32_bf16 v[160:163], a[32:35], a[144:147], v[160:163]\n\t"
"s_xor_b32 s25, s25, 384\n\t"
"ds_write_addtid_b32 v203 offset:14496\n\t"
"ds_write_addtid_b32 v208 offset:1024\n\t"
"v_mfma_f32_16x16x32_bf16 v[160:163], a[36:39], a[160:163], v[160:163]\n\t"
"buffer_load_dwordx4 a[56:59], off, s[84:87], 0 offset:2048 sc1\n\t"
"v_mfma_f32_16x16x32_bf16 v[176:179], a[40:43], a[144:147], v[176:179]\n\t"
"ds_read_b128 a[172:175], v224 offset:20384\n\t"
"v_mfma_f32_16x16x32_bf16 v[176:179], a[44:47], a[160:163], v[176:179]\n\t"
"buffer_load_dwordx4 a[48:51], off, s[84:87], 0 offset:0 sc1\n\t"
"v_mfma_f32_16x16x32_bf16 v[180:183], a[40:43], a[148:151], v[180:183]\n\t"
"ds_read_b64_tr_b16 a[200:201], v250 offset:21888\n\t"
"v_mfma_f32_16x16x32_bf16 v[180:183], a[44:47], a[164:167], v[180:183]\n\t"
"ds_read_b64_tr_b16 a[202:203], v250 offset:22400\n\t"
"v_mfma_f32_16x16x32_bf16 v[164:167], a[32:35], a[148:151], v[164:167]\n\t"
"s_nop 0\n\t"
"ds_read_b64_tr_b16 a[204:205], v251 offset:21888\n\t"
"ds_read_b64_tr_b16 a[206:207], v251 offset:22400\n\t"
"v_mfma_f32_16x16x32_bf16 v[164:167], a[36:39], a[164:167], v[164:167]\n\t"
"buffer_load_dwordx4 a[52:55], off, s[84:87], 0 offset:1024 sc1\n\t"
"v_mfma_f32_16x16x32_bf16 v[148:151], a[24:27], a[148:151], v[148:151]\n\t"
"v_mfma_f32_16x16x32_bf16 v[148:151], a[28:31], a[164:167], v[148:151]\n\t"
"s_waitcnt lgkmcnt(0)\n\t"
"s_barrier\n\t"
"v_mfma_f32_16x16x32_bf16 v[152:155], a[24:27], a[152:155], v[152:155]\n\t"
"ds_read_b128 a[160:163], v224 offset:2048\n\t"
"v_mfma_f32_16x16x32_bf16 v[152:155], a[28:31], a[168:171], v[152:155]\n\t"
"ds_read_b128 a[144:147], v224 offset:0\n\t"
"v_mfma_f32_16x16x32_bf16 v[168:171], a[32:35], a[152:155], v[168:171]\n\t"
"ds_read_b128 a[148:151], v224 offset:256\n\t"
"v_mfma_f32_16x16x32_bf16 v[168:171], a[36:39], a[168:171], v[168:171]\n\t"
"ds_read_b128 a[164:167], v224 offset:2304\n\t"
"v_mfma_f32_16x16x32_bf16 v[184:187], a[40:43], a[152:155], v[184:187]\n\t"
"v_mfma_f32_16x16x32_bf16 v[184:187], a[44:47], a[168:171], v[184:187]\n\t"
"v_mfma_f32_16x16x32_bf16 v[188:191], a[40:43], a[156:159], v[188:191]\n\t"
"ds_read_b128 a[152:155], v224 offset:512\n\t"
"v_mfma_f32_16x16x32_bf16 v[188:191], a[44:47], a[172:175], v[188:191]\n\t"
"v_mfma_f32_16x16x32_bf16 v[172:175], a[32:35], a[156:159], v[172:175]\n\t"
"v_mfma_f32_16x16x32_bf16 v[172:175], a[36:39], a[172:175], v[172:175]\n\t"
"buffer_load_dwordx4 a[88:91], off, s[28:31], s51 offset:0 sc1\n\t"
"v_mfma_f32_16x16x32_bf16 v[156:159], a[24:27], a[156:159], v[156:159]\n\t"
"ds_read_b128 a[168:171], v224 offset:2560\n\t"
"v_mfma_f32_16x16x32_bf16 v[156:159], a[28:31], a[172:175], v[156:159]\n\t"
"buffer_load_dwordx4 a[92:95], off, s[28:31], s51 offset:1024 sc1\n\t"
"v_mfma_f32_16x16x32_bf16 v[192:195], a[224:227], a[176:179], 0\n\t"
"v_mfma_f32_16x16x32_bf16 v[196:199], a[224:227], a[180:183], 0\n\t"
"buffer_load_dwordx4 a[84:87], off, s[84:87], s51 offset:3072 sc1\n\t"
"v_mfma_f32_16x16x32_bf16 v[200:203], a[224:227], a[184:187], 0\n\t"
"ds_read_b128 a[156:159], v224 offset:768\n\t"
"v_mfma_f32_16x16x32_bf16 v[204:207], a[224:227], a[188:191], 0\n\t"
".p2align 3\n\t"
"buffer_load_dwordx4 a[80:83], off, s[84:87], s51 offset:2048 sc1\n\t"
"v_pk_fma_f32 v[48:49], v[144:145], s[90:91], v[48:49]\n\t"
"v_pk_fma_f32 v[50:51], v[146:147], s[90:91], v[50:51]\n\t"
"v_pk_add_f32 v[144:145], v[48:49], v[144:145] neg_lo:[0,1] neg_hi:[0,1]\n\t"
"v_pk_add_f32 v[146:147], v[50:51], v[146:147] neg_lo:[0,1] neg_hi:[0,1]\n\t"
"v_pk_fma_f32 v[64:65], v[160:161], s[90:91], v[64:65]\n\t"
"v_pk_fma_f32 v[66:67], v[162:163], s[90:91], v[66:67]\n\t"
"v_pk_add_f32 v[160:161], v[64:65], v[160:161] neg_lo:[0,1] neg_hi:[0,1]\n\t"
"v_pk_add_f32 v[162:163], v[66:67], v[162:163] neg_lo:[0,1] neg_hi:[0,1]\n\t"
"v_pk_fma_f32 v[80:81], v[176:177], s[90:91], v[80:81]\n\t"
"v_pk_fma_f32 v[82:83], v[178:179], s[90:91], v[82:83]\n\t"
"v_pk_add_f32 v[176:177], v[80:81], v[176:177] neg_lo:[0,1] neg_hi:[0,1]\n\t"
"v_pk_add_f32 v[178:179], v[82:83], v[178:179] neg_lo:[0,1] neg_hi:[0,1]\n\t"
"v_pk_fma_f32 v[84:85], v[180:181], s[90:91], v[84:85]\n\t"
"v_pk_fma_f32 v[86:87], v[182:183], s[90:91], v[86:87]\n\t"
"v_pk_add_f32 v[180:181], v[84:85], v[180:181] neg_lo:[0,1] neg_hi:[0,1]\n\t"
"v_pk_add_f32 v[182:183], v[86:87], v[182:183] neg_lo:[0,1] neg_hi:[0,1]\n\t"
"v_pk_fma_f32 v[68:69], v[164:165], s[90:91], v[68:69]\n\t"
"v_pk_fma_f32 v[70:71], v[166:167], s[90:91], v[70:71]\n\t"
"v_pk_add_f32 v[164:165], v[68:69], v[164:165] neg_lo:[0,1] neg_hi:[0,1]\n\t"
"v_pk_add_f32 v[166:167], v[70:71], v[166:167] neg_lo:[0,1] neg_hi:[0,1]\n\t"
"v_pk_fma_f32 v[52:53], v[148:149], s[90:91], v[52:53]\n\t"
"v_pk_fma_f32 v[54:55], v[150:151], s[90:91], v[54:55]\n\t"
"v_pk_add_f32 v[148:149], v[52:53], v[148:149] neg_lo:[0,1] neg_hi:[0,1]\n\t"
"v_pk_add_f32 v[150:151], v[54:55], v[150:151] neg_lo:[0,1] neg_hi:[0,1]\n\t"
"v_pk_fma_f32 v[56:57], v[152:153], s[90:91], v[56:57]\n\t"
"v_pk_fma_f32 v[58:59], v[154:155], s[90:91], v[58:59]\n\t"
"v_pk_add_f32 v[152:153], v[56:57], v[152:153] neg_lo:[0,1] neg_hi:[0,1]\n\t"
"v_pk_add_f32 v[154:155], v[58:59], v[154:155] neg_lo:[0,1] neg_hi:[0,1]\n\t"
"v_pk_fma_f32 v[72:73], v[168:169], s[90:91], v[72:73]\n\t"
"v_pk_fma_f32 v[74:75], v[170:171], s[90:91], v[74:75]\n\t"
"v_pk_add_f32 v[168:169], v[72:73], v[168:169] neg_lo:[0,1] neg_hi:[0,1]\n\t"
"v_pk_add_f32 v[170:171], v[74:75], v[170:171] neg_lo:[0,1] neg_hi:[0,1]\n\t"
"v_pk_fma_f32 v[88:89], v[184:185], s[90:91], v[88:89]\n\t"
"v_pk_fma_f32 v[90:91], v[186:187], s[90:91], v[90:91]\n\t"
"v_pk_add_f32 v[184:185], v[88:89], v[184:185] neg_lo:[0,1] neg_hi:[0,1]\n\t"
"v_pk_add_f32 v[186:187], v[90:91], v[186:187] neg_lo:[0,1] neg_hi:[0,1]\n\t"
"v_pk_fma_f32 v[92:93], v[188:189], s[90:91], v[92:93]\n\t"
"v_pk_fma_f32 v[94:95], v[190:191], s[90:91], v[94:95]\n\t"
"v_pk_add_f32 v[188:189], v[92:93], v[188:189] neg_lo:[0,1] neg_hi:[0,1]\n\t"
"v_pk_add_f32 v[190:191], v[94:95], v[190:191] neg_lo:[0,1] neg_hi:[0,1]\n\t"
"v_pk_fma_f32 v[76:77], v[172:173], s[90:91], v[76:77]\n\t"
"v_pk_fma_f32 v[78:79], v[174:175], s[90:91], v[78:79]\n\t"
"v_pk_add_f32 v[172:173], v[76:77], v[172:173] neg_lo:[0,1] neg_hi:[0,1]\n\t"
"v_pk_add_f32 v[174:175], v[78:79], v[174:175] neg_lo:[0,1] neg_hi:[0,1]\n\t"
"v_pk_fma_f32 v[60:61], v[156:157], s[90:91], v[60:61]\n\t"
"v_pk_fma_f32 v[62:63], v[158:159], s[90:91], v[62:63]\n\t"
"v_pk_add_f32 v[156:157], v[60:61], v[156:157] neg_lo:[0,1] neg_hi:[0,1]\n\t"
"v_pk_add_f32 v[158:159], v[62:63], v[158:159] neg_lo:[0,1] neg_hi:[0,1]\n\t"
"v_mfma_f32_16x16x32_bf16 v[208:211], a[224:227], a[192:195], 0\n\t"
"buffer_load_dwordx4 a[72:75], off, s[84:87], s51 offset:0 sc1\n\t"
"v_mfma_f32_16x16x32_bf16 v[212:215], a[224:227], a[196:199], 0\n\t"
"ds_read_b128 a[172:175], v224 offset:2816\n\t"
"v_mfma_f32_16x16x32_bf16 v[216:219], a[224:227], a[200:203], 0\n\t"
"buffer_load_dwordx4 a[76:79], off, s[84:87], s51 offset:1024 sc1\n\t"
"v_mfma_f32_16x16x32_bf16 v[220:223], a[224:227], a[204:207], 0\n\t"
"s_waitcnt vmcnt(6) lgkmcnt(3)\n\t"
"v_cvt_pk_bf16_f32 v193, v193, v197\n\t"
"v_cvt_pk_bf16_f32 v194, v194, v198\n\t"
"v_mfma_f32_16x16x32_bf16 v[48:51], a[48:51], a[144:147], v[48:51]\n\t"
"ds_write_addtid_b32 v194 offset:20672\n\t"
"v_cvt_pk_bf16_f32 v195, v195, v199\n\t"
"v_mfma_f32_16x16x32_bf16 v[48:51], a[52:55], a[160:163], v[48:51]\n\t"
"ds_write_addtid_b32 v193 offset:24864\n\t"
"v_cvt_pk_bf16_f32 v201, v201, v205\n\t"
"v_mfma_f32_16x16x32_bf16 v[64:67], a[56:59], a[144:147], v[64:67]\n\t"
"ds_write_addtid_b32 v195 offset:28992\n\t"
"v_cvt_pk_bf16_f32 v192, v192, v196\n\t"
"v_mfma_f32_16x16x32_bf16 v[64:67], a[60:63], a[160:163], v[64:67]\n\t"
"ds_write_addtid_b32 v192 offset:16544\n\t"
"v_cvt_pk_bf16_f32 v209, v209, v213\n\t"
"v_mfma_f32_16x16x32_bf16 v[80:83], a[64:67], a[144:147], v[80:83]\n\t"
"ds_read_b128 a[144:147], v224 offset:1024\n\t"
"v_cvt_pk_bf16_f32 v200, v200, v204\n\t"
"v_mfma_f32_16x16x32_bf16 v[80:83], a[68:71], a[160:163], v[80:83]\n\t"
"ds_write_addtid_b32 v201 offset:26912\n\t"
"v_cvt_pk_bf16_f32 v48, v48, v49\n\t"
"v_mfma_f32_16x16x32_bf16 v[84:87], a[64:67], a[148:151], v[84:87]\n\t"
"ds_read_b128 a[160:163], v224 offset:3072\n\t"
"v_cvt_pk_bf16_f32 v218, v218, v222\n\t"
"v_mfma_f32_16x16x32_bf16 v[84:87], a[68:71], a[164:167], v[84:87]\n\t"
"ds_write_addtid_b32 v209 offset:25888\n\t"
"v_cvt_pk_bf16_f32 v216, v216, v220\n\t"
"v_cvt_pk_bf16_f32 v49, v50, v51\n\t"
"v_mfma_f32_16x16x32_bf16 v[68:71], a[56:59], a[148:151], v[68:71]\n\t"
"ds_write_addtid_b32 v218 offset:23744\n\t"
"v_cvt_pk_bf16_f32 v202, v202, v206\n\t"
"v_mfma_f32_16x16x32_bf16 v[68:71], a[60:63], a[164:167], v[68:71]\n\t"
"ds_write_addtid_b32 v200 offset:18592\n\t"
"v_cvt_pk_bf16_f32 v219, v219, v223\n\t"
"v_mfma_f32_16x16x32_bf16 v[52:55], a[48:51], a[148:151], v[52:55]\n\t"
"ds_read_b128 a[148:151], v224 offset:1280\n\t"
"v_cvt_pk_bf16_f32 v80, v80, v81\n\t"
"v_mfma_f32_16x16x32_bf16 v[52:55], a[52:55], a[164:167], v[52:55]\n\t"
"s_waitcnt lgkmcnt(9)\n\t"
"ds_read_b128 a[164:167], v224 offset:3328\n\t"
"v_cvt_pk_bf16_f32 v208, v208, v212\n\t"
"v_cvt_pk_bf16_f32 v210, v210, v214\n\t"
"v_cvt_pk_bf16_f32 v211, v211, v215\n\t"
"v_cvt_pk_bf16_f32 v217, v217, v221\n\t"
"v_mfma_f32_16x16x32_bf16 v[56:59], a[48:51], a[152:155], v[56:59]\n\t"
"ds_write_addtid_b32 v211 offset:30016\n\t"
"v_cvt_pk_bf16_f32 v203, v203, v207\n\t"
"v_mfma_f32_16x16x32_bf16 v[56:59], a[52:55], a[168:171], v[56:59]\n\t"
"s_nop 0\n\t"
"ds_write_addtid_b32 v202 offset:22720\n\t"
"ds_write_addtid_b32 v203 offset:31040\n\t"
"ds_write_addtid_b32 v208 offset:17568\n\t"
"ds_write_addtid_b32 v216 offset:19616\n\t"
"buffer_load_dwordx4 a[112:115], off, s[28:31], s81 offset:0 sc1\n\t"
"buffer_load_dwordx4 a[116:119], off, s[28:31], s81 offset:1024 sc1\n\t"
"v_cvt_pk_bf16_f32 v84, v84, v85\n\t"
"v_mfma_f32_16x16x32_bf16 v[72:75], a[56:59], a[152:155], v[72:75]\n\t"
"ds_write_addtid_b32 v219 offset:32064\n\t"
"v_cvt_pk_bf16_f32 v81, v82, v83\n\t"
"v_mfma_f32_16x16x32_bf16 v[72:75], a[60:63], a[168:171], v[72:75]\n\t"
"ds_write_addtid_b32 v217 offset:27936\n\t"
"v_cvt_pk_bf16_f32 v52, v52, v53\n\t"
"v_cvt_pk_bf16_f32 v68, v68, v69\n\t"
"v_mfma_f32_16x16x32_bf16 v[88:91], a[64:67], a[152:155], v[88:91]\n\t"
"ds_read_b128 a[152:155], v224 offset:1536\n\t"
"v_cvt_pk_bf16_f32 v53, v54, v55\n\t"
"v_mfma_f32_16x16x32_bf16 v[88:91], a[68:71], a[168:171], v[88:91]\n\t"
"ds_read_b128 a[168:171], v224 offset:3584\n\t"
"buffer_load_dwordx4 a[108:111], off, s[84:87], s81 offset:3072 sc1\n\t"
"buffer_load_dwordx4 a[104:107], off, s[84:87], s81 offset:2048 sc1\n\t"
"v_mfma_f32_16x16x32_bf16 v[92:95], a[64:67], a[156:159], v[92:95]\n\t"
"v_cvt_pk_bf16_f32 v69, v70, v71\n\t"
"v_cvt_pk_bf16_f32 v85, v86, v87\n\t"
"v_mfma_f32_16x16x32_bf16 v[92:95], a[68:71], a[172:175], v[92:95]\n\t"
"ds_write_addtid_b32 v210 offset:21696\n\t"
"ds_write2st64_b64 v247, v[80:81], v[84:85] offset0:0 offset1:13\n\t"
"v_mfma_f32_16x16x32_bf16 v[76:79], a[56:59], a[156:159], v[76:79]\n\t"
"v_cvt_pk_bf16_f32 v64, v64, v65\n\t"
"v_cvt_pk_bf16_f32 v88, v88, v89\n\t"
"v_mfma_f32_16x16x32_bf16 v[76:79], a[60:63], a[172:175], v[76:79]\n\t"
"buffer_load_dwordx4 a[96:99], off, s[84:87], s81 offset:0 sc1\n\t"
"buffer_load_dwordx4 a[100:103], off, s[84:87], s81 offset:1024 sc1\n\t"
"v_cvt_pk_bf16_f32 v89, v90, v91\n\t"
"v_mfma_f32_16x16x32_bf16 v[60:63], a[48:51], a[156:159], v[60:63]\n\t"
"v_cvt_pk_bf16_f32 v65, v66, v67\n\t"
"v_cvt_pk_bf16_f32 v72, v72, v73\n\t"
"v_mfma_f32_16x16x32_bf16 v[60:63], a[52:55], a[172:175], v[60:63]\n\t"
"s_waitcnt vmcnt(6) lgkmcnt(6)\n\t"
"ds_read_b128 a[156:159], v224 offset:1792\n\t"
"ds_read_b128 a[172:175], v224 offset:3840\n\t"
"ds_write2st64_b64 v228, v[48:49], v[52:53] offset0:0 offset1:13\n\t"
"v_cvt_pk_bf16_f32 v73, v74, v75\n\t"
"v_mfma_f32_16x16x32_bf16 v[144:147], a[72:75], a[144:147], v[144:147]\n\t"
"v_cvt_pk_bf16_f32 v56, v56, v57\n\t"
"v_cvt_pk_bf16_f32 v92, v92, v93\n\t"
"v_mfma_f32_16x16x32_bf16 v[144:147], a[76:79], a[160:163], v[144:147]\n\t"
"buffer_load_dwordx4 a[136:139], off, s[28:31], s66 offset:0 sc1\n\t"
"buffer_load_dwordx4 a[140:143], off, s[28:31], s66 offset:1024 sc1\n\t"
"ds_write2st64_b64 v246, v[64:65], v[68:69] offset0:0 offset1:13\n\t"
"v_cvt_pk_bf16_f32 v57, v58, v59\n\t"
"v_cvt_pk_bf16_f32 v93, v94, v95\n\t"
"v_mfma_f32_16x16x32_bf16 v[160:163], a[80:83], a[144:147], v[160:163]\n\t"
"v_cvt_pk_bf16_f32 v60, v60, v61\n\t"
"v_cvt_pk_bf16_f32 v76, v76, v77\n\t"
"v_mfma_f32_16x16x32_bf16 v[160:163], a[84:87], a[160:163], v[160:163]\n\t"
"buffer_load_dwordx4 a[132:135], off, s[84:87], s66 offset:3072 sc1\n\t"
"ds_write2st64_b64 v247, v[88:89], v[92:93] offset0:26 offset1:39\n\t"
"v_mfma_f32_16x16x32_bf16 v[176:179], a[88:91], a[144:147], v[176:179]\n\t"
"v_cvt_pk_bf16_f32 v61, v62, v63\n\t"
"v_cvt_pk_bf16_f32 v77, v78, v79\n\t"
"v_mfma_f32_16x16x32_bf16 v[176:179], a[92:95], a[160:163], v[176:179]\n\t"
"ds_write2st64_b64 v228, v[56:57], v[60:61] offset0:26 offset1:39\n\t"
"v_mfma_f32_16x16x32_bf16 v[180:183], a[88:91], a[148:151], v[180:183]\n\t"
"buffer_load_dwordx4 a[128:131], off, s[84:87], s66 offset:2048 sc1\n\t"
"v_mfma_f32_16x16x32_bf16 v[180:183], a[92:95], a[164:167], v[180:183]\n\t"
"ds_write2st64_b64 v246, v[72:73], v[76:77] offset0:26 offset1:39\n\t"
"v_mfma_f32_16x16x32_bf16 v[164:167], a[80:83], a[148:151], v[164:167]\n\t"
"buffer_load_dwordx4 a[120:123], off, s[84:87], s66 offset:0 sc1\n\t"
"v_mfma_f32_16x16x32_bf16 v[164:167], a[84:87], a[164:167], v[164:167]\n\t"
"v_mfma_f32_16x16x32_bf16 v[148:151], a[72:75], a[148:151], v[148:151]\n\t"
"buffer_load_dwordx4 a[124:127], off, s[84:87], s66 offset:1024 sc1\n\t"
"v_mfma_f32_16x16x32_bf16 v[148:151], a[76:79], a[164:167], v[148:151]\n\t"
"s_waitcnt lgkmcnt(0)\n\t"
"s_barrier\n\t"
"v_mfma_f32_16x16x32_bf16 v[152:155], a[72:75], a[152:155], v[152:155]\n\t"
"s_nop 0\n\t"
"ds_read_b128 a[148:151], v224 offset:16800\n\t"
"ds_read_b128 a[12:15], v229 offset:13312\n\t"
"v_mfma_f32_16x16x32_bf16 v[152:155], a[76:79], a[168:171], v[152:155]\n\t"
"s_nop 0\n\t"
"ds_read_b128 a[144:147], v224 offset:16544\n\t"
"ds_read_b128 a[160:163], v224 offset:18592\n\t"
"v_mfma_f32_16x16x32_bf16 v[168:171], a[80:83], a[152:155], v[168:171]\n\t"
"ds_read_b128 a[0:3], v229 offset:0\n\t"
"v_mfma_f32_16x16x32_bf16 v[168:171], a[84:87], a[168:171], v[168:171]\n\t"
"ds_read_b128 a[164:167], v224 offset:18848\n\t"
"v_mfma_f32_16x16x32_bf16 v[184:187], a[88:91], a[152:155], v[184:187]\n\t"
"s_nop 0\n\t"
"ds_read_b128 a[4:7], v230 offset:0\n\t"
"ds_read_b128 a[16:19], v230 offset:13312\n\t"
"v_mfma_f32_16x16x32_bf16 v[184:187], a[92:95], a[168:171], v[184:187]\n\t"
"s_nop 0\n\t"
"ds_read_b128 a[8:11], v231 offset:0\n\t"
"ds_read_b128 a[20:23], v231 offset:13312\n\t"
"v_mfma_f32_16x16x32_bf16 v[188:191], a[88:91], a[156:159], v[188:191]\n\t"
"s_waitcnt lgkmcnt(8)\n\t"
"buffer_store_dwordx4 a[12:15], v232, s[16:19], s27 offen nt\n\t"
"v_mfma_f32_16x16x32_bf16 v[188:191], a[92:95], a[172:175], v[188:191]\n\t"
"ds_read_b128 a[152:155], v224 offset:17568\n\t"
"v_mfma_f32_16x16x32_bf16 v[172:175], a[80:83], a[156:159], v[172:175]\n\t"
"ds_read_b128 a[168:171], v224 offset:19616\n\t"
"v_mfma_f32_16x16x32_bf16 v[172:175], a[84:87], a[172:175], v[172:175]\n\t"
"s_waitcnt lgkmcnt(7)\n\t"
"buffer_store_dwordx4 a[0:3], v232, s[16:19], s24 offen nt\n\t"
"v_mfma_f32_16x16x32_bf16 v[156:159], a[72:75], a[156:159], v[156:159]\n\t"
"v_mfma_f32_16x16x32_bf16 v[156:159], a[76:79], a[172:175], v[156:159]\n\t"
"s_waitcnt vmcnt(8)\n\t"
"s_waitcnt lgkmcnt(5)\n\t"
"buffer_store_dwordx4 a[4:7], v233, s[16:19], s24 offen nt\n\t"
"v_mfma_f32_16x16x32_bf16 v[144:147], a[96:99], a[144:147], v[144:147]\n\t"
"ds_read_b128 a[156:159], v224 offset:17824\n\t"
"v_mfma_f32_16x16x32_bf16 v[144:147], a[100:103], a[160:163], v[144:147]\n\t"
"s_waitcnt lgkmcnt(5)\n\t"
"buffer_store_dwordx4 a[16:19], v233, s[16:19], s27 offen nt\n\t"
"v_mfma_f32_16x16x32_bf16 v[160:163], a[104:107], a[144:147], v[160:163]\n\t"
"ds_read_b128 a[172:175], v224 offset:19872\n\t"
"v_mfma_f32_16x16x32_bf16 v[160:163], a[108:111], a[160:163], v[160:163]\n\t"
"s_waitcnt lgkmcnt(4)\n\t"
"buffer_store_dwordx4 a[8:11], v234, s[16:19], s24 offen nt\n\t"
"v_mfma_f32_16x16x32_bf16 v[176:179], a[112:115], a[144:147], v[176:179]\n\t"
"ds_read_b128 a[144:147], v224 offset:17056\n\t"
"v_mfma_f32_16x16x32_bf16 v[176:179], a[116:119], a[160:163], v[176:179]\n\t"
"v_mfma_f32_16x16x32_bf16 v[180:183], a[112:115], a[148:151], v[180:183]\n\t"
"buffer_store_dwordx4 a[20:23], v234, s[16:19], s27 offen nt\n\t"
"v_mfma_f32_16x16x32_bf16 v[180:183], a[116:119], a[164:167], v[180:183]\n\t"
"ds_read_b128 a[160:163], v224 offset:19104\n\t"
"v_mfma_f32_16x16x32_bf16 v[164:167], a[104:107], a[148:151], v[164:167]\n\t"
"v_mfma_f32_16x16x32_bf16 v[164:167], a[108:111], a[164:167], v[164:167]\n\t"
"v_mfma_f32_16x16x32_bf16 v[148:151], a[96:99], a[148:151], v[148:151]\n\t"
"ds_read_b128 a[148:151], v224 offset:17312\n\t"
"v_mfma_f32_16x16x32_bf16 v[148:151], a[100:103], a[164:167], v[148:151]\n\t"
"s_waitcnt vmcnt(6) lgkmcnt(4)\n\t"
"v_mfma_f32_16x16x32_bf16 v[144:147], a[120:123], a[152:155], v[144:147]\n\t"
"ds_read_b128 a[164:167], v224 offset:19360\n\t"
"v_mfma_f32_16x16x32_bf16 v[144:147], a[124:127], a[168:171], v[144:147]\n\t"
"v_mfma_f32_16x16x32_bf16 v[160:163], a[128:131], a[152:155], v[160:163]\n\t"
"v_mfma_f32_16x16x32_bf16 v[160:163], a[132:135], a[168:171], v[160:163]\n\t"
"v_mfma_f32_16x16x32_bf16 v[176:179], a[136:139], a[152:155], v[176:179]\n\t"
"ds_read_b128 a[152:155], v224 offset:18080\n\t"
"v_mfma_f32_16x16x32_bf16 v[176:179], a[140:143], a[168:171], v[176:179]\n\t"
"s_waitcnt lgkmcnt(3)\n\t"
"s_nop 1\n\t"
"v_cvt_pk_bf16_f32 v144, v144, v145\n\t"
"v_cvt_pk_bf16_f32 v145, v146, v147\n\t"
"v_mfma_f32_16x16x32_bf16 v[180:183], a[136:139], a[156:159], v[180:183]\n\t"
"ds_read_b128 a[168:171], v224 offset:20128\n\t"
"v_mfma_f32_16x16x32_bf16 v[180:183], a[140:143], a[172:175], v[180:183]\n\t"
"v_mfma_f32_16x16x32_bf16 v[164:167], a[128:131], a[156:159], v[164:167]\n\t"
"v_cvt_pk_bf16_f32 v160, v160, v161\n\t"
"v_cvt_pk_bf16_f32 v176, v176, v177\n\t"
"v_mfma_f32_16x16x32_bf16 v[164:167], a[132:135], a[172:175], v[164:167]\n\t"
"v_cvt_pk_bf16_f32 v161, v162, v163\n\t"
"v_mfma_f32_16x16x32_bf16 v[148:151], a[120:123], a[156:159], v[148:151]\n\t"
"ds_read_b128 a[156:159], v224 offset:18336\n\t"
"s_nop 0\n\t"
"v_cvt_pk_bf16_f32 v180, v180, v181\n\t"
"v_mfma_f32_16x16x32_bf16 v[148:151], a[124:127], a[172:175], v[148:151]\n\t"
"ds_read_b128 a[172:175], v224 offset:20384\n\t"
"v_cvt_pk_bf16_f32 v181, v182, v183\n\t"
"v_mfma_f32_16x16x32_bf16 v[152:155], a[96:99], a[144:147], v[152:155]\n\t"
"v_cvt_pk_bf16_f32 v177, v178, v179\n\t"
"v_mfma_f32_16x16x32_bf16 v[152:155], a[100:103], a[160:163], v[152:155]\n\t"
"ds_write2st64_b64 v247, v[176:177], v[180:181] offset0:52 offset1:65\n\t"
"v_mfma_f32_16x16x32_bf16 v[168:171], a[104:107], a[144:147], v[168:171]\n\t"
"v_cvt_pk_bf16_f32 v164, v164, v165\n\t"
"v_cvt_pk_bf16_f32 v165, v166, v167\n\t"
"v_mfma_f32_16x16x32_bf16 v[168:171], a[108:111], a[160:163], v[168:171]\n\t"
"v_cvt_pk_bf16_f32 v148, v148, v149\n\t"
"v_cvt_pk_bf16_f32 v149, v150, v151\n\t"
"v_mfma_f32_16x16x32_bf16 v[184:187], a[112:115], a[144:147], v[184:187]\n\t"
"ds_write2st64_b64 v228, v[144:145], v[148:149] offset0:52 offset1:65\n\t"
"v_mfma_f32_16x16x32_bf16 v[184:187], a[116:119], a[160:163], v[184:187]\n\t"
"s_waitcnt lgkmcnt(4)\n\t"
"v_mfma_f32_16x16x32_bf16 v[188:191], a[112:115], a[148:151], v[188:191]\n\t"
"v_mfma_f32_16x16x32_bf16 v[188:191], a[116:119], a[164:167], v[188:191]\n\t"
"ds_write2st64_b64 v246, v[160:161], v[164:165] offset0:52 offset1:65\n\t"
"v_mfma_f32_16x16x32_bf16 v[172:175], a[104:107], a[148:151], v[172:175]\n\t"
"v_mfma_f32_16x16x32_bf16 v[172:175], a[108:111], a[164:167], v[172:175]\n\t"
"v_mfma_f32_16x16x32_bf16 v[156:159], a[96:99], a[148:151], v[156:159]\n\t"
"v_mfma_f32_16x16x32_bf16 v[156:159], a[100:103], a[164:167], v[156:159]\n\t"
"s_waitcnt lgkmcnt(0)\n\t"
"s_barrier\n\t"
"v_mfma_f32_16x16x32_bf16 v[152:155], a[120:123], a[152:155], v[152:155]\n\t"
"ds_read_b128 a[32:35], v231 offset:26624\n\t"
"v_mfma_f32_16x16x32_bf16 v[152:155], a[124:127], a[168:171], v[152:155]\n\t"
"ds_read_b128 a[24:27], v229 offset:26624\n\t"
"v_mfma_f32_16x16x32_bf16 v[168:171], a[128:131], a[152:155], v[168:171]\n\t"
"v_mfma_f32_16x16x32_bf16 v[168:171], a[132:135], a[168:171], v[168:171]\n\t"
"v_mfma_f32_16x16x32_bf16 v[184:187], a[136:139], a[152:155], v[184:187]\n\t"
"ds_read_b128 a[28:31], v230 offset:26624\n\t"
"s_nop 2\n\t"
"v_cvt_pk_bf16_f32 v152, v152, v153\n\t"
"v_mfma_f32_16x16x32_bf16 v[184:187], a[140:143], a[168:171], v[184:187]\n\t"
"v_cvt_pk_bf16_f32 v153, v154, v155\n\t"
"v_mfma_f32_16x16x32_bf16 v[188:191], a[136:139], a[156:159], v[188:191]\n\t"
"s_waitcnt lgkmcnt(2)\n\t"
"buffer_store_dwordx4 a[32:35], v234, s[16:19], s20 offen nt\n\t"
"v_mfma_f32_16x16x32_bf16 v[188:191], a[140:143], a[172:175], v[188:191]\n\t"
"v_cvt_pk_bf16_f32 v168, v168, v169\n\t"
"v_cvt_pk_bf16_f32 v169, v170, v171\n\t"
"v_mfma_f32_16x16x32_bf16 v[172:175], a[128:131], a[156:159], v[172:175]\n\t"
"v_cvt_pk_bf16_f32 v184, v184, v185\n\t"
"v_cvt_pk_bf16_f32 v185, v186, v187\n\t"
"v_mfma_f32_16x16x32_bf16 v[172:175], a[132:135], a[172:175], v[172:175]\n\t"
"s_waitcnt lgkmcnt(1)\n\t"
"buffer_store_dwordx4 a[24:27], v232, s[16:19], s20 offen nt\n\t"
"v_mfma_f32_16x16x32_bf16 v[156:159], a[120:123], a[156:159], v[156:159]\n\t"
"v_cvt_pk_bf16_f32 v188, v188, v189\n\t"
"v_cvt_pk_bf16_f32 v189, v190, v191\n\t"
"v_mfma_f32_16x16x32_bf16 v[156:159], a[124:127], a[172:175], v[156:159]\n\t"
"s_waitcnt lgkmcnt(0)\n\t"
"buffer_store_dwordx4 a[28:31], v233, s[16:19], s20 offen nt\n\t"
"s_nop 5\n\t"
"v_cvt_pk_bf16_f32 v156, v156, v157\n\t"
"v_cvt_pk_bf16_f32 v157, v158, v159\n\t"
"ds_write2st64_b64 v228, v[152:153], v[156:157] offset0:78 offset1:91\n\t"
"v_cvt_pk_bf16_f32 v172, v172, v173\n\t"
"v_cvt_pk_bf16_f32 v173, v174, v175\n\t"
"ds_write2st64_b64 v246, v[168:169], v[172:173] offset0:78 offset1:91\n\t"
"ds_write2st64_b64 v247, v[184:185], v[188:189] offset0:78 offset1:91\n\t"
"s_waitcnt lgkmcnt(0)\n\t"
"s_barrier\n\t"
"ds_read_b128 a[36:39], v229 offset:39936\n\t"
"ds_read_b128 a[40:43], v230 offset:39936\n\t"
"ds_read_b128 a[44:47], v231 offset:39936\n\t"
"s_waitcnt lgkmcnt(2)\n\t"
"buffer_store_dwordx4 a[36:39], v232, s[16:19], s25 offen nt\n\t"
"s_waitcnt lgkmcnt(1)\n\t"
"buffer_store_dwordx4 a[40:43], v233, s[16:19], s25 offen nt\n\t"
"s_waitcnt lgkmcnt(0)\n\t"
"buffer_store_dwordx4 a[44:47], v234, s[16:19], s25 offen nt\n\t"
"s_waitcnt vmcnt(0)\n\t"
"Lwino_end:\n\t"
  : "+{v205}"(tid)
  : "s"(__builtin_amdgcn_kernarg_segment_ptr()), "s"(blockIdx.x), "s"((unsigned)(size_t)(__attribute__((address_space(3))) char*)smem)
  : "v0", "v1", "v2", "v3", "v4", "v5", "v6", "v7", "v8", "v9", "v10", "v11", "v12", "v13", "v14", "v15", "v16", "v17", "v18", "v19", "v20", "v21", "v22", "v23", "v24", "v25", "v26", "v27", "v28", "v29", "v30", "v31", "v32", "v33", "v34", "v35", "v36", "v37", "v38", "v39", "v40", "v41", "v42", "v43", "v44", "v45", "v46", "v47", "v48", "v49", "v50", "v51", "v52", "v53", "v54", "v55", "v56", "v57", "v58", "v59", "v60", "v61", "v62", "v63", "v64", "v65", "v66", "v67", "v68", "v69", "v70", "v71", "v72", "v73", "v74", "v75", "v76", "v77", "v78", "v79", "v80", "v81", "v82", "v83", "v84", "v85", "v86", "v87", "v88", "v89", "v90", "v91", "v92", "v93", "v94", "v95", "v96", "v97", "v98", "v99", "v100", "v101", "v102", "v103", "v104", "v105", "v106", "v107", "v108", "v109", "v110", "v111", "v112", "v113", "v114", "v115", "v116", "v117", "v118", "v119", "v120", "v121", "v122", "v123", "v124", "v125", "v126", "v127", "v128", "v129", "v130", "v131", "v132", "v133", "v134", "v135", "v136", "v137", "v138", "v139", "v140", "v141", "v142", "v143", "v144", "v145", "v146", "v147", "v148", "v149", "v150", "v151", "v152", "v153", "v154", "v155", "v156", "v157", "v158", "v159", "v160", "v161", "v162", "v163", "v164", "v165", "v166", "v167", "v168", "v169", "v170", "v171", "v172", "v173", "v174", "v175", "v176", "v177", "v178", "v179", "v180", "v181", "v182", "v183", "v184", "v185", "v186", "v187", "v188", "v189", "v190", "v191", "v192", "v193", "v194", "v195", "v196", "v197", "v198", "v199", "v200", "v201", "v202", "v203", "v204", "v206", "v207", "v208", "v209", "v210", "v211", "v212", "v213", "v214", "v215", "v216", "v217", "v218", "v219", "v220", "v221", "v222", "v223", "v224", "v225", "v226", "v227", "v228", "v229", "v230", "v231", "v232", "v233", "v234", "v235", "v236", "v237", "v238", "v239", "v240", "v241", "v242", "v243", "v244", "v245", "v246", "v247", "v248", "v249", "v250", "v251", "v252", "v253", "v254", "v255", "a0", "a1", "a2", "a3", "a4", "a5", "a6", "a7", "a8", "a9", "a10", "a11", "a12", "a13", "a14", "a15", "a16", "a17", "a18", "a19", "a20", "a21", "a22", "a23", "a24", "a25", "a26", "a27", "a28", "a29", "a30", "a31", "a32", "a33", "a34", "a35", "a36", "a37", "a38", "a39", "a40", "a41", "a42", "a43", "a44", "a45", "a46", "a47", "a48", "a49", "a50", "a51", "a52", "a53", "a54", "a55", "a56", "a57", "a58", "a59", "a60", "a61", "a62", "a63", "a64", "a65", "a66", "a67", "a68", "a69", "a70", "a71", "a72", "a73", "a74", "a75", "a76", "a77", "a78", "a79", "a80", "a81", "a82", "a83", "a84", "a85", "a86", "a87", "a88", "a89", "a90", "a91", "a92", "a93", "a94", "a95", "a96", "a97", "a98", "a99", "a100", "a101", "a102", "a103", "a104", "a105", "a106", "a107", "a108", "a109", "a110", "a111", "a112", "a113", "a114", "a115", "a116", "a117", "a118", "a119", "a120", "a121", "a122", "a123", "a124", "a125", "a126", "a127", "a128", "a129", "a130", "a131", "a132", "a133", "a134", "a135", "a136", "a137", "a138", "a139", "a140", "a141", "a142", "a143", "a144", "a145", "a146", "a147", "a148", "a149", "a150", "a151", "a152", "a153", "a154", "a155", "a156", "a157", "a158", "a159", "a160", "a161", "a162", "a163", "a164", "a165", "a166", "a167", "a168", "a169", "a170", "a171", "a172", "a173", "a174", "a175", "a176", "a177", "a178", "a179", "a180", "a181", "a182", "a183", "a184", "a185", "a186", "a187", "a188", "a189", "a190", "a191", "a192", "a193", "a194", "a195", "a196", "a197", "a198", "a199", "a200", "a201", "a202", "a203", "a204", "a205", "a206", "a207", "a208", "a209", "a210", "a211", "a212", "a213", "a214", "a215", "a216", "a217", "a218", "a219", "a220", "a221", "a222", "a223", "a224", "a225", "a226", "a227", "a228", "a229", "a230", "a231", "a232", "a233", "a234", "a235", "a236", "a237", "a238", "a239", "a240", "a241", "a242", "a243", "a244", "a245", "a246", "a247", "a248", "a249", "a250", "a251", "a252", "a253", "a254", "a255", "s6", "s7", "s8", "s9", "s10", "s11", "s12", "s13", "s14", "s15", "s16", "s17", "s18", "s19", "s20", "s21", "s22", "s23", "s24", "s25", "s26", "s27", "s28", "s29", "s30", "s31", "s32", "s33", "s34", "s35", "s36", "s37", "s38", "s39", "s40", "s41", "s42", "s43", "s44", "s45", "s46", "s47", "s48", "s49", "s50", "s51", "s52", "s53", "s54", "s55", "s56", "s57", "s58", "s59", "s60", "s61", "s62", "s63", "s64", "s65", "s66", "s67", "s68", "s69", "s70", "s71", "s72", "s73", "s74", "s75", "s76", "s77", "s78", "s79", "s80", "s81", "s82", "s83", "s84", "s85", "s86", "s87", "s88", "s89", "s90", "s91", "s92", "s93", "s94", "s95", "s96", "s97", "s98", "s99", "scc", "vcc", "memory");
}

extern "C" void kernel_launch(void* const* d_in, const int* in_sizes, int n_in, void* d_out, int out_size, void* d_ws,
                              size_t ws_size, hipStream_t stream) {
  wino_main_kernel<<<256, 256, 0, stream>>>((const char*)d_in[0], (char*)d_ws, (char*)d_out, (const char*)d_in[1]);
}
```

```cpp
#include <hip/hip_runtime.h>
#include <hip/hip_bf16.h>
#include <stdint.h>

#define U_BYTES 1179648

__global__ __launch_bounds__(256, 1) __attribute__((section(".text.w"))) void wino_main_kernel(const char* __restrict__ x, char* __restrict__ Uq, char* __restrict__ out, const char* __restrict__ wt) {
  __shared__ __attribute__((aligned(1024))) char smem[163840];
  unsigned tid = threadIdx.x;
  asm volatile(
".pushsection .text\n\t"
".p2align 12\n\t"
".fill 576, 4, 0xbf800000\n\t"
".popsection\n\t"
".pushsection .text.x,\"ax\"\n\t"
".fill 256, 4, 0xbf800000\n\t"
".popsection\n\t"
"s_load_dwordx8 s[76:83], %1, 0x0\n\t"
"s_mov_b32 s66, %2\n\t"
"s_getpc_b64 s[6:7]\n\t"
"Lwino_pc:\n\t"
"s_bfe_u32 s42, %2, 0x50003\n\t"
"s_lshl_b32 s42, s42, 12\n\t"
"v_lshl_add_u32 v203, v205, 4, s42\n\t"
"v_min_u32 v203, (Lwino_end-Lwino_pc-16)&-16, v203\n\t"
"global_load_dwordx4 v[196:199], v203, s[6:7]\n\t"
"v_and_b32 v204, 63, v205\n\t"
"v_readfirstlane_b32 s73, v205\n\t"
"v_lshrrev_b32 v120, 3, v204\n\t"
"v_and_b32 v121, 7, v204\n\t"
"s_lshr_b32 s73, s73, 6\n\t"
"s_lshr_b32 s56, s66, 5\n\t"
"s_mul_i32 s56, s56, 12288\n\t"
"s_bfe_u32 s61, s66, 0x20001\n\t"
"s_mul_i32 s61, s61, 1536\n\t"
"s_add_u32 s56, s56, s61\n\t"
"s_bitcmp1_b32 s66, 0\n\t"
"s_cselect_b32 s61, 48, 0\n\t"
"s_add_u32 s56, s56, s61\n\t"
"s_bfe_u32 s62, s66, 0x20003\n\t"
"s_mul_i32 s61, s62, 96\n\t"
"s_add_u32 s56, s56, s61\n\t"
"s_cmp_lt_u32 s66, 192\n\t"
"s_cselect_b32 s56, s56, 2.0\n\t"
"v_bfe_u32 v42, v204, 3, 2\n\t"
"v_lshlrev_b32 v43, 2, v204\n\t"
"v_and_or_b32 v42, v43, 16, v42\n\t"
"v_mul_u32_u24 v42, 0x180, v42\n\t"
"v_lshrrev_b32 v44, 5, v204\n\t"
"v_mul_u32_u24 v43, 0x12000, v44\n\t"
"v_and_b32 v45, 3, v204\n\t"
"v_min_u32 v41, 2, v45\n\t"
"v_lshl_add_u32 v41, v41, 4, v42\n\t"
"v_add_u32 v41, v41, v43\n\t"
"v_lshlrev_b32 v43, 4, v204\n\t"
"v_bfe_u32 v45, v204, 1, 2\n\t"
"v_and_or_b32 v45, v43, 16, v45\n\t"
"v_mul_u32_u24 v45, 0x180, v45\n\t"
"v_lshl_add_u32 v44, v44, 3, v120\n\t"
"v_lshl_add_u32 v45, v44, 1, v45\n\t"
"v_mov_b32 v40, 0\n\t"
"s_mul_i32 s61, s73, 147456\n\t"
"s_add_u32 s61, s61, s56\n\t"
"s_lshl3_add_u32 s62, s73, s56\n\t"
"s_add_u32 s62, s62, 589824\n\t"
"s_lshl_b32 s64, s73, 10\n\t"
"s_and_b32 s60, s66, 7\n\t"
"s_lshr_b32 s65, s66, 3\n\t"
"s_mul_i32 s24, s60, 12582912\n\t"
"s_mul_i32 s63, s65, 6144\n\t"
"s_add_u32 s24, s24, s63\n\t"
"s_add_u32 s22, s24, 196224\n\t"
"s_mul_i32 s63, s73, 786432\n\t"
"s_mul_i32 s26, s73, 4608\n\t"
"s_mov_b32 s48, 189696\n\t"
"s_cmp_eq_u32 s65, 0\n\t"
"s_cselect_b64 s[40:41], 0xff, 0\n\t"
"s_cselect_b64 s[46:47], -1, 0\n\t"
"v_mul_u32_u24 v122, 0x300, v120\n\t"
"v_xor_b32 v123, 4, v121\n\t"
"v_add_u32 v122, s63, v122\n\t"
"v_and_b32 v124, 2, v120\n\t"
"v_xor_b32 v124, v124, v123\n\t"
"v_lshl_add_u32 v128, v124, 4, v122\n\t"
"v_cndmask_b32 v241, v128, 2.0, s[40:41]\n\t"
"v_add_u32 v122, 194816, v122\n\t"
"v_add_u32 v124, -1, v120\n\t"
"v_ashrrev_i32 v125, 31, v124\n\t"
"v_sub_u32 v124, v124, v125\n\t"
"v_and_b32 v124, 2, v124\n\t"
"v_xor_b32 v124, v124, v123\n\t"
"v_mad_i32_i24 v125, v125, s48, v122\n\t"
"v_lshl_add_u32 v129, v124, 4, v125\n\t"
"s_lshl_b64 s[40:41], s[40:41], 8\n\t"
"v_cndmask_b32 v242, v129, 2.0, s[40:41]\n\t"
"v_add_u32 v122, 194816, v122\n\t"
"v_add_u32 v124, -2, v120\n\t"
"v_ashrrev_i32 v125, 31, v124\n\t"
"v_sub_u32 v124, v124, v125\n\t"
"v_and_b32 v124, 2, v124\n\t"
"v_and_or_b32 v124, v125, 4, v124\n\t"
"v_xor_b32 v124, v124, v121\n\t"
"v_mad_i32_i24 v125, v125, s48, v122\n\t"
"v_lshl_add_u32 v130, v124, 4, v125\n\t"
"s_lshl_b64 s[40:41], s[40:41], 8\n\t"
"v_cndmask_b32 v243, v130, 2.0, s[40:41]\n\t"
"v_add_u32 v122, 194816, v122\n\t"
"v_add_u32 v124, -3, v120\n\t"
"v_ashrrev_i32 v125, 31, v124\n\t"
"v_sub_u32 v124, v124, v125\n\t"
"v_and_b32 v124, 2, v124\n\t"
"v_xor_b32 v124, v124, v121\n\t"
"v_mad_i32_i24 v125, v125, s48, v122\n\t"
"v_lshl_add_u32 v131, v124, 4, v125\n\t"
"s_lshl_b64 s[40:41], s[40:41], 8\n\t"
"v_cndmask_b32 v244, v131, 2.0, s[40:41]\n\t"
"v_add_u32 v124, 5, v120\n\t"
"v_and_b32 v124, 2, v124\n\t"
"v_xor_b32 v124, v124, v121\n\t"
"v_add_u32 v122, 2560, v122\n\t"
"v_lshl_add_u32 v245, v124, 4, v122\n\t"
"s_mov_b32 s37, -1\n\t"
"s_mov_b32 s36, 0\n\t"
"s_mul_i32 s67, s73, 48\n\t"
"s_add_u32 s67, s67, 118784\n\t"
"s_mov_b32 s10, 0x200000\n\t"
"s_mov_b32 s11, 0x20000\n\t"
"v_min_u32 v189, 47, v204\n\t"
"v_add_u32 v189, s67, v189\n\t"
"v_mul_u32_u24 v189, 16, v189\n\t"
"s_mov_b32 s54, 663552\n\t"
"s_mov_b32 s55, 0x20000\n\t"
"s_mov_b32 s14, 100663296\n\t"
"s_mov_b32 s15, 0x20000\n\t"
"s_add_u32 m0, s64, 0\n\t"
"s_waitcnt lgkmcnt(0)\n\t"
"s_mov_b32 s52, s82\n\t"
"s_and_b32 s53, s83, 0xffff\n\t"
"buffer_load_dwordx4 v41, s[52:55], s61 offen lds\n\t"
"buffer_load_short_d16_hi v40, v45, s[52:55], s62 offen\n\t"
"s_mov_b32 s8, s78\n\t"
"s_and_b32 s9, s79, 0xffff\n\t"
"buffer_load_dword v188, v189, s[8:11], 0 offen sc1\n\t"
"s_mov_b32 s12, s76\n\t"
"s_and_b32 s13, s77, 0xffff\n\t"
"s_add_u32 m0, s26, 103936\n\t"
"s_nop 0\n\t"
"buffer_load_dwordx4 v241, s[12:15], s22 offen offset:0 nt lds\n\t"
"buffer_load_dwordx4 v242, s[12:15], s22 offen offset:1024 nt lds\n\t"
"buffer_load_dwordx4 v243, s[12:15], s22 offen offset:2048 nt lds\n\t"
"buffer_load_dwordx4 v244, s[12:15], s22 offen offset:3072 nt lds\n\t"
"s_mov_b64 exec, s[36:37]\n\t"
"buffer_load_dwordx4 v245, s[12:15], s22 offen offset:3584 nt lds\n\t"
"s_mov_b64 exec, -1\n\t"
"s_mul_i32 s49, s73, 36\n\t"
"s_mul_i32 s43, s73, 576\n\t"
"s_sub_u32 s42, s22, 203520\n\t"
"s_mov_b32 s44, -1\n\t"
"s_mov_b32 s45, 15\n\t"
"v_min_u32 v124, 35, v204\n\t"
"v_add_u32 v124, s49, v124\n\t"
"v_lshrrev_b32 v119, 3, v124\n\t"
"v_and_b32 v124, 7, v124\n\t"
"v_mul_u32_u24 v127, 0x300, v119\n\t"
"v_cmp_gt_u32 vcc, 9, v119\n\t"
"v_add_u32 v119, -9, v119\n\t"
"v_ashrrev_i32 v125, 31, v119\n\t"
"v_add_u32 v127, s42, v127\n\t"
"v_mad_i32_i24 v127, v125, s48, v127\n\t"
"v_and_b32 v125, 9, v125\n\t"
"v_add_u32 v119, v119, v125\n\t"
"v_and_b32 v125, 2, v119\n\t"
"v_xor_b32 v124, v124, v125\n\t"
"v_lshl_add_u32 v133, v124, 4, v127\n\t"
"s_cmp_eq_u32 s60, 0\n\t"
"s_cselect_b64 s[50:51], vcc, 0\n\t"
"v_cmp_eq_u32 vcc, 0, v119\n\t"
"v_cndmask_b32 v246, v133, 2.0, s[50:51]\n\t"
"s_and_b64 vcc, vcc, s[46:47]\n\t"
"v_cndmask_b32 v246, v246, 2.0, vcc\n\t"
"s_add_u32 m0, s43, 101632\n\t"
"s_mov_b64 exec, s[44:45]\n\t"
"buffer_load_dwordx4 v246, s[12:15], 0 offen offset:0 nt lds\n\t"
"s_mov_b64 exec, -1\n\t"
"s_mul_i32 s70, s66, 16\n\t"
"s_add_u32 s70, s70, 1900544\n\t"
"s_lshl_b32 s61, s73, 8\n\t"
"s_or_b32 s61, s61, s66\n\t"
"s_cmp_lt_u32 s61, 192\n\t"
"s_cselect_b32 s70, s70, 2.0\n\t"
"s_lshr_b32 s61, s66, 3\n\t"
"s_lshl2_add_u32 s61, s61, s73\n\t"
"s_lshl_b32 s60, s61, 8\n\t"
"s_cmp_lt_u32 s61, 96\n\t"
"s_cselect_b32 s60, s60, 2.0\n\t"
"v_lshrrev_b32 v118, 1, v204\n\t"
"v_mul_u32_u24 v118, 220928, v118\n\t"
"v_lshl_add_u32 v118, v204, 7, v118\n\t"
"s_mov_b64 exec, 63\n\t"
"buffer_load_dword v118, v118, s[52:55], s60 offen\n\t"
"s_mov_b64 exec, -1\n\t"
"buffer_load_dword v187, off, s[8:11], s70 sc1\n\t"
"v_add_u32 v107, 0, v204\n\t"
"v_mul_u32_u24 v104, 0xab, v107\n\t"
"v_lshrrev_b32 v104, 10, v104\n\t"
"v_mul_u32_u24 v104, 0x120, v104\n\t"
"v_lshl_add_u32 v104, v107, 4, v104\n\t"
"v_add_u32 v107, 64, v204\n\t"
"v_mul_u32_u24 v105, 0xab, v107\n\t"
"v_lshrrev_b32 v105, 10, v105\n\t"
"v_mul_u32_u24 v105, 0x120, v105\n\t"
"v_lshl_add_u32 v105, v107, 4, v105\n\t"
"v_add_u32 v107, 128, v204\n\t"
"v_mul_u32_u24 v106, 0xab, v107\n\t"
"v_lshrrev_b32 v106, 10, v106\n\t"
"v_mul_u32_u24 v106, 0x120, v106\n\t"
"v_lshl_add_u32 v106, v107, 4, v106\n\t"
"s_mul_i32 s74, s73, 20736\n\t"
"s_mul_i32 s72, s73, 96\n\t"
"s_add_u32 s74, s74, 33280\n\t"
"s_add_u32 s75, s74, 12288\n\t"
"s_cmp_eq_u32 s73, 3\n\t"
"s_cselect_b32 s74, 16544, s74\n\t"
"s_cselect_b32 s75, 95488, s75\n\t"
"s_lshr_b32 s60, s66, 5\n\t"
"s_bfe_u32 s62, s66, 0x20003\n\t"
"s_and_b32 s61, s60, 1\n\t"
"s_lshl1_add_u32 s61, s73, s61\n\t"
"s_mul_i32 s62, s62, 288\n\t"
"s_add_u32 s61, s61, s62\n\t"
"s_lshr_b32 s60, s60, 1\n\t"
"s_mul_i32 s62, s60, 96\n\t"
"s_add_u32 s61, s61, s62\n\t"
"s_lshl_b32 s57, s61, 10\n\t"
"s_and_b32 s61, s66, 7\n\t"
"s_lshl_b32 s61, s61, 7\n\t"
"s_add_u32 s57, s57, s61\n\t"
"s_cmp_lt_u32 s73, 3\n\t"
"s_cselect_b32 s67, s66, 0xff\n\t"
"s_cmp_lt_u32 s67, 192\n\t"
"s_cselect_b32 s57, s57, 2.0\n\t"
"s_lshl_b32 s60, s73, 3\n\t"
"s_add_u32 s60, s60, 0\n\t"
"s_lshl_b32 s61, s73, 11\n\t"
"s_add_u32 s61, s61, 4096\n\t"
"v_lshl_add_u32 v46, v121, 6, s60\n\t"
"v_lshl_add_u32 v46, v44, 1, v46\n\t"
"v_lshl_add_u32 v42, v204, 1, s61\n\t"
"v_lshl_add_u32 v43, v204, 4, s61\n\t"
"v_mul_u32_u24 v44, 6144, v120\n\t"
"v_lshl_add_u32 v44, v121, 4, v44\n\t"
"v_mov_b32 v32, 0\n\t"
"v_mov_b32 v33, 0\n\t"
"v_mov_b32 v34, 0\n\t"
"v_mov_b32 v35, 0\n\t"
"v_mov_b32 v36, 0\n\t"
"v_mov_b32 v37, 0\n\t"
"v_mov_b32 v38, 0\n\t"
"v_mov_b32 v39, 0\n\t"
"s_waitcnt vmcnt(9)\n\t"
"s_barrier\n\t"
".p2align 3\n\t"
"ds_read_u16_d16_hi v32, v46 offset:0\n\t"
"ds_read_u16_d16_hi v33, v46 offset:512\n\t"
"ds_read_u16_d16_hi v34, v46 offset:1024\n\t"
"ds_read_u16_d16_hi v35, v46 offset:1536\n\t"
"ds_read_u16_d16_hi v36, v46 offset:2048\n\t"
"ds_read_u16_d16_hi v37, v46 offset:2560\n\t"
"ds_read_u16_d16_hi v38, v46 offset:3072\n\t"
"ds_read_u16_d16_hi v39, v46 offset:3584\n\t"
"s_waitcnt lgkmcnt(1)\n\t"
"v_add_f32 v48, v32, v38\n\t"
"s_waitcnt lgkmcnt(0)\n\t"
"v_add_f32 v49, v33, v39\n\t"
"v_add_f32 v50, v34, v40\n\t"
"v_add_f32 v51, v48, v35\n\t"
"v_sub_f32 v54, v48, v35\n\t"
"v_add_f32 v52, v49, v36\n\t"
"v_sub_f32 v55, v49, v36\n\t"
"v_add_f32 v53, v50, v37\n\t"
"v_sub_f32 v56, v50, v37\n\t"
"v_add_f32 v57, v32, v34\n\t"
"v_add_f32 v58, v51, v53\n\t"
"v_add_f32 v59, v54, v56\n\t"
"v_add_f32 v60, v38, v40\n\t"
"v_add_f32 v62, v57, v33\n\t"
"v_sub_f32 v63, v57, v33\n\t"
"v_add_f32 v64, v58, v52\n\t"
"v_sub_f32 v65, v58, v52\n\t"
"v_add_f32 v66, v59, v55\n\t"
"v_sub_f32 v67, v59, v55\n\t"
"v_add_f32 v68, v60, v39\n\t"
"v_sub_f32 v69, v60, v39\n\t"
"v_mul_f32 v71, 0.5, v62\n\t"
"v_mul_f32 v72, 0.5, v63\n\t"
"v_mul_f32 v74, 0.5, v51\n\t"
"v_mul_f32 v75, 0x3e800000, v64\n\t"
"v_mul_f32 v76, 0x3e800000, v65\n\t"
"v_mul_f32 v77, -0.5, v53\n\t"
"v_mul_f32 v79, 0.5, v66\n\t"
"v_mul_f32 v80, 0.5, v67\n\t"
"v_mul_f32 v83, -0.5, v68\n\t"
"v_mul_f32 v84, -0.5, v69\n\t"
".p2align 3\n\t"
"v_cvt_pk_bf16_f32 v86, v32, v71\n\t"
"v_cvt_pk_bf16_f32 v87, v72, -v34\n\t"
"v_cvt_pk_bf16_f32 v88, v74, v75\n\t"
"v_cvt_pk_bf16_f32 v89, v76, v77\n\t"
"v_cvt_pk_bf16_f32 v90, v54, v79\n\t"
"v_cvt_pk_bf16_f32 v91, v80, -v56\n\t"
"v_cvt_pk_bf16_f32 v92, -v38, v83\n\t"
"v_cvt_pk_bf16_f32 v93, v84, v40\n\t"
"ds_write_b16 v42, v86 offset:0\n\t"
"ds_write_b16_d16_hi v42, v86 offset:128\n\t"
"ds_write_b16 v42, v87 offset:256\n\t"
"ds_write_b16_d16_hi v42, v87 offset:384\n\t"
"ds_write_b16 v42, v88 offset:512\n\t"
"ds_write_b16_d16_hi v42, v88 offset:640\n\t"
"ds_write_b16 v42, v89 offset:768\n\t"
"ds_write_b16_d16_hi v42, v89 offset:896\n\t"
"ds_write_b16 v42, v90 offset:1024\n\t"
"ds_write_b16_d16_hi v42, v90 offset:1152\n\t"
"ds_write_b16 v42, v91 offset:1280\n\t"
"ds_write_b16_d16_hi v42, v91 offset:1408\n\t"
"ds_write_b16 v42, v92 offset:1536\n\t"
"ds_write_b16_d16_hi v42, v92 offset:1664\n\t"
"ds_write_b16 v42, v93 offset:1792\n\t"
"ds_write_b16_d16_hi v42, v93 offset:1920\n\t"
"ds_read_b128 v[96:99], v43 offset:0\n\t"
"ds_read_b128 v[100:103], v43 offset:1024\n\t"
"s_add_u32 s60, s57, 0\n\t"
"s_waitcnt lgkmcnt(1)\n\t"
"buffer_store_dwordx4 v[96:99], v44, s[8:11], s60 offen sc1\n\t"
"s_add_u32 s60, s57, 49152\n\t"
"s_waitcnt lgkmcnt(0)\n\t"
"buffer_store_dwordx4 v[100:103], v44, s[8:11], s60 offen sc1\n\t"
"v_lshrrev_b32 v123, 4, v204\n\t"
"v_bfe_u32 v124, v204, 2, 2\n\t"
"v_and_b32 v125, 3, v204\n\t"
"v_lshlrev_b32 v125, 3, v125\n\t"
"v_and_b32 v138, 49, v204\n\t"
"v_lshl_or_b32 v138, v124, 1, v138\n\t"
"v_and_b32 v139, 2, v204\n\t"
"v_lshl_or_b32 v138, v139, 2, v138\n\t"
"s_add_u32 s63, s26, 1152\n\t"
"v_and_b32 v126, 48, v204\n\t"
"v_and_b32 v127, 6, v204\n\t"
"v_lshl_add_u32 v126, v127, 5, v126\n\t"
"v_and_b32 v127, 8, v204\n\t"
"s_add_u32 m0, s74, 0\n\t"
"s_add_u32 s27, s72, 0\n\t"
"buffer_load_dwordx4 v104, s[52:55], s27 offen lds\n\t"
"v_mul_u32_u24 v134, 516, v127\n\t"
"v_and_b32 v127, 1, v204\n\t"
"v_mul_u32_u24 v127, 8320, v127\n\t"
"v_add3_u32 v224, v134, v126, v127\n\t"
"v_and_b32 v127, 16, v204\n\t"
"v_lshl_add_u32 v127, v127, 1, v125\n\t"
"v_lshl_add_u32 v127, v123, 8, v127\n\t"
"v_add_u32 v126, 4, v204\n\t"
"v_and_b32 v126, 8, v126\n\t"
"v_lshl_add_u32 v127, v126, 3, v127\n\t"
"v_mul_u32_u24 v126, 1152, v124\n\t"
"v_add3_u32 v226, v126, v127, s63\n\t"
"s_add_u32 m0, s74, 1024\n\t"
"s_add_u32 s27, s72, 0\n\t"
"buffer_load_dwordx4 v105, s[52:55], s27 offen lds\n\t"
"v_min_u32 v126, 2, v124\n\t"
"v_cmp_gt_u32 vcc, 48, v204\n\t"
"s_mov_b32 s48, 384\n\t"
"s_cmp_eq_u32 s65, 31\n\t"
"v_cndmask_b32 v126, v126, v123, vcc\n\t"
"v_cndmask_b32 v127, 4, v124, vcc\n\t"
"v_and_b32 v134, 1, v126\n\t"
"v_lshl_add_u32 v134, v134, 6, v125\n\t"
"v_lshl_add_u32 v134, v127, 7, v134\n\t"
"v_and_b32 v127, 2, v127\n\t"
"v_lshl_add_u32 v134, v127, 4, v134\n\t"
"v_mul_u32_u24 v126, 2304, v126\n\t"
"v_add3_u32 v227, v126, v134, s26\n\t"
"v_lshlrev_b32 v126, 14, v204\n\t"
"v_and_b32 v126, 0x8000, v126\n\t"
"v_or_b32 v134, 0x3f803f80, v126\n\t"
"v_and_b32 v126, 1, v204\n\t"
"v_cmp_eq_u32 vcc, 0, v126\n\t"
"s_cselect_b64 s[40:41], 0xff, 0\n\t"
"s_cselect_b64 s[46:47], -1, 0\n\t"
"s_add_u32 m0, s74, 2048\n\t"
"s_add_u32 s27, s72, 0\n\t"
"buffer_load_dwordx4 v106, s[52:55], s27 offen lds\n\t"
"v_cndmask_b32 v135, v134, 0, vcc\n\t"
"v_cndmask_b32 v134, 0, v134, vcc\n\t"
"v_xor_b32 v136, 0x80008000, v134\n\t"
"v_xor_b32 v137, 0x80008000, v135\n\t"
"s_add_u32 m0, s74, 3072\n\t"
"s_add_u32 s27, s72, 12288\n\t"
"buffer_load_dwordx4 v104, s[52:55], s27 offen lds\n\t"
"v_accvgpr_write_b32 a220, v134\n\t"
"v_accvgpr_write_b32 a221, v135\n\t"
"v_accvgpr_write_b32 a222, v136\n\t"
"v_accvgpr_write_b32 a223, v137\n\t"
"v_xad_u32 v236, v128, 64, s48\n\t"
"v_xad_u32 v237, v129, 64, s48\n\t"
"v_cndmask_b32 v237, v237, 2.0, s[40:41]\n\t"
"s_lshl_b64 s[40:41], s[40:41], 8\n\t"
"v_xad_u32 v238, v130, 64, s48\n\t"
"v_cndmask_b32 v238, v238, 2.0, s[40:41]\n\t"
"s_lshl_b64 s[40:41], s[40:41], 8\n\t"
"v_xad_u32 v239, v131, 64, s48\n\t"
"v_cndmask_b32 v239, v239, 2.0, s[40:41]\n\t"
"s_add_u32 m0, s74, 4096\n\t"
"s_add_u32 s27, s72, 12288\n\t"
"buffer_load_dwordx4 v105, s[52:55], s27 offen lds\n\t"
"s_lshl_b64 s[40:41], s[40:41], 40\n\t"
"v_xad_u32 v240, v245, 64, s48\n\t"
"v_cndmask_b32 v240, v240, 2.0, s[40:41]\n\t"
"v_cmp_eq_u32 vcc, 8, v119\n\t"
"v_xad_u32 v247, v133, 64, s48\n\t"
"s_and_b64 vcc, vcc, s[46:47]\n\t"
"v_cndmask_b32 v247, v247, 2.0, s[50:51]\n\t"
"v_cndmask_b32 v247, v247, 2.0, vcc\n\t"
"v_and_b32 v139, 7, v138\n\t"
"v_bfe_u32 v126, v138, 3, 1\n\t"
"v_lshrrev_b32 v127, 4, v138\n\t"
"v_sub_u32 v127, v127, v126\n\t"
"s_add_u32 m0, s74, 5120\n\t"
"s_add_u32 s27, s72, 12288\n\t"
"buffer_load_dwordx4 v106, s[52:55], s27 offen lds\n\t"
"v_mov_b32 v134, 0xbf803f80\n\t"
"v_mov_b32 v135, 0x3f80bf80\n\t"
"v_cmp_eq_u32 vcc, 0, v127\n\t"
"v_add_u32 v126, 4, v126\n\t"
"s_nop 0\n\t"
"v_cndmask_b32 v134, 0, v134, vcc\n\t"
"v_cmp_eq_u32 vcc, 1, v127\n\t"
"s_nop 1\n\t"
"v_cndmask_b32 v134, v134, v135, vcc\n\t"
"v_cmp_eq_u32 vcc, 7, v139\n\t"
"s_nop 1\n\t"
"v_cndmask_b32 v135, 0, v135, vcc\n\t"
"v_cmp_gt_u32 vcc, 48, v138\n\t"
"s_nop 1\n\t"
"v_cndmask_b32 v134, v135, v134, vcc\n\t"
"v_mov_b32 v135, 0\n\t"
"s_add_u32 m0, s74, 6144\n\t"
"s_add_u32 s27, s72, 442368\n\t"
"buffer_load_dwordx4 v104, s[52:55], s27 offen lds\n\t"
"v_cndmask_b32 v136, v126, v139, vcc\n\t"
"v_and_b32 v137, 1, v136\n\t"
"v_lshlrev_b32 v137, 4, v137\n\t"
"v_lshrrev_b32 v136, 1, v136\n\t"
"v_lshlrev_b64 v[134:135], v137, v[134:135]\n\t"
"v_cmp_eq_u32 vcc, 0, v136\n\t"
"s_nop 1\n\t"
"v_cndmask_b32 v140, 0, v134, vcc\n\t"
"v_accvgpr_write_b32 a224, v140\n\t"
"v_cndmask_b32 v141, 0, v135, vcc\n\t"
"v_cmp_eq_u32 vcc, 1, v136\n\t"
"v_and_b32 v140, 0xff7fff7f, v140\n\t"
"v_xor_b32 v148, 0x80008000, v140\n\t"
"s_add_u32 m0, s74, 7168\n\t"
"s_add_u32 s27, s72, 442368\n\t"
"buffer_load_dwordx4 v105, s[52:55], s27 offen lds\n\t"
"v_cndmask_b32 v141, v141, v134, vcc\n\t"
"v_cndmask_b32 v142, 0, v135, vcc\n\t"
"v_cmp_eq_u32 vcc, 2, v136\n\t"
"v_accvgpr_write_b32 a225, v141\n\t"
"v_accvgpr_write_b32 a216, v140\n\t"
"v_cndmask_b32 v142, v142, v134, vcc\n\t"
"v_cndmask_b32 v143, 0, v135, vcc\n\t"
"v_cmp_eq_u32 vcc, 3, v136\n\t"
"v_accvgpr_write_b32 a226, v142\n\t"
"v_and_b32 v141, 0xff7fff7f, v141\n\t"
"v_cndmask_b32 v143, v143, v134, vcc\n\t"
"v_accvgpr_write_b32 a227, v143\n\t"
"v_xor_b32 v149, 0x80008000, v141\n\t"
"s_add_u32 m0, s74, 8192\n\t"
"s_add_u32 s27, s72, 442368\n\t"
"buffer_load_dwordx4 v106, s[52:55], s27 offen lds\n\t"
"v_accvgpr_write_b32 a217, v141\n\t"
"v_and_b32 v142, 0xff7fff7f, v142\n\t"
"v_xor_b32 v150, 0x80008000, v142\n\t"
"v_accvgpr_write_b32 a218, v142\n\t"
"v_and_b32 v143, 0xff7fff7f, v143\n\t"
"v_xor_b32 v151, 0x80008000, v143\n\t"
"v_accvgpr_write_b32 a219, v143\n\t"
"s_mov_b32 s68, 3000\n\t"
"v_and_b32 v107, 51, v204\n\t"
"v_mul_u32_u24 v107, 24, v107\n\t"
"v_and_b32 v47, 8, v204\n\t"
"v_mad_u32_u24 v107, v47, 12, v107\n\t"
"s_add_u32 m0, s74, 9216\n\t"
"s_add_u32 s27, s72, 454656\n\t"
"buffer_load_dwordx4 v104, s[52:55], s27 offen lds\n\t"
"v_and_b32 v47, 4, v204\n\t"
"v_mul_u32_u24 v47, 0x180, v47\n\t"
"v_add_u32 v107, v47, v107\n\t"
"v_add_u32 v47, s75, v107\n\t"
"v_add_u32 v107, s74, v107\n\t"
"v_lshrrev_b32 v112, 4, v204\n\t"
"v_bfe_u32 v113, v204, 2, 2\n\t"
"v_cmp_eq_u32 vcc, v112, v113\n\t"
"v_lshlrev_b32 v112, 4, v204\n\t"
"v_mov_b32 v113, 0x3e80\n\t"
"v_lshlrev_b32 v113, v112, v113\n\t"
"v_cndmask_b32 v113, 0, v113, vcc\n\t"
"s_add_u32 m0, s74, 10240\n\t"
"s_add_u32 s27, s72, 454656\n\t"
"buffer_load_dwordx4 v105, s[52:55], s27 offen lds\n\t"
"v_and_b32 v112, 2, v204\n\t"
"v_cmp_eq_u32 vcc, 0, v112\n\t"
"v_mov_b32 v108, 0\n\t"
"v_mov_b32 v109, 0\n\t"
"v_cndmask_b32 v110, 0, v113, vcc\n\t"
"v_cndmask_b32 v111, v113, 0, vcc\n\t"
"v_xor_b32 v114, 0x80008000, v110\n\t"
"v_xor_b32 v115, 0x80008000, v111\n\t"
"v_mov_b32 v112, 0\n\t"
"v_mov_b32 v113, 0\n\t"
"v_mov_b32 v116, 0\n\t"
"v_mov_b32 v117, 0\n\t"
"s_add_u32 m0, s74, 11264\n\t"
"s_add_u32 s27, s72, 454656\n\t"
"buffer_load_dwordx4 v106, s[52:55], s27 offen lds\n\t"
"s_waitcnt vmcnt(16)\n\t"
"s_waitcnt vmcnt(12)\n\t"
"v_readfirstlane_b32 s71, v187\n\t"
"s_barrier\n\t"
"s_add_u32 s71, s71, 1\n\t"
"v_mov_b32 v215, s71\n\t"
"s_mov_b64 exec, 1\n\t"
"buffer_store_dword v215, off, s[8:11], s70 sc1\n\t"
"s_mov_b64 exec, -1\n\t"
"v_add_u32 v248, 101632, v226\n\t"
"v_xor_b32 v249, 32, v248\n\t"
"v_xor_b32 v250, 64, v248\n\t"
"v_xor_b32 v251, 96, v248\n\t"
"ds_read_b64_tr_b16 a[178:179], v248 offset:128\n\t"
"ds_read_b64_tr_b16 a[186:187], v249 offset:128\n\t"
"s_add_u32 m0, s75, 0\n\t"
"s_add_u32 s27, s72, 221184\n\t"
"buffer_load_dwordx4 v104, s[52:55], s27 offen lds\n\t"
"ds_read_b64_tr_b16 a[194:195], v250 offset:128\n\t"
"ds_read_b64_tr_b16 a[202:203], v251 offset:128\n\t"
"ds_read_b64_tr_b16 a[176:177], v248 offset:0\n\t"
"s_add_u32 m0, s75, 1024\n\t"
"s_add_u32 s27, s72, 221184\n\t"
"buffer_load_dwordx4 v105, s[52:55], s27 offen lds\n\t"
"ds_read_b64_tr_b16 a[184:185], v249 offset:0\n\t"
"ds_read_b64_tr_b16 a[180:181], v249 offset:256\n\t"
"ds_read_b64_tr_b16 a[188:189], v248 offset:256\n\t"
"s_add_u32 m0, s75, 2048\n\t"
"s_add_u32 s27, s72, 221184\n\t"
"buffer_load_dwordx4 v106, s[52:55], s27 offen lds\n\t"
"ds_read_b64_tr_b16 a[192:193], v250 offset:0\n\t"
"ds_read_b64_tr_b16 a[200:201], v251 offset:0\n\t"
"ds_read_b64_tr_b16 a[196:197], v251 offset:256\n\t"
"s_add_u32 m0, s75, 3072\n\t"
"s_add_u32 s27, s72, 233472\n\t"
"buffer_load_dwordx4 v104, s[52:55], s27 offen lds\n\t"
"ds_read_b64_tr_b16 a[204:205], v250 offset:256\n\t"
"s_waitcnt lgkmcnt(11)\n\t"
"v_mfma_f32_4x4x4_16b_bf16 v[8:11], a[220:221], a[178:179], 0\n\t"
"s_waitcnt lgkmcnt(10)\n\t"
"v_mfma_f32_4x4x4_16b_bf16 v[12:15], a[220:221], a[186:187], 0\n\t"
"s_waitcnt lgkmcnt(9)\n\t"
"v_mfma_f32_4x4x4_16b_bf16 v[24:27], a[220:221], a[194:195], 0\n\t"
"s_waitcnt lgkmcnt(8)\n\t"
"v_mfma_f32_4x4x4_16b_bf16 v[28:31], a[220:221], a[202:203], 0\n\t"
"s_waitcnt lgkmcnt(7)\n\t"
"v_mfma_f32_4x4x4_16b_bf16 v[0:3], a[222:223], a[176:177], v[8:11]\n\t"
"s_waitcnt lgkmcnt(6)\n\t"
"v_mfma_f32_4x4x4_16b_bf16 v[4:7], a[222:223], a[184:185], v[12:15]\n\t"
"s_add_u32 m0, s75, 4096\n\t"
"s_add_u32 s27, s72, 233472\n\t"
"buffer_load_dwordx4 v105, s[52:55], s27 offen lds\n\t"
"s_add_u32 m0, s75, 5120\n\t"
"s_add_u32 s27, s72, 233472\n\t"
"buffer_load_dwordx4 v106, s[52:55], s27 offen lds\n\t"
"s_waitcnt lgkmcnt(5)\n\t"
"v_mfma_f32_4x4x4_16b_bf16 v[8:11], a[222:223], a[180:181], v[8:11]\n\t"
"s_waitcnt lgkmcnt(4)\n\t"
"v_mfma_f32_4x4x4_16b_bf16 v[12:15], a[222:223], a[188:189], v[12:15]\n\t"
"s_waitcnt lgkmcnt(3)\n\t"
"v_mfma_f32_4x4x4_16b_bf16 v[16:19], a[222:223], a[192:193], v[24:27]\n\t"
"s_waitcnt lgkmcnt(2)\n\t"
"v_mfma_f32_4x4x4_16b_bf16 v[20:23], a[222:223], a[200:201], v[28:31]\n\t"
"s_waitcnt lgkmcnt(1)\n\t"
"v_mfma_f32_4x4x4_16b_bf16 v[24:27], a[222:223], a[196:197], v[24:27]\n\t"
"s_waitcnt lgkmcnt(0)\n\t"
"v_mfma_f32_4x4x4_16b_bf16 v[28:31], a[222:223], a[204:205], v[28:31]\n\t"
"s_add_u32 m0, s26, 124544\n\t"
"s_nop 0\n\t"
"buffer_load_dwordx4 v241, s[12:15], s22 offen offset:128 nt lds\n\t"
"buffer_load_dwordx4 v242, s[12:15], s22 offen offset:1152 nt lds\n\t"
"v_cvt_pk_bf16_f32 v0, -v0, -v4\n\t"
"v_cvt_pk_bf16_f32 v1, -v1, -v5\n\t"
"v_cvt_pk_bf16_f32 v2, -v2, -v6\n\t"
"v_cvt_pk_bf16_f32 v3, -v3, -v7\n\t"
"s_lshl_b32 m0, s73, 8\n\t"
"s_nop 0\n\t"
".p2align 3\n\t"
"ds_write_addtid_b32 v0 offset:0\n\t"
"ds_write_addtid_b32 v1 offset:4128\n\t"
"ds_write_addtid_b32 v2 offset:1024\n\t"
"ds_write_addtid_b32 v3 offset:5152\n\t"
"v_cvt_pk_bf16_f32 v8, v8, v12\n\t"
"v_cvt_pk_bf16_f32 v9, v9, v13\n\t"
"v_cvt_pk_bf16_f32 v10, v10, v14\n\t"
"v_cvt_pk_bf16_f32 v11, v11, v15\n\t"
"ds_write_addtid_b32 v8 offset:8320\n\t"
"ds_write_addtid_b32 v9 offset:12448\n\t"
"ds_write_addtid_b32 v10 offset:9344\n\t"
"ds_write_addtid_b32 v11 offset:13472\n\t"
"v_cvt_pk_bf16_f32 v16, -v16, -v20\n\t"
"v_cvt_pk_bf16_f32 v17, -v17, -v21\n\t"
"v_cvt_pk_bf16_f32 v18, -v18, -v22\n\t"
"v_cvt_pk_bf16_f32 v19, -v19, -v23\n\t"
"ds_write_addtid_b32 v16 offset:2048\n\t"
"ds_write_addtid_b32 v17 offset:6176\n\t"
"ds_write_addtid_b32 v18 offset:3072\n\t"
"ds_write_addtid_b32 v19 offset:7200\n\t"
"v_cvt_pk_bf16_f32 v24, v24, v28\n\t"
"v_cvt_pk_bf16_f32 v25, v25, v29\n\t"
"v_cvt_pk_bf16_f32 v26, v26, v30\n\t"
"v_cvt_pk_bf16_f32 v27, v27, v31\n\t"
"ds_write_addtid_b32 v24 offset:10368\n\t"
"ds_write_addtid_b32 v25 offset:14496\n\t"
"ds_write_addtid_b32 v26 offset:11392\n\t"
"ds_write_addtid_b32 v27 offset:15520\n\t"
"v_add_u32 v248, 101632, v227\n\t"
"v_xor_b32 v249, 32, v248\n\t"
"v_xor_b32 v250, 64, v248\n\t"
"v_xor_b32 v251, 96, v248\n\t"
"ds_read_b64_tr_b16 a[176:177], v248 offset:0\n\t"
"ds_read_b64_tr_b16 a[178:179], v248 offset:512\n\t"
"ds_read_b64_tr_b16 a[180:181], v249 offset:0\n\t"
"ds_read_b64_tr_b16 a[182:183], v249 offset:512\n\t"
"s_add_u32 m0, s26, 124544\n\t"
"s_nop 0\n\t"
".p2align 3\n\t"
"buffer_load_dwordx4 v243, s[12:15], s22 offen offset:2176 nt lds\n\t"
"ds_read_b64_tr_b16 a[184:185], v250 offset:0\n\t"
"ds_read_b64_tr_b16 a[186:187], v250 offset:512\n\t"
"ds_read_b64_tr_b16 a[188:189], v251 offset:0\n\t"
"ds_read_b64_tr_b16 a[190:191], v251 offset:512\n\t"
"buffer_load_dwordx4 v244, s[12:15], s22 offen offset:3200 nt lds\n\t"
"ds_read_b64_tr_b16 a[192:193], v248 offset:1152\n\t"
"ds_read_b64_tr_b16 a[194:195], v248 offset:1664\n\t"
"ds_read_b64_tr_b16 a[196:197], v249 offset:1152\n\t"
"ds_read_b64_tr_b16 a[198:199], v249 offset:1664\n\t"
"s_mov_b64 exec, s[36:37]\n\t"
"buffer_load_dwordx4 v245, s[12:15], s22 offen offset:3712 nt lds\n\t"
"s_mov_b64 exec, -1\n\t"
"ds_read_b64_tr_b16 a[200:201], v250 offset:1152\n\t"
"ds_read_b64_tr_b16 a[202:203], v250 offset:1664\n\t"
"ds_read_b64_tr_b16 a[204:205], v251 offset:1152\n\t"
"ds_read_b64_tr_b16 a[206:207], v251 offset:1664\n\t"
"s_add_u32 m0, s43, 122240\n\t"
"s_mov_b64 exec, s[44:45]\n\t"
"buffer_load_dwordx4 v246, s[12:15], 0 offen offset:128 nt lds\n\t"
"s_mov_b64 exec, -1\n\t"
"s_mov_b64 exec, 0\n\t"
"s_cmp_eq_u32 s73, 0\n\t"
"s_cbranch_scc1 Lpad3_%=\n\t"
"s_cmp_eq_u32 s73, 1\n\t"
"s_cbranch_scc1 Lpad2_%=\n\t"
"s_cmp_eq_u32 s73, 2\n\t"
"s_cbranch_scc1 Lpad1_%=\n\t"
"s_branch Lpad0_%=\n\t"
".fill 7, 4, 0xbf800000\n\t"
"Lret0_%=:\n\t"
"s_mov_b64 exec, -1\n\t"
"s_waitcnt vmcnt(19)\n\t"
".p2align 3\n\t"
"ds_read_b64_tr_b16 a[48:49], v107 offset:0\n\t"
"ds_read_b64_tr_b16 a[50:51], v107 offset:192\n\t"
"ds_read_b64_tr_b16 a[52:53], v107 offset:3072\n\t"
"ds_read_b64_tr_b16 a[54:55], v107 offset:3264\n\t"
"ds_read_b64_tr_b16 a[56:57], v107 offset:8\n\t"
"ds_read_b64_tr_b16 a[58:59], v107 offset:200\n\t"
"ds_read_b64_tr_b16 a[60:61], v107 offset:3080\n\t"
"ds_read_b64_tr_b16 a[62:63], v107 offset:3272\n\t"
"ds_read_b64_tr_b16 a[64:65], v107 offset:16\n\t"
"ds_read_b64_tr_b16 a[66:67], v107 offset:208\n\t"
"ds_read_b64_tr_b16 a[68:69], v107 offset:3088\n\t"
"ds_read_b64_tr_b16 a[70:71], v107 offset:3280\n\t"
"s_waitcnt vmcnt(13)\n\t"
".p2align 3\n\t"
"ds_read_b64_tr_b16 a[72:73], v107 offset:6144\n\t"
"ds_read_b64_tr_b16 a[74:75], v107 offset:6336\n\t"
"ds_read_b64_tr_b16 a[76:77], v107 offset:9216\n\t"
"ds_read_b64_tr_b16 a[78:79], v107 offset:9408\n\t"
"ds_read_b64_tr_b16 a[80:81], v107 offset:6152\n\t"
"ds_read_b64_tr_b16 a[82:83], v107 offset:6344\n\t"
"ds_read_b64_tr_b16 a[84:85], v107 offset:9224\n\t"
"ds_read_b64_tr_b16 a[86:87], v107 offset:9416\n\t"
"ds_read_b64_tr_b16 a[88:89], v107 offset:6160\n\t"
"ds_read_b64_tr_b16 a[90:91], v107 offset:6352\n\t"
"ds_read_b64_tr_b16 a[92:93], v107 offset:9232\n\t"
"ds_read_b64_tr_b16 a[94:95], v107 offset:9424\n\t"
"s_waitcnt lgkmcnt(15)\n\t"
"v_mfma_f32_16x16x32_bf16 v[0:3], v[110:113], a[48:51], 0\n\t"
"s_waitcnt lgkmcnt(10)\n\t"
"v_mfma_f32_16x16x32_bf16 v[0:3], v[110:113], a[72:75], v[0:3]\n\t"
"s_nop 0\n\t"
"v_mfma_f32_16x16x32_bf16 v[8:11], v[108:111], a[48:51], 0\n\t"
"s_nop 0\n\t"
"v_mfma_f32_16x16x32_bf16 v[8:11], v[108:111], a[72:75], v[8:11]\n\t"
"s_add_u32 m0, s26, 145152\n\t"
"s_nop 0\n\t"
"buffer_load_dwordx4 v241, s[12:15], s22 offen offset:256 nt lds\n\t"
"v_mfma_f32_16x16x32_bf16 v[16:19], v[110:113], a[52:55], 0\n\t"
"s_waitcnt lgkmcnt(8)\n\t"
"v_mfma_f32_16x16x32_bf16 v[16:19], v[110:113], a[76:79], v[16:19]\n\t"
"s_nop 0\n\t"
"v_mfma_f32_16x16x32_bf16 v[24:27], v[108:111], a[52:55], 0\n\t"
"s_nop 0\n\t"
"v_mfma_f32_16x16x32_bf16 v[24:27], v[108:111], a[76:79], v[24:27]\n\t"
"buffer_load_dwordx4 v242, s[12:15], s22 offen offset:1280 nt lds\n\t"
"s_waitcnt vmcnt(8)\n\t"
".p2align 3\n\t"
"ds_read_b64_tr_b16 a[0:1], v47 offset:0\n\t"
"ds_read_b64_tr_b16 a[2:3], v47 offset:192\n\t"
"ds_read_b64_tr_b16 a[4:5], v47 offset:3072\n\t"
"ds_read_b64_tr_b16 a[6:7], v47 offset:3264\n\t"
"ds_read_b64_tr_b16 a[8:9], v47 offset:8\n\t"
"ds_read_b64_tr_b16 a[10:11], v47 offset:200\n\t"
"ds_read_b64_tr_b16 a[12:13], v47 offset:3080\n\t"
"ds_read_b64_tr_b16 a[14:15], v47 offset:3272\n\t"
"ds_read_b64_tr_b16 a[16:17], v47 offset:16\n\t"
"ds_read_b64_tr_b16 a[18:19], v47 offset:208\n\t"
"ds_read_b64_tr_b16 a[20:21], v47 offset:3088\n\t"
"ds_read_b64_tr_b16 a[22:23], v47 offset:3280\n\t"
"v_mfma_f32_16x16x32_bf16 v[32:35], v[110:113], a[56:59], 0\n\t"
"s_waitcnt lgkmcnt(15)\n\t"
"v_mfma_f32_16x16x32_bf16 v[32:35], v[110:113], a[80:83], v[32:35]\n\t"
"s_nop 0\n\t"
"v_mfma_f32_16x16x32_bf16 v[96:99], v[108:111], a[56:59], 0\n\t"
"s_nop 0\n\t"
"v_mfma_f32_16x16x32_bf16 v[96:99], v[108:111], a[80:83], v[96:99]\n\t"
"buffer_load_dwordx4 v243, s[12:15], s22 offen offset:2304 nt lds\n\t"
"v_mfma_f32_16x16x32_bf16 v[120:123], v[110:113], a[60:63], 0\n\t"
"s_nop 0\n\t"
"v_mfma_f32_16x16x32_bf16 v[120:123], v[110:113], a[84:87], v[120:123]\n\t"
"s_nop 0\n\t"
"v_mfma_f32_16x16x32_bf16 v[128:131], v[108:111], a[60:63], 0\n\t"
"s_nop 0\n\t"
"v_mfma_f32_16x16x32_bf16 v[128:131], v[108:111], a[84:87], v[128:131]\n\t"
"buffer_load_dwordx4 v244, s[12:15], s22 offen offset:3328 nt lds\n\t"
"v_mfma_f32_16x16x32_bf16 v[136:139], v[110:113], a[64:67], 0\n\t"
"s_waitcnt lgkmcnt(14)\n\t"
"v_mfma_f32_16x16x32_bf16 v[136:139], v[110:113], a[88:91], v[136:139]\n\t"
"s_nop 0\n\t"
"v_mfma_f32_16x16x32_bf16 v[152:155], v[108:111], a[64:67], 0\n\t"
"s_nop 0\n\t"
"v_mfma_f32_16x16x32_bf16 v[152:155], v[108:111], a[88:91], v[152:155]\n\t"
"s_mov_b64 exec, s[36:37]\n\t"
"buffer_load_dwordx4 v245, s[12:15], s22 offen offset:3840 nt lds\n\t"
"s_mov_b64 exec, -1\n\t"
"v_mfma_f32_16x16x32_bf16 v[160:163], v[110:113], a[68:71], 0\n\t"
"s_waitcnt lgkmcnt(12)\n\t"
"v_mfma_f32_16x16x32_bf16 v[160:163], v[110:113], a[92:95], v[160:163]\n\t"
"s_nop 0\n\t"
"v_mfma_f32_16x16x32_bf16 v[168:171], v[108:111], a[68:71], 0\n\t"
"s_nop 0\n\t"
"v_mfma_f32_16x16x32_bf16 v[168:171], v[108:111], a[92:95], v[168:171]\n\t"
"s_add_u32 m0, s43, 142848\n\t"
"s_mov_b64 exec, s[44:45]\n\t"
"buffer_load_dwordx4 v246, s[12:15], 0 offen offset:256 nt lds\n\t"
"s_mov_b64 exec, -1\n\t"
"s_waitcnt lgkmcnt(10)\n\t"
"v_mfma_f32_16x16x32_bf16 v[4:7], v[110:113], a[0:3], v[0:3]\n\t"
"s_nop 0\n\t"
"v_mfma_f32_16x16x32_bf16 v[0:3], v[114:117], a[0:3], v[0:3]\n\t"
"s_nop 0\n\t"
"v_mfma_f32_16x16x32_bf16 v[12:15], v[108:111], a[0:3], v[8:11]\n\t"
"s_nop 0\n\t"
"v_mfma_f32_16x16x32_bf16 v[8:11], v[112:115], a[0:3], v[8:11]\n\t"
"s_waitcnt lgkmcnt(8)\n\t"
"v_mfma_f32_16x16x32_bf16 v[20:23], v[110:113], a[4:7], v[16:19]\n\t"
"s_nop 0\n\t"
"v_mfma_f32_16x16x32_bf16 v[16:19], v[114:117], a[4:7], v[16:19]\n\t"
"s_nop 0\n\t"
"v_mfma_f32_16x16x32_bf16 v[28:31], v[108:111], a[4:7], v[24:27]\n\t"
"s_nop 0\n\t"
"v_cvt_pk_bf16_f32 v48, v4, v5\n\t"
"v_cvt_pk_bf16_f32 v49, v6, v7\n\t"
"v_mfma_f32_16x16x32_bf16 v[24:27], v[112:115], a[4:7], v[24:27]\n\t"
"s_nop 0\n\t"
"v_cvt_pk_bf16_f32 v72, v0, v1\n\t"
"v_cvt_pk_bf16_f32 v73, v2, v3\n\t"
"s_waitcnt lgkmcnt(6)\n\t"
"v_mfma_f32_16x16x32_bf16 v[36:39], v[110:113], a[8:11], v[32:35]\n\t"
"s_nop 0\n\t"
"v_cvt_pk_bf16_f32 v50, v12, v13\n\t"
"v_cvt_pk_bf16_f32 v51, v14, v15\n\t"
"v_mfma_f32_16x16x32_bf16 v[32:35], v[114:117], a[8:11], v[32:35]\n\t"
"s_nop 0\n\t"
"v_cvt_pk_bf16_f32 v74, v8, v9\n\t"
"v_cvt_pk_bf16_f32 v75, v10, v11\n\t"
"v_mfma_f32_16x16x32_bf16 v[100:103], v[108:111], a[8:11], v[96:99]\n\t"
"s_nop 0\n\t"
"v_cvt_pk_bf16_f32 v52, v20, v21\n\t"
"v_cvt_pk_bf16_f32 v53, v22, v23\n\t"
"v_mfma_f32_16x16x32_bf16 v[96:99], v[112:115], a[8:11], v[96:99]\n\t"
"s_nop 0\n\t"
"v_cvt_pk_bf16_f32 v76, v16, v17\n\t"
"v_cvt_pk_bf16_f32 v77, v18, v19\n\t"
"s_waitcnt lgkmcnt(4)\n\t"
"v_mfma_f32_16x16x32_bf16 v[124:127], v[110:113], a[12:15], v[120:123]\n\t"
"s_nop 0\n\t"
"v_cvt_pk_bf16_f32 v54, v28, v29\n\t"
"v_cvt_pk_bf16_f32 v55, v30, v31\n\t"
"v_mfma_f32_16x16x32_bf16 v[120:123], v[114:117], a[12:15], v[120:123]\n\t"
"s_nop 0\n\t"
"v_cvt_pk_bf16_f32 v78, v24, v25\n\t"
"v_cvt_pk_bf16_f32 v79, v26, v27\n\t"
"v_mfma_f32_16x16x32_bf16 v[132:135], v[108:111], a[12:15], v[128:131]\n\t"
"s_nop 0\n\t"
"v_cvt_pk_bf16_f32 v56, v36, v37\n\t"
"v_cvt_pk_bf16_f32 v57, v38, v39\n\t"
"v_mfma_f32_16x16x32_bf16 v[128:131], v[112:115], a[12:15], v[128:131]\n\t"
"s_nop 0\n\t"
"v_cvt_pk_bf16_f32 v80, v32, v33\n\t"
"v_cvt_pk_bf16_f32 v81, v34, v35\n\t"
"s_waitcnt lgkmcnt(2)\n\t"
"v_mfma_f32_16x16x32_bf16 v[140:143], v[110:113], a[16:19], v[136:139]\n\t"
"s_nop 0\n\t"
"v_cvt_pk_bf16_f32 v58, v100, v101\n\t"
"v_cvt_pk_bf16_f32 v59, v102, v103\n\t"
"v_mfma_f32_16x16x32_bf16 v[136:139], v[114:117], a[16:19], v[136:139]\n\t"
"s_nop 0\n\t"
"v_cvt_pk_bf16_f32 v82, v96, v97\n\t"
"v_cvt_pk_bf16_f32 v83, v98, v99\n\t"
"v_mfma_f32_16x16x32_bf16 v[156:159], v[108:111], a[16:19], v[152:155]\n\t"
"s_nop 0\n\t"
"v_cvt_pk_bf16_f32 v60, v124, v125\n\t"
"v_cvt_pk_bf16_f32 v61, v126, v127\n\t"
"v_mfma_f32_16x16x32_bf16 v[152:155], v[112:115], a[16:19], v[152:155]\n\t"
"s_nop 0\n\t"
"v_cvt_pk_bf16_f32 v84, v120, v121\n\t"
"v_cvt_pk_bf16_f32 v85, v122, v123\n\t"
"s_waitcnt lgkmcnt(0)\n\t"
"v_mfma_f32_16x16x32_bf16 v[164:167], v[110:113], a[20:23], v[160:163]\n\t"
"s_nop 0\n\t"
"v_cvt_pk_bf16_f32 v62, v132, v133\n\t"
"v_cvt_pk_bf16_f32 v63, v134, v135\n\t"
"v_mfma_f32_16x16x32_bf16 v[160:163], v[114:117], a[20:23], v[160:163]\n\t"
"s_nop 0\n\t"
"v_cvt_pk_bf16_f32 v86, v128, v129\n\t"
"v_cvt_pk_bf16_f32 v87, v130, v131\n\t"
"v_mfma_f32_16x16x32_bf16 v[172:175], v[108:111], a[20:23], v[168:171]\n\t"
"s_nop 0\n\t"
"v_cvt_pk_bf16_f32 v64, v140, v141\n\t"
"v_cvt_pk_bf16_f32 v65, v142, v143\n\t"
"v_mfma_f32_16x16x32_bf16 v[168:171], v[112:115], a[20:23], v[168:171]\n\t"
".p2align 3\n\t"
"v_cvt_pk_bf16_f32 v88, v136, v137\n\t"
"v_cvt_pk_bf16_f32 v89, v138, v139\n\t"
"v_cvt_pk_bf16_f32 v66, v156, v157\n\t"
"v_cvt_pk_bf16_f32 v67, v158, v159\n\t"
"v_cvt_pk_bf16_f32 v90, v152, v153\n\t"
"v_cvt_pk_bf16_f32 v91, v154, v155\n\t"
"v_cvt_pk_bf16_f32 v68, v164, v165\n\t"
"v_cvt_pk_bf16_f32 v69, v166, v167\n\t"
"v_cvt_pk_bf16_f32 v92, v160, v161\n\t"
"v_cvt_pk_bf16_f32 v93, v162, v163\n\t"
"v_cvt_pk_bf16_f32 v70, v172, v173\n\t"
"v_cvt_pk_bf16_f32 v71, v174, v175\n\t"
"v_cvt_pk_bf16_f32 v94, v168, v169\n\t"
"v_cvt_pk_bf16_f32 v95, v170, v171\n\t"
"s_barrier\n\t"
"v_mfma_f32_16x16x32_bf16 v[192:195], a[216:219], a[176:179], 0\n\t"
"s_add_u32 m0, s43, 53888\n\t"
"ds_read_b128 a[144:147], v224 offset:0\n\t"
"ds_read_b128 a[148:151], v224 offset:256\n\t"
"ds_read_b128 a[152:155], v224 offset:512\n\t"
"v_mfma_f32_16x16x32_bf16 v[196:199], a[216:219], a[180:183], 0\n\t"
"s_mov_b64 exec, s[44:45]\n\t"
"buffer_load_dwordx4 v247, s[12:15], 0 offen offset:128 nt lds\n\t"
"s_mov_b64 exec, -1\n\t"
"v_mfma_f32_16x16x32_bf16 v[200:203], a[216:219], a[184:187], 0\n\t"
"s_add_u32 m0, s26, 56192\n\t"
"ds_read_b128 a[160:163], v224 offset:2048\n\t"
"ds_read_b128 a[164:167], v224 offset:2304\n\t"
"s_mul_i32 s92, s73, 294912\n\t"
"v_mfma_f32_16x16x32_bf16 v[204:207], a[216:219], a[188:191], 0\n\t"
"s_add_u32 s92, s92, s8\n\t"
"s_addc_u32 s93, s9, 0x100000\n\t"
"s_mov_b32 s94, s10\n\t"
"v_mfma_f32_16x16x32_bf16 v[208:211], v[148:151], a[192:195], 0\n\t"
"s_mov_b32 s95, 0x800000\n\t"
"ds_read_b128 a[168:171], v224 offset:2560\n\t"
"ds_read_b128 a[156:159], v224 offset:768\n\t"
"v_mfma_f32_16x16x32_bf16 v[212:215], v[148:151], a[196:199], 0\n\t"
"s_waitcnt lgkmcnt(3)\n\t"
"ds_read_b128 a[172:175], v224 offset:2816\n\t"
"s_add_u32 s96, s92, 4096\n\t"
"s_addc_u32 s97, s93, 0\n\t"
"v_mfma_f32_16x16x32_bf16 v[96:99], v[48:51], a[144:147], 0\n\t"
"buffer_load_dwordx4 v236, s[12:15], s22 offen offset:128 nt lds\n\t"
"v_mfma_f32_16x16x32_bf16 v[96:99], v[52:55], a[160:163], v[96:99]\n\t"
"s_lshl_b32 m0, s73, 8\n\t"
"v_cvt_pk_bf16_f32 v192, v192, v196\n\t"
"v_cvt_pk_bf16_f32 v194, v194, v198\n\t"
"v_cvt_pk_bf16_f32 v195, v195, v199\n\t"
"v_mfma_f32_16x16x32_bf16 v[112:115], v[56:59], a[144:147], 0\n\t"
"s_mov_b64 s[98:99], s[94:95]\n\t"
"ds_write_addtid_b32 v192 offset:16544\n\t"
"ds_write_addtid_b32 v195 offset:28992\n\t"
"v_mfma_f32_16x16x32_bf16 v[112:115], v[60:63], a[160:163], v[112:115]\n\t"
"s_mov_b32 s16, s80\n\t"
"ds_write_addtid_b32 v194 offset:20672\n\t"
"v_cvt_pk_bf16_f32 v193, v193, v197\n\t"
"v_cvt_pk_bf16_f32 v200, v200, v204\n\t"
"v_cvt_pk_bf16_f32 v203, v203, v207\n\t"
"v_mfma_f32_16x16x32_bf16 v[128:131], v[64:67], a[144:147], 0\n\t"
"s_and_b32 s17, s81, 0xffff\n\t"
"ds_read_b128 a[144:147], v224 offset:1024\n\t"
"ds_write_addtid_b32 v193 offset:24864\n\t"
"v_mfma_f32_16x16x32_bf16 v[128:131], v[68:71], a[160:163], v[128:131]\n\t"
"s_waitcnt lgkmcnt(8)\n\t"
"v_cvt_pk_bf16_f32 v202, v202, v206\n\t"
"v_cvt_pk_bf16_f32 v209, v209, v213\n\t"
"v_cvt_pk_bf16_f32 v211, v211, v215\n\t"
"s_mov_b32 s18, 100663296\n\t"
"v_mfma_f32_16x16x32_bf16 v[132:135], v[64:67], a[148:151], 0\n\t"
"s_mov_b32 s19, 0x20000\n\t"
"ds_write_addtid_b32 v200 offset:18592\n\t"
"ds_write_addtid_b32 v203 offset:31040\n\t"
"v_mfma_f32_16x16x32_bf16 v[132:135], v[68:71], a[164:167], v[132:135]\n\t"
"s_mov_b32 s21, 4\n\t"
"ds_write_addtid_b32 v209 offset:25888\n\t"
"ds_write_addtid_b32 v211 offset:30016\n\t"
"v_mfma_f32_16x16x32_bf16 v[116:119], v[56:59], a[148:151], 0\n\t"
"s_add_u32 m0, s26, 56192\n\t"
"ds_read_b128 a[160:163], v224 offset:3072\n\t"
"v_cvt_pk_bf16_f32 v201, v201, v205\n\t"
"v_mfma_f32_16x16x32_bf16 v[116:119], v[60:63], a[164:167], v[116:119]\n\t"
"buffer_load_dword v190, v189, s[8:11], 0 offen sc1\n\t"
"s_waitcnt vmcnt(9)\n\t"
"s_barrier\n\t"
"v_mfma_f32_16x16x32_bf16 v[216:219], v[148:151], a[200:203], 0\n\t"
"Lpad0_%=:\n\t"
"s_cbranch_execz Lpad4_%=\n\t"
"s_mov_b32 s38, -2.0\n\t"
"ds_read_b64_tr_b16 a[176:177], v248 offset:20736\n\t"
"ds_read_b64_tr_b16 a[178:179], v248 offset:21248\n\t"
"v_mfma_f32_16x16x32_bf16 v[100:103], v[48:51], a[148:151], 0\n\t"
"s_mov_b32 s39, -2.0\n\t"
"ds_read_b128 a[148:151], v224 offset:1280\n\t"
"v_mbcnt_lo_u32_b32 v176, -1, 0\n\t"
"v_mfma_f32_16x16x32_bf16 v[100:103], v[52:55], a[164:167], v[100:103]\n\t"
"s_waitcnt lgkmcnt(15)\n\t"
"ds_read_b64_tr_b16 a[180:181], v249 offset:20736\n\t"
"v_mbcnt_hi_u32_b32 v176, -1, v176\n\t"
"v_bfe_u32 v156, v176, 2, 2\n\t"
"v_mfma_f32_16x16x32_bf16 v[104:107], v[48:51], a[152:155], 0\n\t"
"ds_read_b64_tr_b16 a[182:183], v249 offset:21248\n\t"
"v_and_b32 v157, 3, v176\n\t"
"v_mfma_f32_16x16x32_bf16 v[104:107], v[52:55], a[168:171], v[104:107]\n\t"
"ds_read_b64_tr_b16 a[184:185], v250 offset:20736\n\t"
"v_lshlrev_b32 v157, 3, v157\n\t"
"v_and_b32 v158, 48, v176\n\t"
"v_mfma_f32_16x16x32_bf16 v[220:223], v[148:151], a[204:207], 0\n\t"
"ds_read_b128 a[164:167], v224 offset:3328\n\t"
"buffer_load_dwordx4 v237, s[12:15], s22 offen offset:1152 nt lds\n\t"
"v_lshl_add_u32 v158, v157, 3, v158\n\t"
"v_and_b32 v159, 8, v176\n\t"
"v_mfma_f32_16x16x32_bf16 v[120:123], v[56:59], a[152:155], 0\n\t"
"s_lshl_b32 m0, s73, 8\n\t"
"ds_read_b64_tr_b16 a[186:187], v250 offset:21248\n\t"
"v_cvt_pk_bf16_f32 v208, v208, v212\n\t"
"v_mfma_f32_16x16x32_bf16 v[120:123], v[60:63], a[168:171], v[120:123]\n\t"
"ds_read_b64_tr_b16 a[188:189], v251 offset:20736\n\t"
"v_mul_u32_u24 v166, 516, v159\n\t"
"v_and_b32 v159, 4, v176\n\t"
"v_mul_u32_u24 v159, 2080, v159\n\t"
"v_mfma_f32_16x16x32_bf16 v[136:139], v[64:67], a[152:155], 0\n\t"
"ds_read_b128 a[152:155], v224 offset:1536\n\t"
"v_add3_u32 v166, v166, v158, v159\n\t"
"v_mfma_f32_16x16x32_bf16 v[136:139], v[68:71], a[168:171], v[136:139]\n\t"
"s_waitcnt lgkmcnt(15)\n\t"
"ds_read_b128 a[168:171], v224 offset:3584\n\t"
"v_accvgpr_write_b32 a212, v224\n\t"
"v_accvgpr_write_b32 a213, v166\n\t"
"v_lshlrev_b32 v158, 15, v176\n\t"
"v_and_b32 v158, 0x8000, v158\n\t"
"v_mfma_f32_16x16x32_bf16 v[140:143], v[64:67], a[156:159], 0\n\t"
"ds_read_b64_tr_b16 a[190:191], v251 offset:21248\n\t"
"v_cvt_pk_bf16_f32 v210, v210, v214\n\t"
"v_mfma_f32_16x16x32_bf16 v[140:143], v[68:71], a[172:175], v[140:143]\n\t"
"ds_read_b64_tr_b16 a[192:193], v248 offset:21888\n\t"
"v_or_b32 v167, 0x3f803f80, v158\n\t"
"v_and_b32 v158, 2, v176\n\t"
"v_mul_u32_u24 v158, 0x4000, v158\n\t"
"v_cvt_pk_bf16_f32 v219, v219, v223\n\t"
"v_mfma_f32_16x16x32_bf16 v[124:127], v[56:59], a[156:159], 0\n\t"
"ds_read_b64_tr_b16 a[194:195], v248 offset:22400\n\t"
"v_lshl_or_b32 v158, v158, 16, v158\n\t"
"v_mfma_f32_16x16x32_bf16 v[124:127], v[60:63], a[172:175], v[124:127]\n\t"
"s_nop 0\n\t"
"ds_write_addtid_b32 v202 offset:22720\n\t"
"ds_write_addtid_b32 v208 offset:17568\n\t"
"v_xor_b32 v166, v167, v158\n\t"
"v_accvgpr_write_b32 a210, v166\n\t"
"v_accvgpr_write_b32 a211, v167\n\t"
"s_add_u32 s61, s26, 4294900096\n\t"
"v_lshrrev_b32 v155, 4, v176\n\t"
"v_cvt_pk_bf16_f32 v218, v218, v222\n\t"
"v_mfma_f32_16x16x32_bf16 v[108:111], v[48:51], a[156:159], 0\n\t"
"ds_read_b128 a[156:159], v224 offset:1792\n\t"
"v_cvt_pk_bf16_f32 v217, v217, v221\n\t"
"v_mfma_f32_16x16x32_bf16 v[108:111], v[52:55], a[172:175], v[108:111]\n\t"
"s_waitcnt lgkmcnt(15)\n\t"
"ds_read_b128 a[172:175], v224 offset:3840\n\t"
"buffer_load_dword v191, v189, s[8:11], 0 offen sc1\n\t"
"v_cvt_pk_bf16_f32 v216, v216, v220\n\t"
"v_mfma_f32_16x16x32_bf16 v[0:3], v[72:75], a[144:147], v[96:99]\n\t"
"ds_write_addtid_b32 v201 offset:26912\n\t"
"v_and_b32 v158, 1, v156\n\t"
"v_mfma_f32_16x16x32_bf16 v[0:3], v[76:79], a[160:163], v[0:3]\n\t"
"s_nop 0\n\t"
"ds_write_addtid_b32 v210 offset:21696\n\t"
"ds_write_addtid_b32 v216 offset:19616\n\t"
"ds_write_addtid_b32 v218 offset:23744\n\t"
"ds_write_addtid_b32 v219 offset:32064\n\t"
"v_mfma_f32_16x16x32_bf16 v[16:19], v[80:83], a[144:147], v[112:115]\n\t"
"ds_write_addtid_b32 v217 offset:27936\n\t"
"v_lshrrev_b32 v159, 1, v156\n\t"
"v_mfma_f32_16x16x32_bf16 v[16:19], v[84:87], a[160:163], v[16:19]\n\t"
"ds_read_b64_tr_b16 a[196:197], v249 offset:21888\n\t"
"v_mul_u32_u24 v166, 1152, v159\n\t"
"v_mul_u32_u24 v167, 68352, v158\n\t"
"v_add3_u32 v166, v166, v157, s61\n\t"
"v_mfma_f32_16x16x32_bf16 v[32:35], v[88:91], a[144:147], v[128:131]\n\t"
"ds_read_b64_tr_b16 a[198:199], v249 offset:22400\n\t"
"v_add_u32 v166, v166, v167\n\t"
"v_mfma_f32_16x16x32_bf16 v[32:35], v[92:95], a[160:163], v[32:35]\n\t"
"s_waitcnt lgkmcnt(15)\n\t"
"ds_read_b64_tr_b16 a[200:201], v250 offset:21888\n\t"
"v_xor_b32 v159, v159, v158\n\t"
"v_lshl_add_u32 v166, v159, 6, v166\n\t"
"v_xad_u32 v167, v158, 0, v155\n\t"
"v_lshl_add_u32 v168, v167, 7, v166\n\t"
"v_and_b32 v167, 2, v167\n\t"
"v_mfma_f32_16x16x32_bf16 v[36:39], v[88:91], a[148:151], v[132:135]\n\t"
"ds_read_b64_tr_b16 a[202:203], v250 offset:22400\n\t"
"v_lshl_add_u32 v168, v167, 4, v168\n\t"
"v_mfma_f32_16x16x32_bf16 v[36:39], v[92:95], a[164:167], v[36:39]\n\t"
"ds_read_b64_tr_b16 a[204:205], v251 offset:21888\n\t"
"v_xor_b32 v168, 64, v168\n\t"
"v_accvgpr_write_b32 a208, v168\n\t"
"v_mfma_f32_16x16x32_bf16 v[20:23], v[80:83], a[148:151], v[116:119]\n\t"
"ds_read_b64_tr_b16 a[206:207], v251 offset:22400\n\t"
"v_xad_u32 v167, v158, 1, v155\n\t"
"v_mfma_f32_16x16x32_bf16 v[20:23], v[84:87], a[164:167], v[20:23]\n\t"
"v_lshl_add_u32 v168, v167, 7, v166\n\t"
"v_and_b32 v167, 2, v167\n\t"
"v_mfma_f32_16x16x32_bf16 v[4:7], v[72:75], a[148:151], v[100:103]\n\t"
"v_lshl_add_u32 v168, v167, 4, v168\n\t"
"v_accvgpr_write_b32 a209, v168\n\t"
"v_mfma_f32_16x16x32_bf16 v[4:7], v[76:79], a[164:167], v[4:7]\n\t"
"s_mov_b64 exec, 0\n\t"
"s_cmp_eq_u32 s73, 0\n\t"
"s_cbranch_scc1 Lpad11_%=\n\t"
"s_cmp_eq_u32 s73, 1\n\t"
"s_cbranch_scc1 Lpad10_%=\n\t"
"s_cmp_eq_u32 s73, 2\n\t"
"s_cbranch_scc1 Lpad9_%=\n\t"
"s_branch Lpad8_%=\n\t"
"Lret1_%=:\n\t"
"s_mov_b64 exec, -1\n\t"
"s_waitcnt lgkmcnt(6)\n\t"
"s_waitcnt vmcnt(2)\n\t"
"v_cmp_eq_u32 vcc, v190, v188\n\t"
"s_nop 4\n\t"
"s_cmp_eq_u64 vcc, 0\n\t"
"s_cbranch_scc1 Lgo_%=\n\t"
"s_sub_u32 s68, s68, 1\n\t"
"s_waitcnt vmcnt(0)\n\t"
"Lchk_%=:\n\t"
"v_cmp_eq_u32 vcc, v191, v188\n\t"
"s_nop 4\n\t"
"s_cmp_eq_u64 vcc, 0\n\t"
"s_cbranch_scc1 Lgo_%=\n\t"
"s_sub_u32 s68, s68, 1\n\t"
"s_cmp_eq_u32 s68, 0\n\t"
"s_cbranch_scc1 Lfb_%=\n\t"
"buffer_load_dword v191, v189, s[8:11], 0 offen sc1\n\t"
"s_waitcnt vmcnt(0)\n\t"
"s_branch Lchk_%=\n\t"
"Lgo_%=:\n\t"
"s_barrier\n\t"
"v_mfma_f32_16x16x32_bf16 v[8:11], v[72:75], a[152:155], v[104:107]\n\t"
"s_nop 0\n\t"
"ds_read_b128 a[144:147], v224 offset:16544\n\t"
"ds_read_b128 a[160:163], v224 offset:18592\n\t"
"v_mfma_f32_16x16x32_bf16 v[8:11], v[76:79], a[168:171], v[8:11]\n\t"
"s_mov_b32 s54, 98304\n\t"
"buffer_load_dwordx4 a[96:99], off, s[92:95], s54 offset:0 sc1\n\t"
"buffer_load_dwordx4 a[100:103], off, s[92:95], s54 offset:1024 sc1\n\t"
"v_mfma_f32_16x16x32_bf16 v[24:27], v[80:83], a[152:155], v[120:123]\n\t"
"ds_read_b128 a[148:151], v224 offset:16800\n\t"
"v_mbcnt_lo_u32_b32 v176, -1, 0\n\t"
"v_mfma_f32_16x16x32_bf16 v[24:27], v[84:87], a[168:171], v[24:27]\n\t"
"ds_read_b128 a[164:167], v224 offset:18848\n\t"
"buffer_load_dwordx4 a[104:107], off, s[92:95], s54 offset:2048 sc1\n\t"
"v_mbcnt_hi_u32_b32 v176, -1, v176\n\t"
"v_mfma_f32_16x16x32_bf16 v[40:43], v[88:91], a[152:155], v[136:139]\n\t"
"v_and_b32 v154, 15, v176\n\t"
"v_lshrrev_b32 v155, 4, v176\n\t"
"v_mfma_f32_16x16x32_bf16 v[40:43], v[92:95], a[168:171], v[40:43]\n\t"
"v_bfe_u32 v156, v176, 2, 2\n\t"
"v_and_b32 v157, 3, v176\n\t"
"v_lshlrev_b32 v157, 3, v157\n\t"
"s_add_u32 s60, s43, 33280\n\t"
"v_mfma_f32_16x16x32_bf16 v[44:47], v[88:91], a[156:159], v[140:143]\n\t"
"s_add_u32 s61, s26, 4294899008\n\t"
"ds_read_b128 a[168:171], v224 offset:19104\n\t"
"s_mul_i32 s62, s73, 96\n\t"
"v_mfma_f32_16x16x32_bf16 v[44:47], v[92:95], a[172:175], v[44:47]\n\t"
"buffer_load_dwordx4 a[108:111], off, s[92:95], s54 offset:3072 sc1\n\t"
"buffer_load_dwordx4 a[112:115], off, s[96:99], s54 offset:0 sc1\n\t"
"v_mfma_f32_16x16x32_bf16 v[28:31], v[80:83], a[156:159], v[124:127]\n\t"
"s_add_u32 s62, s62, 75008\n\t"
"v_min_u32 v158, 35, v176\n\t"
"v_lshl_add_u32 v254, v158, 4, s60\n\t"
"v_mfma_f32_16x16x32_bf16 v[28:31], v[84:87], a[172:175], v[28:31]\n\t"
"buffer_load_dwordx4 a[116:119], off, s[96:99], s54 offset:1024 sc1\n\t"
"v_mfma_f32_16x16x32_bf16 v[12:15], v[72:75], a[156:159], v[108:111]\n\t"
"ds_read_b128 a[152:155], v224 offset:17056\n\t"
"v_add_u32 v255, 68352, v254\n\t"
"v_mfma_f32_16x16x32_bf16 v[12:15], v[76:79], a[172:175], v[12:15]\n\t"
"s_mov_b32 s66, 172032\n\t"
"buffer_load_dwordx4 a[120:123], off, s[92:95], s66 offset:0 sc1\n\t"
"v_mfma_f32_16x16x32_bf16 v[192:195], a[216:219], a[176:179], 0\n\t"
"v_and_b32 v158, 1, v156\n\t"
"v_lshrrev_b32 v156, 3, v176\n\t"
"v_mfma_f32_16x16x32_bf16 v[196:199], a[216:219], a[180:183], 0\n\t"
"buffer_load_dwordx4 a[124:127], off, s[92:95], s66 offset:1024 sc1\n\t"
"v_add_u32 v159, v156, v158\n\t"
"v_lshl_add_u32 v166, v159, 7, v157\n\t"
"v_and_b32 v159, 2, v159\n\t"
"v_lshl_add_u32 v166, v159, 4, v166\n\t"
"v_xad_u32 v167, v158, 1, v156\n\t"
"v_mfma_f32_16x16x32_bf16 v[200:203], a[216:219], a[184:187], 0\n\t"
"ds_read_b128 a[172:175], v224 offset:19360\n\t"
"v_lshl_add_u32 v168, v167, 7, v157\n\t"
"v_mfma_f32_16x16x32_bf16 v[204:207], a[216:219], a[188:191], 0\n\t"
"buffer_load_dwordx4 a[128:131], off, s[92:95], s66 offset:2048 sc1\n\t"
"v_and_b32 v167, 2, v167\n\t"
"v_lshl_add_u32 v168, v167, 4, v168\n\t"
".p2align 3\n\t"
"v_pk_fma_f32 v[96:97], v[96:97], s[38:39], v[0:1] neg_lo:[1,0,1] neg_hi:[1,0,1]\n\t"
"v_pk_fma_f32 v[98:99], v[98:99], s[38:39], v[2:3] neg_lo:[1,0,1] neg_hi:[1,0,1]\n\t"
"v_pk_fma_f32 v[112:113], v[112:113], s[38:39], v[16:17] neg_lo:[1,0,1] neg_hi:[1,0,1]\n\t"
"v_pk_fma_f32 v[114:115], v[114:115], s[38:39], v[18:19] neg_lo:[1,0,1] neg_hi:[1,0,1]\n\t"
"v_pk_fma_f32 v[128:129], v[128:129], s[38:39], v[32:33] neg_lo:[1,0,1] neg_hi:[1,0,1]\n\t"
"v_pk_fma_f32 v[130:131], v[130:131], s[38:39], v[34:35] neg_lo:[1,0,1] neg_hi:[1,0,1]\n\t"
"v_pk_fma_f32 v[132:133], v[132:133], s[38:39], v[36:37] neg_lo:[1,0,1] neg_hi:[1,0,1]\n\t"
"v_pk_fma_f32 v[134:135], v[134:135], s[38:39], v[38:39] neg_lo:[1,0,1] neg_hi:[1,0,1]\n\t"
"v_pk_fma_f32 v[116:117], v[116:117], s[38:39], v[20:21] neg_lo:[1,0,1] neg_hi:[1,0,1]\n\t"
"v_pk_fma_f32 v[118:119], v[118:119], s[38:39], v[22:23] neg_lo:[1,0,1] neg_hi:[1,0,1]\n\t"
"v_pk_fma_f32 v[100:101], v[100:101], s[38:39], v[4:5] neg_lo:[1,0,1] neg_hi:[1,0,1]\n\t"
"v_pk_fma_f32 v[102:103], v[102:103], s[38:39], v[6:7] neg_lo:[1,0,1] neg_hi:[1,0,1]\n\t"
"buffer_load_dwordx4 a[132:135], off, s[92:95], s66 offset:3072 sc1\n\t"
"v_pk_fma_f32 v[104:105], v[104:105], s[38:39], v[8:9] neg_lo:[1,0,1] neg_hi:[1,0,1]\n\t"
"v_pk_fma_f32 v[106:107], v[106:107], s[38:39], v[10:11] neg_lo:[1,0,1] neg_hi:[1,0,1]\n\t"
"v_pk_fma_f32 v[120:121], v[120:121], s[38:39], v[24:25] neg_lo:[1,0,1] neg_hi:[1,0,1]\n\t"
"v_pk_fma_f32 v[122:123], v[122:123], s[38:39], v[26:27] neg_lo:[1,0,1] neg_hi:[1,0,1]\n\t"
"v_pk_fma_f32 v[136:137], v[136:137], s[38:39], v[40:41] neg_lo:[1,0,1] neg_hi:[1,0,1]\n\t"
"v_pk_fma_f32 v[138:139], v[138:139], s[38:39], v[42:43] neg_lo:[1,0,1] neg_hi:[1,0,1]\n\t"
"v_pk_fma_f32 v[140:141], v[140:141], s[38:39], v[44:45] neg_lo:[1,0,1] neg_hi:[1,0,1]\n\t"
"v_pk_fma_f32 v[142:143], v[142:143], s[38:39], v[46:47] neg_lo:[1,0,1] neg_hi:[1,0,1]\n\t"
"v_pk_fma_f32 v[124:125], v[124:125], s[38:39], v[28:29] neg_lo:[1,0,1] neg_hi:[1,0,1]\n\t"
"v_pk_fma_f32 v[126:127], v[126:127], s[38:39], v[30:31] neg_lo:[1,0,1] neg_hi:[1,0,1]\n\t"
"v_pk_fma_f32 v[108:109], v[108:109], s[38:39], v[12:13] neg_lo:[1,0,1] neg_hi:[1,0,1]\n\t"
"v_pk_fma_f32 v[110:111], v[110:111], s[38:39], v[14:15] neg_lo:[1,0,1] neg_hi:[1,0,1]\n\t"
"v_mfma_f32_16x16x32_bf16 v[208:211], a[216:219], a[192:195], 0\n\t"
"s_waitcnt lgkmcnt(11)\n\t"
"ds_read_b128 a[156:159], v224 offset:17312\n\t"
"v_sub_u32 v168, v168, v166\n\t"
"v_mfma_f32_16x16x32_bf16 v[212:215], a[216:219], a[196:199], 0\n\t"
"s_waitcnt lgkmcnt(10)\n\t"
"buffer_load_dwordx4 a[136:139], off, s[96:99], s66 offset:0 sc1\n\t"
"v_mfma_f32_16x16x32_bf16 v[216:219], a[216:219], a[200:203], 0\n\t"
"s_waitcnt lgkmcnt(8)\n\t"
"v_mul_u32_u24 v159, 68288, v158\n\t"
"v_add_u32 v166, v166, v159\n\t"
"v_lshl_add_u32 v168, v158, 7, v168\n\t"
"v_mfma_f32_16x16x32_bf16 v[220:223], a[216:219], a[204:207], 0\n\t"
"s_waitcnt lgkmcnt(6)\n\t"
"v_subrev_u32 v253, 64, v168\n\t"
"v_add_u32 v225, s61, v166\n\t"
"v_cvt_pk_bf16_f32 v193, v193, v197\n\t"
"v_cvt_pk_bf16_f32 v194, v194, v198\n\t"
"v_cvt_pk_bf16_f32 v195, v195, v199\n\t"
"v_mfma_f32_16x16x32_bf16 v[0:3], a[48:51], a[144:147], v[0:3]\n\t"
"ds_write_addtid_b32 v193 offset:8320\n\t"
"v_cvt_pk_bf16_f32 v192, v192, v196\n\t"
"v_mfma_f32_16x16x32_bf16 v[0:3], a[52:55], a[160:163], v[0:3]\n\t"
"s_nop 0\n\t"
"ds_write_addtid_b32 v194 offset:4128\n\t"
"buffer_load_dwordx4 a[140:143], off, s[96:99], s66 offset:1024 sc1\n\t"
"v_mul_u32_u24 v158, 400, v154\n\t"
"v_mfma_f32_16x16x32_bf16 v[16:19], a[56:59], a[144:147], v[16:19]\n\t"
"s_nop 0\n\t"
"ds_write_addtid_b32 v192 offset:0\n\t"
"v_cvt_pk_bf16_f32 v200, v200, v204\n\t"
"v_mfma_f32_16x16x32_bf16 v[16:19], a[60:63], a[160:163], v[16:19]\n\t"
"s_nop 0\n\t"
"ds_write_addtid_b32 v200 offset:2048\n\t"
"s_mov_b32 s69, 196608\n\t"
"buffer_load_dwordx4 a[0:3], off, s[92:95], s69 offset:0 sc1\n\t"
"v_mul_u32_u24 v159, 24, v155\n\t"
"v_mfma_f32_16x16x32_bf16 v[32:35], a[64:67], a[144:147], v[32:35]\n\t"
"s_nop 0\n\t"
"ds_read_b128 a[144:147], v224 offset:17568\n\t"
"v_cvt_pk_bf16_f32 v210, v210, v214\n\t"
"v_mfma_f32_16x16x32_bf16 v[32:35], a[68:71], a[160:163], v[32:35]\n\t"
"s_waitcnt lgkmcnt(9)\n\t"
"ds_read_b128 a[160:163], v224 offset:19616\n\t"
"v_cvt_pk_bf16_f32 v209, v209, v213\n\t"
"v_mfma_f32_16x16x32_bf16 v[36:39], a[64:67], a[148:151], v[36:39]\n\t"
"s_nop 0\n\t"
"ds_write_addtid_b32 v210 offset:5152\n\t"
"v_add3_u32 v228, v158, v159, s62\n\t"
"v_mfma_f32_16x16x32_bf16 v[36:39], a[68:71], a[164:167], v[36:39]\n\t"
"s_nop 0\n\t"
"ds_write_addtid_b32 v195 offset:12448\n\t"
"buffer_load_dwordx4 a[4:7], off, s[92:95], s69 offset:1024 sc1\n\t"
"s_barrier\n\t"
"v_mfma_f32_16x16x32_bf16 v[20:23], a[56:59], a[148:151], v[20:23]\n\t"
"s_mul_i32 s60, s73, 3200\n\t"
"ds_read_b64_tr_b16 a[176:177], v248 offset:41472\n\t"
"ds_read_b64_tr_b16 a[178:179], v248 offset:41984\n\t"
"v_mfma_f32_16x16x32_bf16 v[20:23], a[60:63], a[164:167], v[20:23]\n\t"
"s_nop 0\n\t"
"ds_read_b64_tr_b16 a[180:181], v249 offset:41472\n\t"
"buffer_load_dwordx4 a[8:11], off, s[92:95], s69 offset:2048 sc1\n\t"
"v_cvt_pk_bf16_f32 v217, v217, v221\n\t"
"v_mfma_f32_16x16x32_bf16 v[4:7], a[48:51], a[148:151], v[4:7]\n\t"
"s_nop 0\n\t"
"ds_read_b128 a[148:151], v224 offset:17824\n\t"
"v_cvt_pk_bf16_f32 v201, v201, v205\n\t"
"v_mfma_f32_16x16x32_bf16 v[4:7], a[52:55], a[164:167], v[4:7]\n\t"
"s_waitcnt lgkmcnt(14)\n\t"
"ds_read_b64_tr_b16 a[182:183], v249 offset:41984\n\t"
"v_cvt_pk_bf16_f32 v211, v211, v215\n\t"
"v_mfma_f32_16x16x32_bf16 v[8:11], a[48:51], a[152:155], v[8:11]\n\t"
"s_nop 0\n\t"
"ds_read_b64_tr_b16 a[184:185], v250 offset:41472\n\t"
"v_cvt_pk_bf16_f32 v208, v208, v212\n\t"
"v_mfma_f32_16x16x32_bf16 v[8:11], a[52:55], a[168:171], v[8:11]\n\t"
"s_nop 0\n\t"
"ds_read_b128 a[164:167], v224 offset:19872\n\t"
"buffer_load_dwordx4 a[12:15], off, s[92:95], s69 offset:3072 sc1\n\t"
"v_mfma_f32_16x16x32_bf16 v[24:27], a[56:59], a[152:155], v[24:27]\n\t"
"s_add_u32 s60, s60, 75008\n\t"
"ds_read_b64_tr_b16 a[186:187], v250 offset:41984\n\t"
"ds_read_b64_tr_b16 a[188:189], v251 offset:41472\n\t"
"v_mfma_f32_16x16x32_bf16 v[24:27], a[60:63], a[168:171], v[24:27]\n\t"
"s_and_b32 s61, s73, 2\n\t"
"ds_write_addtid_b32 v217 offset:11392\n\t"
"v_cvt_pk_bf16_f32 v203, v203, v207\n\t"
"v_mfma_f32_16x16x32_bf16 v[40:43], a[64:67], a[152:155], v[40:43]\n\t"
"s_nop 0\n\t"
"ds_read_b128 a[152:155], v224 offset:18080\n\t"
"v_cvt_pk_bf16_f32 v218, v218, v222\n\t"
"v_mfma_f32_16x16x32_bf16 v[40:43], a[68:71], a[168:171], v[40:43]\n\t"
"s_waitcnt lgkmcnt(15)\n\t"
"ds_read_b128 a[168:171], v224 offset:20128\n\t"
"buffer_load_dwordx4 a[16:19], off, s[96:99], s69 offset:0 sc1\n\t"
"v_mfma_f32_16x16x32_bf16 v[44:47], a[64:67], a[156:159], v[44:47]\n\t"
"s_lshl_b32 s61, s61, 7\n\t"
"ds_write_addtid_b32 v203 offset:14496\n\t"
"ds_read_b64_tr_b16 a[190:191], v251 offset:41984\n\t"
"v_mfma_f32_16x16x32_bf16 v[44:47], a[68:71], a[172:175], v[44:47]\n\t"
"s_nop 0\n\t"
"ds_write_addtid_b32 v209 offset:9344\n\t"
"buffer_load_dwordx4 a[20:23], off, s[96:99], s69 offset:1024 sc1\n\t"
"v_cvt_pk_bf16_f32 v216, v216, v220\n\t"
"v_cvt_pk_bf16_f32 v219, v219, v223\n\t"
"v_mfma_f32_16x16x32_bf16 v[28:31], a[56:59], a[156:159], v[28:31]\n\t"
"s_add_u32 s60, s60, s61\n\t"
"ds_read_b64_tr_b16 a[192:193], v248 offset:42624\n\t"
"v_cvt_pk_bf16_f32 v202, v202, v206\n\t"
"v_mfma_f32_16x16x32_bf16 v[28:31], a[60:63], a[172:175], v[28:31]\n\t"
"s_mul_i32 s61, s73, 393216\n\t"
"ds_write_addtid_b32 v201 offset:10368\n\t"
"ds_write_addtid_b32 v211 offset:13472\n\t"
"v_add_u32 v158, 0, v176\n\t"
"v_mfma_f32_16x16x32_bf16 v[12:15], a[48:51], a[156:159], v[12:15]\n\t"
"s_nop 0\n\t"
"ds_read_b128 a[156:159], v224 offset:18336\n\t"
"v_mul_u32_u24 v159, 0xab, v158\n\t"
"v_mfma_f32_16x16x32_bf16 v[12:15], a[52:55], a[172:175], v[12:15]\n\t"
"s_waitcnt lgkmcnt(15)\n\t"
"ds_read_b128 a[172:175], v224 offset:20384\n\t"
"s_mov_b32 s81, 270336\n\t"
"buffer_load_dwordx4 a[24:27], off, s[92:95], s81 offset:0 sc1\n\t"
"v_lshrrev_b32 v159, 12, v159\n\t"
"v_add_lshl_u32 v166, v158, v159, 4\n\t"
"v_mfma_f32_16x16x32_bf16 v[96:99], a[72:75], a[144:147], v[96:99]\n\t"
"s_nop 0\n\t"
"ds_write_addtid_b32 v208 offset:1024\n\t"
"v_add_u32 v229, s60, v166\n\t"
"v_mfma_f32_16x16x32_bf16 v[96:99], a[76:79], a[160:163], v[96:99]\n\t"
"s_nop 0\n\t"
"ds_write_addtid_b32 v202 offset:6176\n\t"
"ds_write_addtid_b32 v216 offset:3072\n\t"
"ds_write_addtid_b32 v218 offset:7200\n\t"
"buffer_load_dwordx4 a[28:31], off, s[92:95], s81 offset:1024 sc1\n\t"
"v_mad_u32_u24 v158, v159, 24, v158\n\t"
"v_lshl_add_u32 v232, v158, 4, s61\n\t"
"v_mfma_f32_16x16x32_bf16 v[112:115], a[80:83], a[144:147], v[112:115]\n\t"
"s_nop 0\n\t"
"ds_write_addtid_b32 v219 offset:15520\n\t"
"v_add_u32 v158, 64, v176\n\t"
"v_mfma_f32_16x16x32_bf16 v[112:115], a[84:87], a[160:163], v[112:115]\n\t"
"s_nop 0\n\t"
"ds_read_b64_tr_b16 a[194:195], v248 offset:43136\n\t"
"v_mul_u32_u24 v159, 0xab, v158\n\t"
"v_lshrrev_b32 v159, 12, v159\n\t"
"v_add_lshl_u32 v166, v158, v159, 4\n\t"
"v_mfma_f32_16x16x32_bf16 v[128:131], a[88:91], a[144:147], v[128:131]\n\t"
"s_nop 0\n\t"
"ds_read_b64_tr_b16 a[196:197], v249 offset:42624\n\t"
"v_add_u32 v230, s60, v166\n\t"
"v_mfma_f32_16x16x32_bf16 v[128:131], a[92:95], a[160:163], v[128:131]\n\t"
"s_waitcnt lgkmcnt(15)\n\t"
"ds_read_b64_tr_b16 a[198:199], v249 offset:43136\n\t"
"buffer_load_dwordx4 a[32:35], off, s[92:95], s81 offset:2048 sc1\n\t"
"v_mfma_f32_16x16x32_bf16 v[132:135], a[88:91], a[148:151], v[132:135]\n\t"
"s_nop 0\n\t"
"ds_read_b64_tr_b16 a[200:201], v250 offset:42624\n\t"
"v_mad_u32_u24 v158, v159, 24, v158\n\t"
"v_mfma_f32_16x16x32_bf16 v[132:135], a[92:95], a[164:167], v[132:135]\n\t"
"s_nop 0\n\t"
"ds_read_b64_tr_b16 a[202:203], v250 offset:43136\n\t"
"buffer_load_dwordx4 a[36:39], off, s[92:95], s81 offset:3072 sc1\n\t"
"v_mfma_f32_16x16x32_bf16 v[116:119], a[80:83], a[148:151], v[116:119]\n\t"
"s_nop 0\n\t"
"ds_read_b64_tr_b16 a[204:205], v251 offset:42624\n\t"
"ds_read_b64_tr_b16 a[206:207], v251 offset:43136\n\t"
"v_mfma_f32_16x16x32_bf16 v[116:119], a[84:87], a[164:167], v[116:119]\n\t"
"s_nop 0\n\t"
"v_lshl_add_u32 v233, v158, 4, s61\n\t"
"v_add_u32 v158, 128, v176\n\t"
"v_mul_u32_u24 v159, 0xab, v158\n\t"
"v_lshrrev_b32 v159, 12, v159\n\t"
"v_add_lshl_u32 v166, v158, v159, 4\n\t"
"v_add_u32 v231, s60, v166\n\t"
"v_mfma_f32_16x16x32_bf16 v[100:103], a[72:75], a[148:151], v[100:103]\n\t"
"s_nop 0\n\t"
"v_mad_u32_u24 v158, v159, 24, v158\n\t"
"v_lshl_add_u32 v234, v158, 4, s61\n\t"
"v_mfma_f32_16x16x32_bf16 v[100:103], a[76:79], a[164:167], v[100:103]\n\t"
"buffer_load_dwordx4 a[40:43], off, s[96:99], s81 offset:0 sc1\n\t"
"s_waitcnt lgkmcnt(7)\n\t"
"s_add_u32 s71, s71, 1\n\t"
"v_mov_b32 v215, s71\n\t"
"s_mov_b64 exec, 1\n\t"
"buffer_store_dword v215, off, s[8:11], s70 sc1\n\t"
"s_mov_b64 exec, -1\n\t"
"s_barrier\n\t"
"v_mfma_f32_16x16x32_bf16 v[104:107], a[72:75], a[152:155], v[104:107]\n\t"
"s_nop 0\n\t"
"ds_read_b128 a[148:151], v224 offset:256\n\t"
"v_add_u32 v248, 122368, v226\n\t"
"v_mfma_f32_16x16x32_bf16 v[104:107], a[76:79], a[168:171], v[104:107]\n\t"
"s_nop 0\n\t"
"ds_read_b128 a[144:147], v224 offset:0\n\t"
"v_xor_b32 v249, 32, v248\n\t"
"v_mfma_f32_16x16x32_bf16 v[120:123], a[80:83], a[152:155], v[120:123]\n\t"
"s_nop 0\n\t"
"ds_read_b128 a[160:163], v224 offset:2048\n\t"
"v_accvgpr_read_b32 v172, a220\n\t"
"v_mfma_f32_16x16x32_bf16 v[120:123], a[84:87], a[168:171], v[120:123]\n\t"
"s_nop 0\n\t"
"ds_read_b128 a[164:167], v224 offset:2304\n\t"
"buffer_load_dwordx4 a[44:47], off, s[96:99], s81 offset:1024 sc1\n\t"
"v_mfma_f32_16x16x32_bf16 v[136:139], a[88:91], a[152:155], v[136:139]\n\t"
"s_nop 0\n\t"
"v_and_b32 v172, 0xff7fff7f, v172\n\t"
"v_accvgpr_write_b32 a252, v172\n\t"
"v_mfma_f32_16x16x32_bf16 v[136:139], a[92:95], a[168:171], v[136:139]\n\t"
"s_mov_b32 s57, 122880\n\t"
"buffer_load_dwordx4 a[48:51], off, s[92:95], s57 offset:0 sc1\n\t"
"v_mfma_f32_16x16x32_bf16 v[140:143], a[88:91], a[156:159], v[140:143]\n\t"
"s_nop 0\n\t"
"ds_read_b128 a[168:171], v224 offset:2560\n\t"
"v_accvgpr_read_b32 v173, a221\n\t"
"v_mfma_f32_16x16x32_bf16 v[140:143], a[92:95], a[172:175], v[140:143]\n\t"
"s_nop 0\n\t"
"ds_read_b128 a[152:155], v224 offset:512\n\t"
"v_and_b32 v173, 0xff7fff7f, v173\n\t"
"v_mfma_f32_16x16x32_bf16 v[124:127], a[80:83], a[156:159], v[124:127]\n\t"
"s_nop 0\n\t"
"v_accvgpr_write_b32 a253, v173\n\t"
"v_accvgpr_read_b32 v174, a222\n\t"
"v_mfma_f32_16x16x32_bf16 v[124:127], a[84:87], a[172:175], v[124:127]\n\t"
"buffer_load_dwordx4 a[52:55], off, s[92:95], s57 offset:1024 sc1\n\t"
"v_mfma_f32_16x16x32_bf16 v[108:111], a[72:75], a[156:159], v[108:111]\n\t"
"s_nop 0\n\t"
"ds_read_b128 a[156:159], v224 offset:768\n\t"
"v_xor_b32 v250, 64, v248\n\t"
"v_mfma_f32_16x16x32_bf16 v[108:111], a[76:79], a[172:175], v[108:111]\n\t"
"buffer_load_dwordx4 a[56:59], off, s[92:95], s57 offset:2048 sc1\n\t"
"v_mfma_f32_16x16x32_bf16 v[192:195], a[216:219], a[176:179], 0\n\t"
"s_nop 0\n\t"
"ds_read_b128 a[172:175], v224 offset:2816\n\t"
"v_and_b32 v174, 0xff7fff7f, v174\n\t"
"v_mfma_f32_16x16x32_bf16 v[196:199], a[216:219], a[180:183], 0\n\t"
"s_nop 0\n\t"
"v_accvgpr_write_b32 a254, v174\n\t"
"v_xor_b32 v251, 96, v248\n\t"
"v_mfma_f32_16x16x32_bf16 v[200:203], a[216:219], a[184:187], 0\n\t"
"s_nop 0\n\t"
"ds_read_b64_tr_b16 a[178:179], v248 offset:128\n\t"
"v_accvgpr_read_b32 v175, a223\n\t"
"v_mfma_f32_16x16x32_bf16 v[204:207], a[216:219], a[188:191], 0\n\t"
"s_waitcnt lgkmcnt(15)\n\t"
"buffer_load_dwordx4 a[60:63], off, s[92:95], s57 offset:3072 sc1\n\t"
"v_mfma_f32_16x16x32_bf16 v[208:211], a[216:219], a[192:195], 0\n\t"
"s_waitcnt lgkmcnt(13)\n\t"
"ds_read_b64_tr_b16 a[176:177], v248 offset:0\n\t"
"v_and_b32 v175, 0xff7fff7f, v175\n\t"
"v_mfma_f32_16x16x32_bf16 v[212:215], a[216:219], a[196:199], 0\n\t"
"s_waitcnt lgkmcnt(12)\n\t"
"ds_read_b64_tr_b16 a[180:181], v249 offset:256\n\t"
"v_accvgpr_write_b32 a255, v175\n\t"
"v_mfma_f32_16x16x32_bf16 v[216:219], a[216:219], a[200:203], 0\n\t"
"s_waitcnt lgkmcnt(11)\n\t"
"buffer_load_dwordx4 a[64:67], off, s[96:99], s57 offset:0 sc1\n\t"
"v_mfma_f32_16x16x32_bf16 v[220:223], a[216:219], a[204:207], 0\n\t"
"s_waitcnt vmcnt(28) lgkmcnt(8)\n\t"
"v_cvt_pk_bf16_f32 v192, v192, v196\n\t"
"v_cvt_pk_bf16_f32 v194, v194, v198\n\t"
"v_mfma_f32_16x16x32_bf16 v[0:3], a[96:99], a[144:147], v[0:3]\n\t"
"s_mov_b32 s90, 0.5\n\t"
"ds_read_b64_tr_b16 a[186:187], v249 offset:128\n\t"
"ds_read_b64_tr_b16 a[184:185], v249 offset:0\n\t"
"v_mfma_f32_16x16x32_bf16 v[0:3], a[100:103], a[160:163], v[0:3]\n\t"
"s_waitcnt vmcnt(26)\n\t"
"buffer_load_dwordx4 a[68:71], off, s[96:99], s57 offset:1024 sc1\n\t"
"v_mfma_f32_16x16x32_bf16 v[16:19], a[104:107], a[144:147], v[16:19]\n\t"
"s_mov_b32 s91, 0.5\n\t"
"ds_write_addtid_b32 v194 offset:20672\n\t"
"v_cvt_pk_bf16_f32 v202, v202, v206\n\t"
"v_mfma_f32_16x16x32_bf16 v[16:19], a[108:111], a[160:163], v[16:19]\n\t"
"s_waitcnt vmcnt(25)\n\t"
"v_cvt_pk_bf16_f32 v208, v208, v212\n\t"
"v_cvt_pk_bf16_f32 v211, v211, v215\n\t"
"v_mfma_f32_16x16x32_bf16 v[32:35], a[112:115], a[144:147], v[32:35]\n\t"
"s_add_u32 s84, s92, 18432\n\t"
"s_addc_u32 s85, s93, 0\n\t"
"ds_read_b128 a[144:147], v224 offset:1024\n\t"
"v_mfma_f32_16x16x32_bf16 v[32:35], a[116:119], a[160:163], v[32:35]\n\t"
"s_waitcnt lgkmcnt(11)\n\t"
"s_mov_b32 s63, 147456\n\t"
"buffer_load_dwordx4 a[72:75], off, s[92:95], s63 offset:0 sc1\n\t"
"v_mfma_f32_16x16x32_bf16 v[36:39], a[112:115], a[148:151], v[36:39]\n\t"
"s_mov_b32 s41, 16384\n\t"
"ds_read_b128 a[160:163], v224 offset:3072\n\t"
"v_cvt_pk_bf16_f32 v201, v201, v205\n\t"
"v_mfma_f32_16x16x32_bf16 v[36:39], a[116:119], a[164:167], v[36:39]\n\t"
"s_nop 0\n\t"
"ds_write_addtid_b32 v202 offset:22720\n\t"
"v_cvt_pk_bf16_f32 v219, v219, v223\n\t"
"v_mfma_f32_16x16x32_bf16 v[20:23], a[104:107], a[148:151], v[20:23]\n\t"
"s_nop 0\n\t"
"ds_write_addtid_b32 v208 offset:17568\n\t"
"v_cvt_pk_bf16_f32 v195, v195, v199\n\t"
"v_mfma_f32_16x16x32_bf16 v[20:23], a[108:111], a[164:167], v[20:23]\n\t"
"buffer_load_dwordx4 a[76:79], off, s[92:95], s63 offset:1024 sc1\n\t"
"v_mfma_f32_16x16x32_bf16 v[4:7], a[96:99], a[148:151], v[4:7]\n\t"
"s_mov_b64 s[86:87], s[94:95]\n\t"
"ds_read_b128 a[148:151], v224 offset:1280\n\t"
"v_cvt_pk_bf16_f32 v200, v200, v204\n\t"
"v_mfma_f32_16x16x32_bf16 v[4:7], a[100:103], a[164:167], v[4:7]\n\t"
"s_waitcnt lgkmcnt(13)\n\t"
"ds_write_addtid_b32 v211 offset:30016\n\t"
"v_cvt_pk_bf16_f32 v218, v218, v222\n\t"
"v_mfma_f32_16x16x32_bf16 v[8:11], a[96:99], a[152:155], v[8:11]\n\t"
"s_nop 0\n\t"
"ds_read_b128 a[164:167], v224 offset:3328\n\t"
"v_cvt_pk_bf16_f32 v209, v209, v213\n\t"
"v_mfma_f32_16x16x32_bf16 v[8:11], a[100:103], a[168:171], v[8:11]\n\t"
"buffer_load_dwordx4 a[80:83], off, s[92:95], s63 offset:2048 sc1\n\t"
"v_mfma_f32_16x16x32_bf16 v[24:27], a[104:107], a[152:155], v[24:27]\n\t"
"s_nop 0\n\t"
"ds_write_addtid_b32 v209 offset:25888\n\t"
"v_cvt_pk_bf16_f32 v216, v216, v220\n\t"
"v_mfma_f32_16x16x32_bf16 v[24:27], a[108:111], a[168:171], v[24:27]\n\t"
"s_nop 0\n\t"
"ds_write_addtid_b32 v216 offset:19616\n\t"
"v_cvt_pk_bf16_f32 v203, v203, v207\n\t"
"v_mfma_f32_16x16x32_bf16 v[40:43], a[112:115], a[152:155], v[40:43]\n\t"
"s_xor_b64 s[88:89], s[84:85], s[92:93]\n\t"
"ds_read_b128 a[152:155], v224 offset:1536\n\t"
"v_cvt_pk_bf16_f32 v193, v193, v197\n\t"
"v_mfma_f32_16x16x32_bf16 v[40:43], a[116:119], a[168:171], v[40:43]\n\t"
"s_waitcnt lgkmcnt(15)\n\t"
"buffer_load_dwordx4 a[84:87], off, s[92:95], s63 offset:3072 sc1\n\t"
"v_mfma_f32_16x16x32_bf16 v[44:47], a[112:115], a[156:159], v[44:47]\n\t"
"s_nop 0\n\t"
"ds_read_b128 a[168:171], v224 offset:3584\n\t"
"v_cvt_pk_bf16_f32 v210, v210, v214\n\t"
"v_mfma_f32_16x16x32_bf16 v[44:47], a[116:119], a[172:175], v[44:47]\n\t"
"s_add_u32 s28, s84, 4096\n\t"
"s_addc_u32 s29, s85, 0\n\t"
"ds_write_addtid_b32 v218 offset:23744\n\t"
"v_mfma_f32_16x16x32_bf16 v[28:31], a[104:107], a[156:159], v[28:31]\n\t"
"s_mov_b32 s46, 32768\n\t"
"ds_write_addtid_b32 v193 offset:24864\n\t"
"ds_write_addtid_b32 v195 offset:28992\n\t"
"v_mfma_f32_16x16x32_bf16 v[28:31], a[108:111], a[172:175], v[28:31]\n\t"
"buffer_load_dwordx4 a[88:91], off, s[96:99], s63 offset:0 sc1\n\t"
"v_mfma_f32_16x16x32_bf16 v[12:15], a[96:99], a[156:159], v[12:15]\n\t"
"s_mov_b32 s40, 8192\n\t"
"ds_write_addtid_b32 v201 offset:26912\n\t"
"ds_write_addtid_b32 v219 offset:32064\n\t"
"v_mfma_f32_16x16x32_bf16 v[12:15], a[100:103], a[172:175], v[12:15]\n\t"
"s_waitcnt vmcnt(28) lgkmcnt(14)\n\t"
"ds_write_addtid_b32 v203 offset:31040\n\t"
"ds_write_addtid_b32 v210 offset:21696\n\t"
"s_mov_b32 s47, 40960\n\t"
"v_mfma_f32_16x16x32_bf16 v[96:99], a[120:123], a[144:147], v[96:99]\n\t"
"s_mov_b64 s[30:31], s[94:95]\n\t"
"ds_write_addtid_b32 v200 offset:18592\n\t"
"v_cvt_pk_bf16_f32 v217, v217, v221\n\t"
"v_mfma_f32_16x16x32_bf16 v[96:99], a[124:127], a[160:163], v[96:99]\n\t"
"Lpad1_%=:\n\t"
"s_cbranch_execz Lpad5_%=\n\t"
"s_waitcnt vmcnt(26)\n\t"
"buffer_load_dwordx4 a[92:95], off, s[96:99], s63 offset:1024 sc1\n\t"
"v_mfma_f32_16x16x32_bf16 v[112:115], a[128:131], a[144:147], v[112:115]\n\t"
"s_add_u32 s25, s24, 0xffeb0000\n\t"
"ds_write_addtid_b32 v192 offset:16544\n\t"
"ds_write_addtid_b32 v217 offset:27936\n\t"
"v_mfma_f32_16x16x32_bf16 v[112:115], a[132:135], a[160:163], v[112:115]\n\t"
"s_waitcnt vmcnt(25)\n\t"
"ds_read_b128 a[156:159], v224 offset:1792\n\t"
"ds_read_b128 a[172:175], v224 offset:3840\n\t"
"s_mov_b32 s49, 57344\n\t"
"v_mfma_f32_16x16x32_bf16 v[128:131], a[136:139], a[144:147], v[128:131]\n\t"
"s_add_u32 m0, s26, 56192\n\t"
"ds_read_b64_tr_b16 a[188:189], v248 offset:256\n\t"
"s_mov_b32 s42, 24576\n\t"
"s_add_u32 s27, s24, 0xffe80000\n\t"
"v_mfma_f32_16x16x32_bf16 v[128:131], a[140:143], a[160:163], v[128:131]\n\t"
"s_waitcnt lgkmcnt(15)\n\t"
"buffer_load_dwordx4 v238, s[12:15], s22 offen offset:2176 nt lds\n\t"
"v_mfma_f32_16x16x32_bf16 v[132:135], a[136:139], a[148:151], v[132:135]\n\t"
"s_mov_b32 s50, 65536\n\t"
"ds_read_b64_tr_b16 a[194:195], v250 offset:128\n\t"
"ds_read_b64_tr_b16 a[192:193], v250 offset:0\n\t"
"v_mfma_f32_16x16x32_bf16 v[132:135], a[140:143], a[164:167], v[132:135]\n\t"
"s_add_u32 s20, s24, 0xffd30000\n\t"
"ds_read_b64_tr_b16 a[196:197], v251 offset:256\n\t"
"ds_read_b64_tr_b16 a[202:203], v251 offset:128\n\t"
"v_mfma_f32_16x16x32_bf16 v[116:119], a[128:131], a[148:151], v[116:119]\n\t"
"s_add_u32 s24, s24, 0xffd00000\n\t"
"ds_read_b64_tr_b16 a[200:201], v251 offset:0\n\t"
"ds_read_b64_tr_b16 a[204:205], v250 offset:256\n\t"
"v_mfma_f32_16x16x32_bf16 v[116:119], a[132:135], a[164:167], v[116:119]\n\t"
"s_mov_b32 s48, 49152\n\t"
"v_mfma_f32_16x16x32_bf16 v[100:103], a[120:123], a[148:151], v[100:103]\n\t"
"v_mfma_f32_16x16x32_bf16 v[100:103], a[124:127], a[164:167], v[100:103]\n\t"
"buffer_load_dwordx4 v239, s[12:15], s22 offen offset:3200 nt lds\n\t"
"s_lshl_b32 m0, s73, 8\n\t"
"s_waitcnt lgkmcnt(7)\n\t"
"s_barrier\n\t"
"v_mfma_f32_16x16x32_bf16 v[104:107], a[120:123], a[152:155], v[104:107]\n\t"
"s_add_u32 m0, s26, 56192\n\t"
"ds_read_b128 a[148:151], v224 offset:16800\n\t"
"v_mfma_f32_16x16x32_bf16 v[104:107], a[124:127], a[168:171], v[104:107]\n\t"
"ds_read_b128 a[144:147], v224 offset:16544\n\t"
"v_mfma_f32_16x16x32_bf16 v[120:123], a[128:131], a[152:155], v[120:123]\n\t"
"ds_read_b128 a[160:163], v224 offset:18592\n\t"
"v_mfma_f32_16x16x32_bf16 v[120:123], a[132:135], a[168:171], v[120:123]\n\t"
"ds_read_b128 a[164:167], v224 offset:18848\n\t"
"v_mfma_f32_16x16x32_bf16 v[136:139], a[136:139], a[152:155], v[136:139]\n\t"
"v_mfma_f32_16x16x32_bf16 v[136:139], a[140:143], a[168:171], v[136:139]\n\t"
"s_mov_b32 s51, 73728\n\t"
"v_mfma_f32_16x16x32_bf16 v[140:143], a[136:139], a[156:159], v[140:143]\n\t"
"ds_read_b128 a[168:171], v224 offset:19104\n\t"
"v_mfma_f32_16x16x32_bf16 v[140:143], a[140:143], a[172:175], v[140:143]\n\t"
"ds_read_b128 a[152:155], v224 offset:17056\n\t"
"v_mfma_f32_16x16x32_bf16 v[124:127], a[128:131], a[156:159], v[124:127]\n\t"
"v_mfma_f32_16x16x32_bf16 v[124:127], a[132:135], a[172:175], v[124:127]\n\t"
"v_mfma_f32_16x16x32_bf16 v[108:111], a[120:123], a[156:159], v[108:111]\n\t"
"ds_read_b128 a[156:159], v224 offset:17312\n\t"
"v_mfma_f32_16x16x32_bf16 v[108:111], a[124:127], a[172:175], v[108:111]\n\t"
"s_waitcnt lgkmcnt(7)\n\t"
"ds_read_b128 a[172:175], v224 offset:19360\n\t"
"v_mfma_f32_4x4x4_16b_bf16 v[200:203], a[252:253], a[178:179], 0\n\t"
"s_mov_b64 exec, s[36:37]\n\t"
"buffer_load_dwordx4 v240, s[12:15], s22 offen offset:3712 nt lds\n\t"
"s_mov_b64 exec, -1\n\t"
"v_mfma_f32_4x4x4_16b_bf16 v[204:207], a[252:253], a[186:187], 0\n\t"
"ds_read_b64_tr_b16 a[178:179], v248 offset:20864\n\t"
"v_mfma_f32_4x4x4_16b_bf16 v[216:219], a[252:253], a[194:195], 0\n\t"
"ds_read_b64_tr_b16 a[186:187], v249 offset:20864\n\t"
"v_mfma_f32_4x4x4_16b_bf16 v[220:223], a[252:253], a[202:203], 0\n\t"
"ds_read_b64_tr_b16 a[194:195], v250 offset:20864\n\t"
"v_mfma_f32_4x4x4_16b_bf16 v[192:195], a[254:255], a[176:177], v[200:203]\n\t"
"s_lshl_b32 m0, s73, 8\n\t"
"v_mfma_f32_4x4x4_16b_bf16 v[196:199], a[254:255], a[184:185], v[204:207]\n\t"
"ds_read_b64_tr_b16 a[202:203], v251 offset:20864\n\t"
"v_mfma_f32_4x4x4_16b_bf16 v[200:203], a[254:255], a[180:181], v[200:203]\n\t"
"ds_read_b64_tr_b16 a[176:177], v248 offset:20736\n\t"
"v_mfma_f32_4x4x4_16b_bf16 v[204:207], a[254:255], a[188:189], v[204:207]\n\t"
"v_mfma_f32_4x4x4_16b_bf16 v[208:211], a[254:255], a[192:193], v[216:219]\n\t"
"ds_read_b64_tr_b16 a[184:185], v249 offset:20736\n\t"
"v_mfma_f32_4x4x4_16b_bf16 v[212:215], a[254:255], a[200:201], v[220:223]\n\t"
"ds_read_b64_tr_b16 a[180:181], v249 offset:20992\n\t"
"v_mfma_f32_4x4x4_16b_bf16 v[216:219], a[254:255], a[196:197], v[216:219]\n\t"
"ds_read_b64_tr_b16 a[188:189], v248 offset:20992\n\t"
"v_mfma_f32_4x4x4_16b_bf16 v[220:223], a[254:255], a[204:205], v[220:223]\n\t"
"s_waitcnt vmcnt(26) lgkmcnt(13)\n\t"
"s_mov_b32 s75, 221184\n\t"
"buffer_load_dwordx4 a[96:99], off, s[92:95], s75 offset:0 sc1\n\t"
"v_mfma_f32_16x16x32_bf16 v[0:3], a[0:3], a[144:147], v[0:3]\n\t"
"ds_read_b64_tr_b16 a[192:193], v250 offset:20736\n\t"
"v_cvt_pk_bf16_f32 v193, -v193, -v197\n\t"
"v_mfma_f32_16x16x32_bf16 v[0:3], a[4:7], a[160:163], v[0:3]\n\t"
"s_waitcnt vmcnt(25)\n\t"
"buffer_load_dwordx4 a[100:103], off, s[92:95], s75 offset:1024 sc1\n\t"
"v_mfma_f32_16x16x32_bf16 v[16:19], a[8:11], a[144:147], v[16:19]\n\t"
"ds_read_b64_tr_b16 a[200:201], v251 offset:20736\n\t"
"v_cvt_pk_bf16_f32 v194, -v194, -v198\n\t"
"v_mfma_f32_16x16x32_bf16 v[16:19], a[12:15], a[160:163], v[16:19]\n\t"
"s_waitcnt vmcnt(24)\n\t"
"buffer_load_dwordx4 a[104:107], off, s[92:95], s75 offset:2048 sc1\n\t"
"v_mfma_f32_16x16x32_bf16 v[32:35], a[16:19], a[144:147], v[32:35]\n\t"
"ds_read_b128 a[144:147], v224 offset:17568\n\t"
"v_cvt_pk_bf16_f32 v211, -v211, -v215\n\t"
"v_mfma_f32_16x16x32_bf16 v[32:35], a[20:23], a[160:163], v[32:35]\n\t"
"s_waitcnt lgkmcnt(15)\n\t"
"buffer_load_dwordx4 a[108:111], off, s[92:95], s75 offset:3072 sc1\n\t"
"v_mfma_f32_16x16x32_bf16 v[36:39], a[16:19], a[148:151], v[36:39]\n\t"
"ds_read_b128 a[160:163], v224 offset:19616\n\t"
"v_cvt_pk_bf16_f32 v195, -v195, -v199\n\t"
"v_mfma_f32_16x16x32_bf16 v[36:39], a[20:23], a[164:167], v[36:39]\n\t"
"ds_write_addtid_b32 v194 offset:1024\n\t"
"v_cvt_pk_bf16_f32 v202, v202, v206\n\t"
"v_mfma_f32_16x16x32_bf16 v[20:23], a[8:11], a[148:151], v[20:23]\n\t"
"ds_write_addtid_b32 v211 offset:7200\n\t"
"v_cvt_pk_bf16_f32 v208, -v208, -v212\n\t"
"v_mfma_f32_16x16x32_bf16 v[20:23], a[12:15], a[164:167], v[20:23]\n\t"
"buffer_load_dwordx4 a[112:115], off, s[96:99], s75 offset:0 sc1\n\t"
"v_mfma_f32_16x16x32_bf16 v[4:7], a[0:3], a[148:151], v[4:7]\n\t"
"ds_read_b128 a[148:151], v224 offset:17824\n\t"
"v_cvt_pk_bf16_f32 v200, v200, v204\n\t"
"v_mfma_f32_16x16x32_bf16 v[4:7], a[4:7], a[164:167], v[4:7]\n\t"
"s_waitcnt lgkmcnt(15)\n\t"
"ds_read_b128 a[164:167], v224 offset:19872\n\t"
"v_cvt_pk_bf16_f32 v192, -v192, -v196\n\t"
"v_mfma_f32_16x16x32_bf16 v[8:11], a[0:3], a[152:155], v[8:11]\n\t"
"ds_write_addtid_b32 v195 offset:5152\n\t"
"v_cvt_pk_bf16_f32 v209, -v209, -v213\n\t"
"v_mfma_f32_16x16x32_bf16 v[8:11], a[4:7], a[168:171], v[8:11]\n\t"
"buffer_load_dwordx4 a[116:119], off, s[96:99], s75 offset:1024 sc1\n\t"
"v_mfma_f32_16x16x32_bf16 v[24:27], a[8:11], a[152:155], v[24:27]\n\t"
"s_mov_b32 s55, 106496\n\t"
"ds_write_addtid_b32 v200 offset:8320\n\t"
"ds_write_addtid_b32 v209 offset:6176\n\t"
"v_mfma_f32_16x16x32_bf16 v[24:27], a[12:15], a[168:171], v[24:27]\n\t"
"ds_write_addtid_b32 v202 offset:9344\n\t"
"v_cvt_pk_bf16_f32 v217, v217, v221\n\t"
"v_mfma_f32_16x16x32_bf16 v[40:43], a[16:19], a[152:155], v[40:43]\n\t"
"ds_read_b128 a[152:155], v224 offset:18080\n\t"
"v_cvt_pk_bf16_f32 v201, v201, v205\n\t"
"v_mfma_f32_16x16x32_bf16 v[40:43], a[20:23], a[168:171], v[40:43]\n\t"
"s_mov_b32 s78, 245760\n\t"
"buffer_load_dwordx4 a[120:123], off, s[92:95], s78 offset:0 sc1\n\t"
"v_mfma_f32_16x16x32_bf16 v[44:47], a[16:19], a[156:159], v[44:47]\n\t"
"ds_read_b128 a[168:171], v224 offset:20128\n\t"
"v_cvt_pk_bf16_f32 v203, v203, v207\n\t"
"v_mfma_f32_16x16x32_bf16 v[44:47], a[20:23], a[172:175], v[44:47]\n\t"
"ds_write_addtid_b32 v193 offset:4128\n\t"
"v_cvt_pk_bf16_f32 v210, -v210, -v214\n\t"
"v_mfma_f32_16x16x32_bf16 v[28:31], a[8:11], a[156:159], v[28:31]\n\t"
"s_mov_b32 s52, 81920\n\t"
"ds_write_addtid_b32 v208 offset:2048\n\t"
"ds_write_addtid_b32 v217 offset:14496\n\t"
"v_mfma_f32_16x16x32_bf16 v[28:31], a[12:15], a[172:175], v[28:31]\n\t"
"buffer_load_dwordx4 a[124:127], off, s[92:95], s78 offset:1024 sc1\n\t"
"v_mfma_f32_16x16x32_bf16 v[12:15], a[0:3], a[156:159], v[12:15]\n\t"
"s_mov_b32 s58, 131072\n\t"
"ds_write_addtid_b32 v201 offset:12448\n\t"
"ds_write_addtid_b32 v210 offset:3072\n\t"
"v_mfma_f32_16x16x32_bf16 v[12:15], a[4:7], a[172:175], v[12:15]\n\t"
"s_waitcnt vmcnt(28) lgkmcnt(15)\n\t"
"ds_write_addtid_b32 v203 offset:13472\n\t"
"v_cvt_pk_bf16_f32 v219, v219, v223\n\t"
"v_mfma_f32_16x16x32_bf16 v[96:99], a[24:27], a[144:147], v[96:99]\n\t"
"ds_write_addtid_b32 v192 offset:0\n\t"
"v_cvt_pk_bf16_f32 v218, v218, v222\n\t"
"v_mfma_f32_16x16x32_bf16 v[96:99], a[28:31], a[160:163], v[96:99]\n\t"
"s_waitcnt vmcnt(26)\n\t"
"ds_write_addtid_b32 v218 offset:11392\n\t"
"v_cvt_pk_bf16_f32 v216, v216, v220\n\t"
"s_mov_b32 s53, 90112\n\t"
"v_mfma_f32_16x16x32_bf16 v[112:115], a[32:35], a[144:147], v[112:115]\n\t"
"s_mov_b32 s59, 139264\n\t"
"ds_write_addtid_b32 v216 offset:10368\n\t"
"ds_write_addtid_b32 v219 offset:15520\n\t"
"v_mfma_f32_16x16x32_bf16 v[112:115], a[36:39], a[160:163], v[112:115]\n\t"
"s_waitcnt vmcnt(23)\n\t"
"buffer_load_dwordx4 a[128:131], off, s[92:95], s78 offset:2048 sc1\n\t"
"v_mfma_f32_16x16x32_bf16 v[128:131], a[40:43], a[144:147], v[128:131]\n\t"
"ds_read_b128 a[156:159], v224 offset:18336\n\t"
"v_mfma_f32_16x16x32_bf16 v[128:131], a[44:47], a[160:163], v[128:131]\n\t"
"s_waitcnt lgkmcnt(15)\n\t"
"buffer_load_dwordx4 a[132:135], off, s[92:95], s78 offset:3072 sc1\n\t"
"v_mfma_f32_16x16x32_bf16 v[132:135], a[40:43], a[148:151], v[132:135]\n\t"
"ds_read_b128 a[172:175], v224 offset:20384\n\t"
"v_mfma_f32_16x16x32_bf16 v[132:135], a[44:47], a[164:167], v[132:135]\n\t"
"buffer_load_dwordx4 a[136:139], off, s[96:99], s78 offset:0 sc1\n\t"
"v_mfma_f32_16x16x32_bf16 v[116:119], a[32:35], a[148:151], v[116:119]\n\t"
"s_mov_b32 s56, 114688\n\t"
"ds_read_b64_tr_b16 a[196:197], v251 offset:20992\n\t"
"ds_read_b64_tr_b16 a[204:205], v250 offset:20992\n\t"
"v_mfma_f32_16x16x32_bf16 v[116:119], a[36:39], a[164:167], v[116:119]\n\t"
"buffer_load_dwordx4 a[140:143], off, s[96:99], s78 offset:1024 sc1\n\t"
"v_mfma_f32_16x16x32_bf16 v[100:103], a[24:27], a[148:151], v[100:103]\n\t"
"v_mfma_f32_16x16x32_bf16 v[100:103], a[28:31], a[164:167], v[100:103]\n\t"
"s_waitcnt lgkmcnt(2)\n\t"
"s_add_u32 s71, s71, 1\n\t"
"v_mov_b32 v215, s71\n\t"
"s_mov_b64 exec, 1\n\t"
"buffer_store_dword v215, off, s[8:11], s70 sc1\n\t"
"s_mov_b64 exec, -1\n\t"
"s_barrier\n\t"
"v_mfma_f32_16x16x32_bf16 v[104:107], a[24:27], a[152:155], v[104:107]\n\t"
"s_nop 0\n\t"
"ds_read_b128 a[144:147], v224 offset:0\n\t"
"ds_read_b128 a[148:151], v224 offset:256\n\t"
"v_mfma_f32_16x16x32_bf16 v[104:107], a[28:31], a[168:171], v[104:107]\n\t"
"buffer_load_dwordx4 a[248:251], v247, s[12:15], 0 offen offset:256 nt\n\t"
"v_mfma_f32_16x16x32_bf16 v[120:123], a[32:35], a[152:155], v[120:123]\n\t"
"ds_read_b128 a[160:163], v224 offset:2048\n\t"
"v_mfma_f32_16x16x32_bf16 v[120:123], a[36:39], a[168:171], v[120:123]\n\t"
"ds_read_b128 a[164:167], v224 offset:2304\n\t"
"v_mfma_f32_16x16x32_bf16 v[136:139], a[40:43], a[152:155], v[136:139]\n\t"
"v_mfma_f32_16x16x32_bf16 v[136:139], a[44:47], a[168:171], v[136:139]\n\t"
"v_mfma_f32_16x16x32_bf16 v[140:143], a[40:43], a[156:159], v[140:143]\n\t"
"v_mfma_f32_16x16x32_bf16 v[140:143], a[44:47], a[172:175], v[140:143]\n\t"
"buffer_load_dwordx4 a[0:3], off, s[96:99], s58 offset:0 sc1\n\t"
"v_mfma_f32_16x16x32_bf16 v[124:127], a[32:35], a[156:159], v[124:127]\n\t"
"ds_read_b128 a[168:171], v224 offset:2560\n\t"
"v_mfma_f32_16x16x32_bf16 v[124:127], a[36:39], a[172:175], v[124:127]\n\t"
"v_accvgpr_read_b32 v248, a208\n\t"
"v_mfma_f32_16x16x32_bf16 v[108:111], a[24:27], a[156:159], v[108:111]\n\t"
"v_mfma_f32_16x16x32_bf16 v[108:111], a[28:31], a[172:175], v[108:111]\n\t"
"s_waitcnt lgkmcnt(5)\n\t"
"buffer_load_dwordx4 a[4:7], off, s[96:99], s58 offset:1024 sc1\n\t"
"v_mfma_f32_4x4x4_16b_bf16 v[200:203], a[252:253], a[178:179], 0\n\t"
"ds_read_b128 a[172:175], v224 offset:2816\n\t"
"v_mfma_f32_4x4x4_16b_bf16 v[204:207], a[252:253], a[186:187], 0\n\t"
"v_mfma_f32_4x4x4_16b_bf16 v[216:219], a[252:253], a[194:195], 0\n\t"
"v_add_u32 v248, 101632, v248\n\t"
"v_mfma_f32_4x4x4_16b_bf16 v[220:223], a[252:253], a[202:203], 0\n\t"
".p2align 3\n\t"
"buffer_load_dwordx4 a[8:11], off, s[96:99], s58 offset:2048 sc1\n\t"
"v_pk_add_f32 v[96:97], v[0:1], v[96:97] neg_lo:[0,1] neg_hi:[0,1]\n\t"
"v_pk_add_f32 v[98:99], v[2:3], v[98:99] neg_lo:[0,1] neg_hi:[0,1]\n\t"
"v_pk_fma_f32 v[0:1], v[96:97], s[90:91], v[0:1] neg_lo:[1,0,0] neg_hi:[1,0,0]\n\t"
"v_pk_fma_f32 v[2:3], v[98:99], s[90:91], v[2:3] neg_lo:[1,0,0] neg_hi:[1,0,0]\n\t"
"v_pk_add_f32 v[112:113], v[16:17], v[112:113] neg_lo:[0,1] neg_hi:[0,1]\n\t"
"v_pk_add_f32 v[114:115], v[18:19], v[114:115] neg_lo:[0,1] neg_hi:[0,1]\n\t"
"v_pk_fma_f32 v[16:17], v[112:113], s[90:91], v[16:17] neg_lo:[1,0,0] neg_hi:[1,0,0]\n\t"
"v_pk_fma_f32 v[18:19], v[114:115], s[90:91], v[18:19] neg_lo:[1,0,0] neg_hi:[1,0,0]\n\t"
"v_pk_add_f32 v[128:129], v[32:33], v[128:129] neg_lo:[0,1] neg_hi:[0,1]\n\t"
"v_pk_add_f32 v[130:131], v[34:35], v[130:131] neg_lo:[0,1] neg_hi:[0,1]\n\t"
"v_pk_fma_f32 v[32:33], v[128:129], s[90:91], v[32:33] neg_lo:[1,0,0] neg_hi:[1,0,0]\n\t"
"v_pk_fma_f32 v[34:35], v[130:131], s[90:91], v[34:35] neg_lo:[1,0,0] neg_hi:[1,0,0]\n\t"
"v_pk_add_f32 v[132:133], v[36:37], v[132:133] neg_lo:[0,1] neg_hi:[0,1]\n\t"
"v_pk_add_f32 v[134:135], v[38:39], v[134:135] neg_lo:[0,1] neg_hi:[0,1]\n\t"
"v_pk_fma_f32 v[36:37], v[132:133], s[90:91], v[36:37] neg_lo:[1,0,0] neg_hi:[1,0,0]\n\t"
"v_pk_fma_f32 v[38:39], v[134:135], s[90:91], v[38:39] neg_lo:[1,0,0] neg_hi:[1,0,0]\n\t"
"v_pk_add_f32 v[116:117], v[20:21], v[116:117] neg_lo:[0,1] neg_hi:[0,1]\n\t"
"v_pk_add_f32 v[118:119], v[22:23], v[118:119] neg_lo:[0,1] neg_hi:[0,1]\n\t"
"v_pk_fma_f32 v[20:21], v[116:117], s[90:91], v[20:21] neg_lo:[1,0,0] neg_hi:[1,0,0]\n\t"
"v_pk_fma_f32 v[22:23], v[118:119], s[90:91], v[22:23] neg_lo:[1,0,0] neg_hi:[1,0,0]\n\t"
"v_pk_add_f32 v[100:101], v[4:5], v[100:101] neg_lo:[0,1] neg_hi:[0,1]\n\t"
"v_pk_add_f32 v[102:103], v[6:7], v[102:103] neg_lo:[0,1] neg_hi:[0,1]\n\t"
"v_pk_fma_f32 v[4:5], v[100:101], s[90:91], v[4:5] neg_lo:[1,0,0] neg_hi:[1,0,0]\n\t"
"v_pk_fma_f32 v[6:7], v[102:103], s[90:91], v[6:7] neg_lo:[1,0,0] neg_hi:[1,0,0]\n\t"
"v_pk_add_f32 v[104:105], v[8:9], v[104:105] neg_lo:[0,1] neg_hi:[0,1]\n\t"
"v_pk_add_f32 v[106:107], v[10:11], v[106:107] neg_lo:[0,1] neg_hi:[0,1]\n\t"
"v_pk_fma_f32 v[8:9], v[104:105], s[90:91], v[8:9] neg_lo:[1,0,0] neg_hi:[1,0,0]\n\t"
"v_pk_fma_f32 v[10:11], v[106:107], s[90:91], v[10:11] neg_lo:[1,0,0] neg_hi:[1,0,0]\n\t"
"v_pk_add_f32 v[120:121], v[24:25], v[120:121] neg_lo:[0,1] neg_hi:[0,1]\n\t"
"v_pk_add_f32 v[122:123], v[26:27], v[122:123] neg_lo:[0,1] neg_hi:[0,1]\n\t"
"v_pk_fma_f32 v[24:25], v[120:121], s[90:91], v[24:25] neg_lo:[1,0,0] neg_hi:[1,0,0]\n\t"
"v_pk_fma_f32 v[26:27], v[122:123], s[90:91], v[26:27] neg_lo:[1,0,0] neg_hi:[1,0,0]\n\t"
"v_pk_add_f32 v[136:137], v[40:41], v[136:137] neg_lo:[0,1] neg_hi:[0,1]\n\t"
"v_pk_add_f32 v[138:139], v[42:43], v[138:139] neg_lo:[0,1] neg_hi:[0,1]\n\t"
"v_pk_fma_f32 v[40:41], v[136:137], s[90:91], v[40:41] neg_lo:[1,0,0] neg_hi:[1,0,0]\n\t"
"v_pk_fma_f32 v[42:43], v[138:139], s[90:91], v[42:43] neg_lo:[1,0,0] neg_hi:[1,0,0]\n\t"
"v_pk_add_f32 v[140:141], v[44:45], v[140:141] neg_lo:[0,1] neg_hi:[0,1]\n\t"
"v_pk_add_f32 v[142:143], v[46:47], v[142:143] neg_lo:[0,1] neg_hi:[0,1]\n\t"
"v_pk_fma_f32 v[44:45], v[140:141], s[90:91], v[44:45] neg_lo:[1,0,0] neg_hi:[1,0,0]\n\t"
"v_pk_fma_f32 v[46:47], v[142:143], s[90:91], v[46:47] neg_lo:[1,0,0] neg_hi:[1,0,0]\n\t"
"v_pk_add_f32 v[124:125], v[28:29], v[124:125] neg_lo:[0,1] neg_hi:[0,1]\n\t"
"v_pk_add_f32 v[126:127], v[30:31], v[126:127] neg_lo:[0,1] neg_hi:[0,1]\n\t"
"v_pk_fma_f32 v[28:29], v[124:125], s[90:91], v[28:29] neg_lo:[1,0,0] neg_hi:[1,0,0]\n\t"
"v_pk_fma_f32 v[30:31], v[126:127], s[90:91], v[30:31] neg_lo:[1,0,0] neg_hi:[1,0,0]\n\t"
"v_pk_add_f32 v[108:109], v[12:13], v[108:109] neg_lo:[0,1] neg_hi:[0,1]\n\t"
"v_pk_add_f32 v[110:111], v[14:15], v[110:111] neg_lo:[0,1] neg_hi:[0,1]\n\t"
"v_pk_fma_f32 v[12:13], v[108:109], s[90:91], v[12:13] neg_lo:[1,0,0] neg_hi:[1,0,0]\n\t"
"v_pk_fma_f32 v[14:15], v[110:111], s[90:91], v[14:15] neg_lo:[1,0,0] neg_hi:[1,0,0]\n\t"
"v_mfma_f32_4x4x4_16b_bf16 v[192:195], a[254:255], a[176:177], v[200:203]\n\t"
"ds_read_b128 a[152:155], v224 offset:512\n\t"
"v_mfma_f32_4x4x4_16b_bf16 v[196:199], a[254:255], a[184:185], v[204:207]\n\t"
"v_mfma_f32_4x4x4_16b_bf16 v[200:203], a[254:255], a[180:181], v[200:203]\n\t"
"v_mfma_f32_4x4x4_16b_bf16 v[204:207], a[254:255], a[188:189], v[204:207]\n\t"
"v_xor_b32 v249, 32, v248\n\t"
"v_mfma_f32_4x4x4_16b_bf16 v[208:211], a[254:255], a[192:193], v[216:219]\n\t"
"ds_read_b128 a[156:159], v224 offset:768\n\t"
"v_mfma_f32_4x4x4_16b_bf16 v[212:215], a[254:255], a[200:201], v[220:223]\n\t"
"v_mfma_f32_4x4x4_16b_bf16 v[216:219], a[254:255], a[196:197], v[216:219]\n\t"
"v_mfma_f32_4x4x4_16b_bf16 v[220:223], a[254:255], a[204:205], v[220:223]\n\t"
"s_waitcnt vmcnt(30) lgkmcnt(5)\n\t"
"buffer_load_dwordx4 a[12:15], off, s[96:99], s58 offset:3072 sc1\n\t"
"v_mfma_f32_16x16x32_bf16 v[0:3], a[48:51], a[144:147], v[0:3]\n\t"
"s_nop 0\n\t"
"v_cvt_pk_bf16_f32 v194, -v194, -v198\n\t"
"v_cvt_pk_bf16_f32 v195, -v195, -v199\n\t"
"v_mfma_f32_16x16x32_bf16 v[0:3], a[52:55], a[160:163], v[0:3]\n\t"
"s_waitcnt vmcnt(29)\n\t"
"buffer_load_dwordx4 a[16:19], off, s[92:95], s59 offset:0 sc1\n\t"
"v_mfma_f32_16x16x32_bf16 v[16:19], a[56:59], a[144:147], v[16:19]\n\t"
"s_nop 0\n\t"
"ds_write_addtid_b32 v194 offset:17568\n\t"
"v_cvt_pk_bf16_f32 v202, v202, v206\n\t"
"v_mfma_f32_16x16x32_bf16 v[16:19], a[60:63], a[160:163], v[16:19]\n\t"
"s_waitcnt vmcnt(28)\n\t"
"buffer_load_dwordx4 a[20:23], off, s[92:95], s59 offset:1024 sc1\n\t"
"v_mfma_f32_16x16x32_bf16 v[32:35], a[64:67], a[144:147], v[32:35]\n\t"
"s_nop 0\n\t"
"ds_read_b128 a[144:147], v224 offset:1024\n\t"
"v_cvt_pk_bf16_f32 v211, -v211, -v215\n\t"
"v_mfma_f32_16x16x32_bf16 v[32:35], a[68:71], a[160:163], v[32:35]\n\t"
"s_waitcnt lgkmcnt(6)\n\t"
"buffer_load_dwordx4 a[24:27], off, s[96:99], s57 offset:2048 sc1\n\t"
"v_mfma_f32_16x16x32_bf16 v[36:39], a[64:67], a[148:151], v[36:39]\n\t"
"s_nop 0\n\t"
"ds_read_b128 a[160:163], v224 offset:3072\n\t"
"v_cvt_pk_bf16_f32 v201, v201, v205\n\t"
"v_mfma_f32_16x16x32_bf16 v[36:39], a[68:71], a[164:167], v[36:39]\n\t"
"s_nop 0\n\t"
"ds_write_addtid_b32 v211 offset:23744\n\t"
"v_cvt_pk_bf16_f32 v203, v203, v207\n\t"
"v_mfma_f32_16x16x32_bf16 v[20:23], a[56:59], a[148:151], v[20:23]\n\t"
"s_nop 0\n\t"
"ds_write_addtid_b32 v203 offset:30016\n\t"
"v_cvt_pk_bf16_f32 v217, v217, v221\n\t"
"v_mfma_f32_16x16x32_bf16 v[20:23], a[60:63], a[164:167], v[20:23]\n\t"
"buffer_load_dwordx4 a[28:31], off, s[96:99], s57 offset:3072 sc1\n\t"
"v_mfma_f32_16x16x32_bf16 v[4:7], a[48:51], a[148:151], v[4:7]\n\t"
"s_nop 0\n\t"
"ds_read_b128 a[148:151], v224 offset:1280\n\t"
"v_cvt_pk_bf16_f32 v192, -v192, -v196\n\t"
"v_mfma_f32_16x16x32_bf16 v[4:7], a[52:55], a[164:167], v[4:7]\n\t"
"s_waitcnt lgkmcnt(7)\n\t"
"ds_write_addtid_b32 v201 offset:28992\n\t"
"v_cvt_pk_bf16_f32 v193, -v193, -v197\n\t"
"s_mov_b32 s65, 163840\n\t"
"v_mfma_f32_16x16x32_bf16 v[8:11], a[48:51], a[152:155], v[8:11]\n\t"
"s_nop 0\n\t"
"ds_read_b128 a[164:167], v224 offset:3328\n\t"
"v_cvt_pk_bf16_f32 v219, v219, v223\n\t"
"v_mfma_f32_16x16x32_bf16 v[8:11], a[52:55], a[168:171], v[8:11]\n\t"
"buffer_load_dwordx4 a[32:35], off, s[92:95], s58 offset:0 sc1\n\t"
"v_mfma_f32_16x16x32_bf16 v[24:27], a[56:59], a[152:155], v[24:27]\n\t"
"s_mov_b32 s68, 188416\n\t"
"ds_write_addtid_b32 v219 offset:32064\n\t"
"v_cvt_pk_bf16_f32 v208, -v208, -v212\n\t"
"v_mfma_f32_16x16x32_bf16 v[24:27], a[60:63], a[168:171], v[24:27]\n\t"
"s_nop 0\n\t"
"ds_write_addtid_b32 v193 offset:20672\n\t"
"v_cvt_pk_bf16_f32 v216, v216, v220\n\t"
"v_mfma_f32_16x16x32_bf16 v[40:43], a[64:67], a[152:155], v[40:43]\n\t"
"s_nop 0\n\t"
"ds_read_b128 a[152:155], v224 offset:1536\n\t"
"v_cvt_pk_bf16_f32 v218, v218, v222\n\t"
"v_mfma_f32_16x16x32_bf16 v[40:43], a[68:71], a[168:171], v[40:43]\n\t"
"s_waitcnt lgkmcnt(11)\n\t"
"buffer_load_dwordx4 a[36:39], off, s[92:95], s58 offset:1024 sc1\n\t"
"v_mfma_f32_16x16x32_bf16 v[44:47], a[64:67], a[156:159], v[44:47]\n\t"
"s_nop 0\n\t"
"ds_read_b128 a[168:171], v224 offset:3584\n\t"
"v_cvt_pk_bf16_f32 v200, v200, v204\n\t"
"v_mfma_f32_16x16x32_bf16 v[44:47], a[68:71], a[172:175], v[44:47]\n\t"
"s_nop 0\n\t"
"ds_write_addtid_b32 v200 offset:24864\n\t"
"v_cvt_pk_bf16_f32 v210, -v210, -v214\n\t"
"v_mfma_f32_16x16x32_bf16 v[28:31], a[56:59], a[156:159], v[28:31]\n\t"
"s_mov_b32 s64, 155648\n\t"
"ds_write_addtid_b32 v195 offset:21696\n\t"
"ds_write_addtid_b32 v217 offset:31040\n\t"
"v_mfma_f32_16x16x32_bf16 v[28:31], a[60:63], a[172:175], v[28:31]\n\t"
"buffer_load_dwordx4 a[40:43], off, s[92:95], s58 offset:2048 sc1\n\t"
"v_mfma_f32_16x16x32_bf16 v[12:15], a[48:51], a[156:159], v[12:15]\n\t"
"s_mov_b32 s72, 204800\n\t"
"ds_write_addtid_b32 v192 offset:16544\n\t"
"ds_write_addtid_b32 v202 offset:25888\n\t"
"v_mfma_f32_16x16x32_bf16 v[12:15], a[52:55], a[172:175], v[12:15]\n\t"
"s_waitcnt vmcnt(32) lgkmcnt(14)\n\t"
"ds_write_addtid_b32 v216 offset:26912\n\t"
"v_cvt_pk_bf16_f32 v209, -v209, -v213\n\t"
"v_mfma_f32_16x16x32_bf16 v[96:99], a[72:75], a[144:147], v[96:99]\n\t"
"s_mov_b32 s74, 212992\n\t"
"ds_write_addtid_b32 v208 offset:18592\n\t"
"ds_write_addtid_b32 v210 offset:19616\n\t"
"v_mfma_f32_16x16x32_bf16 v[96:99], a[76:79], a[160:163], v[96:99]\n\t"
"s_waitcnt vmcnt(30)\n\t"
"buffer_load_dwordx4 a[44:47], off, s[92:95], s58 offset:3072 sc1\n\t"
"v_mfma_f32_16x16x32_bf16 v[112:115], a[80:83], a[144:147], v[112:115]\n\t"
"s_mov_b32 s67, 180224\n\t"
"ds_write_addtid_b32 v209 offset:22720\n\t"
"ds_write_addtid_b32 v218 offset:27936\n\t"
"v_mfma_f32_16x16x32_bf16 v[112:115], a[84:87], a[160:163], v[112:115]\n\t"
"s_waitcnt vmcnt(29)\n\t"
"v_mfma_f32_16x16x32_bf16 v[128:131], a[88:91], a[144:147], v[128:131]\n\t"
"s_add_u32 m0, s43, 33280\n\t"
"ds_read_b128 a[156:159], v224 offset:1792\n\t"
"v_xor_b32 v250, 64, v248\n\t"
"v_mfma_f32_16x16x32_bf16 v[128:131], a[92:95], a[160:163], v[128:131]\n\t"
"s_waitcnt lgkmcnt(15)\n\t"
"s_mov_b64 exec, s[44:45]\n\t"
"buffer_load_dwordx4 v247, s[12:15], 0 offen offset:0 nt lds\n\t"
"s_mov_b64 exec, -1\n\t"
"v_mfma_f32_16x16x32_bf16 v[132:135], a[88:91], a[148:151], v[132:135]\n\t"
"s_add_u32 m0, s26, 35584\n\t"
"ds_read_b128 a[172:175], v224 offset:3840\n\t"
"v_mfma_f32_16x16x32_bf16 v[132:135], a[92:95], a[164:167], v[132:135]\n\t"
"s_nop 0\n\t"
"v_mfma_f32_16x16x32_bf16 v[116:119], a[80:83], a[148:151], v[116:119]\n\t"
"s_nop 0\n\t"
"v_mfma_f32_16x16x32_bf16 v[116:119], a[84:87], a[164:167], v[116:119]\n\t"
"buffer_load_dwordx4 v236, s[12:15], s22 offen offset:0 nt lds\n\t"
"v_mfma_f32_16x16x32_bf16 v[100:103], a[72:75], a[148:151], v[100:103]\n\t"
"s_lshl_b32 m0, s73, 8\n\t"
"v_xor_b32 v251, 96, v248\n\t"
"v_mfma_f32_16x16x32_bf16 v[100:103], a[76:79], a[164:167], v[100:103]\n\t"
"s_waitcnt vmcnt(28)\n\t"
"s_waitcnt lgkmcnt(0)\n\t"
"s_barrier\n\t"
"v_mfma_f32_16x16x32_bf16 v[104:107], a[72:75], a[152:155], v[104:107]\n\t"
"s_add_u32 m0, s26, 35584\n\t"
"ds_read_b128 a[144:147], v224 offset:16544\n\t"
"ds_read_b128 a[164:167], v224 offset:18848\n\t"
"v_mfma_f32_16x16x32_bf16 v[104:107], a[76:79], a[168:171], v[104:107]\n\t"
"s_nop 0\n\t"
"ds_read_b128 a[160:163], v224 offset:18592\n\t"
"v_mfma_f32_16x16x32_bf16 v[120:123], a[80:83], a[152:155], v[120:123]\n\t"
"s_nop 0\n\t"
"ds_read_b128 a[148:151], v224 offset:16800\n\t"
"v_mfma_f32_16x16x32_bf16 v[120:123], a[84:87], a[168:171], v[120:123]\n\t"
"s_nop 0\n\t"
"ds_read_b64_tr_b16 a[176:177], v248 offset:20736\n\t"
"v_mfma_f32_16x16x32_bf16 v[136:139], a[88:91], a[152:155], v[136:139]\n\t"
"s_nop 0\n\t"
"ds_read_b64_tr_b16 a[178:179], v249 offset:20736\n\t"
"v_mfma_f32_16x16x32_bf16 v[136:139], a[92:95], a[168:171], v[136:139]\n\t"
"s_nop 0\n\t"
"ds_read_b64_tr_b16 a[180:181], v248 offset:21248\n\t"
"ds_read_b64_tr_b16 a[182:183], v249 offset:21248\n\t"
"v_mfma_f32_16x16x32_bf16 v[140:143], a[88:91], a[156:159], v[140:143]\n\t"
"s_nop 0\n\t"
"ds_read_b64_tr_b16 a[184:185], v250 offset:23040\n\t"
"v_mfma_f32_16x16x32_bf16 v[140:143], a[92:95], a[172:175], v[140:143]\n\t"
"buffer_load_dwordx4 v237, s[12:15], s22 offen offset:1024 nt lds\n\t"
"v_mfma_f32_16x16x32_bf16 v[124:127], a[80:83], a[156:159], v[124:127]\n\t"
"s_nop 0\n\t"
"ds_read_b128 a[152:155], v224 offset:17056\n\t"
"v_mfma_f32_16x16x32_bf16 v[124:127], a[84:87], a[172:175], v[124:127]\n\t"
"s_waitcnt lgkmcnt(2)\n\t"
"ds_read_b128 a[168:171], v224 offset:19104\n\t"
"v_mfma_f32_4x4x4_16b_bf16 v[192:195], a[210:211], a[176:177], 0\n\t"
"ds_read_b64_tr_b16 a[186:187], v251 offset:23040\n\t"
"v_mfma_f32_4x4x4_16b_bf16 v[196:199], a[210:211], a[178:179], 0\n\t"
"ds_read_b64_tr_b16 a[188:189], v250 offset:23552\n\t"
"v_mfma_f32_4x4x4_16b_bf16 v[200:203], a[210:211], a[180:181], 0\n\t"
"ds_read_b64_tr_b16 a[190:191], v251 offset:23552\n\t"
"v_mfma_f32_4x4x4_16b_bf16 v[204:207], a[210:211], a[182:183], 0\n\t"
"ds_read_b64_tr_b16 a[192:193], v250 offset:20736\n\t"
"v_mfma_f32_16x16x32_bf16 v[108:111], a[72:75], a[156:159], v[108:111]\n\t"
"s_mov_b32 s76, 229376\n\t"
"ds_read_b64_tr_b16 a[194:195], v251 offset:20736\n\t"
"ds_read_b64_tr_b16 a[196:197], v250 offset:21248\n\t"
"v_mfma_f32_16x16x32_bf16 v[108:111], a[76:79], a[172:175], v[108:111]\n\t"
"s_waitcnt vmcnt(27)\n\t"
"buffer_load_dwordx4 v238, s[12:15], s22 offen offset:2048 nt lds\n\t"
"v_mfma_f32_16x16x32_bf16 v[0:3], a[96:99], a[144:147], v[0:3]\n\t"
"s_nop 0\n\t"
"ds_read_b128 a[156:159], v224 offset:17312\n\t"
"v_cvt_pk_bf16_f32 v196, v192, v196\n\t"
"v_mfma_f32_16x16x32_bf16 v[0:3], a[100:103], a[160:163], v[0:3]\n\t"
"s_waitcnt vmcnt(26)\n\t"
"ds_read_b64_tr_b16 a[198:199], v251 offset:21248\n\t"
"v_cvt_pk_bf16_f32 v194, v194, v198\n\t"
"v_mfma_f32_16x16x32_bf16 v[16:19], a[104:107], a[144:147], v[16:19]\n\t"
"s_nop 0\n\t"
"ds_read_b64_tr_b16 a[200:201], v248 offset:23040\n\t"
"v_cvt_pk_bf16_f32 v195, v195, v199\n\t"
"v_mfma_f32_16x16x32_bf16 v[16:19], a[108:111], a[160:163], v[16:19]\n\t"
"s_waitcnt vmcnt(24)\n\t"
"ds_read_b64_tr_b16 a[202:203], v249 offset:23040\n\t"
"v_cvt_pk_bf16_f32 v192, v202, v206\n\t"
"v_mfma_f32_16x16x32_bf16 v[32:35], a[112:115], a[144:147], v[32:35]\n\t"
"s_nop 0\n\t"
"ds_read_b64_tr_b16 a[204:205], v248 offset:23552\n\t"
"v_cvt_pk_bf16_f32 v199, v201, v205\n\t"
"v_mfma_f32_16x16x32_bf16 v[32:35], a[116:119], a[160:163], v[32:35]\n\t"
"s_waitcnt lgkmcnt(8)\n\t"
"ds_read_b128 a[144:147], v224 offset:17568\n\t"
"v_cvt_pk_bf16_f32 v197, v193, v197\n\t"
"v_mfma_f32_4x4x4_16b_bf16 v[208:211], a[210:211], a[184:185], 0\n\t"
"buffer_load_dwordx4 v239, s[12:15], s22 offen offset:3072 nt lds\n\t"
"v_mfma_f32_4x4x4_16b_bf16 v[212:215], a[210:211], a[186:187], 0\n\t"
"ds_read_b128 a[172:175], v224 offset:19360\n\t"
"v_mfma_f32_4x4x4_16b_bf16 v[216:219], a[210:211], a[188:189], 0\n\t"
"s_lshl_b32 m0, s73, 8\n\t"
"ds_read_b64_tr_b16 a[206:207], v249 offset:23552\n\t"
"v_mfma_f32_4x4x4_16b_bf16 v[220:223], a[210:211], a[190:191], 0\n\t"
"ds_write_addtid_b32 v199 offset:9344\n\t"
"v_cvt_pk_bf16_f32 v193, v203, v207\n\t"
"v_mfma_f32_16x16x32_bf16 v[36:39], a[112:115], a[148:151], v[36:39]\n\t"
"s_nop 0\n\t"
"ds_write_addtid_b32 v196 offset:0\n\t"
"v_cvt_pk_bf16_f32 v198, v200, v204\n\t"
"v_mfma_f32_16x16x32_bf16 v[36:39], a[116:119], a[164:167], v[36:39]\n\t"
"s_nop 0\n\t"
"ds_write_addtid_b32 v198 offset:8320\n\t"
"ds_write_addtid_b32 v197 offset:1024\n\t"
"v_mfma_f32_16x16x32_bf16 v[20:23], a[104:107], a[148:151], v[20:23]\n\t"
"s_add_u32 m0, s26, 35584\n\t"
"v_cvt_pk_bf16_f32 v213, v209, v213\n\t"
"v_cvt_pk_bf16_f32 v211, v211, v215\n\t"
"v_mfma_f32_16x16x32_bf16 v[20:23], a[108:111], a[164:167], v[20:23]\n\t"
"s_nop 0\n\t"
"v_cvt_pk_bf16_f32 v212, v208, v212\n\t"
"v_cvt_pk_bf16_f32 v210, v210, v214\n\t"
"v_mfma_f32_16x16x32_bf16 v[4:7], a[96:99], a[148:151], v[4:7]\n\t"
"s_nop 0\n\t"
"ds_read_b128 a[148:151], v224 offset:17824\n\t"
"v_cvt_pk_bf16_f32 v209, v219, v223\n\t"
"v_mfma_f32_16x16x32_bf16 v[4:7], a[100:103], a[164:167], v[4:7]\n\t"
"s_waitcnt lgkmcnt(11)\n\t"
"s_mov_b64 exec, s[36:37]\n\t"
"buffer_load_dwordx4 v240, s[12:15], s22 offen offset:3584 nt lds\n\t"
"s_mov_b64 exec, -1\n\t"
"v_mfma_f32_4x4x4_16b_bf16 v[200:203], a[210:211], a[192:193], 0\n\t"
"ds_read_b128 a[160:163], v224 offset:19616\n\t"
"v_mfma_f32_4x4x4_16b_bf16 v[204:207], a[210:211], a[194:195], 0\n\t"
"v_cvt_pk_bf16_f32 v208, v218, v222\n\t"
"v_cvt_pk_bf16_f32 v215, v217, v221\n\t"
"v_mfma_f32_4x4x4_16b_bf16 v[196:199], a[210:211], a[196:197], 0\n\t"
"s_lshl_b32 m0, s73, 8\n\t"
"v_cvt_pk_bf16_f32 v214, v216, v220\n\t"
"v_mfma_f32_4x4x4_16b_bf16 v[216:219], a[210:211], a[198:199], 0\n\t"
"s_mov_b32 s80, 262144\n\t"
"ds_write_addtid_b32 v212 offset:4128\n\t"
"ds_write_addtid_b32 v213 offset:5152\n\t"
"v_mfma_f32_16x16x32_bf16 v[8:11], a[96:99], a[152:155], v[8:11]\n\t"
"s_nop 0\n\t"
"ds_write_addtid_b32 v214 offset:12448\n\t"
"ds_write_addtid_b32 v215 offset:13472\n\t"
"v_mfma_f32_16x16x32_bf16 v[8:11], a[100:103], a[168:171], v[8:11]\n\t"
"buffer_load_dwordx4 a[48:51], off, s[96:99], s64 offset:0 sc1\n\t"
"v_mfma_f32_16x16x32_bf16 v[24:27], a[104:107], a[152:155], v[24:27]\n\t"
"s_nop 0\n\t"
"v_cvt_pk_bf16_f32 v204, v200, v204\n\t"
"v_cvt_pk_bf16_f32 v203, v203, v207\n\t"
"v_mfma_f32_16x16x32_bf16 v[24:27], a[108:111], a[168:171], v[24:27]\n\t"
"buffer_load_dwordx4 a[52:55], off, s[96:99], s64 offset:1024 sc1\n\t"
"v_mfma_f32_16x16x32_bf16 v[40:43], a[112:115], a[152:155], v[40:43]\n\t"
"s_nop 0\n\t"
"ds_read_b128 a[164:167], v224 offset:19872\n\t"
"v_cvt_pk_bf16_f32 v202, v202, v206\n\t"
"v_mfma_f32_16x16x32_bf16 v[40:43], a[116:119], a[168:171], v[40:43]\n\t"
"s_waitcnt lgkmcnt(12)\n\t"
"v_cvt_pk_bf16_f32 v200, v198, v218\n\t"
"v_cvt_pk_bf16_f32 v206, v196, v216\n\t"
"v_mfma_f32_16x16x32_bf16 v[44:47], a[112:115], a[156:159], v[44:47]\n\t"
"s_nop 0\n\t"
"v_cvt_pk_bf16_f32 v205, v201, v205\n\t"
"v_cvt_pk_bf16_f32 v201, v199, v219\n\t"
"v_mfma_f32_16x16x32_bf16 v[44:47], a[116:119], a[172:175], v[44:47]\n\t"
"Lpad2_%=:\n\t"
"s_cbranch_execz Lpad6_%=\n\t"
"s_waitcnt lgkmcnt(11)\n\t"
"buffer_load_dwordx4 a[56:59], off, s[96:99], s64 offset:2048 sc1\n\t"
"v_mfma_f32_4x4x4_16b_bf16 v[220:223], a[210:211], a[200:201], 0\n\t"
"ds_write_addtid_b32 v205 offset:3072\n\t"
"v_cvt_pk_bf16_f32 v207, v197, v217\n\t"
"v_mfma_f32_4x4x4_16b_bf16 v[212:215], a[210:211], a[202:203], 0\n\t"
"buffer_load_dwordx4 a[60:63], off, s[96:99], s64 offset:3072 sc1\n\t"
"v_mfma_f32_4x4x4_16b_bf16 v[196:199], a[210:211], a[204:205], 0\n\t"
"ds_write_addtid_b32 v204 offset:2048\n\t"
"v_mfma_f32_4x4x4_16b_bf16 v[216:219], a[210:211], a[206:207], 0\n\t"
"buffer_load_dwordx4 a[64:67], off, s[92:95], s65 offset:0 sc1\n\t"
"v_mfma_f32_16x16x32_bf16 v[28:31], a[104:107], a[156:159], v[28:31]\n\t"
"s_mov_b32 s79, 253952\n\t"
"ds_write_addtid_b32 v206 offset:10368\n\t"
"ds_write_addtid_b32 v207 offset:11392\n\t"
"v_mfma_f32_16x16x32_bf16 v[28:31], a[108:111], a[172:175], v[28:31]\n\t"
"buffer_load_dwordx4 a[68:71], off, s[92:95], s65 offset:1024 sc1\n\t"
"v_mfma_f32_16x16x32_bf16 v[12:15], a[96:99], a[156:159], v[12:15]\n\t"
"v_cvt_pk_bf16_f32 v213, v221, v213\n\t"
"v_cvt_pk_bf16_f32 v223, v223, v215\n\t"
"v_mfma_f32_16x16x32_bf16 v[12:15], a[100:103], a[172:175], v[12:15]\n\t"
"s_waitcnt vmcnt(30) lgkmcnt(9)\n\t"
"v_cvt_pk_bf16_f32 v212, v220, v212\n\t"
"v_cvt_pk_bf16_f32 v222, v222, v214\n\t"
"v_mfma_f32_16x16x32_bf16 v[96:99], a[120:123], a[144:147], v[96:99]\n\t"
"ds_write_addtid_b32 v212 offset:6176\n\t"
"v_cvt_pk_bf16_f32 v214, v196, v216\n\t"
"v_mfma_f32_16x16x32_bf16 v[96:99], a[124:127], a[160:163], v[96:99]\n\t"
"s_waitcnt vmcnt(28)\n\t"
"ds_write_addtid_b32 v213 offset:7200\n\t"
"buffer_load_dwordx4 a[72:75], off, s[96:99], s63 offset:2048 sc1\n\t"
"v_cvt_pk_bf16_f32 v215, v197, v217\n\t"
"v_mfma_f32_16x16x32_bf16 v[112:115], a[128:131], a[144:147], v[112:115]\n\t"
"s_mov_b32 s82, 278528\n\t"
"ds_write_addtid_b32 v214 offset:14496\n\t"
"ds_write_addtid_b32 v215 offset:15520\n\t"
"v_mfma_f32_16x16x32_bf16 v[112:115], a[132:135], a[160:163], v[112:115]\n\t"
"s_waitcnt vmcnt(27)\n\t"
"buffer_load_dwordx4 a[76:79], off, s[96:99], s63 offset:3072 sc1\n\t"
"buffer_load_dwordx4 a[80:83], off, s[92:95], s64 offset:0 sc1\n\t"
"v_mfma_f32_16x16x32_bf16 v[128:131], a[136:139], a[144:147], v[128:131]\n\t"
"s_mov_b32 s83, 286720\n\t"
"ds_read_b128 a[152:155], v224 offset:18080\n\t"
"ds_read_b128 a[172:175], v224 offset:20384\n\t"
"v_mfma_f32_16x16x32_bf16 v[128:131], a[140:143], a[160:163], v[128:131]\n\t"
"s_waitcnt lgkmcnt(10)\n\t"
"buffer_load_dwordx4 a[84:87], off, s[92:95], s64 offset:1024 sc1\n\t"
"v_mfma_f32_16x16x32_bf16 v[132:135], a[136:139], a[148:151], v[132:135]\n\t"
"s_mov_b32 s77, 237568\n\t"
"ds_read_b128 a[168:171], v224 offset:20128\n\t"
"ds_read_b128 a[156:159], v224 offset:18336\n\t"
"v_mfma_f32_16x16x32_bf16 v[132:135], a[140:143], a[164:167], v[132:135]\n\t"
"buffer_load_dwordx4 a[88:91], off, s[92:95], s64 offset:2048 sc1\n\t"
"buffer_load_dwordx4 a[92:95], off, s[92:95], s64 offset:3072 sc1\n\t"
"v_mfma_f32_16x16x32_bf16 v[116:119], a[128:131], a[148:151], v[116:119]\n\t"
"v_accvgpr_read_b32 v224, a213\n\t"
"v_cvt_pk_bf16_f32 v221, v199, v219\n\t"
"v_mfma_f32_16x16x32_bf16 v[116:119], a[132:135], a[164:167], v[116:119]\n\t"
"buffer_load_dwordx4 a[108:111], off, s[96:99], s46 offset:3072 sc1\n\t"
"v_mfma_f32_16x16x32_bf16 v[100:103], a[120:123], a[148:151], v[100:103]\n\t"
"v_cvt_pk_bf16_f32 v220, v198, v218\n\t"
"v_mfma_f32_16x16x32_bf16 v[100:103], a[124:127], a[164:167], v[100:103]\n\t"
"buffer_load_dwordx4 a[112:115], off, s[92:95], s47 offset:0 sc1\n\t"
"buffer_load_dwordx4 a[116:119], off, s[92:95], s47 offset:1024 sc1\n\t"
"v_add_u32 v246, 8, v228\n\t"
"v_add_u32 v247, 16, v228\n\t"
"s_waitcnt lgkmcnt(0)\n\t"
"s_barrier\n\t"
"Lunit_%=:\n\t"
"v_mfma_f32_16x16x32_bf16 v[104:107], a[120:123], a[152:155], v[104:107]\n\t"
"s_nop 0\n\t"
"ds_read_b128 a[144:147], v224 offset:1024\n\t"
"ds_read_b128 a[148:151], v224 offset:1280\n\t"
"v_mfma_f32_16x16x32_bf16 v[104:107], a[124:127], a[168:171], v[104:107]\n\t"
"buffer_load_dwordx4 a[104:107], off, s[96:99], s46 offset:2048 sc1\n\t"
"v_mfma_f32_16x16x32_bf16 v[120:123], a[128:131], a[152:155], v[120:123]\n\t"
"ds_read_b128 a[160:163], v224 offset:3072\n\t"
"v_mfma_f32_16x16x32_bf16 v[120:123], a[132:135], a[168:171], v[120:123]\n\t"
"ds_read_b128 a[164:167], v224 offset:3328\n\t"
"v_mfma_f32_16x16x32_bf16 v[136:139], a[136:139], a[152:155], v[136:139]\n\t"
"ds_write_addtid_b32 v208 offset:28992\n\t"
"v_mfma_f32_16x16x32_bf16 v[136:139], a[140:143], a[168:171], v[136:139]\n\t"
"buffer_load_dwordx4 a[96:99], off, s[96:99], s46 offset:0 sc1\n\t"
"v_mfma_f32_16x16x32_bf16 v[140:143], a[136:139], a[156:159], v[140:143]\n\t"
"ds_read_b128 a[168:171], v224 offset:3584\n\t"
"v_mfma_f32_16x16x32_bf16 v[140:143], a[140:143], a[172:175], v[140:143]\n\t"
"ds_read_b128 a[152:155], v224 offset:1536\n\t"
"v_mfma_f32_16x16x32_bf16 v[124:127], a[128:131], a[156:159], v[124:127]\n\t"
"ds_write_addtid_b32 v203 offset:19616\n\t"
"v_mfma_f32_16x16x32_bf16 v[124:127], a[132:135], a[172:175], v[124:127]\n\t"
"buffer_load_dwordx4 a[100:103], off, s[96:99], s46 offset:1024 sc1\n\t"
"v_mfma_f32_16x16x32_bf16 v[108:111], a[120:123], a[156:159], v[108:111]\n\t"
"ds_read_b128 a[156:159], v224 offset:1792\n\t"
"v_mfma_f32_16x16x32_bf16 v[108:111], a[124:127], a[172:175], v[108:111]\n\t"
"s_waitcnt vmcnt(34) lgkmcnt(6)\n\t"
"ds_write_addtid_b32 v194 offset:16544\n\t"
"v_mfma_f32_16x16x32_bf16 v[48:51], a[0:3], a[144:147], v[0:3]\n\t"
"ds_read_b128 a[172:175], v224 offset:3840\n\t"
"v_mfma_f32_16x16x32_bf16 v[48:51], a[4:7], a[160:163], v[48:51]\n\t"
"s_waitcnt vmcnt(32) lgkmcnt(7)\n\t"
"buffer_load_dwordx4 a[136:139], off, s[92:95], s46 offset:2048 sc1\n\t"
"v_mfma_f32_16x16x32_bf16 v[64:67], a[8:11], a[144:147], v[16:19]\n\t"
"ds_write_addtid_b32 v195 offset:17568\n\t"
"v_mfma_f32_16x16x32_bf16 v[64:67], a[12:15], a[160:163], v[64:67]\n\t"
"s_waitcnt vmcnt(31) lgkmcnt(7)\n\t"
"ds_write_addtid_b32 v202 offset:18592\n\t"
"v_mfma_f32_16x16x32_bf16 v[80:83], a[16:19], a[144:147], v[32:35]\n\t"
"ds_read_b128 a[144:147], v224 offset:0\n\t"
"v_mfma_f32_16x16x32_bf16 v[80:83], a[20:23], a[160:163], v[80:83]\n\t"
"buffer_load_dwordx4 a[140:143], off, s[92:95], s46 offset:3072 sc1\n\t"
"v_mfma_f32_16x16x32_bf16 v[84:87], a[16:19], a[148:151], v[36:39]\n\t"
"ds_read_b128 a[160:163], v224 offset:2048\n\t"
"v_mfma_f32_16x16x32_bf16 v[84:87], a[20:23], a[164:167], v[84:87]\n\t"
"ds_write_addtid_b32 v223 offset:23744\n\t"
"v_mfma_f32_16x16x32_bf16 v[68:71], a[8:11], a[148:151], v[20:23]\n\t"
"ds_write_addtid_b32 v201 offset:27936\n\t"
"v_mfma_f32_16x16x32_bf16 v[68:71], a[12:15], a[164:167], v[68:71]\n\t"
"buffer_load_dwordx4 a[132:135], off, s[92:95], s46 offset:1024 sc1\n\t"
"v_mfma_f32_16x16x32_bf16 v[52:55], a[0:3], a[148:151], v[4:7]\n\t"
"ds_read_b128 a[148:151], v224 offset:256\n\t"
"v_mfma_f32_16x16x32_bf16 v[52:55], a[4:7], a[164:167], v[52:55]\n\t"
"s_waitcnt lgkmcnt(7)\n\t"
"ds_write_addtid_b32 v221 offset:32064\n\t"
"v_mfma_f32_16x16x32_bf16 v[56:59], a[0:3], a[152:155], v[8:11]\n\t"
"ds_read_b128 a[164:167], v224 offset:2304\n\t"
"v_mfma_f32_16x16x32_bf16 v[56:59], a[4:7], a[168:171], v[56:59]\n\t"
"buffer_load_dwordx4 a[128:131], off, s[92:95], s46 offset:0 sc1\n\t"
"v_mfma_f32_16x16x32_bf16 v[72:75], a[8:11], a[152:155], v[24:27]\n\t"
"ds_write_addtid_b32 v211 offset:21696\n\t"
"v_mfma_f32_16x16x32_bf16 v[72:75], a[12:15], a[168:171], v[72:75]\n\t"
"ds_write_addtid_b32 v193 offset:25888\n\t"
"v_mfma_f32_16x16x32_bf16 v[88:91], a[16:19], a[152:155], v[40:43]\n\t"
"v_mfma_f32_16x16x32_bf16 v[88:91], a[20:23], a[168:171], v[88:91]\n\t"
"ds_write_b128 v254, a[248:251] offset:41472\n\t"
"v_mfma_f32_16x16x32_bf16 v[92:95], a[16:19], a[156:159], v[44:47]\n\t"
"v_mfma_f32_16x16x32_bf16 v[92:95], a[20:23], a[172:175], v[92:95]\n\t"
"buffer_load_dwordx4 a[120:123], off, s[96:99], s42 offset:2048 sc1\n\t"
"v_mfma_f32_16x16x32_bf16 v[76:79], a[8:11], a[156:159], v[28:31]\n\t"
"v_mfma_f32_16x16x32_bf16 v[76:79], a[12:15], a[172:175], v[76:79]\n\t"
"s_nop 0\n\t"
"ds_read_b128 a[168:171], v224 offset:2560\n\t"
"ds_write_addtid_b32 v209 offset:30016\n\t"
"v_mfma_f32_16x16x32_bf16 v[60:63], a[0:3], a[156:159], v[12:15]\n\t"
"ds_write_addtid_b32 v210 offset:20672\n\t"
"v_mfma_f32_16x16x32_bf16 v[60:63], a[4:7], a[172:175], v[60:63]\n\t"
"s_waitcnt vmcnt(33) lgkmcnt(6)\n\t"
"buffer_load_dwordx4 a[124:127], off, s[96:99], s42 offset:3072 sc1\n\t"
"v_mfma_f32_16x16x32_bf16 v[0:3], a[24:27], a[144:147], v[0:3]\n\t"
"ds_write_addtid_b32 v222 offset:22720\n\t"
"v_mfma_f32_16x16x32_bf16 v[0:3], a[28:31], a[160:163], v[0:3]\n\t"
"s_waitcnt vmcnt(32) lgkmcnt(5)\n\t"
"ds_write_addtid_b32 v192 offset:24864\n\t"
"ds_write_addtid_b32 v200 offset:26912\n\t"
"v_mfma_f32_16x16x32_bf16 v[16:19], a[32:35], a[144:147], v[16:19]\n\t"
"ds_write_addtid_b32 v220 offset:31040\n\t"
"v_mfma_f32_16x16x32_bf16 v[16:19], a[36:39], a[160:163], v[16:19]\n\t"
"s_waitcnt vmcnt(30) lgkmcnt(7)\n\t"
"buffer_load_dwordx4 a[16:19], off, s[92:95], s50 offset:0 sc1\n\t"
"v_mfma_f32_16x16x32_bf16 v[32:35], a[40:43], a[144:147], v[32:35]\n\t"
"ds_read_b128 a[156:159], v224 offset:768\n\t"
"v_mfma_f32_16x16x32_bf16 v[32:35], a[44:47], a[160:163], v[32:35]\n\t"
"ds_read_b128 a[172:175], v224 offset:2816\n\t"
"v_mfma_f32_16x16x32_bf16 v[36:39], a[40:43], a[148:151], v[36:39]\n\t"
"ds_read_b128 a[152:155], v224 offset:512\n\t"
"v_mfma_f32_16x16x32_bf16 v[36:39], a[44:47], a[164:167], v[36:39]\n\t"
"buffer_load_dwordx4 a[20:23], off, s[92:95], s50 offset:1024 sc1\n\t"
"v_mfma_f32_16x16x32_bf16 v[20:23], a[32:35], a[148:151], v[20:23]\n\t"
"v_mfma_f32_16x16x32_bf16 v[20:23], a[36:39], a[164:167], v[20:23]\n\t"
"v_mfma_f32_16x16x32_bf16 v[4:7], a[24:27], a[148:151], v[4:7]\n\t"
"buffer_load_dwordx4 a[12:15], off, s[96:99], s49 offset:3072 sc1\n\t"
"v_mfma_f32_16x16x32_bf16 v[4:7], a[28:31], a[164:167], v[4:7]\n\t"
"s_waitcnt vmcnt(27) lgkmcnt(4)\n\t"
"s_waitcnt lgkmcnt(0)\n\t"
"s_barrier\n\t"
"v_mfma_f32_16x16x32_bf16 v[8:11], a[24:27], a[152:155], v[8:11]\n\t"
"s_add_u32 m0, s26, 76800\n\t"
"ds_read_b128 a[144:147], v224 offset:17568\n\t"
"ds_read_b128 a[160:163], v224 offset:19616\n\t"
"v_mfma_f32_16x16x32_bf16 v[8:11], a[28:31], a[168:171], v[8:11]\n\t"
"ds_read_b128 a[164:167], v224 offset:19872\n\t"
"v_mfma_f32_16x16x32_bf16 v[24:27], a[32:35], a[152:155], v[24:27]\n\t"
"ds_read_b128 a[148:151], v224 offset:17824\n\t"
"v_mfma_f32_16x16x32_bf16 v[24:27], a[36:39], a[168:171], v[24:27]\n\t"
"buffer_load_dwordx4 v236, s[12:15], s22 offen offset:256 nt lds\n\t"
"v_mfma_f32_16x16x32_bf16 v[40:43], a[40:43], a[152:155], v[40:43]\n\t"
"s_add_u32 s24, s24, 3145728\n\t"
"ds_read_b64_tr_b16 a[176:177], v248 offset:0\n\t"
"ds_read_b64_tr_b16 a[178:179], v249 offset:0\n\t"
"v_mfma_f32_16x16x32_bf16 v[40:43], a[44:47], a[168:171], v[40:43]\n\t"
"s_add_u32 s20, s20, 3145728\n\t"
"ds_read_b64_tr_b16 a[180:181], v248 offset:512\n\t"
"ds_read_b64_tr_b16 a[182:183], v249 offset:512\n\t"
"v_mfma_f32_16x16x32_bf16 v[44:47], a[40:43], a[156:159], v[44:47]\n\t"
"ds_read_b64_tr_b16 a[184:185], v250 offset:2304\n\t"
"v_mfma_f32_16x16x32_bf16 v[44:47], a[44:47], a[172:175], v[44:47]\n\t"
"buffer_load_dwordx4 v237, s[12:15], s22 offen offset:1280 nt lds\n\t"
"v_mfma_f32_16x16x32_bf16 v[28:31], a[32:35], a[156:159], v[28:31]\n\t"
"ds_read_b128 a[152:155], v224 offset:18080\n\t"
"v_mfma_f32_16x16x32_bf16 v[28:31], a[36:39], a[172:175], v[28:31]\n\t"
"s_waitcnt lgkmcnt(2)\n\t"
"ds_read_b64_tr_b16 a[186:187], v251 offset:2304\n\t"
"v_mfma_f32_4x4x4_16b_bf16 v[192:195], a[210:211], a[176:177], 0\n\t"
"ds_read_b128 a[168:171], v224 offset:20128\n\t"
"v_mfma_f32_4x4x4_16b_bf16 v[196:199], a[210:211], a[178:179], 0\n\t"
"ds_read_b64_tr_b16 a[188:189], v250 offset:2816\n\t"
"v_mfma_f32_4x4x4_16b_bf16 v[200:203], a[210:211], a[180:181], 0\n\t"
"ds_read_b64_tr_b16 a[190:191], v251 offset:2816\n\t"
"v_mfma_f32_4x4x4_16b_bf16 v[204:207], a[210:211], a[182:183], 0\n\t"
"buffer_load_dwordx4 v238, s[12:15], s22 offen offset:2304 nt lds\n\t"
"v_mfma_f32_16x16x32_bf16 v[12:15], a[24:27], a[156:159], v[12:15]\n\t"
"s_add_u32 s27, s27, 3145728\n\t"
"ds_read_b64_tr_b16 a[192:193], v250 offset:0\n\t"
"ds_read_b64_tr_b16 a[194:195], v251 offset:0\n\t"
"v_mfma_f32_16x16x32_bf16 v[12:15], a[28:31], a[172:175], v[12:15]\n\t"
"s_waitcnt vmcnt(28)\n\t"
"buffer_load_dwordx4 a[8:11], off, s[96:99], s49 offset:2048 sc1\n\t"
"v_mfma_f32_16x16x32_bf16 v[144:147], a[48:51], a[144:147], v[96:99]\n\t"
"ds_read_b128 a[156:159], v224 offset:18336\n\t"
"v_cvt_pk_bf16_f32 v194, v194, v198\n\t"
"v_mfma_f32_16x16x32_bf16 v[144:147], a[52:55], a[160:163], v[144:147]\n\t"
"s_waitcnt vmcnt(27) lgkmcnt(8)\n\t"
"v_cvt_pk_bf16_f32 v197, v193, v197\n\t"
"v_cvt_pk_bf16_f32 v195, v195, v199\n\t"
"v_mfma_f32_16x16x32_bf16 v[160:163], a[56:59], a[144:147], v[112:115]\n\t"
"ds_read_b128 a[172:175], v224 offset:20384\n\t"
"v_cvt_pk_bf16_f32 v196, v192, v196\n\t"
"v_mfma_f32_16x16x32_bf16 v[160:163], a[60:63], a[160:163], v[160:163]\n\t"
"s_waitcnt vmcnt(25) lgkmcnt(7)\n\t"
"buffer_load_dwordx4 v239, s[12:15], s22 offen offset:3328 nt lds\n\t"
"v_mfma_f32_16x16x32_bf16 v[176:179], a[64:67], a[144:147], v[128:131]\n\t"
"ds_read_b128 a[144:147], v224 offset:16544\n\t"
"v_cvt_pk_bf16_f32 v199, v201, v205\n\t"
"v_mfma_f32_16x16x32_bf16 v[176:179], a[68:71], a[160:163], v[176:179]\n\t"
"s_waitcnt lgkmcnt(5)\n\t"
"v_cvt_pk_bf16_f32 v192, v202, v206\n\t"
"v_cvt_pk_bf16_f32 v198, v200, v204\n\t"
"v_mfma_f32_4x4x4_16b_bf16 v[208:211], a[210:211], a[184:185], 0\n\t"
"ds_read_b64_tr_b16 a[196:197], v250 offset:512\n\t"
"v_mfma_f32_4x4x4_16b_bf16 v[212:215], a[210:211], a[186:187], 0\n\t"
"ds_read_b64_tr_b16 a[198:199], v251 offset:512\n\t"
"v_mfma_f32_4x4x4_16b_bf16 v[216:219], a[210:211], a[188:189], 0\n\t"
"ds_read_b64_tr_b16 a[200:201], v248 offset:2304\n\t"
"v_mfma_f32_4x4x4_16b_bf16 v[220:223], a[210:211], a[190:191], 0\n\t"
"s_mov_b64 exec, s[36:37]\n\t"
"buffer_load_dwordx4 v240, s[12:15], s22 offen offset:3840 nt lds\n\t"
"s_mov_b64 exec, -1\n\t"
"v_mfma_f32_16x16x32_bf16 v[180:183], a[64:67], a[148:151], v[132:135]\n\t"
"s_lshl_b32 m0, s73, 8\n\t"
"ds_read_b128 a[160:163], v224 offset:18592\n\t"
"v_cvt_pk_bf16_f32 v193, v203, v207\n\t"
"v_mfma_f32_16x16x32_bf16 v[180:183], a[68:71], a[164:167], v[180:183]\n\t"
"s_nop 0\n\t"
"ds_write_addtid_b32 v198 offset:8320\n\t"
"ds_write_addtid_b32 v197 offset:1024\n\t"
"v_mfma_f32_16x16x32_bf16 v[164:167], a[56:59], a[148:151], v[116:119]\n\t"
"v_cvt_pk_bf16_f32 v213, v209, v213\n\t"
"v_cvt_pk_bf16_f32 v211, v211, v215\n\t"
"v_mfma_f32_16x16x32_bf16 v[164:167], a[60:63], a[164:167], v[164:167]\n\t"
"v_cvt_pk_bf16_f32 v212, v208, v212\n\t"
"v_cvt_pk_bf16_f32 v210, v210, v214\n\t"
"v_mfma_f32_16x16x32_bf16 v[148:151], a[48:51], a[148:151], v[100:103]\n\t"
"ds_write_addtid_b32 v199 offset:9344\n\t"
"v_cvt_pk_bf16_f32 v209, v219, v223\n\t"
"v_mfma_f32_16x16x32_bf16 v[148:151], a[52:55], a[164:167], v[148:151]\n\t"
"s_waitcnt lgkmcnt(5)\n\t"
"ds_read_b128 a[148:151], v224 offset:16800\n\t"
"v_cvt_pk_bf16_f32 v214, v216, v220\n\t"
"v_mfma_f32_4x4x4_16b_bf16 v[200:203], a[210:211], a[192:193], 0\n\t"
"v_cvt_pk_bf16_f32 v208, v218, v222\n\t"
"v_mfma_f32_4x4x4_16b_bf16 v[204:207], a[210:211], a[194:195], 0\n\t"
"ds_write_addtid_b32 v196 offset:0\n\t"
"v_mfma_f32_4x4x4_16b_bf16 v[196:199], a[210:211], a[196:197], 0\n\t"
"v_cvt_pk_bf16_f32 v215, v217, v221\n\t"
"v_mfma_f32_4x4x4_16b_bf16 v[216:219], a[210:211], a[198:199], 0\n\t"
"ds_read_b128 a[164:167], v224 offset:18848\n\t"
"v_mfma_f32_16x16x32_bf16 v[152:155], a[48:51], a[152:155], v[104:107]\n\t"
"ds_read_b64_tr_b16 a[202:203], v249 offset:2304\n\t"
"v_mfma_f32_16x16x32_bf16 v[152:155], a[52:55], a[168:171], v[152:155]\n\t"
"ds_read_b64_tr_b16 a[204:205], v248 offset:2816\n\t"
"v_mfma_f32_16x16x32_bf16 v[168:171], a[56:59], a[152:155], v[120:123]\n\t"
"v_cvt_pk_bf16_f32 v202, v202, v206\n\t"
"v_cvt_pk_bf16_f32 v203, v203, v207\n\t"
"v_mfma_f32_16x16x32_bf16 v[168:171], a[60:63], a[168:171], v[168:171]\n\t"
"ds_read_b64_tr_b16 a[206:207], v249 offset:2816\n\t"
"v_cvt_pk_bf16_f32 v205, v201, v205\n\t"
"v_mfma_f32_16x16x32_bf16 v[184:187], a[64:67], a[152:155], v[136:139]\n\t"
"ds_read_b128 a[152:155], v224 offset:17056\n\t"
"v_cvt_pk_bf16_f32 v204, v200, v204\n\t"
"v_mfma_f32_16x16x32_bf16 v[184:187], a[68:71], a[168:171], v[184:187]\n\t"
"ds_write_addtid_b32 v213 offset:5152\n\t"
"v_cvt_pk_bf16_f32 v207, v197, v217\n\t"
"v_mfma_f32_16x16x32_bf16 v[188:191], a[64:67], a[156:159], v[140:143]\n\t"
"ds_write_addtid_b32 v215 offset:13472\n\t"
"v_cvt_pk_bf16_f32 v201, v199, v219\n\t"
"v_mfma_f32_16x16x32_bf16 v[188:191], a[68:71], a[172:175], v[188:191]\n\t"
"s_waitcnt lgkmcnt(3)\n\t"
"ds_write_addtid_b32 v212 offset:4128\n\t"
"v_cvt_pk_bf16_f32 v200, v198, v218\n\t"
"v_mfma_f32_4x4x4_16b_bf16 v[220:223], a[210:211], a[200:201], 0\n\t"
"ds_write_addtid_b32 v214 offset:12448\n\t"
"v_mfma_f32_4x4x4_16b_bf16 v[212:215], a[210:211], a[202:203], 0\n\t"
"v_cvt_pk_bf16_f32 v206, v196, v216\n\t"
"v_mfma_f32_4x4x4_16b_bf16 v[196:199], a[210:211], a[204:205], 0\n\t"
"ds_read_b128 a[168:171], v224 offset:19104\n\t"
"v_mfma_f32_4x4x4_16b_bf16 v[216:219], a[210:211], a[206:207], 0\n\t"
"ds_write_addtid_b32 v206 offset:10368\n\t"
"v_mfma_f32_16x16x32_bf16 v[172:175], a[56:59], a[156:159], v[124:127]\n\t"
"s_nop 0\n\t"
"ds_write_addtid_b32 v205 offset:3072\n\t"
"ds_write_addtid_b32 v207 offset:11392\n\t"
"v_mfma_f32_16x16x32_bf16 v[172:175], a[60:63], a[172:175], v[172:175]\n\t"
"ds_write_addtid_b32 v204 offset:2048\n\t"
"v_mfma_f32_16x16x32_bf16 v[156:159], a[48:51], a[156:159], v[108:111]\n\t"
"v_cvt_pk_bf16_f32 v212, v220, v212\n\t"
"v_cvt_pk_bf16_f32 v222, v222, v214\n\t"
"v_mfma_f32_16x16x32_bf16 v[156:159], a[52:55], a[172:175], v[156:159]\n\t"
"s_waitcnt vmcnt(22) lgkmcnt(8)\n\t"
"v_cvt_pk_bf16_f32 v213, v221, v213\n\t"
"v_cvt_pk_bf16_f32 v223, v223, v215\n\t"
"v_mfma_f32_16x16x32_bf16 v[96:99], a[72:75], a[144:147], v[96:99]\n\t"
"ds_write_addtid_b32 v212 offset:6176\n\t"
"v_cvt_pk_bf16_f32 v214, v196, v216\n\t"
"v_mfma_f32_16x16x32_bf16 v[96:99], a[76:79], a[160:163], v[96:99]\n\t"
"ds_write_addtid_b32 v213 offset:7200\n\t"
"v_cvt_pk_bf16_f32 v215, v197, v217\n\t"
"v_mfma_f32_16x16x32_bf16 v[112:115], a[80:83], a[144:147], v[112:115]\n\t"
"s_add_u32 s25, s25, 3145728\n\t"
"ds_write_addtid_b32 v214 offset:14496\n\t"
"ds_write_addtid_b32 v215 offset:15520\n\t"
"v_mfma_f32_16x16x32_bf16 v[112:115], a[84:87], a[160:163], v[112:115]\n\t"
"s_waitcnt vmcnt(21) lgkmcnt(9)\n\t"
"v_mfma_f32_16x16x32_bf16 v[128:131], a[88:91], a[144:147], v[128:131]\n\t"
"v_mfma_f32_16x16x32_bf16 v[128:131], a[92:95], a[160:163], v[128:131]\n\t"
"ds_read_b128 a[156:159], v224 offset:17312\n\t"
"v_mfma_f32_16x16x32_bf16 v[132:135], a[88:91], a[148:151], v[132:135]\n\t"
"ds_read_b128 a[172:175], v224 offset:19360\n\t"
"v_cvt_pk_bf16_f32 v221, v199, v219\n\t"
"v_mfma_f32_16x16x32_bf16 v[132:135], a[92:95], a[164:167], v[132:135]\n\t"
"v_mfma_f32_16x16x32_bf16 v[116:119], a[80:83], a[148:151], v[116:119]\n\t"
"v_cvt_pk_bf16_f32 v220, v198, v218\n\t"
"v_mfma_f32_16x16x32_bf16 v[116:119], a[84:87], a[164:167], v[116:119]\n\t"
"buffer_load_dwordx4 a[0:3], off, s[96:99], s49 offset:0 sc1\n\t"
"v_mfma_f32_16x16x32_bf16 v[100:103], a[72:75], a[148:151], v[100:103]\n\t"
"s_waitcnt lgkmcnt(0)\n\t"
"v_mfma_f32_16x16x32_bf16 v[100:103], a[76:79], a[164:167], v[100:103]\n\t"
"s_barrier\n\t"
"v_mfma_f32_16x16x32_bf16 v[104:107], a[72:75], a[152:155], v[104:107]\n\t"
"s_nop 0\n\t"
"ds_read_b128 a[144:147], v224 offset:1024\n\t"
"ds_read_b128 a[160:163], v224 offset:3072\n\t"
"v_mfma_f32_16x16x32_bf16 v[104:107], a[76:79], a[168:171], v[104:107]\n\t"
"buffer_load_dwordx4 a[4:7], off, s[96:99], s49 offset:1024 sc1\n\t"
"v_mfma_f32_16x16x32_bf16 v[120:123], a[80:83], a[152:155], v[120:123]\n\t"
"ds_read_b128 a[148:151], v224 offset:1280\n\t"
"v_mfma_f32_16x16x32_bf16 v[120:123], a[84:87], a[168:171], v[120:123]\n\t"
"ds_read_b128 a[164:167], v224 offset:3328\n\t"
"v_mfma_f32_16x16x32_bf16 v[136:139], a[88:91], a[152:155], v[136:139]\n\t"
"ds_read_b128 a[248:251], v255 offset:59904\n\t"
"v_mfma_f32_16x16x32_bf16 v[136:139], a[92:95], a[168:171], v[136:139]\n\t"
"buffer_load_dwordx4 a[40:43], off, s[92:95], s49 offset:2048 sc1\n\t"
"v_mfma_f32_16x16x32_bf16 v[140:143], a[88:91], a[156:159], v[140:143]\n\t"
"v_mfma_f32_16x16x32_bf16 v[140:143], a[92:95], a[172:175], v[140:143]\n\t"
"ds_read_b128 a[152:155], v224 offset:1536\n\t"
"v_mfma_f32_16x16x32_bf16 v[124:127], a[80:83], a[156:159], v[124:127]\n\t"
"ds_read_b128 a[168:171], v224 offset:3584\n\t"
"v_mfma_f32_16x16x32_bf16 v[124:127], a[84:87], a[172:175], v[124:127]\n\t"
"buffer_load_dwordx4 a[44:47], off, s[92:95], s49 offset:3072 sc1\n\t"
"v_mfma_f32_16x16x32_bf16 v[108:111], a[72:75], a[156:159], v[108:111]\n\t"
"ds_write_addtid_b32 v223 offset:23744\n\t"
"v_mfma_f32_16x16x32_bf16 v[108:111], a[76:79], a[172:175], v[108:111]\n\t"
"s_waitcnt vmcnt(19) lgkmcnt(6)\n\t"
"ds_read_b128 a[156:159], v224 offset:1792\n\t"
"v_mfma_f32_16x16x32_bf16 v[48:51], a[96:99], a[144:147], v[48:51]\n\t"
"ds_read_b128 a[172:175], v224 offset:3840\n\t"
"v_mfma_f32_16x16x32_bf16 v[48:51], a[100:103], a[160:163], v[48:51]\n\t"
"buffer_load_dwordx4 a[36:39], off, s[92:95], s49 offset:1024 sc1\n\t"
"v_mfma_f32_16x16x32_bf16 v[64:67], a[104:107], a[144:147], v[64:67]\n\t"
"ds_write_addtid_b32 v192 offset:24864\n\t"
"v_mfma_f32_16x16x32_bf16 v[64:67], a[108:111], a[160:163], v[64:67]\n\t"
"ds_write_addtid_b32 v200 offset:26912\n\t"
"v_mfma_f32_16x16x32_bf16 v[80:83], a[112:115], a[144:147], v[80:83]\n\t"
"ds_read_b128 a[144:147], v224 offset:0\n\t"
"v_mfma_f32_16x16x32_bf16 v[80:83], a[116:119], a[160:163], v[80:83]\n\t"
"s_waitcnt lgkmcnt(7)\n\t"
"buffer_load_dwordx4 a[32:35], off, s[92:95], s49 offset:0 sc1\n\t"
"v_mfma_f32_16x16x32_bf16 v[84:87], a[112:115], a[148:151], v[84:87]\n\t"
"ds_read_b128 a[160:163], v224 offset:2048\n\t"
"v_mfma_f32_16x16x32_bf16 v[84:87], a[116:119], a[164:167], v[84:87]\n\t"
"ds_write_addtid_b32 v221 offset:32064\n\t"
"v_mfma_f32_16x16x32_bf16 v[68:71], a[104:107], a[148:151], v[68:71]\n\t"
"ds_write_addtid_b32 v209 offset:30016\n\t"
"v_xor_b32 v226, 64, v226\n\t"
"v_mfma_f32_16x16x32_bf16 v[68:71], a[108:111], a[164:167], v[68:71]\n\t"
"buffer_load_dwordx4 a[24:27], off, s[96:99], s48 offset:2048 sc1\n\t"
"v_mfma_f32_16x16x32_bf16 v[52:55], a[96:99], a[148:151], v[52:55]\n\t"
"ds_read_b128 a[148:151], v224 offset:256\n\t"
"v_mfma_f32_16x16x32_bf16 v[52:55], a[100:103], a[164:167], v[52:55]\n\t"
"s_waitcnt lgkmcnt(7)\n\t"
"ds_write_addtid_b32 v193 offset:25888\n\t"
"v_mfma_f32_16x16x32_bf16 v[56:59], a[96:99], a[152:155], v[56:59]\n\t"
"ds_read_b128 a[164:167], v224 offset:2304\n\t"
"v_mfma_f32_16x16x32_bf16 v[56:59], a[100:103], a[168:171], v[56:59]\n\t"
"buffer_load_dwordx4 a[28:31], off, s[96:99], s48 offset:3072 sc1\n\t"
"v_mfma_f32_16x16x32_bf16 v[72:75], a[104:107], a[152:155], v[72:75]\n\t"
"ds_write_addtid_b32 v210 offset:20672\n\t"
"v_xor_b32 v227, 64, v227\n\t"
"v_mfma_f32_16x16x32_bf16 v[72:75], a[108:111], a[168:171], v[72:75]\n\t"
"ds_write_addtid_b32 v195 offset:17568\n\t"
"v_mfma_f32_16x16x32_bf16 v[88:91], a[112:115], a[152:155], v[88:91]\n\t"
"ds_read_b128 a[152:155], v224 offset:512\n\t"
"v_mfma_f32_16x16x32_bf16 v[88:91], a[116:119], a[168:171], v[88:91]\n\t"
"buffer_load_dwordx4 a[64:67], off, s[92:95], s77 offset:0 sc1\n\t"
"v_mfma_f32_16x16x32_bf16 v[92:95], a[112:115], a[156:159], v[92:95]\n\t"
"ds_read_b128 a[168:171], v224 offset:2560\n\t"
"v_mfma_f32_16x16x32_bf16 v[92:95], a[116:119], a[172:175], v[92:95]\n\t"
"s_nop 0\n\t"
"ds_write_addtid_b32 v202 offset:18592\n\t"
"v_mfma_f32_16x16x32_bf16 v[76:79], a[104:107], a[156:159], v[76:79]\n\t"
"s_nop 0\n\t"
"ds_write_addtid_b32 v208 offset:28992\n\t"
"v_mfma_f32_16x16x32_bf16 v[76:79], a[108:111], a[172:175], v[76:79]\n\t"
"buffer_load_dwordx4 a[68:71], off, s[92:95], s77 offset:1024 sc1\n\t"
"v_mfma_f32_16x16x32_bf16 v[60:63], a[96:99], a[156:159], v[60:63]\n\t"
"s_nop 0\n\t"
"ds_write_addtid_b32 v211 offset:21696\n\t"
"v_mfma_f32_16x16x32_bf16 v[60:63], a[100:103], a[172:175], v[60:63]\n\t"
"s_waitcnt vmcnt(19) lgkmcnt(7)\n\t"
"ds_write_addtid_b32 v222 offset:22720\n\t"
"v_mfma_f32_16x16x32_bf16 v[0:3], a[120:123], a[144:147], v[0:3]\n\t"
"s_nop 0\n\t"
"ds_write_addtid_b32 v194 offset:16544\n\t"
"v_mfma_f32_16x16x32_bf16 v[0:3], a[124:127], a[160:163], v[0:3]\n\t"
"s_nop 0\n\t"
"ds_write_addtid_b32 v220 offset:31040\n\t"
"v_mfma_f32_16x16x32_bf16 v[16:19], a[128:131], a[144:147], v[16:19]\n\t"
"s_add_u32 s22, s22, 3145728\n\t"
"ds_write_addtid_b32 v203 offset:19616\n\t"
"ds_write_addtid_b32 v201 offset:27936\n\t"
"v_mfma_f32_16x16x32_bf16 v[16:19], a[132:135], a[160:163], v[16:19]\n\t"
"buffer_load_dwordx4 a[60:63], off, s[96:99], s76 offset:3072 sc1\n\t"
"v_mfma_f32_16x16x32_bf16 v[32:35], a[136:139], a[144:147], v[32:35]\n\t"
"s_nop 0\n\t"
"ds_read_b128 a[172:175], v224 offset:2816\n\t"
"v_mfma_f32_16x16x32_bf16 v[32:35], a[140:143], a[160:163], v[32:35]\n\t"
"buffer_load_dwordx4 a[56:59], off, s[96:99], s76 offset:2048 sc1\n\t"
"v_mfma_f32_16x16x32_bf16 v[36:39], a[136:139], a[148:151], v[36:39]\n\t"
"s_nop 0\n\t"
"ds_read_b128 a[156:159], v224 offset:768\n\t"
"v_mfma_f32_16x16x32_bf16 v[36:39], a[140:143], a[164:167], v[36:39]\n\t"
"s_nop 0\n\t"
"v_mfma_f32_16x16x32_bf16 v[20:23], a[128:131], a[148:151], v[20:23]\n\t"
"s_cmp_eq_u32 s21, 1\n\t"
"s_cselect_b32 s22, 2.0, s22\n\t"
"v_mfma_f32_16x16x32_bf16 v[20:23], a[132:135], a[164:167], v[20:23]\n\t"
"buffer_load_dwordx4 a[48:51], off, s[96:99], s76 offset:0 sc1\n\t"
"v_mfma_f32_16x16x32_bf16 v[4:7], a[120:123], a[148:151], v[4:7]\n\t"
"s_nop 0\n\t"
"v_mfma_f32_16x16x32_bf16 v[4:7], a[124:127], a[164:167], v[4:7]\n\t"
"s_waitcnt vmcnt(13) lgkmcnt(4)\n\t"
"s_waitcnt lgkmcnt(0)\n\t"
"s_barrier\n\t"
"v_mfma_f32_16x16x32_bf16 v[8:11], a[120:123], a[152:155], v[8:11]\n\t"
"s_nop 0\n\t"
"ds_read_b128 a[144:147], v224 offset:17568\n\t"
"ds_read_b128 a[160:163], v224 offset:19616\n\t"
"v_mfma_f32_16x16x32_bf16 v[8:11], a[124:127], a[168:171], v[8:11]\n\t"
"buffer_load_dwordx4 a[52:55], off, s[96:99], s76 offset:1024 sc1\n\t"
"v_mfma_f32_16x16x32_bf16 v[24:27], a[128:131], a[152:155], v[24:27]\n\t"
"s_nop 0\n\t"
"ds_read_b128 a[148:151], v224 offset:17824\n\t"
"ds_read_b128 a[164:167], v224 offset:19872\n\t"
"v_mfma_f32_16x16x32_bf16 v[24:27], a[132:135], a[168:171], v[24:27]\n\t"
"buffer_load_dwordx4 a[88:91], off, s[92:95], s76 offset:2048 sc1\n\t"
"v_mfma_f32_16x16x32_bf16 v[40:43], a[136:139], a[152:155], v[40:43]\n\t"
"s_nop 0\n\t"
"ds_read_b64_tr_b16 a[176:177], v248 offset:41472\n\t"
"ds_read_b64_tr_b16 a[178:179], v249 offset:41472\n\t"
"v_mfma_f32_16x16x32_bf16 v[40:43], a[140:143], a[168:171], v[40:43]\n\t"
"s_nop 0\n\t"
"ds_read_b64_tr_b16 a[180:181], v248 offset:41984\n\t"
"ds_read_b64_tr_b16 a[182:183], v249 offset:41984\n\t"
"v_mfma_f32_16x16x32_bf16 v[44:47], a[136:139], a[156:159], v[44:47]\n\t"
"s_nop 0\n\t"
"ds_read_b64_tr_b16 a[184:185], v250 offset:43776\n\t"
"ds_read_b64_tr_b16 a[186:187], v251 offset:43776\n\t"
"v_mfma_f32_16x16x32_bf16 v[44:47], a[140:143], a[172:175], v[44:47]\n\t"
"buffer_load_dwordx4 a[92:95], off, s[92:95], s76 offset:3072 sc1\n\t"
"v_mfma_f32_16x16x32_bf16 v[28:31], a[128:131], a[156:159], v[28:31]\n\t"
"s_nop 0\n\t"
"ds_read_b64_tr_b16 a[188:189], v250 offset:44288\n\t"
"ds_read_b64_tr_b16 a[190:191], v251 offset:44288\n\t"
"v_mfma_f32_16x16x32_bf16 v[28:31], a[132:135], a[172:175], v[28:31]\n\t"
"s_waitcnt lgkmcnt(4)\n\t"
"buffer_load_dwordx4 a[84:87], off, s[92:95], s76 offset:1024 sc1\n\t"
"v_mfma_f32_4x4x4_16b_bf16 v[192:195], a[210:211], a[176:177], 0\n\t"
"ds_read_b128 a[152:155], v224 offset:18080\n\t"
"v_mfma_f32_4x4x4_16b_bf16 v[196:199], a[210:211], a[178:179], 0\n\t"
"ds_read_b128 a[168:171], v224 offset:20128\n\t"
"v_mfma_f32_4x4x4_16b_bf16 v[200:203], a[210:211], a[180:181], 0\n\t"
"ds_read_b64_tr_b16 a[192:193], v250 offset:41472\n\t"
"v_mfma_f32_4x4x4_16b_bf16 v[204:207], a[210:211], a[182:183], 0\n\t"
"buffer_load_dwordx4 a[80:83], off, s[92:95], s76 offset:0 sc1\n\t"
"v_mfma_f32_16x16x32_bf16 v[12:15], a[120:123], a[156:159], v[12:15]\n\t"
"s_nop 0\n\t"
"ds_read_b64_tr_b16 a[194:195], v251 offset:41472\n\t"
"ds_read_b64_tr_b16 a[196:197], v250 offset:41984\n\t"
"v_mfma_f32_16x16x32_bf16 v[12:15], a[124:127], a[172:175], v[12:15]\n\t"
"s_waitcnt vmcnt(16)\n\t"
"ds_read_b64_tr_b16 a[198:199], v251 offset:41984\n\t"
"ds_read_b64_tr_b16 a[200:201], v248 offset:43776\n\t"
"v_mfma_f32_16x16x32_bf16 v[144:147], a[0:3], a[144:147], v[144:147]\n\t"
"s_nop 0\n\t"
"v_cvt_pk_bf16_f32 v196, v192, v196\n\t"
"v_cvt_pk_bf16_f32 v194, v194, v198\n\t"
"v_mfma_f32_16x16x32_bf16 v[144:147], a[4:7], a[160:163], v[144:147]\n\t"
"buffer_load_dwordx4 a[72:75], off, s[96:99], s75 offset:2048 sc1\n\t"
"v_mfma_f32_16x16x32_bf16 v[160:163], a[8:11], a[144:147], v[160:163]\n\t"
"s_nop 0\n\t"
"ds_read_b128 a[156:159], v224 offset:18336\n\t"
"v_cvt_pk_bf16_f32 v197, v193, v197\n\t"
"v_mfma_f32_16x16x32_bf16 v[160:163], a[12:15], a[160:163], v[160:163]\n\t"
"s_nop 0\n\t"
"ds_read_b128 a[172:175], v224 offset:20384\n\t"
"v_cvt_pk_bf16_f32 v195, v195, v199\n\t"
"v_mfma_f32_16x16x32_bf16 v[176:179], a[16:19], a[144:147], v[176:179]\n\t"
"s_nop 0\n\t"
"v_cvt_pk_bf16_f32 v198, v200, v204\n\t"
"v_cvt_pk_bf16_f32 v199, v201, v205\n\t"
"v_mfma_f32_16x16x32_bf16 v[176:179], a[20:23], a[160:163], v[176:179]\n\t"
"s_waitcnt lgkmcnt(7)\n\t"
"buffer_load_dwordx4 a[76:79], off, s[96:99], s75 offset:3072 sc1\n\t"
"v_mfma_f32_4x4x4_16b_bf16 v[208:211], a[210:211], a[184:185], 0\n\t"
"ds_read_b128 a[144:147], v224 offset:16544\n\t"
"v_mfma_f32_4x4x4_16b_bf16 v[212:215], a[210:211], a[186:187], 0\n\t"
"ds_write_addtid_b32 v198 offset:8320\n\t"
"v_mfma_f32_4x4x4_16b_bf16 v[216:219], a[210:211], a[188:189], 0\n\t"
"ds_write_addtid_b32 v197 offset:1024\n\t"
"v_mfma_f32_4x4x4_16b_bf16 v[220:223], a[210:211], a[190:191], 0\n\t"
"ds_write_addtid_b32 v196 offset:0\n\t"
"v_cvt_pk_bf16_f32 v193, v203, v207\n\t"
"v_mfma_f32_16x16x32_bf16 v[180:183], a[16:19], a[148:151], v[180:183]\n\t"
"s_nop 0\n\t"
"ds_read_b128 a[160:163], v224 offset:18592\n\t"
"v_cvt_pk_bf16_f32 v192, v202, v206\n\t"
"v_mfma_f32_16x16x32_bf16 v[180:183], a[20:23], a[164:167], v[180:183]\n\t"
"Lpad3_%=:\n\t"
"s_cbranch_execz Lpad7_%=\n\t"
"buffer_load_dwordx4 a[112:115], off, s[92:95], s80 offset:0 sc1\n\t"
"v_mfma_f32_16x16x32_bf16 v[164:167], a[8:11], a[148:151], v[164:167]\n\t"
"ds_write_addtid_b32 v199 offset:9344\n\t"
"v_cvt_pk_bf16_f32 v210, v210, v214\n\t"
"v_mfma_f32_16x16x32_bf16 v[164:167], a[12:15], a[164:167], v[164:167]\n\t"
"v_cvt_pk_bf16_f32 v212, v208, v212\n\t"
"v_cvt_pk_bf16_f32 v213, v209, v213\n\t"
"v_mfma_f32_16x16x32_bf16 v[148:151], a[0:3], a[148:151], v[148:151]\n\t"
"ds_read_b128 a[148:151], v224 offset:16800\n\t"
"v_cvt_pk_bf16_f32 v211, v211, v215\n\t"
"v_mfma_f32_16x16x32_bf16 v[148:151], a[4:7], a[164:167], v[148:151]\n\t"
"s_waitcnt lgkmcnt(7)\n\t"
"v_cvt_pk_bf16_f32 v214, v216, v220\n\t"
"v_cvt_pk_bf16_f32 v209, v219, v223\n\t"
"v_cvt_pk_bf16_f32 v215, v217, v221\n\t"
"v_mfma_f32_4x4x4_16b_bf16 v[200:203], a[210:211], a[192:193], 0\n\t"
"ds_read_b128 a[164:167], v224 offset:18848\n\t"
"v_mfma_f32_4x4x4_16b_bf16 v[204:207], a[210:211], a[194:195], 0\n\t"
"v_cvt_pk_bf16_f32 v208, v218, v222\n\t"
"v_mfma_f32_4x4x4_16b_bf16 v[196:199], a[210:211], a[196:197], 0\n\t"
"ds_read_b64_tr_b16 a[202:203], v249 offset:43776\n\t"
"v_mfma_f32_4x4x4_16b_bf16 v[216:219], a[210:211], a[198:199], 0\n\t"
"buffer_load_dwordx4 a[116:119], off, s[92:95], s80 offset:1024 sc1\n\t"
"v_mfma_f32_16x16x32_bf16 v[152:155], a[0:3], a[152:155], v[152:155]\n\t"
"s_nop 0\n\t"
"ds_write_addtid_b32 v214 offset:12448\n\t"
"ds_read_b64_tr_b16 a[204:205], v248 offset:44288\n\t"
"v_mfma_f32_16x16x32_bf16 v[152:155], a[4:7], a[168:171], v[152:155]\n\t"
"buffer_load_dwordx4 a[108:111], off, s[96:99], s79 offset:3072 sc1\n\t"
"v_mfma_f32_16x16x32_bf16 v[168:171], a[8:11], a[152:155], v[168:171]\n\t"
"ds_read_b64_tr_b16 a[206:207], v249 offset:44288\n\t"
"v_cvt_pk_bf16_f32 v205, v201, v205\n\t"
"v_mfma_f32_16x16x32_bf16 v[168:171], a[12:15], a[168:171], v[168:171]\n\t"
"v_cvt_pk_bf16_f32 v204, v200, v204\n\t"
"v_cvt_pk_bf16_f32 v203, v203, v207\n\t"
"v_mfma_f32_16x16x32_bf16 v[184:187], a[16:19], a[152:155], v[184:187]\n\t"
"ds_read_b128 a[152:155], v224 offset:17056\n\t"
"v_cvt_pk_bf16_f32 v201, v199, v219\n\t"
"v_mfma_f32_16x16x32_bf16 v[184:187], a[20:23], a[168:171], v[184:187]\n\t"
"buffer_load_dwordx4 a[104:107], off, s[96:99], s79 offset:2048 sc1\n\t"
"v_cvt_pk_bf16_f32 v207, v197, v217\n\t"
"v_mfma_f32_16x16x32_bf16 v[188:191], a[16:19], a[156:159], v[188:191]\n\t"
"ds_write_addtid_b32 v215 offset:13472\n\t"
"v_cvt_pk_bf16_f32 v202, v202, v206\n\t"
"v_mfma_f32_16x16x32_bf16 v[188:191], a[20:23], a[172:175], v[188:191]\n\t"
"s_waitcnt lgkmcnt(2)\n\t"
"ds_write_addtid_b32 v213 offset:5152\n\t"
"v_cvt_pk_bf16_f32 v206, v196, v216\n\t"
"v_mfma_f32_4x4x4_16b_bf16 v[220:223], a[210:211], a[200:201], 0\n\t"
"ds_write_addtid_b32 v212 offset:4128\n\t"
"v_mfma_f32_4x4x4_16b_bf16 v[212:215], a[210:211], a[202:203], 0\n\t"
"v_cvt_pk_bf16_f32 v200, v198, v218\n\t"
"v_mfma_f32_4x4x4_16b_bf16 v[196:199], a[210:211], a[204:205], 0\n\t"
"ds_read_b128 a[168:171], v224 offset:19104\n\t"
"v_mfma_f32_4x4x4_16b_bf16 v[216:219], a[210:211], a[206:207], 0\n\t"
"buffer_load_dwordx4 a[96:99], off, s[96:99], s79 offset:0 sc1\n\t"
"v_mfma_f32_16x16x32_bf16 v[172:175], a[8:11], a[156:159], v[172:175]\n\t"
"s_nop 0\n\t"
"ds_write_addtid_b32 v204 offset:2048\n\t"
"ds_write_addtid_b32 v207 offset:11392\n\t"
"v_mfma_f32_16x16x32_bf16 v[172:175], a[12:15], a[172:175], v[172:175]\n\t"
"s_nop 0\n\t"
"ds_write_addtid_b32 v206 offset:10368\n\t"
"ds_write_addtid_b32 v205 offset:3072\n\t"
"v_mfma_f32_16x16x32_bf16 v[156:159], a[0:3], a[156:159], v[156:159]\n\t"
"v_cvt_pk_bf16_f32 v222, v222, v214\n\t"
"v_cvt_pk_bf16_f32 v213, v221, v213\n\t"
"v_mfma_f32_16x16x32_bf16 v[156:159], a[4:7], a[172:175], v[156:159]\n\t"
"s_waitcnt vmcnt(17) lgkmcnt(8)\n\t"
"v_cvt_pk_bf16_f32 v212, v220, v212\n\t"
"v_cvt_pk_bf16_f32 v223, v223, v215\n\t"
"v_mfma_f32_16x16x32_bf16 v[96:99], a[24:27], a[144:147], v[96:99]\n\t"
"ds_write_addtid_b32 v212 offset:6176\n\t"
"v_cvt_pk_bf16_f32 v214, v196, v216\n\t"
"v_mfma_f32_16x16x32_bf16 v[96:99], a[28:31], a[160:163], v[96:99]\n\t"
"ds_write_addtid_b32 v214 offset:14496\n\t"
"v_cvt_pk_bf16_f32 v215, v197, v217\n\t"
"v_mfma_f32_16x16x32_bf16 v[112:115], a[32:35], a[144:147], v[112:115]\n\t"
"s_nop 0\n\t"
"ds_write_addtid_b32 v213 offset:7200\n\t"
"ds_write_addtid_b32 v215 offset:15520\n\t"
"v_mfma_f32_16x16x32_bf16 v[112:115], a[36:39], a[160:163], v[112:115]\n\t"
"buffer_load_dwordx4 a[100:103], off, s[96:99], s79 offset:1024 sc1\n\t"
"v_mfma_f32_16x16x32_bf16 v[128:131], a[40:43], a[144:147], v[128:131]\n\t"
"ds_read_b128 a[172:175], v224 offset:19360\n\t"
"v_cvt_pk_bf16_f32 v220, v198, v218\n\t"
"v_mfma_f32_16x16x32_bf16 v[128:131], a[44:47], a[160:163], v[128:131]\n\t"
"buffer_load_dwordx4 a[136:139], off, s[92:95], s79 offset:2048 sc1\n\t"
"v_mfma_f32_16x16x32_bf16 v[132:135], a[40:43], a[148:151], v[132:135]\n\t"
"s_nop 0\n\t"
"ds_read_b128 a[156:159], v224 offset:17312\n\t"
"ds_read_b128 a[244:247], v255 offset:39168\n\t"
"v_mfma_f32_16x16x32_bf16 v[132:135], a[44:47], a[164:167], v[132:135]\n\t"
"buffer_load_dwordx4 a[140:143], off, s[92:95], s79 offset:3072 sc1\n\t"
"v_mfma_f32_16x16x32_bf16 v[116:119], a[32:35], a[148:151], v[116:119]\n\t"
"v_mfma_f32_16x16x32_bf16 v[116:119], a[36:39], a[164:167], v[116:119]\n\t"
"buffer_load_dwordx4 a[132:135], off, s[92:95], s79 offset:1024 sc1\n\t"
"v_mfma_f32_16x16x32_bf16 v[100:103], a[24:27], a[148:151], v[100:103]\n\t"
"s_waitcnt lgkmcnt(0)\n\t"
"v_cvt_pk_bf16_f32 v221, v199, v219\n\t"
"v_mfma_f32_16x16x32_bf16 v[100:103], a[28:31], a[164:167], v[100:103]\n\t"
"s_barrier\n\t"
"v_mfma_f32_16x16x32_bf16 v[104:107], a[24:27], a[152:155], v[104:107]\n\t"
"s_nop 0\n\t"
"ds_read_b128 a[144:147], v224 offset:1024\n\t"
"ds_read_b128 a[148:151], v224 offset:1280\n\t"
"v_mfma_f32_16x16x32_bf16 v[104:107], a[28:31], a[168:171], v[104:107]\n\t"
"buffer_load_dwordx4 a[128:131], off, s[92:95], s79 offset:0 sc1\n\t"
"v_mfma_f32_16x16x32_bf16 v[120:123], a[32:35], a[152:155], v[120:123]\n\t"
"ds_read_b128 a[160:163], v224 offset:3072\n\t"
"v_add_u32 v248, 101632, v225\n\t"
"v_mfma_f32_16x16x32_bf16 v[120:123], a[36:39], a[168:171], v[120:123]\n\t"
"ds_read_b128 a[164:167], v224 offset:3328\n\t"
"v_mfma_f32_16x16x32_bf16 v[136:139], a[40:43], a[152:155], v[136:139]\n\t"
"ds_read_b128 a[152:155], v224 offset:1536\n\t"
"v_xor_b32 v249, 32, v248\n\t"
"v_mfma_f32_16x16x32_bf16 v[136:139], a[44:47], a[168:171], v[136:139]\n\t"
"buffer_load_dwordx4 a[120:123], off, s[96:99], s78 offset:2048 sc1\n\t"
"v_mfma_f32_16x16x32_bf16 v[140:143], a[40:43], a[156:159], v[140:143]\n\t"
"ds_read_b128 a[240:243], v255 offset:18432\n\t"
"v_xor_b32 v250, 64, v248\n\t"
"v_mfma_f32_16x16x32_bf16 v[140:143], a[44:47], a[172:175], v[140:143]\n\t"
"ds_read_b128 a[168:171], v224 offset:3584\n\t"
"v_xor_b32 v251, 96, v248\n\t"
"v_mfma_f32_16x16x32_bf16 v[124:127], a[32:35], a[156:159], v[124:127]\n\t"
"ds_read_b64_tr_b16 a[178:179], v250 offset:43776\n\t"
"v_mfma_f32_16x16x32_bf16 v[124:127], a[36:39], a[172:175], v[124:127]\n\t"
"buffer_load_dwordx4 a[124:127], off, s[96:99], s78 offset:3072 sc1\n\t"
"v_mfma_f32_16x16x32_bf16 v[108:111], a[24:27], a[156:159], v[108:111]\n\t"
"ds_read_b128 a[156:159], v224 offset:1792\n\t"
"v_mfma_f32_16x16x32_bf16 v[108:111], a[28:31], a[172:175], v[108:111]\n\t"
"s_waitcnt vmcnt(18) lgkmcnt(6)\n\t"
"ds_read_b128 a[172:175], v224 offset:3840\n\t"
"v_mfma_f32_16x16x32_bf16 v[48:51], a[48:51], a[144:147], v[48:51]\n\t"
"ds_read_b64_tr_b16 a[186:187], v251 offset:43776\n\t"
"v_mfma_f32_16x16x32_bf16 v[48:51], a[52:55], a[160:163], v[48:51]\n\t"
"buffer_load_dwordx4 a[16:19], off, s[92:95], s74 offset:0 sc1\n\t"
"v_mfma_f32_16x16x32_bf16 v[64:67], a[56:59], a[144:147], v[64:67]\n\t"
"ds_read_b64_tr_b16 a[194:195], v248 offset:43776\n\t"
"v_mfma_f32_16x16x32_bf16 v[64:67], a[60:63], a[160:163], v[64:67]\n\t"
"s_nop 0\n\t"
"ds_read_b64_tr_b16 a[202:203], v249 offset:43776\n\t"
"ds_read_b64_tr_b16 a[176:177], v248 offset:41472\n\t"
"v_mfma_f32_16x16x32_bf16 v[80:83], a[64:67], a[144:147], v[80:83]\n\t"
"ds_read_b64_tr_b16 a[184:185], v249 offset:41472\n\t"
"v_mfma_f32_16x16x32_bf16 v[80:83], a[68:71], a[160:163], v[80:83]\n\t"
"s_waitcnt lgkmcnt(8)\n\t"
"buffer_load_dwordx4 a[20:23], off, s[92:95], s74 offset:1024 sc1\n\t"
"v_mfma_f32_16x16x32_bf16 v[84:87], a[64:67], a[148:151], v[84:87]\n\t"
"ds_read_b128 a[144:147], v224 offset:0\n\t"
"v_mfma_f32_16x16x32_bf16 v[84:87], a[68:71], a[164:167], v[84:87]\n\t"
"ds_read_b128 a[160:163], v224 offset:2048\n\t"
"v_mfma_f32_16x16x32_bf16 v[68:71], a[56:59], a[148:151], v[68:71]\n\t"
"ds_read_b64_tr_b16 a[180:181], v248 offset:46080\n\t"
"v_mfma_f32_16x16x32_bf16 v[68:71], a[60:63], a[164:167], v[68:71]\n\t"
"buffer_load_dwordx4 a[12:15], off, s[96:99], s72 offset:3072 sc1\n\t"
"v_mfma_f32_16x16x32_bf16 v[52:55], a[48:51], a[148:151], v[52:55]\n\t"
"ds_read_b128 a[148:151], v224 offset:256\n\t"
"v_mfma_f32_16x16x32_bf16 v[52:55], a[52:55], a[164:167], v[52:55]\n\t"
"ds_read_b128 a[164:167], v224 offset:2304\n\t"
"v_mfma_f32_16x16x32_bf16 v[56:59], a[48:51], a[152:155], v[56:59]\n\t"
"s_nop 0\n\t"
"ds_write_addtid_b32 v209 offset:30016\n\t"
"ds_write_addtid_b32 v201 offset:27936\n\t"
"v_mfma_f32_16x16x32_bf16 v[56:59], a[52:55], a[168:171], v[56:59]\n\t"
"buffer_load_dwordx4 a[8:11], off, s[96:99], s72 offset:2048 sc1\n\t"
"v_mfma_f32_16x16x32_bf16 v[72:75], a[56:59], a[152:155], v[72:75]\n\t"
"s_nop 0\n\t"
"ds_write_addtid_b32 v194 offset:16544\n\t"
"ds_write_addtid_b32 v211 offset:21696\n\t"
"v_mfma_f32_16x16x32_bf16 v[72:75], a[60:63], a[168:171], v[72:75]\n\t"
"s_nop 0\n\t"
"ds_write_addtid_b32 v200 offset:26912\n\t"
"ds_write_addtid_b32 v222 offset:22720\n\t"
"v_mfma_f32_16x16x32_bf16 v[88:91], a[64:67], a[152:155], v[88:91]\n\t"
"ds_read_b128 a[152:155], v224 offset:512\n\t"
"v_mfma_f32_16x16x32_bf16 v[88:91], a[68:71], a[168:171], v[88:91]\n\t"
"s_waitcnt lgkmcnt(10)\n\t"
"buffer_load_dwordx4 a[0:3], off, s[96:99], s72 offset:0 sc1\n\t"
"v_mfma_f32_16x16x32_bf16 v[92:95], a[64:67], a[156:159], v[92:95]\n\t"
"ds_read_b128 a[168:171], v224 offset:2560\n\t"
"v_mfma_f32_16x16x32_bf16 v[92:95], a[68:71], a[172:175], v[92:95]\n\t"
"s_nop 0\n\t"
"ds_write_addtid_b32 v202 offset:18592\n\t"
"ds_write_addtid_b32 v203 offset:19616\n\t"
"v_mfma_f32_16x16x32_bf16 v[76:79], a[56:59], a[156:159], v[76:79]\n\t"
"s_nop 0\n\t"
"ds_write_addtid_b32 v192 offset:24864\n\t"
"ds_write_addtid_b32 v210 offset:20672\n\t"
"v_mfma_f32_16x16x32_bf16 v[76:79], a[60:63], a[172:175], v[76:79]\n\t"
"buffer_load_dwordx4 a[4:7], off, s[96:99], s72 offset:1024 sc1\n\t"
"v_mfma_f32_16x16x32_bf16 v[60:63], a[48:51], a[156:159], v[60:63]\n\t"
"ds_read_b128 a[156:159], v224 offset:768\n\t"
"v_mfma_f32_16x16x32_bf16 v[60:63], a[52:55], a[172:175], v[60:63]\n\t"
"s_waitcnt vmcnt(18) lgkmcnt(11)\n\t"
"ds_write_addtid_b32 v195 offset:17568\n\t"
"ds_write_addtid_b32 v193 offset:25888\n\t"
"v_mfma_f32_16x16x32_bf16 v[0:3], a[72:75], a[144:147], v[0:3]\n\t"
"s_nop 0\n\t"
"ds_write_addtid_b32 v208 offset:28992\n\t"
"ds_write_addtid_b32 v221 offset:32064\n\t"
"v_mfma_f32_16x16x32_bf16 v[0:3], a[76:79], a[160:163], v[0:3]\n\t"
"buffer_load_dwordx4 a[40:43], off, s[92:95], s72 offset:2048 sc1\n\t"
"v_mfma_f32_16x16x32_bf16 v[16:19], a[80:83], a[144:147], v[16:19]\n\t"
"s_nop 0\n\t"
"ds_write_addtid_b32 v223 offset:23744\n\t"
"ds_write_addtid_b32 v220 offset:31040\n\t"
"v_mfma_f32_16x16x32_bf16 v[16:19], a[84:87], a[160:163], v[16:19]\n\t"
"ds_read_b128 a[172:175], v224 offset:2816\n\t"
"v_mfma_f32_16x16x32_bf16 v[32:35], a[88:91], a[144:147], v[32:35]\n\t"
"ds_read_b64_tr_b16 a[188:189], v249 offset:46080\n\t"
"v_mfma_f32_16x16x32_bf16 v[32:35], a[92:95], a[160:163], v[32:35]\n\t"
"buffer_load_dwordx4 a[44:47], off, s[92:95], s72 offset:3072 sc1\n\t"
"v_mfma_f32_16x16x32_bf16 v[36:39], a[88:91], a[148:151], v[36:39]\n\t"
"ds_read_b64_tr_b16 a[192:193], v250 offset:41472\n\t"
"v_mfma_f32_16x16x32_bf16 v[36:39], a[92:95], a[164:167], v[36:39]\n\t"
"ds_read_b64_tr_b16 a[200:201], v251 offset:41472\n\t"
"v_mfma_f32_16x16x32_bf16 v[20:23], a[80:83], a[148:151], v[20:23]\n\t"
"s_nop 0\n\t"
"ds_read_b64_tr_b16 a[196:197], v250 offset:46080\n\t"
"ds_read_b64_tr_b16 a[204:205], v251 offset:46080\n\t"
"v_mfma_f32_16x16x32_bf16 v[20:23], a[84:87], a[164:167], v[20:23]\n\t"
"buffer_load_dwordx4 a[36:39], off, s[92:95], s72 offset:1024 sc1\n\t"
"v_mfma_f32_16x16x32_bf16 v[4:7], a[72:75], a[148:151], v[4:7]\n\t"
"s_waitcnt lgkmcnt(5)\n\t"
"v_mfma_f32_16x16x32_bf16 v[4:7], a[76:79], a[164:167], v[4:7]\n\t"
"s_barrier\n\t"
"v_mfma_f32_16x16x32_bf16 v[8:11], a[72:75], a[152:155], v[8:11]\n\t"
"s_nop 0\n\t"
"ds_read_b128 a[144:147], v224 offset:17568\n\t"
"ds_read_b128 a[160:163], v224 offset:19616\n\t"
"v_mfma_f32_16x16x32_bf16 v[8:11], a[76:79], a[168:171], v[8:11]\n\t"
"buffer_load_dwordx4 a[32:35], off, s[92:95], s72 offset:0 sc1\n\t"
"v_mfma_f32_16x16x32_bf16 v[24:27], a[80:83], a[152:155], v[24:27]\n\t"
"ds_read_b128 a[148:151], v224 offset:17824\n\t"
"v_mfma_f32_16x16x32_bf16 v[24:27], a[84:87], a[168:171], v[24:27]\n\t"
"ds_read_b128 a[164:167], v224 offset:19872\n\t"
"v_mfma_f32_16x16x32_bf16 v[40:43], a[88:91], a[152:155], v[40:43]\n\t"
"v_mfma_f32_16x16x32_bf16 v[40:43], a[92:95], a[168:171], v[40:43]\n\t"
"buffer_load_dwordx4 a[24:27], off, s[96:99], s69 offset:2048 sc1\n\t"
"v_mfma_f32_16x16x32_bf16 v[44:47], a[88:91], a[156:159], v[44:47]\n\t"
"ds_read_b128 a[168:171], v224 offset:20128\n\t"
"v_mfma_f32_16x16x32_bf16 v[44:47], a[92:95], a[172:175], v[44:47]\n\t"
"buffer_load_dwordx4 a[28:31], off, s[96:99], s69 offset:3072 sc1\n\t"
"v_mfma_f32_16x16x32_bf16 v[28:31], a[80:83], a[156:159], v[28:31]\n\t"
"ds_read_b128 a[152:155], v224 offset:18080\n\t"
"v_mfma_f32_16x16x32_bf16 v[28:31], a[84:87], a[172:175], v[28:31]\n\t"
"buffer_load_dwordx4 a[64:67], off, s[92:95], s83 offset:0 sc1\n\t"
"v_mfma_f32_16x16x32_bf16 v[12:15], a[72:75], a[156:159], v[12:15]\n\t"
"ds_read_b128 a[156:159], v224 offset:18336\n\t"
"v_mfma_f32_16x16x32_bf16 v[12:15], a[76:79], a[172:175], v[12:15]\n\t"
"s_waitcnt lgkmcnt(5)\n\t"
"buffer_load_dwordx4 a[68:71], off, s[92:95], s83 offset:1024 sc1\n\t"
"v_mfma_f32_4x4x4_16b_bf16 v[200:203], a[220:221], a[178:179], 0\n\t"
"ds_read_b128 a[172:175], v224 offset:20384\n\t"
"v_mfma_f32_4x4x4_16b_bf16 v[204:207], a[220:221], a[186:187], 0\n\t"
"ds_read_b64_tr_b16 a[178:179], v250 offset:44928\n\t"
"v_mfma_f32_4x4x4_16b_bf16 v[216:219], a[220:221], a[194:195], 0\n\t"
"ds_read_b64_tr_b16 a[186:187], v251 offset:44928\n\t"
"v_mfma_f32_4x4x4_16b_bf16 v[220:223], a[220:221], a[202:203], 0\n\t"
"v_mfma_f32_4x4x4_16b_bf16 v[192:195], a[222:223], a[176:177], v[200:203]\n\t"
"ds_read_b64_tr_b16 a[194:195], v248 offset:44928\n\t"
"v_mfma_f32_4x4x4_16b_bf16 v[196:199], a[222:223], a[184:185], v[204:207]\n\t"
"ds_read_b64_tr_b16 a[202:203], v249 offset:44928\n\t"
"v_mfma_f32_4x4x4_16b_bf16 v[200:203], a[222:223], a[180:181], v[200:203]\n\t"
"ds_read_b64_tr_b16 a[176:177], v248 offset:42624\n\t"
"v_mfma_f32_4x4x4_16b_bf16 v[204:207], a[222:223], a[188:189], v[204:207]\n\t"
"ds_read_b64_tr_b16 a[184:185], v249 offset:42624\n\t"
"v_mfma_f32_4x4x4_16b_bf16 v[208:211], a[222:223], a[192:193], v[216:219]\n\t"
"ds_read_b64_tr_b16 a[180:181], v248 offset:47232\n\t"
"v_mfma_f32_4x4x4_16b_bf16 v[212:215], a[222:223], a[200:201], v[220:223]\n\t"
"ds_read_b64_tr_b16 a[188:189], v249 offset:47232\n\t"
"v_mfma_f32_4x4x4_16b_bf16 v[216:219], a[222:223], a[196:197], v[216:219]\n\t"
"ds_read_b64_tr_b16 a[192:193], v250 offset:42624\n\t"
"v_mfma_f32_4x4x4_16b_bf16 v[220:223], a[222:223], a[204:205], v[220:223]\n\t"
"s_waitcnt vmcnt(20) lgkmcnt(8)\n\t"
"buffer_load_dwordx4 a[60:63], off, s[96:99], s82 offset:3072 sc1\n\t"
"v_mfma_f32_16x16x32_bf16 v[144:147], a[96:99], a[144:147], v[144:147]\n\t"
"ds_read_b64_tr_b16 a[200:201], v251 offset:42624\n\t"
"v_mfma_f32_16x16x32_bf16 v[144:147], a[100:103], a[160:163], v[144:147]\n\t"
"buffer_load_dwordx4 a[56:59], off, s[96:99], s82 offset:2048 sc1\n\t"
"v_mfma_f32_16x16x32_bf16 v[160:163], a[104:107], a[144:147], v[160:163]\n\t"
"ds_read_b64_tr_b16 a[196:197], v250 offset:47232\n\t"
"v_cvt_pk_bf16_f32 v192, -v192, -v196\n\t"
"v_mfma_f32_16x16x32_bf16 v[160:163], a[108:111], a[160:163], v[160:163]\n\t"
"ds_write_addtid_b32 v192 offset:0\n\t"
"v_cvt_pk_bf16_f32 v201, v201, v205\n\t"
"v_mfma_f32_16x16x32_bf16 v[176:179], a[112:115], a[144:147], v[176:179]\n\t"
"ds_read_b128 a[144:147], v224 offset:16544\n\t"
"v_cvt_pk_bf16_f32 v211, -v211, -v215\n\t"
"v_mfma_f32_16x16x32_bf16 v[176:179], a[116:119], a[160:163], v[176:179]\n\t"
"buffer_load_dwordx4 a[48:51], off, s[96:99], s82 offset:0 sc1\n\t"
"v_mfma_f32_16x16x32_bf16 v[180:183], a[112:115], a[148:151], v[180:183]\n\t"
"ds_read_b128 a[160:163], v224 offset:18592\n\t"
"v_cvt_pk_bf16_f32 v202, v202, v206\n\t"
"v_mfma_f32_16x16x32_bf16 v[180:183], a[116:119], a[164:167], v[180:183]\n\t"
"ds_write_addtid_b32 v202 offset:5152\n\t"
"v_cvt_pk_bf16_f32 v210, -v210, -v214\n\t"
"v_mfma_f32_16x16x32_bf16 v[164:167], a[104:107], a[148:151], v[164:167]\n\t"
"ds_write_addtid_b32 v210 offset:3072\n\t"
"v_cvt_pk_bf16_f32 v219, v219, v223\n\t"
"v_mfma_f32_16x16x32_bf16 v[164:167], a[108:111], a[164:167], v[164:167]\n\t"
"buffer_load_dwordx4 a[52:55], off, s[96:99], s82 offset:1024 sc1\n\t"
"v_mfma_f32_16x16x32_bf16 v[148:151], a[96:99], a[148:151], v[148:151]\n\t"
"ds_read_b128 a[148:151], v224 offset:16800\n\t"
"v_cvt_pk_bf16_f32 v208, -v208, -v212\n\t"
"v_mfma_f32_16x16x32_bf16 v[148:151], a[100:103], a[164:167], v[148:151]\n\t"
"ds_write_addtid_b32 v208 offset:2048\n\t"
"v_cvt_pk_bf16_f32 v216, v216, v220\n\t"
"v_mfma_f32_16x16x32_bf16 v[152:155], a[96:99], a[152:155], v[152:155]\n\t"
"ds_read_b128 a[164:167], v224 offset:18848\n\t"
"v_cvt_pk_bf16_f32 v217, v217, v221\n\t"
"v_mfma_f32_16x16x32_bf16 v[152:155], a[100:103], a[168:171], v[152:155]\n\t"
"buffer_load_dwordx4 a[88:91], off, s[92:95], s82 offset:2048 sc1\n\t"
"v_mfma_f32_16x16x32_bf16 v[168:171], a[104:107], a[152:155], v[168:171]\n\t"
"ds_write_addtid_b32 v219 offset:15520\n\t"
"v_cvt_pk_bf16_f32 v203, v203, v207\n\t"
"v_mfma_f32_16x16x32_bf16 v[168:171], a[108:111], a[168:171], v[168:171]\n\t"
"ds_write_addtid_b32 v216 offset:6176\n\t"
"v_cvt_pk_bf16_f32 v218, v218, v222\n\t"
"v_mfma_f32_16x16x32_bf16 v[184:187], a[112:115], a[152:155], v[184:187]\n\t"
"ds_read_b128 a[152:155], v224 offset:17056\n\t"
"v_cvt_pk_bf16_f32 v200, v200, v204\n\t"
"v_mfma_f32_16x16x32_bf16 v[184:187], a[116:119], a[168:171], v[184:187]\n\t"
"buffer_load_dwordx4 a[92:95], off, s[92:95], s82 offset:3072 sc1\n\t"
"v_mfma_f32_16x16x32_bf16 v[188:191], a[112:115], a[156:159], v[188:191]\n\t"
"ds_read_b128 a[168:171], v224 offset:19104\n\t"
"v_cvt_pk_bf16_f32 v194, -v194, -v198\n\t"
"v_mfma_f32_16x16x32_bf16 v[188:191], a[116:119], a[172:175], v[188:191]\n\t"
"ds_write_addtid_b32 v203 offset:13472\n\t"
"v_cvt_pk_bf16_f32 v193, -v193, -v197\n\t"
"v_mfma_f32_16x16x32_bf16 v[172:175], a[104:107], a[156:159], v[172:175]\n\t"
"ds_write_addtid_b32 v200 offset:4128\n\t"
"v_cvt_pk_bf16_f32 v195, -v195, -v199\n\t"
"v_mfma_f32_16x16x32_bf16 v[172:175], a[108:111], a[172:175], v[172:175]\n\t"
"buffer_load_dwordx4 a[84:87], off, s[92:95], s82 offset:1024 sc1\n\t"
"v_mfma_f32_16x16x32_bf16 v[156:159], a[96:99], a[156:159], v[156:159]\n\t"
"s_nop 0\n\t"
"ds_write_addtid_b32 v193 offset:8320\n\t"
"ds_write_addtid_b32 v201 offset:12448\n\t"
"v_mfma_f32_16x16x32_bf16 v[156:159], a[100:103], a[172:175], v[156:159]\n\t"
"s_waitcnt vmcnt(21) lgkmcnt(8)\n\t"
"ds_write_addtid_b32 v217 offset:14496\n\t"
"v_cvt_pk_bf16_f32 v209, -v209, -v213\n\t"
"v_mfma_f32_16x16x32_bf16 v[96:99], a[120:123], a[144:147], v[96:99]\n\t"
"ds_write_addtid_b32 v209 offset:10368\n\t"
"v_mfma_f32_16x16x32_bf16 v[96:99], a[124:127], a[160:163], v[96:99]\n\t"
"s_nop 0\n\t"
"ds_write_addtid_b32 v211 offset:11392\n\t"
"ds_write_addtid_b32 v218 offset:7200\n\t"
"v_mfma_f32_16x16x32_bf16 v[112:115], a[128:131], a[144:147], v[112:115]\n\t"
"s_nop 0\n\t"
"ds_write_addtid_b32 v194 offset:1024\n\t"
"ds_write_addtid_b32 v195 offset:9344\n\t"
"v_mfma_f32_16x16x32_bf16 v[112:115], a[132:135], a[160:163], v[112:115]\n\t"
".p2align 3\n\t"
"buffer_load_dwordx4 a[80:83], off, s[92:95], s82 offset:0 sc1\n\t"
"v_pk_fma_f32 v[48:49], v[144:145], s[90:91], v[48:49]\n\t"
"v_pk_fma_f32 v[50:51], v[146:147], s[90:91], v[50:51]\n\t"
"v_pk_add_f32 v[144:145], v[48:49], v[144:145] neg_lo:[0,1] neg_hi:[0,1]\n\t"
"v_pk_add_f32 v[146:147], v[50:51], v[146:147] neg_lo:[0,1] neg_hi:[0,1]\n\t"
"v_pk_fma_f32 v[64:65], v[160:161], s[90:91], v[64:65]\n\t"
"v_pk_fma_f32 v[66:67], v[162:163], s[90:91], v[66:67]\n\t"
"v_pk_add_f32 v[160:161], v[64:65], v[160:161] neg_lo:[0,1] neg_hi:[0,1]\n\t"
"v_pk_add_f32 v[162:163], v[66:67], v[162:163] neg_lo:[0,1] neg_hi:[0,1]\n\t"
"v_pk_fma_f32 v[80:81], v[176:177], s[90:91], v[80:81]\n\t"
"v_pk_fma_f32 v[82:83], v[178:179], s[90:91], v[82:83]\n\t"
"v_pk_add_f32 v[176:177], v[80:81], v[176:177] neg_lo:[0,1] neg_hi:[0,1]\n\t"
"v_pk_add_f32 v[178:179], v[82:83], v[178:179] neg_lo:[0,1] neg_hi:[0,1]\n\t"
"buffer_load_dwordx4 a[72:75], off, s[96:99], s81 offset:2048 sc1\n\t"
"v_pk_fma_f32 v[84:85], v[180:181], s[90:91], v[84:85]\n\t"
"v_pk_fma_f32 v[86:87], v[182:183], s[90:91], v[86:87]\n\t"
"v_pk_add_f32 v[180:181], v[84:85], v[180:181] neg_lo:[0,1] neg_hi:[0,1]\n\t"
"v_pk_add_f32 v[182:183], v[86:87], v[182:183] neg_lo:[0,1] neg_hi:[0,1]\n\t"
"v_pk_fma_f32 v[68:69], v[164:165], s[90:91], v[68:69]\n\t"
"v_pk_fma_f32 v[70:71], v[166:167], s[90:91], v[70:71]\n\t"
"v_pk_add_f32 v[164:165], v[68:69], v[164:165] neg_lo:[0,1] neg_hi:[0,1]\n\t"
"v_pk_add_f32 v[166:167], v[70:71], v[166:167] neg_lo:[0,1] neg_hi:[0,1]\n\t"
"v_pk_fma_f32 v[52:53], v[148:149], s[90:91], v[52:53]\n\t"
"v_pk_fma_f32 v[54:55], v[150:151], s[90:91], v[54:55]\n\t"
"v_pk_add_f32 v[148:149], v[52:53], v[148:149] neg_lo:[0,1] neg_hi:[0,1]\n\t"
"v_pk_add_f32 v[150:151], v[54:55], v[150:151] neg_lo:[0,1] neg_hi:[0,1]\n\t"
"buffer_load_dwordx4 a[76:79], off, s[96:99], s81 offset:3072 sc1\n\t"
"v_pk_fma_f32 v[56:57], v[152:153], s[90:91], v[56:57]\n\t"
"v_pk_fma_f32 v[58:59], v[154:155], s[90:91], v[58:59]\n\t"
"v_pk_add_f32 v[152:153], v[56:57], v[152:153] neg_lo:[0,1] neg_hi:[0,1]\n\t"
"v_pk_add_f32 v[154:155], v[58:59], v[154:155] neg_lo:[0,1] neg_hi:[0,1]\n\t"
"v_pk_fma_f32 v[72:73], v[168:169], s[90:91], v[72:73]\n\t"
"v_pk_fma_f32 v[74:75], v[170:171], s[90:91], v[74:75]\n\t"
"v_pk_add_f32 v[168:169], v[72:73], v[168:169] neg_lo:[0,1] neg_hi:[0,1]\n\t"
"v_pk_add_f32 v[170:171], v[74:75], v[170:171] neg_lo:[0,1] neg_hi:[0,1]\n\t"
"v_pk_fma_f32 v[88:89], v[184:185], s[90:91], v[88:89]\n\t"
"v_pk_fma_f32 v[90:91], v[186:187], s[90:91], v[90:91]\n\t"
"v_pk_add_f32 v[184:185], v[88:89], v[184:185] neg_lo:[0,1] neg_hi:[0,1]\n\t"
"v_pk_add_f32 v[186:187], v[90:91], v[186:187] neg_lo:[0,1] neg_hi:[0,1]\n\t"
"buffer_load_dwordx4 a[112:115], off, s[92:95], s56 offset:0 sc1\n\t"
"v_pk_fma_f32 v[92:93], v[188:189], s[90:91], v[92:93]\n\t"
"v_pk_fma_f32 v[94:95], v[190:191], s[90:91], v[94:95]\n\t"
"v_pk_add_f32 v[188:189], v[92:93], v[188:189] neg_lo:[0,1] neg_hi:[0,1]\n\t"
"v_pk_add_f32 v[190:191], v[94:95], v[190:191] neg_lo:[0,1] neg_hi:[0,1]\n\t"
"v_pk_fma_f32 v[76:77], v[172:173], s[90:91], v[76:77]\n\t"
"v_pk_fma_f32 v[78:79], v[174:175], s[90:91], v[78:79]\n\t"
"v_pk_add_f32 v[172:173], v[76:77], v[172:173] neg_lo:[0,1] neg_hi:[0,1]\n\t"
"v_pk_add_f32 v[174:175], v[78:79], v[174:175] neg_lo:[0,1] neg_hi:[0,1]\n\t"
"v_pk_fma_f32 v[60:61], v[156:157], s[90:91], v[60:61]\n\t"
"v_pk_fma_f32 v[62:63], v[158:159], s[90:91], v[62:63]\n\t"
"v_pk_add_f32 v[156:157], v[60:61], v[156:157] neg_lo:[0,1] neg_hi:[0,1]\n\t"
"v_pk_add_f32 v[158:159], v[62:63], v[158:159] neg_lo:[0,1] neg_hi:[0,1]\n\t"
"v_mfma_f32_16x16x32_bf16 v[128:131], a[136:139], a[144:147], v[128:131]\n\t"
"ds_read_b128 a[172:175], v224 offset:19360\n\t"
"v_mfma_f32_16x16x32_bf16 v[128:131], a[140:143], a[160:163], v[128:131]\n\t"
"buffer_load_dwordx4 a[116:119], off, s[92:95], s56 offset:1024 sc1\n\t"
"v_mfma_f32_16x16x32_bf16 v[132:135], a[136:139], a[148:151], v[132:135]\n\t"
"s_nop 0\n\t"
"ds_read_b128 a[156:159], v224 offset:17312\n\t"
"v_mfma_f32_16x16x32_bf16 v[132:135], a[140:143], a[164:167], v[132:135]\n\t"
"buffer_load_dwordx4 a[108:111], off, s[96:99], s55 offset:3072 sc1\n\t"
"v_mfma_f32_16x16x32_bf16 v[116:119], a[128:131], a[148:151], v[116:119]\n\t"
"s_nop 0\n\t"
"ds_read_b64_tr_b16 a[204:205], v251 offset:47232\n\t"
"v_mfma_f32_16x16x32_bf16 v[116:119], a[132:135], a[164:167], v[116:119]\n\t"
"buffer_load_dwordx4 a[104:107], off, s[96:99], s55 offset:2048 sc1\n\t"
"v_mfma_f32_16x16x32_bf16 v[100:103], a[120:123], a[148:151], v[100:103]\n\t"
"s_nop 0\n\t"
"v_accvgpr_read_b32 v224, a212\n\t"
"v_mfma_f32_16x16x32_bf16 v[100:103], a[124:127], a[164:167], v[100:103]\n\t"
"s_waitcnt lgkmcnt(0)\n\t"
"s_barrier\n\t"
"v_mfma_f32_16x16x32_bf16 v[104:107], a[120:123], a[152:155], v[104:107]\n\t"
"s_add_u32 m0, s26, 145152\n\t"
"ds_read_b128 a[144:147], v224 offset:1024\n\t"
"v_mfma_f32_16x16x32_bf16 v[104:107], a[124:127], a[168:171], v[104:107]\n\t"
"s_nop 0\n\t"
"ds_read_b128 a[160:163], v224 offset:3072\n\t"
"v_mfma_f32_16x16x32_bf16 v[120:123], a[128:131], a[152:155], v[120:123]\n\t"
"s_nop 0\n\t"
"ds_read_b128 a[148:151], v224 offset:1280\n\t"
"v_mfma_f32_16x16x32_bf16 v[120:123], a[132:135], a[168:171], v[120:123]\n\t"
"s_nop 0\n\t"
"ds_read_b128 a[164:167], v224 offset:3328\n\t"
"v_mfma_f32_16x16x32_bf16 v[136:139], a[136:139], a[152:155], v[136:139]\n\t"
"s_nop 0\n\t"
"v_mfma_f32_16x16x32_bf16 v[136:139], a[140:143], a[168:171], v[136:139]\n\t"
"buffer_load_dwordx4 v236, s[12:15], s22 offen offset:256 nt lds\n\t"
"v_mfma_f32_16x16x32_bf16 v[140:143], a[136:139], a[156:159], v[140:143]\n\t"
"s_nop 0\n\t"
"ds_read_b128 a[152:155], v224 offset:1536\n\t"
"v_mfma_f32_16x16x32_bf16 v[140:143], a[140:143], a[172:175], v[140:143]\n\t"
"s_nop 0\n\t"
"ds_read_b128 a[228:231], v254 offset:59904\n\t"
"v_mfma_f32_16x16x32_bf16 v[124:127], a[128:131], a[156:159], v[124:127]\n\t"
"s_nop 0\n\t"
"ds_read_b128 a[168:171], v224 offset:3584\n\t"
"v_mfma_f32_16x16x32_bf16 v[124:127], a[132:135], a[172:175], v[124:127]\n\t"
"buffer_load_dwordx4 v237, s[12:15], s22 offen offset:1280 nt lds\n\t"
"v_mfma_f32_16x16x32_bf16 v[108:111], a[120:123], a[156:159], v[108:111]\n\t"
"s_nop 0\n\t"
"ds_read_b128 a[156:159], v224 offset:1792\n\t"
"v_mfma_f32_16x16x32_bf16 v[108:111], a[124:127], a[172:175], v[108:111]\n\t"
"buffer_load_dwordx4 a[96:99], off, s[96:99], s55 offset:0 sc1\n\t"
"v_mfma_f32_4x4x4_16b_bf16 v[200:203], a[220:221], a[178:179], 0\n\t"
"ds_read_b128 a[172:175], v224 offset:3840\n\t"
"v_mfma_f32_4x4x4_16b_bf16 v[204:207], a[220:221], a[186:187], 0\n\t"
"ds_read_b64_tr_b16 a[178:179], v250 offset:23040\n\t"
"v_mfma_f32_4x4x4_16b_bf16 v[216:219], a[220:221], a[194:195], 0\n\t"
"ds_read_b64_tr_b16 a[186:187], v251 offset:23040\n\t"
"v_mfma_f32_4x4x4_16b_bf16 v[220:223], a[220:221], a[202:203], 0\n\t"
"buffer_load_dwordx4 v238, s[12:15], s22 offen offset:2304 nt lds\n\t"
"v_mfma_f32_4x4x4_16b_bf16 v[192:195], a[222:223], a[176:177], v[200:203]\n\t"
"ds_read_b64_tr_b16 a[194:195], v248 offset:23040\n\t"
"v_mfma_f32_4x4x4_16b_bf16 v[196:199], a[222:223], a[184:185], v[204:207]\n\t"
"ds_read_b64_tr_b16 a[202:203], v249 offset:23040\n\t"
"v_mfma_f32_4x4x4_16b_bf16 v[200:203], a[222:223], a[180:181], v[200:203]\n\t"
"ds_read_b64_tr_b16 a[176:177], v248 offset:20736\n\t"
"v_mfma_f32_4x4x4_16b_bf16 v[204:207], a[222:223], a[188:189], v[204:207]\n\t"
".p2align 3\n\t"
"ds_read_b64_tr_b16 a[184:185], v249 offset:20736\n\t"
"v_pk_fma_f32 v[0:1], v[96:97], s[90:91], v[0:1]\n\t"
"v_pk_fma_f32 v[2:3], v[98:99], s[90:91], v[2:3]\n\t"
"v_pk_add_f32 v[96:97], v[0:1], v[96:97] neg_lo:[0,1] neg_hi:[0,1]\n\t"
"v_pk_add_f32 v[98:99], v[2:3], v[98:99] neg_lo:[0,1] neg_hi:[0,1]\n\t"
"v_pk_fma_f32 v[16:17], v[112:113], s[90:91], v[16:17]\n\t"
"v_pk_fma_f32 v[18:19], v[114:115], s[90:91], v[18:19]\n\t"
"v_pk_add_f32 v[112:113], v[16:17], v[112:113] neg_lo:[0,1] neg_hi:[0,1]\n\t"
"v_pk_add_f32 v[114:115], v[18:19], v[114:115] neg_lo:[0,1] neg_hi:[0,1]\n\t"
"v_pk_fma_f32 v[32:33], v[128:129], s[90:91], v[32:33]\n\t"
"v_pk_fma_f32 v[34:35], v[130:131], s[90:91], v[34:35]\n\t"
"v_pk_add_f32 v[128:129], v[32:33], v[128:129] neg_lo:[0,1] neg_hi:[0,1]\n\t"
"v_pk_add_f32 v[130:131], v[34:35], v[130:131] neg_lo:[0,1] neg_hi:[0,1]\n\t"
"v_pk_fma_f32 v[36:37], v[132:133], s[90:91], v[36:37]\n\t"
"v_pk_fma_f32 v[38:39], v[134:135], s[90:91], v[38:39]\n\t"
"v_pk_add_f32 v[132:133], v[36:37], v[132:133] neg_lo:[0,1] neg_hi:[0,1]\n\t"
"v_pk_add_f32 v[134:135], v[38:39], v[134:135] neg_lo:[0,1] neg_hi:[0,1]\n\t"
"v_pk_fma_f32 v[20:21], v[116:117], s[90:91], v[20:21]\n\t"
"v_pk_fma_f32 v[22:23], v[118:119], s[90:91], v[22:23]\n\t"
"v_pk_add_f32 v[116:117], v[20:21], v[116:117] neg_lo:[0,1] neg_hi:[0,1]\n\t"
"v_pk_add_f32 v[118:119], v[22:23], v[118:119] neg_lo:[0,1] neg_hi:[0,1]\n\t"
"v_pk_fma_f32 v[4:5], v[100:101], s[90:91], v[4:5]\n\t"
"v_pk_fma_f32 v[6:7], v[102:103], s[90:91], v[6:7]\n\t"
"v_pk_add_f32 v[100:101], v[4:5], v[100:101] neg_lo:[0,1] neg_hi:[0,1]\n\t"
"v_pk_add_f32 v[102:103], v[6:7], v[102:103] neg_lo:[0,1] neg_hi:[0,1]\n\t"
"v_pk_fma_f32 v[8:9], v[104:105], s[90:91], v[8:9]\n\t"
"v_pk_fma_f32 v[10:11], v[106:107], s[90:91], v[10:11]\n\t"
"v_pk_add_f32 v[104:105], v[8:9], v[104:105] neg_lo:[0,1] neg_hi:[0,1]\n\t"
"v_pk_add_f32 v[106:107], v[10:11], v[106:107] neg_lo:[0,1] neg_hi:[0,1]\n\t"
"v_pk_fma_f32 v[24:25], v[120:121], s[90:91], v[24:25]\n\t"
"v_pk_fma_f32 v[26:27], v[122:123], s[90:91], v[26:27]\n\t"
"v_pk_add_f32 v[120:121], v[24:25], v[120:121] neg_lo:[0,1] neg_hi:[0,1]\n\t"
"v_pk_add_f32 v[122:123], v[26:27], v[122:123] neg_lo:[0,1] neg_hi:[0,1]\n\t"
"v_pk_fma_f32 v[40:41], v[136:137], s[90:91], v[40:41]\n\t"
"v_pk_fma_f32 v[42:43], v[138:139], s[90:91], v[42:43]\n\t"
"v_pk_add_f32 v[136:137], v[40:41], v[136:137] neg_lo:[0,1] neg_hi:[0,1]\n\t"
"v_pk_add_f32 v[138:139], v[42:43], v[138:139] neg_lo:[0,1] neg_hi:[0,1]\n\t"
"v_pk_fma_f32 v[44:45], v[140:141], s[90:91], v[44:45]\n\t"
"v_pk_fma_f32 v[46:47], v[142:143], s[90:91], v[46:47]\n\t"
"v_pk_add_f32 v[140:141], v[44:45], v[140:141] neg_lo:[0,1] neg_hi:[0,1]\n\t"
"v_pk_add_f32 v[142:143], v[46:47], v[142:143] neg_lo:[0,1] neg_hi:[0,1]\n\t"
"v_pk_fma_f32 v[28:29], v[124:125], s[90:91], v[28:29]\n\t"
"v_pk_fma_f32 v[30:31], v[126:127], s[90:91], v[30:31]\n\t"
"v_pk_add_f32 v[124:125], v[28:29], v[124:125] neg_lo:[0,1] neg_hi:[0,1]\n\t"
"v_pk_add_f32 v[126:127], v[30:31], v[126:127] neg_lo:[0,1] neg_hi:[0,1]\n\t"
"v_pk_fma_f32 v[12:13], v[108:109], s[90:91], v[12:13]\n\t"
"v_pk_fma_f32 v[14:15], v[110:111], s[90:91], v[14:15]\n\t"
"v_pk_add_f32 v[108:109], v[12:13], v[108:109] neg_lo:[0,1] neg_hi:[0,1]\n\t"
"v_pk_add_f32 v[110:111], v[14:15], v[110:111] neg_lo:[0,1] neg_hi:[0,1]\n\t"
"v_mfma_f32_4x4x4_16b_bf16 v[208:211], a[222:223], a[192:193], v[216:219]\n\t"
"ds_read_b64_tr_b16 a[180:181], v248 offset:25344\n\t"
"v_mfma_f32_4x4x4_16b_bf16 v[212:215], a[222:223], a[200:201], v[220:223]\n\t"
"ds_read_b64_tr_b16 a[188:189], v249 offset:25344\n\t"
"v_mfma_f32_4x4x4_16b_bf16 v[216:219], a[222:223], a[196:197], v[216:219]\n\t"
"ds_read_b64_tr_b16 a[192:193], v250 offset:20736\n\t"
"v_mfma_f32_4x4x4_16b_bf16 v[220:223], a[222:223], a[204:205], v[220:223]\n\t"
"s_waitcnt vmcnt(26) lgkmcnt(8)\n\t"
"buffer_load_dwordx4 v239, s[12:15], s22 offen offset:3328 nt lds\n\t"
"v_mfma_f32_16x16x32_bf16 v[48:51], a[0:3], a[144:147], v[48:51]\n\t"
"s_nop 0\n\t"
"v_cvt_pk_bf16_f32 v193, -v193, -v197\n\t"
"v_mfma_f32_16x16x32_bf16 v[48:51], a[4:7], a[160:163], v[48:51]\n\t"
"s_nop 0\n\t"
"ds_write_b128 v255, a[228:231] offset:41472\n\t"
"v_mfma_f32_16x16x32_bf16 v[64:67], a[8:11], a[144:147], v[64:67]\n\t"
"Lpad4_%=:\n\t"
"s_cbranch_execz Lret0_%=\n\t"
"v_mfma_f32_16x16x32_bf16 v[64:67], a[12:15], a[160:163], v[64:67]\n\t"
"s_mov_b64 exec, s[36:37]\n\t"
"buffer_load_dwordx4 v240, s[12:15], s22 offen offset:3840 nt lds\n\t"
"s_mov_b64 exec, -1\n\t"
"v_mfma_f32_16x16x32_bf16 v[80:83], a[16:19], a[144:147], v[80:83]\n\t"
"s_lshl_b32 m0, s73, 8\n\t"
"v_cvt_pk_bf16_f32 v211, -v211, -v215\n\t"
"v_mfma_f32_16x16x32_bf16 v[80:83], a[20:23], a[160:163], v[80:83]\n\t"
"ds_read_b128 a[160:163], v224 offset:2048\n\t"
"v_cvt_pk_bf16_f32 v203, v203, v207\n\t"
"v_mfma_f32_16x16x32_bf16 v[84:87], a[16:19], a[148:151], v[84:87]\n\t"
"ds_read_b128 a[144:147], v224 offset:0\n\t"
"v_cvt_pk_bf16_f32 v216, v216, v220\n\t"
"v_mfma_f32_16x16x32_bf16 v[84:87], a[20:23], a[164:167], v[84:87]\n\t"
"buffer_load_dwordx4 a[100:103], off, s[96:99], s55 offset:1024 sc1\n\t"
"v_mfma_f32_16x16x32_bf16 v[68:71], a[8:11], a[148:151], v[68:71]\n\t"
"ds_read_b64_tr_b16 a[200:201], v251 offset:20736\n\t"
"v_cvt_pk_bf16_f32 v192, -v192, -v196\n\t"
"v_mfma_f32_16x16x32_bf16 v[68:71], a[12:15], a[164:167], v[68:71]\n\t"
"v_cvt_pk_bf16_f32 v210, -v210, -v214\n\t"
"v_cvt_pk_bf16_f32 v217, v217, v221\n\t"
"v_mfma_f32_16x16x32_bf16 v[52:55], a[0:3], a[148:151], v[52:55]\n\t"
"ds_read_b128 a[148:151], v224 offset:256\n\t"
"v_mfma_f32_16x16x32_bf16 v[52:55], a[4:7], a[164:167], v[52:55]\n\t"
"ds_read_b128 a[164:167], v224 offset:2304\n\t"
"v_cvt_pk_bf16_f32 v208, -v208, -v212\n\t"
"v_mfma_f32_16x16x32_bf16 v[56:59], a[0:3], a[152:155], v[56:59]\n\t"
"ds_write_addtid_b32 v211 offset:27936\n\t"
"v_cvt_pk_bf16_f32 v201, v201, v205\n\t"
"v_mfma_f32_16x16x32_bf16 v[56:59], a[4:7], a[168:171], v[56:59]\n\t"
"ds_write_addtid_b32 v208 offset:18592\n\t"
"v_cvt_pk_bf16_f32 v195, -v195, -v199\n\t"
"v_mfma_f32_16x16x32_bf16 v[72:75], a[8:11], a[152:155], v[72:75]\n\t"
"ds_write_addtid_b32 v203 offset:30016\n\t"
"v_cvt_pk_bf16_f32 v200, v200, v204\n\t"
"v_mfma_f32_16x16x32_bf16 v[72:75], a[12:15], a[168:171], v[72:75]\n\t"
"ds_write_addtid_b32 v210 offset:19616\n\t"
"v_cvt_pk_bf16_f32 v194, -v194, -v198\n\t"
"v_mfma_f32_16x16x32_bf16 v[88:91], a[16:19], a[152:155], v[88:91]\n\t"
"ds_read_b128 a[152:155], v224 offset:512\n\t"
"v_cvt_pk_bf16_f32 v202, v202, v206\n\t"
"v_mfma_f32_16x16x32_bf16 v[88:91], a[20:23], a[168:171], v[88:91]\n\t"
"ds_write_addtid_b32 v201 offset:28992\n\t"
"v_cvt_pk_bf16_f32 v218, v218, v222\n\t"
"v_mfma_f32_16x16x32_bf16 v[92:95], a[16:19], a[156:159], v[92:95]\n\t"
"ds_write_addtid_b32 v192 offset:16544\n\t"
"v_mfma_f32_16x16x32_bf16 v[92:95], a[20:23], a[172:175], v[92:95]\n\t"
"ds_write_addtid_b32 v195 offset:25888\n\t"
"v_cvt_pk_bf16_f32 v219, v219, v223\n\t"
"v_mfma_f32_16x16x32_bf16 v[76:79], a[8:11], a[156:159], v[76:79]\n\t"
"s_nop 0\n\t"
"ds_write_addtid_b32 v202 offset:21696\n\t"
"ds_write_addtid_b32 v216 offset:22720\n\t"
"v_mfma_f32_16x16x32_bf16 v[76:79], a[12:15], a[172:175], v[76:79]\n\t"
"ds_write_addtid_b32 v217 offset:31040\n\t"
"v_mfma_f32_16x16x32_bf16 v[60:63], a[0:3], a[156:159], v[60:63]\n\t"
"ds_write_addtid_b32 v194 offset:17568\n\t"
"v_cvt_pk_bf16_f32 v209, -v209, -v213\n\t"
"v_mfma_f32_16x16x32_bf16 v[60:63], a[4:7], a[172:175], v[60:63]\n\t"
"s_waitcnt vmcnt(23) lgkmcnt(10)\n\t"
"ds_write_addtid_b32 v218 offset:23744\n\t"
"v_mfma_f32_16x16x32_bf16 v[0:3], a[24:27], a[144:147], v[0:3]\n\t"
"s_nop 0\n\t"
"ds_write_addtid_b32 v193 offset:24864\n\t"
"ds_write_addtid_b32 v219 offset:32064\n\t"
"v_mfma_f32_16x16x32_bf16 v[0:3], a[28:31], a[160:163], v[0:3]\n\t"
"ds_write_addtid_b32 v209 offset:26912\n\t"
"v_mfma_f32_16x16x32_bf16 v[16:19], a[32:35], a[144:147], v[16:19]\n\t"
"ds_write_addtid_b32 v200 offset:20672\n\t"
"v_mfma_f32_16x16x32_bf16 v[16:19], a[36:39], a[160:163], v[16:19]\n\t"
"ds_read_b128 a[172:175], v224 offset:2816\n\t"
"v_mfma_f32_16x16x32_bf16 v[32:35], a[40:43], a[144:147], v[32:35]\n\t"
"ds_read_b128 a[156:159], v224 offset:768\n\t"
"v_mfma_f32_16x16x32_bf16 v[32:35], a[44:47], a[160:163], v[32:35]\n\t"
"v_mfma_f32_16x16x32_bf16 v[36:39], a[40:43], a[148:151], v[36:39]\n\t"
"ds_read_b128 a[168:171], v224 offset:2560\n\t"
"v_mfma_f32_16x16x32_bf16 v[36:39], a[44:47], a[164:167], v[36:39]\n\t"
"ds_read_b64_tr_b16 a[196:197], v250 offset:25344\n\t"
"v_mfma_f32_16x16x32_bf16 v[20:23], a[32:35], a[148:151], v[20:23]\n\t"
"ds_read_b64_tr_b16 a[204:205], v251 offset:25344\n\t"
"v_mfma_f32_16x16x32_bf16 v[20:23], a[36:39], a[164:167], v[20:23]\n\t"
"v_mfma_f32_16x16x32_bf16 v[4:7], a[24:27], a[148:151], v[4:7]\n\t"
"s_waitcnt lgkmcnt(2)\n\t"
"v_mfma_f32_16x16x32_bf16 v[4:7], a[28:31], a[164:167], v[4:7]\n\t"
"s_barrier\n\t"
"v_mfma_f32_16x16x32_bf16 v[8:11], a[24:27], a[152:155], v[8:11]\n\t"
"s_nop 0\n\t"
"ds_read_b128 a[144:147], v224 offset:17568\n\t"
"ds_read_b128 a[148:151], v224 offset:17824\n\t"
"v_mfma_f32_16x16x32_bf16 v[8:11], a[28:31], a[168:171], v[8:11]\n\t"
"buffer_load_dwordx4 a[136:139], off, s[92:95], s55 offset:2048 sc1\n\t"
"v_mfma_f32_16x16x32_bf16 v[24:27], a[32:35], a[152:155], v[24:27]\n\t"
"ds_read_b128 a[160:163], v224 offset:19616\n\t"
"v_mfma_f32_16x16x32_bf16 v[24:27], a[36:39], a[168:171], v[24:27]\n\t"
"ds_read_b128 a[164:167], v224 offset:19872\n\t"
"v_mfma_f32_16x16x32_bf16 v[40:43], a[40:43], a[152:155], v[40:43]\n\t"
"v_mfma_f32_16x16x32_bf16 v[40:43], a[44:47], a[168:171], v[40:43]\n\t"
"buffer_load_dwordx4 a[140:143], off, s[92:95], s55 offset:3072 sc1\n\t"
"v_mfma_f32_16x16x32_bf16 v[44:47], a[40:43], a[156:159], v[44:47]\n\t"
"ds_read_b128 a[168:171], v224 offset:20128\n\t"
"v_mfma_f32_16x16x32_bf16 v[44:47], a[44:47], a[172:175], v[44:47]\n\t"
"buffer_load_dwordx4 a[132:135], off, s[92:95], s55 offset:1024 sc1\n\t"
"v_mfma_f32_16x16x32_bf16 v[28:31], a[32:35], a[156:159], v[28:31]\n\t"
"ds_read_b128 a[152:155], v224 offset:18080\n\t"
"v_mfma_f32_16x16x32_bf16 v[28:31], a[36:39], a[172:175], v[28:31]\n\t"
"v_mfma_f32_16x16x32_bf16 v[12:15], a[24:27], a[156:159], v[12:15]\n\t"
"ds_read_b128 a[156:159], v224 offset:18336\n\t"
"v_mfma_f32_16x16x32_bf16 v[12:15], a[28:31], a[172:175], v[12:15]\n\t"
"s_waitcnt lgkmcnt(5)\n\t"
"buffer_load_dwordx4 a[128:131], off, s[92:95], s55 offset:0 sc1\n\t"
"v_mfma_f32_4x4x4_16b_bf16 v[200:203], a[220:221], a[178:179], 0\n\t"
"ds_read_b128 a[172:175], v224 offset:20384\n\t"
"v_mfma_f32_4x4x4_16b_bf16 v[204:207], a[220:221], a[186:187], 0\n\t"
"ds_read_b64_tr_b16 a[178:179], v250 offset:2304\n\t"
"v_mfma_f32_4x4x4_16b_bf16 v[216:219], a[220:221], a[194:195], 0\n\t"
"ds_read_b64_tr_b16 a[186:187], v251 offset:2304\n\t"
"v_mfma_f32_4x4x4_16b_bf16 v[220:223], a[220:221], a[202:203], 0\n\t"
"v_mfma_f32_4x4x4_16b_bf16 v[192:195], a[222:223], a[176:177], v[200:203]\n\t"
"ds_read_b64_tr_b16 a[194:195], v248 offset:2304\n\t"
"v_mfma_f32_4x4x4_16b_bf16 v[196:199], a[222:223], a[184:185], v[204:207]\n\t"
"ds_read_b64_tr_b16 a[202:203], v249 offset:2304\n\t"
"v_mfma_f32_4x4x4_16b_bf16 v[200:203], a[222:223], a[180:181], v[200:203]\n\t"
"ds_read_b64_tr_b16 a[176:177], v248 offset:0\n\t"
"v_mfma_f32_4x4x4_16b_bf16 v[204:207], a[222:223], a[188:189], v[204:207]\n\t"
"v_mfma_f32_4x4x4_16b_bf16 v[208:211], a[222:223], a[192:193], v[216:219]\n\t"
"ds_read_b64_tr_b16 a[184:185], v249 offset:0\n\t"
"v_mfma_f32_4x4x4_16b_bf16 v[212:215], a[222:223], a[200:201], v[220:223]\n\t"
"ds_read_b64_tr_b16 a[180:181], v248 offset:4608\n\t"
"v_mfma_f32_4x4x4_16b_bf16 v[216:219], a[222:223], a[196:197], v[216:219]\n\t"
"ds_read_b64_tr_b16 a[188:189], v249 offset:4608\n\t"
"v_mfma_f32_4x4x4_16b_bf16 v[220:223], a[222:223], a[204:205], v[220:223]\n\t"
"s_waitcnt vmcnt(21) lgkmcnt(7)\n\t"
"buffer_load_dwordx4 a[120:123], off, s[96:99], s54 offset:2048 sc1\n\t"
"v_mfma_f32_16x16x32_bf16 v[144:147], a[48:51], a[144:147], v[144:147]\n\t"
"ds_read_b64_tr_b16 a[192:193], v250 offset:0\n\t"
"v_cvt_pk_bf16_f32 v192, -v192, -v196\n\t"
"v_mfma_f32_16x16x32_bf16 v[144:147], a[52:55], a[160:163], v[144:147]\n\t"
"buffer_load_dwordx4 a[124:127], off, s[96:99], s54 offset:3072 sc1\n\t"
"v_mfma_f32_16x16x32_bf16 v[160:163], a[56:59], a[144:147], v[160:163]\n\t"
"ds_read_b64_tr_b16 a[200:201], v251 offset:0\n\t"
"v_cvt_pk_bf16_f32 v193, -v193, -v197\n\t"
"v_mfma_f32_16x16x32_bf16 v[160:163], a[60:63], a[160:163], v[160:163]\n\t"
"ds_write_addtid_b32 v193 offset:8320\n\t"
"v_cvt_pk_bf16_f32 v194, -v194, -v198\n\t"
"v_mfma_f32_16x16x32_bf16 v[176:179], a[64:67], a[144:147], v[176:179]\n\t"
"ds_read_b128 a[144:147], v224 offset:16544\n\t"
"v_cvt_pk_bf16_f32 v211, -v211, -v215\n\t"
"v_mfma_f32_16x16x32_bf16 v[176:179], a[68:71], a[160:163], v[176:179]\n\t"
"buffer_load_dwordx4 a[16:19], off, s[92:95], s41 offset:0 sc1\n\t"
"v_mfma_f32_16x16x32_bf16 v[180:183], a[64:67], a[148:151], v[180:183]\n\t"
"ds_read_b128 a[160:163], v224 offset:18592\n\t"
"v_cvt_pk_bf16_f32 v219, v219, v223\n\t"
"v_mfma_f32_16x16x32_bf16 v[180:183], a[68:71], a[164:167], v[180:183]\n\t"
"ds_write_addtid_b32 v194 offset:1024\n\t"
"v_cvt_pk_bf16_f32 v217, v217, v221\n\t"
"v_mfma_f32_16x16x32_bf16 v[164:167], a[56:59], a[148:151], v[164:167]\n\t"
"ds_write_addtid_b32 v192 offset:0\n\t"
"v_cvt_pk_bf16_f32 v209, -v209, -v213\n\t"
"v_mfma_f32_16x16x32_bf16 v[164:167], a[60:63], a[164:167], v[164:167]\n\t"
"buffer_load_dwordx4 a[20:23], off, s[92:95], s41 offset:1024 sc1\n\t"
"v_mfma_f32_16x16x32_bf16 v[148:151], a[48:51], a[148:151], v[148:151]\n\t"
"ds_read_b128 a[148:151], v224 offset:16800\n\t"
"v_cvt_pk_bf16_f32 v195, -v195, -v199\n\t"
"v_mfma_f32_16x16x32_bf16 v[148:151], a[52:55], a[164:167], v[148:151]\n\t"
"ds_write_addtid_b32 v211 offset:11392\n\t"
"v_cvt_pk_bf16_f32 v201, v201, v205\n\t"
"v_mfma_f32_16x16x32_bf16 v[152:155], a[48:51], a[152:155], v[152:155]\n\t"
"ds_read_b128 a[164:167], v224 offset:18848\n\t"
"v_cvt_pk_bf16_f32 v216, v216, v220\n\t"
"v_mfma_f32_16x16x32_bf16 v[152:155], a[52:55], a[168:171], v[152:155]\n\t"
"buffer_load_dwordx4 a[12:15], off, s[96:99], s40 offset:3072 sc1\n\t"
"v_mfma_f32_16x16x32_bf16 v[168:171], a[56:59], a[152:155], v[168:171]\n\t"
"ds_write_addtid_b32 v195 offset:9344\n\t"
"v_cvt_pk_bf16_f32 v210, -v210, -v214\n\t"
"v_mfma_f32_16x16x32_bf16 v[168:171], a[60:63], a[168:171], v[168:171]\n\t"
"ds_write_addtid_b32 v209 offset:10368\n\t"
"v_cvt_pk_bf16_f32 v218, v218, v222\n\t"
"v_mfma_f32_16x16x32_bf16 v[184:187], a[64:67], a[152:155], v[184:187]\n\t"
"ds_read_b128 a[152:155], v224 offset:17056\n\t"
"v_cvt_pk_bf16_f32 v200, v200, v204\n\t"
"v_mfma_f32_16x16x32_bf16 v[184:187], a[68:71], a[168:171], v[184:187]\n\t"
"buffer_load_dwordx4 a[8:11], off, s[96:99], s40 offset:2048 sc1\n\t"
"v_mfma_f32_16x16x32_bf16 v[188:191], a[64:67], a[156:159], v[188:191]\n\t"
"ds_read_b128 a[168:171], v224 offset:19104\n\t"
"v_cvt_pk_bf16_f32 v203, v203, v207\n\t"
"v_mfma_f32_16x16x32_bf16 v[188:191], a[68:71], a[172:175], v[188:191]\n\t"
"ds_write_addtid_b32 v217 offset:14496\n\t"
"v_cvt_pk_bf16_f32 v202, v202, v206\n\t"
"v_mfma_f32_16x16x32_bf16 v[172:175], a[56:59], a[156:159], v[172:175]\n\t"
"s_nop 0\n\t"
"ds_write_addtid_b32 v202 offset:5152\n\t"
"ds_write_addtid_b32 v216 offset:6176\n\t"
"v_mfma_f32_16x16x32_bf16 v[172:175], a[60:63], a[172:175], v[172:175]\n\t"
"buffer_load_dwordx4 a[0:3], off, s[96:99], s40 offset:0 sc1\n\t"
"v_mfma_f32_16x16x32_bf16 v[156:159], a[48:51], a[156:159], v[156:159]\n\t"
"s_nop 0\n\t"
"ds_write_addtid_b32 v210 offset:3072\n\t"
"ds_write_addtid_b32 v219 offset:15520\n\t"
"v_mfma_f32_16x16x32_bf16 v[156:159], a[52:55], a[172:175], v[156:159]\n\t"
"s_waitcnt vmcnt(22) lgkmcnt(9)\n\t"
"ds_write_addtid_b32 v203 offset:13472\n\t"
"v_mfma_f32_16x16x32_bf16 v[96:99], a[72:75], a[144:147], v[96:99]\n\t"
"ds_write_addtid_b32 v200 offset:4128\n\t"
"v_cvt_pk_bf16_f32 v208, -v208, -v212\n\t"
"v_mfma_f32_16x16x32_bf16 v[96:99], a[76:79], a[160:163], v[96:99]\n\t"
"ds_write_addtid_b32 v218 offset:7200\n\t"
"v_mfma_f32_16x16x32_bf16 v[112:115], a[80:83], a[144:147], v[112:115]\n\t"
"s_nop 0\n\t"
"ds_write_addtid_b32 v201 offset:12448\n\t"
"ds_write_addtid_b32 v208 offset:2048\n\t"
"v_mfma_f32_16x16x32_bf16 v[112:115], a[84:87], a[160:163], v[112:115]\n\t"
"buffer_load_dwordx4 a[4:7], off, s[96:99], s40 offset:1024 sc1\n\t"
"v_mfma_f32_16x16x32_bf16 v[128:131], a[88:91], a[144:147], v[128:131]\n\t"
"ds_read_b128 a[172:175], v224 offset:19360\n\t"
"v_mfma_f32_16x16x32_bf16 v[128:131], a[92:95], a[160:163], v[128:131]\n\t"
"buffer_load_dwordx4 a[40:43], off, s[92:95], s40 offset:2048 sc1\n\t"
"v_mfma_f32_16x16x32_bf16 v[132:135], a[88:91], a[148:151], v[132:135]\n\t"
"ds_read_b128 a[156:159], v224 offset:17312\n\t"
"v_mfma_f32_16x16x32_bf16 v[132:135], a[92:95], a[164:167], v[132:135]\n\t"
"ds_read_b64_tr_b16 a[196:197], v250 offset:4608\n\t"
"v_mfma_f32_16x16x32_bf16 v[116:119], a[80:83], a[148:151], v[116:119]\n\t"
"ds_read_b64_tr_b16 a[204:205], v251 offset:4608\n\t"
"v_mfma_f32_16x16x32_bf16 v[116:119], a[84:87], a[164:167], v[116:119]\n\t"
"buffer_load_dwordx4 a[44:47], off, s[92:95], s40 offset:3072 sc1\n\t"
"v_mfma_f32_16x16x32_bf16 v[100:103], a[72:75], a[148:151], v[100:103]\n\t"
"s_waitcnt lgkmcnt(2)\n\t"
"v_mfma_f32_16x16x32_bf16 v[100:103], a[76:79], a[164:167], v[100:103]\n\t"
"buffer_load_dwordx4 a[36:39], off, s[92:95], s40 offset:1024 sc1\n\t"
"s_barrier\n\t"
"v_mfma_f32_16x16x32_bf16 v[104:107], a[72:75], a[152:155], v[104:107]\n\t"
"ds_read_b128 a[144:147], v224 offset:1024\n\t"
"v_mfma_f32_16x16x32_bf16 v[104:107], a[76:79], a[168:171], v[104:107]\n\t"
"ds_read_b128 a[160:163], v224 offset:3072\n\t"
"v_mfma_f32_16x16x32_bf16 v[120:123], a[80:83], a[152:155], v[120:123]\n\t"
"ds_read_b128 a[148:151], v224 offset:1280\n\t"
"v_mfma_f32_16x16x32_bf16 v[120:123], a[84:87], a[168:171], v[120:123]\n\t"
"ds_read_b128 a[164:167], v224 offset:3328\n\t"
"v_mfma_f32_16x16x32_bf16 v[136:139], a[88:91], a[152:155], v[136:139]\n\t"
"v_mfma_f32_16x16x32_bf16 v[136:139], a[92:95], a[168:171], v[136:139]\n\t"
"buffer_load_dwordx4 a[32:35], off, s[92:95], s40 offset:0 sc1\n\t"
"v_mfma_f32_16x16x32_bf16 v[140:143], a[88:91], a[156:159], v[140:143]\n\t"
"ds_read_b128 a[168:171], v224 offset:3584\n\t"
"v_mfma_f32_16x16x32_bf16 v[140:143], a[92:95], a[172:175], v[140:143]\n\t"
"ds_read_b128 a[152:155], v224 offset:1536\n\t"
"v_mfma_f32_16x16x32_bf16 v[124:127], a[80:83], a[156:159], v[124:127]\n\t"
"v_mfma_f32_16x16x32_bf16 v[124:127], a[84:87], a[172:175], v[124:127]\n\t"
"buffer_load_dwordx4 a[24:27], off, s[96:99], 0 offset:2048 sc1\n\t"
"v_mfma_f32_16x16x32_bf16 v[108:111], a[72:75], a[156:159], v[108:111]\n\t"
"ds_read_b128 a[156:159], v224 offset:1792\n\t"
"v_mfma_f32_16x16x32_bf16 v[108:111], a[76:79], a[172:175], v[108:111]\n\t"
"s_waitcnt lgkmcnt(5)\n\t"
"ds_read_b128 a[172:175], v224 offset:3840\n\t"
"v_mfma_f32_4x4x4_16b_bf16 v[200:203], a[220:221], a[178:179], 0\n\t"
"v_mfma_f32_4x4x4_16b_bf16 v[204:207], a[220:221], a[186:187], 0\n\t"
"ds_read_b64_tr_b16 a[178:179], v250 offset:24192\n\t"
"v_mfma_f32_4x4x4_16b_bf16 v[216:219], a[220:221], a[194:195], 0\n\t"
"ds_read_b64_tr_b16 a[186:187], v251 offset:24192\n\t"
"v_mfma_f32_4x4x4_16b_bf16 v[220:223], a[220:221], a[202:203], 0\n\t"
"ds_read_b64_tr_b16 a[194:195], v248 offset:24192\n\t"
"v_mfma_f32_4x4x4_16b_bf16 v[192:195], a[222:223], a[176:177], v[200:203]\n\t"
"ds_read_b64_tr_b16 a[202:203], v249 offset:24192\n\t"
"v_mfma_f32_4x4x4_16b_bf16 v[196:199], a[222:223], a[184:185], v[204:207]\n\t"
"v_mfma_f32_4x4x4_16b_bf16 v[200:203], a[222:223], a[180:181], v[200:203]\n\t"
"ds_read_b64_tr_b16 a[176:177], v248 offset:21888\n\t"
"v_mfma_f32_4x4x4_16b_bf16 v[204:207], a[222:223], a[188:189], v[204:207]\n\t"
"ds_read_b64_tr_b16 a[184:185], v249 offset:21888\n\t"
"v_mfma_f32_4x4x4_16b_bf16 v[208:211], a[222:223], a[192:193], v[216:219]\n\t"
"ds_read_b64_tr_b16 a[180:181], v248 offset:26496\n\t"
"v_mfma_f32_4x4x4_16b_bf16 v[212:215], a[222:223], a[200:201], v[220:223]\n\t"
"ds_read_b64_tr_b16 a[188:189], v249 offset:26496\n\t"
"v_mfma_f32_4x4x4_16b_bf16 v[216:219], a[222:223], a[196:197], v[216:219]\n\t"
"ds_read_b64_tr_b16 a[192:193], v250 offset:21888\n\t"
"v_mfma_f32_4x4x4_16b_bf16 v[220:223], a[222:223], a[204:205], v[220:223]\n\t"
"s_waitcnt vmcnt(17) lgkmcnt(8)\n\t"
"buffer_load_dwordx4 a[28:31], off, s[96:99], 0 offset:3072 sc1\n\t"
"v_mfma_f32_16x16x32_bf16 v[48:51], a[96:99], a[144:147], v[48:51]\n\t"
"ds_read_b64_tr_b16 a[200:201], v251 offset:21888\n\t"
"v_cvt_pk_bf16_f32 v192, -v192, -v196\n\t"
"v_mfma_f32_16x16x32_bf16 v[48:51], a[100:103], a[160:163], v[48:51]\n\t"
"buffer_load_dwordx4 a[64:67], off, s[92:95], s68 offset:0 sc1\n\t"
"v_mfma_f32_16x16x32_bf16 v[64:67], a[104:107], a[144:147], v[64:67]\n\t"
"ds_read_b64_tr_b16 a[196:197], v250 offset:26496\n\t"
"v_cvt_pk_bf16_f32 v195, -v195, -v199\n\t"
"v_mfma_f32_16x16x32_bf16 v[64:67], a[108:111], a[160:163], v[64:67]\n\t"
"ds_write_addtid_b32 v192 offset:16544\n\t"
"v_cvt_pk_bf16_f32 v202, v202, v206\n\t"
"v_mfma_f32_16x16x32_bf16 v[80:83], a[112:115], a[144:147], v[80:83]\n\t"
"ds_read_b128 a[144:147], v224 offset:0\n\t"
"v_cvt_pk_bf16_f32 v193, -v193, -v197\n\t"
"v_mfma_f32_16x16x32_bf16 v[80:83], a[116:119], a[160:163], v[80:83]\n\t"
"buffer_load_dwordx4 a[68:71], off, s[92:95], s68 offset:1024 sc1\n\t"
"v_mfma_f32_16x16x32_bf16 v[84:87], a[112:115], a[148:151], v[84:87]\n\t"
"ds_read_b128 a[160:163], v224 offset:2048\n\t"
"v_cvt_pk_bf16_f32 v203, v203, v207\n\t"
"v_mfma_f32_16x16x32_bf16 v[84:87], a[116:119], a[164:167], v[84:87]\n\t"
"ds_write_addtid_b32 v202 offset:21696\n\t"
"v_cvt_pk_bf16_f32 v200, v200, v204\n\t"
"v_mfma_f32_16x16x32_bf16 v[68:71], a[104:107], a[148:151], v[68:71]\n\t"
"ds_write_addtid_b32 v200 offset:20672\n\t"
"v_mfma_f32_16x16x32_bf16 v[68:71], a[108:111], a[164:167], v[68:71]\n\t"
"buffer_load_dwordx4 a[60:63], off, s[96:99], s67 offset:3072 sc1\n\t"
"v_mfma_f32_16x16x32_bf16 v[52:55], a[96:99], a[148:151], v[52:55]\n\t"
"ds_read_b128 a[148:151], v224 offset:256\n\t"
"v_cvt_pk_bf16_f32 v217, v217, v221\n\t"
"v_mfma_f32_16x16x32_bf16 v[52:55], a[100:103], a[164:167], v[52:55]\n\t"
"ds_write_addtid_b32 v217 offset:31040\n\t"
"v_cvt_pk_bf16_f32 v216, v216, v220\n\t"
"v_mfma_f32_16x16x32_bf16 v[56:59], a[96:99], a[152:155], v[56:59]\n\t"
"ds_read_b128 a[164:167], v224 offset:2304\n\t"
"v_cvt_pk_bf16_f32 v211, -v211, -v215\n\t"
"v_mfma_f32_16x16x32_bf16 v[56:59], a[100:103], a[168:171], v[56:59]\n\t"
"buffer_load_dwordx4 a[56:59], off, s[96:99], s67 offset:2048 sc1\n\t"
"v_mfma_f32_16x16x32_bf16 v[72:75], a[104:107], a[152:155], v[72:75]\n\t"
"ds_write_addtid_b32 v211 offset:27936\n\t"
"v_cvt_pk_bf16_f32 v208, -v208, -v212\n\t"
"v_mfma_f32_16x16x32_bf16 v[72:75], a[108:111], a[168:171], v[72:75]\n\t"
"ds_write_addtid_b32 v216 offset:22720\n\t"
"v_cvt_pk_bf16_f32 v218, v218, v222\n\t"
"v_mfma_f32_16x16x32_bf16 v[88:91], a[112:115], a[152:155], v[88:91]\n\t"
"ds_read_b128 a[152:155], v224 offset:512\n\t"
"v_cvt_pk_bf16_f32 v209, -v209, -v213\n\t"
"v_mfma_f32_16x16x32_bf16 v[88:91], a[116:119], a[168:171], v[88:91]\n\t"
"buffer_load_dwordx4 a[48:51], off, s[96:99], s67 offset:0 sc1\n\t"
"v_mfma_f32_16x16x32_bf16 v[92:95], a[112:115], a[156:159], v[92:95]\n\t"
"ds_read_b128 a[168:171], v224 offset:2560\n\t"
"v_cvt_pk_bf16_f32 v201, v201, v205\n\t"
"v_mfma_f32_16x16x32_bf16 v[92:95], a[116:119], a[172:175], v[92:95]\n\t"
"ds_write_addtid_b32 v209 offset:26912\n\t"
"v_cvt_pk_bf16_f32 v210, -v210, -v214\n\t"
"v_mfma_f32_16x16x32_bf16 v[76:79], a[104:107], a[156:159], v[76:79]\n\t"
"s_nop 0\n\t"
"ds_write_addtid_b32 v193 offset:24864\n\t"
"ds_write_addtid_b32 v195 offset:25888\n\t"
"v_mfma_f32_16x16x32_bf16 v[76:79], a[108:111], a[172:175], v[76:79]\n\t"
"buffer_load_dwordx4 a[52:55], off, s[96:99], s67 offset:1024 sc1\n\t"
"v_mfma_f32_16x16x32_bf16 v[60:63], a[96:99], a[156:159], v[60:63]\n\t"
"s_nop 0\n\t"
"ds_write_addtid_b32 v203 offset:30016\n\t"
"ds_write_addtid_b32 v210 offset:19616\n\t"
"v_mfma_f32_16x16x32_bf16 v[60:63], a[100:103], a[172:175], v[60:63]\n\t"
"s_waitcnt vmcnt(18) lgkmcnt(9)\n\t"
"ds_write_addtid_b32 v208 offset:18592\n\t"
"v_cvt_pk_bf16_f32 v219, v219, v223\n\t"
"v_mfma_f32_16x16x32_bf16 v[0:3], a[120:123], a[144:147], v[0:3]\n\t"
"ds_write_addtid_b32 v219 offset:32064\n\t"
"v_cvt_pk_bf16_f32 v194, -v194, -v198\n\t"
"v_mfma_f32_16x16x32_bf16 v[0:3], a[124:127], a[160:163], v[0:3]\n\t"
"ds_write_addtid_b32 v194 offset:17568\n\t"
"v_mfma_f32_16x16x32_bf16 v[16:19], a[128:131], a[144:147], v[16:19]\n\t"
"s_nop 0\n\t"
"ds_write_addtid_b32 v201 offset:28992\n\t"
"ds_write_addtid_b32 v218 offset:23744\n\t"
"v_mfma_f32_16x16x32_bf16 v[16:19], a[132:135], a[160:163], v[16:19]\n\t"
"buffer_load_dwordx4 a[88:91], off, s[92:95], s67 offset:2048 sc1\n\t"
"v_mfma_f32_16x16x32_bf16 v[32:35], a[136:139], a[144:147], v[32:35]\n\t"
"ds_read_b128 a[156:159], v224 offset:768\n\t"
"v_mfma_f32_16x16x32_bf16 v[32:35], a[140:143], a[160:163], v[32:35]\n\t"
"buffer_load_dwordx4 a[92:95], off, s[92:95], s67 offset:3072 sc1\n\t"
"v_mfma_f32_16x16x32_bf16 v[36:39], a[136:139], a[148:151], v[36:39]\n\t"
"ds_read_b128 a[172:175], v224 offset:2816\n\t"
"v_mfma_f32_16x16x32_bf16 v[36:39], a[140:143], a[164:167], v[36:39]\n\t"
"buffer_load_dwordx4 a[84:87], off, s[92:95], s67 offset:1024 sc1\n\t"
"v_mfma_f32_16x16x32_bf16 v[20:23], a[128:131], a[148:151], v[20:23]\n\t"
"ds_read_b64_tr_b16 a[204:205], v251 offset:26496\n\t"
"v_mfma_f32_16x16x32_bf16 v[20:23], a[132:135], a[164:167], v[20:23]\n\t"
"v_mfma_f32_16x16x32_bf16 v[4:7], a[120:123], a[148:151], v[4:7]\n\t"
"buffer_load_dwordx4 a[80:83], off, s[92:95], s67 offset:0 sc1\n\t"
"v_mfma_f32_16x16x32_bf16 v[4:7], a[124:127], a[164:167], v[4:7]\n\t"
"s_waitcnt lgkmcnt(0)\n\t"
"s_barrier\n\t"
"v_mfma_f32_16x16x32_bf16 v[8:11], a[120:123], a[152:155], v[8:11]\n\t"
"s_nop 0\n\t"
"ds_read_b128 a[144:147], v224 offset:17568\n\t"
"ds_read_b128 a[160:163], v224 offset:19616\n\t"
"v_mfma_f32_16x16x32_bf16 v[8:11], a[124:127], a[168:171], v[8:11]\n\t"
"buffer_load_dwordx4 a[72:75], off, s[96:99], s66 offset:2048 sc1\n\t"
"v_mfma_f32_16x16x32_bf16 v[24:27], a[128:131], a[152:155], v[24:27]\n\t"
"ds_read_b128 a[148:151], v224 offset:17824\n\t"
"v_mfma_f32_16x16x32_bf16 v[24:27], a[132:135], a[168:171], v[24:27]\n\t"
"ds_read_b128 a[164:167], v224 offset:19872\n\t"
"v_mfma_f32_16x16x32_bf16 v[40:43], a[136:139], a[152:155], v[40:43]\n\t"
"ds_read_b128 a[228:231], v254 offset:39168\n\t"
"v_mfma_f32_16x16x32_bf16 v[40:43], a[140:143], a[168:171], v[40:43]\n\t"
"buffer_load_dwordx4 a[76:79], off, s[96:99], s66 offset:3072 sc1\n\t"
"v_mfma_f32_16x16x32_bf16 v[44:47], a[136:139], a[156:159], v[44:47]\n\t"
"ds_read_b128 a[152:155], v224 offset:18080\n\t"
"v_mfma_f32_16x16x32_bf16 v[44:47], a[140:143], a[172:175], v[44:47]\n\t"
"buffer_load_dwordx4 a[112:115], off, s[92:95], s53 offset:0 sc1\n\t"
"v_mfma_f32_16x16x32_bf16 v[28:31], a[128:131], a[156:159], v[28:31]\n\t"
"ds_read_b128 a[168:171], v224 offset:20128\n\t"
"v_mfma_f32_16x16x32_bf16 v[28:31], a[132:135], a[172:175], v[28:31]\n\t"
"v_mfma_f32_16x16x32_bf16 v[12:15], a[120:123], a[156:159], v[12:15]\n\t"
"ds_read_b128 a[156:159], v224 offset:18336\n\t"
"v_mfma_f32_16x16x32_bf16 v[12:15], a[124:127], a[172:175], v[12:15]\n\t"
"buffer_load_dwordx4 a[116:119], off, s[92:95], s53 offset:1024 sc1\n\t"
"v_mfma_f32_4x4x4_16b_bf16 v[200:203], a[220:221], a[178:179], 0\n\t"
"ds_read_b64_tr_b16 a[178:179], v250 offset:3456\n\t"
"v_mfma_f32_4x4x4_16b_bf16 v[204:207], a[220:221], a[186:187], 0\n\t"
"ds_read_b64_tr_b16 a[186:187], v251 offset:3456\n\t"
"v_mfma_f32_4x4x4_16b_bf16 v[216:219], a[220:221], a[194:195], 0\n\t"
"ds_read_b64_tr_b16 a[194:195], v248 offset:3456\n\t"
"v_mfma_f32_4x4x4_16b_bf16 v[220:223], a[220:221], a[202:203], 0\n\t"
"buffer_load_dwordx4 a[108:111], off, s[96:99], s52 offset:3072 sc1\n\t"
"v_mfma_f32_4x4x4_16b_bf16 v[192:195], a[222:223], a[176:177], v[200:203]\n\t"
"s_waitcnt lgkmcnt(5)\n\t"
"ds_write_b128 v255, a[228:231] offset:20736\n\t"
"v_mfma_f32_4x4x4_16b_bf16 v[196:199], a[222:223], a[184:185], v[204:207]\n\t"
"v_mfma_f32_4x4x4_16b_bf16 v[200:203], a[222:223], a[180:181], v[200:203]\n\t"
"v_mfma_f32_4x4x4_16b_bf16 v[204:207], a[222:223], a[188:189], v[204:207]\n\t"
"v_mfma_f32_4x4x4_16b_bf16 v[208:211], a[222:223], a[192:193], v[216:219]\n\t"
"ds_read_b128 a[172:175], v224 offset:20384\n\t"
"v_mfma_f32_4x4x4_16b_bf16 v[212:215], a[222:223], a[200:201], v[220:223]\n\t"
"ds_read_b64_tr_b16 a[202:203], v249 offset:3456\n\t"
"v_mfma_f32_4x4x4_16b_bf16 v[216:219], a[222:223], a[196:197], v[216:219]\n\t"
"ds_read_b64_tr_b16 a[176:177], v248 offset:1152\n\t"
"v_mfma_f32_4x4x4_16b_bf16 v[220:223], a[222:223], a[204:205], v[220:223]\n\t"
"s_waitcnt vmcnt(21) lgkmcnt(5)\n\t"
"buffer_load_dwordx4 a[104:107], off, s[96:99], s52 offset:2048 sc1\n\t"
"v_mfma_f32_16x16x32_bf16 v[48:51], a[0:3], a[144:147], v[48:51]\n\t"
"s_nop 0\n\t"
"ds_read_b64_tr_b16 a[184:185], v249 offset:1152\n\t"
"v_cvt_pk_bf16_f32 v192, -v192, -v196\n\t"
"v_mfma_f32_16x16x32_bf16 v[48:51], a[4:7], a[160:163], v[48:51]\n\t"
"s_nop 0\n\t"
"ds_write_addtid_b32 v192 offset:0\n\t"
"v_cvt_pk_bf16_f32 v193, -v193, -v197\n\t"
"v_mfma_f32_16x16x32_bf16 v[64:67], a[8:11], a[144:147], v[64:67]\n\t"
"s_nop 0\n\t"
"ds_write_addtid_b32 v193 offset:8320\n\t"
"v_cvt_pk_bf16_f32 v195, -v195, -v199\n\t"
"v_mfma_f32_16x16x32_bf16 v[64:67], a[12:15], a[160:163], v[64:67]\n\t"
"buffer_load_dwordx4 a[96:99], off, s[96:99], s52 offset:0 sc1\n\t"
"v_mfma_f32_16x16x32_bf16 v[80:83], a[16:19], a[144:147], v[80:83]\n\t"
"s_nop 0\n\t"
"ds_read_b128 a[144:147], v224 offset:16544\n\t"
"v_cvt_pk_bf16_f32 v201, v201, v205\n\t"
"v_mfma_f32_16x16x32_bf16 v[80:83], a[20:23], a[160:163], v[80:83]\n\t"
"s_nop 0\n\t"
"ds_write_addtid_b32 v201 offset:12448\n\t"
"v_cvt_pk_bf16_f32 v194, -v194, -v198\n\t"
"v_mfma_f32_16x16x32_bf16 v[84:87], a[16:19], a[148:151], v[84:87]\n\t"
"s_nop 0\n\t"
"ds_read_b128 a[160:163], v224 offset:18592\n\t"
"v_cvt_pk_bf16_f32 v209, -v209, -v213\n\t"
"v_mfma_f32_16x16x32_bf16 v[84:87], a[20:23], a[164:167], v[84:87]\n\t"
"buffer_load_dwordx4 a[100:103], off, s[96:99], s52 offset:1024 sc1\n\t"
"v_mfma_f32_16x16x32_bf16 v[68:71], a[8:11], a[148:151], v[68:71]\n\t"
"s_nop 0\n\t"
"ds_write_addtid_b32 v195 offset:9344\n\t"
"v_cvt_pk_bf16_f32 v211, -v211, -v215\n\t"
"v_mfma_f32_16x16x32_bf16 v[68:71], a[12:15], a[164:167], v[68:71]\n\t"
"s_nop 0\n\t"
"ds_write_addtid_b32 v194 offset:1024\n\t"
"v_cvt_pk_bf16_f32 v210, -v210, -v214\n\t"
"v_mfma_f32_16x16x32_bf16 v[52:55], a[0:3], a[148:151], v[52:55]\n\t"
"s_nop 0\n\t"
"ds_read_b128 a[148:151], v224 offset:16800\n\t"
"v_cvt_pk_bf16_f32 v202, v202, v206\n\t"
"v_mfma_f32_16x16x32_bf16 v[52:55], a[4:7], a[164:167], v[52:55]\n\t"
"buffer_load_dwordx4 a[136:139], off, s[92:95], s52 offset:2048 sc1\n\t"
"v_mfma_f32_16x16x32_bf16 v[56:59], a[0:3], a[152:155], v[56:59]\n\t"
"s_nop 0\n\t"
"ds_read_b128 a[164:167], v224 offset:18848\n\t"
"v_cvt_pk_bf16_f32 v200, v200, v204\n\t"
"v_mfma_f32_16x16x32_bf16 v[56:59], a[4:7], a[168:171], v[56:59]\n\t"
"s_nop 0\n\t"
"ds_write_addtid_b32 v211 offset:11392\n\t"
"v_cvt_pk_bf16_f32 v219, v219, v223\n\t"
"v_mfma_f32_16x16x32_bf16 v[72:75], a[8:11], a[152:155], v[72:75]\n\t"
"s_nop 0\n\t"
"ds_write_addtid_b32 v210 offset:3072\n\t"
"v_cvt_pk_bf16_f32 v217, v217, v221\n\t"
"v_mfma_f32_16x16x32_bf16 v[72:75], a[12:15], a[168:171], v[72:75]\n\t"
"s_nop 0\n\t"
"ds_write_addtid_b32 v200 offset:4128\n\t"
"buffer_load_dwordx4 a[140:143], off, s[92:95], s52 offset:3072 sc1\n\t"
"v_mfma_f32_16x16x32_bf16 v[88:91], a[16:19], a[152:155], v[88:91]\n\t"
"s_nop 0\n\t"
"ds_write_addtid_b32 v202 offset:5152\n\t"
"v_cvt_pk_bf16_f32 v203, v203, v207\n\t"
"v_mfma_f32_16x16x32_bf16 v[88:91], a[20:23], a[168:171], v[88:91]\n\t"
"s_waitcnt lgkmcnt(8)\n\t"
"ds_write_addtid_b32 v209 offset:10368\n\t"
"ds_write_addtid_b32 v217 offset:14496\n\t"
"v_mfma_f32_16x16x32_bf16 v[92:95], a[16:19], a[156:159], v[92:95]\n\t"
"s_nop 0\n\t"
"ds_write_addtid_b32 v203 offset:13472\n\t"
"v_cvt_pk_bf16_f32 v216, v216, v220\n\t"
"v_mfma_f32_16x16x32_bf16 v[92:95], a[20:23], a[172:175], v[92:95]\n\t"
"s_nop 0\n\t"
"ds_write_addtid_b32 v219 offset:15520\n\t"
"v_cvt_pk_bf16_f32 v218, v218, v222\n\t"
"v_mfma_f32_16x16x32_bf16 v[76:79], a[8:11], a[156:159], v[76:79]\n\t"
"s_nop 0\n\t"
"ds_write_addtid_b32 v218 offset:7200\n\t"
"v_cvt_pk_bf16_f32 v208, -v208, -v212\n\t"
"v_mfma_f32_16x16x32_bf16 v[76:79], a[12:15], a[172:175], v[76:79]\n\t"
"s_nop 0\n\t"
"ds_write_addtid_b32 v208 offset:2048\n\t"
"ds_write_addtid_b32 v216 offset:6176\n\t"
"v_mfma_f32_16x16x32_bf16 v[60:63], a[0:3], a[156:159], v[60:63]\n\t"
"s_add_u32 m0, s26, 124544\n\t"
"ds_read_b128 a[152:155], v224 offset:17056\n\t"
"v_mfma_f32_16x16x32_bf16 v[60:63], a[4:7], a[172:175], v[60:63]\n\t"
"s_waitcnt vmcnt(20) lgkmcnt(11)\n\t"
"buffer_load_dwordx4 v236, s[12:15], s22 offen offset:128 nt lds\n\t"
"v_mfma_f32_16x16x32_bf16 v[0:3], a[24:27], a[144:147], v[0:3]\n\t"
"s_nop 0\n\t"
"ds_read_b128 a[156:159], v224 offset:17312\n\t"
"ds_read_b128 a[172:175], v224 offset:19360\n\t"
"v_mfma_f32_16x16x32_bf16 v[0:3], a[28:31], a[160:163], v[0:3]\n\t"
"buffer_load_dwordx4 v237, s[12:15], s22 offen offset:1152 nt lds\n\t"
"v_mfma_f32_16x16x32_bf16 v[16:19], a[32:35], a[144:147], v[16:19]\n\t"
"s_nop 0\n\t"
"ds_read_b128 a[168:171], v224 offset:19104\n\t"
"v_mfma_f32_16x16x32_bf16 v[16:19], a[36:39], a[160:163], v[16:19]\n\t"
"buffer_load_dwordx4 v238, s[12:15], s22 offen offset:2176 nt lds\n\t"
"v_mfma_f32_16x16x32_bf16 v[32:35], a[40:43], a[144:147], v[32:35]\n\t"
"s_nop 0\n\t"
"ds_read_b64_tr_b16 a[180:181], v248 offset:5760\n\t"
"ds_read_b64_tr_b16 a[188:189], v249 offset:5760\n\t"
"v_mfma_f32_16x16x32_bf16 v[32:35], a[44:47], a[160:163], v[32:35]\n\t"
"buffer_load_dwordx4 v239, s[12:15], s22 offen offset:3200 nt lds\n\t"
"v_mfma_f32_16x16x32_bf16 v[36:39], a[40:43], a[148:151], v[36:39]\n\t"
"s_nop 0\n\t"
"ds_read_b64_tr_b16 a[192:193], v250 offset:1152\n\t"
"ds_read_b64_tr_b16 a[200:201], v251 offset:1152\n\t"
"v_mfma_f32_16x16x32_bf16 v[36:39], a[44:47], a[164:167], v[36:39]\n\t"
"buffer_load_dwordx4 a[132:135], off, s[92:95], s52 offset:1024 sc1\n\t"
"v_mfma_f32_16x16x32_bf16 v[20:23], a[32:35], a[148:151], v[20:23]\n\t"
"s_nop 0\n\t"
"ds_read_b64_tr_b16 a[196:197], v250 offset:5760\n\t"
"ds_read_b64_tr_b16 a[204:205], v251 offset:5760\n\t"
"v_mfma_f32_16x16x32_bf16 v[20:23], a[36:39], a[164:167], v[20:23]\n\t"
"s_mov_b64 exec, s[36:37]\n\t"
"buffer_load_dwordx4 v240, s[12:15], s22 offen offset:3712 nt lds\n\t"
"s_mov_b64 exec, -1\n\t"
"v_mfma_f32_16x16x32_bf16 v[4:7], a[24:27], a[148:151], v[4:7]\n\t"
"Lpad5_%=:\n\t"
"s_cbranch_execz Lret0_%=\n\t"
"s_lshl_b32 m0, s73, 8\n\t"
"v_mfma_f32_16x16x32_bf16 v[4:7], a[28:31], a[164:167], v[4:7]\n\t"
"s_waitcnt lgkmcnt(0)\n\t"
"s_barrier\n\t"
"v_mfma_f32_16x16x32_bf16 v[8:11], a[24:27], a[152:155], v[8:11]\n\t"
"ds_read_b128 a[144:147], v224 offset:1024\n\t"
"v_add_u32 v248, 33280, v227\n\t"
"v_mfma_f32_16x16x32_bf16 v[8:11], a[28:31], a[168:171], v[8:11]\n\t"
"ds_read_b128 a[160:163], v224 offset:3072\n\t"
"v_xor_b32 v249, 32, v248\n\t"
"v_mfma_f32_16x16x32_bf16 v[24:27], a[32:35], a[152:155], v[24:27]\n\t"
"ds_read_b128 a[148:151], v224 offset:1280\n\t"
"v_xor_b32 v250, 64, v248\n\t"
"v_mfma_f32_16x16x32_bf16 v[24:27], a[36:39], a[168:171], v[24:27]\n\t"
"ds_read_b128 a[164:167], v224 offset:3328\n\t"
"v_xor_b32 v251, 96, v248\n\t"
"v_mfma_f32_16x16x32_bf16 v[40:43], a[40:43], a[152:155], v[40:43]\n\t"
"ds_read_b64_tr_b16 a[182:183], v249 offset:41984\n\t"
"v_mfma_f32_16x16x32_bf16 v[40:43], a[44:47], a[168:171], v[40:43]\n\t"
"ds_read_b128 a[152:155], v224 offset:1536\n\t"
"v_mfma_f32_16x16x32_bf16 v[44:47], a[40:43], a[156:159], v[44:47]\n\t"
"ds_read_b128 a[168:171], v224 offset:3584\n\t"
"v_mfma_f32_16x16x32_bf16 v[44:47], a[44:47], a[172:175], v[44:47]\n\t"
"ds_read_b64_tr_b16 a[190:191], v251 offset:41984\n\t"
"v_mfma_f32_16x16x32_bf16 v[28:31], a[32:35], a[156:159], v[28:31]\n\t"
"ds_read_b64_tr_b16 a[198:199], v249 offset:21248\n\t"
"v_mfma_f32_16x16x32_bf16 v[28:31], a[36:39], a[172:175], v[28:31]\n\t"
"ds_read_b64_tr_b16 a[206:207], v251 offset:21248\n\t"
"v_mfma_f32_16x16x32_bf16 v[12:15], a[24:27], a[156:159], v[12:15]\n\t"
"ds_read_b128 a[156:159], v224 offset:1792\n\t"
"v_mfma_f32_16x16x32_bf16 v[12:15], a[28:31], a[172:175], v[12:15]\n\t"
"ds_read_b128 a[172:175], v224 offset:3840\n\t"
"v_mfma_f32_4x4x4_16b_bf16 v[200:203], a[220:221], a[178:179], 0\n\t"
"ds_read_b64_tr_b16 a[178:179], v248 offset:41984\n\t"
"v_mfma_f32_4x4x4_16b_bf16 v[204:207], a[220:221], a[186:187], 0\n\t"
"ds_read_b64_tr_b16 a[186:187], v250 offset:41984\n\t"
"v_mfma_f32_4x4x4_16b_bf16 v[216:219], a[220:221], a[194:195], 0\n\t"
"ds_read_b64_tr_b16 a[194:195], v248 offset:21248\n\t"
"v_mfma_f32_4x4x4_16b_bf16 v[220:223], a[220:221], a[202:203], 0\n\t"
"ds_read_b64_tr_b16 a[202:203], v250 offset:21248\n\t"
"v_mfma_f32_4x4x4_16b_bf16 v[192:195], a[222:223], a[176:177], v[200:203]\n\t"
"ds_read_b64_tr_b16 a[176:177], v248 offset:41472\n\t"
"v_mfma_f32_4x4x4_16b_bf16 v[196:199], a[222:223], a[184:185], v[204:207]\n\t"
".p2align 3\n\t"
"ds_read_b64_tr_b16 a[184:185], v250 offset:41472\n\t"
"v_pk_add_f32 v[0:1], v[0:1], v[48:49]\n\t"
"v_pk_add_f32 v[2:3], v[2:3], v[50:51]\n\t"
"v_pk_fma_f32 v[48:49], v[48:49], s[38:39], v[0:1]\n\t"
"v_pk_fma_f32 v[50:51], v[50:51], s[38:39], v[2:3]\n\t"
"v_pk_add_f32 v[16:17], v[16:17], v[64:65]\n\t"
"v_pk_add_f32 v[18:19], v[18:19], v[66:67]\n\t"
"v_pk_fma_f32 v[64:65], v[64:65], s[38:39], v[16:17]\n\t"
"v_pk_fma_f32 v[66:67], v[66:67], s[38:39], v[18:19]\n\t"
"v_pk_add_f32 v[32:33], v[32:33], v[80:81]\n\t"
"v_pk_add_f32 v[34:35], v[34:35], v[82:83]\n\t"
"v_pk_fma_f32 v[80:81], v[80:81], s[38:39], v[32:33]\n\t"
"v_pk_fma_f32 v[82:83], v[82:83], s[38:39], v[34:35]\n\t"
"v_pk_add_f32 v[36:37], v[36:37], v[84:85]\n\t"
"v_pk_add_f32 v[38:39], v[38:39], v[86:87]\n\t"
"v_pk_fma_f32 v[84:85], v[84:85], s[38:39], v[36:37]\n\t"
"v_pk_fma_f32 v[86:87], v[86:87], s[38:39], v[38:39]\n\t"
"v_pk_add_f32 v[20:21], v[20:21], v[68:69]\n\t"
"v_pk_add_f32 v[22:23], v[22:23], v[70:71]\n\t"
"v_pk_fma_f32 v[68:69], v[68:69], s[38:39], v[20:21]\n\t"
"v_pk_fma_f32 v[70:71], v[70:71], s[38:39], v[22:23]\n\t"
"v_pk_add_f32 v[4:5], v[4:5], v[52:53]\n\t"
"v_pk_add_f32 v[6:7], v[6:7], v[54:55]\n\t"
"v_pk_fma_f32 v[52:53], v[52:53], s[38:39], v[4:5]\n\t"
"v_pk_fma_f32 v[54:55], v[54:55], s[38:39], v[6:7]\n\t"
"v_pk_add_f32 v[8:9], v[8:9], v[56:57]\n\t"
"v_pk_add_f32 v[10:11], v[10:11], v[58:59]\n\t"
"v_pk_fma_f32 v[56:57], v[56:57], s[38:39], v[8:9]\n\t"
"v_pk_fma_f32 v[58:59], v[58:59], s[38:39], v[10:11]\n\t"
"v_pk_add_f32 v[24:25], v[24:25], v[72:73]\n\t"
"v_pk_add_f32 v[26:27], v[26:27], v[74:75]\n\t"
"v_pk_fma_f32 v[72:73], v[72:73], s[38:39], v[24:25]\n\t"
"v_pk_fma_f32 v[74:75], v[74:75], s[38:39], v[26:27]\n\t"
"v_pk_add_f32 v[40:41], v[40:41], v[88:89]\n\t"
"v_pk_add_f32 v[42:43], v[42:43], v[90:91]\n\t"
"v_pk_fma_f32 v[88:89], v[88:89], s[38:39], v[40:41]\n\t"
"v_pk_fma_f32 v[90:91], v[90:91], s[38:39], v[42:43]\n\t"
"v_pk_add_f32 v[44:45], v[44:45], v[92:93]\n\t"
"v_pk_add_f32 v[46:47], v[46:47], v[94:95]\n\t"
"v_pk_fma_f32 v[92:93], v[92:93], s[38:39], v[44:45]\n\t"
"v_pk_fma_f32 v[94:95], v[94:95], s[38:39], v[46:47]\n\t"
"v_pk_add_f32 v[28:29], v[28:29], v[76:77]\n\t"
"v_pk_add_f32 v[30:31], v[30:31], v[78:79]\n\t"
"v_pk_fma_f32 v[76:77], v[76:77], s[38:39], v[28:29]\n\t"
"v_pk_fma_f32 v[78:79], v[78:79], s[38:39], v[30:31]\n\t"
"v_pk_add_f32 v[12:13], v[12:13], v[60:61]\n\t"
"v_pk_add_f32 v[14:15], v[14:15], v[62:63]\n\t"
"v_pk_fma_f32 v[60:61], v[60:61], s[38:39], v[12:13]\n\t"
"v_pk_fma_f32 v[62:63], v[62:63], s[38:39], v[14:15]\n\t"
"v_cvt_pk_bf16_f32 v4, v4, v5\n\t"
"v_mfma_f32_4x4x4_16b_bf16 v[200:203], a[222:223], a[180:181], v[200:203]\n\t"
"ds_read_b64_tr_b16 a[180:181], v249 offset:41472\n\t"
"v_mfma_f32_4x4x4_16b_bf16 v[204:207], a[222:223], a[188:189], v[204:207]\n\t"
"ds_read_b64_tr_b16 a[188:189], v251 offset:41472\n\t"
"v_mfma_f32_4x4x4_16b_bf16 v[208:211], a[222:223], a[192:193], v[216:219]\n\t"
"v_cvt_pk_bf16_f32 v0, v0, v1\n\t"
"v_cvt_pk_bf16_f32 v1, v2, v3\n\t"
"v_cvt_pk_bf16_f32 v5, v6, v7\n\t"
"v_cvt_pk_bf16_f32 v24, v24, v25\n\t"
"v_mfma_f32_4x4x4_16b_bf16 v[212:215], a[222:223], a[200:201], v[220:223]\n\t"
"ds_write2st64_b64 v228, v[0:1], v[4:5] offset0:40 offset1:53\n\t"
"v_mfma_f32_4x4x4_16b_bf16 v[216:219], a[222:223], a[196:197], v[216:219]\n\t"
"v_cvt_pk_bf16_f32 v16, v16, v17\n\t"
"v_mfma_f32_4x4x4_16b_bf16 v[220:223], a[222:223], a[204:205], v[220:223]\n\t"
"s_waitcnt vmcnt(20) lgkmcnt(7)\n\t"
"v_cvt_pk_bf16_f32 v17, v18, v19\n\t"
"v_cvt_pk_bf16_f32 v20, v20, v21\n\t"
"v_mfma_f32_16x16x32_bf16 v[144:147], a[48:51], a[144:147], v[144:147]\n\t"
"v_cvt_pk_bf16_f32 v21, v22, v23\n\t"
"v_cvt_pk_bf16_f32 v28, v28, v29\n\t"
"v_mfma_f32_16x16x32_bf16 v[144:147], a[52:55], a[160:163], v[144:147]\n\t"
"ds_write2st64_b64 v246, v[16:17], v[20:21] offset0:40 offset1:53\n\t"
"v_mfma_f32_16x16x32_bf16 v[160:163], a[56:59], a[144:147], v[160:163]\n\t"
"v_cvt_pk_bf16_f32 v32, v32, v33\n\t"
"v_cvt_pk_bf16_f32 v29, v30, v31\n\t"
"v_mfma_f32_16x16x32_bf16 v[160:163], a[60:63], a[160:163], v[160:163]\n\t"
"v_cvt_pk_bf16_f32 v33, v34, v35\n\t"
"v_cvt_pk_bf16_f32 v25, v26, v27\n\t"
"v_mfma_f32_16x16x32_bf16 v[176:179], a[64:67], a[144:147], v[176:179]\n\t"
"v_cvt_pk_bf16_f32 v195, -v195, -v199\n\t"
"v_cvt_pk_bf16_f32 v12, v12, v13\n\t"
"v_mfma_f32_16x16x32_bf16 v[176:179], a[68:71], a[160:163], v[176:179]\n\t"
"s_nop 0\n\t"
"ds_read_b128 a[144:147], v224 offset:0\n\t"
"ds_read_b128 a[160:163], v224 offset:2048\n\t"
"ds_write2st64_b64 v246, v[24:25], v[28:29] offset0:66 offset1:79\n\t"
"v_cvt_pk_bf16_f32 v40, v40, v41\n\t"
"v_mfma_f32_16x16x32_bf16 v[180:183], a[64:67], a[148:151], v[180:183]\n\t"
"v_cvt_pk_bf16_f32 v208, -v208, -v212\n\t"
"v_cvt_pk_bf16_f32 v36, v36, v37\n\t"
"v_mfma_f32_16x16x32_bf16 v[180:183], a[68:71], a[164:167], v[180:183]\n\t"
"v_cvt_pk_bf16_f32 v216, v216, v220\n\t"
"v_cvt_pk_bf16_f32 v37, v38, v39\n\t"
"v_mfma_f32_16x16x32_bf16 v[164:167], a[56:59], a[148:151], v[164:167]\n\t"
"v_cvt_pk_bf16_f32 v8, v8, v9\n\t"
"v_cvt_pk_bf16_f32 v44, v44, v45\n\t"
"v_mfma_f32_16x16x32_bf16 v[164:167], a[60:63], a[164:167], v[164:167]\n\t"
"ds_write_addtid_b32 v195 offset:25888\n\t"
"ds_write2st64_b64 v247, v[32:33], v[36:37] offset0:40 offset1:53\n\t"
"v_mfma_f32_16x16x32_bf16 v[148:151], a[48:51], a[148:151], v[148:151]\n\t"
"v_cvt_pk_bf16_f32 v9, v10, v11\n\t"
"v_cvt_pk_bf16_f32 v45, v46, v47\n\t"
"v_mfma_f32_16x16x32_bf16 v[148:151], a[52:55], a[164:167], v[148:151]\n\t"
"buffer_load_dwordx4 a[128:131], off, s[92:95], s52 offset:0 sc1\n\t"
"v_mfma_f32_16x16x32_bf16 v[152:155], a[48:51], a[152:155], v[152:155]\n\t"
"v_cvt_pk_bf16_f32 v202, v202, v206\n\t"
"v_cvt_pk_bf16_f32 v13, v14, v15\n\t"
"v_mfma_f32_16x16x32_bf16 v[152:155], a[52:55], a[168:171], v[152:155]\n\t"
"s_nop 0\n\t"
"ds_read_b128 a[148:151], v224 offset:256\n\t"
"ds_read_b128 a[164:167], v224 offset:2304\n\t"
"ds_write2st64_b64 v228, v[8:9], v[12:13] offset0:66 offset1:79\n\t"
"v_mfma_f32_16x16x32_bf16 v[168:171], a[56:59], a[152:155], v[168:171]\n\t"
"v_cvt_pk_bf16_f32 v217, v217, v221\n\t"
"v_cvt_pk_bf16_f32 v41, v42, v43\n\t"
"v_mfma_f32_16x16x32_bf16 v[168:171], a[60:63], a[168:171], v[168:171]\n\t"
"v_cvt_pk_bf16_f32 v193, -v193, -v197\n\t"
"v_cvt_pk_bf16_f32 v200, v200, v204\n\t"
"v_mfma_f32_16x16x32_bf16 v[184:187], a[64:67], a[152:155], v[184:187]\n\t"
"v_cvt_pk_bf16_f32 v210, -v210, -v214\n\t"
"v_cvt_pk_bf16_f32 v219, v219, v223\n\t"
"v_mfma_f32_16x16x32_bf16 v[184:187], a[68:71], a[168:171], v[184:187]\n\t"
"ds_write_addtid_b32 v202 offset:21696\n\t"
"ds_write2st64_b64 v247, v[40:41], v[44:45] offset0:66 offset1:79\n\t"
"v_mfma_f32_16x16x32_bf16 v[188:191], a[64:67], a[156:159], v[188:191]\n\t"
"v_cvt_pk_bf16_f32 v201, v201, v205\n\t"
"v_cvt_pk_bf16_f32 v218, v218, v222\n\t"
"v_mfma_f32_16x16x32_bf16 v[188:191], a[68:71], a[172:175], v[188:191]\n\t"
"v_cvt_pk_bf16_f32 v194, -v194, -v198\n\t"
"v_cvt_pk_bf16_f32 v203, v203, v207\n\t"
"v_mfma_f32_16x16x32_bf16 v[172:175], a[56:59], a[156:159], v[172:175]\n\t"
"v_cvt_pk_bf16_f32 v209, -v209, -v213\n\t"
"v_cvt_pk_bf16_f32 v211, -v211, -v215\n\t"
"v_mfma_f32_16x16x32_bf16 v[172:175], a[60:63], a[172:175], v[172:175]\n\t"
"s_nop 0\n\t"
"ds_write_addtid_b32 v193 offset:24864\n\t"
"ds_write_addtid_b32 v201 offset:28992\n\t"
"ds_write_addtid_b32 v203 offset:30016\n\t"
"ds_write_addtid_b32 v209 offset:26912\n\t"
"ds_write_addtid_b32 v210 offset:19616\n\t"
"v_mfma_f32_16x16x32_bf16 v[156:159], a[48:51], a[156:159], v[156:159]\n\t"
"s_nop 0\n\t"
"ds_write_addtid_b32 v208 offset:18592\n\t"
"ds_write_addtid_b32 v216 offset:22720\n\t"
"v_mfma_f32_16x16x32_bf16 v[156:159], a[52:55], a[172:175], v[156:159]\n\t"
"s_waitcnt vmcnt(15) lgkmcnt(9)\n\t"
"ds_write_addtid_b32 v218 offset:23744\n\t"
"v_cvt_pk_bf16_f32 v192, -v192, -v196\n\t"
"v_mfma_f32_16x16x32_bf16 v[96:99], a[72:75], a[144:147], v[96:99]\n\t"
"s_nop 0\n\t"
"ds_write_addtid_b32 v217 offset:31040\n\t"
"ds_write_addtid_b32 v219 offset:32064\n\t"
"v_mfma_f32_16x16x32_bf16 v[96:99], a[76:79], a[160:163], v[96:99]\n\t"
"s_nop 0\n\t"
"ds_write_addtid_b32 v192 offset:16544\n\t"
"ds_write_addtid_b32 v200 offset:20672\n\t"
"v_mfma_f32_16x16x32_bf16 v[112:115], a[80:83], a[144:147], v[112:115]\n\t"
"s_nop 0\n\t"
"ds_write_addtid_b32 v194 offset:17568\n\t"
"ds_write_addtid_b32 v211 offset:27936\n\t"
"v_mfma_f32_16x16x32_bf16 v[112:115], a[84:87], a[160:163], v[112:115]\n\t"
"buffer_load_dwordx4 a[120:123], off, s[96:99], s51 offset:2048 sc1\n\t"
"v_mfma_f32_16x16x32_bf16 v[128:131], a[88:91], a[144:147], v[128:131]\n\t"
"s_nop 0\n\t"
"ds_read_b128 a[168:171], v224 offset:2560\n\t"
"ds_read_b128 a[156:159], v224 offset:768\n\t"
"v_mfma_f32_16x16x32_bf16 v[128:131], a[92:95], a[160:163], v[128:131]\n\t"
"buffer_load_dwordx4 a[124:127], off, s[96:99], s51 offset:3072 sc1\n\t"
"v_mfma_f32_16x16x32_bf16 v[132:135], a[88:91], a[148:151], v[132:135]\n\t"
"s_nop 0\n\t"
"ds_read_b128 a[152:155], v224 offset:512\n\t"
"ds_read_b128 a[172:175], v224 offset:2816\n\t"
"v_mfma_f32_16x16x32_bf16 v[132:135], a[92:95], a[164:167], v[132:135]\n\t"
"s_nop 0\n\t"
"ds_read_b64_tr_b16 a[192:193], v248 offset:20736\n\t"
"ds_read_b64_tr_b16 a[200:201], v250 offset:20736\n\t"
"v_mfma_f32_16x16x32_bf16 v[116:119], a[80:83], a[148:151], v[116:119]\n\t"
"s_nop 0\n\t"
"ds_read_b64_tr_b16 a[196:197], v249 offset:20736\n\t"
"ds_read_b64_tr_b16 a[204:205], v251 offset:20736\n\t"
"v_mfma_f32_16x16x32_bf16 v[116:119], a[84:87], a[164:167], v[116:119]\n\t"
"buffer_load_dwordx4 a[16:19], off, s[28:31], s69 offset:0 sc1\n\t"
"v_mfma_f32_16x16x32_bf16 v[100:103], a[72:75], a[148:151], v[100:103]\n\t"
"s_waitcnt lgkmcnt(4)\n\t"
"v_mfma_f32_16x16x32_bf16 v[100:103], a[76:79], a[164:167], v[100:103]\n\t"
"s_barrier\n\t"
"s_cmp_eq_u32 s21, 1\n\t"
"s_cbranch_scc1 Ltail_%=\n\t"
"v_mfma_f32_16x16x32_bf16 v[104:107], a[72:75], a[152:155], v[104:107]\n\t"
"s_nop 0\n\t"
"ds_read_b128 a[144:147], v224 offset:17568\n\t"
"ds_read_b128 a[160:163], v224 offset:19616\n\t"
"v_mfma_f32_16x16x32_bf16 v[104:107], a[76:79], a[168:171], v[104:107]\n\t"
"buffer_load_dwordx4 a[20:23], off, s[28:31], s69 offset:1024 sc1\n\t"
"v_mfma_f32_16x16x32_bf16 v[120:123], a[80:83], a[152:155], v[120:123]\n\t"
"ds_read_b128 a[148:151], v224 offset:17824\n\t"
"v_mfma_f32_16x16x32_bf16 v[120:123], a[84:87], a[168:171], v[120:123]\n\t"
"ds_read_b128 a[164:167], v224 offset:19872\n\t"
"v_mfma_f32_16x16x32_bf16 v[136:139], a[88:91], a[152:155], v[136:139]\n\t"
"ds_read_b128 a[152:155], v224 offset:18080\n\t"
"v_mfma_f32_16x16x32_bf16 v[136:139], a[92:95], a[168:171], v[136:139]\n\t"
"buffer_load_dwordx4 a[12:15], off, s[84:87], s69 offset:3072 sc1\n\t"
"v_mfma_f32_16x16x32_bf16 v[140:143], a[88:91], a[156:159], v[140:143]\n\t"
"ds_read_b128 a[168:171], v224 offset:20128\n\t"
"v_mfma_f32_16x16x32_bf16 v[140:143], a[92:95], a[172:175], v[140:143]\n\t"
"buffer_load_dwordx4 a[8:11], off, s[84:87], s69 offset:2048 sc1\n\t"
"v_mfma_f32_16x16x32_bf16 v[124:127], a[80:83], a[156:159], v[124:127]\n\t"
"ds_read_b128 v[8:11], v229 offset:20480\n\t"
"v_mfma_f32_16x16x32_bf16 v[124:127], a[84:87], a[172:175], v[124:127]\n\t"
"buffer_load_dwordx4 a[0:3], off, s[84:87], s69 offset:0 sc1\n\t"
"v_mfma_f32_16x16x32_bf16 v[108:111], a[72:75], a[156:159], v[108:111]\n\t"
"ds_read_b128 a[156:159], v224 offset:18336\n\t"
"v_mfma_f32_16x16x32_bf16 v[108:111], a[76:79], a[172:175], v[108:111]\n\t"
"ds_read_b128 a[172:175], v224 offset:20384\n\t"
"v_mfma_f32_16x16x32_bf16 v[192:195], a[224:227], a[176:179], 0\n\t"
"s_nop 0\n\t"
"ds_read_b64_tr_b16 a[176:177], v248 offset:0\n\t"
"ds_read_b64_tr_b16 a[178:179], v248 offset:512\n\t"
"v_mfma_f32_16x16x32_bf16 v[196:199], a[224:227], a[180:183], 0\n\t"
"s_nop 0\n\t"
"ds_read_b64_tr_b16 a[180:181], v249 offset:0\n\t"
"ds_read_b64_tr_b16 a[182:183], v249 offset:512\n\t"
"v_mfma_f32_16x16x32_bf16 v[200:203], a[224:227], a[184:187], 0\n\t"
"buffer_load_dwordx4 a[4:7], off, s[84:87], s69 offset:1024 sc1\n\t"
"v_mfma_f32_16x16x32_bf16 v[204:207], a[224:227], a[188:191], 0\n\t"
"s_waitcnt lgkmcnt(8)\n\t"
"ds_read_b64_tr_b16 a[184:185], v250 offset:0\n\t"
"v_mfma_f32_16x16x32_bf16 v[208:211], a[224:227], a[192:195], 0\n\t"
"s_nop 0\n\t"
"ds_read_b64_tr_b16 a[186:187], v250 offset:512\n\t"
"ds_read_b64_tr_b16 a[188:189], v251 offset:0\n\t"
"v_mfma_f32_16x16x32_bf16 v[212:215], a[224:227], a[196:199], 0\n\t"
"s_nop 0\n\t"
"ds_read_b64_tr_b16 a[190:191], v251 offset:512\n\t"
"ds_read_b64_tr_b16 a[192:193], v248 offset:1152\n\t"
"v_mfma_f32_16x16x32_bf16 v[216:219], a[224:227], a[200:203], 0\n\t"
"s_waitcnt lgkmcnt(11)\n\t"
"buffer_store_dwordx4 v[8:11], v232, s[16:19], s24 offen nt\n\t"
"v_mfma_f32_16x16x32_bf16 v[220:223], a[224:227], a[204:207], 0\n\t"
"s_waitcnt vmcnt(18)\n\t"
"ds_read_b64_tr_b16 a[194:195], v248 offset:1664\n\t"
"v_cvt_pk_bf16_f32 v195, v195, v199\n\t"
"v_mfma_f32_16x16x32_bf16 v[144:147], a[96:99], a[144:147], v[144:147]\n\t"
"ds_read_b64_tr_b16 a[196:197], v249 offset:1152\n\t"
"v_cvt_pk_bf16_f32 v194, v194, v198\n\t"
"v_mfma_f32_16x16x32_bf16 v[144:147], a[100:103], a[160:163], v[144:147]\n\t"
"buffer_load_dwordx4 a[40:43], off, s[28:31], s54 offset:0 sc1\n\t"
"v_mfma_f32_16x16x32_bf16 v[160:163], a[104:107], a[144:147], v[160:163]\n\t"
"s_nop 0\n\t"
"ds_write_addtid_b32 v195 offset:12448\n\t"
"ds_read_b64_tr_b16 a[198:199], v249 offset:1664\n\t"
"v_mfma_f32_16x16x32_bf16 v[160:163], a[108:111], a[160:163], v[160:163]\n\t"
"ds_write_addtid_b32 v194 offset:4128\n\t"
"v_cvt_pk_bf16_f32 v201, v201, v205\n\t"
"v_mfma_f32_16x16x32_bf16 v[176:179], a[112:115], a[144:147], v[176:179]\n\t"
"ds_read_b128 a[144:147], v224 offset:16544\n\t"
"v_cvt_pk_bf16_f32 v211, v211, v215\n\t"
"v_mfma_f32_16x16x32_bf16 v[176:179], a[116:119], a[160:163], v[176:179]\n\t"
"buffer_load_dwordx4 a[44:47], off, s[28:31], s54 offset:1024 sc1\n\t"
"v_mfma_f32_16x16x32_bf16 v[180:183], a[112:115], a[148:151], v[180:183]\n\t"
"ds_read_b128 a[160:163], v224 offset:18592\n\t"
"v_cvt_pk_bf16_f32 v208, v208, v212\n\t"
"v_mfma_f32_16x16x32_bf16 v[180:183], a[116:119], a[164:167], v[180:183]\n\t"
"ds_read_b128 v[24:27], v230 offset:20480\n\t"
"v_cvt_pk_bf16_f32 v202, v202, v206\n\t"
"v_mfma_f32_16x16x32_bf16 v[164:167], a[104:107], a[148:151], v[164:167]\n\t"
"ds_read_b128 v[12:15], v229 offset:33792\n\t"
"v_cvt_pk_bf16_f32 v200, v200, v204\n\t"
"v_mfma_f32_16x16x32_bf16 v[164:167], a[108:111], a[164:167], v[164:167]\n\t"
"buffer_load_dwordx4 a[36:39], off, s[84:87], s54 offset:3072 sc1\n\t"
"v_mfma_f32_16x16x32_bf16 v[148:151], a[96:99], a[148:151], v[148:151]\n\t"
"ds_read_b128 v[28:31], v230 offset:33792\n\t"
"v_cvt_pk_bf16_f32 v216, v216, v220\n\t"
"v_mfma_f32_16x16x32_bf16 v[148:151], a[100:103], a[164:167], v[148:151]\n\t"
"ds_read_b128 a[164:167], v224 offset:18848\n\t"
"v_cvt_pk_bf16_f32 v209, v209, v213\n\t"
"v_mfma_f32_16x16x32_bf16 v[152:155], a[96:99], a[152:155], v[152:155]\n\t"
"ds_read_b128 a[148:151], v224 offset:16800\n\t"
"v_cvt_pk_bf16_f32 v218, v218, v222\n\t"
"v_mfma_f32_16x16x32_bf16 v[152:155], a[100:103], a[168:171], v[152:155]\n\t"
"buffer_load_dwordx4 a[32:35], off, s[84:87], s54 offset:2048 sc1\n\t"
"v_mfma_f32_16x16x32_bf16 v[168:171], a[104:107], a[152:155], v[168:171]\n\t"
"ds_write_addtid_b32 v202 offset:6176\n\t"
"v_cvt_pk_bf16_f32 v203, v203, v207\n\t"
"v_mfma_f32_16x16x32_bf16 v[168:171], a[108:111], a[168:171], v[168:171]\n\t"
"s_nop 0\n\t"
"ds_write_addtid_b32 v200 offset:2048\n\t"
"ds_write_addtid_b32 v203 offset:14496\n\t"
"ds_write_addtid_b32 v211 offset:13472\n\t"
"v_mfma_f32_16x16x32_bf16 v[184:187], a[112:115], a[152:155], v[184:187]\n\t"
"ds_read_b128 a[152:155], v224 offset:17056\n\t"
"v_cvt_pk_bf16_f32 v210, v210, v214\n\t"
"v_mfma_f32_16x16x32_bf16 v[184:187], a[116:119], a[168:171], v[184:187]\n\t"
"s_waitcnt lgkmcnt(9)\n\t"
"buffer_store_dwordx4 v[24:27], v233, s[16:19], s24 offen nt\n\t"
"v_mfma_f32_16x16x32_bf16 v[188:191], a[112:115], a[156:159], v[188:191]\n\t"
"ds_read_b128 a[168:171], v224 offset:19104\n\t"
"v_cvt_pk_bf16_f32 v217, v217, v221\n\t"
"v_mfma_f32_16x16x32_bf16 v[188:191], a[116:119], a[172:175], v[188:191]\n\t"
"ds_read_b128 v[44:47], v231 offset:33792\n\t"
"v_cvt_pk_bf16_f32 v192, v192, v196\n\t"
"v_mfma_f32_16x16x32_bf16 v[172:175], a[104:107], a[156:159], v[172:175]\n\t"
"s_nop 0\n\t"
"ds_write_addtid_b32 v209 offset:9344\n\t"
"ds_write_addtid_b32 v210 offset:5152\n\t"
"v_mfma_f32_16x16x32_bf16 v[172:175], a[108:111], a[172:175], v[172:175]\n\t"
"ds_write_addtid_b32 v216 offset:3072\n\t"
"buffer_load_dwordx4 a[24:27], off, s[84:87], s54 offset:0 sc1\n\t"
"v_mfma_f32_16x16x32_bf16 v[156:159], a[96:99], a[156:159], v[156:159]\n\t"
"ds_read_b128 v[40:43], v231 offset:20480\n\t"
"v_cvt_pk_bf16_f32 v219, v219, v223\n\t"
"v_mfma_f32_16x16x32_bf16 v[156:159], a[100:103], a[172:175], v[156:159]\n\t"
"s_waitcnt vmcnt(13) lgkmcnt(11)\n\t"
"ds_write_addtid_b32 v208 offset:1024\n\t"
"v_cvt_pk_bf16_f32 v193, v193, v197\n\t"
"v_mfma_f32_16x16x32_bf16 v[96:99], a[120:123], a[144:147], v[96:99]\n\t"
"s_nop 0\n\t"
"ds_write_addtid_b32 v217 offset:11392\n\t"
"ds_write_addtid_b32 v218 offset:7200\n\t"
"v_mfma_f32_16x16x32_bf16 v[96:99], a[124:127], a[160:163], v[96:99]\n\t"
"s_nop 0\n\t"
"ds_write_addtid_b32 v193 offset:8320\n\t"
"ds_write_addtid_b32 v201 offset:10368\n\t"
"v_mfma_f32_16x16x32_bf16 v[112:115], a[128:131], a[144:147], v[112:115]\n\t"
"s_nop 0\n\t"
"ds_write_addtid_b32 v192 offset:0\n\t"
"ds_write_addtid_b32 v219 offset:15520\n\t"
"v_mfma_f32_16x16x32_bf16 v[112:115], a[132:135], a[160:163], v[112:115]\n\t"
"buffer_load_dwordx4 a[28:31], off, s[84:87], s54 offset:1024 sc1\n\t"
"v_mfma_f32_16x16x32_bf16 v[128:131], a[136:139], a[144:147], v[128:131]\n\t"
"s_nop 0\n\t"
"ds_read_b128 a[156:159], v224 offset:17312\n\t"
"ds_read_b128 a[172:175], v224 offset:19360\n\t"
"v_mfma_f32_16x16x32_bf16 v[128:131], a[140:143], a[160:163], v[128:131]\n\t"
"buffer_load_dwordx4 a[64:67], off, s[28:31], 0 offset:0 sc1\n\t"
"v_mfma_f32_16x16x32_bf16 v[132:135], a[136:139], a[148:151], v[132:135]\n\t"
"s_nop 0\n\t"
"ds_read_b64_tr_b16 a[200:201], v250 offset:1152\n\t"
"ds_read_b64_tr_b16 a[202:203], v250 offset:1664\n\t"
"v_mfma_f32_16x16x32_bf16 v[132:135], a[140:143], a[164:167], v[132:135]\n\t"
"buffer_load_dwordx4 a[68:71], off, s[28:31], 0 offset:1024 sc1\n\t"
"v_mfma_f32_16x16x32_bf16 v[116:119], a[128:131], a[148:151], v[116:119]\n\t"
"s_nop 0\n\t"
"ds_read_b64_tr_b16 a[204:205], v251 offset:1152\n\t"
"ds_read_b64_tr_b16 a[206:207], v251 offset:1664\n\t"
"v_mfma_f32_16x16x32_bf16 v[116:119], a[132:135], a[164:167], v[116:119]\n\t"
"s_waitcnt lgkmcnt(12)\n\t"
"buffer_store_dwordx4 v[40:43], v234, s[16:19], s24 offen nt\n\t"
"v_mfma_f32_16x16x32_bf16 v[100:103], a[120:123], a[148:151], v[100:103]\n\t"
"v_mfma_f32_16x16x32_bf16 v[100:103], a[124:127], a[164:167], v[100:103]\n\t"
"s_waitcnt lgkmcnt(4)\n\t"
"s_barrier\n\t"
"v_mfma_f32_16x16x32_bf16 v[104:107], a[120:123], a[152:155], v[104:107]\n\t"
"ds_read_b128 a[144:147], v224 offset:0\n\t"
"v_mfma_f32_16x16x32_bf16 v[104:107], a[124:127], a[168:171], v[104:107]\n\t"
"ds_read_b128 a[148:151], v224 offset:256\n\t"
"v_mfma_f32_16x16x32_bf16 v[120:123], a[128:131], a[152:155], v[120:123]\n\t"
"ds_read_b128 a[160:163], v224 offset:2048\n\t"
"v_add_u32 v248, 122368, v227\n\t"
"v_mfma_f32_16x16x32_bf16 v[120:123], a[132:135], a[168:171], v[120:123]\n\t"
"ds_read_b128 a[164:167], v224 offset:2304\n\t"
"v_mfma_f32_16x16x32_bf16 v[136:139], a[136:139], a[152:155], v[136:139]\n\t"
"v_mfma_f32_16x16x32_bf16 v[136:139], a[140:143], a[168:171], v[136:139]\n\t"
"buffer_load_dwordx4 a[60:63], off, s[84:87], 0 offset:3072 sc1\n\t"
"v_mfma_f32_16x16x32_bf16 v[140:143], a[136:139], a[156:159], v[140:143]\n\t"
"ds_read_b128 a[152:155], v224 offset:512\n\t"
"v_xor_b32 v249, 32, v248\n\t"
"v_mfma_f32_16x16x32_bf16 v[140:143], a[140:143], a[172:175], v[140:143]\n\t"
"ds_read_b128 a[168:171], v224 offset:2560\n\t"
"v_xor_b32 v250, 64, v248\n\t"
"v_mfma_f32_16x16x32_bf16 v[124:127], a[128:131], a[156:159], v[124:127]\n\t"
"v_mfma_f32_16x16x32_bf16 v[124:127], a[132:135], a[172:175], v[124:127]\n\t"
"buffer_load_dwordx4 a[56:59], off, s[84:87], 0 offset:2048 sc1\n\t"
"v_mfma_f32_16x16x32_bf16 v[108:111], a[120:123], a[156:159], v[108:111]\n\t"
"ds_read_b128 a[156:159], v224 offset:768\n\t"
"v_mfma_f32_16x16x32_bf16 v[108:111], a[124:127], a[172:175], v[108:111]\n\t"
"ds_read_b128 a[172:175], v224 offset:2816\n\t"
"v_xor_b32 v251, 96, v248\n\t"
"v_mfma_f32_16x16x32_bf16 v[192:195], a[224:227], a[176:179], 0\n\t"
"s_nop 0\n\t"
"ds_read_b64_tr_b16 a[176:177], v248 offset:20736\n\t"
"ds_read_b64_tr_b16 a[178:179], v248 offset:21248\n\t"
"v_mfma_f32_16x16x32_bf16 v[196:199], a[224:227], a[180:183], 0\n\t"
"buffer_load_dwordx4 a[48:51], off, s[84:87], 0 offset:0 sc1\n\t"
"v_mfma_f32_16x16x32_bf16 v[200:203], a[224:227], a[184:187], 0\n\t"
"s_nop 0\n\t"
"ds_read_b64_tr_b16 a[180:181], v249 offset:20736\n\t"
"ds_read_b64_tr_b16 a[182:183], v249 offset:21248\n\t"
"v_mfma_f32_16x16x32_bf16 v[204:207], a[224:227], a[188:191], 0\n\t"
"buffer_load_dwordx4 a[52:55], off, s[84:87], 0 offset:1024 sc1\n\t"
"v_mfma_f32_16x16x32_bf16 v[208:211], a[224:227], a[192:195], 0\n\t"
"s_nop 0\n\t"
"ds_read_b64_tr_b16 a[184:185], v250 offset:20736\n\t"
"ds_read_b64_tr_b16 a[186:187], v250 offset:21248\n\t"
"v_mfma_f32_16x16x32_bf16 v[212:215], a[224:227], a[196:199], 0\n\t"
"s_waitcnt lgkmcnt(8)\n\t"
"ds_read_b64_tr_b16 a[188:189], v251 offset:20736\n\t"
"ds_read_b64_tr_b16 a[190:191], v251 offset:21248\n\t"
"v_mfma_f32_16x16x32_bf16 v[216:219], a[224:227], a[200:203], 0\n\t"
".p2align 3\n\t"
"ds_read_b64_tr_b16 a[192:193], v248 offset:0\n\t"
"buffer_load_dwordx4 a[88:91], off, s[28:31], s51 offset:0 sc1\n\t"
"v_pk_add_f32 v[96:97], v[96:97], v[144:145]\n\t"
"v_pk_add_f32 v[98:99], v[98:99], v[146:147]\n\t"
"v_pk_fma_f32 v[144:145], v[144:145], s[38:39], v[96:97]\n\t"
"v_pk_fma_f32 v[146:147], v[146:147], s[38:39], v[98:99]\n\t"
"v_pk_add_f32 v[112:113], v[112:113], v[160:161]\n\t"
"v_pk_add_f32 v[114:115], v[114:115], v[162:163]\n\t"
"v_pk_fma_f32 v[160:161], v[160:161], s[38:39], v[112:113]\n\t"
"v_pk_fma_f32 v[162:163], v[162:163], s[38:39], v[114:115]\n\t"
"v_pk_add_f32 v[128:129], v[128:129], v[176:177]\n\t"
"v_pk_add_f32 v[130:131], v[130:131], v[178:179]\n\t"
"v_pk_fma_f32 v[176:177], v[176:177], s[38:39], v[128:129]\n\t"
"v_pk_fma_f32 v[178:179], v[178:179], s[38:39], v[130:131]\n\t"
"buffer_load_dwordx4 a[92:95], off, s[28:31], s51 offset:1024 sc1\n\t"
"v_pk_add_f32 v[132:133], v[132:133], v[180:181]\n\t"
"v_pk_add_f32 v[134:135], v[134:135], v[182:183]\n\t"
"v_pk_fma_f32 v[180:181], v[180:181], s[38:39], v[132:133]\n\t"
"v_pk_fma_f32 v[182:183], v[182:183], s[38:39], v[134:135]\n\t"
"v_pk_add_f32 v[116:117], v[116:117], v[164:165]\n\t"
"v_pk_add_f32 v[118:119], v[118:119], v[166:167]\n\t"
"v_pk_fma_f32 v[164:165], v[164:165], s[38:39], v[116:117]\n\t"
"v_pk_fma_f32 v[166:167], v[166:167], s[38:39], v[118:119]\n\t"
"v_pk_add_f32 v[100:101], v[100:101], v[148:149]\n\t"
"v_pk_add_f32 v[102:103], v[102:103], v[150:151]\n\t"
"v_pk_fma_f32 v[148:149], v[148:149], s[38:39], v[100:101]\n\t"
"v_pk_fma_f32 v[150:151], v[150:151], s[38:39], v[102:103]\n\t"
"buffer_load_dwordx4 a[84:87], off, s[84:87], s51 offset:3072 sc1\n\t"
"v_pk_add_f32 v[104:105], v[104:105], v[152:153]\n\t"
"v_pk_add_f32 v[106:107], v[106:107], v[154:155]\n\t"
"v_pk_fma_f32 v[152:153], v[152:153], s[38:39], v[104:105]\n\t"
"v_pk_fma_f32 v[154:155], v[154:155], s[38:39], v[106:107]\n\t"
"v_pk_add_f32 v[120:121], v[120:121], v[168:169]\n\t"
"v_pk_add_f32 v[122:123], v[122:123], v[170:171]\n\t"
"v_pk_fma_f32 v[168:169], v[168:169], s[38:39], v[120:121]\n\t"
"v_pk_fma_f32 v[170:171], v[170:171], s[38:39], v[122:123]\n\t"
"v_pk_add_f32 v[136:137], v[136:137], v[184:185]\n\t"
"v_pk_add_f32 v[138:139], v[138:139], v[186:187]\n\t"
"v_pk_fma_f32 v[184:185], v[184:185], s[38:39], v[136:137]\n\t"
"v_pk_fma_f32 v[186:187], v[186:187], s[38:39], v[138:139]\n\t"
"buffer_load_dwordx4 a[80:83], off, s[84:87], s51 offset:2048 sc1\n\t"
"v_pk_add_f32 v[140:141], v[140:141], v[188:189]\n\t"
"v_pk_add_f32 v[142:143], v[142:143], v[190:191]\n\t"
"v_pk_fma_f32 v[188:189], v[188:189], s[38:39], v[140:141]\n\t"
"v_pk_fma_f32 v[190:191], v[190:191], s[38:39], v[142:143]\n\t"
"v_pk_add_f32 v[124:125], v[124:125], v[172:173]\n\t"
"v_pk_add_f32 v[126:127], v[126:127], v[174:175]\n\t"
"v_pk_fma_f32 v[172:173], v[172:173], s[38:39], v[124:125]\n\t"
"v_pk_fma_f32 v[174:175], v[174:175], s[38:39], v[126:127]\n\t"
"v_pk_add_f32 v[108:109], v[108:109], v[156:157]\n\t"
"v_pk_add_f32 v[110:111], v[110:111], v[158:159]\n\t"
"v_pk_fma_f32 v[156:157], v[156:157], s[38:39], v[108:109]\n\t"
"v_pk_fma_f32 v[158:159], v[158:159], s[38:39], v[110:111]\n\t"
"v_cvt_pk_bf16_f32 v100, v100, v101\n\t"
"v_cvt_pk_bf16_f32 v101, v102, v103\n\t"
"v_cvt_pk_bf16_f32 v108, v108, v109\n\t"
"v_mfma_f32_16x16x32_bf16 v[220:223], a[224:227], a[204:207], 0\n\t"
"s_waitcnt vmcnt(19)\n\t"
"ds_read_b64_tr_b16 a[194:195], v248 offset:512\n\t"
"buffer_store_dwordx4 v[12:15], v232, s[16:19], s27 offen nt\n\t"
"v_cvt_pk_bf16_f32 v193, v193, v197\n\t"
"v_cvt_pk_bf16_f32 v96, v96, v97\n\t"
"v_cvt_pk_bf16_f32 v116, v116, v117\n\t"
"v_cvt_pk_bf16_f32 v132, v132, v133\n\t"
"v_mfma_f32_16x16x32_bf16 v[48:51], a[0:3], a[144:147], v[48:51]\n\t"
"v_cvt_pk_bf16_f32 v97, v98, v99\n\t"
"v_cvt_pk_bf16_f32 v128, v128, v129\n\t"
"v_mfma_f32_16x16x32_bf16 v[48:51], a[4:7], a[160:163], v[48:51]\n\t"
"ds_write_addtid_b32 v193 offset:24864\n\t"
"ds_write2st64_b64 v228, v[96:97], v[100:101] offset0:40 offset1:53\n\t"
"v_cvt_pk_bf16_f32 v117, v118, v119\n\t"
"v_mfma_f32_16x16x32_bf16 v[64:67], a[8:11], a[144:147], v[64:67]\n\t"
"s_nop 0\n\t"
"v_cvt_pk_bf16_f32 v133, v134, v135\n\t"
"v_cvt_pk_bf16_f32 v104, v104, v105\n\t"
"v_mfma_f32_16x16x32_bf16 v[64:67], a[12:15], a[160:163], v[64:67]\n\t"
"buffer_load_dwordx4 a[72:75], off, s[84:87], s51 offset:0 sc1\n\t"
"v_cvt_pk_bf16_f32 v210, v210, v214\n\t"
"v_cvt_pk_bf16_f32 v105, v106, v107\n\t"
"v_mfma_f32_16x16x32_bf16 v[80:83], a[16:19], a[144:147], v[80:83]\n\t"
"s_nop 0\n\t"
"v_cvt_pk_bf16_f32 v112, v112, v113\n\t"
"v_cvt_pk_bf16_f32 v113, v114, v115\n\t"
"v_mfma_f32_16x16x32_bf16 v[80:83], a[20:23], a[160:163], v[80:83]\n\t"
"s_nop 0\n\t"
"ds_read_b128 a[144:147], v224 offset:1024\n\t"
"ds_read_b128 a[160:163], v224 offset:3072\n\t"
"ds_write2st64_b64 v246, v[112:113], v[116:117] offset0:40 offset1:53\n\t"
"v_mfma_f32_16x16x32_bf16 v[84:87], a[16:19], a[148:151], v[84:87]\n\t"
"s_nop 0\n\t"
"v_cvt_pk_bf16_f32 v195, v195, v199\n\t"
"v_cvt_pk_bf16_f32 v218, v218, v222\n\t"
"v_mfma_f32_16x16x32_bf16 v[84:87], a[20:23], a[164:167], v[84:87]\n\t"
"Lpad6_%=:\n\t"
"s_cbranch_execz Lret0_%=\n\t"
"buffer_load_dwordx4 a[76:79], off, s[84:87], s51 offset:1024 sc1\n\t"
"v_mfma_f32_16x16x32_bf16 v[68:71], a[8:11], a[148:151], v[68:71]\n\t"
"v_cvt_pk_bf16_f32 v129, v130, v131\n\t"
"v_cvt_pk_bf16_f32 v109, v110, v111\n\t"
"v_mfma_f32_16x16x32_bf16 v[68:71], a[12:15], a[164:167], v[68:71]\n\t"
"ds_write_addtid_b32 v210 offset:21696\n\t"
"ds_write2st64_b64 v247, v[128:129], v[132:133] offset0:40 offset1:53\n\t"
"v_cvt_pk_bf16_f32 v140, v140, v141\n\t"
"v_mfma_f32_16x16x32_bf16 v[52:55], a[0:3], a[148:151], v[52:55]\n\t"
"v_cvt_pk_bf16_f32 v202, v202, v206\n\t"
"v_cvt_pk_bf16_f32 v124, v124, v125\n\t"
"v_mfma_f32_16x16x32_bf16 v[52:55], a[4:7], a[164:167], v[52:55]\n\t"
"buffer_load_dwordx4 a[112:115], off, s[28:31], s81 offset:0 sc1\n\t"
"v_cvt_pk_bf16_f32 v120, v120, v121\n\t"
"v_mfma_f32_16x16x32_bf16 v[56:59], a[0:3], a[152:155], v[56:59]\n\t"
"v_cvt_pk_bf16_f32 v208, v208, v212\n\t"
"v_cvt_pk_bf16_f32 v219, v219, v223\n\t"
"v_mfma_f32_16x16x32_bf16 v[56:59], a[4:7], a[168:171], v[56:59]\n\t"
"s_nop 0\n\t"
"ds_read_b128 a[148:151], v224 offset:1280\n\t"
"ds_read_b128 a[164:167], v224 offset:3328\n\t"
"ds_write_addtid_b32 v208 offset:17568\n\t"
"ds_write_addtid_b32 v219 offset:32064\n\t"
"v_cvt_pk_bf16_f32 v203, v203, v207\n\t"
"v_mfma_f32_16x16x32_bf16 v[72:75], a[8:11], a[152:155], v[72:75]\n\t"
"v_cvt_pk_bf16_f32 v194, v194, v198\n\t"
"v_cvt_pk_bf16_f32 v201, v201, v205\n\t"
"v_mfma_f32_16x16x32_bf16 v[72:75], a[12:15], a[168:171], v[72:75]\n\t"
"ds_write_addtid_b32 v203 offset:31040\n\t"
"buffer_store_dwordx4 v[28:31], v233, s[16:19], s27 offen nt\n\t"
"ds_write2st64_b64 v228, v[104:105], v[108:109] offset0:66 offset1:79\n\t"
"v_cvt_pk_bf16_f32 v136, v136, v137\n\t"
"v_mfma_f32_16x16x32_bf16 v[88:91], a[16:19], a[152:155], v[88:91]\n\t"
"v_cvt_pk_bf16_f32 v121, v122, v123\n\t"
"v_cvt_pk_bf16_f32 v137, v138, v139\n\t"
"v_mfma_f32_16x16x32_bf16 v[88:91], a[20:23], a[168:171], v[88:91]\n\t"
"s_waitcnt lgkmcnt(8)\n\t"
"v_cvt_pk_bf16_f32 v217, v217, v221\n\t"
"v_cvt_pk_bf16_f32 v125, v126, v127\n\t"
"v_mfma_f32_16x16x32_bf16 v[92:95], a[16:19], a[156:159], v[92:95]\n\t"
"v_cvt_pk_bf16_f32 v200, v200, v204\n\t"
"v_cvt_pk_bf16_f32 v141, v142, v143\n\t"
"v_mfma_f32_16x16x32_bf16 v[92:95], a[20:23], a[172:175], v[92:95]\n\t"
"s_nop 0\n\t"
"ds_write_addtid_b32 v195 offset:28992\n\t"
"ds_write_addtid_b32 v200 offset:18592\n\t"
"ds_write_addtid_b32 v201 offset:26912\n\t"
"ds_write_addtid_b32 v202 offset:22720\n\t"
"ds_write_addtid_b32 v217 offset:27936\n\t"
"ds_write2st64_b64 v246, v[120:121], v[124:125] offset0:66 offset1:79\n\t"
"v_mfma_f32_16x16x32_bf16 v[76:79], a[8:11], a[156:159], v[76:79]\n\t"
"v_cvt_pk_bf16_f32 v209, v209, v213\n\t"
"v_cvt_pk_bf16_f32 v216, v216, v220\n\t"
"v_mfma_f32_16x16x32_bf16 v[76:79], a[12:15], a[172:175], v[76:79]\n\t"
"buffer_load_dwordx4 a[116:119], off, s[28:31], s81 offset:1024 sc1\n\t"
"v_mfma_f32_16x16x32_bf16 v[60:63], a[0:3], a[156:159], v[60:63]\n\t"
"v_cvt_pk_bf16_f32 v192, v192, v196\n\t"
"v_cvt_pk_bf16_f32 v211, v211, v215\n\t"
"v_mfma_f32_16x16x32_bf16 v[60:63], a[4:7], a[172:175], v[60:63]\n\t"
"s_waitcnt vmcnt(17) lgkmcnt(8)\n\t"
"ds_write_addtid_b32 v192 offset:16544\n\t"
"ds_write_addtid_b32 v194 offset:20672\n\t"
"ds_write_addtid_b32 v209 offset:25888\n\t"
"ds_write_addtid_b32 v211 offset:30016\n\t"
"ds_write2st64_b64 v247, v[136:137], v[140:141] offset0:66 offset1:79\n\t"
"v_mfma_f32_16x16x32_bf16 v[48:51], a[24:27], a[144:147], v[48:51]\n\t"
"buffer_load_dwordx4 a[108:111], off, s[84:87], s81 offset:3072 sc1\n\t"
"v_mfma_f32_16x16x32_bf16 v[48:51], a[28:31], a[160:163], v[48:51]\n\t"
"s_nop 0\n\t"
"ds_write_addtid_b32 v216 offset:19616\n\t"
"ds_write_addtid_b32 v218 offset:23744\n\t"
"v_mfma_f32_16x16x32_bf16 v[64:67], a[32:35], a[144:147], v[64:67]\n\t"
"buffer_load_dwordx4 a[104:107], off, s[84:87], s81 offset:2048 sc1\n\t"
"v_mfma_f32_16x16x32_bf16 v[64:67], a[36:39], a[160:163], v[64:67]\n\t"
"s_nop 0\n\t"
"ds_read_b128 a[152:155], v224 offset:1536\n\t"
"ds_read_b128 a[156:159], v224 offset:1792\n\t"
"v_mfma_f32_16x16x32_bf16 v[80:83], a[40:43], a[144:147], v[80:83]\n\t"
"s_nop 0\n\t"
"ds_read_b128 a[168:171], v224 offset:3584\n\t"
"ds_read_b128 a[172:175], v224 offset:3840\n\t"
"v_mfma_f32_16x16x32_bf16 v[80:83], a[44:47], a[160:163], v[80:83]\n\t"
"buffer_load_dwordx4 a[96:99], off, s[84:87], s81 offset:0 sc1\n\t"
"v_mfma_f32_16x16x32_bf16 v[84:87], a[40:43], a[148:151], v[84:87]\n\t"
"s_nop 0\n\t"
"ds_read_b64_tr_b16 a[196:197], v249 offset:0\n\t"
"ds_read_b64_tr_b16 a[198:199], v249 offset:512\n\t"
"v_mfma_f32_16x16x32_bf16 v[84:87], a[44:47], a[164:167], v[84:87]\n\t"
"s_nop 0\n\t"
"ds_read_b64_tr_b16 a[200:201], v250 offset:0\n\t"
"ds_read_b64_tr_b16 a[202:203], v250 offset:512\n\t"
"v_mfma_f32_16x16x32_bf16 v[68:71], a[32:35], a[148:151], v[68:71]\n\t"
"s_nop 0\n\t"
"ds_read_b64_tr_b16 a[204:205], v251 offset:0\n\t"
"ds_read_b64_tr_b16 a[206:207], v251 offset:512\n\t"
"v_mfma_f32_16x16x32_bf16 v[68:71], a[36:39], a[164:167], v[68:71]\n\t"
"buffer_store_dwordx4 v[44:47], v234, s[16:19], s27 offen nt\n\t"
"v_mfma_f32_16x16x32_bf16 v[52:55], a[24:27], a[148:151], v[52:55]\n\t"
"s_waitcnt lgkmcnt(6)\n\t"
"v_mfma_f32_16x16x32_bf16 v[52:55], a[28:31], a[164:167], v[52:55]\n\t"
"buffer_load_dwordx4 a[100:103], off, s[84:87], s81 offset:1024 sc1\n\t"
"s_barrier\n\t"
"v_mfma_f32_16x16x32_bf16 v[56:59], a[24:27], a[152:155], v[56:59]\n\t"
"s_nop 0\n\t"
"ds_read_b128 a[144:147], v224 offset:16544\n\t"
"ds_read_b128 a[164:167], v224 offset:18848\n\t"
"v_mfma_f32_16x16x32_bf16 v[56:59], a[28:31], a[168:171], v[56:59]\n\t"
"buffer_load_dwordx4 a[136:139], off, s[28:31], s66 offset:0 sc1\n\t"
"v_mfma_f32_16x16x32_bf16 v[72:75], a[32:35], a[152:155], v[72:75]\n\t"
"s_nop 0\n\t"
"ds_read_b128 a[160:163], v224 offset:18592\n\t"
"ds_read_b128 a[148:151], v224 offset:16800\n\t"
"v_mfma_f32_16x16x32_bf16 v[72:75], a[36:39], a[168:171], v[72:75]\n\t"
"buffer_load_dwordx4 a[140:143], off, s[28:31], s66 offset:1024 sc1\n\t"
"v_mfma_f32_16x16x32_bf16 v[88:91], a[40:43], a[152:155], v[88:91]\n\t"
"ds_read_b128 v[108:111], v229 offset:33792\n\t"
"v_add_u32 v248, 54016, v227\n\t"
"v_mfma_f32_16x16x32_bf16 v[88:91], a[44:47], a[168:171], v[88:91]\n\t"
"ds_read_b128 v[104:107], v229 offset:20480\n\t"
"v_xor_b32 v249, 32, v248\n\t"
"v_mfma_f32_16x16x32_bf16 v[92:95], a[40:43], a[156:159], v[92:95]\n\t"
"s_nop 0\n\t"
"ds_read_b128 v[136:139], v231 offset:20480\n\t"
"ds_read_b128 a[232:235], v230 offset:33792\n\t"
"v_mfma_f32_16x16x32_bf16 v[92:95], a[44:47], a[172:175], v[92:95]\n\t"
"buffer_load_dwordx4 a[132:135], off, s[84:87], s66 offset:3072 sc1\n\t"
"v_mfma_f32_16x16x32_bf16 v[76:79], a[32:35], a[156:159], v[76:79]\n\t"
"ds_read_b128 a[236:239], v231 offset:33792\n\t"
"v_xor_b32 v250, 64, v248\n\t"
"v_mfma_f32_16x16x32_bf16 v[76:79], a[36:39], a[172:175], v[76:79]\n\t"
"ds_read_b128 a[152:155], v224 offset:17056\n\t"
"v_xor_b32 v251, 96, v248\n\t"
"v_mfma_f32_16x16x32_bf16 v[60:63], a[24:27], a[156:159], v[60:63]\n\t"
"ds_read_b128 v[120:123], v230 offset:20480\n\t"
"v_mfma_f32_16x16x32_bf16 v[60:63], a[28:31], a[172:175], v[60:63]\n\t"
"buffer_load_dwordx4 a[128:131], off, s[84:87], s66 offset:2048 sc1\n\t"
"v_mfma_f32_16x16x32_bf16 v[192:195], a[216:219], a[176:179], 0\n\t"
"s_nop 0\n\t"
"ds_read_b128 a[168:171], v224 offset:19104\n\t"
"ds_read_b64_tr_b16 a[176:177], v248 offset:21888\n\t"
"v_mfma_f32_16x16x32_bf16 v[196:199], a[216:219], a[180:183], 0\n\t"
"ds_read_b64_tr_b16 a[178:179], v248 offset:22400\n\t"
"v_mfma_f32_16x16x32_bf16 v[200:203], a[216:219], a[184:187], 0\n\t"
"s_waitcnt lgkmcnt(8)\n\t"
"buffer_store_dwordx4 v[104:107], v232, s[16:19], s20 offen nt\n\t"
"v_mfma_f32_16x16x32_bf16 v[204:207], a[216:219], a[188:191], 0\n\t"
"s_nop 0\n\t"
"ds_read_b64_tr_b16 a[180:181], v249 offset:21888\n\t"
"ds_read_b64_tr_b16 a[182:183], v249 offset:22400\n\t"
"v_mfma_f32_16x16x32_bf16 v[208:211], a[216:219], a[192:195], 0\n\t"
"s_nop 0\n\t"
"ds_read_b64_tr_b16 a[184:185], v250 offset:21888\n\t"
"ds_read_b64_tr_b16 a[186:187], v250 offset:22400\n\t"
"v_mfma_f32_16x16x32_bf16 v[212:215], a[216:219], a[196:199], 0\n\t"
"s_waitcnt lgkmcnt(7)\n\t"
"s_barrier\n\t"
"v_mfma_f32_16x16x32_bf16 v[216:219], a[216:219], a[200:203], 0\n\t"
"buffer_load_dwordx4 a[120:123], off, s[84:87], s66 offset:0 sc1\n\t"
"v_mfma_f32_16x16x32_bf16 v[220:223], a[216:219], a[204:207], 0\n\t"
"s_waitcnt vmcnt(21)\n\t"
"ds_read_b128 a[156:159], v224 offset:17312\n\t"
"v_cvt_pk_bf16_f32 v195, v195, v199\n\t"
"v_mfma_f32_16x16x32_bf16 v[48:51], a[48:51], a[144:147], v[48:51]\n\t"
"ds_read_b128 a[172:175], v224 offset:19360\n\t"
"v_cvt_pk_bf16_f32 v194, v194, v198\n\t"
"v_mfma_f32_16x16x32_bf16 v[48:51], a[52:55], a[160:163], v[48:51]\n\t"
"ds_read_b64_tr_b16 a[188:189], v251 offset:21888\n\t"
"v_cvt_pk_bf16_f32 v200, v200, v204\n\t"
"v_mfma_f32_16x16x32_bf16 v[64:67], a[56:59], a[144:147], v[64:67]\n\t"
"ds_read_b64_tr_b16 a[190:191], v251 offset:22400\n\t"
"v_cvt_pk_bf16_f32 v203, v203, v207\n\t"
"v_mfma_f32_16x16x32_bf16 v[64:67], a[60:63], a[160:163], v[64:67]\n\t"
"buffer_load_dwordx4 a[124:127], off, s[84:87], s66 offset:1024 sc1\n\t"
"v_mfma_f32_16x16x32_bf16 v[80:83], a[64:67], a[144:147], v[80:83]\n\t"
"ds_read_b128 a[144:147], v224 offset:17568\n\t"
"v_cvt_pk_bf16_f32 v201, v201, v205\n\t"
"v_mfma_f32_16x16x32_bf16 v[80:83], a[68:71], a[160:163], v[80:83]\n\t"
"ds_read_b128 a[160:163], v224 offset:19616\n\t"
"v_cvt_pk_bf16_f32 v208, v208, v212\n\t"
"v_mfma_f32_16x16x32_bf16 v[84:87], a[64:67], a[148:151], v[84:87]\n\t"
"ds_write_addtid_b32 v194 offset:4128\n\t"
"v_cvt_pk_bf16_f32 v219, v219, v223\n\t"
"v_mfma_f32_16x16x32_bf16 v[84:87], a[68:71], a[164:167], v[84:87]\n\t"
"ds_write_addtid_b32 v203 offset:14496\n\t"
"buffer_store_dwordx4 v[120:123], v233, s[16:19], s20 offen nt\n\t"
"v_mfma_f32_16x16x32_bf16 v[68:71], a[56:59], a[148:151], v[68:71]\n\t"
"ds_write_addtid_b32 v195 offset:12448\n\t"
"v_cvt_pk_bf16_f32 v216, v216, v220\n\t"
"v_mfma_f32_16x16x32_bf16 v[68:71], a[60:63], a[164:167], v[68:71]\n\t"
"ds_write_addtid_b32 v208 offset:1024\n\t"
"v_cvt_pk_bf16_f32 v192, v192, v196\n\t"
"v_mfma_f32_16x16x32_bf16 v[52:55], a[48:51], a[148:151], v[52:55]\n\t"
"ds_read_b128 a[148:151], v224 offset:17824\n\t"
"v_cvt_pk_bf16_f32 v210, v210, v214\n\t"
"v_mfma_f32_16x16x32_bf16 v[52:55], a[52:55], a[164:167], v[52:55]\n\t"
"s_waitcnt lgkmcnt(8)\n\t"
"ds_write_addtid_b32 v216 offset:3072\n\t"
"v_cvt_pk_bf16_f32 v202, v202, v206\n\t"
"v_mfma_f32_16x16x32_bf16 v[56:59], a[48:51], a[152:155], v[56:59]\n\t"
"ds_read_b128 a[164:167], v224 offset:19872\n\t"
"v_cvt_pk_bf16_f32 v209, v209, v213\n\t"
"v_mfma_f32_16x16x32_bf16 v[56:59], a[52:55], a[168:171], v[56:59]\n\t"
"ds_write_addtid_b32 v210 offset:5152\n\t"
"v_cvt_pk_bf16_f32 v217, v217, v221\n\t"
"v_mfma_f32_16x16x32_bf16 v[72:75], a[56:59], a[152:155], v[72:75]\n\t"
"s_nop 0\n\t"
"ds_write_addtid_b32 v201 offset:10368\n\t"
"ds_write_addtid_b32 v217 offset:11392\n\t"
"v_mfma_f32_16x16x32_bf16 v[72:75], a[60:63], a[168:171], v[72:75]\n\t"
"buffer_store_dwordx4 v[136:139], v234, s[16:19], s20 offen nt\n\t"
"v_mfma_f32_16x16x32_bf16 v[88:91], a[64:67], a[152:155], v[88:91]\n\t"
"s_nop 0\n\t"
"ds_write_addtid_b32 v202 offset:6176\n\t"
"ds_write_addtid_b32 v209 offset:9344\n\t"
"v_mfma_f32_16x16x32_bf16 v[88:91], a[68:71], a[168:171], v[88:91]\n\t"
"s_nop 0\n\t"
"ds_write_addtid_b32 v200 offset:2048\n\t"
"ds_write_addtid_b32 v219 offset:15520\n\t"
"v_mfma_f32_16x16x32_bf16 v[92:95], a[64:67], a[156:159], v[92:95]\n\t"
"s_add_u32 m0, s26, 103936\n\t"
"ds_read_b128 a[152:155], v224 offset:18080\n\t"
"v_cvt_pk_bf16_f32 v193, v193, v197\n\t"
"v_mfma_f32_16x16x32_bf16 v[92:95], a[68:71], a[172:175], v[92:95]\n\t"
"buffer_load_dwordx4 v236, s[12:15], s22 offen offset:0 nt lds\n\t"
"v_mfma_f32_16x16x32_bf16 v[76:79], a[56:59], a[156:159], v[76:79]\n\t"
"ds_read_b128 a[168:171], v224 offset:20128\n\t"
"v_cvt_pk_bf16_f32 v218, v218, v222\n\t"
"v_mfma_f32_16x16x32_bf16 v[76:79], a[60:63], a[172:175], v[76:79]\n\t"
"buffer_load_dwordx4 v237, s[12:15], s22 offen offset:1024 nt lds\n\t"
"v_mfma_f32_16x16x32_bf16 v[60:63], a[48:51], a[156:159], v[60:63]\n\t"
"s_lshl_b32 m0, s73, 8\n\t"
"ds_read_b128 a[156:159], v224 offset:18336\n\t"
"v_cvt_pk_bf16_f32 v211, v211, v215\n\t"
"v_mfma_f32_16x16x32_bf16 v[60:63], a[52:55], a[172:175], v[60:63]\n\t"
"s_waitcnt vmcnt(19) lgkmcnt(9)\n\t"
"ds_write_addtid_b32 v192 offset:0\n\t"
"ds_write_addtid_b32 v193 offset:8320\n\t"
"v_mfma_f32_16x16x32_bf16 v[144:147], a[72:75], a[144:147], v[144:147]\n\t"
"s_nop 0\n\t"
"ds_write_addtid_b32 v211 offset:13472\n\t"
"ds_write_addtid_b32 v218 offset:7200\n\t"
"v_mfma_f32_16x16x32_bf16 v[144:147], a[76:79], a[160:163], v[144:147]\n\t"
"s_add_u32 m0, s26, 103936\n\t"
"ds_read_b128 a[172:175], v224 offset:20384\n\t"
"v_mfma_f32_16x16x32_bf16 v[160:163], a[80:83], a[144:147], v[160:163]\n\t"
"buffer_load_dwordx4 v238, s[12:15], s22 offen offset:2048 nt lds\n\t"
"v_mfma_f32_16x16x32_bf16 v[160:163], a[84:87], a[160:163], v[160:163]\n\t"
"s_nop 0\n\t"
"ds_read_b64_tr_b16 a[192:193], v248 offset:1152\n\t"
"ds_read_b64_tr_b16 a[194:195], v248 offset:1664\n\t"
"v_mfma_f32_16x16x32_bf16 v[176:179], a[88:91], a[144:147], v[176:179]\n\t"
"s_nop 0\n\t"
"ds_read_b64_tr_b16 a[196:197], v249 offset:1152\n\t"
"ds_read_b64_tr_b16 a[198:199], v249 offset:1664\n\t"
"v_mfma_f32_16x16x32_bf16 v[176:179], a[92:95], a[160:163], v[176:179]\n\t"
"buffer_load_dwordx4 v239, s[12:15], s22 offen offset:3072 nt lds\n\t"
"v_mfma_f32_16x16x32_bf16 v[180:183], a[88:91], a[148:151], v[180:183]\n\t"
"s_nop 0\n\t"
"ds_read_b64_tr_b16 a[200:201], v250 offset:1152\n\t"
"ds_read_b64_tr_b16 a[202:203], v250 offset:1664\n\t"
"v_mfma_f32_16x16x32_bf16 v[180:183], a[92:95], a[164:167], v[180:183]\n\t"
"s_mov_b64 exec, s[36:37]\n\t"
"buffer_load_dwordx4 v240, s[12:15], s22 offen offset:3584 nt lds\n\t"
"s_mov_b64 exec, -1\n\t"
"v_mfma_f32_16x16x32_bf16 v[164:167], a[80:83], a[148:151], v[164:167]\n\t"
"s_lshl_b32 m0, s73, 8\n\t"
"ds_read_b64_tr_b16 a[204:205], v251 offset:1152\n\t"
"ds_read_b64_tr_b16 a[206:207], v251 offset:1664\n\t"
"v_mfma_f32_16x16x32_bf16 v[164:167], a[84:87], a[164:167], v[164:167]\n\t"
"buffer_store_dwordx4 v[108:111], v232, s[16:19], s25 offen nt\n\t"
"v_mfma_f32_16x16x32_bf16 v[148:151], a[72:75], a[148:151], v[148:151]\n\t"
"s_waitcnt lgkmcnt(8)\n\t"
"v_mfma_f32_16x16x32_bf16 v[148:151], a[76:79], a[164:167], v[148:151]\n\t"
"s_barrier\n\t"
"v_mfma_f32_16x16x32_bf16 v[152:155], a[72:75], a[152:155], v[152:155]\n\t"
"ds_read_b128 a[144:147], v224 offset:0\n\t"
"v_mfma_f32_16x16x32_bf16 v[152:155], a[76:79], a[168:171], v[152:155]\n\t"
"ds_read_b128 a[160:163], v224 offset:2048\n\t"
"v_add_u32 v248, 54016, v226\n\t"
"v_mfma_f32_16x16x32_bf16 v[168:171], a[80:83], a[152:155], v[168:171]\n\t"
"ds_read_b128 a[148:151], v224 offset:256\n\t"
"v_mfma_f32_16x16x32_bf16 v[168:171], a[84:87], a[168:171], v[168:171]\n\t"
"ds_read_b128 a[164:167], v224 offset:2304\n\t"
"v_mfma_f32_16x16x32_bf16 v[184:187], a[88:91], a[152:155], v[184:187]\n\t"
"v_mfma_f32_16x16x32_bf16 v[184:187], a[92:95], a[168:171], v[184:187]\n\t"
"ds_read_b128 a[152:155], v224 offset:512\n\t"
"v_xor_b32 v249, 32, v248\n\t"
"v_mfma_f32_16x16x32_bf16 v[188:191], a[88:91], a[156:159], v[188:191]\n\t"
"ds_read_b128 a[228:231], v254 offset:18432\n\t"
"v_mfma_f32_16x16x32_bf16 v[188:191], a[92:95], a[172:175], v[188:191]\n\t"
"ds_read_b128 a[168:171], v224 offset:2560\n\t"
"v_mfma_f32_16x16x32_bf16 v[172:175], a[80:83], a[156:159], v[172:175]\n\t"
"v_mfma_f32_16x16x32_bf16 v[172:175], a[84:87], a[172:175], v[172:175]\n\t"
"v_xor_b32 v250, 64, v248\n\t"
"v_mfma_f32_16x16x32_bf16 v[156:159], a[72:75], a[156:159], v[156:159]\n\t"
"ds_read_b128 a[156:159], v224 offset:768\n\t"
"v_xor_b32 v251, 96, v248\n\t"
"v_mfma_f32_16x16x32_bf16 v[156:159], a[76:79], a[172:175], v[156:159]\n\t"
"ds_read_b128 a[172:175], v224 offset:2816\n\t"
"v_mfma_f32_16x16x32_bf16 v[192:195], a[224:227], a[176:179], 0\n\t"
"ds_read_b64_tr_b16 a[178:179], v248 offset:128\n\t"
"v_mfma_f32_16x16x32_bf16 v[196:199], a[224:227], a[180:183], 0\n\t"
"ds_read_b64_tr_b16 a[176:177], v248 offset:0\n\t"
"v_mfma_f32_16x16x32_bf16 v[200:203], a[224:227], a[184:187], 0\n\t"
"ds_read_b64_tr_b16 a[180:181], v249 offset:256\n\t"
"v_mfma_f32_16x16x32_bf16 v[204:207], a[224:227], a[188:191], 0\n\t"
"s_waitcnt lgkmcnt(7)\n\t"
"v_mfma_f32_16x16x32_bf16 v[208:211], a[224:227], a[192:195], 0\n\t"
"s_waitcnt lgkmcnt(6)\n\t"
"ds_write_b128 v255, a[228:231] offset:0\n\t"
"v_mfma_f32_16x16x32_bf16 v[212:215], a[224:227], a[196:199], 0\n\t"
"v_mfma_f32_16x16x32_bf16 v[216:219], a[224:227], a[200:203], 0\n\t"
"v_mfma_f32_16x16x32_bf16 v[220:223], a[224:227], a[204:207], 0\n\t"
"v_cvt_pk_bf16_f32 v195, v195, v199\n\t"
"v_mfma_f32_16x16x32_bf16 v[0:3], a[0:3], a[144:147], 0\n\t"
"ds_read_b64_tr_b16 a[186:187], v249 offset:128\n\t"
"v_cvt_pk_bf16_f32 v193, v193, v197\n\t"
"v_mfma_f32_16x16x32_bf16 v[0:3], a[4:7], a[160:163], v[0:3]\n\t"
"ds_read_b64_tr_b16 a[184:185], v249 offset:0\n\t"
"v_mfma_f32_16x16x32_bf16 v[16:19], a[8:11], a[144:147], 0\n\t"
"ds_read_b64_tr_b16 a[188:189], v248 offset:256\n\t"
"v_cvt_pk_bf16_f32 v201, v201, v205\n\t"
"v_mfma_f32_16x16x32_bf16 v[16:19], a[12:15], a[160:163], v[16:19]\n\t"
"ds_read_b64_tr_b16 a[194:195], v250 offset:128\n\t"
"v_cvt_pk_bf16_f32 v200, v200, v204\n\t"
"v_mfma_f32_16x16x32_bf16 v[32:35], a[16:19], a[144:147], 0\n\t"
"ds_read_b128 a[144:147], v224 offset:1024\n\t"
"v_mfma_f32_16x16x32_bf16 v[32:35], a[20:23], a[160:163], v[32:35]\n\t"
"ds_write_addtid_b32 v201 offset:26912\n\t"
"v_cvt_pk_bf16_f32 v203, v203, v207\n\t"
"v_mfma_f32_16x16x32_bf16 v[36:39], a[16:19], a[148:151], 0\n\t"
"ds_read_b64_tr_b16 a[192:193], v250 offset:0\n\t"
"v_cvt_pk_bf16_f32 v211, v211, v215\n\t"
"v_mfma_f32_16x16x32_bf16 v[36:39], a[20:23], a[164:167], v[36:39]\n\t"
"ds_write_addtid_b32 v193 offset:24864\n\t"
"v_cvt_pk_bf16_f32 v219, v219, v223\n\t"
"v_mfma_f32_16x16x32_bf16 v[20:23], a[8:11], a[148:151], 0\n\t"
"ds_read_b128 a[160:163], v224 offset:3072\n\t"
"v_cvt_pk_bf16_f32 v209, v209, v213\n\t"
"v_mfma_f32_16x16x32_bf16 v[20:23], a[12:15], a[164:167], v[20:23]\n\t"
"ds_write_addtid_b32 v195 offset:28992\n\t"
"v_mfma_f32_16x16x32_bf16 v[4:7], a[0:3], a[148:151], 0\n\t"
"ds_read_b128 a[148:151], v224 offset:1280\n\t"
"v_cvt_pk_bf16_f32 v216, v216, v220\n\t"
"v_mfma_f32_16x16x32_bf16 v[4:7], a[4:7], a[164:167], v[4:7]\n\t"
"s_waitcnt lgkmcnt(9)\n\t"
"ds_write_addtid_b32 v200 offset:18592\n\t"
"v_cvt_pk_bf16_f32 v202, v202, v206\n\t"
"v_mfma_f32_16x16x32_bf16 v[8:11], a[0:3], a[152:155], 0\n\t"
"ds_read_b128 a[164:167], v224 offset:3328\n\t"
"v_cvt_pk_bf16_f32 v218, v218, v222\n\t"
"v_mfma_f32_16x16x32_bf16 v[8:11], a[4:7], a[168:171], v[8:11]\n\t"
"ds_write_addtid_b32 v219 offset:32064\n\t"
"v_cvt_pk_bf16_f32 v217, v217, v221\n\t"
"v_mfma_f32_16x16x32_bf16 v[24:27], a[8:11], a[152:155], 0\n\t"
"ds_write_addtid_b32 v217 offset:27936\n\t"
"v_cvt_pk_bf16_f32 v194, v194, v198\n\t"
"v_mfma_f32_16x16x32_bf16 v[24:27], a[12:15], a[168:171], v[24:27]\n\t"
"ds_write_addtid_b32 v216 offset:19616\n\t"
"v_cvt_pk_bf16_f32 v210, v210, v214\n\t"
"v_mfma_f32_16x16x32_bf16 v[40:43], a[16:19], a[152:155], 0\n\t"
"ds_read_b128 a[152:155], v224 offset:1536\n\t"
"v_mfma_f32_16x16x32_bf16 v[40:43], a[20:23], a[168:171], v[40:43]\n\t"
"ds_read_b128 a[168:171], v224 offset:3584\n\t"
"v_mfma_f32_16x16x32_bf16 v[44:47], a[16:19], a[156:159], 0\n\t"
"ds_write_addtid_b32 v218 offset:23744\n\t"
"v_mfma_f32_16x16x32_bf16 v[44:47], a[20:23], a[172:175], v[44:47]\n\t"
"ds_write_addtid_b32 v203 offset:31040\n\t"
"v_cvt_pk_bf16_f32 v192, v192, v196\n\t"
"v_mfma_f32_16x16x32_bf16 v[28:31], a[8:11], a[156:159], 0\n\t"
"ds_write_addtid_b32 v210 offset:21696\n\t"
"v_cvt_pk_bf16_f32 v208, v208, v212\n\t"
"v_mfma_f32_16x16x32_bf16 v[28:31], a[12:15], a[172:175], v[28:31]\n\t"
"ds_write_addtid_b32 v208 offset:17568\n\t"
"v_mfma_f32_16x16x32_bf16 v[12:15], a[0:3], a[156:159], 0\n\t"
"ds_write_addtid_b32 v202 offset:22720\n\t"
"v_mfma_f32_16x16x32_bf16 v[12:15], a[4:7], a[172:175], v[12:15]\n\t"
"s_waitcnt lgkmcnt(9)\n\t"
"ds_write_addtid_b32 v192 offset:16544\n\t"
"v_mfma_f32_16x16x32_bf16 v[0:3], a[24:27], a[144:147], v[0:3]\n\t"
"ds_write_addtid_b32 v209 offset:25888\n\t"
"v_mfma_f32_16x16x32_bf16 v[0:3], a[28:31], a[160:163], v[0:3]\n\t"
"ds_write_addtid_b32 v211 offset:30016\n\t"
"v_mfma_f32_16x16x32_bf16 v[16:19], a[32:35], a[144:147], v[16:19]\n\t"
"ds_write_addtid_b32 v194 offset:20672\n\t"
"v_mfma_f32_16x16x32_bf16 v[16:19], a[36:39], a[160:163], v[16:19]\n\t"
"ds_read_b128 a[156:159], v224 offset:1792\n\t"
"v_mfma_f32_16x16x32_bf16 v[32:35], a[40:43], a[144:147], v[32:35]\n\t"
"ds_read_b128 a[172:175], v224 offset:3840\n\t"
"v_mfma_f32_16x16x32_bf16 v[32:35], a[44:47], a[160:163], v[32:35]\n\t"
"ds_read_b64_tr_b16 a[196:197], v251 offset:256\n\t"
"v_mfma_f32_16x16x32_bf16 v[36:39], a[40:43], a[148:151], v[36:39]\n\t"
"ds_read_b64_tr_b16 a[202:203], v251 offset:128\n\t"
"v_mfma_f32_16x16x32_bf16 v[36:39], a[44:47], a[164:167], v[36:39]\n\t"
"ds_read_b64_tr_b16 a[200:201], v251 offset:0\n\t"
"v_mfma_f32_16x16x32_bf16 v[20:23], a[32:35], a[148:151], v[20:23]\n\t"
"ds_read_b64_tr_b16 a[204:205], v250 offset:256\n\t"
"v_mfma_f32_16x16x32_bf16 v[20:23], a[36:39], a[164:167], v[20:23]\n\t"
"v_mfma_f32_16x16x32_bf16 v[4:7], a[24:27], a[148:151], v[4:7]\n\t"
"v_mfma_f32_16x16x32_bf16 v[4:7], a[28:31], a[164:167], v[4:7]\n\t"
"s_waitcnt lgkmcnt(0)\n\t"
"s_barrier\n\t"
"v_mfma_f32_16x16x32_bf16 v[8:11], a[24:27], a[152:155], v[8:11]\n\t"
"ds_read_b128 a[160:163], v224 offset:18592\n\t"
"v_mfma_f32_16x16x32_bf16 v[8:11], a[28:31], a[168:171], v[8:11]\n\t"
"ds_read_b128 a[144:147], v224 offset:16544\n\t"
"v_mfma_f32_16x16x32_bf16 v[24:27], a[32:35], a[152:155], v[24:27]\n\t"
"ds_read_b128 a[148:151], v224 offset:16800\n\t"
"v_swap_b32 v236, v241\n\t"
"v_mfma_f32_16x16x32_bf16 v[24:27], a[36:39], a[168:171], v[24:27]\n\t"
"ds_read_b128 a[164:167], v224 offset:18848\n\t"
"v_mfma_f32_16x16x32_bf16 v[40:43], a[40:43], a[152:155], v[40:43]\n\t"
"v_swap_b32 v237, v242\n\t"
"v_mfma_f32_16x16x32_bf16 v[40:43], a[44:47], a[168:171], v[40:43]\n\t"
"v_swap_b32 v238, v243\n\t"
"v_add_u32 v248, 101632, v227\n\t"
"v_mfma_f32_16x16x32_bf16 v[44:47], a[40:43], a[156:159], v[44:47]\n\t"
"v_swap_b32 v239, v244\n\t"
"v_mfma_f32_16x16x32_bf16 v[44:47], a[44:47], a[172:175], v[44:47]\n\t"
"ds_write_b128 v254, a[244:247] offset:20736\n\t"
"v_mfma_f32_16x16x32_bf16 v[28:31], a[32:35], a[156:159], v[28:31]\n\t"
"v_swap_b32 v240, v245\n\t"
"v_mfma_f32_16x16x32_bf16 v[28:31], a[36:39], a[172:175], v[28:31]\n\t"
"v_mfma_f32_16x16x32_bf16 v[12:15], a[24:27], a[156:159], v[12:15]\n\t"
"v_add_u32 v225, v225, v253\n\t"
"v_mfma_f32_16x16x32_bf16 v[12:15], a[28:31], a[172:175], v[12:15]\n\t"
"ds_read_b128 a[156:159], v224 offset:17312\n\t"
"v_sub_u32 v253, 0, v253\n\t"
"v_mfma_f32_4x4x4_16b_bf16 v[200:203], a[220:221], a[178:179], 0\n\t"
"v_accvgpr_mov_b32 a228, a208\n\t"
"v_mfma_f32_4x4x4_16b_bf16 v[204:207], a[220:221], a[186:187], 0\n\t"
"v_mfma_f32_4x4x4_16b_bf16 v[216:219], a[220:221], a[194:195], 0\n\t"
"v_mfma_f32_4x4x4_16b_bf16 v[220:223], a[220:221], a[202:203], 0\n\t"
"ds_read_b128 a[152:155], v224 offset:17056\n\t"
"v_mfma_f32_4x4x4_16b_bf16 v[192:195], a[222:223], a[176:177], v[200:203]\n\t"
"v_xor_b32 v249, 32, v248\n\t"
"v_mfma_f32_4x4x4_16b_bf16 v[196:199], a[222:223], a[184:185], v[204:207]\n\t"
"v_mfma_f32_4x4x4_16b_bf16 v[200:203], a[222:223], a[180:181], v[200:203]\n\t"
"ds_read_b128 a[168:171], v224 offset:19104\n\t"
"v_mfma_f32_4x4x4_16b_bf16 v[204:207], a[222:223], a[188:189], v[204:207]\n\t"
"v_accvgpr_mov_b32 a208, a209\n\t"
"v_mfma_f32_4x4x4_16b_bf16 v[208:211], a[222:223], a[192:193], v[216:219]\n\t"
"v_xor_b32 v250, 64, v248\n\t"
"v_mfma_f32_4x4x4_16b_bf16 v[212:215], a[222:223], a[200:201], v[220:223]\n\t"
"v_xor_b32 v251, 96, v248\n\t"
"v_mfma_f32_4x4x4_16b_bf16 v[216:219], a[222:223], a[196:197], v[216:219]\n\t"
"ds_read_b128 a[172:175], v224 offset:19360\n\t"
"v_mfma_f32_4x4x4_16b_bf16 v[220:223], a[222:223], a[204:205], v[220:223]\n\t"
"s_waitcnt vmcnt(15) lgkmcnt(3)\n\t"
"buffer_load_dwordx4 a[16:19], off, s[28:31], s57 offset:0 sc1\n\t"
"v_mfma_f32_16x16x32_bf16 v[144:147], a[96:99], a[144:147], v[144:147]\n\t"
"s_nop 0\n\t"
"v_cvt_pk_bf16_f32 v193, -v193, -v197\n\t"
"v_cvt_pk_bf16_f32 v194, -v194, -v198\n\t"
"v_mfma_f32_16x16x32_bf16 v[144:147], a[100:103], a[160:163], v[144:147]\n\t"
"s_nop 0\n\t"
"ds_write_addtid_b32 v193 offset:4128\n\t"
"v_cvt_pk_bf16_f32 v192, -v192, -v196\n\t"
"v_mfma_f32_16x16x32_bf16 v[160:163], a[104:107], a[144:147], v[160:163]\n\t"
"s_nop 0\n\t"
"ds_write_addtid_b32 v194 offset:1024\n\t"
"v_cvt_pk_bf16_f32 v200, v200, v204\n\t"
"v_mfma_f32_16x16x32_bf16 v[160:163], a[108:111], a[160:163], v[160:163]\n\t"
"buffer_load_dwordx4 a[20:23], off, s[28:31], s57 offset:1024 sc1\n\t"
"v_mfma_f32_16x16x32_bf16 v[176:179], a[112:115], a[144:147], v[176:179]\n\t"
"s_nop 0\n\t"
"ds_read_b128 a[144:147], v224 offset:17568\n\t"
"v_cvt_pk_bf16_f32 v211, -v211, -v215\n\t"
"v_mfma_f32_16x16x32_bf16 v[176:179], a[116:119], a[160:163], v[176:179]\n\t"
"buffer_load_dwordx4 a[12:15], off, s[84:87], s57 offset:3072 sc1\n\t"
"v_mfma_f32_16x16x32_bf16 v[180:183], a[112:115], a[148:151], v[180:183]\n\t"
"s_nop 0\n\t"
"ds_read_b128 a[160:163], v224 offset:19616\n\t"
"v_cvt_pk_bf16_f32 v208, -v208, -v212\n\t"
"v_mfma_f32_16x16x32_bf16 v[180:183], a[116:119], a[164:167], v[180:183]\n\t"
"buffer_load_dwordx4 a[8:11], off, s[84:87], s57 offset:2048 sc1\n\t"
"v_mfma_f32_16x16x32_bf16 v[164:167], a[104:107], a[148:151], v[164:167]\n\t"
"s_nop 0\n\t"
"ds_write_addtid_b32 v211 offset:7200\n\t"
"v_cvt_pk_bf16_f32 v201, v201, v205\n\t"
"v_mfma_f32_16x16x32_bf16 v[164:167], a[108:111], a[164:167], v[164:167]\n\t"
"s_nop 0\n\t"
"ds_write_addtid_b32 v208 offset:2048\n\t"
"v_cvt_pk_bf16_f32 v209, -v209, -v213\n\t"
"v_mfma_f32_16x16x32_bf16 v[148:151], a[96:99], a[148:151], v[148:151]\n\t"
"s_nop 0\n\t"
"ds_read_b128 a[148:151], v224 offset:17824\n\t"
"v_cvt_pk_bf16_f32 v218, v218, v222\n\t"
"v_mfma_f32_16x16x32_bf16 v[148:151], a[100:103], a[164:167], v[148:151]\n\t"
"s_waitcnt lgkmcnt(6)\n\t"
"buffer_load_dwordx4 a[0:3], off, s[84:87], s57 offset:0 sc1\n\t"
"v_mfma_f32_16x16x32_bf16 v[152:155], a[96:99], a[152:155], v[152:155]\n\t"
"s_nop 0\n\t"
"ds_read_b128 a[164:167], v224 offset:19872\n\t"
"v_cvt_pk_bf16_f32 v203, v203, v207\n\t"
"v_mfma_f32_16x16x32_bf16 v[152:155], a[100:103], a[168:171], v[152:155]\n\t"
"s_nop 0\n\t"
"ds_write_addtid_b32 v218 offset:11392\n\t"
"v_cvt_pk_bf16_f32 v219, v219, v223\n\t"
"v_mfma_f32_16x16x32_bf16 v[168:171], a[104:107], a[152:155], v[168:171]\n\t"
"s_nop 0\n\t"
"ds_write_addtid_b32 v200 offset:8320\n\t"
"v_cvt_pk_bf16_f32 v216, v216, v220\n\t"
"v_mfma_f32_16x16x32_bf16 v[168:171], a[108:111], a[168:171], v[168:171]\n\t"
"buffer_load_dwordx4 a[4:7], off, s[84:87], s57 offset:1024 sc1\n\t"
"v_mfma_f32_16x16x32_bf16 v[184:187], a[112:115], a[152:155], v[184:187]\n\t"
"s_nop 0\n\t"
"ds_read_b128 a[152:155], v224 offset:18080\n\t"
"v_cvt_pk_bf16_f32 v195, -v195, -v199\n\t"
"v_mfma_f32_16x16x32_bf16 v[184:187], a[116:119], a[168:171], v[184:187]\n\t"
"s_nop 0\n\t"
"ds_read_b128 a[168:171], v224 offset:20128\n\t"
"v_cvt_pk_bf16_f32 v217, v217, v221\n\t"
"v_mfma_f32_16x16x32_bf16 v[188:191], a[112:115], a[156:159], v[188:191]\n\t"
"s_nop 0\n\t"
"ds_write_addtid_b32 v219 offset:15520\n\t"
"v_cvt_pk_bf16_f32 v202, v202, v206\n\t"
"v_mfma_f32_16x16x32_bf16 v[188:191], a[116:119], a[172:175], v[188:191]\n\t"
"buffer_load_dwordx4 a[40:43], off, s[28:31], s63 offset:0 sc1\n\t"
"v_mfma_f32_16x16x32_bf16 v[172:175], a[104:107], a[156:159], v[172:175]\n\t"
"s_nop 0\n\t"
"ds_write_addtid_b32 v209 offset:6176\n\t"
"v_cvt_pk_bf16_f32 v210, -v210, -v214\n\t"
"v_mfma_f32_16x16x32_bf16 v[172:175], a[108:111], a[172:175], v[172:175]\n\t"
"s_nop 0\n\t"
"ds_write_addtid_b32 v202 offset:9344\n\t"
"ds_write_addtid_b32 v216 offset:10368\n\t"
"v_mfma_f32_16x16x32_bf16 v[156:159], a[96:99], a[156:159], v[156:159]\n\t"
"s_nop 0\n\t"
"ds_write_addtid_b32 v203 offset:13472\n\t"
"v_mfma_f32_16x16x32_bf16 v[156:159], a[100:103], a[172:175], v[156:159]\n\t"
"s_waitcnt vmcnt(15) lgkmcnt(8)\n\t"
"buffer_load_dwordx4 a[44:47], off, s[28:31], s63 offset:1024 sc1\n\t"
"v_mfma_f32_16x16x32_bf16 v[144:147], a[120:123], a[144:147], v[144:147]\n\t"
"s_nop 0\n\t"
"ds_write_addtid_b32 v201 offset:12448\n\t"
"ds_write_addtid_b32 v210 offset:3072\n\t"
"v_mfma_f32_16x16x32_bf16 v[144:147], a[124:127], a[160:163], v[144:147]\n\t"
"s_nop 0\n\t"
"ds_write_addtid_b32 v217 offset:14496\n\t"
"v_mfma_f32_16x16x32_bf16 v[160:163], a[128:131], a[144:147], v[160:163]\n\t"
"s_nop 0\n\t"
"ds_write_addtid_b32 v192 offset:0\n\t"
"ds_write_addtid_b32 v195 offset:5152\n\t"
"v_mfma_f32_16x16x32_bf16 v[160:163], a[132:135], a[160:163], v[160:163]\n\t"
"buffer_load_dwordx4 a[36:39], off, s[84:87], s63 offset:3072 sc1\n\t"
"v_mfma_f32_16x16x32_bf16 v[176:179], a[136:139], a[144:147], v[176:179]\n\t"
"s_nop 0\n\t"
"ds_read_b128 a[156:159], v224 offset:18336\n\t"
"v_mfma_f32_16x16x32_bf16 v[176:179], a[140:143], a[160:163], v[176:179]\n\t"
"buffer_load_dwordx4 a[32:35], off, s[84:87], s63 offset:2048 sc1\n\t"
"v_mfma_f32_16x16x32_bf16 v[180:183], a[136:139], a[148:151], v[180:183]\n\t"
"s_nop 0\n\t"
"ds_read_b128 a[172:175], v224 offset:20384\n\t"
"v_mfma_f32_16x16x32_bf16 v[180:183], a[140:143], a[164:167], v[180:183]\n\t"
"buffer_load_dwordx4 a[24:27], off, s[84:87], s63 offset:0 sc1\n\t"
"v_mfma_f32_16x16x32_bf16 v[164:167], a[128:131], a[148:151], v[164:167]\n\t"
"Lpad7_%=:\n\t"
"s_cbranch_execz Lret0_%=\n\t"
"v_mfma_f32_16x16x32_bf16 v[164:167], a[132:135], a[164:167], v[164:167]\n\t"
"buffer_load_dwordx4 a[28:31], off, s[84:87], s63 offset:1024 sc1\n\t"
"v_mfma_f32_16x16x32_bf16 v[148:151], a[120:123], a[148:151], v[148:151]\n\t"
"v_accvgpr_mov_b32 a209, a228\n\t"
"v_mfma_f32_16x16x32_bf16 v[148:151], a[124:127], a[164:167], v[148:151]\n\t"
"s_waitcnt vmcnt(13) lgkmcnt(4)\n\t"
"s_waitcnt lgkmcnt(0)\n\t"
"s_barrier\n\t"
"v_mfma_f32_16x16x32_bf16 v[152:155], a[120:123], a[152:155], v[152:155]\n\t"
"s_add_u32 m0, s26, 56192\n\t"
"ds_read_b128 a[144:147], v224 offset:0\n\t"
"ds_read_b128 a[160:163], v224 offset:2048\n\t"
"v_mfma_f32_16x16x32_bf16 v[152:155], a[124:127], a[168:171], v[152:155]\n\t"
"buffer_load_dwordx4 v236, s[12:15], s22 offen offset:128 nt lds\n\t"
"v_mfma_f32_16x16x32_bf16 v[168:171], a[128:131], a[152:155], v[168:171]\n\t"
"ds_read_b128 a[148:151], v224 offset:256\n\t"
"v_mfma_f32_16x16x32_bf16 v[168:171], a[132:135], a[168:171], v[168:171]\n\t"
"ds_read_b128 a[164:167], v224 offset:2304\n\t"
"v_mfma_f32_16x16x32_bf16 v[184:187], a[136:139], a[152:155], v[184:187]\n\t"
"s_nop 0\n\t"
"ds_read_b64_tr_b16 a[176:177], v248 offset:0\n\t"
"ds_read_b64_tr_b16 a[178:179], v248 offset:512\n\t"
"v_mfma_f32_16x16x32_bf16 v[184:187], a[140:143], a[168:171], v[184:187]\n\t"
"buffer_store_dwordx4 a[232:235], v233, s[16:19], s25 offen nt\n\t"
"v_mfma_f32_16x16x32_bf16 v[188:191], a[136:139], a[156:159], v[188:191]\n\t"
"s_nop 0\n\t"
"ds_read_b128 a[152:155], v224 offset:512\n\t"
"ds_read_b64_tr_b16 a[180:181], v249 offset:0\n\t"
"v_mfma_f32_16x16x32_bf16 v[188:191], a[140:143], a[172:175], v[188:191]\n\t"
"ds_read_b64_tr_b16 a[182:183], v249 offset:512\n\t"
"v_mfma_f32_16x16x32_bf16 v[172:175], a[128:131], a[156:159], v[172:175]\n\t"
"buffer_load_dwordx4 v237, s[12:15], s22 offen offset:1152 nt lds\n\t"
"v_mfma_f32_16x16x32_bf16 v[172:175], a[132:135], a[172:175], v[172:175]\n\t"
"s_nop 0\n\t"
"ds_read_b64_tr_b16 a[184:185], v250 offset:0\n\t"
"ds_read_b64_tr_b16 a[186:187], v250 offset:512\n\t"
"v_mfma_f32_16x16x32_bf16 v[156:159], a[120:123], a[156:159], v[156:159]\n\t"
"s_nop 0\n\t"
"ds_read_b128 a[168:171], v224 offset:2560\n\t"
"ds_read_b128 a[156:159], v224 offset:768\n\t"
"v_mfma_f32_16x16x32_bf16 v[156:159], a[124:127], a[172:175], v[156:159]\n\t"
"s_waitcnt vmcnt(9) lgkmcnt(11)\n\t"
".p2align 3\n\t"
"buffer_store_dwordx4 a[236:239], v234, s[16:19], s25 offen nt\n\t"
"v_pk_add_f32 v[144:145], v[48:49], v[144:145] neg_lo:[0,1] neg_hi:[0,1]\n\t"
"v_pk_add_f32 v[146:147], v[50:51], v[146:147] neg_lo:[0,1] neg_hi:[0,1]\n\t"
"v_pk_fma_f32 v[48:49], v[144:145], s[90:91], v[48:49] neg_lo:[1,0,0] neg_hi:[1,0,0]\n\t"
"v_pk_fma_f32 v[50:51], v[146:147], s[90:91], v[50:51] neg_lo:[1,0,0] neg_hi:[1,0,0]\n\t"
"v_pk_add_f32 v[160:161], v[64:65], v[160:161] neg_lo:[0,1] neg_hi:[0,1]\n\t"
"v_pk_add_f32 v[162:163], v[66:67], v[162:163] neg_lo:[0,1] neg_hi:[0,1]\n\t"
"v_pk_fma_f32 v[64:65], v[160:161], s[90:91], v[64:65] neg_lo:[1,0,0] neg_hi:[1,0,0]\n\t"
"v_pk_fma_f32 v[66:67], v[162:163], s[90:91], v[66:67] neg_lo:[1,0,0] neg_hi:[1,0,0]\n\t"
"v_pk_add_f32 v[176:177], v[80:81], v[176:177] neg_lo:[0,1] neg_hi:[0,1]\n\t"
"v_pk_add_f32 v[178:179], v[82:83], v[178:179] neg_lo:[0,1] neg_hi:[0,1]\n\t"
"v_pk_fma_f32 v[80:81], v[176:177], s[90:91], v[80:81] neg_lo:[1,0,0] neg_hi:[1,0,0]\n\t"
"v_pk_fma_f32 v[82:83], v[178:179], s[90:91], v[82:83] neg_lo:[1,0,0] neg_hi:[1,0,0]\n\t"
"v_pk_add_f32 v[180:181], v[84:85], v[180:181] neg_lo:[0,1] neg_hi:[0,1]\n\t"
"v_pk_add_f32 v[182:183], v[86:87], v[182:183] neg_lo:[0,1] neg_hi:[0,1]\n\t"
"v_pk_fma_f32 v[84:85], v[180:181], s[90:91], v[84:85] neg_lo:[1,0,0] neg_hi:[1,0,0]\n\t"
"v_pk_fma_f32 v[86:87], v[182:183], s[90:91], v[86:87] neg_lo:[1,0,0] neg_hi:[1,0,0]\n\t"
"v_pk_add_f32 v[164:165], v[68:69], v[164:165] neg_lo:[0,1] neg_hi:[0,1]\n\t"
"v_pk_add_f32 v[166:167], v[70:71], v[166:167] neg_lo:[0,1] neg_hi:[0,1]\n\t"
"v_pk_fma_f32 v[68:69], v[164:165], s[90:91], v[68:69] neg_lo:[1,0,0] neg_hi:[1,0,0]\n\t"
"v_pk_fma_f32 v[70:71], v[166:167], s[90:91], v[70:71] neg_lo:[1,0,0] neg_hi:[1,0,0]\n\t"
"v_pk_add_f32 v[148:149], v[52:53], v[148:149] neg_lo:[0,1] neg_hi:[0,1]\n\t"
"v_pk_add_f32 v[150:151], v[54:55], v[150:151] neg_lo:[0,1] neg_hi:[0,1]\n\t"
"v_pk_fma_f32 v[52:53], v[148:149], s[90:91], v[52:53] neg_lo:[1,0,0] neg_hi:[1,0,0]\n\t"
"v_pk_fma_f32 v[54:55], v[150:151], s[90:91], v[54:55] neg_lo:[1,0,0] neg_hi:[1,0,0]\n\t"
"v_pk_add_f32 v[152:153], v[56:57], v[152:153] neg_lo:[0,1] neg_hi:[0,1]\n\t"
"v_pk_add_f32 v[154:155], v[58:59], v[154:155] neg_lo:[0,1] neg_hi:[0,1]\n\t"
"v_pk_fma_f32 v[56:57], v[152:153], s[90:91], v[56:57] neg_lo:[1,0,0] neg_hi:[1,0,0]\n\t"
"v_pk_fma_f32 v[58:59], v[154:155], s[90:91], v[58:59] neg_lo:[1,0,0] neg_hi:[1,0,0]\n\t"
"v_pk_add_f32 v[168:169], v[72:73], v[168:169] neg_lo:[0,1] neg_hi:[0,1]\n\t"
"v_pk_add_f32 v[170:171], v[74:75], v[170:171] neg_lo:[0,1] neg_hi:[0,1]\n\t"
"v_pk_fma_f32 v[72:73], v[168:169], s[90:91], v[72:73] neg_lo:[1,0,0] neg_hi:[1,0,0]\n\t"
"v_pk_fma_f32 v[74:75], v[170:171], s[90:91], v[74:75] neg_lo:[1,0,0] neg_hi:[1,0,0]\n\t"
"v_pk_add_f32 v[184:185], v[88:89], v[184:185] neg_lo:[0,1] neg_hi:[0,1]\n\t"
"v_pk_add_f32 v[186:187], v[90:91], v[186:187] neg_lo:[0,1] neg_hi:[0,1]\n\t"
"v_pk_fma_f32 v[88:89], v[184:185], s[90:91], v[88:89] neg_lo:[1,0,0] neg_hi:[1,0,0]\n\t"
"v_pk_fma_f32 v[90:91], v[186:187], s[90:91], v[90:91] neg_lo:[1,0,0] neg_hi:[1,0,0]\n\t"
"v_pk_add_f32 v[188:189], v[92:93], v[188:189] neg_lo:[0,1] neg_hi:[0,1]\n\t"
"v_pk_add_f32 v[190:191], v[94:95], v[190:191] neg_lo:[0,1] neg_hi:[0,1]\n\t"
"v_pk_fma_f32 v[92:93], v[188:189], s[90:91], v[92:93] neg_lo:[1,0,0] neg_hi:[1,0,0]\n\t"
"v_pk_fma_f32 v[94:95], v[190:191], s[90:91], v[94:95] neg_lo:[1,0,0] neg_hi:[1,0,0]\n\t"
"v_pk_add_f32 v[172:173], v[76:77], v[172:173] neg_lo:[0,1] neg_hi:[0,1]\n\t"
"v_pk_add_f32 v[174:175], v[78:79], v[174:175] neg_lo:[0,1] neg_hi:[0,1]\n\t"
"v_pk_fma_f32 v[76:77], v[172:173], s[90:91], v[76:77] neg_lo:[1,0,0] neg_hi:[1,0,0]\n\t"
"v_pk_fma_f32 v[78:79], v[174:175], s[90:91], v[78:79] neg_lo:[1,0,0] neg_hi:[1,0,0]\n\t"
"v_pk_add_f32 v[156:157], v[60:61], v[156:157] neg_lo:[0,1] neg_hi:[0,1]\n\t"
"v_pk_add_f32 v[158:159], v[62:63], v[158:159] neg_lo:[0,1] neg_hi:[0,1]\n\t"
"v_pk_fma_f32 v[60:61], v[156:157], s[90:91], v[60:61] neg_lo:[1,0,0] neg_hi:[1,0,0]\n\t"
"v_pk_fma_f32 v[62:63], v[158:159], s[90:91], v[62:63] neg_lo:[1,0,0] neg_hi:[1,0,0]\n\t"
"v_mfma_f32_16x16x32_bf16 v[48:51], a[0:3], a[144:147], v[48:51]\n\t"
"s_nop 0\n\t"
"ds_read_b128 a[172:175], v224 offset:2816\n\t"
"ds_read_b64_tr_b16 a[188:189], v251 offset:0\n\t"
"v_mfma_f32_16x16x32_bf16 v[48:51], a[4:7], a[160:163], v[48:51]\n\t"
"s_nop 0\n\t"
"ds_read_b64_tr_b16 a[190:191], v251 offset:512\n\t"
"ds_read_b64_tr_b16 a[192:193], v248 offset:1152\n\t"
"v_mfma_f32_16x16x32_bf16 v[64:67], a[8:11], a[144:147], v[64:67]\n\t"
"ds_read_b64_tr_b16 a[194:195], v248 offset:1664\n\t"
"v_mfma_f32_16x16x32_bf16 v[64:67], a[12:15], a[160:163], v[64:67]\n\t"
"buffer_load_dwordx4 v238, s[12:15], s22 offen offset:2176 nt lds\n\t"
"v_mfma_f32_16x16x32_bf16 v[80:83], a[16:19], a[144:147], v[80:83]\n\t"
"s_nop 0\n\t"
"ds_read_b64_tr_b16 a[196:197], v249 offset:1152\n\t"
"ds_read_b64_tr_b16 a[198:199], v249 offset:1664\n\t"
"v_mfma_f32_16x16x32_bf16 v[80:83], a[20:23], a[160:163], v[80:83]\n\t"
"s_waitcnt lgkmcnt(11)\n\t"
"ds_read_b128 a[160:163], v224 offset:3072\n\t"
"v_mfma_f32_16x16x32_bf16 v[192:195], a[216:219], a[176:179], 0\n\t"
"ds_read_b64_tr_b16 a[200:201], v250 offset:1152\n\t"
"v_mfma_f32_16x16x32_bf16 v[196:199], a[216:219], a[180:183], 0\n\t"
"s_waitcnt lgkmcnt(11)\n\t"
"ds_read_b64_tr_b16 a[202:203], v250 offset:1664\n\t"
"v_mfma_f32_16x16x32_bf16 v[200:203], a[216:219], a[184:187], 0\n\t"
"s_waitcnt lgkmcnt(7)\n\t"
"ds_read_b64_tr_b16 a[204:205], v251 offset:1152\n\t"
"ds_read_b64_tr_b16 a[206:207], v251 offset:1664\n\t"
"v_mfma_f32_16x16x32_bf16 v[204:207], a[216:219], a[188:191], 0\n\t"
"s_waitcnt lgkmcnt(7)\n\t"
"buffer_load_dwordx4 v239, s[12:15], s22 offen offset:3200 nt lds\n\t"
"v_mfma_f32_16x16x32_bf16 v[208:211], a[216:219], a[192:195], 0\n\t"
"s_waitcnt lgkmcnt(5)\n\t"
"ds_read_b64_tr_b16 a[176:177], v248 offset:42624\n\t"
"ds_read_b64_tr_b16 a[178:179], v248 offset:43136\n\t"
"v_mfma_f32_16x16x32_bf16 v[212:215], a[216:219], a[196:199], 0\n\t"
"s_waitcnt lgkmcnt(4)\n\t"
"ds_read_b64_tr_b16 a[180:181], v249 offset:42624\n\t"
"v_mfma_f32_16x16x32_bf16 v[216:219], a[216:219], a[200:203], 0\n\t"
"s_waitcnt lgkmcnt(3)\n\t"
"ds_read_b64_tr_b16 a[182:183], v249 offset:43136\n\t"
"ds_read_b64_tr_b16 a[184:185], v250 offset:42624\n\t"
"v_mfma_f32_16x16x32_bf16 v[220:223], a[216:219], a[204:207], 0\n\t"
"s_mov_b64 exec, s[36:37]\n\t"
"buffer_load_dwordx4 v240, s[12:15], s22 offen offset:3712 nt lds\n\t"
"s_mov_b64 exec, -1\n\t"
"v_mfma_f32_16x16x32_bf16 v[84:87], a[16:19], a[148:151], v[84:87]\n\t"
"s_lshl_b32 m0, s73, 8\n\t"
"ds_read_b64_tr_b16 a[186:187], v250 offset:43136\n\t"
"v_cvt_pk_bf16_f32 v193, v193, v197\n\t"
"v_mfma_f32_16x16x32_bf16 v[84:87], a[20:23], a[164:167], v[84:87]\n\t"
"ds_read_b128 a[144:147], v224 offset:1024\n\t"
"v_cvt_pk_bf16_f32 v201, v201, v205\n\t"
"v_mfma_f32_16x16x32_bf16 v[68:71], a[8:11], a[148:151], v[68:71]\n\t"
"ds_read_b64_tr_b16 a[188:189], v251 offset:42624\n\t"
"v_cvt_pk_bf16_f32 v200, v200, v204\n\t"
"v_mfma_f32_16x16x32_bf16 v[68:71], a[12:15], a[164:167], v[68:71]\n\t"
"ds_read_b64_tr_b16 a[190:191], v251 offset:43136\n\t"
"v_cvt_pk_bf16_f32 v194, v194, v198\n\t"
"v_mfma_f32_16x16x32_bf16 v[52:55], a[0:3], a[148:151], v[52:55]\n\t"
"ds_read_b64_tr_b16 a[192:193], v248 offset:21888\n\t"
"v_cvt_pk_bf16_f32 v210, -v210, -v214\n\t"
"v_mfma_f32_16x16x32_bf16 v[52:55], a[4:7], a[164:167], v[52:55]\n\t"
"ds_read_b128 a[148:151], v224 offset:1280\n\t"
"v_cvt_pk_bf16_f32 v211, -v211, -v215\n\t"
"v_mfma_f32_16x16x32_bf16 v[56:59], a[0:3], a[152:155], v[56:59]\n\t"
"ds_read_b128 a[164:167], v224 offset:3328\n\t"
"v_cvt_pk_bf16_f32 v195, v195, v199\n\t"
"v_mfma_f32_16x16x32_bf16 v[56:59], a[4:7], a[168:171], v[56:59]\n\t"
"ds_write_addtid_b32 v195 offset:28992\n\t"
"v_cvt_pk_bf16_f32 v209, -v209, -v213\n\t"
"v_mfma_f32_16x16x32_bf16 v[72:75], a[8:11], a[152:155], v[72:75]\n\t"
"ds_write_addtid_b32 v211 offset:30016\n\t"
"v_cvt_pk_bf16_f32 v217, -v217, -v221\n\t"
"v_mfma_f32_16x16x32_bf16 v[72:75], a[12:15], a[168:171], v[72:75]\n\t"
"s_nop 0\n\t"
"ds_write_addtid_b32 v193 offset:24864\n\t"
"ds_write_addtid_b32 v210 offset:21696\n\t"
"v_mfma_f32_16x16x32_bf16 v[88:91], a[16:19], a[152:155], v[88:91]\n\t"
"ds_read_b128 a[152:155], v224 offset:1536\n\t"
"v_cvt_pk_bf16_f32 v208, -v208, -v212\n\t"
"v_mfma_f32_16x16x32_bf16 v[88:91], a[20:23], a[168:171], v[88:91]\n\t"
"ds_read_b128 a[168:171], v224 offset:3584\n\t"
"v_cvt_pk_bf16_f32 v202, v202, v206\n\t"
"v_mfma_f32_16x16x32_bf16 v[92:95], a[16:19], a[156:159], v[92:95]\n\t"
"ds_write_addtid_b32 v217 offset:27936\n\t"
"v_cvt_pk_bf16_f32 v218, -v218, -v222\n\t"
"v_mfma_f32_16x16x32_bf16 v[92:95], a[20:23], a[172:175], v[92:95]\n\t"
"ds_write_addtid_b32 v202 offset:22720\n\t"
"v_cvt_pk_bf16_f32 v219, -v219, -v223\n\t"
"v_mfma_f32_16x16x32_bf16 v[76:79], a[8:11], a[156:159], v[76:79]\n\t"
"s_nop 0\n\t"
"ds_write_addtid_b32 v200 offset:18592\n\t"
"ds_write_addtid_b32 v218 offset:23744\n\t"
"v_mfma_f32_16x16x32_bf16 v[76:79], a[12:15], a[172:175], v[76:79]\n\t"
"s_nop 0\n\t"
"ds_write_addtid_b32 v208 offset:17568\n\t"
"ds_write_addtid_b32 v209 offset:25888\n\t"
"v_mfma_f32_16x16x32_bf16 v[60:63], a[0:3], a[156:159], v[60:63]\n\t"
"ds_read_b128 a[156:159], v224 offset:1792\n\t"
"v_cvt_pk_bf16_f32 v216, -v216, -v220\n\t"
"v_mfma_f32_16x16x32_bf16 v[60:63], a[4:7], a[172:175], v[60:63]\n\t"
"s_waitcnt vmcnt(7) lgkmcnt(12)\n\t"
"ds_write_addtid_b32 v219 offset:32064\n\t"
"v_cvt_pk_bf16_f32 v203, v203, v207\n\t"
"v_mfma_f32_16x16x32_bf16 v[144:147], a[24:27], a[144:147], v[144:147]\n\t"
"s_nop 0\n\t"
"ds_write_addtid_b32 v201 offset:26912\n\t"
"ds_write_addtid_b32 v216 offset:19616\n\t"
"v_mfma_f32_16x16x32_bf16 v[144:147], a[28:31], a[160:163], v[144:147]\n\t"
"ds_write_addtid_b32 v203 offset:31040\n\t"
"v_cvt_pk_bf16_f32 v192, v192, v196\n\t"
"v_mfma_f32_16x16x32_bf16 v[160:163], a[32:35], a[144:147], v[160:163]\n\t"
"s_nop 0\n\t"
"ds_write_addtid_b32 v192 offset:16544\n\t"
"ds_write_addtid_b32 v194 offset:20672\n\t"
"v_mfma_f32_16x16x32_bf16 v[160:163], a[36:39], a[160:163], v[160:163]\n\t"
"ds_read_b128 a[172:175], v224 offset:3840\n\t"
"v_mfma_f32_16x16x32_bf16 v[176:179], a[40:43], a[144:147], v[176:179]\n\t"
"ds_read_b64_tr_b16 a[194:195], v248 offset:22400\n\t"
"v_mfma_f32_16x16x32_bf16 v[176:179], a[44:47], a[160:163], v[176:179]\n\t"
"ds_read_b64_tr_b16 a[196:197], v249 offset:21888\n\t"
"v_mfma_f32_16x16x32_bf16 v[180:183], a[40:43], a[148:151], v[180:183]\n\t"
"ds_read_b64_tr_b16 a[198:199], v249 offset:22400\n\t"
"v_mfma_f32_16x16x32_bf16 v[180:183], a[44:47], a[164:167], v[180:183]\n\t"
"s_nop 0\n\t"
"ds_read_b64_tr_b16 a[200:201], v250 offset:21888\n\t"
"ds_read_b64_tr_b16 a[202:203], v250 offset:22400\n\t"
"v_mfma_f32_16x16x32_bf16 v[164:167], a[32:35], a[148:151], v[164:167]\n\t"
"s_nop 0\n\t"
"ds_read_b64_tr_b16 a[204:205], v251 offset:21888\n\t"
"ds_read_b64_tr_b16 a[206:207], v251 offset:22400\n\t"
"v_mfma_f32_16x16x32_bf16 v[164:167], a[36:39], a[164:167], v[164:167]\n\t"
"v_mfma_f32_16x16x32_bf16 v[148:151], a[24:27], a[148:151], v[148:151]\n\t"
"s_waitcnt lgkmcnt(7)\n\t"
"v_mfma_f32_16x16x32_bf16 v[148:151], a[28:31], a[164:167], v[148:151]\n\t"
"s_barrier\n\t"
"v_mfma_f32_16x16x32_bf16 v[152:155], a[24:27], a[152:155], v[152:155]\n\t"
"ds_read_b128 a[148:151], v224 offset:16800\n\t"
"v_add_u32 v248, 33280, v226\n\t"
"v_mfma_f32_16x16x32_bf16 v[152:155], a[28:31], a[168:171], v[152:155]\n\t"
"ds_read_b128 a[144:147], v224 offset:16544\n\t"
"v_xor_b32 v249, 32, v248\n\t"
"v_mfma_f32_16x16x32_bf16 v[168:171], a[32:35], a[152:155], v[168:171]\n\t"
"ds_read_b128 a[160:163], v224 offset:18592\n\t"
"v_mfma_f32_16x16x32_bf16 v[168:171], a[36:39], a[168:171], v[168:171]\n\t"
"ds_read_b128 a[164:167], v224 offset:18848\n\t"
"v_mfma_f32_16x16x32_bf16 v[184:187], a[40:43], a[152:155], v[184:187]\n\t"
"v_mfma_f32_16x16x32_bf16 v[184:187], a[44:47], a[168:171], v[184:187]\n\t"
"v_mfma_f32_16x16x32_bf16 v[188:191], a[40:43], a[156:159], v[188:191]\n\t"
"ds_read_b128 a[152:155], v224 offset:17056\n\t"
"v_mfma_f32_16x16x32_bf16 v[188:191], a[44:47], a[172:175], v[188:191]\n\t"
"ds_read_b128 a[168:171], v224 offset:19104\n\t"
"v_xor_b32 v250, 64, v248\n\t"
"v_mfma_f32_16x16x32_bf16 v[172:175], a[32:35], a[156:159], v[172:175]\n\t"
"v_mfma_f32_16x16x32_bf16 v[172:175], a[36:39], a[172:175], v[172:175]\n\t"
"v_mfma_f32_16x16x32_bf16 v[156:159], a[24:27], a[156:159], v[156:159]\n\t"
"ds_read_b128 a[156:159], v224 offset:17312\n\t"
"v_xor_b32 v251, 96, v248\n\t"
"v_mfma_f32_16x16x32_bf16 v[156:159], a[28:31], a[172:175], v[156:159]\n\t"
"ds_read_b128 a[172:175], v224 offset:19360\n\t"
"v_mfma_f32_16x16x32_bf16 v[192:195], a[216:219], a[176:179], 0\n\t"
"ds_read_b64_tr_b16 a[178:179], v248 offset:128\n\t"
"v_mfma_f32_16x16x32_bf16 v[196:199], a[216:219], a[180:183], 0\n\t"
"v_mfma_f32_16x16x32_bf16 v[200:203], a[216:219], a[184:187], 0\n\t"
"ds_read_b64_tr_b16 a[176:177], v248 offset:0\n\t"
"v_mfma_f32_16x16x32_bf16 v[204:207], a[216:219], a[188:191], 0\n\t"
"s_waitcnt lgkmcnt(6)\n\t"
"ds_read_b64_tr_b16 a[180:181], v249 offset:256\n\t"
"v_mfma_f32_16x16x32_bf16 v[208:211], a[216:219], a[192:195], 0\n\t"
"ds_read_b64_tr_b16 a[186:187], v249 offset:128\n\t"
"v_mfma_f32_16x16x32_bf16 v[212:215], a[216:219], a[196:199], 0\n\t"
"v_mfma_f32_16x16x32_bf16 v[216:219], a[216:219], a[200:203], 0\n\t"
"ds_read_b64_tr_b16 a[184:185], v249 offset:0\n\t"
"v_mfma_f32_16x16x32_bf16 v[220:223], a[216:219], a[204:207], 0\n\t"
"ds_read_b64_tr_b16 a[188:189], v248 offset:256\n\t"
"v_mfma_f32_16x16x32_bf16 v[0:3], a[48:51], a[144:147], v[0:3]\n\t"
"ds_read_b64_tr_b16 a[194:195], v250 offset:128\n\t"
"v_mfma_f32_16x16x32_bf16 v[0:3], a[52:55], a[160:163], v[0:3]\n\t"
"v_cvt_pk_bf16_f32 v195, -v195, -v199\n\t"
"v_cvt_pk_bf16_f32 v201, -v201, -v205\n\t"
"v_mfma_f32_16x16x32_bf16 v[16:19], a[56:59], a[144:147], v[16:19]\n\t"
"ds_read_b64_tr_b16 a[192:193], v250 offset:0\n\t"
"v_mfma_f32_16x16x32_bf16 v[16:19], a[60:63], a[160:163], v[16:19]\n\t"
"ds_read_b64_tr_b16 a[196:197], v251 offset:256\n\t"
"v_cvt_pk_bf16_f32 v200, -v200, -v204\n\t"
"v_mfma_f32_16x16x32_bf16 v[32:35], a[64:67], a[144:147], v[32:35]\n\t"
"ds_read_b128 a[144:147], v224 offset:17568\n\t"
"v_cvt_pk_bf16_f32 v209, -v209, -v213\n\t"
"v_mfma_f32_16x16x32_bf16 v[32:35], a[68:71], a[160:163], v[32:35]\n\t"
"v_cvt_pk_bf16_f32 v203, -v203, -v207\n\t"
"v_cvt_pk_bf16_f32 v217, -v217, -v221\n\t"
"v_mfma_f32_16x16x32_bf16 v[36:39], a[64:67], a[148:151], v[36:39]\n\t"
"ds_read_b128 a[160:163], v224 offset:19616\n\t"
"v_cvt_pk_bf16_f32 v219, -v219, -v223\n\t"
"v_mfma_f32_16x16x32_bf16 v[36:39], a[68:71], a[164:167], v[36:39]\n\t"
"ds_read_b64_tr_b16 a[202:203], v251 offset:128\n\t"
"v_cvt_pk_bf16_f32 v208, -v208, -v212\n\t"
"v_mfma_f32_16x16x32_bf16 v[20:23], a[56:59], a[148:151], v[20:23]\n\t"
"ds_write_addtid_b32 v203 offset:14496\n\t"
"v_cvt_pk_bf16_f32 v194, -v194, -v198\n\t"
"v_mfma_f32_16x16x32_bf16 v[20:23], a[60:63], a[164:167], v[20:23]\n\t"
"ds_write_addtid_b32 v200 offset:2048\n\t"
"v_cvt_pk_bf16_f32 v211, -v211, -v215\n\t"
"v_mfma_f32_16x16x32_bf16 v[4:7], a[48:51], a[148:151], v[4:7]\n\t"
"ds_read_b128 a[148:151], v224 offset:17824\n\t"
"v_mfma_f32_16x16x32_bf16 v[4:7], a[52:55], a[164:167], v[4:7]\n\t"
"s_waitcnt lgkmcnt(8)\n\t"
"ds_write_addtid_b32 v201 offset:10368\n\t"
"v_cvt_pk_bf16_f32 v218, -v218, -v222\n\t"
"v_mfma_f32_16x16x32_bf16 v[8:11], a[48:51], a[152:155], v[8:11]\n\t"
"ds_read_b128 a[164:167], v224 offset:19872\n\t"
"v_cvt_pk_bf16_f32 v210, -v210, -v214\n\t"
"v_mfma_f32_16x16x32_bf16 v[8:11], a[52:55], a[168:171], v[8:11]\n\t"
"ds_write_addtid_b32 v194 offset:4128\n\t"
"v_cvt_pk_bf16_f32 v202, -v202, -v206\n\t"
"v_mfma_f32_16x16x32_bf16 v[24:27], a[56:59], a[152:155], v[24:27]\n\t"
"ds_write_addtid_b32 v209 offset:9344\n\t"
"v_mfma_f32_16x16x32_bf16 v[24:27], a[60:63], a[168:171], v[24:27]\n\t"
"ds_write_addtid_b32 v202 offset:6176\n\t"
"v_mfma_f32_16x16x32_bf16 v[40:43], a[64:67], a[152:155], v[40:43]\n\t"
"ds_read_b128 a[152:155], v224 offset:18080\n\t"
"v_cvt_pk_bf16_f32 v193, -v193, -v197\n\t"
"v_mfma_f32_16x16x32_bf16 v[40:43], a[68:71], a[168:171], v[40:43]\n\t"
"ds_read_b128 a[168:171], v224 offset:20128\n\t"
"v_mfma_f32_16x16x32_bf16 v[44:47], a[64:67], a[156:159], v[44:47]\n\t"
"ds_write_addtid_b32 v210 offset:5152\n\t"
"v_cvt_pk_bf16_f32 v192, -v192, -v196\n\t"
"v_mfma_f32_16x16x32_bf16 v[44:47], a[68:71], a[172:175], v[44:47]\n\t"
"buffer_load_dwordx4 a[64:67], off, s[28:31], s42 offset:0 sc1\n\t"
"v_mfma_f32_16x16x32_bf16 v[28:31], a[56:59], a[156:159], v[28:31]\n\t"
"ds_write_addtid_b32 v217 offset:11392\n\t"
"v_mfma_f32_16x16x32_bf16 v[28:31], a[60:63], a[172:175], v[28:31]\n\t"
"s_xor_b32 s24, s24, 384\n\t"
"ds_write_addtid_b32 v195 offset:12448\n\t"
"ds_write_addtid_b32 v208 offset:1024\n\t"
"v_mfma_f32_16x16x32_bf16 v[12:15], a[48:51], a[156:159], v[12:15]\n\t"
"ds_write_addtid_b32 v219 offset:15520\n\t"
"v_mfma_f32_16x16x32_bf16 v[12:15], a[52:55], a[172:175], v[12:15]\n\t"
"s_waitcnt lgkmcnt(9)\n\t"
"buffer_load_dwordx4 a[68:71], off, s[28:31], s42 offset:1024 sc1\n\t"
"v_mfma_f32_16x16x32_bf16 v[96:99], a[72:75], a[144:147], v[0:3]\n\t"
"s_xor_b32 s20, s20, 384\n\t"
"ds_write_addtid_b32 v193 offset:8320\n\t"
"ds_write_addtid_b32 v211 offset:13472\n\t"
"v_mfma_f32_16x16x32_bf16 v[96:99], a[76:79], a[160:163], v[96:99]\n\t"
"ds_write_addtid_b32 v218 offset:7200\n\t"
"v_cvt_pk_bf16_f32 v216, -v216, -v220\n\t"
"v_mfma_f32_16x16x32_bf16 v[112:115], a[80:83], a[144:147], v[16:19]\n\t"
"s_xor_b32 s27, s27, 384\n\t"
"ds_write_addtid_b32 v192 offset:0\n\t"
"ds_write_addtid_b32 v216 offset:3072\n\t"
"v_mfma_f32_16x16x32_bf16 v[112:115], a[84:87], a[160:163], v[112:115]\n\t"
"buffer_load_dwordx4 a[60:63], off, s[84:87], s42 offset:3072 sc1\n\t"
"v_mfma_f32_16x16x32_bf16 v[128:131], a[88:91], a[144:147], v[32:35]\n\t"
"ds_read_b128 a[156:159], v224 offset:18336\n\t"
"v_mfma_f32_16x16x32_bf16 v[128:131], a[92:95], a[160:163], v[128:131]\n\t"
"buffer_load_dwordx4 a[56:59], off, s[84:87], s42 offset:2048 sc1\n\t"
"v_mfma_f32_16x16x32_bf16 v[132:135], a[88:91], a[148:151], v[36:39]\n\t"
"ds_read_b128 a[172:175], v224 offset:20384\n\t"
"v_mfma_f32_16x16x32_bf16 v[132:135], a[92:95], a[164:167], v[132:135]\n\t"
"buffer_load_dwordx4 a[48:51], off, s[84:87], s42 offset:0 sc1\n\t"
"v_mfma_f32_16x16x32_bf16 v[116:119], a[80:83], a[148:151], v[20:23]\n\t"
"s_xor_b32 s25, s25, 384\n\t"
"ds_read_b64_tr_b16 a[200:201], v251 offset:0\n\t"
"ds_read_b64_tr_b16 a[204:205], v250 offset:256\n\t"
"v_mfma_f32_16x16x32_bf16 v[116:119], a[84:87], a[164:167], v[116:119]\n\t"
"buffer_load_dwordx4 a[52:55], off, s[84:87], s42 offset:1024 sc1\n\t"
"v_mfma_f32_16x16x32_bf16 v[100:103], a[72:75], a[148:151], v[4:7]\n\t"
"v_mfma_f32_16x16x32_bf16 v[100:103], a[76:79], a[164:167], v[100:103]\n\t"
"s_waitcnt lgkmcnt(0)\n\t"
"s_barrier\n\t"
"v_mfma_f32_16x16x32_bf16 v[104:107], a[72:75], a[152:155], v[8:11]\n\t"
"ds_read_b128 a[160:163], v224 offset:2048\n\t"
"v_mfma_f32_16x16x32_bf16 v[104:107], a[76:79], a[168:171], v[104:107]\n\t"
"ds_read_b128 a[144:147], v224 offset:0\n\t"
"v_mfma_f32_16x16x32_bf16 v[120:123], a[80:83], a[152:155], v[24:27]\n\t"
"ds_read_b128 a[148:151], v224 offset:256\n\t"
"v_mfma_f32_16x16x32_bf16 v[120:123], a[84:87], a[168:171], v[120:123]\n\t"
"ds_read_b128 a[164:167], v224 offset:2304\n\t"
"v_mfma_f32_16x16x32_bf16 v[136:139], a[88:91], a[152:155], v[40:43]\n\t"
"v_mfma_f32_16x16x32_bf16 v[136:139], a[92:95], a[168:171], v[136:139]\n\t"
"ds_write_b128 v254, a[240:243] offset:0\n\t"
"v_mfma_f32_16x16x32_bf16 v[140:143], a[88:91], a[156:159], v[44:47]\n\t"
"v_mfma_f32_16x16x32_bf16 v[140:143], a[92:95], a[172:175], v[140:143]\n\t"
"buffer_load_dwordx4 a[88:91], off, s[28:31], s48 offset:0 sc1\n\t"
"v_mfma_f32_16x16x32_bf16 v[124:127], a[80:83], a[156:159], v[28:31]\n\t"
"v_mfma_f32_16x16x32_bf16 v[124:127], a[84:87], a[172:175], v[124:127]\n\t"
"buffer_load_dwordx4 a[92:95], off, s[28:31], s48 offset:1024 sc1\n\t"
"v_mfma_f32_16x16x32_bf16 v[108:111], a[72:75], a[156:159], v[12:15]\n\t"
"ds_read_b128 a[152:155], v224 offset:512\n\t"
"v_mfma_f32_16x16x32_bf16 v[108:111], a[76:79], a[172:175], v[108:111]\n\t"
"buffer_load_dwordx4 a[84:87], off, s[84:87], s48 offset:3072 sc1\n\t"
"v_mfma_f32_4x4x4_16b_bf16 v[200:203], a[220:221], a[178:179], 0\n\t"
"ds_read_b128 a[168:171], v224 offset:2560\n\t"
"v_mfma_f32_4x4x4_16b_bf16 v[204:207], a[220:221], a[186:187], 0\n\t"
"ds_read_b64_tr_b16 a[178:179], v248 offset:41600\n\t"
"v_mfma_f32_4x4x4_16b_bf16 v[216:219], a[220:221], a[194:195], 0\n\t"
"ds_read_b128 a[156:159], v224 offset:768\n\t"
"v_mfma_f32_4x4x4_16b_bf16 v[220:223], a[220:221], a[202:203], 0\n\t"
"v_mfma_f32_4x4x4_16b_bf16 v[192:195], a[222:223], a[176:177], v[200:203]\n\t"
"ds_read_b128 a[172:175], v224 offset:2816\n\t"
"v_mfma_f32_4x4x4_16b_bf16 v[196:199], a[222:223], a[184:185], v[204:207]\n\t"
"ds_read_b64_tr_b16 a[186:187], v249 offset:41600\n\t"
"v_mfma_f32_4x4x4_16b_bf16 v[200:203], a[222:223], a[180:181], v[200:203]\n\t"
"ds_read_b64_tr_b16 a[194:195], v250 offset:41600\n\t"
"v_mfma_f32_4x4x4_16b_bf16 v[204:207], a[222:223], a[188:189], v[204:207]\n\t"
"ds_read_b64_tr_b16 a[202:203], v251 offset:41600\n\t"
"v_mfma_f32_4x4x4_16b_bf16 v[208:211], a[222:223], a[192:193], v[216:219]\n\t"
"ds_read_b64_tr_b16 a[176:177], v248 offset:41472\n\t"
"v_mfma_f32_4x4x4_16b_bf16 v[212:215], a[222:223], a[200:201], v[220:223]\n\t"
"ds_read_b64_tr_b16 a[184:185], v249 offset:41472\n\t"
"v_mfma_f32_4x4x4_16b_bf16 v[216:219], a[222:223], a[196:197], v[216:219]\n\t"
"ds_read_b64_tr_b16 a[180:181], v249 offset:41728\n\t"
"v_mfma_f32_4x4x4_16b_bf16 v[220:223], a[222:223], a[204:205], v[220:223]\n\t"
"s_waitcnt lgkmcnt(8)\n\t"
"buffer_load_dwordx4 a[80:83], off, s[84:87], s48 offset:2048 sc1\n\t"
"v_mfma_f32_16x16x32_bf16 v[96:99], a[96:99], a[144:147], v[96:99]\n\t"
"ds_read_b64_tr_b16 a[188:189], v248 offset:41728\n\t"
"v_cvt_pk_bf16_f32 v195, -v195, -v199\n\t"
"v_mfma_f32_16x16x32_bf16 v[96:99], a[100:103], a[160:163], v[96:99]\n\t"
"ds_read_b64_tr_b16 a[192:193], v250 offset:41472\n\t"
"v_cvt_pk_bf16_f32 v193, -v193, -v197\n\t"
"v_mfma_f32_16x16x32_bf16 v[112:115], a[104:107], a[144:147], v[112:115]\n\t"
"ds_read_b64_tr_b16 a[200:201], v251 offset:41472\n\t"
"v_cvt_pk_bf16_f32 v201, v201, v205\n\t"
"v_mfma_f32_16x16x32_bf16 v[112:115], a[108:111], a[160:163], v[112:115]\n\t"
"buffer_load_dwordx4 a[72:75], off, s[84:87], s48 offset:0 sc1\n\t"
"v_mfma_f32_16x16x32_bf16 v[128:131], a[112:115], a[144:147], v[128:131]\n\t"
"ds_read_b128 a[144:147], v224 offset:1024\n\t"
"v_cvt_pk_bf16_f32 v200, v200, v204\n\t"
"v_mfma_f32_16x16x32_bf16 v[128:131], a[116:119], a[160:163], v[128:131]\n\t"
"ds_read_b64_tr_b16 a[196:197], v251 offset:41728\n\t"
"v_cvt_pk_bf16_f32 v194, -v194, -v198\n\t"
"v_mfma_f32_16x16x32_bf16 v[132:135], a[112:115], a[148:151], v[132:135]\n\t"
"ds_read_b128 a[160:163], v224 offset:3072\n\t"
"v_cvt_pk_bf16_f32 v217, v217, v221\n\t"
"v_mfma_f32_16x16x32_bf16 v[132:135], a[116:119], a[164:167], v[132:135]\n\t"
"buffer_load_dwordx4 a[76:79], off, s[84:87], s48 offset:1024 sc1\n\t"
"v_mfma_f32_16x16x32_bf16 v[116:119], a[104:107], a[148:151], v[116:119]\n\t"
"ds_write_addtid_b32 v193 offset:20672\n\t"
"v_cvt_pk_bf16_f32 v219, v219, v223\n\t"
"v_mfma_f32_16x16x32_bf16 v[116:119], a[108:111], a[164:167], v[116:119]\n\t"
"ds_write_addtid_b32 v194 offset:17568\n\t"
"v_cvt_pk_bf16_f32 v210, -v210, -v214\n\t"
"v_mfma_f32_16x16x32_bf16 v[100:103], a[96:99], a[148:151], v[100:103]\n\t"
"ds_read_b128 a[148:151], v224 offset:1280\n\t"
"v_cvt_pk_bf16_f32 v211, -v211, -v215\n\t"
"v_mfma_f32_16x16x32_bf16 v[100:103], a[100:103], a[164:167], v[100:103]\n\t"
"ds_write_addtid_b32 v201 offset:28992\n\t"
"v_cvt_pk_bf16_f32 v218, v218, v222\n\t"
"v_mfma_f32_16x16x32_bf16 v[104:107], a[96:99], a[152:155], v[104:107]\n\t"
"ds_read_b128 a[164:167], v224 offset:3328\n\t"
"v_cvt_pk_bf16_f32 v203, v203, v207\n\t"
"v_mfma_f32_16x16x32_bf16 v[104:107], a[100:103], a[168:171], v[104:107]\n\t"
"ds_write_addtid_b32 v218 offset:27936\n\t"
"v_mfma_f32_16x16x32_bf16 v[120:123], a[104:107], a[152:155], v[120:123]\n\t"
"ds_write_addtid_b32 v217 offset:31040\n\t"
"v_cvt_pk_bf16_f32 v208, -v208, -v212\n\t"
"v_mfma_f32_16x16x32_bf16 v[120:123], a[108:111], a[168:171], v[120:123]\n\t"
"ds_write_addtid_b32 v208 offset:18592\n\t"
"v_mfma_f32_16x16x32_bf16 v[136:139], a[112:115], a[152:155], v[136:139]\n\t"
"ds_read_b128 a[152:155], v224 offset:1536\n\t"
"v_mfma_f32_16x16x32_bf16 v[136:139], a[116:119], a[168:171], v[136:139]\n\t"
"s_waitcnt lgkmcnt(9)\n\t"
"ds_read_b128 a[168:171], v224 offset:3584\n\t"
"v_cvt_pk_bf16_f32 v216, v216, v220\n\t"
"v_mfma_f32_16x16x32_bf16 v[140:143], a[112:115], a[156:159], v[140:143]\n\t"
"s_nop 0\n\t"
"ds_write_addtid_b32 v210 offset:19616\n\t"
"v_cvt_pk_bf16_f32 v209, -v209, -v213\n\t"
"v_mfma_f32_16x16x32_bf16 v[140:143], a[116:119], a[172:175], v[140:143]\n\t"
"buffer_load_dwordx4 a[112:115], off, s[28:31], s75 offset:0 sc1\n\t"
"v_mfma_f32_16x16x32_bf16 v[124:127], a[104:107], a[156:159], v[124:127]\n\t"
"s_nop 0\n\t"
"ds_write_addtid_b32 v203 offset:30016\n\t"
"v_cvt_pk_bf16_f32 v202, v202, v206\n\t"
"v_mfma_f32_16x16x32_bf16 v[124:127], a[108:111], a[172:175], v[124:127]\n\t"
"s_nop 0\n\t"
"ds_write_addtid_b32 v202 offset:25888\n\t"
"ds_write_addtid_b32 v216 offset:26912\n\t"
"v_mfma_f32_16x16x32_bf16 v[108:111], a[96:99], a[156:159], v[108:111]\n\t"
"s_nop 0\n\t"
"ds_write_addtid_b32 v219 offset:32064\n\t"
"v_cvt_pk_bf16_f32 v192, -v192, -v196\n\t"
"v_mfma_f32_16x16x32_bf16 v[108:111], a[100:103], a[172:175], v[108:111]\n\t"
"buffer_load_dwordx4 a[116:119], off, s[28:31], s75 offset:1024 sc1\n\t"
"v_mfma_f32_16x16x32_bf16 v[96:99], a[120:123], a[144:147], v[96:99]\n\t"
"s_nop 0\n\t"
"ds_write_addtid_b32 v209 offset:22720\n\t"
"ds_write_addtid_b32 v211 offset:23744\n\t"
"v_mfma_f32_16x16x32_bf16 v[96:99], a[124:127], a[160:163], v[96:99]\n\t"
"s_nop 0\n\t"
"ds_write_addtid_b32 v195 offset:21696\n\t"
"v_mfma_f32_16x16x32_bf16 v[112:115], a[128:131], a[144:147], v[112:115]\n\t"
"s_nop 0\n\t"
"ds_write_addtid_b32 v192 offset:16544\n\t"
"ds_write_addtid_b32 v200 offset:24864\n\t"
"v_mfma_f32_16x16x32_bf16 v[112:115], a[132:135], a[160:163], v[112:115]\n\t"
"buffer_load_dwordx4 a[108:111], off, s[84:87], s75 offset:3072 sc1\n\t"
"v_mfma_f32_16x16x32_bf16 v[128:131], a[136:139], a[144:147], v[128:131]\n\t"
"s_nop 0\n\t"
"ds_read_b128 a[172:175], v224 offset:3840\n\t"
"v_mfma_f32_16x16x32_bf16 v[128:131], a[140:143], a[160:163], v[128:131]\n\t"
"s_waitcnt lgkmcnt(10)\n\t"
"buffer_load_dwordx4 a[104:107], off, s[84:87], s75 offset:2048 sc1\n\t"
"v_mfma_f32_16x16x32_bf16 v[132:135], a[136:139], a[148:151], v[132:135]\n\t"
"s_nop 0\n\t"
"ds_read_b128 a[156:159], v224 offset:1792\n\t"
"v_mfma_f32_16x16x32_bf16 v[132:135], a[140:143], a[164:167], v[132:135]\n\t"
"buffer_load_dwordx4 a[96:99], off, s[84:87], s75 offset:0 sc1\n\t"
"v_mfma_f32_16x16x32_bf16 v[116:119], a[128:131], a[148:151], v[116:119]\n\t"
"s_nop 0\n\t"
"ds_read_b64_tr_b16 a[204:205], v250 offset:41728\n\t"
"v_mfma_f32_16x16x32_bf16 v[116:119], a[132:135], a[164:167], v[116:119]\n\t"
"buffer_load_dwordx4 a[100:103], off, s[84:87], s75 offset:1024 sc1\n\t"
"v_mfma_f32_16x16x32_bf16 v[100:103], a[120:123], a[148:151], v[100:103]\n\t"
"s_nop 0\n\t"
"v_mfma_f32_16x16x32_bf16 v[100:103], a[124:127], a[164:167], v[100:103]\n\t"
"s_waitcnt lgkmcnt(0)\n\t"
"s_barrier\n\t"
"v_mfma_f32_16x16x32_bf16 v[104:107], a[120:123], a[152:155], v[104:107]\n\t"
"s_nop 0\n\t"
"ds_read_b128 a[144:147], v224 offset:16544\n\t"
"v_add_u32 v248, 101632, v226\n\t"
"v_mfma_f32_16x16x32_bf16 v[104:107], a[124:127], a[168:171], v[104:107]\n\t"
"s_nop 0\n\t"
"ds_read_b128 a[148:151], v224 offset:16800\n\t"
"v_mfma_f32_16x16x32_bf16 v[120:123], a[128:131], a[152:155], v[120:123]\n\t"
"s_nop 0\n\t"
"ds_read_b128 a[160:163], v224 offset:18592\n\t"
"v_xor_b32 v249, 32, v248\n\t"
"v_mfma_f32_16x16x32_bf16 v[120:123], a[132:135], a[168:171], v[120:123]\n\t"
"s_nop 0\n\t"
"ds_read_b128 a[164:167], v224 offset:18848\n\t"
"v_mfma_f32_16x16x32_bf16 v[136:139], a[136:139], a[152:155], v[136:139]\n\t"
"s_nop 0\n\t"
"v_xor_b32 v250, 64, v248\n\t"
"v_mfma_f32_16x16x32_bf16 v[136:139], a[140:143], a[168:171], v[136:139]\n\t"
"s_nop 0\n\t"
"v_mfma_f32_16x16x32_bf16 v[140:143], a[136:139], a[156:159], v[140:143]\n\t"
"s_nop 0\n\t"
"ds_read_b128 a[152:155], v224 offset:17056\n\t"
"v_mfma_f32_16x16x32_bf16 v[140:143], a[140:143], a[172:175], v[140:143]\n\t"
"buffer_load_dwordx4 a[136:139], off, s[28:31], s78 offset:0 sc1\n\t"
"v_mfma_f32_16x16x32_bf16 v[124:127], a[128:131], a[156:159], v[124:127]\n\t"
"s_nop 0\n\t"
"ds_read_b128 a[168:171], v224 offset:19104\n\t"
"v_xor_b32 v251, 96, v248\n\t"
"v_mfma_f32_16x16x32_bf16 v[124:127], a[132:135], a[172:175], v[124:127]\n\t"
"Lpad8_%=:\n\t"
"s_cbranch_execz Lpad12_%=\n\t"
"v_mfma_f32_16x16x32_bf16 v[108:111], a[120:123], a[156:159], v[108:111]\n\t"
"ds_read_b128 a[156:159], v224 offset:17312\n\t"
"v_mfma_f32_16x16x32_bf16 v[108:111], a[124:127], a[172:175], v[108:111]\n\t"
"buffer_load_dwordx4 a[140:143], off, s[28:31], s78 offset:1024 sc1\n\t"
"v_mfma_f32_4x4x4_16b_bf16 v[200:203], a[220:221], a[178:179], 0\n\t"
"ds_read_b64_tr_b16 a[178:179], v248 offset:20864\n\t"
"v_mfma_f32_4x4x4_16b_bf16 v[204:207], a[220:221], a[186:187], 0\n\t"
"v_mfma_f32_4x4x4_16b_bf16 v[216:219], a[220:221], a[194:195], 0\n\t"
"ds_read_b64_tr_b16 a[186:187], v249 offset:20864\n\t"
"v_mfma_f32_4x4x4_16b_bf16 v[220:223], a[220:221], a[202:203], 0\n\t"
".p2align 3\n\t"
"ds_read_b128 a[172:175], v224 offset:19360\n\t"
"v_pk_fma_f32 v[0:1], v[96:97], s[90:91], v[0:1] neg_lo:[1,0,0] neg_hi:[1,0,0]\n\t"
"v_pk_fma_f32 v[2:3], v[98:99], s[90:91], v[2:3] neg_lo:[1,0,0] neg_hi:[1,0,0]\n\t"
"v_pk_fma_f32 v[16:17], v[112:113], s[90:91], v[16:17] neg_lo:[1,0,0] neg_hi:[1,0,0]\n\t"
"v_pk_fma_f32 v[18:19], v[114:115], s[90:91], v[18:19] neg_lo:[1,0,0] neg_hi:[1,0,0]\n\t"
"v_pk_fma_f32 v[32:33], v[128:129], s[90:91], v[32:33] neg_lo:[1,0,0] neg_hi:[1,0,0]\n\t"
"v_pk_fma_f32 v[34:35], v[130:131], s[90:91], v[34:35] neg_lo:[1,0,0] neg_hi:[1,0,0]\n\t"
"v_pk_fma_f32 v[36:37], v[132:133], s[90:91], v[36:37] neg_lo:[1,0,0] neg_hi:[1,0,0]\n\t"
"v_pk_fma_f32 v[38:39], v[134:135], s[90:91], v[38:39] neg_lo:[1,0,0] neg_hi:[1,0,0]\n\t"
"v_pk_fma_f32 v[20:21], v[116:117], s[90:91], v[20:21] neg_lo:[1,0,0] neg_hi:[1,0,0]\n\t"
"v_pk_fma_f32 v[22:23], v[118:119], s[90:91], v[22:23] neg_lo:[1,0,0] neg_hi:[1,0,0]\n\t"
"v_pk_fma_f32 v[4:5], v[100:101], s[90:91], v[4:5] neg_lo:[1,0,0] neg_hi:[1,0,0]\n\t"
"v_pk_fma_f32 v[6:7], v[102:103], s[90:91], v[6:7] neg_lo:[1,0,0] neg_hi:[1,0,0]\n\t"
"v_pk_fma_f32 v[8:9], v[104:105], s[90:91], v[8:9] neg_lo:[1,0,0] neg_hi:[1,0,0]\n\t"
"v_pk_fma_f32 v[10:11], v[106:107], s[90:91], v[10:11] neg_lo:[1,0,0] neg_hi:[1,0,0]\n\t"
"v_pk_fma_f32 v[24:25], v[120:121], s[90:91], v[24:25] neg_lo:[1,0,0] neg_hi:[1,0,0]\n\t"
"v_pk_fma_f32 v[26:27], v[122:123], s[90:91], v[26:27] neg_lo:[1,0,0] neg_hi:[1,0,0]\n\t"
"v_pk_fma_f32 v[40:41], v[136:137], s[90:91], v[40:41] neg_lo:[1,0,0] neg_hi:[1,0,0]\n\t"
"v_pk_fma_f32 v[42:43], v[138:139], s[90:91], v[42:43] neg_lo:[1,0,0] neg_hi:[1,0,0]\n\t"
"v_pk_fma_f32 v[44:45], v[140:141], s[90:91], v[44:45] neg_lo:[1,0,0] neg_hi:[1,0,0]\n\t"
"v_pk_fma_f32 v[46:47], v[142:143], s[90:91], v[46:47] neg_lo:[1,0,0] neg_hi:[1,0,0]\n\t"
"v_pk_fma_f32 v[28:29], v[124:125], s[90:91], v[28:29] neg_lo:[1,0,0] neg_hi:[1,0,0]\n\t"
"v_pk_fma_f32 v[30:31], v[126:127], s[90:91], v[30:31] neg_lo:[1,0,0] neg_hi:[1,0,0]\n\t"
"v_pk_fma_f32 v[12:13], v[108:109], s[90:91], v[12:13] neg_lo:[1,0,0] neg_hi:[1,0,0]\n\t"
"v_pk_fma_f32 v[14:15], v[110:111], s[90:91], v[14:15] neg_lo:[1,0,0] neg_hi:[1,0,0]\n\t"
"v_mfma_f32_4x4x4_16b_bf16 v[192:195], a[222:223], a[176:177], v[200:203]\n\t"
"ds_read_b64_tr_b16 a[194:195], v250 offset:20864\n\t"
"v_mfma_f32_4x4x4_16b_bf16 v[196:199], a[222:223], a[184:185], v[204:207]\n\t"
"ds_read_b64_tr_b16 a[202:203], v251 offset:20864\n\t"
"v_mfma_f32_4x4x4_16b_bf16 v[200:203], a[222:223], a[180:181], v[200:203]\n\t"
"ds_read_b64_tr_b16 a[176:177], v248 offset:20736\n\t"
"v_mfma_f32_4x4x4_16b_bf16 v[204:207], a[222:223], a[188:189], v[204:207]\n\t"
"v_mfma_f32_4x4x4_16b_bf16 v[208:211], a[222:223], a[192:193], v[216:219]\n\t"
"ds_read_b64_tr_b16 a[184:185], v249 offset:20736\n\t"
"v_mfma_f32_4x4x4_16b_bf16 v[212:215], a[222:223], a[200:201], v[220:223]\n\t"
"ds_read_b64_tr_b16 a[180:181], v249 offset:20992\n\t"
"v_mfma_f32_4x4x4_16b_bf16 v[216:219], a[222:223], a[196:197], v[216:219]\n\t"
"ds_read_b64_tr_b16 a[188:189], v248 offset:20992\n\t"
"v_mfma_f32_4x4x4_16b_bf16 v[220:223], a[222:223], a[204:205], v[220:223]\n\t"
"s_waitcnt vmcnt(14) lgkmcnt(8)\n\t"
"buffer_load_dwordx4 a[132:135], off, s[84:87], s78 offset:3072 sc1\n\t"
"v_mfma_f32_16x16x32_bf16 v[48:51], a[48:51], a[144:147], v[48:51]\n\t"
"ds_read_b64_tr_b16 a[192:193], v250 offset:20736\n\t"
"v_cvt_pk_bf16_f32 v195, -v195, -v199\n\t"
"v_mfma_f32_16x16x32_bf16 v[48:51], a[52:55], a[160:163], v[48:51]\n\t"
"ds_read_b64_tr_b16 a[200:201], v251 offset:20736\n\t"
"v_cvt_pk_bf16_f32 v193, -v193, -v197\n\t"
"v_mfma_f32_16x16x32_bf16 v[64:67], a[56:59], a[144:147], v[64:67]\n\t"
"ds_write_addtid_b32 v193 offset:4128\n\t"
"v_cvt_pk_bf16_f32 v202, v202, v206\n\t"
"v_mfma_f32_16x16x32_bf16 v[64:67], a[60:63], a[160:163], v[64:67]\n\t"
"buffer_load_dwordx4 a[128:131], off, s[84:87], s78 offset:2048 sc1\n\t"
"v_mfma_f32_16x16x32_bf16 v[80:83], a[64:67], a[144:147], v[80:83]\n\t"
"ds_read_b128 a[144:147], v224 offset:17568\n\t"
"v_mfma_f32_16x16x32_bf16 v[80:83], a[68:71], a[160:163], v[80:83]\n\t"
"ds_write_addtid_b32 v202 offset:9344\n\t"
"v_cvt_pk_bf16_f32 v211, -v211, -v215\n\t"
"v_mfma_f32_16x16x32_bf16 v[84:87], a[64:67], a[148:151], v[84:87]\n\t"
"ds_read_b128 a[160:163], v224 offset:19616\n\t"
"v_cvt_pk_bf16_f32 v203, v203, v207\n\t"
"v_mfma_f32_16x16x32_bf16 v[84:87], a[68:71], a[164:167], v[84:87]\n\t"
"buffer_load_dwordx4 a[120:123], off, s[84:87], s78 offset:0 sc1\n\t"
"v_mfma_f32_16x16x32_bf16 v[68:71], a[56:59], a[148:151], v[68:71]\n\t"
"ds_write_addtid_b32 v195 offset:5152\n\t"
"v_mfma_f32_16x16x32_bf16 v[68:71], a[60:63], a[164:167], v[68:71]\n\t"
"ds_write_addtid_b32 v203 offset:13472\n\t"
"v_cvt_pk_bf16_f32 v192, -v192, -v196\n\t"
"v_mfma_f32_16x16x32_bf16 v[52:55], a[48:51], a[148:151], v[52:55]\n\t"
"ds_read_b128 a[148:151], v224 offset:17824\n\t"
"v_cvt_pk_bf16_f32 v201, v201, v205\n\t"
"v_mfma_f32_16x16x32_bf16 v[52:55], a[52:55], a[164:167], v[52:55]\n\t"
"buffer_load_dwordx4 a[124:127], off, s[84:87], s78 offset:1024 sc1\n\t"
"v_mfma_f32_16x16x32_bf16 v[56:59], a[48:51], a[152:155], v[56:59]\n\t"
"ds_read_b128 a[164:167], v224 offset:19872\n\t"
"v_cvt_pk_bf16_f32 v194, -v194, -v198\n\t"
"v_mfma_f32_16x16x32_bf16 v[56:59], a[52:55], a[168:171], v[56:59]\n\t"
"ds_write_addtid_b32 v194 offset:1024\n\t"
"v_mfma_f32_16x16x32_bf16 v[72:75], a[56:59], a[152:155], v[72:75]\n\t"
"ds_write_addtid_b32 v201 offset:12448\n\t"
"v_cvt_pk_bf16_f32 v218, v218, v222\n\t"
"v_mfma_f32_16x16x32_bf16 v[72:75], a[60:63], a[168:171], v[72:75]\n\t"
"ds_write_addtid_b32 v211 offset:7200\n\t"
"v_mfma_f32_16x16x32_bf16 v[88:91], a[64:67], a[152:155], v[88:91]\n\t"
"ds_read_b128 a[152:155], v224 offset:18080\n\t"
"v_cvt_pk_bf16_f32 v200, v200, v204\n\t"
"v_mfma_f32_16x16x32_bf16 v[88:91], a[68:71], a[168:171], v[88:91]\n\t"
"s_waitcnt lgkmcnt(8)\n\t"
"ds_read_b128 a[168:171], v224 offset:20128\n\t"
"v_cvt_pk_bf16_f32 v208, -v208, -v212\n\t"
"v_mfma_f32_16x16x32_bf16 v[92:95], a[64:67], a[156:159], v[92:95]\n\t"
"ds_write_addtid_b32 v200 offset:8320\n\t"
"v_cvt_pk_bf16_f32 v209, -v209, -v213\n\t"
"v_mfma_f32_16x16x32_bf16 v[92:95], a[68:71], a[172:175], v[92:95]\n\t"
"ds_write_addtid_b32 v209 offset:6176\n\t"
"v_cvt_pk_bf16_f32 v217, v217, v221\n\t"
"v_mfma_f32_16x16x32_bf16 v[76:79], a[56:59], a[156:159], v[76:79]\n\t"
"ds_write_addtid_b32 v192 offset:0\n\t"
"v_cvt_pk_bf16_f32 v210, -v210, -v214\n\t"
"v_mfma_f32_16x16x32_bf16 v[76:79], a[60:63], a[172:175], v[76:79]\n\t"
"ds_write_addtid_b32 v208 offset:2048\n\t"
"v_cvt_pk_bf16_f32 v216, v216, v220\n\t"
"v_mfma_f32_16x16x32_bf16 v[60:63], a[48:51], a[156:159], v[60:63]\n\t"
"ds_write_addtid_b32 v210 offset:3072\n\t"
"v_cvt_pk_bf16_f32 v219, v219, v223\n\t"
"v_mfma_f32_16x16x32_bf16 v[60:63], a[52:55], a[172:175], v[60:63]\n\t"
"s_waitcnt vmcnt(12) lgkmcnt(9)\n\t"
"ds_write_addtid_b32 v219 offset:15520\n\t"
"v_mfma_f32_16x16x32_bf16 v[144:147], a[72:75], a[144:147], v[144:147]\n\t"
"ds_write_addtid_b32 v218 offset:11392\n\t"
"v_mfma_f32_16x16x32_bf16 v[144:147], a[76:79], a[160:163], v[144:147]\n\t"
"ds_write_addtid_b32 v217 offset:14496\n\t"
"v_mfma_f32_16x16x32_bf16 v[160:163], a[80:83], a[144:147], v[160:163]\n\t"
"ds_write_addtid_b32 v216 offset:10368\n\t"
"v_mfma_f32_16x16x32_bf16 v[160:163], a[84:87], a[160:163], v[160:163]\n\t"
"ds_read_b128 a[156:159], v224 offset:18336\n\t"
"v_mfma_f32_16x16x32_bf16 v[176:179], a[88:91], a[144:147], v[176:179]\n\t"
"v_mfma_f32_16x16x32_bf16 v[176:179], a[92:95], a[160:163], v[176:179]\n\t"
"ds_read_b128 a[172:175], v224 offset:20384\n\t"
"v_mfma_f32_16x16x32_bf16 v[180:183], a[88:91], a[148:151], v[180:183]\n\t"
"v_mfma_f32_16x16x32_bf16 v[180:183], a[92:95], a[164:167], v[180:183]\n\t"
"ds_read_b64_tr_b16 a[196:197], v251 offset:20992\n\t"
"v_mfma_f32_16x16x32_bf16 v[164:167], a[80:83], a[148:151], v[164:167]\n\t"
"ds_read_b64_tr_b16 a[204:205], v250 offset:20992\n\t"
"v_mfma_f32_16x16x32_bf16 v[164:167], a[84:87], a[164:167], v[164:167]\n\t"
"v_mfma_f32_16x16x32_bf16 v[148:151], a[72:75], a[148:151], v[148:151]\n\t"
"s_waitcnt lgkmcnt(2)\n\t"
"v_mfma_f32_16x16x32_bf16 v[148:151], a[76:79], a[164:167], v[148:151]\n\t"
"s_barrier\n\t"
"v_mfma_f32_16x16x32_bf16 v[152:155], a[72:75], a[152:155], v[152:155]\n\t"
"s_add_u32 m0, s26, 35584\n\t"
"ds_read_b128 a[144:147], v224 offset:0\n\t"
"ds_read_b128 a[160:163], v224 offset:2048\n\t"
"v_mfma_f32_16x16x32_bf16 v[152:155], a[76:79], a[168:171], v[152:155]\n\t"
"buffer_load_dwordx4 v236, s[12:15], s22 offen offset:0 nt lds\n\t"
"v_mfma_f32_16x16x32_bf16 v[168:171], a[80:83], a[152:155], v[168:171]\n\t"
"ds_read_b128 a[148:151], v224 offset:256\n\t"
"v_mfma_f32_16x16x32_bf16 v[168:171], a[84:87], a[168:171], v[168:171]\n\t"
"ds_read_b128 a[164:167], v224 offset:2304\n\t"
"v_mfma_f32_16x16x32_bf16 v[184:187], a[88:91], a[152:155], v[184:187]\n\t"
"v_mfma_f32_16x16x32_bf16 v[184:187], a[92:95], a[168:171], v[184:187]\n\t"
"buffer_load_dwordx4 v237, s[12:15], s22 offen offset:1024 nt lds\n\t"
"v_mfma_f32_16x16x32_bf16 v[188:191], a[88:91], a[156:159], v[188:191]\n\t"
"ds_read_b128 a[168:171], v224 offset:2560\n\t"
"v_mfma_f32_16x16x32_bf16 v[188:191], a[92:95], a[172:175], v[188:191]\n\t"
"ds_read_b128 a[152:155], v224 offset:512\n\t"
"v_mfma_f32_16x16x32_bf16 v[172:175], a[80:83], a[156:159], v[172:175]\n\t"
"v_mfma_f32_16x16x32_bf16 v[172:175], a[84:87], a[172:175], v[172:175]\n\t"
"buffer_load_dwordx4 v238, s[12:15], s22 offen offset:2048 nt lds\n\t"
"v_mfma_f32_16x16x32_bf16 v[156:159], a[72:75], a[156:159], v[156:159]\n\t"
"ds_read_b128 a[156:159], v224 offset:768\n\t"
"v_mfma_f32_16x16x32_bf16 v[156:159], a[76:79], a[172:175], v[156:159]\n\t"
"s_waitcnt lgkmcnt(5)\n\t"
"ds_read_b128 a[172:175], v224 offset:2816\n\t"
"v_mfma_f32_4x4x4_16b_bf16 v[200:203], a[252:253], a[178:179], 0\n\t"
"v_mfma_f32_4x4x4_16b_bf16 v[204:207], a[252:253], a[186:187], 0\n\t"
"buffer_load_dwordx4 v239, s[12:15], s22 offen offset:3072 nt lds\n\t"
"v_mfma_f32_4x4x4_16b_bf16 v[216:219], a[252:253], a[194:195], 0\n\t"
"ds_read_b64_tr_b16 a[178:179], v248 offset:128\n\t"
"v_mfma_f32_4x4x4_16b_bf16 v[220:223], a[252:253], a[202:203], 0\n\t"
"ds_read_b64_tr_b16 a[186:187], v249 offset:128\n\t"
"v_mfma_f32_4x4x4_16b_bf16 v[192:195], a[254:255], a[176:177], v[200:203]\n\t"
"ds_read_b64_tr_b16 a[194:195], v250 offset:128\n\t"
"v_mfma_f32_4x4x4_16b_bf16 v[196:199], a[254:255], a[184:185], v[204:207]\n\t"
"s_mov_b64 exec, s[36:37]\n\t"
"buffer_load_dwordx4 v240, s[12:15], s22 offen offset:3584 nt lds\n\t"
"s_mov_b64 exec, -1\n\t"
"v_mfma_f32_4x4x4_16b_bf16 v[200:203], a[254:255], a[180:181], v[200:203]\n\t"
"ds_read_b64_tr_b16 a[202:203], v251 offset:128\n\t"
"v_mfma_f32_4x4x4_16b_bf16 v[204:207], a[254:255], a[188:189], v[204:207]\n\t"
"ds_read_b64_tr_b16 a[176:177], v248 offset:0\n\t"
"v_mfma_f32_4x4x4_16b_bf16 v[208:211], a[254:255], a[192:193], v[216:219]\n\t"
"ds_read_b64_tr_b16 a[184:185], v249 offset:0\n\t"
"v_mfma_f32_4x4x4_16b_bf16 v[212:215], a[254:255], a[200:201], v[220:223]\n\t"
"s_lshl_b32 m0, s73, 8\n\t"
"v_mfma_f32_4x4x4_16b_bf16 v[216:219], a[254:255], a[196:197], v[216:219]\n\t"
"ds_read_b64_tr_b16 a[180:181], v249 offset:256\n\t"
"v_mfma_f32_4x4x4_16b_bf16 v[220:223], a[254:255], a[204:205], v[220:223]\n\t"
"s_waitcnt vmcnt(11) lgkmcnt(7)\n\t"
"ds_read_b64_tr_b16 a[188:189], v248 offset:256\n\t"
"v_mfma_f32_16x16x32_bf16 v[48:51], a[96:99], a[144:147], v[48:51]\n\t"
"ds_read_b64_tr_b16 a[192:193], v250 offset:0\n\t"
"v_mfma_f32_16x16x32_bf16 v[48:51], a[100:103], a[160:163], v[48:51]\n\t"
"v_cvt_pk_bf16_f32 v193, -v193, -v197\n\t"
"v_cvt_pk_bf16_f32 v195, -v195, -v199\n\t"
"v_mfma_f32_16x16x32_bf16 v[64:67], a[104:107], a[144:147], v[64:67]\n\t"
"ds_read_b64_tr_b16 a[200:201], v251 offset:0\n\t"
"v_mfma_f32_16x16x32_bf16 v[64:67], a[108:111], a[160:163], v[64:67]\n\t"
"ds_write_addtid_b32 v193 offset:20672\n\t"
"v_cvt_pk_bf16_f32 v194, -v194, -v198\n\t"
"v_mfma_f32_16x16x32_bf16 v[80:83], a[112:115], a[144:147], v[80:83]\n\t"
"ds_read_b128 a[144:147], v224 offset:1024\n\t"
"v_cvt_pk_bf16_f32 v201, v201, v205\n\t"
"v_mfma_f32_16x16x32_bf16 v[80:83], a[116:119], a[160:163], v[80:83]\n\t"
"v_cvt_pk_bf16_f32 v209, -v209, -v213\n\t"
"v_mfma_f32_16x16x32_bf16 v[84:87], a[112:115], a[148:151], v[84:87]\n\t"
"ds_read_b128 a[160:163], v224 offset:3072\n\t"
"v_mfma_f32_16x16x32_bf16 v[84:87], a[116:119], a[164:167], v[84:87]\n\t"
"ds_write_addtid_b32 v201 offset:28992\n\t"
"v_cvt_pk_bf16_f32 v217, v217, v221\n\t"
"v_mfma_f32_16x16x32_bf16 v[68:71], a[104:107], a[148:151], v[68:71]\n\t"
"ds_write_addtid_b32 v195 offset:21696\n\t"
"v_cvt_pk_bf16_f32 v203, v203, v207\n\t"
"v_mfma_f32_16x16x32_bf16 v[68:71], a[108:111], a[164:167], v[68:71]\n\t"
"v_cvt_pk_bf16_f32 v200, v200, v204\n\t"
"v_cvt_pk_bf16_f32 v216, v216, v220\n\t"
"v_mfma_f32_16x16x32_bf16 v[52:55], a[96:99], a[148:151], v[52:55]\n\t"
"ds_read_b128 a[148:151], v224 offset:1280\n\t"
"v_mfma_f32_16x16x32_bf16 v[52:55], a[100:103], a[164:167], v[52:55]\n\t"
"ds_read_b128 a[164:167], v224 offset:3328\n\t"
"v_mfma_f32_16x16x32_bf16 v[56:59], a[96:99], a[152:155], v[56:59]\n\t"
"ds_write_addtid_b32 v194 offset:17568\n\t"
"v_cvt_pk_bf16_f32 v211, -v211, -v215\n\t"
"v_mfma_f32_16x16x32_bf16 v[56:59], a[100:103], a[168:171], v[56:59]\n\t"
"ds_write_addtid_b32 v211 offset:23744\n\t"
"v_cvt_pk_bf16_f32 v219, v219, v223\n\t"
"v_mfma_f32_16x16x32_bf16 v[72:75], a[104:107], a[152:155], v[72:75]\n\t"
"ds_write_addtid_b32 v209 offset:22720\n\t"
"v_cvt_pk_bf16_f32 v208, -v208, -v212\n\t"
"v_mfma_f32_16x16x32_bf16 v[72:75], a[108:111], a[168:171], v[72:75]\n\t"
"ds_write_addtid_b32 v203 offset:30016\n\t"
"v_cvt_pk_bf16_f32 v218, v218, v222\n\t"
"v_mfma_f32_16x16x32_bf16 v[88:91], a[112:115], a[152:155], v[88:91]\n\t"
"ds_read_b128 a[152:155], v224 offset:1536\n\t"
"v_cvt_pk_bf16_f32 v210, -v210, -v214\n\t"
"v_mfma_f32_16x16x32_bf16 v[88:91], a[116:119], a[168:171], v[88:91]\n\t"
"ds_read_b128 a[168:171], v224 offset:3584\n\t"
"v_cvt_pk_bf16_f32 v192, -v192, -v196\n\t"
"v_mfma_f32_16x16x32_bf16 v[92:95], a[112:115], a[156:159], v[92:95]\n\t"
"ds_write_addtid_b32 v217 offset:31040\n\t"
"v_mfma_f32_16x16x32_bf16 v[92:95], a[116:119], a[172:175], v[92:95]\n\t"
"ds_write_addtid_b32 v216 offset:26912\n\t"
"v_mfma_f32_16x16x32_bf16 v[76:79], a[104:107], a[156:159], v[76:79]\n\t"
"ds_write_addtid_b32 v200 offset:24864\n\t"
"v_cvt_pk_bf16_f32 v202, v202, v206\n\t"
"v_mfma_f32_16x16x32_bf16 v[76:79], a[108:111], a[172:175], v[76:79]\n\t"
"ds_write_addtid_b32 v210 offset:19616\n\t"
"v_mfma_f32_16x16x32_bf16 v[60:63], a[96:99], a[156:159], v[60:63]\n\t"
"ds_write_addtid_b32 v208 offset:18592\n\t"
"v_mfma_f32_16x16x32_bf16 v[60:63], a[100:103], a[172:175], v[60:63]\n\t"
"s_waitcnt vmcnt(5) lgkmcnt(9)\n\t"
"ds_write_addtid_b32 v218 offset:27936\n\t"
"v_mfma_f32_16x16x32_bf16 v[144:147], a[120:123], a[144:147], v[144:147]\n\t"
"ds_write_addtid_b32 v219 offset:32064\n\t"
"v_mfma_f32_16x16x32_bf16 v[144:147], a[124:127], a[160:163], v[144:147]\n\t"
"ds_write_addtid_b32 v202 offset:25888\n\t"
"v_mfma_f32_16x16x32_bf16 v[160:163], a[128:131], a[144:147], v[160:163]\n\t"
"ds_write_addtid_b32 v192 offset:16544\n\t"
"v_mfma_f32_16x16x32_bf16 v[160:163], a[132:135], a[160:163], v[160:163]\n\t"
"ds_read_b128 a[156:159], v224 offset:1792\n\t"
"v_mfma_f32_16x16x32_bf16 v[176:179], a[136:139], a[144:147], v[176:179]\n\t"
"v_mfma_f32_16x16x32_bf16 v[176:179], a[140:143], a[160:163], v[176:179]\n\t"
"ds_read_b128 a[172:175], v224 offset:3840\n\t"
"v_mfma_f32_16x16x32_bf16 v[180:183], a[136:139], a[148:151], v[180:183]\n\t"
"v_mfma_f32_16x16x32_bf16 v[180:183], a[140:143], a[164:167], v[180:183]\n\t"
"ds_read_b64_tr_b16 a[196:197], v251 offset:256\n\t"
"v_mfma_f32_16x16x32_bf16 v[164:167], a[128:131], a[148:151], v[164:167]\n\t"
"ds_read_b64_tr_b16 a[204:205], v250 offset:256\n\t"
"v_mfma_f32_16x16x32_bf16 v[164:167], a[132:135], a[164:167], v[164:167]\n\t"
"v_mfma_f32_16x16x32_bf16 v[148:151], a[120:123], a[148:151], v[148:151]\n\t"
"s_waitcnt lgkmcnt(2)\n\t"
"v_mfma_f32_16x16x32_bf16 v[148:151], a[124:127], a[164:167], v[148:151]\n\t"
"s_barrier\n\t"
"v_mfma_f32_16x16x32_bf16 v[152:155], a[120:123], a[152:155], v[152:155]\n\t"
"ds_read_b128 a[160:163], v224 offset:18592\n\t"
"v_mfma_f32_16x16x32_bf16 v[152:155], a[124:127], a[168:171], v[152:155]\n\t"
"ds_read_b128 a[144:147], v224 offset:16544\n\t"
"v_mfma_f32_16x16x32_bf16 v[168:171], a[128:131], a[152:155], v[168:171]\n\t"
"ds_read_b128 a[148:151], v224 offset:16800\n\t"
"v_mfma_f32_16x16x32_bf16 v[168:171], a[132:135], a[168:171], v[168:171]\n\t"
"ds_read_b128 a[164:167], v224 offset:18848\n\t"
"v_mfma_f32_16x16x32_bf16 v[184:187], a[136:139], a[152:155], v[184:187]\n\t"
"v_mfma_f32_16x16x32_bf16 v[184:187], a[140:143], a[168:171], v[184:187]\n\t"
"v_mfma_f32_16x16x32_bf16 v[188:191], a[136:139], a[156:159], v[188:191]\n\t"
"ds_read_b128 a[152:155], v224 offset:17056\n\t"
"v_mfma_f32_16x16x32_bf16 v[188:191], a[140:143], a[172:175], v[188:191]\n\t"
"ds_read_b128 a[168:171], v224 offset:19104\n\t"
"v_mfma_f32_16x16x32_bf16 v[172:175], a[128:131], a[156:159], v[172:175]\n\t"
"v_mfma_f32_16x16x32_bf16 v[172:175], a[132:135], a[172:175], v[172:175]\n\t"
"v_mfma_f32_16x16x32_bf16 v[156:159], a[120:123], a[156:159], v[156:159]\n\t"
"ds_read_b128 a[156:159], v224 offset:17312\n\t"
"v_mfma_f32_16x16x32_bf16 v[156:159], a[124:127], a[172:175], v[156:159]\n\t"
"s_waitcnt lgkmcnt(5)\n\t"
"ds_read_b128 a[172:175], v224 offset:19360\n\t"
"v_mfma_f32_4x4x4_16b_bf16 v[200:203], a[252:253], a[178:179], 0\n\t"
"ds_read_b64_tr_b16 a[178:179], v248 offset:41600\n\t"
"v_mfma_f32_4x4x4_16b_bf16 v[204:207], a[252:253], a[186:187], 0\n\t"
"ds_read_b64_tr_b16 a[186:187], v249 offset:41600\n\t"
"v_mfma_f32_4x4x4_16b_bf16 v[216:219], a[252:253], a[194:195], 0\n\t"
"ds_read_b64_tr_b16 a[194:195], v250 offset:41600\n\t"
"v_mfma_f32_4x4x4_16b_bf16 v[220:223], a[252:253], a[202:203], 0\n\t"
".p2align 3\n\t"
"ds_read_b64_tr_b16 a[202:203], v251 offset:41600\n\t"
"v_pk_fma_f32 v[48:49], v[144:145], s[90:91], v[48:49]\n\t"
"v_pk_fma_f32 v[50:51], v[146:147], s[90:91], v[50:51]\n\t"
"v_pk_add_f32 v[144:145], v[48:49], v[144:145] neg_lo:[0,1] neg_hi:[0,1]\n\t"
"v_pk_add_f32 v[146:147], v[50:51], v[146:147] neg_lo:[0,1] neg_hi:[0,1]\n\t"
"v_pk_fma_f32 v[64:65], v[160:161], s[90:91], v[64:65]\n\t"
"v_pk_fma_f32 v[66:67], v[162:163], s[90:91], v[66:67]\n\t"
"v_pk_add_f32 v[160:161], v[64:65], v[160:161] neg_lo:[0,1] neg_hi:[0,1]\n\t"
"v_pk_add_f32 v[162:163], v[66:67], v[162:163] neg_lo:[0,1] neg_hi:[0,1]\n\t"
"v_pk_fma_f32 v[80:81], v[176:177], s[90:91], v[80:81]\n\t"
"v_pk_fma_f32 v[82:83], v[178:179], s[90:91], v[82:83]\n\t"
"v_pk_add_f32 v[176:177], v[80:81], v[176:177] neg_lo:[0,1] neg_hi:[0,1]\n\t"
"v_pk_add_f32 v[178:179], v[82:83], v[178:179] neg_lo:[0,1] neg_hi:[0,1]\n\t"
"v_pk_fma_f32 v[84:85], v[180:181], s[90:91], v[84:85]\n\t"
"v_pk_fma_f32 v[86:87], v[182:183], s[90:91], v[86:87]\n\t"
"v_pk_add_f32 v[180:181], v[84:85], v[180:181] neg_lo:[0,1] neg_hi:[0,1]\n\t"
"v_pk_add_f32 v[182:183], v[86:87], v[182:183] neg_lo:[0,1] neg_hi:[0,1]\n\t"
"v_pk_fma_f32 v[68:69], v[164:165], s[90:91], v[68:69]\n\t"
"v_pk_fma_f32 v[70:71], v[166:167], s[90:91], v[70:71]\n\t"
"v_pk_add_f32 v[164:165], v[68:69], v[164:165] neg_lo:[0,1] neg_hi:[0,1]\n\t"
"v_pk_add_f32 v[166:167], v[70:71], v[166:167] neg_lo:[0,1] neg_hi:[0,1]\n\t"
"v_pk_fma_f32 v[52:53], v[148:149], s[90:91], v[52:53]\n\t"
"v_pk_fma_f32 v[54:55], v[150:151], s[90:91], v[54:55]\n\t"
"v_pk_add_f32 v[148:149], v[52:53], v[148:149] neg_lo:[0,1] neg_hi:[0,1]\n\t"
"v_pk_add_f32 v[150:151], v[54:55], v[150:151] neg_lo:[0,1] neg_hi:[0,1]\n\t"
"v_pk_fma_f32 v[56:57], v[152:153], s[90:91], v[56:57]\n\t"
"v_pk_fma_f32 v[58:59], v[154:155], s[90:91], v[58:59]\n\t"
"v_pk_add_f32 v[152:153], v[56:57], v[152:153] neg_lo:[0,1] neg_hi:[0,1]\n\t"
"v_pk_add_f32 v[154:155], v[58:59], v[154:155] neg_lo:[0,1] neg_hi:[0,1]\n\t"
"v_pk_fma_f32 v[72:73], v[168:169], s[90:91], v[72:73]\n\t"
"v_pk_fma_f32 v[74:75], v[170:171], s[90:91], v[74:75]\n\t"
"v_pk_add_f32 v[168:169], v[72:73], v[168:169] neg_lo:[0,1] neg_hi:[0,1]\n\t"
"v_pk_add_f32 v[170:171], v[74:75], v[170:171] neg_lo:[0,1] neg_hi:[0,1]\n\t"
"v_pk_fma_f32 v[88:89], v[184:185], s[90:91], v[88:89]\n\t"
"v_pk_fma_f32 v[90:91], v[186:187], s[90:91], v[90:91]\n\t"
"v_pk_add_f32 v[184:185], v[88:89], v[184:185] neg_lo:[0,1] neg_hi:[0,1]\n\t"
"v_pk_add_f32 v[186:187], v[90:91], v[186:187] neg_lo:[0,1] neg_hi:[0,1]\n\t"
"v_pk_fma_f32 v[92:93], v[188:189], s[90:91], v[92:93]\n\t"
"v_pk_fma_f32 v[94:95], v[190:191], s[90:91], v[94:95]\n\t"
"v_pk_add_f32 v[188:189], v[92:93], v[188:189] neg_lo:[0,1] neg_hi:[0,1]\n\t"
"v_pk_add_f32 v[190:191], v[94:95], v[190:191] neg_lo:[0,1] neg_hi:[0,1]\n\t"
"v_pk_fma_f32 v[76:77], v[172:173], s[90:91], v[76:77]\n\t"
"v_pk_fma_f32 v[78:79], v[174:175], s[90:91], v[78:79]\n\t"
"v_pk_add_f32 v[172:173], v[76:77], v[172:173] neg_lo:[0,1] neg_hi:[0,1]\n\t"
"v_pk_add_f32 v[174:175], v[78:79], v[174:175] neg_lo:[0,1] neg_hi:[0,1]\n\t"
"v_pk_fma_f32 v[60:61], v[156:157], s[90:91], v[60:61]\n\t"
"v_pk_fma_f32 v[62:63], v[158:159], s[90:91], v[62:63]\n\t"
"v_pk_add_f32 v[156:157], v[60:61], v[156:157] neg_lo:[0,1] neg_hi:[0,1]\n\t"
"v_pk_add_f32 v[158:159], v[62:63], v[158:159] neg_lo:[0,1] neg_hi:[0,1]\n\t"
"v_cvt_pk_bf16_f32 v64, v64, v65\n\t"
"v_mfma_f32_4x4x4_16b_bf16 v[192:195], a[254:255], a[176:177], v[200:203]\n\t"
"ds_read_b64_tr_b16 a[176:177], v248 offset:41472\n\t"
"v_cvt_pk_bf16_f32 v48, v48, v49\n\t"
"v_cvt_pk_bf16_f32 v68, v68, v69\n\t"
"v_mfma_f32_4x4x4_16b_bf16 v[196:199], a[254:255], a[184:185], v[204:207]\n\t"
"ds_read_b64_tr_b16 a[184:185], v249 offset:41472\n\t"
"v_cvt_pk_bf16_f32 v52, v52, v53\n\t"
"v_cvt_pk_bf16_f32 v65, v66, v67\n\t"
"v_mfma_f32_4x4x4_16b_bf16 v[200:203], a[254:255], a[180:181], v[200:203]\n\t"
"ds_read_b64_tr_b16 a[180:181], v249 offset:41728\n\t"
"v_cvt_pk_bf16_f32 v53, v54, v55\n\t"
"v_mfma_f32_4x4x4_16b_bf16 v[204:207], a[254:255], a[188:189], v[204:207]\n\t"
"v_cvt_pk_bf16_f32 v49, v50, v51\n\t"
"v_mfma_f32_4x4x4_16b_bf16 v[208:211], a[254:255], a[192:193], v[216:219]\n\t"
"ds_write2st64_b64 v228, v[48:49], v[52:53] offset0:0 offset1:13\n\t"
"v_mfma_f32_4x4x4_16b_bf16 v[212:215], a[254:255], a[200:201], v[220:223]\n\t"
"v_cvt_pk_bf16_f32 v69, v70, v71\n\t"
"v_mfma_f32_4x4x4_16b_bf16 v[216:219], a[254:255], a[196:197], v[216:219]\n\t"
"v_cvt_pk_bf16_f32 v56, v56, v57\n\t"
"v_mfma_f32_4x4x4_16b_bf16 v[220:223], a[254:255], a[204:205], v[220:223]\n\t"
"ds_write2st64_b64 v246, v[64:65], v[68:69] offset0:0 offset1:13\n\t"
"v_mfma_f32_16x16x32_bf16 v[0:3], a[0:3], a[144:147], v[0:3]\n\t"
"v_cvt_pk_bf16_f32 v84, v84, v85\n\t"
"v_cvt_pk_bf16_f32 v60, v60, v61\n\t"
"v_mfma_f32_16x16x32_bf16 v[0:3], a[4:7], a[160:163], v[0:3]\n\t"
"v_cvt_pk_bf16_f32 v80, v80, v81\n\t"
"v_cvt_pk_bf16_f32 v61, v62, v63\n\t"
"v_mfma_f32_16x16x32_bf16 v[16:19], a[8:11], a[144:147], v[16:19]\n\t"
"v_cvt_pk_bf16_f32 v195, -v195, -v199\n\t"
"v_cvt_pk_bf16_f32 v57, v58, v59\n\t"
"v_mfma_f32_16x16x32_bf16 v[16:19], a[12:15], a[160:163], v[16:19]\n\t"
"ds_read_b64_tr_b16 a[188:189], v248 offset:41728\n\t"
"ds_write2st64_b64 v228, v[56:57], v[60:61] offset0:26 offset1:39\n\t"
"v_mfma_f32_16x16x32_bf16 v[32:35], a[16:19], a[144:147], v[32:35]\n\t"
"v_cvt_pk_bf16_f32 v208, -v208, -v212\n\t"
"v_cvt_pk_bf16_f32 v81, v82, v83\n\t"
"v_mfma_f32_16x16x32_bf16 v[32:35], a[20:23], a[160:163], v[32:35]\n\t"
"s_waitcnt lgkmcnt(4)\n\t"
"v_cvt_pk_bf16_f32 v201, v201, v205\n\t"
"v_cvt_pk_bf16_f32 v76, v76, v77\n\t"
"v_mfma_f32_16x16x32_bf16 v[36:39], a[16:19], a[148:151], v[36:39]\n\t"
"v_cvt_pk_bf16_f32 v202, v202, v206\n\t"
"v_cvt_pk_bf16_f32 v85, v86, v87\n\t"
"v_mfma_f32_16x16x32_bf16 v[36:39], a[20:23], a[164:167], v[36:39]\n\t"
"s_nop 0\n\t"
"ds_read_b128 a[144:147], v224 offset:17568\n\t"
"ds_read_b128 a[160:163], v224 offset:19616\n\t"
"ds_write2st64_b64 v247, v[80:81], v[84:85] offset0:0 offset1:13\n\t"
"v_mfma_f32_16x16x32_bf16 v[20:23], a[8:11], a[148:151], v[20:23]\n\t"
"v_cvt_pk_bf16_f32 v72, v72, v73\n\t"
"v_cvt_pk_bf16_f32 v73, v74, v75\n\t"
"v_mfma_f32_16x16x32_bf16 v[20:23], a[12:15], a[164:167], v[20:23]\n\t"
"v_cvt_pk_bf16_f32 v211, -v211, -v215\n\t"
"v_cvt_pk_bf16_f32 v219, v219, v223\n\t"
"v_mfma_f32_16x16x32_bf16 v[4:7], a[0:3], a[148:151], v[4:7]\n\t"
"v_cvt_pk_bf16_f32 v200, v200, v204\n\t"
"v_cvt_pk_bf16_f32 v77, v78, v79\n\t"
"v_mfma_f32_16x16x32_bf16 v[4:7], a[4:7], a[164:167], v[4:7]\n\t"
"s_nop 0\n\t"
"ds_read_b128 a[148:151], v224 offset:17824\n\t"
"ds_read_b128 a[164:167], v224 offset:19872\n\t"
"ds_write_addtid_b32 v195 offset:5152\n\t"
"ds_write_addtid_b32 v211 offset:7200\n\t"
"v_mfma_f32_16x16x32_bf16 v[8:11], a[0:3], a[152:155], v[8:11]\n\t"
"ds_write_addtid_b32 v201 offset:12448\n\t"
"v_cvt_pk_bf16_f32 v209, -v209, -v213\n\t"
"v_mfma_f32_16x16x32_bf16 v[8:11], a[4:7], a[168:171], v[8:11]\n\t"
"ds_write_addtid_b32 v209 offset:6176\n\t"
"v_cvt_pk_bf16_f32 v194, -v194, -v198\n\t"
"v_mfma_f32_16x16x32_bf16 v[24:27], a[8:11], a[152:155], v[24:27]\n\t"
"ds_write_addtid_b32 v194 offset:1024\n\t"
"v_cvt_pk_bf16_f32 v92, v92, v93\n\t"
"v_mfma_f32_16x16x32_bf16 v[24:27], a[12:15], a[168:171], v[24:27]\n\t"
"s_nop 0\n\t"
"ds_write_addtid_b32 v202 offset:9344\n\t"
"ds_write_addtid_b32 v208 offset:2048\n\t"
"ds_write_addtid_b32 v219 offset:15520\n\t"
"v_mfma_f32_16x16x32_bf16 v[40:43], a[16:19], a[152:155], v[40:43]\n\t"
"v_cvt_pk_bf16_f32 v192, -v192, -v196\n\t"
"v_cvt_pk_bf16_f32 v93, v94, v95\n\t"
"v_mfma_f32_16x16x32_bf16 v[40:43], a[20:23], a[168:171], v[40:43]\n\t"
"ds_read_b128 a[152:155], v224 offset:18080\n\t"
"ds_write2st64_b64 v246, v[72:73], v[76:77] offset0:26 offset1:39\n\t"
"v_mfma_f32_16x16x32_bf16 v[44:47], a[16:19], a[156:159], v[44:47]\n\t"
"v_cvt_pk_bf16_f32 v218, v218, v222\n\t"
"v_cvt_pk_bf16_f32 v88, v88, v89\n\t"
"v_mfma_f32_16x16x32_bf16 v[44:47], a[20:23], a[172:175], v[44:47]\n\t"
"buffer_load_dwordx4 a[16:19], off, s[92:95], s59 offset:0 sc1\n\t"
"v_mfma_f32_16x16x32_bf16 v[28:31], a[8:11], a[156:159], v[28:31]\n\t"
"v_cvt_pk_bf16_f32 v216, v216, v220\n\t"
"v_cvt_pk_bf16_f32 v89, v90, v91\n\t"
"v_mfma_f32_16x16x32_bf16 v[28:31], a[12:15], a[172:175], v[28:31]\n\t"
"ds_read_b128 a[168:171], v224 offset:20128\n\t"
"ds_write2st64_b64 v247, v[88:89], v[92:93] offset0:26 offset1:39\n\t"
"v_mfma_f32_16x16x32_bf16 v[12:15], a[0:3], a[156:159], v[12:15]\n\t"
"v_cvt_pk_bf16_f32 v210, -v210, -v214\n\t"
"v_cvt_pk_bf16_f32 v217, v217, v221\n\t"
"v_mfma_f32_16x16x32_bf16 v[12:15], a[4:7], a[172:175], v[12:15]\n\t"
"s_waitcnt lgkmcnt(8)\n\t"
"buffer_load_dwordx4 a[20:23], off, s[92:95], s59 offset:1024 sc1\n\t"
"v_mfma_f32_16x16x32_bf16 v[96:99], a[24:27], a[144:147], v[96:99]\n\t"
"s_nop 0\n\t"
"v_cvt_pk_bf16_f32 v193, -v193, -v197\n\t"
"v_cvt_pk_bf16_f32 v203, v203, v207\n\t"
"v_mfma_f32_16x16x32_bf16 v[96:99], a[28:31], a[160:163], v[96:99]\n\t"
"s_nop 0\n\t"
"ds_write_addtid_b32 v192 offset:0\n\t"
"ds_write_addtid_b32 v193 offset:4128\n\t"
"ds_write_addtid_b32 v200 offset:8320\n\t"
"ds_write_addtid_b32 v210 offset:3072\n\t"
"ds_write_addtid_b32 v216 offset:10368\n\t"
"ds_write_addtid_b32 v218 offset:11392\n\t"
"v_mfma_f32_16x16x32_bf16 v[112:115], a[32:35], a[144:147], v[112:115]\n\t"
"s_nop 0\n\t"
"ds_write_addtid_b32 v203 offset:13472\n\t"
"ds_write_addtid_b32 v217 offset:14496\n\t"
"v_mfma_f32_16x16x32_bf16 v[112:115], a[36:39], a[160:163], v[112:115]\n\t"
"buffer_load_dwordx4 a[12:15], off, s[96:99], s58 offset:3072 sc1\n\t"
"v_mfma_f32_16x16x32_bf16 v[128:131], a[40:43], a[144:147], v[128:131]\n\t"
"s_nop 0\n\t"
"ds_read_b128 a[156:159], v224 offset:18336\n\t"
"ds_read_b128 a[172:175], v224 offset:20384\n\t"
"v_mfma_f32_16x16x32_bf16 v[128:131], a[44:47], a[160:163], v[128:131]\n\t"
"buffer_load_dwordx4 a[8:11], off, s[96:99], s58 offset:2048 sc1\n\t"
"v_mfma_f32_16x16x32_bf16 v[132:135], a[40:43], a[148:151], v[132:135]\n\t"
"s_nop 0\n\t"
"ds_read_b64_tr_b16 a[192:193], v250 offset:41472\n\t"
"ds_read_b64_tr_b16 a[200:201], v251 offset:41472\n\t"
"v_mfma_f32_16x16x32_bf16 v[132:135], a[44:47], a[164:167], v[132:135]\n\t"
"buffer_load_dwordx4 a[0:3], off, s[96:99], s58 offset:0 sc1\n\t"
"v_mfma_f32_16x16x32_bf16 v[116:119], a[32:35], a[148:151], v[116:119]\n\t"
"s_nop 0\n\t"
"ds_read_b64_tr_b16 a[196:197], v251 offset:41728\n\t"
"ds_read_b64_tr_b16 a[204:205], v250 offset:41728\n\t"
"v_mfma_f32_16x16x32_bf16 v[116:119], a[36:39], a[164:167], v[116:119]\n\t"
"buffer_load_dwordx4 a[4:7], off, s[96:99], s58 offset:1024 sc1\n\t"
"v_mfma_f32_16x16x32_bf16 v[100:103], a[24:27], a[148:151], v[100:103]\n\t"
"s_waitcnt lgkmcnt(4)\n\t"
"v_mfma_f32_16x16x32_bf16 v[100:103], a[28:31], a[164:167], v[100:103]\n\t"
"s_barrier\n\t"
"v_mfma_f32_16x16x32_bf16 v[104:107], a[24:27], a[152:155], v[104:107]\n\t"
"s_nop 0\n\t"
"ds_read_b128 a[144:147], v224 offset:0\n\t"
"v_cvt_pk_bf16_f32 v144, v144, v145\n\t"
"v_mfma_f32_16x16x32_bf16 v[104:107], a[28:31], a[168:171], v[104:107]\n\t"
"s_nop 0\n\t"
"ds_read_b128 a[148:151], v224 offset:256\n\t"
"v_cvt_pk_bf16_f32 v180, v180, v181\n\t"
"v_mfma_f32_16x16x32_bf16 v[120:123], a[32:35], a[152:155], v[120:123]\n\t"
"s_nop 0\n\t"
"ds_read_b128 a[160:163], v224 offset:2048\n\t"
"v_cvt_pk_bf16_f32 v160, v160, v161\n\t"
"v_mfma_f32_16x16x32_bf16 v[120:123], a[36:39], a[168:171], v[120:123]\n\t"
"s_nop 0\n\t"
"ds_read_b128 a[164:167], v224 offset:2304\n\t"
"v_cvt_pk_bf16_f32 v161, v162, v163\n\t"
"v_mfma_f32_16x16x32_bf16 v[136:139], a[40:43], a[152:155], v[136:139]\n\t"
"s_nop 0\n\t"
"v_cvt_pk_bf16_f32 v145, v146, v147\n\t"
"v_cvt_pk_bf16_f32 v172, v172, v173\n\t"
"v_mfma_f32_16x16x32_bf16 v[136:139], a[44:47], a[168:171], v[136:139]\n\t"
"s_nop 0\n\t"
"v_cvt_pk_bf16_f32 v176, v176, v177\n\t"
"v_cvt_pk_bf16_f32 v168, v168, v169\n\t"
"v_mfma_f32_16x16x32_bf16 v[140:143], a[40:43], a[156:159], v[140:143]\n\t"
"s_nop 0\n\t"
"ds_read_b128 v[56:59], v229 offset:0\n\t"
"v_cvt_pk_bf16_f32 v148, v148, v149\n\t"
"v_mfma_f32_16x16x32_bf16 v[140:143], a[44:47], a[172:175], v[140:143]\n\t"
"s_nop 0\n\t"
"ds_read_b128 v[72:75], v230 offset:0\n\t"
"buffer_load_dwordx4 a[40:43], off, s[92:95], s58 offset:2048 sc1\n\t"
"v_mfma_f32_16x16x32_bf16 v[124:127], a[32:35], a[156:159], v[124:127]\n\t"
"s_nop 0\n\t"
"v_cvt_pk_bf16_f32 v177, v178, v179\n\t"
"v_cvt_pk_bf16_f32 v156, v156, v157\n\t"
"v_mfma_f32_16x16x32_bf16 v[124:127], a[36:39], a[172:175], v[124:127]\n\t"
"Lpad9_%=:\n\t"
"s_cbranch_execz Lret1_%=\n\t"
"buffer_load_dwordx4 a[44:47], off, s[92:95], s58 offset:3072 sc1\n\t"
"v_mfma_f32_16x16x32_bf16 v[108:111], a[24:27], a[156:159], v[108:111]\n\t"
"ds_read_b128 v[92:95], v231 offset:13312\n\t"
"v_cvt_pk_bf16_f32 v184, v184, v185\n\t"
"v_mfma_f32_16x16x32_bf16 v[108:111], a[28:31], a[172:175], v[108:111]\n\t"
"s_waitcnt lgkmcnt(5)\n\t"
"ds_read_b128 v[76:79], v230 offset:13312\n\t"
"v_cvt_pk_bf16_f32 v164, v164, v165\n\t"
"v_mfma_f32_4x4x4_16b_bf16 v[200:203], a[252:253], a[178:179], 0\n\t"
"buffer_load_dwordx4 a[36:39], off, s[92:95], s58 offset:1024 sc1\n\t"
"v_mfma_f32_4x4x4_16b_bf16 v[204:207], a[252:253], a[186:187], 0\n\t"
"v_cvt_pk_bf16_f32 v157, v158, v159\n\t"
"v_mfma_f32_4x4x4_16b_bf16 v[216:219], a[252:253], a[194:195], 0\n\t"
"ds_read_b128 v[88:91], v231 offset:0\n\t"
"v_mfma_f32_4x4x4_16b_bf16 v[220:223], a[252:253], a[202:203], 0\n\t"
"ds_read_b128 v[60:63], v229 offset:13312\n\t"
"buffer_load_dwordx4 a[32:35], off, s[92:95], s58 offset:0 sc1\n\t"
"v_mfma_f32_4x4x4_16b_bf16 v[192:195], a[254:255], a[176:177], v[200:203]\n\t"
"v_cvt_pk_bf16_f32 v149, v150, v151\n\t"
"v_cvt_pk_bf16_f32 v181, v182, v183\n\t"
"v_mfma_f32_4x4x4_16b_bf16 v[196:199], a[254:255], a[184:185], v[204:207]\n\t"
"ds_read_b128 a[156:159], v224 offset:768\n\t"
"v_mfma_f32_4x4x4_16b_bf16 v[200:203], a[254:255], a[180:181], v[200:203]\n\t"
"ds_read_b128 a[168:171], v224 offset:2560\n\t"
"buffer_load_dwordx4 a[24:27], off, s[96:99], s57 offset:2048 sc1\n\t"
"v_mfma_f32_4x4x4_16b_bf16 v[204:207], a[254:255], a[188:189], v[204:207]\n\t"
"v_cvt_pk_bf16_f32 v152, v152, v153\n\t"
"v_mfma_f32_4x4x4_16b_bf16 v[208:211], a[254:255], a[192:193], v[216:219]\n\t"
"ds_read_b128 a[152:155], v224 offset:512\n\t"
"v_mfma_f32_4x4x4_16b_bf16 v[212:215], a[254:255], a[200:201], v[220:223]\n\t"
"ds_read_b128 a[172:175], v224 offset:2816\n\t"
"s_waitcnt lgkmcnt(6)\n\t"
"buffer_store_dwordx4 v[56:59], v232, s[16:19], s24 offen nt\n\t"
"v_mfma_f32_4x4x4_16b_bf16 v[216:219], a[254:255], a[196:197], v[216:219]\n\t"
"v_cvt_pk_bf16_f32 v165, v166, v167\n\t"
"v_mfma_f32_4x4x4_16b_bf16 v[220:223], a[254:255], a[204:205], v[220:223]\n\t"
"s_waitcnt lgkmcnt(4)\n\t"
"s_barrier\n\t"
"v_mfma_f32_16x16x32_bf16 v[0:3], a[48:51], a[144:147], v[0:3]\n\t"
"ds_write2st64_b64 v228, v[144:145], v[148:149] offset0:0 offset1:13\n\t"
"v_mfma_f32_16x16x32_bf16 v[0:3], a[52:55], a[160:163], v[0:3]\n\t"
"buffer_load_dwordx4 a[28:31], off, s[96:99], s57 offset:3072 sc1\n\t"
"v_mfma_f32_16x16x32_bf16 v[16:19], a[56:59], a[144:147], v[16:19]\n\t"
"v_cvt_pk_bf16_f32 v203, v203, v207\n\t"
"v_cvt_pk_bf16_f32 v153, v154, v155\n\t"
"v_mfma_f32_16x16x32_bf16 v[16:19], a[60:63], a[160:163], v[16:19]\n\t"
"ds_write2st64_b64 v246, v[160:161], v[164:165] offset0:0 offset1:13\n\t"
"v_mfma_f32_16x16x32_bf16 v[32:35], a[64:67], a[144:147], v[32:35]\n\t"
"v_cvt_pk_bf16_f32 v208, -v208, -v212\n\t"
"v_cvt_pk_bf16_f32 v188, v188, v189\n\t"
"v_mfma_f32_16x16x32_bf16 v[32:35], a[68:71], a[160:163], v[32:35]\n\t"
"buffer_store_dwordx4 v[72:75], v233, s[16:19], s24 offen nt\n\t"
"v_mfma_f32_16x16x32_bf16 v[36:39], a[64:67], a[148:151], v[36:39]\n\t"
"v_cvt_pk_bf16_f32 v209, -v209, -v213\n\t"
"v_cvt_pk_bf16_f32 v218, v218, v222\n\t"
"v_mfma_f32_16x16x32_bf16 v[36:39], a[68:71], a[164:167], v[36:39]\n\t"
"s_nop 0\n\t"
"ds_read_b128 a[144:147], v224 offset:1024\n\t"
"ds_read_b128 a[160:163], v224 offset:3072\n\t"
"ds_write2st64_b64 v247, v[176:177], v[180:181] offset0:0 offset1:13\n\t"
"v_mfma_f32_16x16x32_bf16 v[20:23], a[56:59], a[148:151], v[20:23]\n\t"
"v_cvt_pk_bf16_f32 v194, -v194, -v198\n\t"
"v_cvt_pk_bf16_f32 v169, v170, v171\n\t"
"v_mfma_f32_16x16x32_bf16 v[20:23], a[60:63], a[164:167], v[20:23]\n\t"
"buffer_store_dwordx4 v[88:91], v234, s[16:19], s24 offen nt\n\t"
"v_mfma_f32_16x16x32_bf16 v[4:7], a[48:51], a[148:151], v[4:7]\n\t"
"v_cvt_pk_bf16_f32 v219, v219, v223\n\t"
"v_cvt_pk_bf16_f32 v173, v174, v175\n\t"
"v_mfma_f32_16x16x32_bf16 v[4:7], a[52:55], a[164:167], v[4:7]\n\t"
"s_waitcnt lgkmcnt(4)\n\t"
"ds_read_b128 a[148:151], v224 offset:1280\n\t"
"ds_read_b128 a[164:167], v224 offset:3328\n\t"
"ds_write2st64_b64 v246, v[168:169], v[172:173] offset0:26 offset1:39\n\t"
"v_mfma_f32_16x16x32_bf16 v[8:11], a[48:51], a[152:155], v[8:11]\n\t"
"v_cvt_pk_bf16_f32 v201, v201, v205\n\t"
"v_cvt_pk_bf16_f32 v216, v216, v220\n\t"
"v_mfma_f32_16x16x32_bf16 v[8:11], a[52:55], a[168:171], v[8:11]\n\t"
"buffer_store_dwordx4 v[60:63], v232, s[16:19], s27 offen nt\n\t"
"v_mfma_f32_16x16x32_bf16 v[24:27], a[56:59], a[152:155], v[24:27]\n\t"
"v_cvt_pk_bf16_f32 v200, v200, v204\n\t"
"v_cvt_pk_bf16_f32 v211, -v211, -v215\n\t"
"v_mfma_f32_16x16x32_bf16 v[24:27], a[60:63], a[168:171], v[24:27]\n\t"
"s_nop 0\n\t"
"ds_write_addtid_b32 v208 offset:18592\n\t"
"ds_write_addtid_b32 v209 offset:22720\n\t"
"ds_write_addtid_b32 v216 offset:26912\n\t"
"ds_write_addtid_b32 v219 offset:32064\n\t"
"ds_write2st64_b64 v228, v[152:153], v[156:157] offset0:26 offset1:39\n\t"
"v_mfma_f32_16x16x32_bf16 v[40:43], a[64:67], a[152:155], v[40:43]\n\t"
"v_cvt_pk_bf16_f32 v185, v186, v187\n\t"
"v_cvt_pk_bf16_f32 v189, v190, v191\n\t"
"v_mfma_f32_16x16x32_bf16 v[40:43], a[68:71], a[168:171], v[40:43]\n\t"
"v_cvt_pk_bf16_f32 v192, -v192, -v196\n\t"
"v_cvt_pk_bf16_f32 v210, -v210, -v214\n\t"
"v_mfma_f32_16x16x32_bf16 v[44:47], a[64:67], a[156:159], v[44:47]\n\t"
"buffer_load_dwordx4 a[64:67], off, s[92:95], s65 offset:0 sc1\n\t"
"v_mfma_f32_16x16x32_bf16 v[44:47], a[68:71], a[172:175], v[44:47]\n\t"
"ds_write_addtid_b32 v200 offset:24864\n\t"
"ds_write2st64_b64 v247, v[184:185], v[188:189] offset0:26 offset1:39\n\t"
"v_mfma_f32_16x16x32_bf16 v[28:31], a[56:59], a[156:159], v[28:31]\n\t"
"buffer_load_dwordx4 a[68:71], off, s[92:95], s65 offset:1024 sc1\n\t"
"v_mfma_f32_16x16x32_bf16 v[28:31], a[60:63], a[172:175], v[28:31]\n\t"
"v_cvt_pk_bf16_f32 v202, v202, v206\n\t"
"v_cvt_pk_bf16_f32 v217, v217, v221\n\t"
"v_mfma_f32_16x16x32_bf16 v[12:15], a[48:51], a[156:159], v[12:15]\n\t"
"v_cvt_pk_bf16_f32 v193, -v193, -v197\n\t"
"v_cvt_pk_bf16_f32 v195, -v195, -v199\n\t"
"v_mfma_f32_16x16x32_bf16 v[12:15], a[52:55], a[172:175], v[12:15]\n\t"
"s_waitcnt lgkmcnt(7)\n\t"
"ds_write_addtid_b32 v192 offset:16544\n\t"
"ds_write_addtid_b32 v201 offset:28992\n\t"
"ds_write_addtid_b32 v203 offset:30016\n\t"
"ds_write_addtid_b32 v211 offset:23744\n\t"
"ds_write_addtid_b32 v218 offset:27936\n\t"
"buffer_store_dwordx4 v[76:79], v233, s[16:19], s27 offen nt\n\t"
"v_mfma_f32_16x16x32_bf16 v[96:99], a[72:75], a[144:147], v[96:99]\n\t"
"s_nop 0\n\t"
"ds_write_addtid_b32 v210 offset:19616\n\t"
"ds_write_addtid_b32 v217 offset:31040\n\t"
"v_mfma_f32_16x16x32_bf16 v[96:99], a[76:79], a[160:163], v[96:99]\n\t"
"s_nop 0\n\t"
"ds_write_addtid_b32 v193 offset:20672\n\t"
"ds_write_addtid_b32 v194 offset:17568\n\t"
"v_mfma_f32_16x16x32_bf16 v[112:115], a[80:83], a[144:147], v[112:115]\n\t"
"s_nop 0\n\t"
"ds_write_addtid_b32 v195 offset:21696\n\t"
"ds_write_addtid_b32 v202 offset:25888\n\t"
"v_mfma_f32_16x16x32_bf16 v[112:115], a[84:87], a[160:163], v[112:115]\n\t"
"buffer_load_dwordx4 a[60:63], off, s[96:99], s64 offset:3072 sc1\n\t"
"buffer_load_dwordx4 a[56:59], off, s[96:99], s64 offset:2048 sc1\n\t"
"v_mfma_f32_16x16x32_bf16 v[128:131], a[88:91], a[144:147], v[128:131]\n\t"
"ds_read_b128 a[152:155], v224 offset:1536\n\t"
"v_accvgpr_read_b32 v248, a208\n\t"
"v_mfma_f32_16x16x32_bf16 v[128:131], a[92:95], a[160:163], v[128:131]\n\t"
"ds_read_b128 a[168:171], v224 offset:3584\n\t"
"buffer_load_dwordx4 a[48:51], off, s[96:99], s64 offset:0 sc1\n\t"
"v_mfma_f32_16x16x32_bf16 v[132:135], a[88:91], a[148:151], v[132:135]\n\t"
"s_nop 0\n\t"
"ds_read_b128 a[156:159], v224 offset:1792\n\t"
"ds_read_b128 a[172:175], v224 offset:3840\n\t"
"v_mfma_f32_16x16x32_bf16 v[132:135], a[92:95], a[164:167], v[132:135]\n\t"
"buffer_store_dwordx4 v[92:95], v234, s[16:19], s27 offen nt\n\t"
"v_mfma_f32_16x16x32_bf16 v[116:119], a[80:83], a[148:151], v[116:119]\n\t"
"v_add_u32 v248, 101632, v248\n\t"
"v_xor_b32 v249, 32, v248\n\t"
"v_mfma_f32_16x16x32_bf16 v[116:119], a[84:87], a[164:167], v[116:119]\n\t"
"v_xor_b32 v250, 64, v248\n\t"
"v_xor_b32 v251, 96, v248\n\t"
"v_mfma_f32_16x16x32_bf16 v[100:103], a[72:75], a[148:151], v[100:103]\n\t"
"buffer_load_dwordx4 a[52:55], off, s[96:99], s64 offset:1024 sc1\n\t"
"v_mfma_f32_16x16x32_bf16 v[100:103], a[76:79], a[164:167], v[100:103]\n\t"
"s_waitcnt lgkmcnt(0)\n\t"
"s_barrier\n\t"
"v_mfma_f32_16x16x32_bf16 v[104:107], a[72:75], a[152:155], v[104:107]\n\t"
"s_nop 0\n\t"
"ds_read_b128 a[144:147], v224 offset:16544\n\t"
"ds_read_b128 a[160:163], v224 offset:18592\n\t"
"v_mfma_f32_16x16x32_bf16 v[104:107], a[76:79], a[168:171], v[104:107]\n\t"
"s_nop 0\n\t"
"ds_read_b128 a[148:151], v224 offset:16800\n\t"
"ds_read_b128 a[164:167], v224 offset:18848\n\t"
"v_mfma_f32_16x16x32_bf16 v[120:123], a[80:83], a[152:155], v[120:123]\n\t"
"ds_read_b64_tr_b16 a[176:177], v248 offset:20736\n\t"
"v_mfma_f32_16x16x32_bf16 v[120:123], a[84:87], a[168:171], v[120:123]\n\t"
"ds_read_b64_tr_b16 a[178:179], v249 offset:20736\n\t"
"v_mfma_f32_16x16x32_bf16 v[136:139], a[88:91], a[152:155], v[136:139]\n\t"
"s_nop 0\n\t"
"ds_read_b64_tr_b16 a[180:181], v248 offset:21248\n\t"
"ds_read_b64_tr_b16 a[182:183], v249 offset:21248\n\t"
"v_mfma_f32_16x16x32_bf16 v[136:139], a[92:95], a[168:171], v[136:139]\n\t"
"s_nop 0\n\t"
"ds_read_b64_tr_b16 a[184:185], v250 offset:23040\n\t"
"ds_read_b64_tr_b16 a[186:187], v251 offset:23040\n\t"
"v_mfma_f32_16x16x32_bf16 v[140:143], a[88:91], a[156:159], v[140:143]\n\t"
"ds_read_b64_tr_b16 a[188:189], v250 offset:23552\n\t"
"v_mfma_f32_16x16x32_bf16 v[140:143], a[92:95], a[172:175], v[140:143]\n\t"
"s_nop 0\n\t"
"ds_read_b64_tr_b16 a[190:191], v251 offset:23552\n\t"
"ds_read_b64_tr_b16 a[192:193], v250 offset:20736\n\t"
"v_mfma_f32_16x16x32_bf16 v[124:127], a[80:83], a[156:159], v[124:127]\n\t"
"s_nop 0\n\t"
"ds_read_b64_tr_b16 a[194:195], v251 offset:20736\n\t"
"ds_read_b64_tr_b16 a[196:197], v250 offset:21248\n\t"
"v_mfma_f32_16x16x32_bf16 v[124:127], a[84:87], a[172:175], v[124:127]\n\t"
"s_waitcnt lgkmcnt(7)\n\t"
"ds_read_b64_tr_b16 a[198:199], v251 offset:21248\n\t"
"ds_read_b64_tr_b16 a[200:201], v248 offset:23040\n\t"
"v_mfma_f32_4x4x4_16b_bf16 v[192:195], a[210:211], a[176:177], 0\n\t"
"ds_read_b64_tr_b16 a[202:203], v249 offset:23040\n\t"
"v_mfma_f32_4x4x4_16b_bf16 v[196:199], a[210:211], a[178:179], 0\n\t"
"ds_read_b128 v[152:155], v229 offset:0\n\t"
"v_mfma_f32_4x4x4_16b_bf16 v[200:203], a[210:211], a[180:181], 0\n\t"
"ds_read_b128 a[168:171], v224 offset:19104\n\t"
"v_mfma_f32_4x4x4_16b_bf16 v[204:207], a[210:211], a[182:183], 0\n\t"
"buffer_load_dwordx4 a[88:91], off, s[92:95], s64 offset:2048 sc1\n\t"
"v_mfma_f32_16x16x32_bf16 v[108:111], a[72:75], a[156:159], v[108:111]\n\t"
"s_nop 0\n\t"
"ds_read_b128 a[156:159], v224 offset:17312\n\t"
"ds_read_b64_tr_b16 a[204:205], v248 offset:23552\n\t"
"v_mfma_f32_16x16x32_bf16 v[108:111], a[76:79], a[172:175], v[108:111]\n\t"
"buffer_load_dwordx4 a[92:95], off, s[92:95], s64 offset:3072 sc1\n\t"
"v_mfma_f32_16x16x32_bf16 v[0:3], a[96:99], a[144:147], v[0:3]\n\t"
"ds_read_b128 v[168:171], v230 offset:0\n\t"
"v_cvt_pk_bf16_f32 v194, v194, v198\n\t"
"v_mfma_f32_16x16x32_bf16 v[0:3], a[100:103], a[160:163], v[0:3]\n\t"
"v_cvt_pk_bf16_f32 v196, v192, v196\n\t"
"v_cvt_pk_bf16_f32 v195, v195, v199\n\t"
"v_mfma_f32_16x16x32_bf16 v[16:19], a[104:107], a[144:147], v[16:19]\n\t"
"ds_read_b128 a[172:175], v224 offset:19360\n\t"
"v_cvt_pk_bf16_f32 v199, v201, v205\n\t"
"v_mfma_f32_16x16x32_bf16 v[16:19], a[108:111], a[160:163], v[16:19]\n\t"
"s_waitcnt lgkmcnt(5)\n\t"
"buffer_store_dwordx4 v[152:155], v232, s[16:19], s20 offen nt\n\t"
"v_mfma_f32_16x16x32_bf16 v[32:35], a[112:115], a[144:147], v[32:35]\n\t"
"ds_read_b128 a[152:155], v224 offset:17056\n\t"
"v_cvt_pk_bf16_f32 v198, v200, v204\n\t"
"v_mfma_f32_16x16x32_bf16 v[32:35], a[116:119], a[160:163], v[32:35]\n\t"
"ds_read_b128 v[184:187], v231 offset:0\n\t"
"v_cvt_pk_bf16_f32 v197, v193, v197\n\t"
"v_mfma_f32_4x4x4_16b_bf16 v[208:211], a[210:211], a[184:185], 0\n\t"
"ds_write_addtid_b32 v198 offset:8320\n\t"
"v_mfma_f32_4x4x4_16b_bf16 v[212:215], a[210:211], a[186:187], 0\n\t"
"s_waitcnt lgkmcnt(4)\n\t"
"buffer_store_dwordx4 v[168:171], v233, s[16:19], s20 offen nt\n\t"
"v_mfma_f32_4x4x4_16b_bf16 v[216:219], a[210:211], a[188:189], 0\n\t"
"ds_read_b128 v[156:159], v229 offset:13312\n\t"
"v_mfma_f32_4x4x4_16b_bf16 v[220:223], a[210:211], a[190:191], 0\n\t"
"ds_read_b128 a[144:147], v224 offset:17568\n\t"
"v_cvt_pk_bf16_f32 v193, v203, v207\n\t"
"v_mfma_f32_16x16x32_bf16 v[36:39], a[112:115], a[148:151], v[36:39]\n\t"
"ds_write_addtid_b32 v196 offset:0\n\t"
"v_cvt_pk_bf16_f32 v192, v202, v206\n\t"
"v_mfma_f32_16x16x32_bf16 v[36:39], a[116:119], a[164:167], v[36:39]\n\t"
"buffer_load_dwordx4 a[84:87], off, s[92:95], s64 offset:1024 sc1\n\t"
"v_mfma_f32_16x16x32_bf16 v[20:23], a[104:107], a[148:151], v[20:23]\n\t"
"ds_write_addtid_b32 v199 offset:9344\n\t"
"v_cvt_pk_bf16_f32 v212, v208, v212\n\t"
"v_mfma_f32_16x16x32_bf16 v[20:23], a[108:111], a[164:167], v[20:23]\n\t"
"ds_read_b128 v[172:175], v230 offset:13312\n\t"
"v_cvt_pk_bf16_f32 v210, v210, v214\n\t"
"v_mfma_f32_16x16x32_bf16 v[4:7], a[96:99], a[148:151], v[4:7]\n\t"
"v_cvt_pk_bf16_f32 v208, v218, v222\n\t"
"v_cvt_pk_bf16_f32 v213, v209, v213\n\t"
"v_mfma_f32_16x16x32_bf16 v[4:7], a[100:103], a[164:167], v[4:7]\n\t"
"s_waitcnt lgkmcnt(6)\n\t"
"buffer_store_dwordx4 v[184:187], v234, s[16:19], s20 offen nt\n\t"
"v_cvt_pk_bf16_f32 v211, v211, v215\n\t"
"v_mfma_f32_4x4x4_16b_bf16 v[200:203], a[210:211], a[192:193], 0\n\t"
"ds_read_b128 a[148:151], v224 offset:17824\n\t"
"v_cvt_pk_bf16_f32 v215, v217, v221\n\t"
"v_mfma_f32_4x4x4_16b_bf16 v[204:207], a[210:211], a[194:195], 0\n\t"
"ds_write_addtid_b32 v197 offset:1024\n\t"
"v_cvt_pk_bf16_f32 v214, v216, v220\n\t"
"v_mfma_f32_4x4x4_16b_bf16 v[196:199], a[210:211], a[196:197], 0\n\t"
"v_cvt_pk_bf16_f32 v209, v219, v223\n\t"
"v_mfma_f32_4x4x4_16b_bf16 v[216:219], a[210:211], a[198:199], 0\n\t"
"buffer_load_dwordx4 a[80:83], off, s[92:95], s64 offset:0 sc1\n\t"
"v_mfma_f32_16x16x32_bf16 v[8:11], a[96:99], a[152:155], v[8:11]\n\t"
"s_nop 0\n\t"
"ds_read_b128 a[160:163], v224 offset:19616\n\t"
"ds_write_addtid_b32 v215 offset:13472\n\t"
"v_mfma_f32_16x16x32_bf16 v[8:11], a[100:103], a[168:171], v[8:11]\n\t"
"s_nop 0\n\t"
"ds_write_addtid_b32 v212 offset:4128\n\t"
"ds_read_b64_tr_b16 a[206:207], v249 offset:23552\n\t"
"v_mfma_f32_16x16x32_bf16 v[24:27], a[104:107], a[152:155], v[24:27]\n\t"
"v_cvt_pk_bf16_f32 v204, v200, v204\n\t"
"v_cvt_pk_bf16_f32 v202, v202, v206\n\t"
"v_mfma_f32_16x16x32_bf16 v[24:27], a[108:111], a[168:171], v[24:27]\n\t"
"s_waitcnt lgkmcnt(6)\n\t"
"buffer_store_dwordx4 v[156:159], v232, s[16:19], s25 offen nt\n\t"
"v_cvt_pk_bf16_f32 v203, v203, v207\n\t"
"v_mfma_f32_16x16x32_bf16 v[40:43], a[112:115], a[152:155], v[40:43]\n\t"
"ds_read_b128 v[188:191], v231 offset:13312\n\t"
"v_cvt_pk_bf16_f32 v206, v196, v216\n\t"
"v_mfma_f32_16x16x32_bf16 v[40:43], a[116:119], a[168:171], v[40:43]\n\t"
"ds_read_b128 a[164:167], v224 offset:19872\n\t"
"v_cvt_pk_bf16_f32 v207, v197, v217\n\t"
"v_mfma_f32_16x16x32_bf16 v[44:47], a[112:115], a[156:159], v[44:47]\n\t"
"v_cvt_pk_bf16_f32 v200, v198, v218\n\t"
"v_cvt_pk_bf16_f32 v205, v201, v205\n\t"
"v_mfma_f32_16x16x32_bf16 v[44:47], a[116:119], a[172:175], v[44:47]\n\t"
"s_waitcnt lgkmcnt(2)\n\t"
"ds_write_addtid_b32 v213 offset:5152\n\t"
"buffer_load_dwordx4 a[72:75], off, s[96:99], s63 offset:2048 sc1\n\t"
"v_mfma_f32_4x4x4_16b_bf16 v[220:223], a[210:211], a[200:201], 0\n\t"
"ds_write_addtid_b32 v214 offset:12448\n\t"
"v_mfma_f32_4x4x4_16b_bf16 v[212:215], a[210:211], a[202:203], 0\n\t"
"v_cvt_pk_bf16_f32 v201, v199, v219\n\t"
"v_mfma_f32_4x4x4_16b_bf16 v[196:199], a[210:211], a[204:205], 0\n\t"
"buffer_store_dwordx4 v[172:175], v233, s[16:19], s25 offen nt\n\t"
"v_mfma_f32_4x4x4_16b_bf16 v[216:219], a[210:211], a[206:207], 0\n\t"
"s_nop 0\n\t"
"ds_write_addtid_b32 v205 offset:3072\n\t"
"ds_write_addtid_b32 v207 offset:11392\n\t"
"v_mfma_f32_16x16x32_bf16 v[28:31], a[104:107], a[156:159], v[28:31]\n\t"
"s_nop 0\n\t"
"ds_write_addtid_b32 v204 offset:2048\n\t"
"ds_write_addtid_b32 v206 offset:10368\n\t"
"v_mfma_f32_16x16x32_bf16 v[28:31], a[108:111], a[172:175], v[28:31]\n\t"
"s_waitcnt lgkmcnt(7)\n\t"
"buffer_store_dwordx4 v[188:191], v234, s[16:19], s25 offen nt\n\t"
"v_mfma_f32_16x16x32_bf16 v[12:15], a[96:99], a[156:159], v[12:15]\n\t"
"v_cvt_pk_bf16_f32 v222, v222, v214\n\t"
"v_cvt_pk_bf16_f32 v213, v221, v213\n\t"
"v_mfma_f32_16x16x32_bf16 v[12:15], a[100:103], a[172:175], v[12:15]\n\t"
"v_cvt_pk_bf16_f32 v212, v220, v212\n\t"
"v_cvt_pk_bf16_f32 v223, v223, v215\n\t"
"v_mfma_f32_16x16x32_bf16 v[96:99], a[120:123], a[144:147], v[96:99]\n\t"
"ds_write_addtid_b32 v213 offset:7200\n\t"
"v_cvt_pk_bf16_f32 v214, v196, v216\n\t"
"v_mfma_f32_16x16x32_bf16 v[96:99], a[124:127], a[160:163], v[96:99]\n\t"
"ds_write_addtid_b32 v214 offset:14496\n\t"
"v_cvt_pk_bf16_f32 v215, v197, v217\n\t"
"v_mfma_f32_16x16x32_bf16 v[112:115], a[128:131], a[144:147], v[112:115]\n\t"
"s_nop 0\n\t"
"ds_write_addtid_b32 v212 offset:6176\n\t"
"ds_write_addtid_b32 v215 offset:15520\n\t"
"v_mfma_f32_16x16x32_bf16 v[112:115], a[132:135], a[160:163], v[112:115]\n\t"
"buffer_load_dwordx4 a[76:79], off, s[96:99], s63 offset:3072 sc1\n\t"
"v_mfma_f32_16x16x32_bf16 v[128:131], a[136:139], a[144:147], v[128:131]\n\t"
"ds_read_b128 a[152:155], v224 offset:18080\n\t"
"v_cvt_pk_bf16_f32 v221, v199, v219\n\t"
"v_mfma_f32_16x16x32_bf16 v[128:131], a[140:143], a[160:163], v[128:131]\n\t"
"s_waitcnt lgkmcnt(7)\n\t"
"ds_read_b128 a[172:175], v224 offset:20384\n\t"
"v_mfma_f32_16x16x32_bf16 v[132:135], a[136:139], a[148:151], v[132:135]\n\t"
"s_nop 0\n\t"
"ds_read_b128 a[168:171], v224 offset:20128\n\t"
"ds_read_b128 a[156:159], v224 offset:18336\n\t"
"v_mfma_f32_16x16x32_bf16 v[132:135], a[140:143], a[164:167], v[132:135]\n\t"
"buffer_load_dwordx4 a[112:115], off, s[92:95], s47 offset:0 sc1\n\t"
"v_mfma_f32_16x16x32_bf16 v[116:119], a[128:131], a[148:151], v[116:119]\n\t"
"v_cvt_pk_bf16_f32 v220, v198, v218\n\t"
"v_mfma_f32_16x16x32_bf16 v[116:119], a[132:135], a[164:167], v[116:119]\n\t"
"buffer_load_dwordx4 a[116:119], off, s[92:95], s47 offset:1024 sc1\n\t"
"v_mfma_f32_16x16x32_bf16 v[100:103], a[120:123], a[148:151], v[100:103]\n\t"
"s_waitcnt lgkmcnt(0)\n\t"
"v_accvgpr_read_b32 v224, a213\n\t"
"v_mfma_f32_16x16x32_bf16 v[100:103], a[124:127], a[164:167], v[100:103]\n\t"
"buffer_load_dwordx4 a[108:111], off, s[96:99], s46 offset:3072 sc1\n\t"
"s_barrier\n\t"
"s_add_u32 s71, s71, 1\n\t"
"v_mov_b32 v215, s71\n\t"
"s_mov_b64 exec, 1\n\t"
"buffer_store_dword v215, off, s[8:11], s70 sc1\n\t"
"s_mov_b64 exec, -1\n\t"
"s_sub_u32 s21, s21, 1\n\t"
"s_xor_b64 s[84:85], s[84:85], s[88:89]\n\t"
"s_add_u32 s28, s84, 4096\n\t"
"s_addc_u32 s29, s85, 0\n\t"
"s_branch Lunit_%=\n\t"
"Lfb_%=:\n\t"
"v_mbcnt_lo_u32_b32 v162, -1, 0\n\t"
"v_mbcnt_hi_u32_b32 v162, -1, v162\n\t"
"v_bfe_u32 v161, v162, 1, 2\n\t"
"v_lshlrev_b32 v163, 4, v162\n\t"
"v_and_or_b32 v161, v163, 16, v161\n\t"
"v_mul_u32_u24 v161, 0xc0, v161\n\t"
"v_lshrrev_b32 v163, 5, v162\n\t"
"v_mul_u32_u24 v163, 12, v163\n\t"
"v_bfe_u32 v164, v162, 3, 2\n\t"
"v_add3_u32 v161, v161, v163, v164\n\t"
"v_lshlrev_b32 v161, 1, v161\n\t"
"v_lshlrev_b32 v162, 1, v162\n\t"
"v_mov_b32 v152, 0\n\t"
"v_mov_b32 v153, 0\n\t"
"v_mov_b32 v154, 0\n\t"
"v_mov_b32 v155, 0\n\t"
"v_mov_b32 v156, 0\n\t"
"v_mov_b32 v157, 0\n\t"
"v_mov_b32 v158, 0\n\t"
"v_mov_b32 v159, 0\n\t"
"v_mov_b32 v160, 0\n\t"
"s_mov_b32 s67, 0\n\t"
"Lfb0_%=:\n\t"
"s_mov_b32 s69, 0\n\t"
"Lfb1_%=:\n\t"
"s_lshr_b32 s60, s69, 5\n\t"
"s_mul_i32 s56, s60, 12288\n\t"
"s_bfe_u32 s61, s69, 0x20001\n\t"
"s_mul_i32 s61, s61, 1536\n\t"
"s_add_u32 s56, s56, s61\n\t"
"s_bitcmp1_b32 s69, 0\n\t"
"s_cselect_b32 s61, 48, 0\n\t"
"s_add_u32 s56, s56, s61\n\t"
"s_bfe_u32 s62, s69, 0x20003\n\t"
"s_mul_i32 s61, s62, 96\n\t"
"s_add_u32 s56, s56, s61\n\t"
"s_lshl3_add_u32 s56, s67, s56\n\t"
"s_lshr_b32 s60, s69, 5\n\t"
"s_bfe_u32 s62, s69, 0x20003\n\t"
"s_and_b32 s61, s60, 1\n\t"
"s_lshl1_add_u32 s61, s67, s61\n\t"
"s_mul_i32 s62, s62, 288\n\t"
"s_add_u32 s61, s61, s62\n\t"
"s_lshr_b32 s60, s60, 1\n\t"
"s_mul_i32 s62, s60, 96\n\t"
"s_add_u32 s61, s61, s62\n\t"
"s_lshl_b32 s57, s61, 10\n\t"
"s_and_b32 s61, s69, 7\n\t"
"s_lshl_b32 s61, s61, 7\n\t"
"s_add_u32 s57, s57, s61\n\t"
"s_add_u32 s60, s56, 0\n\t"
"buffer_load_short_d16_hi v152, v161, s[52:55], s60 offen\n\t"
"s_add_u32 s60, s56, 73728\n\t"
"buffer_load_short_d16_hi v153, v161, s[52:55], s60 offen\n\t"
"s_add_u32 s60, s56, 147456\n\t"
"buffer_load_short_d16_hi v154, v161, s[52:55], s60 offen\n\t"
"s_add_u32 s60, s56, 221184\n\t"
"buffer_load_short_d16_hi v155, v161, s[52:55], s60 offen\n\t"
"s_add_u32 s60, s56, 294912\n\t"
"buffer_load_short_d16_hi v156, v161, s[52:55], s60 offen\n\t"
"s_add_u32 s60, s56, 368640\n\t"
"buffer_load_short_d16_hi v157, v161, s[52:55], s60 offen\n\t"
"s_add_u32 s60, s56, 442368\n\t"
"buffer_load_short_d16_hi v158, v161, s[52:55], s60 offen\n\t"
"s_add_u32 s60, s56, 516096\n\t"
"buffer_load_short_d16_hi v159, v161, s[52:55], s60 offen\n\t"
"s_add_u32 s60, s56, 589824\n\t"
"buffer_load_short_d16_hi v160, v161, s[52:55], s60 offen\n\t"
"s_waitcnt vmcnt(2)\n\t"
"v_add_f32 v168, v152, v158\n\t"
"s_waitcnt vmcnt(1)\n\t"
"v_add_f32 v169, v153, v159\n\t"
"s_waitcnt vmcnt(0)\n\t"
"v_add_f32 v170, v154, v160\n\t"
"v_add_f32 v171, v168, v155\n\t"
"v_sub_f32 v174, v168, v155\n\t"
"v_add_f32 v172, v169, v156\n\t"
"v_sub_f32 v175, v169, v156\n\t"
"v_add_f32 v173, v170, v157\n\t"
"v_sub_f32 v176, v170, v157\n\t"
"v_add_f32 v177, v152, v154\n\t"
"v_add_f32 v178, v171, v173\n\t"
"v_add_f32 v179, v174, v176\n\t"
"v_add_f32 v180, v158, v160\n\t"
"v_add_f32 v182, v177, v153\n\t"
"v_sub_f32 v183, v177, v153\n\t"
"v_add_f32 v184, v178, v172\n\t"
"v_sub_f32 v185, v178, v172\n\t"
"v_add_f32 v186, v179, v175\n\t"
"v_sub_f32 v187, v179, v175\n\t"
"v_add_f32 v188, v180, v159\n\t"
"v_sub_f32 v189, v180, v159\n\t"
"v_mul_f32 v191, 0.5, v182\n\t"
"v_mul_f32 v192, 0.5, v183\n\t"
"v_mul_f32 v194, 0.5, v171\n\t"
"v_mul_f32 v195, 0x3e800000, v184\n\t"
"v_mul_f32 v196, 0x3e800000, v185\n\t"
"v_mul_f32 v197, -0.5, v173\n\t"
"v_mul_f32 v199, 0.5, v186\n\t"
"v_mul_f32 v200, 0.5, v187\n\t"
"v_mul_f32 v203, -0.5, v188\n\t"
"v_mul_f32 v204, -0.5, v189\n\t"
"v_cvt_pk_bf16_f32 v206, v152, v191\n\t"
"v_cvt_pk_bf16_f32 v207, v192, -v154\n\t"
"v_cvt_pk_bf16_f32 v208, v194, v195\n\t"
"v_cvt_pk_bf16_f32 v209, v196, v197\n\t"
"v_cvt_pk_bf16_f32 v210, v174, v199\n\t"
"v_cvt_pk_bf16_f32 v211, v200, -v176\n\t"
"v_cvt_pk_bf16_f32 v212, -v158, v203\n\t"
"v_cvt_pk_bf16_f32 v213, v204, v160\n\t"
"s_add_u32 s60, s57, 0\n\t"
"buffer_store_short v206, v162, s[8:11], s60 offen sc1\n\t"
"s_add_u32 s60, s57, 6144\n\t"
"buffer_store_short_d16_hi v206, v162, s[8:11], s60 offen sc1\n\t"
"s_add_u32 s60, s57, 12288\n\t"
"buffer_store_short v207, v162, s[8:11], s60 offen sc1\n\t"
"s_add_u32 s60, s57, 18432\n\t"
"buffer_store_short_d16_hi v207, v162, s[8:11], s60 offen sc1\n\t"
"s_add_u32 s60, s57, 24576\n\t"
"buffer_store_short v208, v162, s[8:11], s60 offen sc1\n\t"
"s_add_u32 s60, s57, 30720\n\t"
"buffer_store_short_d16_hi v208, v162, s[8:11], s60 offen sc1\n\t"
"s_add_u32 s60, s57, 36864\n\t"
"buffer_store_short v209, v162, s[8:11], s60 offen sc1\n\t"
"s_add_u32 s60, s57, 43008\n\t"
"buffer_store_short_d16_hi v209, v162, s[8:11], s60 offen sc1\n\t"
"s_add_u32 s60, s57, 49152\n\t"
"buffer_store_short v210, v162, s[8:11], s60 offen sc1\n\t"
"s_add_u32 s60, s57, 55296\n\t"
"buffer_store_short_d16_hi v210, v162, s[8:11], s60 offen sc1\n\t"
"s_add_u32 s60, s57, 61440\n\t"
"buffer_store_short v211, v162, s[8:11], s60 offen sc1\n\t"
"s_add_u32 s60, s57, 67584\n\t"
"buffer_store_short_d16_hi v211, v162, s[8:11], s60 offen sc1\n\t"
"s_add_u32 s60, s57, 73728\n\t"
"buffer_store_short v212, v162, s[8:11], s60 offen sc1\n\t"
"s_add_u32 s60, s57, 79872\n\t"
"buffer_store_short_d16_hi v212, v162, s[8:11], s60 offen sc1\n\t"
"s_add_u32 s60, s57, 86016\n\t"
"buffer_store_short v213, v162, s[8:11], s60 offen sc1\n\t"
"s_add_u32 s60, s57, 92160\n\t"
"buffer_store_short_d16_hi v213, v162, s[8:11], s60 offen sc1\n\t"
"s_waitcnt vmcnt(0)\n\t"
"s_add_u32 s69, s69, 1\n\t"
"s_cmp_lt_u32 s69, 192\n\t"
"s_cbranch_scc1 Lfb1_%=\n\t"
"s_add_u32 s67, s67, 1\n\t"
"s_cmp_lt_u32 s67, 3\n\t"
"s_cbranch_scc1 Lfb0_%=\n\t"
"s_branch Lgo_%=\n\t"
".p2align 6\n\t.fill 448, 4, 0xbf800000\n\t"
"Lpad10_%=:\n\t"
"s_cbranch_execz Lret1_%=\n\t"
"s_nop 0\n\t"
"Ltail_%=:\n\t"
"v_mfma_f32_16x16x32_bf16 v[104:107], a[72:75], a[152:155], v[104:107]\n\t"
"s_nop 0\n\t"
"ds_read_b128 a[144:147], v224 offset:17568\n\t"
"ds_read_b128 a[160:163], v224 offset:19616\n\t"
"v_mfma_f32_16x16x32_bf16 v[104:107], a[76:79], a[168:171], v[104:107]\n\t"
"buffer_load_dwordx4 a[20:23], off, s[28:31], s69 offset:1024 sc1\n\t"
"v_mfma_f32_16x16x32_bf16 v[120:123], a[80:83], a[152:155], v[120:123]\n\t"
"ds_read_b128 a[148:151], v224 offset:17824\n\t"
"v_add_u32 v248, 33280, v226\n\t"
"v_mfma_f32_16x16x32_bf16 v[120:123], a[84:87], a[168:171], v[120:123]\n\t"
"ds_read_b128 a[164:167], v224 offset:19872\n\t"
"v_xor_b32 v249, 32, v248\n\t"
"v_mfma_f32_16x16x32_bf16 v[136:139], a[88:91], a[152:155], v[136:139]\n\t"
"ds_read_b128 a[152:155], v224 offset:18080\n\t"
"v_mfma_f32_16x16x32_bf16 v[136:139], a[92:95], a[168:171], v[136:139]\n\t"
"buffer_load_dwordx4 a[12:15], off, s[84:87], s69 offset:3072 sc1\n\t"
"v_mfma_f32_16x16x32_bf16 v[140:143], a[88:91], a[156:159], v[140:143]\n\t"
"ds_read_b128 v[8:11], v229 offset:20480\n\t"
"v_mfma_f32_16x16x32_bf16 v[140:143], a[92:95], a[172:175], v[140:143]\n\t"
"buffer_load_dwordx4 a[8:11], off, s[84:87], s69 offset:2048 sc1\n\t"
"v_mfma_f32_16x16x32_bf16 v[124:127], a[80:83], a[156:159], v[124:127]\n\t"
"ds_read_b128 a[168:171], v224 offset:20128\n\t"
"v_xor_b32 v250, 64, v248\n\t"
"v_mfma_f32_16x16x32_bf16 v[124:127], a[84:87], a[172:175], v[124:127]\n\t"
"buffer_load_dwordx4 a[0:3], off, s[84:87], s69 offset:0 sc1\n\t"
"v_mfma_f32_16x16x32_bf16 v[108:111], a[72:75], a[156:159], v[108:111]\n\t"
"ds_read_b128 a[156:159], v224 offset:18336\n\t"
"v_xor_b32 v251, 96, v248\n\t"
"v_mfma_f32_16x16x32_bf16 v[108:111], a[76:79], a[172:175], v[108:111]\n\t"
"buffer_load_dwordx4 a[4:7], off, s[84:87], s69 offset:1024 sc1\n\t"
"v_mfma_f32_16x16x32_bf16 v[192:195], a[224:227], a[176:179], 0\n\t"
"s_nop 0\n\t"
"ds_read_b64_tr_b16 a[178:179], v248 offset:20864\n\t"
"ds_read_b64_tr_b16 a[176:177], v248 offset:20736\n\t"
"v_mfma_f32_16x16x32_bf16 v[196:199], a[224:227], a[180:183], 0\n\t"
"ds_read_b64_tr_b16 a[180:181], v249 offset:20992\n\t"
"v_mfma_f32_16x16x32_bf16 v[200:203], a[224:227], a[184:187], 0\n\t"
"s_nop 0\n\t"
"ds_read_b64_tr_b16 a[186:187], v249 offset:20864\n\t"
"ds_read_b64_tr_b16 a[184:185], v249 offset:20736\n\t"
"v_mfma_f32_16x16x32_bf16 v[204:207], a[224:227], a[188:191], 0\n\t"
"s_waitcnt lgkmcnt(8)\n\t"
"s_waitcnt lgkmcnt(7)\n\t"
"buffer_store_dwordx4 v[8:11], v232, s[16:19], s24 offen nt\n\t"
"v_mfma_f32_16x16x32_bf16 v[208:211], a[224:227], a[192:195], 0\n\t"
"ds_read_b128 v[24:27], v230 offset:20480\n\t"
"v_mfma_f32_16x16x32_bf16 v[212:215], a[224:227], a[196:199], 0\n\t"
"ds_read_b64_tr_b16 a[188:189], v248 offset:20992\n\t"
"v_mfma_f32_16x16x32_bf16 v[216:219], a[224:227], a[200:203], 0\n\t"
"s_nop 0\n\t"
"ds_read_b64_tr_b16 a[194:195], v250 offset:20864\n\t"
"ds_read_b64_tr_b16 a[192:193], v250 offset:20736\n\t"
"v_mfma_f32_16x16x32_bf16 v[220:223], a[224:227], a[204:207], 0\n\t"
"s_waitcnt vmcnt(18) lgkmcnt(10)\n\t"
"buffer_load_dwordx4 a[40:43], off, s[28:31], s54 offset:0 sc1\n\t"
"v_mfma_f32_16x16x32_bf16 v[144:147], a[96:99], a[144:147], v[144:147]\n\t"
"ds_read_b64_tr_b16 a[196:197], v251 offset:20992\n\t"
"v_cvt_pk_bf16_f32 v195, v195, v199\n\t"
"v_mfma_f32_16x16x32_bf16 v[144:147], a[100:103], a[160:163], v[144:147]\n\t"
"ds_read_b128 a[172:175], v224 offset:20384\n\t"
"v_cvt_pk_bf16_f32 v194, v194, v198\n\t"
"v_mfma_f32_16x16x32_bf16 v[160:163], a[104:107], a[144:147], v[160:163]\n\t"
"ds_read_b64_tr_b16 a[202:203], v251 offset:20864\n\t"
"v_cvt_pk_bf16_f32 v200, v200, v204\n\t"
"v_mfma_f32_16x16x32_bf16 v[160:163], a[108:111], a[160:163], v[160:163]\n\t"
"buffer_load_dwordx4 a[44:47], off, s[28:31], s54 offset:1024 sc1\n\t"
"v_mfma_f32_16x16x32_bf16 v[176:179], a[112:115], a[144:147], v[176:179]\n\t"
"ds_read_b128 v[12:15], v229 offset:33792\n\t"
"v_cvt_pk_bf16_f32 v209, v209, v213\n\t"
"v_mfma_f32_16x16x32_bf16 v[176:179], a[116:119], a[160:163], v[176:179]\n\t"
"ds_read_b128 a[144:147], v224 offset:16544\n\t"
"v_cvt_pk_bf16_f32 v193, v193, v197\n\t"
"v_mfma_f32_16x16x32_bf16 v[180:183], a[112:115], a[148:151], v[180:183]\n\t"
"ds_read_b128 a[160:163], v224 offset:18592\n\t"
"v_cvt_pk_bf16_f32 v216, v216, v220\n\t"
"v_mfma_f32_16x16x32_bf16 v[180:183], a[116:119], a[164:167], v[180:183]\n\t"
"s_waitcnt lgkmcnt(8)\n\t"
"buffer_store_dwordx4 v[24:27], v233, s[16:19], s24 offen nt\n\t"
"v_mfma_f32_16x16x32_bf16 v[164:167], a[104:107], a[148:151], v[164:167]\n\t"
"ds_write_addtid_b32 v200 offset:2048\n\t"
"v_cvt_pk_bf16_f32 v217, v217, v221\n\t"
"v_mfma_f32_16x16x32_bf16 v[164:167], a[108:111], a[164:167], v[164:167]\n\t"
"s_nop 0\n\t"
"ds_write_addtid_b32 v193 offset:8320\n\t"
"ds_write_addtid_b32 v209 offset:9344\n\t"
"v_mfma_f32_16x16x32_bf16 v[148:151], a[96:99], a[148:151], v[148:151]\n\t"
"ds_read_b128 v[40:43], v231 offset:20480\n\t"
"v_cvt_pk_bf16_f32 v201, v201, v205\n\t"
"v_mfma_f32_16x16x32_bf16 v[148:151], a[100:103], a[164:167], v[148:151]\n\t"
"buffer_load_dwordx4 a[36:39], off, s[84:87], s54 offset:3072 sc1\n\t"
"v_mfma_f32_16x16x32_bf16 v[152:155], a[96:99], a[152:155], v[152:155]\n\t"
"ds_read_b128 v[28:31], v230 offset:33792\n\t"
"v_cvt_pk_bf16_f32 v192, v192, v196\n\t"
"v_mfma_f32_16x16x32_bf16 v[152:155], a[100:103], a[168:171], v[152:155]\n\t"
"ds_read_b128 a[148:151], v224 offset:16800\n\t"
"v_cvt_pk_bf16_f32 v210, v210, v214\n\t"
"v_mfma_f32_16x16x32_bf16 v[168:171], a[104:107], a[152:155], v[168:171]\n\t"
"ds_read_b128 a[164:167], v224 offset:18848\n\t"
"v_cvt_pk_bf16_f32 v219, v219, v223\n\t"
"v_mfma_f32_16x16x32_bf16 v[168:171], a[108:111], a[168:171], v[168:171]\n\t"
"buffer_load_dwordx4 a[32:35], off, s[84:87], s54 offset:2048 sc1\n\t"
"v_mfma_f32_16x16x32_bf16 v[184:187], a[112:115], a[152:155], v[184:187]\n\t"
"ds_read_b128 a[152:155], v224 offset:17056\n\t"
"v_cvt_pk_bf16_f32 v202, v202, v206\n\t"
"v_mfma_f32_16x16x32_bf16 v[184:187], a[116:119], a[168:171], v[184:187]\n\t"
"s_waitcnt lgkmcnt(8)\n\t"
"ds_read_b128 v[44:47], v231 offset:33792\n\t"
"v_cvt_pk_bf16_f32 v208, v208, v212\n\t"
"v_mfma_f32_16x16x32_bf16 v[188:191], a[112:115], a[156:159], v[188:191]\n\t"
"ds_write_addtid_b32 v195 offset:12448\n\t"
"v_cvt_pk_bf16_f32 v203, v203, v207\n\t"
"v_mfma_f32_16x16x32_bf16 v[188:191], a[116:119], a[172:175], v[188:191]\n\t"
"ds_write_addtid_b32 v194 offset:4128\n\t"
"v_cvt_pk_bf16_f32 v211, v211, v215\n\t"
"v_mfma_f32_16x16x32_bf16 v[172:175], a[104:107], a[156:159], v[172:175]\n\t"
"ds_write_addtid_b32 v201 offset:10368\n\t"
"v_cvt_pk_bf16_f32 v218, v218, v222\n\t"
"v_mfma_f32_16x16x32_bf16 v[172:175], a[108:111], a[172:175], v[172:175]\n\t"
"s_nop 0\n\t"
"ds_write_addtid_b32 v218 offset:7200\n\t"
"ds_write_addtid_b32 v219 offset:15520\n\t"
"v_mfma_f32_16x16x32_bf16 v[156:159], a[96:99], a[156:159], v[156:159]\n\t"
"s_nop 0\n\t"
"ds_write_addtid_b32 v202 offset:6176\n\t"
"ds_write_addtid_b32 v210 offset:5152\n\t"
"v_mfma_f32_16x16x32_bf16 v[156:159], a[100:103], a[172:175], v[156:159]\n\t"
"s_waitcnt vmcnt(12) lgkmcnt(10)\n\t"
"buffer_store_dwordx4 v[40:43], v234, s[16:19], s24 offen nt\n\t"
"v_mfma_f32_16x16x32_bf16 v[96:99], a[120:123], a[144:147], v[96:99]\n\t"
"s_nop 0\n\t"
"ds_write_addtid_b32 v192 offset:0\n\t"
"ds_write_addtid_b32 v211 offset:13472\n\t"
"v_mfma_f32_16x16x32_bf16 v[96:99], a[124:127], a[160:163], v[96:99]\n\t"
"s_nop 0\n\t"
"ds_write_addtid_b32 v203 offset:14496\n\t"
"ds_write_addtid_b32 v216 offset:3072\n\t"
"v_mfma_f32_16x16x32_bf16 v[112:115], a[128:131], a[144:147], v[112:115]\n\t"
"s_nop 0\n\t"
"ds_write_addtid_b32 v208 offset:1024\n\t"
"ds_write_addtid_b32 v217 offset:11392\n\t"
"v_mfma_f32_16x16x32_bf16 v[112:115], a[132:135], a[160:163], v[112:115]\n\t"
"buffer_load_dwordx4 a[24:27], off, s[84:87], s54 offset:0 sc1\n\t"
"v_mfma_f32_16x16x32_bf16 v[128:131], a[136:139], a[144:147], v[128:131]\n\t"
"ds_read_b128 a[172:175], v224 offset:19360\n\t"
"v_mfma_f32_16x16x32_bf16 v[128:131], a[140:143], a[160:163], v[128:131]\n\t"
"s_waitcnt lgkmcnt(12)\n\t"
"ds_read_b128 a[168:171], v224 offset:19104\n\t"
"v_mfma_f32_16x16x32_bf16 v[132:135], a[136:139], a[148:151], v[132:135]\n\t"
"ds_read_b128 a[156:159], v224 offset:17312\n\t"
"v_mfma_f32_16x16x32_bf16 v[132:135], a[140:143], a[164:167], v[132:135]\n\t"
"buffer_load_dwordx4 a[28:31], off, s[84:87], s54 offset:1024 sc1\n\t"
"v_mfma_f32_16x16x32_bf16 v[116:119], a[128:131], a[148:151], v[116:119]\n\t"
"s_nop 0\n\t"
"ds_read_b64_tr_b16 a[200:201], v251 offset:20736\n\t"
"ds_read_b64_tr_b16 a[204:205], v250 offset:20992\n\t"
"v_mfma_f32_16x16x32_bf16 v[116:119], a[132:135], a[164:167], v[116:119]\n\t"
"v_mfma_f32_16x16x32_bf16 v[100:103], a[120:123], a[148:151], v[100:103]\n\t"
"buffer_load_dwordx4 a[64:67], off, s[28:31], s57 offset:0 sc1\n\t"
"v_mfma_f32_16x16x32_bf16 v[100:103], a[124:127], a[164:167], v[100:103]\n\t"
"s_waitcnt lgkmcnt(0)\n\t"
"s_barrier\n\t"
"v_mfma_f32_16x16x32_bf16 v[104:107], a[120:123], a[152:155], v[104:107]\n\t"
"s_nop 0\n\t"
"ds_read_b128 a[144:147], v224 offset:0\n\t"
"ds_read_b128 a[148:151], v224 offset:256\n\t"
"v_mfma_f32_16x16x32_bf16 v[104:107], a[124:127], a[168:171], v[104:107]\n\t"
"buffer_store_dwordx4 v[12:15], v232, s[16:19], s27 offen nt\n\t"
"v_mfma_f32_16x16x32_bf16 v[120:123], a[128:131], a[152:155], v[120:123]\n\t"
"ds_read_b128 a[160:163], v224 offset:2048\n\t"
"v_mfma_f32_16x16x32_bf16 v[120:123], a[132:135], a[168:171], v[120:123]\n\t"
"ds_read_b128 a[164:167], v224 offset:2304\n\t"
"v_mfma_f32_16x16x32_bf16 v[136:139], a[136:139], a[152:155], v[136:139]\n\t"
"v_mfma_f32_16x16x32_bf16 v[136:139], a[140:143], a[168:171], v[136:139]\n\t"
"buffer_load_dwordx4 a[68:71], off, s[28:31], s57 offset:1024 sc1\n\t"
"v_mfma_f32_16x16x32_bf16 v[140:143], a[136:139], a[156:159], v[140:143]\n\t"
"ds_read_b128 a[168:171], v224 offset:2560\n\t"
"v_mfma_f32_16x16x32_bf16 v[140:143], a[140:143], a[172:175], v[140:143]\n\t"
"ds_read_b128 a[152:155], v224 offset:512\n\t"
"v_mfma_f32_16x16x32_bf16 v[124:127], a[128:131], a[156:159], v[124:127]\n\t"
"v_mfma_f32_16x16x32_bf16 v[124:127], a[132:135], a[172:175], v[124:127]\n\t"
"v_mfma_f32_16x16x32_bf16 v[108:111], a[120:123], a[156:159], v[108:111]\n\t"
"ds_read_b128 a[156:159], v224 offset:768\n\t"
"v_mfma_f32_16x16x32_bf16 v[108:111], a[124:127], a[172:175], v[108:111]\n\t"
"ds_read_b128 a[172:175], v224 offset:2816\n\t"
"buffer_load_dwordx4 a[60:63], off, s[84:87], s57 offset:3072 sc1\n\t"
"v_mfma_f32_4x4x4_16b_bf16 v[200:203], a[220:221], a[178:179], 0\n\t"
"ds_read_b64_tr_b16 a[178:179], v248 offset:128\n\t"
"v_mfma_f32_4x4x4_16b_bf16 v[204:207], a[220:221], a[186:187], 0\n\t"
"ds_read_b64_tr_b16 a[186:187], v249 offset:128\n\t"
"v_mfma_f32_4x4x4_16b_bf16 v[216:219], a[220:221], a[194:195], 0\n\t"
"ds_read_b64_tr_b16 a[194:195], v250 offset:128\n\t"
"v_mfma_f32_4x4x4_16b_bf16 v[220:223], a[220:221], a[202:203], 0\n\t"
"ds_read_b64_tr_b16 a[202:203], v251 offset:128\n\t"
"buffer_load_dwordx4 a[56:59], off, s[84:87], s57 offset:2048 sc1\n\t"
"v_mfma_f32_4x4x4_16b_bf16 v[192:195], a[222:223], a[176:177], v[200:203]\n\t"
"ds_read_b64_tr_b16 a[176:177], v248 offset:0\n\t"
"v_mfma_f32_4x4x4_16b_bf16 v[196:199], a[222:223], a[184:185], v[204:207]\n\t"
"ds_read_b64_tr_b16 a[184:185], v249 offset:0\n\t"
"v_mfma_f32_4x4x4_16b_bf16 v[200:203], a[222:223], a[180:181], v[200:203]\n\t"
".p2align 3\n\t"
"ds_read_b64_tr_b16 a[180:181], v249 offset:256\n\t"
"buffer_store_dwordx4 v[28:31], v233, s[16:19], s27 offen nt\n\t"
"v_pk_add_f32 v[96:97], v[96:97], v[144:145]\n\t"
"v_pk_add_f32 v[98:99], v[98:99], v[146:147]\n\t"
"v_pk_fma_f32 v[144:145], v[144:145], s[38:39], v[96:97]\n\t"
"v_pk_fma_f32 v[146:147], v[146:147], s[38:39], v[98:99]\n\t"
"v_pk_add_f32 v[112:113], v[112:113], v[160:161]\n\t"
"v_pk_add_f32 v[114:115], v[114:115], v[162:163]\n\t"
"v_pk_fma_f32 v[160:161], v[160:161], s[38:39], v[112:113]\n\t"
"v_pk_fma_f32 v[162:163], v[162:163], s[38:39], v[114:115]\n\t"
"v_pk_add_f32 v[128:129], v[128:129], v[176:177]\n\t"
"v_pk_add_f32 v[130:131], v[130:131], v[178:179]\n\t"
"v_pk_fma_f32 v[176:177], v[176:177], s[38:39], v[128:129]\n\t"
"v_pk_fma_f32 v[178:179], v[178:179], s[38:39], v[130:131]\n\t"
"v_pk_add_f32 v[132:133], v[132:133], v[180:181]\n\t"
"v_pk_add_f32 v[134:135], v[134:135], v[182:183]\n\t"
"v_pk_fma_f32 v[180:181], v[180:181], s[38:39], v[132:133]\n\t"
"v_pk_fma_f32 v[182:183], v[182:183], s[38:39], v[134:135]\n\t"
"v_pk_add_f32 v[116:117], v[116:117], v[164:165]\n\t"
"v_pk_add_f32 v[118:119], v[118:119], v[166:167]\n\t"
"v_pk_fma_f32 v[164:165], v[164:165], s[38:39], v[116:117]\n\t"
"v_pk_fma_f32 v[166:167], v[166:167], s[38:39], v[118:119]\n\t"
"v_pk_add_f32 v[100:101], v[100:101], v[148:149]\n\t"
"v_pk_add_f32 v[102:103], v[102:103], v[150:151]\n\t"
"v_pk_fma_f32 v[148:149], v[148:149], s[38:39], v[100:101]\n\t"
"v_pk_fma_f32 v[150:151], v[150:151], s[38:39], v[102:103]\n\t"
"v_pk_add_f32 v[104:105], v[104:105], v[152:153]\n\t"
"v_pk_add_f32 v[106:107], v[106:107], v[154:155]\n\t"
"v_pk_fma_f32 v[152:153], v[152:153], s[38:39], v[104:105]\n\t"
"v_pk_fma_f32 v[154:155], v[154:155], s[38:39], v[106:107]\n\t"
"v_pk_add_f32 v[120:121], v[120:121], v[168:169]\n\t"
"v_pk_add_f32 v[122:123], v[122:123], v[170:171]\n\t"
"v_pk_fma_f32 v[168:169], v[168:169], s[38:39], v[120:121]\n\t"
"v_pk_fma_f32 v[170:171], v[170:171], s[38:39], v[122:123]\n\t"
"v_pk_add_f32 v[136:137], v[136:137], v[184:185]\n\t"
"v_pk_add_f32 v[138:139], v[138:139], v[186:187]\n\t"
"v_pk_fma_f32 v[184:185], v[184:185], s[38:39], v[136:137]\n\t"
"v_pk_fma_f32 v[186:187], v[186:187], s[38:39], v[138:139]\n\t"
"v_pk_add_f32 v[140:141], v[140:141], v[188:189]\n\t"
"v_pk_add_f32 v[142:143], v[142:143], v[190:191]\n\t"
"v_pk_fma_f32 v[188:189], v[188:189], s[38:39], v[140:141]\n\t"
"v_pk_fma_f32 v[190:191], v[190:191], s[38:39], v[142:143]\n\t"
"v_pk_add_f32 v[124:125], v[124:125], v[172:173]\n\t"
"v_pk_add_f32 v[126:127], v[126:127], v[174:175]\n\t"
"v_pk_fma_f32 v[172:173], v[172:173], s[38:39], v[124:125]\n\t"
"v_pk_fma_f32 v[174:175], v[174:175], s[38:39], v[126:127]\n\t"
"v_pk_add_f32 v[108:109], v[108:109], v[156:157]\n\t"
"v_pk_add_f32 v[110:111], v[110:111], v[158:159]\n\t"
"v_pk_fma_f32 v[156:157], v[156:157], s[38:39], v[108:109]\n\t"
"v_pk_fma_f32 v[158:159], v[158:159], s[38:39], v[110:111]\n\t"
"v_cvt_pk_bf16_f32 v116, v116, v117\n\t"
"v_mfma_f32_4x4x4_16b_bf16 v[204:207], a[222:223], a[188:189], v[204:207]\n\t"
"ds_read_b64_tr_b16 a[188:189], v248 offset:256\n\t"
"v_cvt_pk_bf16_f32 v96, v96, v97\n\t"
"v_mfma_f32_4x4x4_16b_bf16 v[208:211], a[222:223], a[192:193], v[216:219]\n\t"
"v_cvt_pk_bf16_f32 v97, v98, v99\n\t"
"v_cvt_pk_bf16_f32 v100, v100, v101\n\t"
"v_cvt_pk_bf16_f32 v101, v102, v103\n\t"
"v_cvt_pk_bf16_f32 v112, v112, v113\n\t"
"v_mfma_f32_4x4x4_16b_bf16 v[212:215], a[222:223], a[200:201], v[220:223]\n\t"
"buffer_load_dwordx4 a[48:51], off, s[84:87], s57 offset:0 sc1\n\t"
"ds_write2st64_b64 v228, v[96:97], v[100:101] offset0:40 offset1:53\n\t"
"v_cvt_pk_bf16_f32 v117, v118, v119\n\t"
"v_mfma_f32_4x4x4_16b_bf16 v[216:219], a[222:223], a[196:197], v[216:219]\n\t"
"v_cvt_pk_bf16_f32 v124, v124, v125\n\t"
"v_cvt_pk_bf16_f32 v125, v126, v127\n\t"
"v_mfma_f32_4x4x4_16b_bf16 v[220:223], a[222:223], a[204:205], v[220:223]\n\t"
"s_waitcnt vmcnt(16) lgkmcnt(7)\n\t"
"v_cvt_pk_bf16_f32 v113, v114, v115\n\t"
"v_cvt_pk_bf16_f32 v140, v140, v141\n\t"
"v_mfma_f32_16x16x32_bf16 v[48:51], a[0:3], a[144:147], v[48:51]\n\t"
"v_cvt_pk_bf16_f32 v192, -v192, -v196\n\t"
"v_cvt_pk_bf16_f32 v120, v120, v121\n\t"
"v_mfma_f32_16x16x32_bf16 v[48:51], a[4:7], a[160:163], v[48:51]\n\t"
"ds_write_addtid_b32 v192 offset:16544\n\t"
"buffer_load_dwordx4 a[52:55], off, s[84:87], s57 offset:1024 sc1\n\t"
"ds_write2st64_b64 v246, v[112:113], v[116:117] offset0:40 offset1:53\n\t"
"v_cvt_pk_bf16_f32 v136, v136, v137\n\t"
"v_mfma_f32_16x16x32_bf16 v[64:67], a[8:11], a[144:147], v[64:67]\n\t"
"v_cvt_pk_bf16_f32 v104, v104, v105\n\t"
"v_cvt_pk_bf16_f32 v108, v108, v109\n\t"
"v_mfma_f32_16x16x32_bf16 v[64:67], a[12:15], a[160:163], v[64:67]\n\t"
"v_cvt_pk_bf16_f32 v128, v128, v129\n\t"
"v_cvt_pk_bf16_f32 v132, v132, v133\n\t"
"v_cvt_pk_bf16_f32 v133, v134, v135\n\t"
"v_mfma_f32_16x16x32_bf16 v[80:83], a[16:19], a[144:147], v[80:83]\n\t"
"v_cvt_pk_bf16_f32 v203, v203, v207\n\t"
"v_cvt_pk_bf16_f32 v129, v130, v131\n\t"
"v_mfma_f32_16x16x32_bf16 v[80:83], a[20:23], a[160:163], v[80:83]\n\t"
"s_nop 0\n\t"
"ds_read_b128 a[144:147], v224 offset:1024\n\t"
"ds_read_b128 a[160:163], v224 offset:3072\n\t"
"ds_write2st64_b64 v247, v[128:129], v[132:133] offset0:40 offset1:53\n\t"
"v_mfma_f32_16x16x32_bf16 v[84:87], a[16:19], a[148:151], v[84:87]\n\t"
"v_cvt_pk_bf16_f32 v105, v106, v107\n\t"
"v_cvt_pk_bf16_f32 v109, v110, v111\n\t"
"v_mfma_f32_16x16x32_bf16 v[84:87], a[20:23], a[164:167], v[84:87]\n\t"
"buffer_load_dwordx4 a[88:91], off, s[28:31], s63 offset:0 sc1\n\t"
"v_mfma_f32_16x16x32_bf16 v[68:71], a[8:11], a[148:151], v[68:71]\n\t"
"v_cvt_pk_bf16_f32 v195, -v195, -v199\n\t"
"v_cvt_pk_bf16_f32 v217, v217, v221\n\t"
"v_mfma_f32_16x16x32_bf16 v[68:71], a[12:15], a[164:167], v[68:71]\n\t"
"ds_write_addtid_b32 v203 offset:30016\n\t"
"ds_write2st64_b64 v228, v[104:105], v[108:109] offset0:66 offset1:79\n\t"
"v_mfma_f32_16x16x32_bf16 v[52:55], a[0:3], a[148:151], v[52:55]\n\t"
"v_cvt_pk_bf16_f32 v137, v138, v139\n\t"
"v_cvt_pk_bf16_f32 v141, v142, v143\n\t"
"v_mfma_f32_16x16x32_bf16 v[52:55], a[4:7], a[164:167], v[52:55]\n\t"
"buffer_store_dwordx4 v[44:47], v234, s[16:19], s27 offen nt\n\t"
"v_mfma_f32_16x16x32_bf16 v[56:59], a[0:3], a[152:155], v[56:59]\n\t"
"v_cvt_pk_bf16_f32 v211, -v211, -v215\n\t"
"v_cvt_pk_bf16_f32 v121, v122, v123\n\t"
"v_mfma_f32_16x16x32_bf16 v[56:59], a[4:7], a[168:171], v[56:59]\n\t"
"s_nop 0\n\t"
"ds_read_b128 a[148:151], v224 offset:1280\n\t"
"ds_read_b128 a[164:167], v224 offset:3328\n\t"
"ds_write2st64_b64 v247, v[136:137], v[140:141] offset0:66 offset1:79\n\t"
"v_mfma_f32_16x16x32_bf16 v[72:75], a[8:11], a[152:155], v[72:75]\n\t"
"v_cvt_pk_bf16_f32 v208, -v208, -v212\n\t"
"v_cvt_pk_bf16_f32 v210, -v210, -v214\n\t"
"v_mfma_f32_16x16x32_bf16 v[72:75], a[12:15], a[168:171], v[72:75]\n\t"
"buffer_load_dwordx4 a[92:95], off, s[28:31], s63 offset:1024 sc1\n\t"
"v_mfma_f32_16x16x32_bf16 v[88:91], a[16:19], a[152:155], v[88:91]\n\t"
"v_cvt_pk_bf16_f32 v200, v200, v204\n\t"
"v_cvt_pk_bf16_f32 v202, v202, v206\n\t"
"v_mfma_f32_16x16x32_bf16 v[88:91], a[20:23], a[168:171], v[88:91]\n\t"
"ds_write_addtid_b32 v208 offset:18592\n\t"
"ds_write2st64_b64 v246, v[120:121], v[124:125] offset0:66 offset1:79\n\t"
"v_mfma_f32_16x16x32_bf16 v[92:95], a[16:19], a[156:159], v[92:95]\n\t"
"v_cvt_pk_bf16_f32 v193, -v193, -v197\n\t"
"v_cvt_pk_bf16_f32 v201, v201, v205\n\t"
"v_mfma_f32_16x16x32_bf16 v[92:95], a[20:23], a[172:175], v[92:95]\n\t"
"buffer_load_dwordx4 a[84:87], off, s[84:87], s63 offset:3072 sc1\n\t"
"v_mfma_f32_16x16x32_bf16 v[76:79], a[8:11], a[156:159], v[76:79]\n\t"
"v_cvt_pk_bf16_f32 v216, v216, v220\n\t"
"v_cvt_pk_bf16_f32 v219, v219, v223\n\t"
"v_mfma_f32_16x16x32_bf16 v[76:79], a[12:15], a[172:175], v[76:79]\n\t"
"s_nop 0\n\t"
"ds_write_addtid_b32 v193 offset:20672\n\t"
"ds_write_addtid_b32 v210 offset:19616\n\t"
"ds_write_addtid_b32 v211 offset:23744\n\t"
"ds_write_addtid_b32 v216 offset:26912\n\t"
"ds_write_addtid_b32 v219 offset:32064\n\t"
"v_mfma_f32_16x16x32_bf16 v[60:63], a[0:3], a[156:159], v[60:63]\n\t"
"s_nop 0\n\t"
"ds_write_addtid_b32 v217 offset:31040\n\t"
"v_cvt_pk_bf16_f32 v194, -v194, -v198\n\t"
"v_mfma_f32_16x16x32_bf16 v[60:63], a[4:7], a[172:175], v[60:63]\n\t"
"s_waitcnt vmcnt(12) lgkmcnt(8)\n\t"
"ds_write_addtid_b32 v201 offset:28992\n\t"
"v_cvt_pk_bf16_f32 v218, v218, v222\n\t"
"v_mfma_f32_16x16x32_bf16 v[48:51], a[24:27], a[144:147], v[48:51]\n\t"
"s_nop 0\n\t"
"ds_write_addtid_b32 v202 offset:25888\n\t"
"v_cvt_pk_bf16_f32 v209, -v209, -v213\n\t"
"v_mfma_f32_16x16x32_bf16 v[48:51], a[28:31], a[160:163], v[48:51]\n\t"
"s_nop 0\n\t"
"ds_write_addtid_b32 v194 offset:17568\n\t"
"ds_write_addtid_b32 v209 offset:22720\n\t"
"ds_write_addtid_b32 v218 offset:27936\n\t"
"v_mfma_f32_16x16x32_bf16 v[64:67], a[32:35], a[144:147], v[64:67]\n\t"
"s_nop 0\n\t"
"ds_write_addtid_b32 v195 offset:21696\n\t"
"ds_write_addtid_b32 v200 offset:24864\n\t"
"v_mfma_f32_16x16x32_bf16 v[64:67], a[36:39], a[160:163], v[64:67]\n\t"
"buffer_load_dwordx4 a[80:83], off, s[84:87], s63 offset:2048 sc1\n\t"
"v_mfma_f32_16x16x32_bf16 v[80:83], a[40:43], a[144:147], v[80:83]\n\t"
"s_nop 0\n\t"
"ds_read_b128 a[156:159], v224 offset:1792\n\t"
"ds_read_b128 a[172:175], v224 offset:3840\n\t"
"v_mfma_f32_16x16x32_bf16 v[80:83], a[44:47], a[160:163], v[80:83]\n\t"
"buffer_load_dwordx4 a[72:75], off, s[84:87], s63 offset:0 sc1\n\t"
"v_mfma_f32_16x16x32_bf16 v[84:87], a[40:43], a[148:151], v[84:87]\n\t"
"s_nop 0\n\t"
"ds_read_b128 a[152:155], v224 offset:1536\n\t"
"ds_read_b128 a[168:171], v224 offset:3584\n\t"
"v_mfma_f32_16x16x32_bf16 v[84:87], a[44:47], a[164:167], v[84:87]\n\t"
"s_nop 0\n\t"
"ds_read_b64_tr_b16 a[192:193], v250 offset:0\n\t"
"ds_read_b64_tr_b16 a[200:201], v251 offset:0\n\t"
"v_mfma_f32_16x16x32_bf16 v[68:71], a[32:35], a[148:151], v[68:71]\n\t"
"s_nop 0\n\t"
"ds_read_b64_tr_b16 a[196:197], v251 offset:256\n\t"
"ds_read_b64_tr_b16 a[204:205], v250 offset:256\n\t"
"v_mfma_f32_16x16x32_bf16 v[68:71], a[36:39], a[164:167], v[68:71]\n\t"
"Lpad11_%=:\n\t"
"s_cbranch_execz Lret1_%=\n\t"
"v_mfma_f32_16x16x32_bf16 v[52:55], a[24:27], a[148:151], v[52:55]\n\t"
"buffer_load_dwordx4 a[76:79], off, s[84:87], s63 offset:1024 sc1\n\t"
"v_mfma_f32_16x16x32_bf16 v[52:55], a[28:31], a[164:167], v[52:55]\n\t"
"s_waitcnt lgkmcnt(0)\n\t"
"s_barrier\n\t"
"v_mfma_f32_16x16x32_bf16 v[56:59], a[24:27], a[152:155], v[56:59]\n\t"
"ds_read_b128 a[144:147], v224 offset:16544\n\t"
"v_mfma_f32_16x16x32_bf16 v[56:59], a[28:31], a[168:171], v[56:59]\n\t"
"ds_read_b128 a[160:163], v224 offset:18592\n\t"
"v_mfma_f32_16x16x32_bf16 v[72:75], a[32:35], a[152:155], v[72:75]\n\t"
"ds_read_b128 a[148:151], v224 offset:16800\n\t"
"v_mfma_f32_16x16x32_bf16 v[72:75], a[36:39], a[168:171], v[72:75]\n\t"
"ds_read_b128 a[164:167], v224 offset:18848\n\t"
"v_mfma_f32_16x16x32_bf16 v[88:91], a[40:43], a[152:155], v[88:91]\n\t"
"v_mfma_f32_16x16x32_bf16 v[88:91], a[44:47], a[168:171], v[88:91]\n\t"
"buffer_load_dwordx4 a[112:115], off, s[28:31], s42 offset:0 sc1\n\t"
"v_mfma_f32_16x16x32_bf16 v[92:95], a[40:43], a[156:159], v[92:95]\n\t"
"ds_read_b128 a[168:171], v224 offset:19104\n\t"
"v_mfma_f32_16x16x32_bf16 v[92:95], a[44:47], a[172:175], v[92:95]\n\t"
"ds_read_b128 v[104:107], v229 offset:20480\n\t"
"v_mfma_f32_16x16x32_bf16 v[76:79], a[32:35], a[156:159], v[76:79]\n\t"
"v_mfma_f32_16x16x32_bf16 v[76:79], a[36:39], a[172:175], v[76:79]\n\t"
"ds_read_b128 a[152:155], v224 offset:17056\n\t"
"v_mfma_f32_16x16x32_bf16 v[60:63], a[24:27], a[156:159], v[60:63]\n\t"
"ds_read_b128 a[156:159], v224 offset:17312\n\t"
"v_mfma_f32_16x16x32_bf16 v[60:63], a[28:31], a[172:175], v[60:63]\n\t"
"buffer_load_dwordx4 a[116:119], off, s[28:31], s42 offset:1024 sc1\n\t"
"v_mfma_f32_4x4x4_16b_bf16 v[200:203], a[220:221], a[178:179], 0\n\t"
"ds_read_b64_tr_b16 a[178:179], v248 offset:41600\n\t"
"v_mfma_f32_4x4x4_16b_bf16 v[204:207], a[220:221], a[186:187], 0\n\t"
"ds_read_b64_tr_b16 a[186:187], v249 offset:41600\n\t"
"v_mfma_f32_4x4x4_16b_bf16 v[216:219], a[220:221], a[194:195], 0\n\t"
"ds_read_b128 a[172:175], v224 offset:19360\n\t"
"v_mfma_f32_4x4x4_16b_bf16 v[220:223], a[220:221], a[202:203], 0\n\t"
"s_waitcnt lgkmcnt(5)\n\t"
".p2align 3\n\t"
"buffer_store_dwordx4 v[104:107], v232, s[16:19], s20 offen nt\n\t"
"v_pk_add_f32 v[144:145], v[48:49], v[144:145] neg_lo:[0,1] neg_hi:[0,1]\n\t"
"v_pk_add_f32 v[146:147], v[50:51], v[146:147] neg_lo:[0,1] neg_hi:[0,1]\n\t"
"v_pk_fma_f32 v[48:49], v[144:145], s[90:91], v[48:49] neg_lo:[1,0,0] neg_hi:[1,0,0]\n\t"
"v_pk_fma_f32 v[50:51], v[146:147], s[90:91], v[50:51] neg_lo:[1,0,0] neg_hi:[1,0,0]\n\t"
"v_pk_add_f32 v[160:161], v[64:65], v[160:161] neg_lo:[0,1] neg_hi:[0,1]\n\t"
"v_pk_add_f32 v[162:163], v[66:67], v[162:163] neg_lo:[0,1] neg_hi:[0,1]\n\t"
"v_pk_fma_f32 v[64:65], v[160:161], s[90:91], v[64:65] neg_lo:[1,0,0] neg_hi:[1,0,0]\n\t"
"v_pk_fma_f32 v[66:67], v[162:163], s[90:91], v[66:67] neg_lo:[1,0,0] neg_hi:[1,0,0]\n\t"
"v_pk_add_f32 v[176:177], v[80:81], v[176:177] neg_lo:[0,1] neg_hi:[0,1]\n\t"
"v_pk_add_f32 v[178:179], v[82:83], v[178:179] neg_lo:[0,1] neg_hi:[0,1]\n\t"
"v_pk_fma_f32 v[80:81], v[176:177], s[90:91], v[80:81] neg_lo:[1,0,0] neg_hi:[1,0,0]\n\t"
"v_pk_fma_f32 v[82:83], v[178:179], s[90:91], v[82:83] neg_lo:[1,0,0] neg_hi:[1,0,0]\n\t"
"v_pk_add_f32 v[180:181], v[84:85], v[180:181] neg_lo:[0,1] neg_hi:[0,1]\n\t"
"v_pk_add_f32 v[182:183], v[86:87], v[182:183] neg_lo:[0,1] neg_hi:[0,1]\n\t"
"v_pk_fma_f32 v[84:85], v[180:181], s[90:91], v[84:85] neg_lo:[1,0,0] neg_hi:[1,0,0]\n\t"
"v_pk_fma_f32 v[86:87], v[182:183], s[90:91], v[86:87] neg_lo:[1,0,0] neg_hi:[1,0,0]\n\t"
"v_pk_add_f32 v[164:165], v[68:69], v[164:165] neg_lo:[0,1] neg_hi:[0,1]\n\t"
"v_pk_add_f32 v[166:167], v[70:71], v[166:167] neg_lo:[0,1] neg_hi:[0,1]\n\t"
"v_pk_fma_f32 v[68:69], v[164:165], s[90:91], v[68:69] neg_lo:[1,0,0] neg_hi:[1,0,0]\n\t"
"v_pk_fma_f32 v[70:71], v[166:167], s[90:91], v[70:71] neg_lo:[1,0,0] neg_hi:[1,0,0]\n\t"
"v_pk_add_f32 v[148:149], v[52:53], v[148:149] neg_lo:[0,1] neg_hi:[0,1]\n\t"
"v_pk_add_f32 v[150:151], v[54:55], v[150:151] neg_lo:[0,1] neg_hi:[0,1]\n\t"
"v_pk_fma_f32 v[52:53], v[148:149], s[90:91], v[52:53] neg_lo:[1,0,0] neg_hi:[1,0,0]\n\t"
"v_pk_fma_f32 v[54:55], v[150:151], s[90:91], v[54:55] neg_lo:[1,0,0] neg_hi:[1,0,0]\n\t"
"v_pk_add_f32 v[152:153], v[56:57], v[152:153] neg_lo:[0,1] neg_hi:[0,1]\n\t"
"v_pk_add_f32 v[154:155], v[58:59], v[154:155] neg_lo:[0,1] neg_hi:[0,1]\n\t"
"v_pk_fma_f32 v[56:57], v[152:153], s[90:91], v[56:57] neg_lo:[1,0,0] neg_hi:[1,0,0]\n\t"
"v_pk_fma_f32 v[58:59], v[154:155], s[90:91], v[58:59] neg_lo:[1,0,0] neg_hi:[1,0,0]\n\t"
"v_pk_add_f32 v[168:169], v[72:73], v[168:169] neg_lo:[0,1] neg_hi:[0,1]\n\t"
"v_pk_add_f32 v[170:171], v[74:75], v[170:171] neg_lo:[0,1] neg_hi:[0,1]\n\t"
"v_pk_fma_f32 v[72:73], v[168:169], s[90:91], v[72:73] neg_lo:[1,0,0] neg_hi:[1,0,0]\n\t"
"v_pk_fma_f32 v[74:75], v[170:171], s[90:91], v[74:75] neg_lo:[1,0,0] neg_hi:[1,0,0]\n\t"
"v_pk_add_f32 v[184:185], v[88:89], v[184:185] neg_lo:[0,1] neg_hi:[0,1]\n\t"
"v_pk_add_f32 v[186:187], v[90:91], v[186:187] neg_lo:[0,1] neg_hi:[0,1]\n\t"
"v_pk_fma_f32 v[88:89], v[184:185], s[90:91], v[88:89] neg_lo:[1,0,0] neg_hi:[1,0,0]\n\t"
"v_pk_fma_f32 v[90:91], v[186:187], s[90:91], v[90:91] neg_lo:[1,0,0] neg_hi:[1,0,0]\n\t"
"v_pk_add_f32 v[188:189], v[92:93], v[188:189] neg_lo:[0,1] neg_hi:[0,1]\n\t"
"v_pk_add_f32 v[190:191], v[94:95], v[190:191] neg_lo:[0,1] neg_hi:[0,1]\n\t"
"v_pk_fma_f32 v[92:93], v[188:189], s[90:91], v[92:93] neg_lo:[1,0,0] neg_hi:[1,0,0]\n\t"
"v_pk_fma_f32 v[94:95], v[190:191], s[90:91], v[94:95] neg_lo:[1,0,0] neg_hi:[1,0,0]\n\t"
"v_pk_add_f32 v[172:173], v[76:77], v[172:173] neg_lo:[0,1] neg_hi:[0,1]\n\t"
"v_pk_add_f32 v[174:175], v[78:79], v[174:175] neg_lo:[0,1] neg_hi:[0,1]\n\t"
"v_pk_fma_f32 v[76:77], v[172:173], s[90:91], v[76:77] neg_lo:[1,0,0] neg_hi:[1,0,0]\n\t"
"v_pk_fma_f32 v[78:79], v[174:175], s[90:91], v[78:79] neg_lo:[1,0,0] neg_hi:[1,0,0]\n\t"
"v_pk_add_f32 v[156:157], v[60:61], v[156:157] neg_lo:[0,1] neg_hi:[0,1]\n\t"
"v_pk_add_f32 v[158:159], v[62:63], v[158:159] neg_lo:[0,1] neg_hi:[0,1]\n\t"
"v_pk_fma_f32 v[60:61], v[156:157], s[90:91], v[60:61] neg_lo:[1,0,0] neg_hi:[1,0,0]\n\t"
"v_pk_fma_f32 v[62:63], v[158:159], s[90:91], v[62:63] neg_lo:[1,0,0] neg_hi:[1,0,0]\n\t"
"v_mfma_f32_4x4x4_16b_bf16 v[192:195], a[222:223], a[176:177], v[200:203]\n\t"
"ds_read_b128 v[120:123], v230 offset:20480\n\t"
"v_mfma_f32_4x4x4_16b_bf16 v[196:199], a[222:223], a[184:185], v[204:207]\n\t"
"ds_read_b64_tr_b16 a[194:195], v250 offset:41600\n\t"
"v_mfma_f32_4x4x4_16b_bf16 v[200:203], a[222:223], a[180:181], v[200:203]\n\t"
"buffer_load_dwordx4 a[108:111], off, s[84:87], s42 offset:3072 sc1\n\t"
"v_mfma_f32_4x4x4_16b_bf16 v[204:207], a[222:223], a[188:189], v[204:207]\n\t"
"ds_read_b64_tr_b16 a[202:203], v251 offset:41600\n\t"
"v_mfma_f32_4x4x4_16b_bf16 v[208:211], a[222:223], a[192:193], v[216:219]\n\t"
"ds_read_b64_tr_b16 a[176:177], v248 offset:41472\n\t"
"v_mfma_f32_4x4x4_16b_bf16 v[212:215], a[222:223], a[200:201], v[220:223]\n\t"
"ds_read_b64_tr_b16 a[184:185], v249 offset:41472\n\t"
"v_mfma_f32_4x4x4_16b_bf16 v[216:219], a[222:223], a[196:197], v[216:219]\n\t"
"ds_read_b64_tr_b16 a[180:181], v249 offset:41728\n\t"
"v_mfma_f32_4x4x4_16b_bf16 v[220:223], a[222:223], a[204:205], v[220:223]\n\t"
"s_waitcnt vmcnt(11) lgkmcnt(7)\n\t"
"buffer_load_dwordx4 a[104:107], off, s[84:87], s42 offset:2048 sc1\n\t"
"v_mfma_f32_16x16x32_bf16 v[48:51], a[48:51], a[144:147], v[48:51]\n\t"
"ds_read_b64_tr_b16 a[188:189], v248 offset:41728\n\t"
"v_cvt_pk_bf16_f32 v195, -v195, -v199\n\t"
"v_mfma_f32_16x16x32_bf16 v[48:51], a[52:55], a[160:163], v[48:51]\n\t"
"buffer_load_dwordx4 a[96:99], off, s[84:87], s42 offset:0 sc1\n\t"
"v_mfma_f32_16x16x32_bf16 v[64:67], a[56:59], a[144:147], v[64:67]\n\t"
"ds_write_addtid_b32 v195 offset:5152\n\t"
"v_cvt_pk_bf16_f32 v194, -v194, -v198\n\t"
"v_mfma_f32_16x16x32_bf16 v[64:67], a[60:63], a[160:163], v[64:67]\n\t"
"ds_write_addtid_b32 v194 offset:1024\n\t"
"v_cvt_pk_bf16_f32 v203, v203, v207\n\t"
"v_mfma_f32_16x16x32_bf16 v[80:83], a[64:67], a[144:147], v[80:83]\n\t"
"ds_read_b128 a[232:235], v230 offset:33792\n\t"
"v_cvt_pk_bf16_f32 v211, -v211, -v215\n\t"
"v_mfma_f32_16x16x32_bf16 v[80:83], a[68:71], a[160:163], v[80:83]\n\t"
"s_waitcnt lgkmcnt(7)\n\t"
"buffer_store_dwordx4 v[120:123], v233, s[16:19], s20 offen nt\n\t"
"v_mfma_f32_16x16x32_bf16 v[84:87], a[64:67], a[148:151], v[84:87]\n\t"
"ds_read_b128 a[144:147], v224 offset:17568\n\t"
"v_cvt_pk_bf16_f32 v201, v201, v205\n\t"
"v_mfma_f32_16x16x32_bf16 v[84:87], a[68:71], a[164:167], v[84:87]\n\t"
"ds_read_b128 v[136:139], v231 offset:20480\n\t"
"v_cvt_pk_bf16_f32 v202, v202, v206\n\t"
"v_mfma_f32_16x16x32_bf16 v[68:71], a[56:59], a[148:151], v[68:71]\n\t"
"ds_read_b128 a[160:163], v224 offset:19616\n\t"
"v_cvt_pk_bf16_f32 v193, -v193, -v197\n\t"
"v_mfma_f32_16x16x32_bf16 v[68:71], a[60:63], a[164:167], v[68:71]\n\t"
"buffer_load_dwordx4 a[100:103], off, s[84:87], s42 offset:1024 sc1\n\t"
"v_mfma_f32_16x16x32_bf16 v[52:55], a[48:51], a[148:151], v[52:55]\n\t"
"ds_read_b128 a[148:151], v224 offset:17824\n\t"
"v_cvt_pk_bf16_f32 v209, -v209, -v213\n\t"
"v_mfma_f32_16x16x32_bf16 v[52:55], a[52:55], a[164:167], v[52:55]\n\t"
"ds_write_addtid_b32 v211 offset:7200\n\t"
"v_cvt_pk_bf16_f32 v200, v200, v204\n\t"
"v_mfma_f32_16x16x32_bf16 v[56:59], a[48:51], a[152:155], v[56:59]\n\t"
"ds_read_b128 a[236:239], v231 offset:33792\n\t"
"v_cvt_pk_bf16_f32 v216, v216, v220\n\t"
"v_mfma_f32_16x16x32_bf16 v[56:59], a[52:55], a[168:171], v[56:59]\n\t"
"buffer_load_dwordx4 a[136:139], off, s[28:31], s48 offset:0 sc1\n\t"
"v_mfma_f32_16x16x32_bf16 v[72:75], a[56:59], a[152:155], v[72:75]\n\t"
"ds_read_b128 a[164:167], v224 offset:19872\n\t"
"v_cvt_pk_bf16_f32 v210, -v210, -v214\n\t"
"v_mfma_f32_16x16x32_bf16 v[72:75], a[60:63], a[168:171], v[72:75]\n\t"
"ds_write_addtid_b32 v193 offset:4128\n\t"
"v_cvt_pk_bf16_f32 v218, v218, v222\n\t"
"v_mfma_f32_16x16x32_bf16 v[88:91], a[64:67], a[152:155], v[88:91]\n\t"
"s_nop 0\n\t"
"ds_write_addtid_b32 v201 offset:12448\n\t"
"ds_write_addtid_b32 v202 offset:9344\n\t"
"v_mfma_f32_16x16x32_bf16 v[88:91], a[68:71], a[168:171], v[88:91]\n\t"
"s_waitcnt lgkmcnt(8)\n\t"
"buffer_store_dwordx4 v[136:139], v234, s[16:19], s20 offen nt\n\t"
"v_mfma_f32_16x16x32_bf16 v[92:95], a[64:67], a[156:159], v[92:95]\n\t"
"ds_read_b128 v[108:111], v229 offset:33792\n\t"
"v_cvt_pk_bf16_f32 v192, -v192, -v196\n\t"
"v_mfma_f32_16x16x32_bf16 v[92:95], a[68:71], a[172:175], v[92:95]\n\t"
"ds_write_addtid_b32 v210 offset:3072\n\t"
"v_cvt_pk_bf16_f32 v219, v219, v223\n\t"
"v_mfma_f32_16x16x32_bf16 v[76:79], a[56:59], a[156:159], v[76:79]\n\t"
"ds_write_addtid_b32 v219 offset:15520\n\t"
"v_cvt_pk_bf16_f32 v208, -v208, -v212\n\t"
"v_mfma_f32_16x16x32_bf16 v[76:79], a[60:63], a[172:175], v[76:79]\n\t"
"ds_write_addtid_b32 v200 offset:8320\n\t"
"buffer_load_dwordx4 a[140:143], off, s[28:31], s48 offset:1024 sc1\n\t"
"v_mfma_f32_16x16x32_bf16 v[60:63], a[48:51], a[156:159], v[60:63]\n\t"
"s_nop 0\n\t"
"ds_write_addtid_b32 v209 offset:6176\n\t"
"ds_write_addtid_b32 v218 offset:11392\n\t"
"v_mfma_f32_16x16x32_bf16 v[60:63], a[52:55], a[172:175], v[60:63]\n\t"
"s_waitcnt vmcnt(11) lgkmcnt(10)\n\t"
"ds_write_addtid_b32 v192 offset:0\n\t"
"ds_write_addtid_b32 v216 offset:10368\n\t"
"v_mfma_f32_16x16x32_bf16 v[144:147], a[72:75], a[144:147], v[144:147]\n\t"
"ds_write_addtid_b32 v203 offset:13472\n\t"
"v_cvt_pk_bf16_f32 v217, v217, v221\n\t"
"v_mfma_f32_16x16x32_bf16 v[144:147], a[76:79], a[160:163], v[144:147]\n\t"
"buffer_load_dwordx4 a[132:135], off, s[84:87], s48 offset:3072 sc1\n\t"
"v_mfma_f32_16x16x32_bf16 v[160:163], a[80:83], a[144:147], v[160:163]\n\t"
"s_nop 0\n\t"
"ds_write_addtid_b32 v208 offset:2048\n\t"
"ds_write_addtid_b32 v217 offset:14496\n\t"
"v_mfma_f32_16x16x32_bf16 v[160:163], a[84:87], a[160:163], v[160:163]\n\t"
"s_nop 0\n\t"
"ds_read_b128 a[152:155], v224 offset:18080\n\t"
"ds_read_b128 a[156:159], v224 offset:18336\n\t"
"v_mfma_f32_16x16x32_bf16 v[176:179], a[88:91], a[144:147], v[176:179]\n\t"
"s_nop 0\n\t"
"ds_read_b128 a[172:175], v224 offset:20384\n\t"
"ds_read_b64_tr_b16 a[192:193], v250 offset:41472\n\t"
"v_mfma_f32_16x16x32_bf16 v[176:179], a[92:95], a[160:163], v[176:179]\n\t"
"s_waitcnt lgkmcnt(13)\n\t"
"buffer_store_dwordx4 v[108:111], v232, s[16:19], s25 offen nt\n\t"
"v_mfma_f32_16x16x32_bf16 v[180:183], a[88:91], a[148:151], v[180:183]\n\t"
"s_nop 0\n\t"
"ds_read_b128 a[168:171], v224 offset:20128\n\t"
"ds_read_b64_tr_b16 a[200:201], v251 offset:41472\n\t"
"v_mfma_f32_16x16x32_bf16 v[180:183], a[92:95], a[164:167], v[180:183]\n\t"
"s_nop 0\n\t"
"ds_read_b64_tr_b16 a[196:197], v251 offset:41728\n\t"
"ds_read_b64_tr_b16 a[204:205], v250 offset:41728\n\t"
"v_mfma_f32_16x16x32_bf16 v[164:167], a[80:83], a[148:151], v[164:167]\n\t"
"buffer_load_dwordx4 a[128:131], off, s[84:87], s48 offset:2048 sc1\n\t"
"v_mfma_f32_16x16x32_bf16 v[164:167], a[84:87], a[164:167], v[164:167]\n\t"
"v_mfma_f32_16x16x32_bf16 v[148:151], a[72:75], a[148:151], v[148:151]\n\t"
"buffer_load_dwordx4 a[120:123], off, s[84:87], s48 offset:0 sc1\n\t"
"v_mfma_f32_16x16x32_bf16 v[148:151], a[76:79], a[164:167], v[148:151]\n\t"
"s_waitcnt lgkmcnt(0)\n\t"
"s_barrier\n\t"
"v_mfma_f32_16x16x32_bf16 v[152:155], a[72:75], a[152:155], v[152:155]\n\t"
"ds_read_b128 a[144:147], v224 offset:0\n\t"
"v_mfma_f32_16x16x32_bf16 v[152:155], a[76:79], a[168:171], v[152:155]\n\t"
"ds_read_b128 a[160:163], v224 offset:2048\n\t"
"v_mfma_f32_16x16x32_bf16 v[168:171], a[80:83], a[152:155], v[168:171]\n\t"
"ds_read_b128 a[148:151], v224 offset:256\n\t"
"v_add_u32 v248, 33280, v227\n\t"
"v_mfma_f32_16x16x32_bf16 v[168:171], a[84:87], a[168:171], v[168:171]\n\t"
"ds_read_b128 a[164:167], v224 offset:2304\n\t"
"v_xor_b32 v249, 32, v248\n\t"
"v_mfma_f32_16x16x32_bf16 v[184:187], a[88:91], a[152:155], v[184:187]\n\t"
"v_xor_b32 v250, 64, v248\n\t"
"v_xor_b32 v251, 96, v248\n\t"
"v_mfma_f32_16x16x32_bf16 v[184:187], a[92:95], a[168:171], v[184:187]\n\t"
"buffer_load_dwordx4 a[124:127], off, s[84:87], s48 offset:1024 sc1\n\t"
"v_mfma_f32_16x16x32_bf16 v[188:191], a[88:91], a[156:159], v[188:191]\n\t"
"ds_read_b128 a[168:171], v224 offset:2560\n\t"
"v_mfma_f32_16x16x32_bf16 v[188:191], a[92:95], a[172:175], v[188:191]\n\t"
"ds_read_b128 a[152:155], v224 offset:512\n\t"
"v_mfma_f32_16x16x32_bf16 v[172:175], a[80:83], a[156:159], v[172:175]\n\t"
"v_mfma_f32_16x16x32_bf16 v[172:175], a[84:87], a[172:175], v[172:175]\n\t"
"ds_read_b64_tr_b16 a[182:183], v249 offset:512\n\t"
"v_mfma_f32_16x16x32_bf16 v[156:159], a[72:75], a[156:159], v[156:159]\n\t"
"ds_read_b128 a[156:159], v224 offset:768\n\t"
"v_mfma_f32_16x16x32_bf16 v[156:159], a[76:79], a[172:175], v[156:159]\n\t"
"buffer_load_dwordx4 a[16:19], off, s[28:31], s75 offset:0 sc1\n\t"
"v_mfma_f32_4x4x4_16b_bf16 v[200:203], a[220:221], a[178:179], 0\n\t"
"ds_read_b64_tr_b16 a[190:191], v251 offset:512\n\t"
"v_mfma_f32_4x4x4_16b_bf16 v[204:207], a[220:221], a[186:187], 0\n\t"
"ds_read_b64_tr_b16 a[198:199], v249 offset:1664\n\t"
"v_mfma_f32_4x4x4_16b_bf16 v[216:219], a[220:221], a[194:195], 0\n\t"
"ds_read_b128 a[172:175], v224 offset:2816\n\t"
"v_mfma_f32_4x4x4_16b_bf16 v[220:223], a[220:221], a[202:203], 0\n\t"
"buffer_store_dwordx4 a[232:235], v233, s[16:19], s25 offen nt\n\t"
"v_mfma_f32_4x4x4_16b_bf16 v[192:195], a[222:223], a[176:177], v[200:203]\n\t"
"ds_read_b64_tr_b16 a[206:207], v251 offset:1664\n\t"
"v_mfma_f32_4x4x4_16b_bf16 v[196:199], a[222:223], a[184:185], v[204:207]\n\t"
"ds_read_b64_tr_b16 a[178:179], v248 offset:512\n\t"
"v_mfma_f32_4x4x4_16b_bf16 v[200:203], a[222:223], a[180:181], v[200:203]\n\t"
"ds_read_b64_tr_b16 a[186:187], v250 offset:512\n\t"
"v_mfma_f32_4x4x4_16b_bf16 v[204:207], a[222:223], a[188:189], v[204:207]\n\t"
"ds_read_b64_tr_b16 a[194:195], v248 offset:1664\n\t"
"v_mfma_f32_4x4x4_16b_bf16 v[208:211], a[222:223], a[192:193], v[216:219]\n\t"
"ds_read_b64_tr_b16 a[202:203], v250 offset:1664\n\t"
"v_mfma_f32_4x4x4_16b_bf16 v[212:215], a[222:223], a[200:201], v[220:223]\n\t"
"ds_read_b64_tr_b16 a[176:177], v248 offset:0\n\t"
"v_mfma_f32_4x4x4_16b_bf16 v[216:219], a[222:223], a[196:197], v[216:219]\n\t"
"ds_read_b64_tr_b16 a[184:185], v250 offset:0\n\t"
"v_mfma_f32_4x4x4_16b_bf16 v[220:223], a[222:223], a[204:205], v[220:223]\n\t"
"s_waitcnt vmcnt(10) lgkmcnt(8)\n\t"
"buffer_load_dwordx4 a[20:23], off, s[28:31], s75 offset:1024 sc1\n\t"
"v_mfma_f32_16x16x32_bf16 v[48:51], a[96:99], a[144:147], v[48:51]\n\t"
"ds_read_b64_tr_b16 a[180:181], v249 offset:0\n\t"
"v_cvt_pk_bf16_f32 v193, -v193, -v197\n\t"
"v_mfma_f32_16x16x32_bf16 v[48:51], a[100:103], a[160:163], v[48:51]\n\t"
"ds_read_b64_tr_b16 a[188:189], v251 offset:0\n\t"
"v_cvt_pk_bf16_f32 v194, -v194, -v198\n\t"
"v_mfma_f32_16x16x32_bf16 v[64:67], a[104:107], a[144:147], v[64:67]\n\t"
"ds_read_b64_tr_b16 a[192:193], v248 offset:1152\n\t"
"v_cvt_pk_bf16_f32 v201, v201, v205\n\t"
"v_mfma_f32_16x16x32_bf16 v[64:67], a[108:111], a[160:163], v[64:67]\n\t"
"buffer_store_dwordx4 a[236:239], v234, s[16:19], s25 offen nt\n\t"
"v_mfma_f32_16x16x32_bf16 v[80:83], a[112:115], a[144:147], v[80:83]\n\t"
"ds_read_b128 a[144:147], v224 offset:1024\n\t"
"v_cvt_pk_bf16_f32 v211, -v211, -v215\n\t"
"v_mfma_f32_16x16x32_bf16 v[80:83], a[116:119], a[160:163], v[80:83]\n\t"
"ds_read_b64_tr_b16 a[200:201], v250 offset:1152\n\t"
"v_cvt_pk_bf16_f32 v202, v202, v206\n\t"
"v_mfma_f32_16x16x32_bf16 v[84:87], a[112:115], a[148:151], v[84:87]\n\t"
"ds_read_b128 a[160:163], v224 offset:3072\n\t"
"v_cvt_pk_bf16_f32 v208, -v208, -v212\n\t"
"v_mfma_f32_16x16x32_bf16 v[84:87], a[116:119], a[164:167], v[84:87]\n\t"
"buffer_load_dwordx4 a[12:15], off, s[84:87], s75 offset:3072 sc1\n\t"
"v_mfma_f32_16x16x32_bf16 v[68:71], a[104:107], a[148:151], v[68:71]\n\t"
"ds_write_addtid_b32 v201 offset:28992\n\t"
"v_cvt_pk_bf16_f32 v192, -v192, -v196\n\t"
"v_mfma_f32_16x16x32_bf16 v[68:71], a[108:111], a[164:167], v[68:71]\n\t"
"ds_write_addtid_b32 v211 offset:23744\n\t"
"v_cvt_pk_bf16_f32 v203, v203, v207\n\t"
"v_mfma_f32_16x16x32_bf16 v[52:55], a[96:99], a[148:151], v[52:55]\n\t"
"ds_read_b128 a[148:151], v224 offset:1280\n\t"
"v_mfma_f32_16x16x32_bf16 v[52:55], a[100:103], a[164:167], v[52:55]\n\t"
"ds_write_addtid_b32 v193 offset:20672\n\t"
"v_cvt_pk_bf16_f32 v218, v218, v222\n\t"
"v_mfma_f32_16x16x32_bf16 v[56:59], a[96:99], a[152:155], v[56:59]\n\t"
"ds_read_b128 a[164:167], v224 offset:3328\n\t"
"v_cvt_pk_bf16_f32 v217, v217, v221\n\t"
"v_mfma_f32_16x16x32_bf16 v[56:59], a[100:103], a[168:171], v[56:59]\n\t"
"buffer_load_dwordx4 a[8:11], off, s[84:87], s75 offset:2048 sc1\n\t"
"v_mfma_f32_16x16x32_bf16 v[72:75], a[104:107], a[152:155], v[72:75]\n\t"
"ds_write_addtid_b32 v218 offset:27936\n\t"
"v_cvt_pk_bf16_f32 v195, -v195, -v199\n\t"
"v_mfma_f32_16x16x32_bf16 v[72:75], a[108:111], a[168:171], v[72:75]\n\t"
"ds_write_addtid_b32 v217 offset:31040\n\t"
"v_mfma_f32_16x16x32_bf16 v[88:91], a[112:115], a[152:155], v[88:91]\n\t"
"ds_read_b128 a[152:155], v224 offset:1536\n\t"
"v_cvt_pk_bf16_f32 v219, v219, v223\n\t"
"v_mfma_f32_16x16x32_bf16 v[88:91], a[116:119], a[168:171], v[88:91]\n\t"
"s_waitcnt lgkmcnt(8)\n\t"
"buffer_load_dwordx4 a[0:3], off, s[84:87], s75 offset:0 sc1\n\t"
"v_mfma_f32_16x16x32_bf16 v[92:95], a[112:115], a[156:159], v[92:95]\n\t"
"ds_write_addtid_b32 v194 offset:17568\n\t"
"v_cvt_pk_bf16_f32 v210, -v210, -v214\n\t"
"v_mfma_f32_16x16x32_bf16 v[92:95], a[116:119], a[172:175], v[92:95]\n\t"
"s_nop 0\n\t"
"ds_write_addtid_b32 v192 offset:16544\n\t"
"ds_write_addtid_b32 v202 offset:25888\n\t"
"v_mfma_f32_16x16x32_bf16 v[76:79], a[104:107], a[156:159], v[76:79]\n\t"
"ds_write_addtid_b32 v210 offset:19616\n\t"
"v_mfma_f32_16x16x32_bf16 v[76:79], a[108:111], a[172:175], v[76:79]\n\t"
"ds_write_addtid_b32 v208 offset:18592\n\t"
"v_cvt_pk_bf16_f32 v209, -v209, -v213\n\t"
"v_mfma_f32_16x16x32_bf16 v[60:63], a[96:99], a[156:159], v[60:63]\n\t"
"ds_write_addtid_b32 v219 offset:32064\n\t"
"v_cvt_pk_bf16_f32 v216, v216, v220\n\t"
"v_mfma_f32_16x16x32_bf16 v[60:63], a[100:103], a[172:175], v[60:63]\n\t"
"s_waitcnt vmcnt(7) lgkmcnt(9)\n\t"
"ds_write_addtid_b32 v203 offset:30016\n\t"
"ds_write_addtid_b32 v216 offset:26912\n\t"
"v_mfma_f32_16x16x32_bf16 v[144:147], a[120:123], a[144:147], v[144:147]\n\t"
"ds_write_addtid_b32 v195 offset:21696\n\t"
"v_cvt_pk_bf16_f32 v200, v200, v204\n\t"
"v_mfma_f32_16x16x32_bf16 v[144:147], a[124:127], a[160:163], v[144:147]\n\t"
"ds_write_addtid_b32 v200 offset:24864\n\t"
"v_mfma_f32_16x16x32_bf16 v[160:163], a[128:131], a[144:147], v[160:163]\n\t"
"ds_write_addtid_b32 v209 offset:22720\n\t"
"v_mfma_f32_16x16x32_bf16 v[160:163], a[132:135], a[160:163], v[160:163]\n\t"
"ds_read_b128 a[172:175], v224 offset:3840\n\t"
"v_mfma_f32_16x16x32_bf16 v[176:179], a[136:139], a[144:147], v[176:179]\n\t"
"ds_read_b128 a[168:171], v224 offset:3584\n\t"
"v_mfma_f32_16x16x32_bf16 v[176:179], a[140:143], a[160:163], v[176:179]\n\t"
"buffer_load_dwordx4 a[4:7], off, s[84:87], s75 offset:1024 sc1\n\t"
"v_mfma_f32_16x16x32_bf16 v[180:183], a[136:139], a[148:151], v[180:183]\n\t"
"ds_read_b128 a[156:159], v224 offset:1792\n\t"
"v_mfma_f32_16x16x32_bf16 v[180:183], a[140:143], a[164:167], v[180:183]\n\t"
"ds_read_b64_tr_b16 a[196:197], v249 offset:1152\n\t"
"v_mfma_f32_16x16x32_bf16 v[164:167], a[128:131], a[148:151], v[164:167]\n\t"
"ds_read_b64_tr_b16 a[204:205], v251 offset:1152\n\t"
"v_mfma_f32_16x16x32_bf16 v[164:167], a[132:135], a[164:167], v[164:167]\n\t"
"buffer_load_dwordx4 a[40:43], off, s[28:31], s78 offset:0 sc1\n\t"
"v_mfma_f32_16x16x32_bf16 v[148:151], a[120:123], a[148:151], v[148:151]\n\t"
"v_mfma_f32_16x16x32_bf16 v[148:151], a[124:127], a[164:167], v[148:151]\n\t"
"s_waitcnt lgkmcnt(0)\n\t"
"s_barrier\n\t"
"v_mfma_f32_16x16x32_bf16 v[152:155], a[120:123], a[152:155], v[152:155]\n\t"
"ds_read_b128 a[144:147], v224 offset:16544\n\t"
"v_mfma_f32_16x16x32_bf16 v[152:155], a[124:127], a[168:171], v[152:155]\n\t"
"ds_read_b128 a[148:151], v224 offset:16800\n\t"
"v_mfma_f32_16x16x32_bf16 v[168:171], a[128:131], a[152:155], v[168:171]\n\t"
"ds_read_b128 a[160:163], v224 offset:18592\n\t"
"v_mfma_f32_16x16x32_bf16 v[168:171], a[132:135], a[168:171], v[168:171]\n\t"
"ds_read_b128 a[164:167], v224 offset:18848\n\t"
"v_mfma_f32_16x16x32_bf16 v[184:187], a[136:139], a[152:155], v[184:187]\n\t"
"v_mfma_f32_16x16x32_bf16 v[184:187], a[140:143], a[168:171], v[184:187]\n\t"
"buffer_load_dwordx4 a[44:47], off, s[28:31], s78 offset:1024 sc1\n\t"
"v_mfma_f32_16x16x32_bf16 v[188:191], a[136:139], a[156:159], v[188:191]\n\t"
"ds_read_b128 a[168:171], v224 offset:19104\n\t"
"v_mfma_f32_16x16x32_bf16 v[188:191], a[140:143], a[172:175], v[188:191]\n\t"
"buffer_load_dwordx4 a[36:39], off, s[84:87], s78 offset:3072 sc1\n\t"
"v_mfma_f32_16x16x32_bf16 v[172:175], a[128:131], a[156:159], v[172:175]\n\t"
"ds_read_b128 a[152:155], v224 offset:17056\n\t"
"v_mfma_f32_16x16x32_bf16 v[172:175], a[132:135], a[172:175], v[172:175]\n\t"
"buffer_load_dwordx4 a[32:35], off, s[84:87], s78 offset:2048 sc1\n\t"
"v_mfma_f32_16x16x32_bf16 v[156:159], a[120:123], a[156:159], v[156:159]\n\t"
"ds_read_b128 a[156:159], v224 offset:17312\n\t"
"v_mfma_f32_16x16x32_bf16 v[156:159], a[124:127], a[172:175], v[156:159]\n\t"
"ds_read_b128 a[172:175], v224 offset:19360\n\t"
"v_mfma_f32_16x16x32_bf16 v[192:195], a[224:227], a[176:179], 0\n\t"
"ds_read_b64_tr_b16 a[176:177], v248 offset:42624\n\t"
"v_mfma_f32_16x16x32_bf16 v[196:199], a[224:227], a[180:183], 0\n\t"
"buffer_load_dwordx4 a[24:27], off, s[84:87], s78 offset:0 sc1\n\t"
"v_mfma_f32_16x16x32_bf16 v[200:203], a[224:227], a[184:187], 0\n\t"
"ds_read_b64_tr_b16 a[178:179], v248 offset:43136\n\t"
"v_mfma_f32_16x16x32_bf16 v[204:207], a[224:227], a[188:191], 0\n\t"
"ds_read_b64_tr_b16 a[180:181], v249 offset:42624\n\t"
"v_mfma_f32_16x16x32_bf16 v[208:211], a[224:227], a[192:195], 0\n\t"
"ds_read_b64_tr_b16 a[182:183], v249 offset:43136\n\t"
"v_mfma_f32_16x16x32_bf16 v[212:215], a[224:227], a[196:199], 0\n\t"
"buffer_load_dwordx4 a[28:31], off, s[84:87], s78 offset:1024 sc1\n\t"
"v_mfma_f32_16x16x32_bf16 v[216:219], a[224:227], a[200:203], 0\n\t"
"ds_read_b64_tr_b16 a[184:185], v250 offset:42624\n\t"
"v_mfma_f32_16x16x32_bf16 v[220:223], a[224:227], a[204:207], 0\n\t"
"s_waitcnt vmcnt(6) lgkmcnt(7)\n\t"
"ds_read_b64_tr_b16 a[186:187], v250 offset:43136\n\t"
"v_cvt_pk_bf16_f32 v192, v192, v196\n\t"
"v_mfma_f32_16x16x32_bf16 v[48:51], a[0:3], a[144:147], v[48:51]\n\t"
"ds_read_b64_tr_b16 a[188:189], v251 offset:42624\n\t"
"v_cvt_pk_bf16_f32 v194, v194, v198\n\t"
"v_mfma_f32_16x16x32_bf16 v[48:51], a[4:7], a[160:163], v[48:51]\n\t"
"ds_read_b64_tr_b16 a[190:191], v251 offset:43136\n\t"
"v_cvt_pk_bf16_f32 v193, v193, v197\n\t"
"v_mfma_f32_16x16x32_bf16 v[64:67], a[8:11], a[144:147], v[64:67]\n\t"
"ds_read_b64_tr_b16 a[192:193], v248 offset:21888\n\t"
"v_mfma_f32_16x16x32_bf16 v[64:67], a[12:15], a[160:163], v[64:67]\n\t"
"ds_read_b64_tr_b16 a[194:195], v248 offset:22400\n\t"
"v_cvt_pk_bf16_f32 v211, v211, v215\n\t"
"v_mfma_f32_16x16x32_bf16 v[80:83], a[16:19], a[144:147], v[80:83]\n\t"
"ds_read_b128 a[144:147], v224 offset:17568\n\t"
"v_cvt_pk_bf16_f32 v209, v209, v213\n\t"
"v_mfma_f32_16x16x32_bf16 v[80:83], a[20:23], a[160:163], v[80:83]\n\t"
"ds_read_b64_tr_b16 a[196:197], v249 offset:21888\n\t"
"v_cvt_pk_bf16_f32 v219, v219, v223\n\t"
"v_mfma_f32_16x16x32_bf16 v[84:87], a[16:19], a[148:151], v[84:87]\n\t"
"ds_write_addtid_b32 v209 offset:9344\n\t"
"v_mfma_f32_16x16x32_bf16 v[84:87], a[20:23], a[164:167], v[84:87]\n\t"
"ds_read_b64_tr_b16 a[198:199], v249 offset:22400\n\t"
"v_cvt_pk_bf16_f32 v200, v200, v204\n\t"
"v_mfma_f32_16x16x32_bf16 v[68:71], a[8:11], a[148:151], v[68:71]\n\t"
"ds_read_b128 a[160:163], v224 offset:19616\n\t"
"v_cvt_pk_bf16_f32 v208, v208, v212\n\t"
"v_mfma_f32_16x16x32_bf16 v[68:71], a[12:15], a[164:167], v[68:71]\n\t"
"ds_write_addtid_b32 v219 offset:15520\n\t"
"v_cvt_pk_bf16_f32 v217, v217, v221\n\t"
"v_mfma_f32_16x16x32_bf16 v[52:55], a[0:3], a[148:151], v[52:55]\n\t"
"ds_write_addtid_b32 v200 offset:2048\n\t"
"v_cvt_pk_bf16_f32 v218, v218, v222\n\t"
"v_mfma_f32_16x16x32_bf16 v[52:55], a[4:7], a[164:167], v[52:55]\n\t"
"ds_write_addtid_b32 v211 offset:13472\n\t"
"v_mfma_f32_16x16x32_bf16 v[56:59], a[0:3], a[152:155], v[56:59]\n\t"
"ds_read_b128 a[148:151], v224 offset:17824\n\t"
"v_cvt_pk_bf16_f32 v216, v216, v220\n\t"
"v_mfma_f32_16x16x32_bf16 v[56:59], a[4:7], a[168:171], v[56:59]\n\t"
"ds_read_b128 a[164:167], v224 offset:19872\n\t"
"v_cvt_pk_bf16_f32 v195, v195, v199\n\t"
"v_mfma_f32_16x16x32_bf16 v[72:75], a[8:11], a[152:155], v[72:75]\n\t"
"ds_write_addtid_b32 v195 offset:12448\n\t"
"v_cvt_pk_bf16_f32 v210, v210, v214\n\t"
"v_mfma_f32_16x16x32_bf16 v[72:75], a[12:15], a[168:171], v[72:75]\n\t"
"buffer_load_dwordx4 a[64:67], off, s[28:31], 0 offset:0 sc1\n\t"
"v_mfma_f32_16x16x32_bf16 v[88:91], a[16:19], a[152:155], v[88:91]\n\t"
"ds_read_b128 a[152:155], v224 offset:18080\n\t"
"v_cvt_pk_bf16_f32 v201, v201, v205\n\t"
"v_mfma_f32_16x16x32_bf16 v[88:91], a[20:23], a[168:171], v[88:91]\n\t"
"s_waitcnt lgkmcnt(8)\n\t"
"ds_read_b128 a[168:171], v224 offset:20128\n\t"
"v_cvt_pk_bf16_f32 v202, v202, v206\n\t"
"v_mfma_f32_16x16x32_bf16 v[92:95], a[16:19], a[156:159], v[92:95]\n\t"
"ds_write_addtid_b32 v216 offset:3072\n\t"
"v_cvt_pk_bf16_f32 v203, v203, v207\n\t"
"v_mfma_f32_16x16x32_bf16 v[92:95], a[20:23], a[172:175], v[92:95]\n\t"
"ds_write_addtid_b32 v194 offset:4128\n\t"
"v_mfma_f32_16x16x32_bf16 v[76:79], a[8:11], a[156:159], v[76:79]\n\t"
"s_xor_b32 s24, s24, 384\n\t"
"ds_write_addtid_b32 v208 offset:1024\n\t"
"ds_write_addtid_b32 v217 offset:11392\n\t"
"v_mfma_f32_16x16x32_bf16 v[76:79], a[12:15], a[172:175], v[76:79]\n\t"
"buffer_load_dwordx4 a[68:71], off, s[28:31], 0 offset:1024 sc1\n\t"
"v_mfma_f32_16x16x32_bf16 v[60:63], a[0:3], a[156:159], v[60:63]\n\t"
"s_xor_b32 s20, s20, 384\n\t"
"ds_read_b128 a[156:159], v224 offset:18336\n\t"
"ds_write_addtid_b32 v218 offset:7200\n\t"
"v_mfma_f32_16x16x32_bf16 v[60:63], a[4:7], a[172:175], v[60:63]\n\t"
"s_waitcnt vmcnt(2) lgkmcnt(9)\n\t"
"buffer_load_dwordx4 a[60:63], off, s[84:87], 0 offset:3072 sc1\n\t"
"v_mfma_f32_16x16x32_bf16 v[144:147], a[24:27], a[144:147], v[144:147]\n\t"
"s_nop 0\n\t"
"ds_write_addtid_b32 v192 offset:0\n\t"
"ds_write_addtid_b32 v202 offset:6176\n\t"
"v_mfma_f32_16x16x32_bf16 v[144:147], a[28:31], a[160:163], v[144:147]\n\t"
"s_nop 0\n\t"
"ds_write_addtid_b32 v193 offset:8320\n\t"
"ds_write_addtid_b32 v210 offset:5152\n\t"
"v_mfma_f32_16x16x32_bf16 v[160:163], a[32:35], a[144:147], v[160:163]\n\t"
"s_xor_b32 s27, s27, 384\n\t"
"ds_write_addtid_b32 v201 offset:10368\n\t"
"ds_write_addtid_b32 v203 offset:14496\n\t"
"v_mfma_f32_16x16x32_bf16 v[160:163], a[36:39], a[160:163], v[160:163]\n\t"
"buffer_load_dwordx4 a[56:59], off, s[84:87], 0 offset:2048 sc1\n\t"
"v_mfma_f32_16x16x32_bf16 v[176:179], a[40:43], a[144:147], v[176:179]\n\t"
"ds_read_b128 a[172:175], v224 offset:20384\n\t"
"v_mfma_f32_16x16x32_bf16 v[176:179], a[44:47], a[160:163], v[176:179]\n\t"
"buffer_load_dwordx4 a[48:51], off, s[84:87], 0 offset:0 sc1\n\t"
"v_mfma_f32_16x16x32_bf16 v[180:183], a[40:43], a[148:151], v[180:183]\n\t"
"ds_read_b64_tr_b16 a[200:201], v250 offset:21888\n\t"
"v_mfma_f32_16x16x32_bf16 v[180:183], a[44:47], a[164:167], v[180:183]\n\t"
"ds_read_b64_tr_b16 a[202:203], v250 offset:22400\n\t"
"v_mfma_f32_16x16x32_bf16 v[164:167], a[32:35], a[148:151], v[164:167]\n\t"
"s_xor_b32 s25, s25, 384\n\t"
"ds_read_b64_tr_b16 a[204:205], v251 offset:21888\n\t"
"ds_read_b64_tr_b16 a[206:207], v251 offset:22400\n\t"
"v_mfma_f32_16x16x32_bf16 v[164:167], a[36:39], a[164:167], v[164:167]\n\t"
"buffer_load_dwordx4 a[52:55], off, s[84:87], 0 offset:1024 sc1\n\t"
"v_mfma_f32_16x16x32_bf16 v[148:151], a[24:27], a[148:151], v[148:151]\n\t"
"v_mfma_f32_16x16x32_bf16 v[148:151], a[28:31], a[164:167], v[148:151]\n\t"
"s_waitcnt lgkmcnt(0)\n\t"
"s_barrier\n\t"
"v_mfma_f32_16x16x32_bf16 v[152:155], a[24:27], a[152:155], v[152:155]\n\t"
"ds_read_b128 a[160:163], v224 offset:2048\n\t"
"v_mfma_f32_16x16x32_bf16 v[152:155], a[28:31], a[168:171], v[152:155]\n\t"
"ds_read_b128 a[144:147], v224 offset:0\n\t"
"v_mfma_f32_16x16x32_bf16 v[168:171], a[32:35], a[152:155], v[168:171]\n\t"
"ds_read_b128 a[148:151], v224 offset:256\n\t"
"v_mfma_f32_16x16x32_bf16 v[168:171], a[36:39], a[168:171], v[168:171]\n\t"
"s_nop 0\n\t"
"ds_read_b128 a[164:167], v224 offset:2304\n\t"
"v_mfma_f32_16x16x32_bf16 v[184:187], a[40:43], a[152:155], v[184:187]\n\t"
"s_nop 0\n\t"
"v_mfma_f32_16x16x32_bf16 v[184:187], a[44:47], a[168:171], v[184:187]\n\t"
"s_nop 0\n\t"
"v_mfma_f32_16x16x32_bf16 v[188:191], a[40:43], a[156:159], v[188:191]\n\t"
"s_nop 0\n\t"
"ds_read_b128 a[152:155], v224 offset:512\n\t"
"v_mfma_f32_16x16x32_bf16 v[188:191], a[44:47], a[172:175], v[188:191]\n\t"
"s_nop 0\n\t"
"v_mfma_f32_16x16x32_bf16 v[172:175], a[32:35], a[156:159], v[172:175]\n\t"
"s_nop 0\n\t"
"v_mfma_f32_16x16x32_bf16 v[172:175], a[36:39], a[172:175], v[172:175]\n\t"
"buffer_load_dwordx4 a[88:91], off, s[28:31], s51 offset:0 sc1\n\t"
"v_mfma_f32_16x16x32_bf16 v[156:159], a[24:27], a[156:159], v[156:159]\n\t"
"s_nop 0\n\t"
"ds_read_b128 a[168:171], v224 offset:2560\n\t"
"v_mfma_f32_16x16x32_bf16 v[156:159], a[28:31], a[172:175], v[156:159]\n\t"
"buffer_load_dwordx4 a[92:95], off, s[28:31], s51 offset:1024 sc1\n\t"
"v_mfma_f32_16x16x32_bf16 v[192:195], a[224:227], a[176:179], 0\n\t"
"Lpad12_%=:\n\t"
"s_cbranch_execz Lret1_%=\n\t"
"v_mfma_f32_16x16x32_bf16 v[196:199], a[224:227], a[180:183], 0\n\t"
"buffer_load_dwordx4 a[84:87], off, s[84:87], s51 offset:3072 sc1\n\t"
"v_mfma_f32_16x16x32_bf16 v[200:203], a[224:227], a[184:187], 0\n\t"
"ds_read_b128 a[156:159], v224 offset:768\n\t"
"v_mfma_f32_16x16x32_bf16 v[204:207], a[224:227], a[188:191], 0\n\t"
".p2align 3\n\t"
"buffer_load_dwordx4 a[80:83], off, s[84:87], s51 offset:2048 sc1\n\t"
"v_pk_fma_f32 v[48:49], v[144:145], s[90:91], v[48:49]\n\t"
"v_pk_fma_f32 v[50:51], v[146:147], s[90:91], v[50:51]\n\t"
"v_pk_add_f32 v[144:145], v[48:49], v[144:145] neg_lo:[0,1] neg_hi:[0,1]\n\t"
"v_pk_add_f32 v[146:147], v[50:51], v[146:147] neg_lo:[0,1] neg_hi:[0,1]\n\t"
"v_pk_fma_f32 v[64:65], v[160:161], s[90:91], v[64:65]\n\t"
"v_pk_fma_f32 v[66:67], v[162:163], s[90:91], v[66:67]\n\t"
"v_pk_add_f32 v[160:161], v[64:65], v[160:161] neg_lo:[0,1] neg_hi:[0,1]\n\t"
"v_pk_add_f32 v[162:163], v[66:67], v[162:163] neg_lo:[0,1] neg_hi:[0,1]\n\t"
"v_pk_fma_f32 v[80:81], v[176:177], s[90:91], v[80:81]\n\t"
"v_pk_fma_f32 v[82:83], v[178:179], s[90:91], v[82:83]\n\t"
"v_pk_add_f32 v[176:177], v[80:81], v[176:177] neg_lo:[0,1] neg_hi:[0,1]\n\t"
"v_pk_add_f32 v[178:179], v[82:83], v[178:179] neg_lo:[0,1] neg_hi:[0,1]\n\t"
"v_pk_fma_f32 v[84:85], v[180:181], s[90:91], v[84:85]\n\t"
"v_pk_fma_f32 v[86:87], v[182:183], s[90:91], v[86:87]\n\t"
"v_pk_add_f32 v[180:181], v[84:85], v[180:181] neg_lo:[0,1] neg_hi:[0,1]\n\t"
"v_pk_add_f32 v[182:183], v[86:87], v[182:183] neg_lo:[0,1] neg_hi:[0,1]\n\t"
"v_pk_fma_f32 v[68:69], v[164:165], s[90:91], v[68:69]\n\t"
"v_pk_fma_f32 v[70:71], v[166:167], s[90:91], v[70:71]\n\t"
"v_pk_add_f32 v[164:165], v[68:69], v[164:165] neg_lo:[0,1] neg_hi:[0,1]\n\t"
"v_pk_add_f32 v[166:167], v[70:71], v[166:167] neg_lo:[0,1] neg_hi:[0,1]\n\t"
"v_pk_fma_f32 v[52:53], v[148:149], s[90:91], v[52:53]\n\t"
"v_pk_fma_f32 v[54:55], v[150:151], s[90:91], v[54:55]\n\t"
"v_pk_add_f32 v[148:149], v[52:53], v[148:149] neg_lo:[0,1] neg_hi:[0,1]\n\t"
"v_pk_add_f32 v[150:151], v[54:55], v[150:151] neg_lo:[0,1] neg_hi:[0,1]\n\t"
"v_pk_fma_f32 v[56:57], v[152:153], s[90:91], v[56:57]\n\t"
"v_pk_fma_f32 v[58:59], v[154:155], s[90:91], v[58:59]\n\t"
"v_pk_add_f32 v[152:153], v[56:57], v[152:153] neg_lo:[0,1] neg_hi:[0,1]\n\t"
"v_pk_add_f32 v[154:155], v[58:59], v[154:155] neg_lo:[0,1] neg_hi:[0,1]\n\t"
"v_pk_fma_f32 v[72:73], v[168:169], s[90:91], v[72:73]\n\t"
"v_pk_fma_f32 v[74:75], v[170:171], s[90:91], v[74:75]\n\t"
"v_pk_add_f32 v[168:169], v[72:73], v[168:169] neg_lo:[0,1] neg_hi:[0,1]\n\t"
"v_pk_add_f32 v[170:171], v[74:75], v[170:171] neg_lo:[0,1] neg_hi:[0,1]\n\t"
"v_pk_fma_f32 v[88:89], v[184:185], s[90:91], v[88:89]\n\t"
"v_pk_fma_f32 v[90:91], v[186:187], s[90:91], v[90:91]\n\t"
"v_pk_add_f32 v[184:185], v[88:89], v[184:185] neg_lo:[0,1] neg_hi:[0,1]\n\t"
"v_pk_add_f32 v[186:187], v[90:91], v[186:187] neg_lo:[0,1] neg_hi:[0,1]\n\t"
"v_pk_fma_f32 v[92:93], v[188:189], s[90:91], v[92:93]\n\t"
"v_pk_fma_f32 v[94:95], v[190:191], s[90:91], v[94:95]\n\t"
"v_pk_add_f32 v[188:189], v[92:93], v[188:189] neg_lo:[0,1] neg_hi:[0,1]\n\t"
"v_pk_add_f32 v[190:191], v[94:95], v[190:191] neg_lo:[0,1] neg_hi:[0,1]\n\t"
"v_pk_fma_f32 v[76:77], v[172:173], s[90:91], v[76:77]\n\t"
"v_pk_fma_f32 v[78:79], v[174:175], s[90:91], v[78:79]\n\t"
"v_pk_add_f32 v[172:173], v[76:77], v[172:173] neg_lo:[0,1] neg_hi:[0,1]\n\t"
"v_pk_add_f32 v[174:175], v[78:79], v[174:175] neg_lo:[0,1] neg_hi:[0,1]\n\t"
"v_pk_fma_f32 v[60:61], v[156:157], s[90:91], v[60:61]\n\t"
"v_pk_fma_f32 v[62:63], v[158:159], s[90:91], v[62:63]\n\t"
"v_pk_add_f32 v[156:157], v[60:61], v[156:157] neg_lo:[0,1] neg_hi:[0,1]\n\t"
"v_pk_add_f32 v[158:159], v[62:63], v[158:159] neg_lo:[0,1] neg_hi:[0,1]\n\t"
"v_mfma_f32_16x16x32_bf16 v[208:211], a[224:227], a[192:195], 0\n\t"
"buffer_load_dwordx4 a[72:75], off, s[84:87], s51 offset:0 sc1\n\t"
"v_mfma_f32_16x16x32_bf16 v[212:215], a[224:227], a[196:199], 0\n\t"
"ds_read_b128 a[172:175], v224 offset:2816\n\t"
"v_mfma_f32_16x16x32_bf16 v[216:219], a[224:227], a[200:203], 0\n\t"
"buffer_load_dwordx4 a[76:79], off, s[84:87], s51 offset:1024 sc1\n\t"
"v_mfma_f32_16x16x32_bf16 v[220:223], a[224:227], a[204:207], 0\n\t"
"s_waitcnt vmcnt(6) lgkmcnt(3)\n\t"
"v_cvt_pk_bf16_f32 v193, v193, v197\n\t"
"v_cvt_pk_bf16_f32 v194, v194, v198\n\t"
"v_mfma_f32_16x16x32_bf16 v[48:51], a[48:51], a[144:147], v[48:51]\n\t"
"ds_write_addtid_b32 v194 offset:20672\n\t"
"v_cvt_pk_bf16_f32 v195, v195, v199\n\t"
"v_mfma_f32_16x16x32_bf16 v[48:51], a[52:55], a[160:163], v[48:51]\n\t"
"ds_write_addtid_b32 v193 offset:24864\n\t"
"v_cvt_pk_bf16_f32 v201, v201, v205\n\t"
"v_mfma_f32_16x16x32_bf16 v[64:67], a[56:59], a[144:147], v[64:67]\n\t"
"ds_write_addtid_b32 v195 offset:28992\n\t"
"v_cvt_pk_bf16_f32 v192, v192, v196\n\t"
"v_mfma_f32_16x16x32_bf16 v[64:67], a[60:63], a[160:163], v[64:67]\n\t"
"ds_write_addtid_b32 v192 offset:16544\n\t"
"v_cvt_pk_bf16_f32 v209, v209, v213\n\t"
"v_mfma_f32_16x16x32_bf16 v[80:83], a[64:67], a[144:147], v[80:83]\n\t"
"ds_read_b128 a[144:147], v224 offset:1024\n\t"
"v_cvt_pk_bf16_f32 v200, v200, v204\n\t"
"v_mfma_f32_16x16x32_bf16 v[80:83], a[68:71], a[160:163], v[80:83]\n\t"
"ds_write_addtid_b32 v201 offset:26912\n\t"
"v_cvt_pk_bf16_f32 v48, v48, v49\n\t"
"v_mfma_f32_16x16x32_bf16 v[84:87], a[64:67], a[148:151], v[84:87]\n\t"
"ds_read_b128 a[160:163], v224 offset:3072\n\t"
"v_cvt_pk_bf16_f32 v218, v218, v222\n\t"
"v_mfma_f32_16x16x32_bf16 v[84:87], a[68:71], a[164:167], v[84:87]\n\t"
"ds_write_addtid_b32 v209 offset:25888\n\t"
"v_cvt_pk_bf16_f32 v216, v216, v220\n\t"
"v_cvt_pk_bf16_f32 v49, v50, v51\n\t"
"v_mfma_f32_16x16x32_bf16 v[68:71], a[56:59], a[148:151], v[68:71]\n\t"
"ds_write_addtid_b32 v218 offset:23744\n\t"
"v_cvt_pk_bf16_f32 v202, v202, v206\n\t"
"v_mfma_f32_16x16x32_bf16 v[68:71], a[60:63], a[164:167], v[68:71]\n\t"
"ds_write_addtid_b32 v200 offset:18592\n\t"
"v_cvt_pk_bf16_f32 v219, v219, v223\n\t"
"v_mfma_f32_16x16x32_bf16 v[52:55], a[48:51], a[148:151], v[52:55]\n\t"
"ds_read_b128 a[148:151], v224 offset:1280\n\t"
"v_cvt_pk_bf16_f32 v80, v80, v81\n\t"
"v_mfma_f32_16x16x32_bf16 v[52:55], a[52:55], a[164:167], v[52:55]\n\t"
"s_waitcnt lgkmcnt(9)\n\t"
"ds_read_b128 a[164:167], v224 offset:3328\n\t"
"v_cvt_pk_bf16_f32 v208, v208, v212\n\t"
"v_cvt_pk_bf16_f32 v210, v210, v214\n\t"
"v_cvt_pk_bf16_f32 v211, v211, v215\n\t"
"v_cvt_pk_bf16_f32 v217, v217, v221\n\t"
"v_mfma_f32_16x16x32_bf16 v[56:59], a[48:51], a[152:155], v[56:59]\n\t"
"ds_write_addtid_b32 v211 offset:30016\n\t"
"v_cvt_pk_bf16_f32 v203, v203, v207\n\t"
"v_mfma_f32_16x16x32_bf16 v[56:59], a[52:55], a[168:171], v[56:59]\n\t"
"s_nop 0\n\t"
"ds_write_addtid_b32 v202 offset:22720\n\t"
"ds_write_addtid_b32 v203 offset:31040\n\t"
"ds_write_addtid_b32 v208 offset:17568\n\t"
"ds_write_addtid_b32 v216 offset:19616\n\t"
"buffer_load_dwordx4 a[112:115], off, s[28:31], s81 offset:0 sc1\n\t"
"buffer_load_dwordx4 a[116:119], off, s[28:31], s81 offset:1024 sc1\n\t"
"v_cvt_pk_bf16_f32 v84, v84, v85\n\t"
"v_mfma_f32_16x16x32_bf16 v[72:75], a[56:59], a[152:155], v[72:75]\n\t"
"ds_write_addtid_b32 v219 offset:32064\n\t"
"v_cvt_pk_bf16_f32 v81, v82, v83\n\t"
"v_mfma_f32_16x16x32_bf16 v[72:75], a[60:63], a[168:171], v[72:75]\n\t"
"ds_write_addtid_b32 v217 offset:27936\n\t"
"v_cvt_pk_bf16_f32 v52, v52, v53\n\t"
"v_cvt_pk_bf16_f32 v68, v68, v69\n\t"
"v_mfma_f32_16x16x32_bf16 v[88:91], a[64:67], a[152:155], v[88:91]\n\t"
"ds_read_b128 a[152:155], v224 offset:1536\n\t"
"v_cvt_pk_bf16_f32 v53, v54, v55\n\t"
"v_mfma_f32_16x16x32_bf16 v[88:91], a[68:71], a[168:171], v[88:91]\n\t"
"ds_read_b128 a[168:171], v224 offset:3584\n\t"
"buffer_load_dwordx4 a[108:111], off, s[84:87], s81 offset:3072 sc1\n\t"
"buffer_load_dwordx4 a[104:107], off, s[84:87], s81 offset:2048 sc1\n\t"
"v_mfma_f32_16x16x32_bf16 v[92:95], a[64:67], a[156:159], v[92:95]\n\t"
"v_cvt_pk_bf16_f32 v69, v70, v71\n\t"
"v_cvt_pk_bf16_f32 v85, v86, v87\n\t"
"v_mfma_f32_16x16x32_bf16 v[92:95], a[68:71], a[172:175], v[92:95]\n\t"
"ds_write_addtid_b32 v210 offset:21696\n\t"
"ds_write2st64_b64 v247, v[80:81], v[84:85] offset0:0 offset1:13\n\t"
"v_mfma_f32_16x16x32_bf16 v[76:79], a[56:59], a[156:159], v[76:79]\n\t"
"v_cvt_pk_bf16_f32 v64, v64, v65\n\t"
"v_cvt_pk_bf16_f32 v88, v88, v89\n\t"
"v_mfma_f32_16x16x32_bf16 v[76:79], a[60:63], a[172:175], v[76:79]\n\t"
"buffer_load_dwordx4 a[96:99], off, s[84:87], s81 offset:0 sc1\n\t"
"buffer_load_dwordx4 a[100:103], off, s[84:87], s81 offset:1024 sc1\n\t"
"v_cvt_pk_bf16_f32 v89, v90, v91\n\t"
"v_mfma_f32_16x16x32_bf16 v[60:63], a[48:51], a[156:159], v[60:63]\n\t"
"v_cvt_pk_bf16_f32 v65, v66, v67\n\t"
"v_cvt_pk_bf16_f32 v72, v72, v73\n\t"
"v_mfma_f32_16x16x32_bf16 v[60:63], a[52:55], a[172:175], v[60:63]\n\t"
"s_waitcnt vmcnt(6) lgkmcnt(6)\n\t"
"ds_read_b128 a[156:159], v224 offset:1792\n\t"
"ds_read_b128 a[172:175], v224 offset:3840\n\t"
"ds_write2st64_b64 v228, v[48:49], v[52:53] offset0:0 offset1:13\n\t"
"v_cvt_pk_bf16_f32 v73, v74, v75\n\t"
"v_mfma_f32_16x16x32_bf16 v[144:147], a[72:75], a[144:147], v[144:147]\n\t"
"v_cvt_pk_bf16_f32 v56, v56, v57\n\t"
"v_cvt_pk_bf16_f32 v92, v92, v93\n\t"
"v_mfma_f32_16x16x32_bf16 v[144:147], a[76:79], a[160:163], v[144:147]\n\t"
"buffer_load_dwordx4 a[136:139], off, s[28:31], s66 offset:0 sc1\n\t"
"buffer_load_dwordx4 a[140:143], off, s[28:31], s66 offset:1024 sc1\n\t"
"ds_write2st64_b64 v246, v[64:65], v[68:69] offset0:0 offset1:13\n\t"
"v_cvt_pk_bf16_f32 v57, v58, v59\n\t"
"v_cvt_pk_bf16_f32 v93, v94, v95\n\t"
"v_mfma_f32_16x16x32_bf16 v[160:163], a[80:83], a[144:147], v[160:163]\n\t"
"v_cvt_pk_bf16_f32 v60, v60, v61\n\t"
"v_cvt_pk_bf16_f32 v76, v76, v77\n\t"
"v_mfma_f32_16x16x32_bf16 v[160:163], a[84:87], a[160:163], v[160:163]\n\t"
"buffer_load_dwordx4 a[132:135], off, s[84:87], s66 offset:3072 sc1\n\t"
"ds_write2st64_b64 v247, v[88:89], v[92:93] offset0:26 offset1:39\n\t"
"v_mfma_f32_16x16x32_bf16 v[176:179], a[88:91], a[144:147], v[176:179]\n\t"
"v_cvt_pk_bf16_f32 v61, v62, v63\n\t"
"v_cvt_pk_bf16_f32 v77, v78, v79\n\t"
"v_mfma_f32_16x16x32_bf16 v[176:179], a[92:95], a[160:163], v[176:179]\n\t"
"ds_write2st64_b64 v228, v[56:57], v[60:61] offset0:26 offset1:39\n\t"
"v_mfma_f32_16x16x32_bf16 v[180:183], a[88:91], a[148:151], v[180:183]\n\t"
"buffer_load_dwordx4 a[128:131], off, s[84:87], s66 offset:2048 sc1\n\t"
"v_mfma_f32_16x16x32_bf16 v[180:183], a[92:95], a[164:167], v[180:183]\n\t"
"ds_write2st64_b64 v246, v[72:73], v[76:77] offset0:26 offset1:39\n\t"
"v_mfma_f32_16x16x32_bf16 v[164:167], a[80:83], a[148:151], v[164:167]\n\t"
"buffer_load_dwordx4 a[120:123], off, s[84:87], s66 offset:0 sc1\n\t"
"v_mfma_f32_16x16x32_bf16 v[164:167], a[84:87], a[164:167], v[164:167]\n\t"
"v_mfma_f32_16x16x32_bf16 v[148:151], a[72:75], a[148:151], v[148:151]\n\t"
"buffer_load_dwordx4 a[124:127], off, s[84:87], s66 offset:1024 sc1\n\t"
"v_mfma_f32_16x16x32_bf16 v[148:151], a[76:79], a[164:167], v[148:151]\n\t"
"s_waitcnt lgkmcnt(0)\n\t"
"s_barrier\n\t"
"v_mfma_f32_16x16x32_bf16 v[152:155], a[72:75], a[152:155], v[152:155]\n\t"
"s_nop 0\n\t"
"ds_read_b128 a[148:151], v224 offset:16800\n\t"
"ds_read_b128 a[12:15], v229 offset:13312\n\t"
"v_mfma_f32_16x16x32_bf16 v[152:155], a[76:79], a[168:171], v[152:155]\n\t"
"s_nop 0\n\t"
"ds_read_b128 a[144:147], v224 offset:16544\n\t"
"ds_read_b128 a[160:163], v224 offset:18592\n\t"
"v_mfma_f32_16x16x32_bf16 v[168:171], a[80:83], a[152:155], v[168:171]\n\t"
"ds_read_b128 a[0:3], v229 offset:0\n\t"
"v_mfma_f32_16x16x32_bf16 v[168:171], a[84:87], a[168:171], v[168:171]\n\t"
"ds_read_b128 a[164:167], v224 offset:18848\n\t"
"v_mfma_f32_16x16x32_bf16 v[184:187], a[88:91], a[152:155], v[184:187]\n\t"
"s_nop 0\n\t"
"ds_read_b128 a[4:7], v230 offset:0\n\t"
"ds_read_b128 a[16:19], v230 offset:13312\n\t"
"v_mfma_f32_16x16x32_bf16 v[184:187], a[92:95], a[168:171], v[184:187]\n\t"
"s_nop 0\n\t"
"ds_read_b128 a[8:11], v231 offset:0\n\t"
"ds_read_b128 a[20:23], v231 offset:13312\n\t"
"v_mfma_f32_16x16x32_bf16 v[188:191], a[88:91], a[156:159], v[188:191]\n\t"
"s_waitcnt lgkmcnt(8)\n\t"
"buffer_store_dwordx4 a[12:15], v232, s[16:19], s27 offen nt\n\t"
"v_mfma_f32_16x16x32_bf16 v[188:191], a[92:95], a[172:175], v[188:191]\n\t"
"ds_read_b128 a[152:155], v224 offset:17568\n\t"
"v_mfma_f32_16x16x32_bf16 v[172:175], a[80:83], a[156:159], v[172:175]\n\t"
"ds_read_b128 a[168:171], v224 offset:19616\n\t"
"v_mfma_f32_16x16x32_bf16 v[172:175], a[84:87], a[172:175], v[172:175]\n\t"
"s_waitcnt lgkmcnt(7)\n\t"
"buffer_store_dwordx4 a[0:3], v232, s[16:19], s24 offen nt\n\t"
"v_mfma_f32_16x16x32_bf16 v[156:159], a[72:75], a[156:159], v[156:159]\n\t"
"v_mfma_f32_16x16x32_bf16 v[156:159], a[76:79], a[172:175], v[156:159]\n\t"
"s_waitcnt vmcnt(8)\n\t"
"s_waitcnt lgkmcnt(5)\n\t"
"buffer_store_dwordx4 a[4:7], v233, s[16:19], s24 offen nt\n\t"
"v_mfma_f32_16x16x32_bf16 v[144:147], a[96:99], a[144:147], v[144:147]\n\t"
"ds_read_b128 a[156:159], v224 offset:17824\n\t"
"v_mfma_f32_16x16x32_bf16 v[144:147], a[100:103], a[160:163], v[144:147]\n\t"
"s_waitcnt lgkmcnt(5)\n\t"
"buffer_store_dwordx4 a[16:19], v233, s[16:19], s27 offen nt\n\t"
"v_mfma_f32_16x16x32_bf16 v[160:163], a[104:107], a[144:147], v[160:163]\n\t"
"ds_read_b128 a[172:175], v224 offset:19872\n\t"
"v_mfma_f32_16x16x32_bf16 v[160:163], a[108:111], a[160:163], v[160:163]\n\t"
"s_waitcnt lgkmcnt(4)\n\t"
"buffer_store_dwordx4 a[8:11], v234, s[16:19], s24 offen nt\n\t"
"v_mfma_f32_16x16x32_bf16 v[176:179], a[112:115], a[144:147], v[176:179]\n\t"
"ds_read_b128 a[144:147], v224 offset:17056\n\t"
"v_mfma_f32_16x16x32_bf16 v[176:179], a[116:119], a[160:163], v[176:179]\n\t"
"v_mfma_f32_16x16x32_bf16 v[180:183], a[112:115], a[148:151], v[180:183]\n\t"
"buffer_store_dwordx4 a[20:23], v234, s[16:19], s27 offen nt\n\t"
"v_mfma_f32_16x16x32_bf16 v[180:183], a[116:119], a[164:167], v[180:183]\n\t"
"ds_read_b128 a[160:163], v224 offset:19104\n\t"
"v_mfma_f32_16x16x32_bf16 v[164:167], a[104:107], a[148:151], v[164:167]\n\t"
"v_mfma_f32_16x16x32_bf16 v[164:167], a[108:111], a[164:167], v[164:167]\n\t"
"v_mfma_f32_16x16x32_bf16 v[148:151], a[96:99], a[148:151], v[148:151]\n\t"
"ds_read_b128 a[148:151], v224 offset:17312\n\t"
"v_mfma_f32_16x16x32_bf16 v[148:151], a[100:103], a[164:167], v[148:151]\n\t"
"s_waitcnt vmcnt(6) lgkmcnt(4)\n\t"
"v_mfma_f32_16x16x32_bf16 v[144:147], a[120:123], a[152:155], v[144:147]\n\t"
"ds_read_b128 a[164:167], v224 offset:19360\n\t"
"v_mfma_f32_16x16x32_bf16 v[144:147], a[124:127], a[168:171], v[144:147]\n\t"
"v_mfma_f32_16x16x32_bf16 v[160:163], a[128:131], a[152:155], v[160:163]\n\t"
"v_mfma_f32_16x16x32_bf16 v[160:163], a[132:135], a[168:171], v[160:163]\n\t"
"v_mfma_f32_16x16x32_bf16 v[176:179], a[136:139], a[152:155], v[176:179]\n\t"
"ds_read_b128 a[152:155], v224 offset:18080\n\t"
"v_mfma_f32_16x16x32_bf16 v[176:179], a[140:143], a[168:171], v[176:179]\n\t"
"s_waitcnt lgkmcnt(3)\n\t"
"s_nop 1\n\t"
"v_cvt_pk_bf16_f32 v144, v144, v145\n\t"
"v_cvt_pk_bf16_f32 v145, v146, v147\n\t"
"v_mfma_f32_16x16x32_bf16 v[180:183], a[136:139], a[156:159], v[180:183]\n\t"
"ds_read_b128 a[168:171], v224 offset:20128\n\t"
"v_mfma_f32_16x16x32_bf16 v[180:183], a[140:143], a[172:175], v[180:183]\n\t"
"v_mfma_f32_16x16x32_bf16 v[164:167], a[128:131], a[156:159], v[164:167]\n\t"
"v_cvt_pk_bf16_f32 v160, v160, v161\n\t"
"v_cvt_pk_bf16_f32 v176, v176, v177\n\t"
"v_mfma_f32_16x16x32_bf16 v[164:167], a[132:135], a[172:175], v[164:167]\n\t"
"v_cvt_pk_bf16_f32 v161, v162, v163\n\t"
"v_mfma_f32_16x16x32_bf16 v[148:151], a[120:123], a[156:159], v[148:151]\n\t"
"ds_read_b128 a[156:159], v224 offset:18336\n\t"
"s_nop 0\n\t"
"v_cvt_pk_bf16_f32 v180, v180, v181\n\t"
"v_mfma_f32_16x16x32_bf16 v[148:151], a[124:127], a[172:175], v[148:151]\n\t"
"ds_read_b128 a[172:175], v224 offset:20384\n\t"
"v_cvt_pk_bf16_f32 v181, v182, v183\n\t"
"v_mfma_f32_16x16x32_bf16 v[152:155], a[96:99], a[144:147], v[152:155]\n\t"
"v_cvt_pk_bf16_f32 v177, v178, v179\n\t"
"v_mfma_f32_16x16x32_bf16 v[152:155], a[100:103], a[160:163], v[152:155]\n\t"
"ds_write2st64_b64 v247, v[176:177], v[180:181] offset0:52 offset1:65\n\t"
"v_mfma_f32_16x16x32_bf16 v[168:171], a[104:107], a[144:147], v[168:171]\n\t"
"v_cvt_pk_bf16_f32 v164, v164, v165\n\t"
"v_cvt_pk_bf16_f32 v165, v166, v167\n\t"
"v_mfma_f32_16x16x32_bf16 v[168:171], a[108:111], a[160:163], v[168:171]\n\t"
"v_cvt_pk_bf16_f32 v148, v148, v149\n\t"
"v_cvt_pk_bf16_f32 v149, v150, v151\n\t"
"v_mfma_f32_16x16x32_bf16 v[184:187], a[112:115], a[144:147], v[184:187]\n\t"
"ds_write2st64_b64 v228, v[144:145], v[148:149] offset0:52 offset1:65\n\t"
"v_mfma_f32_16x16x32_bf16 v[184:187], a[116:119], a[160:163], v[184:187]\n\t"
"s_waitcnt lgkmcnt(4)\n\t"
"v_mfma_f32_16x16x32_bf16 v[188:191], a[112:115], a[148:151], v[188:191]\n\t"
"v_mfma_f32_16x16x32_bf16 v[188:191], a[116:119], a[164:167], v[188:191]\n\t"
"ds_write2st64_b64 v246, v[160:161], v[164:165] offset0:52 offset1:65\n\t"
"v_mfma_f32_16x16x32_bf16 v[172:175], a[104:107], a[148:151], v[172:175]\n\t"
"v_mfma_f32_16x16x32_bf16 v[172:175], a[108:111], a[164:167], v[172:175]\n\t"
"v_mfma_f32_16x16x32_bf16 v[156:159], a[96:99], a[148:151], v[156:159]\n\t"
"v_mfma_f32_16x16x32_bf16 v[156:159], a[100:103], a[164:167], v[156:159]\n\t"
"s_waitcnt lgkmcnt(0)\n\t"
"s_barrier\n\t"
"v_mfma_f32_16x16x32_bf16 v[152:155], a[120:123], a[152:155], v[152:155]\n\t"
"ds_read_b128 a[32:35], v231 offset:26624\n\t"
"v_mfma_f32_16x16x32_bf16 v[152:155], a[124:127], a[168:171], v[152:155]\n\t"
"ds_read_b128 a[24:27], v229 offset:26624\n\t"
"v_mfma_f32_16x16x32_bf16 v[168:171], a[128:131], a[152:155], v[168:171]\n\t"
"v_mfma_f32_16x16x32_bf16 v[168:171], a[132:135], a[168:171], v[168:171]\n\t"
"v_mfma_f32_16x16x32_bf16 v[184:187], a[136:139], a[152:155], v[184:187]\n\t"
"ds_read_b128 a[28:31], v230 offset:26624\n\t"
"s_nop 2\n\t"
"v_cvt_pk_bf16_f32 v152, v152, v153\n\t"
"v_mfma_f32_16x16x32_bf16 v[184:187], a[140:143], a[168:171], v[184:187]\n\t"
"v_cvt_pk_bf16_f32 v153, v154, v155\n\t"
"v_mfma_f32_16x16x32_bf16 v[188:191], a[136:139], a[156:159], v[188:191]\n\t"
"s_waitcnt lgkmcnt(2)\n\t"
"buffer_store_dwordx4 a[32:35], v234, s[16:19], s20 offen nt\n\t"
"v_mfma_f32_16x16x32_bf16 v[188:191], a[140:143], a[172:175], v[188:191]\n\t"
"v_cvt_pk_bf16_f32 v168, v168, v169\n\t"
"v_cvt_pk_bf16_f32 v169, v170, v171\n\t"
"v_mfma_f32_16x16x32_bf16 v[172:175], a[128:131], a[156:159], v[172:175]\n\t"
"v_cvt_pk_bf16_f32 v184, v184, v185\n\t"
"v_cvt_pk_bf16_f32 v185, v186, v187\n\t"
"v_mfma_f32_16x16x32_bf16 v[172:175], a[132:135], a[172:175], v[172:175]\n\t"
"s_waitcnt lgkmcnt(1)\n\t"
"buffer_store_dwordx4 a[24:27], v232, s[16:19], s20 offen nt\n\t"
"v_mfma_f32_16x16x32_bf16 v[156:159], a[120:123], a[156:159], v[156:159]\n\t"
"v_cvt_pk_bf16_f32 v188, v188, v189\n\t"
"v_cvt_pk_bf16_f32 v189, v190, v191\n\t"
"v_mfma_f32_16x16x32_bf16 v[156:159], a[124:127], a[172:175], v[156:159]\n\t"
"s_waitcnt lgkmcnt(0)\n\t"
"buffer_store_dwordx4 a[28:31], v233, s[16:19], s20 offen nt\n\t"
"s_nop 5\n\t"
"v_cvt_pk_bf16_f32 v156, v156, v157\n\t"
"v_cvt_pk_bf16_f32 v157, v158, v159\n\t"
"ds_write2st64_b64 v228, v[152:153], v[156:157] offset0:78 offset1:91\n\t"
"v_cvt_pk_bf16_f32 v172, v172, v173\n\t"
"v_cvt_pk_bf16_f32 v173, v174, v175\n\t"
"ds_write2st64_b64 v246, v[168:169], v[172:173] offset0:78 offset1:91\n\t"
"ds_write2st64_b64 v247, v[184:185], v[188:189] offset0:78 offset1:91\n\t"
"s_waitcnt lgkmcnt(0)\n\t"
"s_barrier\n\t"
"ds_read_b128 a[36:39], v229 offset:39936\n\t"
"ds_read_b128 a[40:43], v230 offset:39936\n\t"
"ds_read_b128 a[44:47], v231 offset:39936\n\t"
"s_waitcnt lgkmcnt(2)\n\t"
"buffer_store_dwordx4 a[36:39], v232, s[16:19], s25 offen nt\n\t"
"s_waitcnt lgkmcnt(1)\n\t"
"buffer_store_dwordx4 a[40:43], v233, s[16:19], s25 offen nt\n\t"
"s_waitcnt lgkmcnt(0)\n\t"
"buffer_store_dwordx4 a[44:47], v234, s[16:19], s25 offen nt\n\t"
"s_waitcnt vmcnt(0)\n\t"
"Lwino_end:\n\t"
  : "+{v205}"(tid)
  : "s"(__builtin_amdgcn_kernarg_segment_ptr()), "s"(blockIdx.x), "s"((unsigned)(size_t)(__attribute__((address_space(3))) char*)smem)
  : "v0", "v1", "v2", "v3", "v4", "v5", "v6", "v7", "v8", "v9", "v10", "v11", "v12", "v13", "v14", "v15", "v16", "v17", "v18", "v19", "v20", "v21", "v22", "v23", "v24", "v25", "v26", "v27", "v28", "v29", "v30", "v31", "v32", "v33", "v34", "v35", "v36", "v37", "v38", "v39", "v40", "v41", "v42", "v43", "v44", "v45", "v46", "v47", "v48", "v49", "v50", "v51", "v52", "v53", "v54", "v55", "v56", "v57", "v58", "v59", "v60", "v61", "v62", "v63", "v64", "v65", "v66", "v67", "v68", "v69", "v70", "v71", "v72", "v73", "v74", "v75", "v76", "v77", "v78", "v79", "v80", "v81", "v82", "v83", "v84", "v85", "v86", "v87", "v88", "v89", "v90", "v91", "v92", "v93", "v94", "v95", "v96", "v97", "v98", "v99", "v100", "v101", "v102", "v103", "v104", "v105", "v106", "v107", "v108", "v109", "v110", "v111", "v112", "v113", "v114", "v115", "v116", "v117", "v118", "v119", "v120", "v121", "v122", "v123", "v124", "v125", "v126", "v127", "v128", "v129", "v130", "v131", "v132", "v133", "v134", "v135", "v136", "v137", "v138", "v139", "v140", "v141", "v142", "v143", "v144", "v145", "v146", "v147", "v148", "v149", "v150", "v151", "v152", "v153", "v154", "v155", "v156", "v157", "v158", "v159", "v160", "v161", "v162", "v163", "v164", "v165", "v166", "v167", "v168", "v169", "v170", "v171", "v172", "v173", "v174", "v175", "v176", "v177", "v178", "v179", "v180", "v181", "v182", "v183", "v184", "v185", "v186", "v187", "v188", "v189", "v190", "v191", "v192", "v193", "v194", "v195", "v196", "v197", "v198", "v199", "v200", "v201", "v202", "v203", "v204", "v206", "v207", "v208", "v209", "v210", "v211", "v212", "v213", "v214", "v215", "v216", "v217", "v218", "v219", "v220", "v221", "v222", "v223", "v224", "v225", "v226", "v227", "v228", "v229", "v230", "v231", "v232", "v233", "v234", "v235", "v236", "v237", "v238", "v239", "v240", "v241", "v242", "v243", "v244", "v245", "v246", "v247", "v248", "v249", "v250", "v251", "v252", "v253", "v254", "v255", "a0", "a1", "a2", "a3", "a4", "a5", "a6", "a7", "a8", "a9", "a10", "a11", "a12", "a13", "a14", "a15", "a16", "a17", "a18", "a19", "a20", "a21", "a22", "a23", "a24", "a25", "a26", "a27", "a28", "a29", "a30", "a31", "a32", "a33", "a34", "a35", "a36", "a37", "a38", "a39", "a40", "a41", "a42", "a43", "a44", "a45", "a46", "a47", "a48", "a49", "a50", "a51", "a52", "a53", "a54", "a55", "a56", "a57", "a58", "a59", "a60", "a61", "a62", "a63", "a64", "a65", "a66", "a67", "a68", "a69", "a70", "a71", "a72", "a73", "a74", "a75", "a76", "a77", "a78", "a79", "a80", "a81", "a82", "a83", "a84", "a85", "a86", "a87", "a88", "a89", "a90", "a91", "a92", "a93", "a94", "a95", "a96", "a97", "a98", "a99", "a100", "a101", "a102", "a103", "a104", "a105", "a106", "a107", "a108", "a109", "a110", "a111", "a112", "a113", "a114", "a115", "a116", "a117", "a118", "a119", "a120", "a121", "a122", "a123", "a124", "a125", "a126", "a127", "a128", "a129", "a130", "a131", "a132", "a133", "a134", "a135", "a136", "a137", "a138", "a139", "a140", "a141", "a142", "a143", "a144", "a145", "a146", "a147", "a148", "a149", "a150", "a151", "a152", "a153", "a154", "a155", "a156", "a157", "a158", "a159", "a160", "a161", "a162", "a163", "a164", "a165", "a166", "a167", "a168", "a169", "a170", "a171", "a172", "a173", "a174", "a175", "a176", "a177", "a178", "a179", "a180", "a181", "a182", "a183", "a184", "a185", "a186", "a187", "a188", "a189", "a190", "a191", "a192", "a193", "a194", "a195", "a196", "a197", "a198", "a199", "a200", "a201", "a202", "a203", "a204", "a205", "a206", "a207", "a208", "a209", "a210", "a211", "a212", "a213", "a214", "a215", "a216", "a217", "a218", "a219", "a220", "a221", "a222", "a223", "a224", "a225", "a226", "a227", "a228", "a229", "a230", "a231", "a232", "a233", "a234", "a235", "a236", "a237", "a238", "a239", "a240", "a241", "a242", "a243", "a244", "a245", "a246", "a247", "a248", "a249", "a250", "a251", "a252", "a253", "a254", "a255", "s6", "s7", "s8", "s9", "s10", "s11", "s12", "s13", "s14", "s15", "s16", "s17", "s18", "s19", "s20", "s21", "s22", "s23", "s24", "s25", "s26", "s27", "s28", "s29", "s30", "s31", "s32", "s33", "s34", "s35", "s36", "s37", "s38", "s39", "s40", "s41", "s42", "s43", "s44", "s45", "s46", "s47", "s48", "s49", "s50", "s51", "s52", "s53", "s54", "s55", "s56", "s57", "s58", "s59", "s60", "s61", "s62", "s63", "s64", "s65", "s66", "s67", "s68", "s69", "s70", "s71", "s72", "s73", "s74", "s75", "s76", "s77", "s78", "s79", "s80", "s81", "s82", "s83", "s84", "s85", "s86", "s87", "s88", "s89", "s90", "s91", "s92", "s93", "s94", "s95", "s96", "s97", "s98", "s99", "scc", "vcc", "memory");
}

extern "C" void kernel_launch(void* const* d_in, const int* in_sizes, int n_in, void* d_out, int out_size, void* d_ws,
                              size_t ws_size, hipStream_t stream) {
  wino_main_kernel<<<256, 256, 0, stream>>>((const char*)d_in[0], (char*)d_ws, (char*)d_out, (const char*)d_in[1]);
}
```

```cpp
#include <hip/hip_runtime.h>
#include <hip/hip_bf16.h>
#include <stdint.h>

#define U_BYTES 1179648

__global__ __launch_bounds__(256, 1) __attribute__((section(".text.w"))) void wino_main_kernel(const char* __restrict__ x, char* __restrict__ Uq, char* __restrict__ out, const char* __restrict__ wt) {
  __shared__ __attribute__((aligned(1024))) char smem[163840];
  unsigned tid = threadIdx.x;
  asm volatile(
".pushsection .text\n\t"
".p2align 12\n\t"
".fill 576, 4, 0xbf800000\n\t"
".popsection\n\t"
".pushsection .text.x,\"ax\"\n\t"
".fill 256, 4, 0xbf800000\n\t"
".popsection\n\t"
"s_load_dwordx8 s[76:83], %1, 0x0\n\t"
"s_mov_b32 s66, %2\n\t"
"s_getpc_b64 s[6:7]\n\t"
"Lwino_pc:\n\t"
"s_bfe_u32 s42, %2, 0x50003\n\t"
"s_lshl_b32 s42, s42, 12\n\t"
"v_lshl_add_u32 v203, v205, 4, s42\n\t"
"v_min_u32 v203, (Lwino_end-Lwino_pc-16)&-16, v203\n\t"
"global_load_dwordx4 v[196:199], v203, s[6:7]\n\t"
"v_and_b32 v204, 63, v205\n\t"
"v_readfirstlane_b32 s73, v205\n\t"
"v_lshrrev_b32 v120, 3, v204\n\t"
"v_and_b32 v121, 7, v204\n\t"
"s_lshr_b32 s73, s73, 6\n\t"
"s_lshr_b32 s56, s66, 5\n\t"
"s_mul_i32 s56, s56, 12288\n\t"
"s_bfe_u32 s61, s66, 0x20001\n\t"
"s_mul_i32 s61, s61, 1536\n\t"
"s_add_u32 s56, s56, s61\n\t"
"s_bitcmp1_b32 s66, 0\n\t"
"s_cselect_b32 s61, 48, 0\n\t"
"s_add_u32 s56, s56, s61\n\t"
"s_bfe_u32 s62, s66, 0x20003\n\t"
"s_mul_i32 s61, s62, 96\n\t"
"s_add_u32 s56, s56, s61\n\t"
"s_cmp_lt_u32 s66, 192\n\t"
"s_cselect_b32 s56, s56, 2.0\n\t"
"v_bfe_u32 v42, v204, 3, 2\n\t"
"v_lshlrev_b32 v43, 2, v204\n\t"
"v_and_or_b32 v42, v43, 16, v42\n\t"
"v_mul_u32_u24 v42, 0x180, v42\n\t"
"v_lshrrev_b32 v44, 5, v204\n\t"
"v_mul_u32_u24 v43, 0x12000, v44\n\t"
"v_and_b32 v45, 3, v204\n\t"
"v_min_u32 v41, 2, v45\n\t"
"v_lshl_add_u32 v41, v41, 4, v42\n\t"
"v_add_u32 v41, v41, v43\n\t"
"v_lshlrev_b32 v43, 4, v204\n\t"
"v_bfe_u32 v45, v204, 1, 2\n\t"
"v_and_or_b32 v45, v43, 16, v45\n\t"
"v_mul_u32_u24 v45, 0x180, v45\n\t"
"v_lshl_add_u32 v44, v44, 3, v120\n\t"
"v_lshl_add_u32 v45, v44, 1, v45\n\t"
"v_mov_b32 v40, 0\n\t"
"s_mul_i32 s61, s73, 147456\n\t"
"s_add_u32 s61, s61, s56\n\t"
"s_lshl3_add_u32 s62, s73, s56\n\t"
"s_add_u32 s62, s62, 589824\n\t"
"s_lshl_b32 s64, s73, 10\n\t"
"s_and_b32 s60, s66, 7\n\t"
"s_lshr_b32 s65, s66, 3\n\t"
"s_mul_i32 s24, s60, 12582912\n\t"
"s_mul_i32 s63, s65, 6144\n\t"
"s_add_u32 s24, s24, s63\n\t"
"s_add_u32 s22, s24, 196224\n\t"
"s_mul_i32 s63, s73, 786432\n\t"
"s_mul_i32 s26, s73, 4608\n\t"
"s_mov_b32 s48, 189696\n\t"
"s_cmp_eq_u32 s65, 0\n\t"
"s_cselect_b64 s[40:41], 0xff, 0\n\t"
"s_cselect_b64 s[46:47], -1, 0\n\t"
"v_mul_u32_u24 v122, 0x300, v120\n\t"
"v_xor_b32 v123, 4, v121\n\t"
"v_add_u32 v122, s63, v122\n\t"
"v_and_b32 v124, 2, v120\n\t"
"v_xor_b32 v124, v124, v123\n\t"
"v_lshl_add_u32 v128, v124, 4, v122\n\t"
"v_cndmask_b32 v241, v128, 2.0, s[40:41]\n\t"
"v_add_u32 v122, 194816, v122\n\t"
"v_add_u32 v124, -1, v120\n\t"
"v_ashrrev_i32 v125, 31, v124\n\t"
"v_sub_u32 v124, v124, v125\n\t"
"v_and_b32 v124, 2, v124\n\t"
"v_xor_b32 v124, v124, v123\n\t"
"v_mad_i32_i24 v125, v125, s48, v122\n\t"
"v_lshl_add_u32 v129, v124, 4, v125\n\t"
"s_lshl_b64 s[40:41], s[40:41], 8\n\t"
"v_cndmask_b32 v242, v129, 2.0, s[40:41]\n\t"
"v_add_u32 v122, 194816, v122\n\t"
"v_add_u32 v124, -2, v120\n\t"
"v_ashrrev_i32 v125, 31, v124\n\t"
"v_sub_u32 v124, v124, v125\n\t"
"v_and_b32 v124, 2, v124\n\t"
"v_and_or_b32 v124, v125, 4, v124\n\t"
"v_xor_b32 v124, v124, v121\n\t"
"v_mad_i32_i24 v125, v125, s48, v122\n\t"
"v_lshl_add_u32 v130, v124, 4, v125\n\t"
"s_lshl_b64 s[40:41], s[40:41], 8\n\t"
"v_cndmask_b32 v243, v130, 2.0, s[40:41]\n\t"
"v_add_u32 v122, 194816, v122\n\t"
"v_add_u32 v124, -3, v120\n\t"
"v_ashrrev_i32 v125, 31, v124\n\t"
"v_sub_u32 v124, v124, v125\n\t"
"v_and_b32 v124, 2, v124\n\t"
"v_xor_b32 v124, v124, v121\n\t"
"v_mad_i32_i24 v125, v125, s48, v122\n\t"
"v_lshl_add_u32 v131, v124, 4, v125\n\t"
"s_lshl_b64 s[40:41], s[40:41], 8\n\t"
"v_cndmask_b32 v244, v131, 2.0, s[40:41]\n\t"
"v_add_u32 v124, 5, v120\n\t"
"v_and_b32 v124, 2, v124\n\t"
"v_xor_b32 v124, v124, v121\n\t"
"v_add_u32 v122, 2560, v122\n\t"
"v_lshl_add_u32 v245, v124, 4, v122\n\t"
"s_mov_b32 s37, -1\n\t"
"s_mov_b32 s36, 0\n\t"
"s_mul_i32 s67, s73, 48\n\t"
"s_add_u32 s67, s67, 118784\n\t"
"s_mov_b32 s10, 0x200000\n\t"
"s_mov_b32 s11, 0x20000\n\t"
"v_min_u32 v189, 47, v204\n\t"
"v_add_u32 v189, s67, v189\n\t"
"v_mul_u32_u24 v189, 16, v189\n\t"
"s_mov_b32 s54, 663552\n\t"
"s_mov_b32 s55, 0x20000\n\t"
"s_mov_b32 s14, 100663296\n\t"
"s_mov_b32 s15, 0x20000\n\t"
"s_add_u32 m0, s64, 0\n\t"
"s_waitcnt lgkmcnt(0)\n\t"
"s_mov_b32 s52, s82\n\t"
"s_and_b32 s53, s83, 0xffff\n\t"
"buffer_load_dwordx4 v41, s[52:55], s61 offen lds\n\t"
"buffer_load_short_d16_hi v40, v45, s[52:55], s62 offen\n\t"
"s_mov_b32 s8, s78\n\t"
"s_and_b32 s9, s79, 0xffff\n\t"
"buffer_load_dword v188, v189, s[8:11], 0 offen sc1\n\t"
"s_mov_b32 s12, s76\n\t"
"s_and_b32 s13, s77, 0xffff\n\t"
"s_add_u32 m0, s26, 103936\n\t"
"s_nop 0\n\t"
"buffer_load_dwordx4 v241, s[12:15], s22 offen offset:0 nt lds\n\t"
"buffer_load_dwordx4 v242, s[12:15], s22 offen offset:1024 nt lds\n\t"
"buffer_load_dwordx4 v243, s[12:15], s22 offen offset:2048 nt lds\n\t"
"buffer_load_dwordx4 v244, s[12:15], s22 offen offset:3072 nt lds\n\t"
"s_mov_b64 exec, s[36:37]\n\t"
"buffer_load_dwordx4 v245, s[12:15], s22 offen offset:3584 nt lds\n\t"
"s_mov_b64 exec, -1\n\t"
"s_mul_i32 s49, s73, 36\n\t"
"s_mul_i32 s43, s73, 576\n\t"
"s_sub_u32 s42, s22, 203520\n\t"
"s_mov_b32 s44, -1\n\t"
"s_mov_b32 s45, 15\n\t"
"v_min_u32 v124, 35, v204\n\t"
"v_add_u32 v124, s49, v124\n\t"
"v_lshrrev_b32 v119, 3, v124\n\t"
"v_and_b32 v124, 7, v124\n\t"
"v_mul_u32_u24 v127, 0x300, v119\n\t"
"v_cmp_gt_u32 vcc, 9, v119\n\t"
"v_add_u32 v119, -9, v119\n\t"
"v_ashrrev_i32 v125, 31, v119\n\t"
"v_add_u32 v127, s42, v127\n\t"
"v_mad_i32_i24 v127, v125, s48, v127\n\t"
"v_and_b32 v125, 9, v125\n\t"
"v_add_u32 v119, v119, v125\n\t"
"v_and_b32 v125, 2, v119\n\t"
"v_xor_b32 v124, v124, v125\n\t"
"v_lshl_add_u32 v133, v124, 4, v127\n\t"
"s_cmp_eq_u32 s60, 0\n\t"
"s_cselect_b64 s[50:51], vcc, 0\n\t"
"v_cmp_eq_u32 vcc, 0, v119\n\t"
"v_cndmask_b32 v246, v133, 2.0, s[50:51]\n\t"
"s_and_b64 vcc, vcc, s[46:47]\n\t"
"v_cndmask_b32 v246, v246, 2.0, vcc\n\t"
"s_add_u32 m0, s43, 101632\n\t"
"s_mov_b64 exec, s[44:45]\n\t"
"buffer_load_dwordx4 v246, s[12:15], 0 offen offset:0 nt lds\n\t"
"s_mov_b64 exec, -1\n\t"
"s_mul_i32 s70, s66, 16\n\t"
"s_add_u32 s70, s70, 1900544\n\t"
"s_lshl_b32 s61, s73, 8\n\t"
"s_or_b32 s61, s61, s66\n\t"
"s_cmp_lt_u32 s61, 192\n\t"
"s_cselect_b32 s70, s70, 2.0\n\t"
"s_lshr_b32 s61, s66, 3\n\t"
"s_lshl2_add_u32 s61, s61, s73\n\t"
"s_lshl_b32 s60, s61, 8\n\t"
"s_cmp_lt_u32 s61, 96\n\t"
"s_cselect_b32 s60, s60, 2.0\n\t"
"v_lshrrev_b32 v118, 1, v204\n\t"
"v_mul_u32_u24 v118, 220928, v118\n\t"
"v_lshl_add_u32 v118, v204, 7, v118\n\t"
"s_mov_b64 exec, 63\n\t"
"buffer_load_dword v118, v118, s[52:55], s60 offen\n\t"
"s_mov_b64 exec, -1\n\t"
"buffer_load_dword v187, off, s[8:11], s70 sc1\n\t"
"v_add_u32 v107, 0, v204\n\t"
"v_mul_u32_u24 v104, 0xab, v107\n\t"
"v_lshrrev_b32 v104, 10, v104\n\t"
"v_mul_u32_u24 v104, 0x120, v104\n\t"
"v_lshl_add_u32 v104, v107, 4, v104\n\t"
"v_add_u32 v107, 64, v204\n\t"
"v_mul_u32_u24 v105, 0xab, v107\n\t"
"v_lshrrev_b32 v105, 10, v105\n\t"
"v_mul_u32_u24 v105, 0x120, v105\n\t"
"v_lshl_add_u32 v105, v107, 4, v105\n\t"
"v_add_u32 v107, 128, v204\n\t"
"v_mul_u32_u24 v106, 0xab, v107\n\t"
"v_lshrrev_b32 v106, 10, v106\n\t"
"v_mul_u32_u24 v106, 0x120, v106\n\t"
"v_lshl_add_u32 v106, v107, 4, v106\n\t"
"s_mul_i32 s74, s73, 20736\n\t"
"s_mul_i32 s72, s73, 96\n\t"
"s_add_u32 s74, s74, 33280\n\t"
"s_add_u32 s75, s74, 12288\n\t"
"s_cmp_eq_u32 s73, 3\n\t"
"s_cselect_b32 s74, 16544, s74\n\t"
"s_cselect_b32 s75, 95488, s75\n\t"
"s_lshr_b32 s60, s66, 5\n\t"
"s_bfe_u32 s62, s66, 0x20003\n\t"
"s_and_b32 s61, s60, 1\n\t"
"s_lshl1_add_u32 s61, s73, s61\n\t"
"s_mul_i32 s62, s62, 288\n\t"
"s_add_u32 s61, s61, s62\n\t"
"s_lshr_b32 s60, s60, 1\n\t"
"s_mul_i32 s62, s60, 96\n\t"
"s_add_u32 s61, s61, s62\n\t"
"s_lshl_b32 s57, s61, 10\n\t"
"s_and_b32 s61, s66, 7\n\t"
"s_lshl_b32 s61, s61, 7\n\t"
"s_add_u32 s57, s57, s61\n\t"
"s_cmp_lt_u32 s73, 3\n\t"
"s_cselect_b32 s67, s66, 0xff\n\t"
"s_cmp_lt_u32 s67, 192\n\t"
"s_cselect_b32 s57, s57, 2.0\n\t"
"s_lshl_b32 s60, s73, 3\n\t"
"s_add_u32 s60, s60, 0\n\t"
"s_lshl_b32 s61, s73, 11\n\t"
"s_add_u32 s61, s61, 4096\n\t"
"v_lshl_add_u32 v46, v121, 6, s60\n\t"
"v_lshl_add_u32 v46, v44, 1, v46\n\t"
"v_lshl_add_u32 v42, v204, 1, s61\n\t"
"v_lshl_add_u32 v43, v204, 4, s61\n\t"
"v_mul_u32_u24 v44, 6144, v120\n\t"
"v_lshl_add_u32 v44, v121, 4, v44\n\t"
"v_mov_b32 v32, 0\n\t"
"v_mov_b32 v33, 0\n\t"
"v_mov_b32 v34, 0\n\t"
"v_mov_b32 v35, 0\n\t"
"v_mov_b32 v36, 0\n\t"
"v_mov_b32 v37, 0\n\t"
"v_mov_b32 v38, 0\n\t"
"v_mov_b32 v39, 0\n\t"
"s_waitcnt vmcnt(9)\n\t"
"s_barrier\n\t"
".p2align 3\n\t"
"ds_read_u16_d16_hi v32, v46 offset:0\n\t"
"ds_read_u16_d16_hi v33, v46 offset:512\n\t"
"ds_read_u16_d16_hi v34, v46 offset:1024\n\t"
"ds_read_u16_d16_hi v35, v46 offset:1536\n\t"
"ds_read_u16_d16_hi v36, v46 offset:2048\n\t"
"ds_read_u16_d16_hi v37, v46 offset:2560\n\t"
"ds_read_u16_d16_hi v38, v46 offset:3072\n\t"
"ds_read_u16_d16_hi v39, v46 offset:3584\n\t"
"s_waitcnt lgkmcnt(1)\n\t"
"v_add_f32 v48, v32, v38\n\t"
"s_waitcnt lgkmcnt(0)\n\t"
"v_add_f32 v49, v33, v39\n\t"
"v_add_f32 v50, v34, v40\n\t"
"v_add_f32 v51, v48, v35\n\t"
"v_sub_f32 v54, v48, v35\n\t"
"v_add_f32 v52, v49, v36\n\t"
"v_sub_f32 v55, v49, v36\n\t"
"v_add_f32 v53, v50, v37\n\t"
"v_sub_f32 v56, v50, v37\n\t"
"v_add_f32 v57, v32, v34\n\t"
"v_add_f32 v58, v51, v53\n\t"
"v_add_f32 v59, v54, v56\n\t"
"v_add_f32 v60, v38, v40\n\t"
"v_add_f32 v62, v57, v33\n\t"
"v_sub_f32 v63, v57, v33\n\t"
"v_add_f32 v64, v58, v52\n\t"
"v_sub_f32 v65, v58, v52\n\t"
"v_add_f32 v66, v59, v55\n\t"
"v_sub_f32 v67, v59, v55\n\t"
"v_add_f32 v68, v60, v39\n\t"
"v_sub_f32 v69, v60, v39\n\t"
"v_mul_f32 v71, 0.5, v62\n\t"
"v_mul_f32 v72, 0.5, v63\n\t"
"v_mul_f32 v74, 0.5, v51\n\t"
"v_mul_f32 v75, 0x3e800000, v64\n\t"
"v_mul_f32 v76, 0x3e800000, v65\n\t"
"v_mul_f32 v77, -0.5, v53\n\t"
"v_mul_f32 v79, 0.5, v66\n\t"
"v_mul_f32 v80, 0.5, v67\n\t"
"v_mul_f32 v83, -0.5, v68\n\t"
"v_mul_f32 v84, -0.5, v69\n\t"
".p2align 3\n\t"
"v_cvt_pk_bf16_f32 v86, v32, v71\n\t"
"v_cvt_pk_bf16_f32 v87, v72, -v34\n\t"
"v_cvt_pk_bf16_f32 v88, v74, v75\n\t"
"v_cvt_pk_bf16_f32 v89, v76, v77\n\t"
"v_cvt_pk_bf16_f32 v90, v54, v79\n\t"
"v_cvt_pk_bf16_f32 v91, v80, -v56\n\t"
"v_cvt_pk_bf16_f32 v92, -v38, v83\n\t"
"v_cvt_pk_bf16_f32 v93, v84, v40\n\t"
"ds_write_b16 v42, v86 offset:0\n\t"
"ds_write_b16_d16_hi v42, v86 offset:128\n\t"
"ds_write_b16 v42, v87 offset:256\n\t"
"ds_write_b16_d16_hi v42, v87 offset:384\n\t"
"ds_write_b16 v42, v88 offset:512\n\t"
"ds_write_b16_d16_hi v42, v88 offset:640\n\t"
"ds_write_b16 v42, v89 offset:768\n\t"
"ds_write_b16_d16_hi v42, v89 offset:896\n\t"
"ds_write_b16 v42, v90 offset:1024\n\t"
"ds_write_b16_d16_hi v42, v90 offset:1152\n\t"
"ds_write_b16 v42, v91 offset:1280\n\t"
"ds_write_b16_d16_hi v42, v91 offset:1408\n\t"
"ds_write_b16 v42, v92 offset:1536\n\t"
"ds_write_b16_d16_hi v42, v92 offset:1664\n\t"
"ds_write_b16 v42, v93 offset:1792\n\t"
"ds_write_b16_d16_hi v42, v93 offset:1920\n\t"
"ds_read_b128 v[96:99], v43 offset:0\n\t"
"ds_read_b128 v[100:103], v43 offset:1024\n\t"
"s_add_u32 s60, s57, 0\n\t"
"s_waitcnt lgkmcnt(1)\n\t"
"buffer_store_dwordx4 v[96:99], v44, s[8:11], s60 offen sc1\n\t"
"s_add_u32 s60, s57, 49152\n\t"
"s_waitcnt lgkmcnt(0)\n\t"
"buffer_store_dwordx4 v[100:103], v44, s[8:11], s60 offen sc1\n\t"
"v_lshrrev_b32 v123, 4, v204\n\t"
"v_bfe_u32 v124, v204, 2, 2\n\t"
"v_and_b32 v125, 3, v204\n\t"
"v_lshlrev_b32 v125, 3, v125\n\t"
"v_and_b32 v138, 49, v204\n\t"
"v_lshl_or_b32 v138, v124, 1, v138\n\t"
"v_and_b32 v139, 2, v204\n\t"
"v_lshl_or_b32 v138, v139, 2, v138\n\t"
"s_add_u32 s63, s26, 1152\n\t"
"v_and_b32 v126, 48, v204\n\t"
"v_and_b32 v127, 6, v204\n\t"
"v_lshl_add_u32 v126, v127, 5, v126\n\t"
"v_and_b32 v127, 8, v204\n\t"
"s_add_u32 m0, s74, 0\n\t"
"s_add_u32 s27, s72, 0\n\t"
"buffer_load_dwordx4 v104, s[52:55], s27 offen lds\n\t"
"v_mul_u32_u24 v134, 516, v127\n\t"
"v_and_b32 v127, 1, v204\n\t"
"v_mul_u32_u24 v127, 8320, v127\n\t"
"v_add3_u32 v224, v134, v126, v127\n\t"
"v_and_b32 v127, 16, v204\n\t"
"v_lshl_add_u32 v127, v127, 1, v125\n\t"
"v_lshl_add_u32 v127, v123, 8, v127\n\t"
"v_add_u32 v126, 4, v204\n\t"
"v_and_b32 v126, 8, v126\n\t"
"v_lshl_add_u32 v127, v126, 3, v127\n\t"
"v_mul_u32_u24 v126, 1152, v124\n\t"
"v_add3_u32 v226, v126, v127, s63\n\t"
"s_add_u32 m0, s74, 1024\n\t"
"s_add_u32 s27, s72, 0\n\t"
"buffer_load_dwordx4 v105, s[52:55], s27 offen lds\n\t"
"v_min_u32 v126, 2, v124\n\t"
"v_cmp_gt_u32 vcc, 48, v204\n\t"
"s_mov_b32 s48, 384\n\t"
"s_cmp_eq_u32 s65, 31\n\t"
"v_cndmask_b32 v126, v126, v123, vcc\n\t"
"v_cndmask_b32 v127, 4, v124, vcc\n\t"
"v_and_b32 v134, 1, v126\n\t"
"v_lshl_add_u32 v134, v134, 6, v125\n\t"
"v_lshl_add_u32 v134, v127, 7, v134\n\t"
"v_and_b32 v127, 2, v127\n\t"
"v_lshl_add_u32 v134, v127, 4, v134\n\t"
"v_mul_u32_u24 v126, 2304, v126\n\t"
"v_add3_u32 v227, v126, v134, s26\n\t"
"v_lshlrev_b32 v126, 14, v204\n\t"
"v_and_b32 v126, 0x8000, v126\n\t"
"v_or_b32 v134, 0x3f803f80, v126\n\t"
"v_and_b32 v126, 1, v204\n\t"
"v_cmp_eq_u32 vcc, 0, v126\n\t"
"s_cselect_b64 s[40:41], 0xff, 0\n\t"
"s_cselect_b64 s[46:47], -1, 0\n\t"
"s_add_u32 m0, s74, 2048\n\t"
"s_add_u32 s27, s72, 0\n\t"
"buffer_load_dwordx4 v106, s[52:55], s27 offen lds\n\t"
"v_cndmask_b32 v135, v134, 0, vcc\n\t"
"v_cndmask_b32 v134, 0, v134, vcc\n\t"
"v_xor_b32 v136, 0x80008000, v134\n\t"
"v_xor_b32 v137, 0x80008000, v135\n\t"
"s_add_u32 m0, s74, 3072\n\t"
"s_add_u32 s27, s72, 12288\n\t"
"buffer_load_dwordx4 v104, s[52:55], s27 offen lds\n\t"
"v_accvgpr_write_b32 a220, v134\n\t"
"v_accvgpr_write_b32 a221, v135\n\t"
"v_accvgpr_write_b32 a222, v136\n\t"
"v_accvgpr_write_b32 a223, v137\n\t"
"v_xad_u32 v236, v128, 64, s48\n\t"
"v_xad_u32 v237, v129, 64, s48\n\t"
"v_cndmask_b32 v237, v237, 2.0, s[40:41]\n\t"
"s_lshl_b64 s[40:41], s[40:41], 8\n\t"
"v_xad_u32 v238, v130, 64, s48\n\t"
"v_cndmask_b32 v238, v238, 2.0, s[40:41]\n\t"
"s_lshl_b64 s[40:41], s[40:41], 8\n\t"
"v_xad_u32 v239, v131, 64, s48\n\t"
"v_cndmask_b32 v239, v239, 2.0, s[40:41]\n\t"
"s_add_u32 m0, s74, 4096\n\t"
"s_add_u32 s27, s72, 12288\n\t"
"buffer_load_dwordx4 v105, s[52:55], s27 offen lds\n\t"
"s_lshl_b64 s[40:41], s[40:41], 40\n\t"
"v_xad_u32 v240, v245, 64, s48\n\t"
"v_cndmask_b32 v240, v240, 2.0, s[40:41]\n\t"
"v_cmp_eq_u32 vcc, 8, v119\n\t"
"v_xad_u32 v247, v133, 64, s48\n\t"
"s_and_b64 vcc, vcc, s[46:47]\n\t"
"v_cndmask_b32 v247, v247, 2.0, s[50:51]\n\t"
"v_cndmask_b32 v247, v247, 2.0, vcc\n\t"
"v_and_b32 v139, 7, v138\n\t"
"v_bfe_u32 v126, v138, 3, 1\n\t"
"v_lshrrev_b32 v127, 4, v138\n\t"
"v_sub_u32 v127, v127, v126\n\t"
"s_add_u32 m0, s74, 5120\n\t"
"s_add_u32 s27, s72, 12288\n\t"
"buffer_load_dwordx4 v106, s[52:55], s27 offen lds\n\t"
"v_mov_b32 v134, 0xbf803f80\n\t"
"v_mov_b32 v135, 0x3f80bf80\n\t"
"v_cmp_eq_u32 vcc, 0, v127\n\t"
"v_add_u32 v126, 4, v126\n\t"
"s_nop 0\n\t"
"v_cndmask_b32 v134, 0, v134, vcc\n\t"
"v_cmp_eq_u32 vcc, 1, v127\n\t"
"s_nop 1\n\t"
"v_cndmask_b32 v134, v134, v135, vcc\n\t"
"v_cmp_eq_u32 vcc, 7, v139\n\t"
"s_nop 1\n\t"
"v_cndmask_b32 v135, 0, v135, vcc\n\t"
"v_cmp_gt_u32 vcc, 48, v138\n\t"
"s_nop 1\n\t"
"v_cndmask_b32 v134, v135, v134, vcc\n\t"
"v_mov_b32 v135, 0\n\t"
"s_add_u32 m0, s74, 6144\n\t"
"s_add_u32 s27, s72, 442368\n\t"
"buffer_load_dwordx4 v104, s[52:55], s27 offen lds\n\t"
"v_cndmask_b32 v136, v126, v139, vcc\n\t"
"v_and_b32 v137, 1, v136\n\t"
"v_lshlrev_b32 v137, 4, v137\n\t"
"v_lshrrev_b32 v136, 1, v136\n\t"
"v_lshlrev_b64 v[134:135], v137, v[134:135]\n\t"
"v_cmp_eq_u32 vcc, 0, v136\n\t"
"s_nop 1\n\t"
"v_cndmask_b32 v140, 0, v134, vcc\n\t"
"v_accvgpr_write_b32 a224, v140\n\t"
"v_cndmask_b32 v141, 0, v135, vcc\n\t"
"v_cmp_eq_u32 vcc, 1, v136\n\t"
"v_and_b32 v140, 0xff7fff7f, v140\n\t"
"v_xor_b32 v148, 0x80008000, v140\n\t"
"s_add_u32 m0, s74, 7168\n\t"
"s_add_u32 s27, s72, 442368\n\t"
"buffer_load_dwordx4 v105, s[52:55], s27 offen lds\n\t"
"v_cndmask_b32 v141, v141, v134, vcc\n\t"
"v_cndmask_b32 v142, 0, v135, vcc\n\t"
"v_cmp_eq_u32 vcc, 2, v136\n\t"
"v_accvgpr_write_b32 a225, v141\n\t"
"v_accvgpr_write_b32 a216, v140\n\t"
"v_cndmask_b32 v142, v142, v134, vcc\n\t"
"v_cndmask_b32 v143, 0, v135, vcc\n\t"
"v_cmp_eq_u32 vcc, 3, v136\n\t"
"v_accvgpr_write_b32 a226, v142\n\t"
"v_and_b32 v141, 0xff7fff7f, v141\n\t"
"v_cndmask_b32 v143, v143, v134, vcc\n\t"
"v_accvgpr_write_b32 a227, v143\n\t"
"v_xor_b32 v149, 0x80008000, v141\n\t"
"s_add_u32 m0, s74, 8192\n\t"
"s_add_u32 s27, s72, 442368\n\t"
"buffer_load_dwordx4 v106, s[52:55], s27 offen lds\n\t"
"v_accvgpr_write_b32 a217, v141\n\t"
"v_and_b32 v142, 0xff7fff7f, v142\n\t"
"v_xor_b32 v150, 0x80008000, v142\n\t"
"v_accvgpr_write_b32 a218, v142\n\t"
"v_and_b32 v143, 0xff7fff7f, v143\n\t"
"v_xor_b32 v151, 0x80008000, v143\n\t"
"v_accvgpr_write_b32 a219, v143\n\t"
"s_mov_b32 s68, 3000\n\t"
"v_and_b32 v107, 51, v204\n\t"
"v_mul_u32_u24 v107, 24, v107\n\t"
"v_and_b32 v47, 8, v204\n\t"
"v_mad_u32_u24 v107, v47, 12, v107\n\t"
"s_add_u32 m0, s74, 9216\n\t"
"s_add_u32 s27, s72, 454656\n\t"
"buffer_load_dwordx4 v104, s[52:55], s27 offen lds\n\t"
"v_and_b32 v47, 4, v204\n\t"
"v_mul_u32_u24 v47, 0x180, v47\n\t"
"v_add_u32 v107, v47, v107\n\t"
"v_add_u32 v47, s75, v107\n\t"
"v_add_u32 v107, s74, v107\n\t"
"v_lshrrev_b32 v112, 4, v204\n\t"
"v_bfe_u32 v113, v204, 2, 2\n\t"
"v_cmp_eq_u32 vcc, v112, v113\n\t"
"v_lshlrev_b32 v112, 4, v204\n\t"
"v_mov_b32 v113, 0x3e80\n\t"
"v_lshlrev_b32 v113, v112, v113\n\t"
"v_cndmask_b32 v113, 0, v113, vcc\n\t"
"s_add_u32 m0, s74, 10240\n\t"
"s_add_u32 s27, s72, 454656\n\t"
"buffer_load_dwordx4 v105, s[52:55], s27 offen lds\n\t"
"v_and_b32 v112, 2, v204\n\t"
"v_cmp_eq_u32 vcc, 0, v112\n\t"
"v_mov_b32 v108, 0\n\t"
"v_mov_b32 v109, 0\n\t"
"v_cndmask_b32 v110, 0, v113, vcc\n\t"
"v_cndmask_b32 v111, v113, 0, vcc\n\t"
"v_xor_b32 v114, 0x80008000, v110\n\t"
"v_xor_b32 v115, 0x80008000, v111\n\t"
"v_mov_b32 v112, 0\n\t"
"v_mov_b32 v113, 0\n\t"
"v_mov_b32 v116, 0\n\t"
"v_mov_b32 v117, 0\n\t"
"s_add_u32 m0, s74, 11264\n\t"
"s_add_u32 s27, s72, 454656\n\t"
"buffer_load_dwordx4 v106, s[52:55], s27 offen lds\n\t"
"s_waitcnt vmcnt(16)\n\t"
"s_waitcnt vmcnt(12)\n\t"
"v_readfirstlane_b32 s71, v187\n\t"
"s_barrier\n\t"
"s_add_u32 s71, s71, 1\n\t"
"v_mov_b32 v215, s71\n\t"
"s_mov_b64 exec, 1\n\t"
"buffer_store_dword v215, off, s[8:11], s70 sc1\n\t"
"s_mov_b64 exec, -1\n\t"
"v_add_u32 v248, 101632, v226\n\t"
"v_xor_b32 v249, 32, v248\n\t"
"v_xor_b32 v250, 64, v248\n\t"
"v_xor_b32 v251, 96, v248\n\t"
"ds_read_b64_tr_b16 a[178:179], v248 offset:128\n\t"
"ds_read_b64_tr_b16 a[186:187], v249 offset:128\n\t"
"s_add_u32 m0, s75, 0\n\t"
"s_add_u32 s27, s72, 221184\n\t"
"buffer_load_dwordx4 v104, s[52:55], s27 offen lds\n\t"
"ds_read_b64_tr_b16 a[194:195], v250 offset:128\n\t"
"ds_read_b64_tr_b16 a[202:203], v251 offset:128\n\t"
"ds_read_b64_tr_b16 a[176:177], v248 offset:0\n\t"
"s_add_u32 m0, s75, 1024\n\t"
"s_add_u32 s27, s72, 221184\n\t"
"buffer_load_dwordx4 v105, s[52:55], s27 offen lds\n\t"
"ds_read_b64_tr_b16 a[184:185], v249 offset:0\n\t"
"ds_read_b64_tr_b16 a[180:181], v249 offset:256\n\t"
"ds_read_b64_tr_b16 a[188:189], v248 offset:256\n\t"
"s_add_u32 m0, s75, 2048\n\t"
"s_add_u32 s27, s72, 221184\n\t"
"buffer_load_dwordx4 v106, s[52:55], s27 offen lds\n\t"
"ds_read_b64_tr_b16 a[192:193], v250 offset:0\n\t"
"ds_read_b64_tr_b16 a[200:201], v251 offset:0\n\t"
"ds_read_b64_tr_b16 a[196:197], v251 offset:256\n\t"
"s_add_u32 m0, s75, 3072\n\t"
"s_add_u32 s27, s72, 233472\n\t"
"buffer_load_dwordx4 v104, s[52:55], s27 offen lds\n\t"
"ds_read_b64_tr_b16 a[204:205], v250 offset:256\n\t"
"s_waitcnt lgkmcnt(11)\n\t"
"v_mfma_f32_4x4x4_16b_bf16 v[8:11], a[220:221], a[178:179], 0\n\t"
"s_waitcnt lgkmcnt(10)\n\t"
"v_mfma_f32_4x4x4_16b_bf16 v[12:15], a[220:221], a[186:187], 0\n\t"
"s_waitcnt lgkmcnt(9)\n\t"
"v_mfma_f32_4x4x4_16b_bf16 v[24:27], a[220:221], a[194:195], 0\n\t"
"s_waitcnt lgkmcnt(8)\n\t"
"v_mfma_f32_4x4x4_16b_bf16 v[28:31], a[220:221], a[202:203], 0\n\t"
"s_waitcnt lgkmcnt(7)\n\t"
"v_mfma_f32_4x4x4_16b_bf16 v[0:3], a[222:223], a[176:177], v[8:11]\n\t"
"s_waitcnt lgkmcnt(6)\n\t"
"v_mfma_f32_4x4x4_16b_bf16 v[4:7], a[222:223], a[184:185], v[12:15]\n\t"
"s_add_u32 m0, s75, 4096\n\t"
"s_add_u32 s27, s72, 233472\n\t"
"buffer_load_dwordx4 v105, s[52:55], s27 offen lds\n\t"
"s_add_u32 m0, s75, 5120\n\t"
"s_add_u32 s27, s72, 233472\n\t"
"buffer_load_dwordx4 v106, s[52:55], s27 offen lds\n\t"
"s_waitcnt lgkmcnt(5)\n\t"
"v_mfma_f32_4x4x4_16b_bf16 v[8:11], a[222:223], a[180:181], v[8:11]\n\t"
"s_waitcnt lgkmcnt(4)\n\t"
"v_mfma_f32_4x4x4_16b_bf16 v[12:15], a[222:223], a[188:189], v[12:15]\n\t"
"s_waitcnt lgkmcnt(3)\n\t"
"v_mfma_f32_4x4x4_16b_bf16 v[16:19], a[222:223], a[192:193], v[24:27]\n\t"
"s_waitcnt lgkmcnt(2)\n\t"
"v_mfma_f32_4x4x4_16b_bf16 v[20:23], a[222:223], a[200:201], v[28:31]\n\t"
"s_waitcnt lgkmcnt(1)\n\t"
"v_mfma_f32_4x4x4_16b_bf16 v[24:27], a[222:223], a[196:197], v[24:27]\n\t"
"s_waitcnt lgkmcnt(0)\n\t"
"v_mfma_f32_4x4x4_16b_bf16 v[28:31], a[222:223], a[204:205], v[28:31]\n\t"
"s_add_u32 m0, s26, 124544\n\t"
"s_nop 0\n\t"
"buffer_load_dwordx4 v241, s[12:15], s22 offen offset:128 nt lds\n\t"
"buffer_load_dwordx4 v242, s[12:15], s22 offen offset:1152 nt lds\n\t"
"v_cvt_pk_bf16_f32 v0, -v0, -v4\n\t"
"v_cvt_pk_bf16_f32 v1, -v1, -v5\n\t"
"v_cvt_pk_bf16_f32 v2, -v2, -v6\n\t"
"v_cvt_pk_bf16_f32 v3, -v3, -v7\n\t"
"s_lshl_b32 m0, s73, 8\n\t"
"s_nop 0\n\t"
".p2align 3\n\t"
"ds_write_addtid_b32 v0 offset:0\n\t"
"ds_write_addtid_b32 v1 offset:4128\n\t"
"ds_write_addtid_b32 v2 offset:1024\n\t"
"ds_write_addtid_b32 v3 offset:5152\n\t"
"v_cvt_pk_bf16_f32 v8, v8, v12\n\t"
"v_cvt_pk_bf16_f32 v9, v9, v13\n\t"
"v_cvt_pk_bf16_f32 v10, v10, v14\n\t"
"v_cvt_pk_bf16_f32 v11, v11, v15\n\t"
"ds_write_addtid_b32 v8 offset:8320\n\t"
"ds_write_addtid_b32 v9 offset:12448\n\t"
"ds_write_addtid_b32 v10 offset:9344\n\t"
"ds_write_addtid_b32 v11 offset:13472\n\t"
"v_cvt_pk_bf16_f32 v16, -v16, -v20\n\t"
"v_cvt_pk_bf16_f32 v17, -v17, -v21\n\t"
"v_cvt_pk_bf16_f32 v18, -v18, -v22\n\t"
"v_cvt_pk_bf16_f32 v19, -v19, -v23\n\t"
"ds_write_addtid_b32 v16 offset:2048\n\t"
"ds_write_addtid_b32 v17 offset:6176\n\t"
"ds_write_addtid_b32 v18 offset:3072\n\t"
"ds_write_addtid_b32 v19 offset:7200\n\t"
"v_cvt_pk_bf16_f32 v24, v24, v28\n\t"
"v_cvt_pk_bf16_f32 v25, v25, v29\n\t"
"v_cvt_pk_bf16_f32 v26, v26, v30\n\t"
"v_cvt_pk_bf16_f32 v27, v27, v31\n\t"
"ds_write_addtid_b32 v24 offset:10368\n\t"
"ds_write_addtid_b32 v25 offset:14496\n\t"
"ds_write_addtid_b32 v26 offset:11392\n\t"
"ds_write_addtid_b32 v27 offset:15520\n\t"
"v_add_u32 v248, 101632, v227\n\t"
"v_xor_b32 v249, 32, v248\n\t"
"v_xor_b32 v250, 64, v248\n\t"
"v_xor_b32 v251, 96, v248\n\t"
"ds_read_b64_tr_b16 a[176:177], v248 offset:0\n\t"
"ds_read_b64_tr_b16 a[178:179], v248 offset:512\n\t"
"ds_read_b64_tr_b16 a[180:181], v249 offset:0\n\t"
"ds_read_b64_tr_b16 a[182:183], v249 offset:512\n\t"
"s_add_u32 m0, s26, 124544\n\t"
"s_nop 0\n\t"
".p2align 3\n\t"
"buffer_load_dwordx4 v243, s[12:15], s22 offen offset:2176 nt lds\n\t"
"ds_read_b64_tr_b16 a[184:185], v250 offset:0\n\t"
"ds_read_b64_tr_b16 a[186:187], v250 offset:512\n\t"
"ds_read_b64_tr_b16 a[188:189], v251 offset:0\n\t"
"ds_read_b64_tr_b16 a[190:191], v251 offset:512\n\t"
"buffer_load_dwordx4 v244, s[12:15], s22 offen offset:3200 nt lds\n\t"
"ds_read_b64_tr_b16 a[192:193], v248 offset:1152\n\t"
"ds_read_b64_tr_b16 a[194:195], v248 offset:1664\n\t"
"ds_read_b64_tr_b16 a[196:197], v249 offset:1152\n\t"
"ds_read_b64_tr_b16 a[198:199], v249 offset:1664\n\t"
"s_mov_b64 exec, s[36:37]\n\t"
"buffer_load_dwordx4 v245, s[12:15], s22 offen offset:3712 nt lds\n\t"
"s_mov_b64 exec, -1\n\t"
"ds_read_b64_tr_b16 a[200:201], v250 offset:1152\n\t"
"ds_read_b64_tr_b16 a[202:203], v250 offset:1664\n\t"
"ds_read_b64_tr_b16 a[204:205], v251 offset:1152\n\t"
"ds_read_b64_tr_b16 a[206:207], v251 offset:1664\n\t"
"s_add_u32 m0, s43, 122240\n\t"
"s_mov_b64 exec, s[44:45]\n\t"
"buffer_load_dwordx4 v246, s[12:15], 0 offen offset:128 nt lds\n\t"
"s_mov_b64 exec, -1\n\t"
"s_mov_b64 exec, 0\n\t"
"s_cmp_eq_u32 s73, 0\n\t"
"s_cbranch_scc1 Lpad3_%=\n\t"
"s_cmp_eq_u32 s73, 1\n\t"
"s_cbranch_scc1 Lpad2_%=\n\t"
"s_cmp_eq_u32 s73, 2\n\t"
"s_cbranch_scc1 Lpad1_%=\n\t"
"s_branch Lpad0_%=\n\t"
".fill 7, 4, 0xbf800000\n\t"
"Lret0_%=:\n\t"
"s_mov_b64 exec, -1\n\t"
"s_waitcnt vmcnt(19)\n\t"
".p2align 3\n\t"
"ds_read_b64_tr_b16 a[48:49], v107 offset:0\n\t"
"ds_read_b64_tr_b16 a[50:51], v107 offset:192\n\t"
"ds_read_b64_tr_b16 a[52:53], v107 offset:3072\n\t"
"ds_read_b64_tr_b16 a[54:55], v107 offset:3264\n\t"
"ds_read_b64_tr_b16 a[56:57], v107 offset:8\n\t"
"ds_read_b64_tr_b16 a[58:59], v107 offset:200\n\t"
"ds_read_b64_tr_b16 a[60:61], v107 offset:3080\n\t"
"ds_read_b64_tr_b16 a[62:63], v107 offset:3272\n\t"
"ds_read_b64_tr_b16 a[64:65], v107 offset:16\n\t"
"ds_read_b64_tr_b16 a[66:67], v107 offset:208\n\t"
"ds_read_b64_tr_b16 a[68:69], v107 offset:3088\n\t"
"ds_read_b64_tr_b16 a[70:71], v107 offset:3280\n\t"
"s_waitcnt vmcnt(13)\n\t"
".p2align 3\n\t"
"ds_read_b64_tr_b16 a[72:73], v107 offset:6144\n\t"
"ds_read_b64_tr_b16 a[74:75], v107 offset:6336\n\t"
"ds_read_b64_tr_b16 a[76:77], v107 offset:9216\n\t"
"ds_read_b64_tr_b16 a[78:79], v107 offset:9408\n\t"
"ds_read_b64_tr_b16 a[80:81], v107 offset:6152\n\t"
"ds_read_b64_tr_b16 a[82:83], v107 offset:6344\n\t"
"ds_read_b64_tr_b16 a[84:85], v107 offset:9224\n\t"
"ds_read_b64_tr_b16 a[86:87], v107 offset:9416\n\t"
"ds_read_b64_tr_b16 a[88:89], v107 offset:6160\n\t"
"ds_read_b64_tr_b16 a[90:91], v107 offset:6352\n\t"
"ds_read_b64_tr_b16 a[92:93], v107 offset:9232\n\t"
"ds_read_b64_tr_b16 a[94:95], v107 offset:9424\n\t"
"s_waitcnt lgkmcnt(15)\n\t"
"v_mfma_f32_16x16x32_bf16 v[0:3], v[110:113], a[48:51], 0\n\t"
"s_waitcnt lgkmcnt(10)\n\t"
"v_mfma_f32_16x16x32_bf16 v[0:3], v[110:113], a[72:75], v[0:3]\n\t"
"s_nop 0\n\t"
"v_mfma_f32_16x16x32_bf16 v[8:11], v[108:111], a[48:51], 0\n\t"
"s_nop 0\n\t"
"v_mfma_f32_16x16x32_bf16 v[8:11], v[108:111], a[72:75], v[8:11]\n\t"
"s_add_u32 m0, s26, 145152\n\t"
"s_nop 0\n\t"
"buffer_load_dwordx4 v241, s[12:15], s22 offen offset:256 nt lds\n\t"
"v_mfma_f32_16x16x32_bf16 v[16:19], v[110:113], a[52:55], 0\n\t"
"s_waitcnt lgkmcnt(8)\n\t"
"v_mfma_f32_16x16x32_bf16 v[16:19], v[110:113], a[76:79], v[16:19]\n\t"
"s_nop 0\n\t"
"v_mfma_f32_16x16x32_bf16 v[24:27], v[108:111], a[52:55], 0\n\t"
"s_nop 0\n\t"
"v_mfma_f32_16x16x32_bf16 v[24:27], v[108:111], a[76:79], v[24:27]\n\t"
"buffer_load_dwordx4 v242, s[12:15], s22 offen offset:1280 nt lds\n\t"
"s_waitcnt vmcnt(8)\n\t"
".p2align 3\n\t"
"ds_read_b64_tr_b16 a[0:1], v47 offset:0\n\t"
"ds_read_b64_tr_b16 a[2:3], v47 offset:192\n\t"
"ds_read_b64_tr_b16 a[4:5], v47 offset:3072\n\t"
"ds_read_b64_tr_b16 a[6:7], v47 offset:3264\n\t"
"ds_read_b64_tr_b16 a[8:9], v47 offset:8\n\t"
"ds_read_b64_tr_b16 a[10:11], v47 offset:200\n\t"
"ds_read_b64_tr_b16 a[12:13], v47 offset:3080\n\t"
"ds_read_b64_tr_b16 a[14:15], v47 offset:3272\n\t"
"ds_read_b64_tr_b16 a[16:17], v47 offset:16\n\t"
"ds_read_b64_tr_b16 a[18:19], v47 offset:208\n\t"
"ds_read_b64_tr_b16 a[20:21], v47 offset:3088\n\t"
"ds_read_b64_tr_b16 a[22:23], v47 offset:3280\n\t"
"v_mfma_f32_16x16x32_bf16 v[32:35], v[110:113], a[56:59], 0\n\t"
"s_waitcnt lgkmcnt(15)\n\t"
"v_mfma_f32_16x16x32_bf16 v[32:35], v[110:113], a[80:83], v[32:35]\n\t"
"s_nop 0\n\t"
"v_mfma_f32_16x16x32_bf16 v[96:99], v[108:111], a[56:59], 0\n\t"
"s_nop 0\n\t"
"v_mfma_f32_16x16x32_bf16 v[96:99], v[108:111], a[80:83], v[96:99]\n\t"
"buffer_load_dwordx4 v243, s[12:15], s22 offen offset:2304 nt lds\n\t"
"v_mfma_f32_16x16x32_bf16 v[120:123], v[110:113], a[60:63], 0\n\t"
"s_nop 0\n\t"
"v_mfma_f32_16x16x32_bf16 v[120:123], v[110:113], a[84:87], v[120:123]\n\t"
"s_nop 0\n\t"
"v_mfma_f32_16x16x32_bf16 v[128:131], v[108:111], a[60:63], 0\n\t"
"s_nop 0\n\t"
"v_mfma_f32_16x16x32_bf16 v[128:131], v[108:111], a[84:87], v[128:131]\n\t"
"buffer_load_dwordx4 v244, s[12:15], s22 offen offset:3328 nt lds\n\t"
"v_mfma_f32_16x16x32_bf16 v[136:139], v[110:113], a[64:67], 0\n\t"
"s_waitcnt lgkmcnt(14)\n\t"
"v_mfma_f32_16x16x32_bf16 v[136:139], v[110:113], a[88:91], v[136:139]\n\t"
"s_nop 0\n\t"
"v_mfma_f32_16x16x32_bf16 v[152:155], v[108:111], a[64:67], 0\n\t"
"s_nop 0\n\t"
"v_mfma_f32_16x16x32_bf16 v[152:155], v[108:111], a[88:91], v[152:155]\n\t"
"s_mov_b64 exec, s[36:37]\n\t"
"buffer_load_dwordx4 v245, s[12:15], s22 offen offset:3840 nt lds\n\t"
"s_mov_b64 exec, -1\n\t"
"v_mfma_f32_16x16x32_bf16 v[160:163], v[110:113], a[68:71], 0\n\t"
"s_waitcnt lgkmcnt(12)\n\t"
"v_mfma_f32_16x16x32_bf16 v[160:163], v[110:113], a[92:95], v[160:163]\n\t"
"s_nop 0\n\t"
"v_mfma_f32_16x16x32_bf16 v[168:171], v[108:111], a[68:71], 0\n\t"
"s_nop 0\n\t"
"v_mfma_f32_16x16x32_bf16 v[168:171], v[108:111], a[92:95], v[168:171]\n\t"
"s_add_u32 m0, s43, 142848\n\t"
"s_mov_b64 exec, s[44:45]\n\t"
"buffer_load_dwordx4 v246, s[12:15], 0 offen offset:256 nt lds\n\t"
"s_mov_b64 exec, -1\n\t"
"s_waitcnt lgkmcnt(10)\n\t"
"v_mfma_f32_16x16x32_bf16 v[4:7], v[110:113], a[0:3], v[0:3]\n\t"
"s_nop 0\n\t"
"v_mfma_f32_16x16x32_bf16 v[0:3], v[114:117], a[0:3], v[0:3]\n\t"
"s_nop 0\n\t"
"v_mfma_f32_16x16x32_bf16 v[12:15], v[108:111], a[0:3], v[8:11]\n\t"
"s_nop 0\n\t"
"v_mfma_f32_16x16x32_bf16 v[8:11], v[112:115], a[0:3], v[8:11]\n\t"
"s_waitcnt lgkmcnt(8)\n\t"
"v_mfma_f32_16x16x32_bf16 v[20:23], v[110:113], a[4:7], v[16:19]\n\t"
"s_nop 0\n\t"
"v_mfma_f32_16x16x32_bf16 v[16:19], v[114:117], a[4:7], v[16:19]\n\t"
"s_nop 0\n\t"
"v_mfma_f32_16x16x32_bf16 v[28:31], v[108:111], a[4:7], v[24:27]\n\t"
"s_nop 0\n\t"
"v_cvt_pk_bf16_f32 v48, v4, v5\n\t"
"v_cvt_pk_bf16_f32 v49, v6, v7\n\t"
"v_mfma_f32_16x16x32_bf16 v[24:27], v[112:115], a[4:7], v[24:27]\n\t"
"s_nop 0\n\t"
"v_cvt_pk_bf16_f32 v72, v0, v1\n\t"
"v_cvt_pk_bf16_f32 v73, v2, v3\n\t"
"s_waitcnt lgkmcnt(6)\n\t"
"v_mfma_f32_16x16x32_bf16 v[36:39], v[110:113], a[8:11], v[32:35]\n\t"
"s_nop 0\n\t"
"v_cvt_pk_bf16_f32 v50, v12, v13\n\t"
"v_cvt_pk_bf16_f32 v51, v14, v15\n\t"
"v_mfma_f32_16x16x32_bf16 v[32:35], v[114:117], a[8:11], v[32:35]\n\t"
"s_nop 0\n\t"
"v_cvt_pk_bf16_f32 v74, v8, v9\n\t"
"v_cvt_pk_bf16_f32 v75, v10, v11\n\t"
"v_mfma_f32_16x16x32_bf16 v[100:103], v[108:111], a[8:11], v[96:99]\n\t"
"s_nop 0\n\t"
"v_cvt_pk_bf16_f32 v52, v20, v21\n\t"
"v_cvt_pk_bf16_f32 v53, v22, v23\n\t"
"v_mfma_f32_16x16x32_bf16 v[96:99], v[112:115], a[8:11], v[96:99]\n\t"
"s_nop 0\n\t"
"v_cvt_pk_bf16_f32 v76, v16, v17\n\t"
"v_cvt_pk_bf16_f32 v77, v18, v19\n\t"
"s_waitcnt lgkmcnt(4)\n\t"
"v_mfma_f32_16x16x32_bf16 v[124:127], v[110:113], a[12:15], v[120:123]\n\t"
"s_nop 0\n\t"
"v_cvt_pk_bf16_f32 v54, v28, v29\n\t"
"v_cvt_pk_bf16_f32 v55, v30, v31\n\t"
"v_mfma_f32_16x16x32_bf16 v[120:123], v[114:117], a[12:15], v[120:123]\n\t"
"s_nop 0\n\t"
"v_cvt_pk_bf16_f32 v78, v24, v25\n\t"
"v_cvt_pk_bf16_f32 v79, v26, v27\n\t"
"v_mfma_f32_16x16x32_bf16 v[132:135], v[108:111], a[12:15], v[128:131]\n\t"
"s_nop 0\n\t"
"v_cvt_pk_bf16_f32 v56, v36, v37\n\t"
"v_cvt_pk_bf16_f32 v57, v38, v39\n\t"
"v_mfma_f32_16x16x32_bf16 v[128:131], v[112:115], a[12:15], v[128:131]\n\t"
"s_nop 0\n\t"
"v_cvt_pk_bf16_f32 v80, v32, v33\n\t"
"v_cvt_pk_bf16_f32 v81, v34, v35\n\t"
"s_waitcnt lgkmcnt(2)\n\t"
"v_mfma_f32_16x16x32_bf16 v[140:143], v[110:113], a[16:19], v[136:139]\n\t"
"s_nop 0\n\t"
"v_cvt_pk_bf16_f32 v58, v100, v101\n\t"
"v_cvt_pk_bf16_f32 v59, v102, v103\n\t"
"v_mfma_f32_16x16x32_bf16 v[136:139], v[114:117], a[16:19], v[136:139]\n\t"
"s_nop 0\n\t"
"v_cvt_pk_bf16_f32 v82, v96, v97\n\t"
"v_cvt_pk_bf16_f32 v83, v98, v99\n\t"
"v_mfma_f32_16x16x32_bf16 v[156:159], v[108:111], a[16:19], v[152:155]\n\t"
"s_nop 0\n\t"
"v_cvt_pk_bf16_f32 v60, v124, v125\n\t"
"v_cvt_pk_bf16_f32 v61, v126, v127\n\t"
"v_mfma_f32_16x16x32_bf16 v[152:155], v[112:115], a[16:19], v[152:155]\n\t"
"s_nop 0\n\t"
"v_cvt_pk_bf16_f32 v84, v120, v121\n\t"
"v_cvt_pk_bf16_f32 v85, v122, v123\n\t"
"s_waitcnt lgkmcnt(0)\n\t"
"v_mfma_f32_16x16x32_bf16 v[164:167], v[110:113], a[20:23], v[160:163]\n\t"
"s_nop 0\n\t"
"v_cvt_pk_bf16_f32 v62, v132, v133\n\t"
"v_cvt_pk_bf16_f32 v63, v134, v135\n\t"
"v_mfma_f32_16x16x32_bf16 v[160:163], v[114:117], a[20:23], v[160:163]\n\t"
"s_nop 0\n\t"
"v_cvt_pk_bf16_f32 v86, v128, v129\n\t"
"v_cvt_pk_bf16_f32 v87, v130, v131\n\t"
"v_mfma_f32_16x16x32_bf16 v[172:175], v[108:111], a[20:23], v[168:171]\n\t"
"s_nop 0\n\t"
"v_cvt_pk_bf16_f32 v64, v140, v141\n\t"
"v_cvt_pk_bf16_f32 v65, v142, v143\n\t"
"v_mfma_f32_16x16x32_bf16 v[168:171], v[112:115], a[20:23], v[168:171]\n\t"
".p2align 3\n\t"
"v_cvt_pk_bf16_f32 v88, v136, v137\n\t"
"v_cvt_pk_bf16_f32 v89, v138, v139\n\t"
"v_cvt_pk_bf16_f32 v66, v156, v157\n\t"
"v_cvt_pk_bf16_f32 v67, v158, v159\n\t"
"v_cvt_pk_bf16_f32 v90, v152, v153\n\t"
"v_cvt_pk_bf16_f32 v91, v154, v155\n\t"
"v_cvt_pk_bf16_f32 v68, v164, v165\n\t"
"v_cvt_pk_bf16_f32 v69, v166, v167\n\t"
"v_cvt_pk_bf16_f32 v92, v160, v161\n\t"
"v_cvt_pk_bf16_f32 v93, v162, v163\n\t"
"v_cvt_pk_bf16_f32 v70, v172, v173\n\t"
"v_cvt_pk_bf16_f32 v71, v174, v175\n\t"
"v_cvt_pk_bf16_f32 v94, v168, v169\n\t"
"v_cvt_pk_bf16_f32 v95, v170, v171\n\t"
"s_barrier\n\t"
"v_mfma_f32_16x16x32_bf16 v[192:195], a[216:219], a[176:179], 0\n\t"
"s_add_u32 m0, s43, 53888\n\t"
"ds_read_b128 a[144:147], v224 offset:0\n\t"
"ds_read_b128 a[164:167], v224 offset:2304\n\t"
"v_mfma_f32_16x16x32_bf16 v[196:199], a[216:219], a[180:183], 0\n\t"
"s_mov_b64 exec, s[44:45]\n\t"
"buffer_load_dwordx4 v247, s[12:15], 0 offen offset:128 nt lds\n\t"
"s_mov_b64 exec, -1\n\t"
"v_mfma_f32_16x16x32_bf16 v[200:203], a[216:219], a[184:187], 0\n\t"
"s_lshl_b32 m0, s73, 8\n\t"
"ds_read_b128 a[160:163], v224 offset:2048\n\t"
"ds_read_b128 a[148:151], v224 offset:256\n\t"
"s_mul_i32 s92, s73, 294912\n\t"
"v_mfma_f32_16x16x32_bf16 v[204:207], a[216:219], a[188:191], 0\n\t"
"s_add_u32 s92, s92, s8\n\t"
"s_addc_u32 s93, s9, 0x100000\n\t"
"ds_read_b128 a[152:155], v224 offset:512\n\t"
"v_mfma_f32_16x16x32_bf16 v[208:211], v[148:151], a[192:195], 0\n\t"
"s_mov_b32 s94, s10\n\t"
"ds_read_b128 a[168:171], v224 offset:2560\n\t"
"ds_read_b128 a[156:159], v224 offset:768\n\t"
"v_mfma_f32_16x16x32_bf16 v[212:215], v[148:151], a[196:199], 0\n\t"
"s_waitcnt lgkmcnt(4)\n\t"
"ds_read_b128 a[172:175], v224 offset:2816\n\t"
"s_mov_b32 s95, 0x800000\n\t"
"v_mfma_f32_16x16x32_bf16 v[96:99], v[48:51], a[144:147], 0\n\t"
"s_add_u32 s96, s92, 4096\n\t"
"s_addc_u32 s97, s93, 0\n\t"
"v_mfma_f32_16x16x32_bf16 v[96:99], v[52:55], a[160:163], v[96:99]\n\t"
"s_mov_b64 s[98:99], s[94:95]\n\t"
"v_cvt_pk_bf16_f32 v192, v192, v196\n\t"
"v_cvt_pk_bf16_f32 v195, v195, v199\n\t"
"v_mfma_f32_16x16x32_bf16 v[112:115], v[56:59], a[144:147], 0\n\t"
"s_mov_b32 s16, s80\n\t"
"v_cvt_pk_bf16_f32 v193, v193, v197\n\t"
"v_cvt_pk_bf16_f32 v194, v194, v198\n\t"
"v_mfma_f32_16x16x32_bf16 v[112:115], v[60:63], a[160:163], v[112:115]\n\t"
"s_and_b32 s17, s81, 0xffff\n\t"
"ds_write_addtid_b32 v192 offset:16544\n\t"
"ds_write_addtid_b32 v193 offset:24864\n\t"
"ds_write_addtid_b32 v194 offset:20672\n\t"
"v_mfma_f32_16x16x32_bf16 v[128:131], v[64:67], a[144:147], 0\n\t"
"s_nop 0\n\t"
"ds_read_b128 a[144:147], v224 offset:1024\n\t"
"v_cvt_pk_bf16_f32 v203, v203, v207\n\t"
"v_mfma_f32_16x16x32_bf16 v[128:131], v[68:71], a[160:163], v[128:131]\n\t"
"s_waitcnt lgkmcnt(8)\n\t"
"ds_write_addtid_b32 v203 offset:31040\n\t"
"v_cvt_pk_bf16_f32 v200, v200, v204\n\t"
"v_cvt_pk_bf16_f32 v208, v208, v212\n\t"
"v_cvt_pk_bf16_f32 v209, v209, v213\n\t"
"s_mov_b32 s18, 100663296\n\t"
"v_mfma_f32_16x16x32_bf16 v[132:135], v[64:67], a[148:151], 0\n\t"
"s_mov_b32 s19, 0x20000\n\t"
"ds_read_b128 a[160:163], v224 offset:3072\n\t"
"v_cvt_pk_bf16_f32 v211, v211, v215\n\t"
"v_mfma_f32_16x16x32_bf16 v[132:135], v[68:71], a[164:167], v[132:135]\n\t"
"s_mov_b32 s21, 4\n\t"
"ds_write_addtid_b32 v200 offset:18592\n\t"
"ds_write_addtid_b32 v211 offset:30016\n\t"
"v_mfma_f32_16x16x32_bf16 v[116:119], v[56:59], a[148:151], 0\n\t"
"s_mov_b32 s38, -2.0\n\t"
"ds_write_addtid_b32 v195 offset:28992\n\t"
"ds_write_addtid_b32 v209 offset:25888\n\t"
"v_mfma_f32_16x16x32_bf16 v[116:119], v[60:63], a[164:167], v[116:119]\n\t"
"s_waitcnt vmcnt(7)\n\t"
"s_barrier\n\t"
"v_mfma_f32_16x16x32_bf16 v[216:219], v[148:151], a[200:203], 0\n\t"
"Lpad0_%=:\n\t"
"s_cbranch_execz Lpad4_%=\n\t"
"s_mov_b32 s39, -2.0\n\t"
"ds_read_b64_tr_b16 a[176:177], v248 offset:20736\n\t"
"ds_read_b64_tr_b16 a[178:179], v248 offset:21248\n\t"
"v_mbcnt_lo_u32_b32 v176, -1, 0\n\t"
"v_mfma_f32_16x16x32_bf16 v[100:103], v[48:51], a[148:151], 0\n\t"
"ds_read_b128 a[148:151], v224 offset:1280\n\t"
"v_mbcnt_hi_u32_b32 v176, -1, v176\n\t"
"v_mfma_f32_16x16x32_bf16 v[100:103], v[52:55], a[164:167], v[100:103]\n\t"
"s_waitcnt lgkmcnt(15)\n\t"
"ds_read_b64_tr_b16 a[180:181], v249 offset:20736\n\t"
"v_bfe_u32 v156, v176, 2, 2\n\t"
"v_and_b32 v157, 3, v176\n\t"
"v_lshlrev_b32 v157, 3, v157\n\t"
"v_cvt_pk_bf16_f32 v202, v202, v206\n\t"
"v_mfma_f32_16x16x32_bf16 v[104:107], v[48:51], a[152:155], 0\n\t"
"ds_read_b64_tr_b16 a[182:183], v249 offset:21248\n\t"
"v_cvt_pk_bf16_f32 v210, v210, v214\n\t"
"v_mfma_f32_16x16x32_bf16 v[104:107], v[52:55], a[168:171], v[104:107]\n\t"
"ds_read_b128 a[164:167], v224 offset:3328\n\t"
"v_and_b32 v158, 48, v176\n\t"
"v_lshl_add_u32 v158, v157, 3, v158\n\t"
"v_mfma_f32_16x16x32_bf16 v[220:223], v[148:151], a[204:207], 0\n\t"
"ds_read_b64_tr_b16 a[184:185], v250 offset:20736\n\t"
"v_and_b32 v159, 8, v176\n\t"
"v_mul_u32_u24 v166, 516, v159\n\t"
"v_and_b32 v159, 4, v176\n\t"
"v_mfma_f32_16x16x32_bf16 v[120:123], v[56:59], a[152:155], 0\n\t"
"ds_read_b64_tr_b16 a[186:187], v250 offset:21248\n\t"
"v_mul_u32_u24 v159, 2080, v159\n\t"
"v_mfma_f32_16x16x32_bf16 v[120:123], v[60:63], a[168:171], v[120:123]\n\t"
"ds_read_b64_tr_b16 a[188:189], v251 offset:20736\n\t"
"v_add3_u32 v166, v166, v158, v159\n\t"
"v_accvgpr_write_b32 a212, v224\n\t"
"v_accvgpr_write_b32 a213, v166\n\t"
"v_lshlrev_b32 v158, 15, v176\n\t"
"v_mfma_f32_16x16x32_bf16 v[136:139], v[64:67], a[152:155], 0\n\t"
"ds_read_b128 a[152:155], v224 offset:1536\n\t"
"v_and_b32 v158, 0x8000, v158\n\t"
"v_mfma_f32_16x16x32_bf16 v[136:139], v[68:71], a[168:171], v[136:139]\n\t"
"s_waitcnt lgkmcnt(15)\n\t"
"ds_read_b128 a[168:171], v224 offset:3584\n\t"
"buffer_load_dword v190, v189, s[8:11], 0 offen sc1\n\t"
"v_or_b32 v167, 0x3f803f80, v158\n\t"
"v_and_b32 v158, 2, v176\n\t"
"v_mul_u32_u24 v158, 0x4000, v158\n\t"
"v_cvt_pk_bf16_f32 v201, v201, v205\n\t"
"v_mfma_f32_16x16x32_bf16 v[140:143], v[64:67], a[156:159], 0\n\t"
"ds_write_addtid_b32 v210 offset:21696\n\t"
"v_lshl_or_b32 v158, v158, 16, v158\n\t"
"v_mfma_f32_16x16x32_bf16 v[140:143], v[68:71], a[172:175], v[140:143]\n\t"
"s_nop 0\n\t"
"ds_write_addtid_b32 v201 offset:26912\n\t"
"ds_write_addtid_b32 v202 offset:22720\n\t"
"ds_write_addtid_b32 v208 offset:17568\n\t"
"v_xor_b32 v166, v167, v158\n\t"
"v_accvgpr_write_b32 a210, v166\n\t"
"v_accvgpr_write_b32 a211, v167\n\t"
"s_add_u32 s61, s26, 4294900096\n\t"
"v_lshrrev_b32 v155, 4, v176\n\t"
"v_cvt_pk_bf16_f32 v216, v216, v220\n\t"
"v_cvt_pk_bf16_f32 v217, v217, v221\n\t"
"v_cvt_pk_bf16_f32 v218, v218, v222\n\t"
"v_mfma_f32_16x16x32_bf16 v[124:127], v[56:59], a[156:159], 0\n\t"
"ds_read_b64_tr_b16 a[190:191], v251 offset:21248\n\t"
"v_cvt_pk_bf16_f32 v219, v219, v223\n\t"
"v_mfma_f32_16x16x32_bf16 v[124:127], v[60:63], a[172:175], v[124:127]\n\t"
"ds_read_b64_tr_b16 a[192:193], v248 offset:21888\n\t"
"buffer_load_dword v191, v189, s[8:11], 0 offen sc1\n\t"
"v_mfma_f32_16x16x32_bf16 v[108:111], v[48:51], a[156:159], 0\n\t"
"ds_write_addtid_b32 v216 offset:19616\n\t"
"v_and_b32 v158, 1, v156\n\t"
"v_mfma_f32_16x16x32_bf16 v[108:111], v[52:55], a[172:175], v[108:111]\n\t"
"s_waitcnt lgkmcnt(15)\n\t"
"ds_write_addtid_b32 v217 offset:27936\n\t"
"ds_write_addtid_b32 v218 offset:23744\n\t"
"ds_write_addtid_b32 v219 offset:32064\n\t"
"v_lshrrev_b32 v159, 1, v156\n\t"
"v_mfma_f32_16x16x32_bf16 v[0:3], v[72:75], a[144:147], v[96:99]\n\t"
"s_add_u32 m0, s26, 56192\n\t"
"ds_read_b128 a[156:159], v224 offset:1792\n\t"
"v_mul_u32_u24 v166, 1152, v159\n\t"
"v_mfma_f32_16x16x32_bf16 v[0:3], v[76:79], a[160:163], v[0:3]\n\t"
"ds_read_b64_tr_b16 a[194:195], v248 offset:22400\n\t"
"buffer_load_dwordx4 v236, s[12:15], s22 offen offset:128 nt lds\n\t"
"v_mul_u32_u24 v167, 68352, v158\n\t"
"v_add3_u32 v166, v166, v157, s61\n\t"
"v_mfma_f32_16x16x32_bf16 v[16:19], v[80:83], a[144:147], v[112:115]\n\t"
"ds_read_b128 a[172:175], v224 offset:3840\n\t"
"v_add_u32 v166, v166, v167\n\t"
"v_mfma_f32_16x16x32_bf16 v[16:19], v[84:87], a[160:163], v[16:19]\n\t"
"ds_read_b64_tr_b16 a[196:197], v249 offset:21888\n\t"
"buffer_load_dwordx4 v237, s[12:15], s22 offen offset:1152 nt lds\n\t"
"v_xor_b32 v159, v159, v158\n\t"
"v_lshl_add_u32 v166, v159, 6, v166\n\t"
"v_xad_u32 v167, v158, 0, v155\n\t"
"v_mfma_f32_16x16x32_bf16 v[32:35], v[88:91], a[144:147], v[128:131]\n\t"
"ds_read_b64_tr_b16 a[198:199], v249 offset:22400\n\t"
"v_lshl_add_u32 v168, v167, 7, v166\n\t"
"v_mfma_f32_16x16x32_bf16 v[32:35], v[92:95], a[160:163], v[32:35]\n\t"
"s_waitcnt lgkmcnt(15)\n\t"
"ds_read_b64_tr_b16 a[200:201], v250 offset:21888\n\t"
"v_and_b32 v167, 2, v167\n\t"
"v_mfma_f32_16x16x32_bf16 v[36:39], v[88:91], a[148:151], v[132:135]\n\t"
"ds_read_b64_tr_b16 a[202:203], v250 offset:22400\n\t"
"v_lshl_add_u32 v168, v167, 4, v168\n\t"
"v_mfma_f32_16x16x32_bf16 v[36:39], v[92:95], a[164:167], v[36:39]\n\t"
"ds_read_b64_tr_b16 a[204:205], v251 offset:21888\n\t"
"buffer_load_dwordx4 v238, s[12:15], s22 offen offset:2176 nt lds\n\t"
"v_xor_b32 v168, 64, v168\n\t"
"v_accvgpr_write_b32 a208, v168\n\t"
"v_mfma_f32_16x16x32_bf16 v[20:23], v[80:83], a[148:151], v[116:119]\n\t"
"ds_read_b64_tr_b16 a[206:207], v251 offset:22400\n\t"
"v_xad_u32 v167, v158, 1, v155\n\t"
"v_mfma_f32_16x16x32_bf16 v[20:23], v[84:87], a[164:167], v[20:23]\n\t"
"v_lshl_add_u32 v168, v167, 7, v166\n\t"
"v_and_b32 v167, 2, v167\n\t"
"v_mfma_f32_16x16x32_bf16 v[4:7], v[72:75], a[148:151], v[100:103]\n\t"
"v_lshl_add_u32 v168, v167, 4, v168\n\t"
"v_accvgpr_write_b32 a209, v168\n\t"
"v_mfma_f32_16x16x32_bf16 v[4:7], v[76:79], a[164:167], v[4:7]\n\t"
"buffer_load_dwordx4 v239, s[12:15], s22 offen offset:3200 nt lds\n\t"
"s_lshl_b32 m0, s73, 8\n\t"
"s_mov_b64 exec, 0\n\t"
"s_cmp_eq_u32 s73, 0\n\t"
"s_cbranch_scc1 Lpad11_%=\n\t"
"s_cmp_eq_u32 s73, 1\n\t"
"s_cbranch_scc1 Lpad10_%=\n\t"
"s_cmp_eq_u32 s73, 2\n\t"
"s_cbranch_scc1 Lpad9_%=\n\t"
"s_branch Lpad8_%=\n\t"
"Lret1_%=:\n\t"
"s_mov_b64 exec, -1\n\t"
"s_waitcnt lgkmcnt(6)\n\t"
"s_barrier\n\t"
"v_mfma_f32_16x16x32_bf16 v[8:11], v[72:75], a[152:155], v[104:107]\n\t"
"s_add_u32 m0, s26, 56192\n\t"
"ds_read_b128 a[144:147], v224 offset:16544\n\t"
"ds_read_b128 a[160:163], v224 offset:18592\n\t"
"v_mfma_f32_16x16x32_bf16 v[8:11], v[76:79], a[168:171], v[8:11]\n\t"
"s_nop 0\n\t"
"ds_read_b128 a[148:151], v224 offset:16800\n\t"
"ds_read_b128 a[164:167], v224 offset:18848\n\t"
"v_mfma_f32_16x16x32_bf16 v[24:27], v[80:83], a[152:155], v[120:123]\n\t"
"v_mbcnt_lo_u32_b32 v176, -1, 0\n\t"
"v_mbcnt_hi_u32_b32 v176, -1, v176\n\t"
"v_mfma_f32_16x16x32_bf16 v[24:27], v[84:87], a[168:171], v[24:27]\n\t"
"v_and_b32 v154, 15, v176\n\t"
"v_lshrrev_b32 v155, 4, v176\n\t"
"v_mfma_f32_16x16x32_bf16 v[40:43], v[88:91], a[152:155], v[136:139]\n\t"
"v_bfe_u32 v156, v176, 2, 2\n\t"
"v_and_b32 v157, 3, v176\n\t"
"v_mfma_f32_16x16x32_bf16 v[40:43], v[92:95], a[168:171], v[40:43]\n\t"
"ds_read_b128 a[168:171], v224 offset:19104\n\t"
"v_lshlrev_b32 v157, 3, v157\n\t"
"s_add_u32 s60, s43, 33280\n\t"
"v_mfma_f32_16x16x32_bf16 v[44:47], v[88:91], a[156:159], v[140:143]\n\t"
"s_add_u32 s61, s26, 4294899008\n\t"
"ds_read_b128 a[152:155], v224 offset:17056\n\t"
"s_mul_i32 s62, s73, 96\n\t"
"v_mfma_f32_16x16x32_bf16 v[44:47], v[92:95], a[172:175], v[44:47]\n\t"
"s_add_u32 s62, s62, 75008\n\t"
"v_min_u32 v158, 35, v176\n\t"
"v_lshl_add_u32 v254, v158, 4, s60\n\t"
"v_add_u32 v255, 68352, v254\n\t"
"v_mfma_f32_16x16x32_bf16 v[28:31], v[80:83], a[156:159], v[124:127]\n\t"
"v_and_b32 v158, 1, v156\n\t"
"v_lshrrev_b32 v156, 3, v176\n\t"
"v_mfma_f32_16x16x32_bf16 v[28:31], v[84:87], a[172:175], v[28:31]\n\t"
"s_mov_b64 exec, s[36:37]\n\t"
"buffer_load_dwordx4 v240, s[12:15], s22 offen offset:3712 nt lds\n\t"
"s_mov_b64 exec, -1\n\t"
"v_add_u32 v159, v156, v158\n\t"
"v_mfma_f32_16x16x32_bf16 v[12:15], v[72:75], a[156:159], v[108:111]\n\t"
"s_lshl_b32 m0, s73, 8\n\t"
"ds_read_b128 a[156:159], v224 offset:17312\n\t"
"v_lshl_add_u32 v166, v159, 7, v157\n\t"
"v_mfma_f32_16x16x32_bf16 v[12:15], v[76:79], a[172:175], v[12:15]\n\t"
"v_and_b32 v159, 2, v159\n\t"
"v_lshl_add_u32 v166, v159, 4, v166\n\t"
"v_mfma_f32_16x16x32_bf16 v[192:195], a[216:219], a[176:179], 0\n\t"
"s_waitcnt vmcnt(6)\n\t"
"v_cmp_eq_u32 vcc, v190, v188\n\t"
"s_nop 4\n\t"
"s_cmp_eq_u64 vcc, 0\n\t"
"s_cbranch_scc1 Lgo_%=\n\t"
"s_sub_u32 s68, s68, 1\n\t"
"s_waitcnt vmcnt(5)\n\t"
"Lchk_%=:\n\t"
"v_cmp_eq_u32 vcc, v191, v188\n\t"
"s_nop 4\n\t"
"s_cmp_eq_u64 vcc, 0\n\t"
"s_cbranch_scc1 Lgo_%=\n\t"
"s_sub_u32 s68, s68, 1\n\t"
"s_cmp_eq_u32 s68, 0\n\t"
"s_cbranch_scc1 Lfb_%=\n\t"
"buffer_load_dword v191, v189, s[8:11], 0 offen sc1\n\t"
"s_waitcnt vmcnt(0)\n\t"
"s_branch Lchk_%=\n\t"
"Lgo_%=:\n\t"
"v_xad_u32 v167, v158, 1, v156\n\t"
"v_lshl_add_u32 v168, v167, 7, v157\n\t"
"v_and_b32 v167, 2, v167\n\t"
"v_lshl_add_u32 v168, v167, 4, v168\n\t"
"v_sub_u32 v168, v168, v166\n\t"
"v_mfma_f32_16x16x32_bf16 v[196:199], a[216:219], a[180:183], 0\n\t"
"v_mul_u32_u24 v159, 68288, v158\n\t"
"v_add_u32 v166, v166, v159\n\t"
"v_mfma_f32_16x16x32_bf16 v[200:203], a[216:219], a[184:187], 0\n\t"
"ds_read_b128 a[172:175], v224 offset:19360\n\t"
"v_lshl_add_u32 v168, v158, 7, v168\n\t"
"v_mfma_f32_16x16x32_bf16 v[204:207], a[216:219], a[188:191], 0\n\t"
"s_mov_b32 s54, 98304\n\t"
"buffer_load_dwordx4 a[96:99], off, s[92:95], s54 offset:0 sc1\n\t"
"v_subrev_u32 v253, 64, v168\n\t"
".p2align 3\n\t"
"v_pk_fma_f32 v[96:97], v[96:97], s[38:39], v[0:1] neg_lo:[1,0,1] neg_hi:[1,0,1]\n\t"
"v_pk_fma_f32 v[98:99], v[98:99], s[38:39], v[2:3] neg_lo:[1,0,1] neg_hi:[1,0,1]\n\t"
"v_pk_fma_f32 v[112:113], v[112:113], s[38:39], v[16:17] neg_lo:[1,0,1] neg_hi:[1,0,1]\n\t"
"v_pk_fma_f32 v[114:115], v[114:115], s[38:39], v[18:19] neg_lo:[1,0,1] neg_hi:[1,0,1]\n\t"
"v_pk_fma_f32 v[128:129], v[128:129], s[38:39], v[32:33] neg_lo:[1,0,1] neg_hi:[1,0,1]\n\t"
"v_pk_fma_f32 v[130:131], v[130:131], s[38:39], v[34:35] neg_lo:[1,0,1] neg_hi:[1,0,1]\n\t"
"v_pk_fma_f32 v[132:133], v[132:133], s[38:39], v[36:37] neg_lo:[1,0,1] neg_hi:[1,0,1]\n\t"
"v_pk_fma_f32 v[134:135], v[134:135], s[38:39], v[38:39] neg_lo:[1,0,1] neg_hi:[1,0,1]\n\t"
"v_pk_fma_f32 v[116:117], v[116:117], s[38:39], v[20:21] neg_lo:[1,0,1] neg_hi:[1,0,1]\n\t"
"v_pk_fma_f32 v[118:119], v[118:119], s[38:39], v[22:23] neg_lo:[1,0,1] neg_hi:[1,0,1]\n\t"
"v_pk_fma_f32 v[100:101], v[100:101], s[38:39], v[4:5] neg_lo:[1,0,1] neg_hi:[1,0,1]\n\t"
"v_pk_fma_f32 v[102:103], v[102:103], s[38:39], v[6:7] neg_lo:[1,0,1] neg_hi:[1,0,1]\n\t"
"v_pk_fma_f32 v[104:105], v[104:105], s[38:39], v[8:9] neg_lo:[1,0,1] neg_hi:[1,0,1]\n\t"
"v_pk_fma_f32 v[106:107], v[106:107], s[38:39], v[10:11] neg_lo:[1,0,1] neg_hi:[1,0,1]\n\t"
"v_pk_fma_f32 v[120:121], v[120:121], s[38:39], v[24:25] neg_lo:[1,0,1] neg_hi:[1,0,1]\n\t"
"v_pk_fma_f32 v[122:123], v[122:123], s[38:39], v[26:27] neg_lo:[1,0,1] neg_hi:[1,0,1]\n\t"
"v_pk_fma_f32 v[136:137], v[136:137], s[38:39], v[40:41] neg_lo:[1,0,1] neg_hi:[1,0,1]\n\t"
"v_pk_fma_f32 v[138:139], v[138:139], s[38:39], v[42:43] neg_lo:[1,0,1] neg_hi:[1,0,1]\n\t"
"v_pk_fma_f32 v[140:141], v[140:141], s[38:39], v[44:45] neg_lo:[1,0,1] neg_hi:[1,0,1]\n\t"
"v_pk_fma_f32 v[142:143], v[142:143], s[38:39], v[46:47] neg_lo:[1,0,1] neg_hi:[1,0,1]\n\t"
"v_pk_fma_f32 v[124:125], v[124:125], s[38:39], v[28:29] neg_lo:[1,0,1] neg_hi:[1,0,1]\n\t"
"v_pk_fma_f32 v[126:127], v[126:127], s[38:39], v[30:31] neg_lo:[1,0,1] neg_hi:[1,0,1]\n\t"
"v_pk_fma_f32 v[108:109], v[108:109], s[38:39], v[12:13] neg_lo:[1,0,1] neg_hi:[1,0,1]\n\t"
"v_pk_fma_f32 v[110:111], v[110:111], s[38:39], v[14:15] neg_lo:[1,0,1] neg_hi:[1,0,1]\n\t"
"v_mfma_f32_16x16x32_bf16 v[208:211], a[216:219], a[192:195], 0\n\t"
"s_waitcnt lgkmcnt(12)\n\t"
"v_add_u32 v225, s61, v166\n\t"
"v_mul_u32_u24 v158, 400, v154\n\t"
"v_mfma_f32_16x16x32_bf16 v[212:215], a[216:219], a[196:199], 0\n\t"
"s_waitcnt lgkmcnt(10)\n\t"
"buffer_load_dwordx4 a[100:103], off, s[92:95], s54 offset:1024 sc1\n\t"
"v_mfma_f32_16x16x32_bf16 v[216:219], a[216:219], a[200:203], 0\n\t"
"s_waitcnt lgkmcnt(8)\n\t"
"v_mul_u32_u24 v159, 24, v155\n\t"
"v_add3_u32 v228, v158, v159, s62\n\t"
"s_mul_i32 s60, s73, 3200\n\t"
"v_mfma_f32_16x16x32_bf16 v[220:223], a[216:219], a[204:207], 0\n\t"
"s_waitcnt lgkmcnt(6)\n\t"
"v_cvt_pk_bf16_f32 v194, v194, v198\n\t"
"v_cvt_pk_bf16_f32 v195, v195, v199\n\t"
"v_mfma_f32_16x16x32_bf16 v[0:3], a[48:51], a[144:147], v[0:3]\n\t"
"s_add_u32 s60, s60, 75008\n\t"
"ds_write_addtid_b32 v194 offset:4128\n\t"
"v_cvt_pk_bf16_f32 v193, v193, v197\n\t"
"v_mfma_f32_16x16x32_bf16 v[0:3], a[52:55], a[160:163], v[0:3]\n\t"
"ds_write_addtid_b32 v195 offset:12448\n\t"
"buffer_load_dwordx4 a[104:107], off, s[92:95], s54 offset:2048 sc1\n\t"
"v_cvt_pk_bf16_f32 v201, v201, v205\n\t"
"v_cvt_pk_bf16_f32 v203, v203, v207\n\t"
"v_mfma_f32_16x16x32_bf16 v[16:19], a[56:59], a[144:147], v[16:19]\n\t"
"s_and_b32 s61, s73, 2\n\t"
"ds_write_addtid_b32 v193 offset:8320\n\t"
"ds_write_addtid_b32 v203 offset:14496\n\t"
"v_mfma_f32_16x16x32_bf16 v[16:19], a[60:63], a[160:163], v[16:19]\n\t"
"s_nop 0\n\t"
"ds_write_addtid_b32 v201 offset:10368\n\t"
"v_cvt_pk_bf16_f32 v200, v200, v204\n\t"
"v_mfma_f32_16x16x32_bf16 v[32:35], a[64:67], a[144:147], v[32:35]\n\t"
"s_lshl_b32 s61, s61, 7\n\t"
"ds_read_b128 a[144:147], v224 offset:17568\n\t"
"ds_write_addtid_b32 v200 offset:2048\n\t"
"v_mfma_f32_16x16x32_bf16 v[32:35], a[68:71], a[160:163], v[32:35]\n\t"
"s_waitcnt lgkmcnt(11)\n\t"
"ds_read_b128 a[160:163], v224 offset:19616\n\t"
"buffer_load_dwordx4 a[108:111], off, s[92:95], s54 offset:3072 sc1\n\t"
"v_cvt_pk_bf16_f32 v202, v202, v206\n\t"
"v_cvt_pk_bf16_f32 v210, v210, v214\n\t"
"v_mfma_f32_16x16x32_bf16 v[36:39], a[64:67], a[148:151], v[36:39]\n\t"
"s_nop 0\n\t"
"ds_write_addtid_b32 v210 offset:5152\n\t"
"v_cvt_pk_bf16_f32 v219, v219, v223\n\t"
"v_mfma_f32_16x16x32_bf16 v[36:39], a[68:71], a[164:167], v[36:39]\n\t"
"s_nop 0\n\t"
"ds_write_addtid_b32 v219 offset:15520\n\t"
"buffer_load_dwordx4 a[112:115], off, s[96:99], s54 offset:0 sc1\n\t"
"s_barrier\n\t"
"v_mfma_f32_16x16x32_bf16 v[20:23], a[56:59], a[148:151], v[20:23]\n\t"
"s_add_u32 s60, s60, s61\n\t"
"ds_read_b64_tr_b16 a[176:177], v248 offset:41472\n\t"
"ds_read_b64_tr_b16 a[178:179], v248 offset:41984\n\t"
"v_mfma_f32_16x16x32_bf16 v[20:23], a[60:63], a[164:167], v[20:23]\n\t"
"s_mul_i32 s61, s73, 393216\n\t"
"ds_read_b64_tr_b16 a[180:181], v249 offset:41472\n\t"
"v_cvt_pk_bf16_f32 v216, v216, v220\n\t"
"v_mfma_f32_16x16x32_bf16 v[4:7], a[48:51], a[148:151], v[4:7]\n\t"
"s_nop 0\n\t"
"ds_read_b128 a[148:151], v224 offset:17824\n\t"
"v_cvt_pk_bf16_f32 v217, v217, v221\n\t"
"v_mfma_f32_16x16x32_bf16 v[4:7], a[52:55], a[164:167], v[4:7]\n\t"
"s_waitcnt lgkmcnt(15)\n\t"
"ds_read_b64_tr_b16 a[182:183], v249 offset:41984\n\t"
"buffer_load_dwordx4 a[116:119], off, s[96:99], s54 offset:1024 sc1\n\t"
"v_add_u32 v158, 0, v176\n\t"
"v_cvt_pk_bf16_f32 v209, v209, v213\n\t"
"v_mfma_f32_16x16x32_bf16 v[8:11], a[48:51], a[152:155], v[8:11]\n\t"
"s_nop 0\n\t"
"ds_read_b128 a[164:167], v224 offset:19872\n\t"
"v_mul_u32_u24 v159, 0xab, v158\n\t"
"v_mfma_f32_16x16x32_bf16 v[8:11], a[52:55], a[168:171], v[8:11]\n\t"
"s_nop 0\n\t"
"ds_read_b64_tr_b16 a[184:185], v250 offset:41472\n\t"
"s_mov_b32 s66, 172032\n\t"
"buffer_load_dwordx4 a[120:123], off, s[92:95], s66 offset:0 sc1\n\t"
"v_lshrrev_b32 v159, 12, v159\n\t"
"v_add_lshl_u32 v166, v158, v159, 4\n\t"
"v_mfma_f32_16x16x32_bf16 v[24:27], a[56:59], a[152:155], v[24:27]\n\t"
"s_nop 0\n\t"
"ds_read_b64_tr_b16 a[186:187], v250 offset:41984\n\t"
"v_cvt_pk_bf16_f32 v211, v211, v215\n\t"
"v_mfma_f32_16x16x32_bf16 v[24:27], a[60:63], a[168:171], v[24:27]\n\t"
"s_nop 0\n\t"
"ds_read_b64_tr_b16 a[188:189], v251 offset:41472\n\t"
"v_cvt_pk_bf16_f32 v192, v192, v196\n\t"
"v_mfma_f32_16x16x32_bf16 v[40:43], a[64:67], a[152:155], v[40:43]\n\t"
"s_nop 0\n\t"
"ds_read_b128 a[152:155], v224 offset:18080\n\t"
"v_add_u32 v229, s60, v166\n\t"
"v_mfma_f32_16x16x32_bf16 v[40:43], a[68:71], a[168:171], v[40:43]\n\t"
"s_waitcnt lgkmcnt(15)\n\t"
"ds_read_b128 a[168:171], v224 offset:20128\n\t"
"buffer_load_dwordx4 a[124:127], off, s[92:95], s66 offset:1024 sc1\n\t"
"v_mfma_f32_16x16x32_bf16 v[44:47], a[64:67], a[156:159], v[44:47]\n\t"
"s_nop 0\n\t"
"ds_read_b64_tr_b16 a[190:191], v251 offset:41984\n\t"
"v_mad_u32_u24 v158, v159, 24, v158\n\t"
"v_mfma_f32_16x16x32_bf16 v[44:47], a[68:71], a[172:175], v[44:47]\n\t"
"s_nop 0\n\t"
"ds_write_addtid_b32 v216 offset:3072\n\t"
"buffer_load_dwordx4 a[128:131], off, s[92:95], s66 offset:2048 sc1\n\t"
"v_lshl_add_u32 v232, v158, 4, s61\n\t"
"v_add_u32 v158, 64, v176\n\t"
"v_mfma_f32_16x16x32_bf16 v[28:31], a[56:59], a[156:159], v[28:31]\n\t"
"s_nop 0\n\t"
"ds_read_b64_tr_b16 a[192:193], v248 offset:42624\n\t"
"v_mul_u32_u24 v159, 0xab, v158\n\t"
"v_mfma_f32_16x16x32_bf16 v[28:31], a[60:63], a[172:175], v[28:31]\n\t"
"s_nop 0\n\t"
"ds_write_addtid_b32 v192 offset:0\n\t"
"v_lshrrev_b32 v159, 12, v159\n\t"
"v_mfma_f32_16x16x32_bf16 v[12:15], a[48:51], a[156:159], v[12:15]\n\t"
"s_nop 0\n\t"
"ds_read_b128 a[156:159], v224 offset:18336\n\t"
"v_cvt_pk_bf16_f32 v218, v218, v222\n\t"
"v_mfma_f32_16x16x32_bf16 v[12:15], a[52:55], a[172:175], v[12:15]\n\t"
"s_waitcnt lgkmcnt(15)\n\t"
"ds_read_b128 a[172:175], v224 offset:20384\n\t"
"buffer_load_dwordx4 a[132:135], off, s[92:95], s66 offset:3072 sc1\n\t"
"v_cvt_pk_bf16_f32 v208, v208, v212\n\t"
"v_mfma_f32_16x16x32_bf16 v[96:99], a[72:75], a[144:147], v[96:99]\n\t"
"s_nop 0\n\t"
"ds_write_addtid_b32 v211 offset:13472\n\t"
"v_add_lshl_u32 v166, v158, v159, 4\n\t"
"v_mfma_f32_16x16x32_bf16 v[96:99], a[76:79], a[160:163], v[96:99]\n\t"
"s_nop 0\n\t"
"ds_write_addtid_b32 v202 offset:6176\n\t"
"ds_write_addtid_b32 v209 offset:9344\n\t"
"ds_write_addtid_b32 v217 offset:11392\n\t"
"ds_write_addtid_b32 v218 offset:7200\n\t"
"v_add_u32 v230, s60, v166\n\t"
"v_mfma_f32_16x16x32_bf16 v[112:115], a[80:83], a[144:147], v[112:115]\n\t"
"s_nop 0\n\t"
"ds_write_addtid_b32 v208 offset:1024\n\t"
"v_mad_u32_u24 v158, v159, 24, v158\n\t"
"v_mfma_f32_16x16x32_bf16 v[112:115], a[84:87], a[160:163], v[112:115]\n\t"
"s_nop 0\n\t"
"ds_read_b64_tr_b16 a[194:195], v248 offset:43136\n\t"
"buffer_load_dwordx4 a[136:139], off, s[96:99], s66 offset:0 sc1\n\t"
"v_mfma_f32_16x16x32_bf16 v[128:131], a[88:91], a[144:147], v[128:131]\n\t"
"s_nop 0\n\t"
"ds_read_b64_tr_b16 a[196:197], v249 offset:42624\n\t"
"v_lshl_add_u32 v233, v158, 4, s61\n\t"
"v_mfma_f32_16x16x32_bf16 v[128:131], a[92:95], a[160:163], v[128:131]\n\t"
"s_waitcnt lgkmcnt(15)\n\t"
"ds_read_b64_tr_b16 a[198:199], v249 offset:43136\n\t"
"buffer_load_dwordx4 a[140:143], off, s[96:99], s66 offset:1024 sc1\n\t"
"v_add_u32 v158, 128, v176\n\t"
"v_mfma_f32_16x16x32_bf16 v[132:135], a[88:91], a[148:151], v[132:135]\n\t"
"s_nop 0\n\t"
"ds_read_b64_tr_b16 a[200:201], v250 offset:42624\n\t"
"v_mul_u32_u24 v159, 0xab, v158\n\t"
"v_mfma_f32_16x16x32_bf16 v[132:135], a[92:95], a[164:167], v[132:135]\n\t"
"s_nop 0\n\t"
"ds_read_b64_tr_b16 a[202:203], v250 offset:43136\n\t"
"v_lshrrev_b32 v159, 12, v159\n\t"
"v_add_lshl_u32 v166, v158, v159, 4\n\t"
"v_add_u32 v231, s60, v166\n\t"
"v_mfma_f32_16x16x32_bf16 v[116:119], a[80:83], a[148:151], v[116:119]\n\t"
"s_nop 0\n\t"
"ds_read_b64_tr_b16 a[204:205], v251 offset:42624\n\t"
"ds_read_b64_tr_b16 a[206:207], v251 offset:43136\n\t"
"v_mfma_f32_16x16x32_bf16 v[116:119], a[84:87], a[164:167], v[116:119]\n\t"
"s_mov_b32 s69, 196608\n\t"
"buffer_load_dwordx4 a[0:3], off, s[92:95], s69 offset:0 sc1\n\t"
"v_mfma_f32_16x16x32_bf16 v[100:103], a[72:75], a[148:151], v[100:103]\n\t"
"s_nop 0\n\t"
"v_mad_u32_u24 v158, v159, 24, v158\n\t"
"v_lshl_add_u32 v234, v158, 4, s61\n\t"
"v_mfma_f32_16x16x32_bf16 v[100:103], a[76:79], a[164:167], v[100:103]\n\t"
"buffer_load_dwordx4 a[4:7], off, s[92:95], s69 offset:1024 sc1\n\t"
"s_waitcnt lgkmcnt(7)\n\t"
"s_add_u32 s71, s71, 1\n\t"
"v_mov_b32 v215, s71\n\t"
"s_mov_b64 exec, 1\n\t"
"buffer_store_dword v215, off, s[8:11], s70 sc1\n\t"
"s_mov_b64 exec, -1\n\t"
"s_barrier\n\t"
"v_mfma_f32_16x16x32_bf16 v[104:107], a[72:75], a[152:155], v[104:107]\n\t"
"s_nop 0\n\t"
"ds_read_b128 a[160:163], v224 offset:2048\n\t"
"v_add_u32 v248, 122368, v226\n\t"
"v_mfma_f32_16x16x32_bf16 v[104:107], a[76:79], a[168:171], v[104:107]\n\t"
"s_nop 0\n\t"
"ds_read_b128 a[144:147], v224 offset:0\n\t"
"v_xor_b32 v249, 32, v248\n\t"
"v_mfma_f32_16x16x32_bf16 v[120:123], a[80:83], a[152:155], v[120:123]\n\t"
"s_nop 0\n\t"
"ds_read_b128 a[148:151], v224 offset:256\n\t"
"v_accvgpr_read_b32 v172, a220\n\t"
"v_mfma_f32_16x16x32_bf16 v[120:123], a[84:87], a[168:171], v[120:123]\n\t"
"s_nop 0\n\t"
"ds_read_b128 a[164:167], v224 offset:2304\n\t"
"v_and_b32 v172, 0xff7fff7f, v172\n\t"
"v_mfma_f32_16x16x32_bf16 v[136:139], a[88:91], a[152:155], v[136:139]\n\t"
"s_nop 0\n\t"
"v_accvgpr_write_b32 a252, v172\n\t"
"v_xor_b32 v250, 64, v248\n\t"
"v_mfma_f32_16x16x32_bf16 v[136:139], a[92:95], a[168:171], v[136:139]\n\t"
"buffer_load_dwordx4 a[8:11], off, s[92:95], s69 offset:2048 sc1\n\t"
"v_mfma_f32_16x16x32_bf16 v[140:143], a[88:91], a[156:159], v[140:143]\n\t"
"s_nop 0\n\t"
"ds_read_b128 a[168:171], v224 offset:2560\n\t"
"v_xor_b32 v251, 96, v248\n\t"
"v_mfma_f32_16x16x32_bf16 v[140:143], a[92:95], a[172:175], v[140:143]\n\t"
"buffer_load_dwordx4 a[12:15], off, s[92:95], s69 offset:3072 sc1\n\t"
"v_mfma_f32_16x16x32_bf16 v[124:127], a[80:83], a[156:159], v[124:127]\n\t"
"s_nop 0\n\t"
"ds_read_b128 a[152:155], v224 offset:512\n\t"
"v_accvgpr_read_b32 v173, a221\n\t"
"v_mfma_f32_16x16x32_bf16 v[124:127], a[84:87], a[172:175], v[124:127]\n\t"
"buffer_load_dwordx4 a[16:19], off, s[96:99], s69 offset:0 sc1\n\t"
"v_mfma_f32_16x16x32_bf16 v[108:111], a[72:75], a[156:159], v[108:111]\n\t"
"s_nop 0\n\t"
"ds_read_b128 a[156:159], v224 offset:768\n\t"
"v_and_b32 v173, 0xff7fff7f, v173\n\t"
"v_mfma_f32_16x16x32_bf16 v[108:111], a[76:79], a[172:175], v[108:111]\n\t"
"s_mov_b32 s41, 16384\n\t"
"v_accvgpr_write_b32 a253, v173\n\t"
"v_accvgpr_read_b32 v174, a222\n\t"
"v_mfma_f32_16x16x32_bf16 v[192:195], a[216:219], a[176:179], 0\n\t"
"s_nop 0\n\t"
"ds_read_b128 a[172:175], v224 offset:2816\n\t"
"v_and_b32 v174, 0xff7fff7f, v174\n\t"
"v_mfma_f32_16x16x32_bf16 v[196:199], a[216:219], a[180:183], 0\n\t"
"buffer_load_dwordx4 a[20:23], off, s[96:99], s69 offset:1024 sc1\n\t"
"v_mfma_f32_16x16x32_bf16 v[200:203], a[216:219], a[184:187], 0\n\t"
"s_nop 0\n\t"
"ds_read_b64_tr_b16 a[178:179], v248 offset:128\n\t"
"v_accvgpr_write_b32 a254, v174\n\t"
"v_mfma_f32_16x16x32_bf16 v[204:207], a[216:219], a[188:191], 0\n\t"
"s_waitcnt lgkmcnt(15)\n\t"
"s_mov_b32 s81, 270336\n\t"
"buffer_load_dwordx4 a[24:27], off, s[92:95], s81 offset:0 sc1\n\t"
"v_mfma_f32_16x16x32_bf16 v[208:211], a[216:219], a[192:195], 0\n\t"
"s_waitcnt lgkmcnt(13)\n\t"
"ds_read_b64_tr_b16 a[176:177], v248 offset:0\n\t"
"v_accvgpr_read_b32 v175, a223\n\t"
"v_mfma_f32_16x16x32_bf16 v[212:215], a[216:219], a[196:199], 0\n\t"
"s_waitcnt lgkmcnt(12)\n\t"
"ds_read_b64_tr_b16 a[180:181], v249 offset:256\n\t"
"v_and_b32 v175, 0xff7fff7f, v175\n\t"
"v_mfma_f32_16x16x32_bf16 v[216:219], a[216:219], a[200:203], 0\n\t"
"s_waitcnt lgkmcnt(11)\n\t"
"ds_read_b64_tr_b16 a[186:187], v249 offset:128\n\t"
"v_accvgpr_write_b32 a255, v175\n\t"
"v_mfma_f32_16x16x32_bf16 v[220:223], a[216:219], a[204:207], 0\n\t"
"s_waitcnt vmcnt(18) lgkmcnt(10)\n\t"
"buffer_load_dwordx4 a[28:31], off, s[92:95], s81 offset:1024 sc1\n\t"
"v_mfma_f32_16x16x32_bf16 v[0:3], a[96:99], a[144:147], v[0:3]\n\t"
"s_mov_b32 s90, 0.5\n\t"
"ds_read_b64_tr_b16 a[184:185], v249 offset:0\n\t"
"v_cvt_pk_bf16_f32 v194, v194, v198\n\t"
"v_mfma_f32_16x16x32_bf16 v[0:3], a[100:103], a[160:163], v[0:3]\n\t"
"s_waitcnt vmcnt(17)\n\t"
"ds_read_b64_tr_b16 a[188:189], v248 offset:256\n\t"
"v_cvt_pk_bf16_f32 v202, v202, v206\n\t"
"s_mov_b32 s91, 0.5\n\t"
"v_mfma_f32_16x16x32_bf16 v[16:19], a[104:107], a[144:147], v[16:19]\n\t"
"s_nop 0\n\t"
"ds_read_b64_tr_b16 a[194:195], v250 offset:128\n\t"
"v_cvt_pk_bf16_f32 v200, v200, v204\n\t"
"v_mfma_f32_16x16x32_bf16 v[16:19], a[108:111], a[160:163], v[16:19]\n\t"
"s_waitcnt vmcnt(15)\n\t"
"buffer_load_dwordx4 a[32:35], off, s[92:95], s81 offset:2048 sc1\n\t"
"v_mfma_f32_16x16x32_bf16 v[32:35], a[112:115], a[144:147], v[32:35]\n\t"
"s_mov_b32 s46, 32768\n\t"
"ds_read_b128 a[144:147], v224 offset:1024\n\t"
"v_cvt_pk_bf16_f32 v192, v192, v196\n\t"
"v_mfma_f32_16x16x32_bf16 v[32:35], a[116:119], a[160:163], v[32:35]\n\t"
"s_waitcnt lgkmcnt(12)\n\t"
"buffer_load_dwordx4 a[36:39], off, s[92:95], s81 offset:3072 sc1\n\t"
"v_cvt_pk_bf16_f32 v210, v210, v214\n\t"
"v_cvt_pk_bf16_f32 v211, v211, v215\n\t"
"v_mfma_f32_16x16x32_bf16 v[36:39], a[112:115], a[148:151], v[36:39]\n\t"
"s_add_u32 s84, s92, 18432\n\t"
"s_addc_u32 s85, s93, 0\n\t"
"ds_read_b128 a[160:163], v224 offset:3072\n\t"
"v_mfma_f32_16x16x32_bf16 v[36:39], a[116:119], a[164:167], v[36:39]\n\t"
"s_mov_b64 s[86:87], s[94:95]\n\t"
"v_cvt_pk_bf16_f32 v209, v209, v213\n\t"
"v_cvt_pk_bf16_f32 v216, v216, v220\n\t"
"v_mfma_f32_16x16x32_bf16 v[20:23], a[104:107], a[148:151], v[20:23]\n\t"
"s_xor_b64 s[88:89], s[84:85], s[92:93]\n\t"
"ds_write_addtid_b32 v192 offset:16544\n\t"
"ds_write_addtid_b32 v210 offset:21696\n\t"
"v_mfma_f32_16x16x32_bf16 v[20:23], a[108:111], a[164:167], v[20:23]\n\t"
"s_nop 0\n\t"
"ds_write_addtid_b32 v194 offset:20672\n\t"
"buffer_load_dwordx4 a[40:43], off, s[96:99], s81 offset:0 sc1\n\t"
"v_mfma_f32_16x16x32_bf16 v[4:7], a[96:99], a[148:151], v[4:7]\n\t"
"s_nop 0\n\t"
"ds_read_b128 a[148:151], v224 offset:1280\n\t"
"v_cvt_pk_bf16_f32 v195, v195, v199\n\t"
"v_mfma_f32_16x16x32_bf16 v[4:7], a[100:103], a[164:167], v[4:7]\n\t"
"s_waitcnt lgkmcnt(15)\n\t"
"buffer_load_dwordx4 a[44:47], off, s[96:99], s81 offset:1024 sc1\n\t"
"v_cvt_pk_bf16_f32 v201, v201, v205\n\t"
"v_mfma_f32_16x16x32_bf16 v[8:11], a[96:99], a[152:155], v[8:11]\n\t"
"s_mov_b32 s40, 8192\n\t"
"ds_read_b128 a[164:167], v224 offset:3328\n\t"
"v_cvt_pk_bf16_f32 v217, v217, v221\n\t"
"v_mfma_f32_16x16x32_bf16 v[8:11], a[100:103], a[168:171], v[8:11]\n\t"
"s_add_u32 s28, s84, 4096\n\t"
"s_addc_u32 s29, s85, 0\n\t"
"ds_write_addtid_b32 v217 offset:27936\n\t"
"v_mfma_f32_16x16x32_bf16 v[24:27], a[104:107], a[152:155], v[24:27]\n\t"
"s_mov_b64 s[30:31], s[94:95]\n\t"
"ds_write_addtid_b32 v201 offset:26912\n\t"
"ds_write_addtid_b32 v209 offset:25888\n\t"
"v_mfma_f32_16x16x32_bf16 v[24:27], a[108:111], a[168:171], v[24:27]\n\t"
"s_mov_b32 s57, 122880\n\t"
"buffer_load_dwordx4 a[48:51], off, s[92:95], s57 offset:0 sc1\n\t"
"v_mfma_f32_16x16x32_bf16 v[40:43], a[112:115], a[152:155], v[40:43]\n\t"
"s_nop 0\n\t"
"ds_read_b128 a[152:155], v224 offset:1536\n\t"
"v_cvt_pk_bf16_f32 v218, v218, v222\n\t"
"v_mfma_f32_16x16x32_bf16 v[40:43], a[116:119], a[168:171], v[40:43]\n\t"
"s_waitcnt lgkmcnt(15)\n\t"
"ds_read_b128 a[168:171], v224 offset:3584\n\t"
"v_cvt_pk_bf16_f32 v203, v203, v207\n\t"
"s_add_u32 s25, s24, 0xffeb0000\n\t"
"v_mfma_f32_16x16x32_bf16 v[44:47], a[112:115], a[156:159], v[44:47]\n\t"
"s_nop 0\n\t"
"ds_write_addtid_b32 v216 offset:19616\n\t"
"v_cvt_pk_bf16_f32 v208, v208, v212\n\t"
"v_mfma_f32_16x16x32_bf16 v[44:47], a[116:119], a[172:175], v[44:47]\n\t"
"buffer_load_dwordx4 a[52:55], off, s[92:95], s57 offset:1024 sc1\n\t"
"v_mfma_f32_16x16x32_bf16 v[28:31], a[104:107], a[156:159], v[28:31]\n\t"
"s_mov_b32 s42, 24576\n\t"
"ds_write_addtid_b32 v200 offset:18592\n\t"
"ds_write_addtid_b32 v211 offset:30016\n\t"
"v_mfma_f32_16x16x32_bf16 v[28:31], a[108:111], a[172:175], v[28:31]\n\t"
"s_add_u32 s27, s24, 0xffe80000\n\t"
"ds_write_addtid_b32 v202 offset:22720\n\t"
"ds_write_addtid_b32 v208 offset:17568\n\t"
"v_mfma_f32_16x16x32_bf16 v[12:15], a[96:99], a[156:159], v[12:15]\n\t"
"s_nop 0\n\t"
"ds_read_b128 a[156:159], v224 offset:1792\n\t"
"v_cvt_pk_bf16_f32 v193, v193, v197\n\t"
"v_mfma_f32_16x16x32_bf16 v[12:15], a[100:103], a[172:175], v[12:15]\n\t"
"s_waitcnt vmcnt(19) lgkmcnt(15)\n\t"
"buffer_load_dwordx4 a[56:59], off, s[92:95], s57 offset:2048 sc1\n\t"
"v_mfma_f32_16x16x32_bf16 v[96:99], a[120:123], a[144:147], v[96:99]\n\t"
"s_mov_b32 s47, 40960\n\t"
"ds_write_addtid_b32 v193 offset:24864\n\t"
"ds_write_addtid_b32 v195 offset:28992\n\t"
"v_mfma_f32_16x16x32_bf16 v[96:99], a[124:127], a[160:163], v[96:99]\n\t"
"s_waitcnt vmcnt(18)\n\t"
"ds_write_addtid_b32 v203 offset:31040\n\t"
"Lpad1_%=:\n\t"
"s_cbranch_execz Lpad5_%=\n\t"
"v_cvt_pk_bf16_f32 v219, v219, v223\n\t"
"s_mov_b32 s49, 57344\n\t"
"v_mfma_f32_16x16x32_bf16 v[112:115], a[128:131], a[144:147], v[112:115]\n\t"
"s_add_u32 s20, s24, 0xffd30000\n\t"
"ds_write_addtid_b32 v218 offset:23744\n\t"
"ds_write_addtid_b32 v219 offset:32064\n\t"
"v_mfma_f32_16x16x32_bf16 v[112:115], a[132:135], a[160:163], v[112:115]\n\t"
"s_waitcnt vmcnt(16)\n\t"
"buffer_load_dwordx4 a[60:63], off, s[92:95], s57 offset:3072 sc1\n\t"
"v_mfma_f32_16x16x32_bf16 v[128:131], a[136:139], a[144:147], v[128:131]\n\t"
"s_mov_b32 s48, 49152\n\t"
"ds_read_b128 a[172:175], v224 offset:3840\n\t"
"ds_read_b64_tr_b16 a[192:193], v250 offset:0\n\t"
"v_mfma_f32_16x16x32_bf16 v[128:131], a[140:143], a[160:163], v[128:131]\n\t"
"s_waitcnt lgkmcnt(15)\n\t"
"buffer_load_dwordx4 a[64:67], off, s[96:99], s57 offset:0 sc1\n\t"
"v_mfma_f32_16x16x32_bf16 v[132:135], a[136:139], a[148:151], v[132:135]\n\t"
"s_add_u32 s24, s24, 0xffd00000\n\t"
"ds_read_b64_tr_b16 a[196:197], v251 offset:256\n\t"
"ds_read_b64_tr_b16 a[202:203], v251 offset:128\n\t"
"v_mfma_f32_16x16x32_bf16 v[132:135], a[140:143], a[164:167], v[132:135]\n\t"
"buffer_load_dwordx4 a[68:71], off, s[96:99], s57 offset:1024 sc1\n\t"
"v_mfma_f32_16x16x32_bf16 v[116:119], a[128:131], a[148:151], v[116:119]\n\t"
"s_mov_b32 s50, 65536\n\t"
"ds_read_b64_tr_b16 a[200:201], v251 offset:0\n\t"
"ds_read_b64_tr_b16 a[204:205], v250 offset:256\n\t"
"v_mfma_f32_16x16x32_bf16 v[116:119], a[132:135], a[164:167], v[116:119]\n\t"
"v_mfma_f32_16x16x32_bf16 v[100:103], a[120:123], a[148:151], v[100:103]\n\t"
"s_mov_b32 s63, 147456\n\t"
"buffer_load_dwordx4 a[72:75], off, s[92:95], s63 offset:0 sc1\n\t"
"v_mfma_f32_16x16x32_bf16 v[100:103], a[124:127], a[164:167], v[100:103]\n\t"
"s_waitcnt lgkmcnt(5)\n\t"
"s_barrier\n\t"
"v_mfma_f32_16x16x32_bf16 v[104:107], a[120:123], a[152:155], v[104:107]\n\t"
"s_nop 0\n\t"
"ds_read_b128 a[144:147], v224 offset:16544\n\t"
"ds_read_b128 a[148:151], v224 offset:16800\n\t"
"v_mfma_f32_16x16x32_bf16 v[104:107], a[124:127], a[168:171], v[104:107]\n\t"
"buffer_load_dwordx4 a[76:79], off, s[92:95], s63 offset:1024 sc1\n\t"
"v_mfma_f32_16x16x32_bf16 v[120:123], a[128:131], a[152:155], v[120:123]\n\t"
"ds_read_b128 a[160:163], v224 offset:18592\n\t"
"v_mfma_f32_16x16x32_bf16 v[120:123], a[132:135], a[168:171], v[120:123]\n\t"
"ds_read_b128 a[164:167], v224 offset:18848\n\t"
"v_mfma_f32_16x16x32_bf16 v[136:139], a[136:139], a[152:155], v[136:139]\n\t"
"v_mfma_f32_16x16x32_bf16 v[136:139], a[140:143], a[168:171], v[136:139]\n\t"
"buffer_load_dwordx4 a[80:83], off, s[92:95], s63 offset:2048 sc1\n\t"
"v_mfma_f32_16x16x32_bf16 v[140:143], a[136:139], a[156:159], v[140:143]\n\t"
"ds_read_b128 a[168:171], v224 offset:19104\n\t"
"v_mfma_f32_16x16x32_bf16 v[140:143], a[140:143], a[172:175], v[140:143]\n\t"
"ds_read_b128 a[152:155], v224 offset:17056\n\t"
"v_mfma_f32_16x16x32_bf16 v[124:127], a[128:131], a[156:159], v[124:127]\n\t"
"v_mfma_f32_16x16x32_bf16 v[124:127], a[132:135], a[172:175], v[124:127]\n\t"
"buffer_load_dwordx4 a[84:87], off, s[92:95], s63 offset:3072 sc1\n\t"
"v_mfma_f32_16x16x32_bf16 v[108:111], a[120:123], a[156:159], v[108:111]\n\t"
"ds_read_b128 a[156:159], v224 offset:17312\n\t"
"v_mfma_f32_16x16x32_bf16 v[108:111], a[124:127], a[172:175], v[108:111]\n\t"
"s_waitcnt lgkmcnt(7)\n\t"
"ds_read_b128 a[172:175], v224 offset:19360\n\t"
"v_mfma_f32_4x4x4_16b_bf16 v[200:203], a[252:253], a[178:179], 0\n\t"
"ds_read_b64_tr_b16 a[178:179], v248 offset:20864\n\t"
"v_mfma_f32_4x4x4_16b_bf16 v[204:207], a[252:253], a[186:187], 0\n\t"
"buffer_load_dwordx4 a[88:91], off, s[96:99], s63 offset:0 sc1\n\t"
"v_mfma_f32_4x4x4_16b_bf16 v[216:219], a[252:253], a[194:195], 0\n\t"
"ds_read_b64_tr_b16 a[186:187], v249 offset:20864\n\t"
"v_mfma_f32_4x4x4_16b_bf16 v[220:223], a[252:253], a[202:203], 0\n\t"
"ds_read_b64_tr_b16 a[194:195], v250 offset:20864\n\t"
"v_mfma_f32_4x4x4_16b_bf16 v[192:195], a[254:255], a[176:177], v[200:203]\n\t"
"ds_read_b64_tr_b16 a[202:203], v251 offset:20864\n\t"
"v_mfma_f32_4x4x4_16b_bf16 v[196:199], a[254:255], a[184:185], v[204:207]\n\t"
"buffer_load_dwordx4 a[92:95], off, s[96:99], s63 offset:1024 sc1\n\t"
"v_mfma_f32_4x4x4_16b_bf16 v[200:203], a[254:255], a[180:181], v[200:203]\n\t"
"ds_read_b64_tr_b16 a[176:177], v248 offset:20736\n\t"
"v_mfma_f32_4x4x4_16b_bf16 v[204:207], a[254:255], a[188:189], v[204:207]\n\t"
"ds_read_b64_tr_b16 a[184:185], v249 offset:20736\n\t"
"v_mfma_f32_4x4x4_16b_bf16 v[208:211], a[254:255], a[192:193], v[216:219]\n\t"
"v_mfma_f32_4x4x4_16b_bf16 v[212:215], a[254:255], a[200:201], v[220:223]\n\t"
"ds_read_b64_tr_b16 a[180:181], v249 offset:20992\n\t"
"v_mfma_f32_4x4x4_16b_bf16 v[216:219], a[254:255], a[196:197], v[216:219]\n\t"
"ds_read_b64_tr_b16 a[188:189], v248 offset:20992\n\t"
"v_mfma_f32_4x4x4_16b_bf16 v[220:223], a[254:255], a[204:205], v[220:223]\n\t"
"s_waitcnt vmcnt(23) lgkmcnt(13)\n\t"
"s_mov_b32 s75, 221184\n\t"
"buffer_load_dwordx4 a[96:99], off, s[92:95], s75 offset:0 sc1\n\t"
"v_mfma_f32_16x16x32_bf16 v[0:3], a[0:3], a[144:147], v[0:3]\n\t"
"ds_read_b64_tr_b16 a[192:193], v250 offset:20736\n\t"
"v_cvt_pk_bf16_f32 v192, -v192, -v196\n\t"
"v_mfma_f32_16x16x32_bf16 v[0:3], a[4:7], a[160:163], v[0:3]\n\t"
"s_waitcnt vmcnt(21)\n\t"
"buffer_load_dwordx4 a[100:103], off, s[92:95], s75 offset:1024 sc1\n\t"
"v_mfma_f32_16x16x32_bf16 v[16:19], a[8:11], a[144:147], v[16:19]\n\t"
"ds_read_b64_tr_b16 a[200:201], v251 offset:20736\n\t"
"v_cvt_pk_bf16_f32 v200, v200, v204\n\t"
"v_mfma_f32_16x16x32_bf16 v[16:19], a[12:15], a[160:163], v[16:19]\n\t"
"s_waitcnt vmcnt(20)\n\t"
"buffer_load_dwordx4 a[104:107], off, s[92:95], s75 offset:2048 sc1\n\t"
"v_mfma_f32_16x16x32_bf16 v[32:35], a[16:19], a[144:147], v[32:35]\n\t"
"ds_read_b128 a[144:147], v224 offset:17568\n\t"
"v_cvt_pk_bf16_f32 v202, v202, v206\n\t"
"v_mfma_f32_16x16x32_bf16 v[32:35], a[20:23], a[160:163], v[32:35]\n\t"
"s_waitcnt lgkmcnt(15)\n\t"
"buffer_load_dwordx4 a[108:111], off, s[92:95], s75 offset:3072 sc1\n\t"
"v_mfma_f32_16x16x32_bf16 v[36:39], a[16:19], a[148:151], v[36:39]\n\t"
"s_mov_b32 s53, 90112\n\t"
"ds_read_b128 a[160:163], v224 offset:19616\n\t"
"v_cvt_pk_bf16_f32 v193, -v193, -v197\n\t"
"v_mfma_f32_16x16x32_bf16 v[36:39], a[20:23], a[164:167], v[36:39]\n\t"
"ds_write_addtid_b32 v202 offset:9344\n\t"
"v_cvt_pk_bf16_f32 v219, v219, v223\n\t"
"v_mfma_f32_16x16x32_bf16 v[20:23], a[8:11], a[148:151], v[20:23]\n\t"
"ds_write_addtid_b32 v193 offset:4128\n\t"
"v_cvt_pk_bf16_f32 v208, -v208, -v212\n\t"
"v_mfma_f32_16x16x32_bf16 v[20:23], a[12:15], a[164:167], v[20:23]\n\t"
"buffer_load_dwordx4 a[112:115], off, s[96:99], s75 offset:0 sc1\n\t"
"v_mfma_f32_16x16x32_bf16 v[4:7], a[0:3], a[148:151], v[4:7]\n\t"
"ds_read_b128 a[148:151], v224 offset:17824\n\t"
"v_cvt_pk_bf16_f32 v203, v203, v207\n\t"
"v_mfma_f32_16x16x32_bf16 v[4:7], a[4:7], a[164:167], v[4:7]\n\t"
"s_waitcnt lgkmcnt(15)\n\t"
"ds_write_addtid_b32 v200 offset:8320\n\t"
"v_cvt_pk_bf16_f32 v201, v201, v205\n\t"
"v_mfma_f32_16x16x32_bf16 v[8:11], a[0:3], a[152:155], v[8:11]\n\t"
"s_mov_b32 s52, 81920\n\t"
"ds_read_b128 a[164:167], v224 offset:19872\n\t"
"v_cvt_pk_bf16_f32 v211, -v211, -v215\n\t"
"v_mfma_f32_16x16x32_bf16 v[8:11], a[4:7], a[168:171], v[8:11]\n\t"
"buffer_load_dwordx4 a[116:119], off, s[96:99], s75 offset:1024 sc1\n\t"
"v_mfma_f32_16x16x32_bf16 v[24:27], a[8:11], a[152:155], v[24:27]\n\t"
"ds_write_addtid_b32 v201 offset:12448\n\t"
"v_cvt_pk_bf16_f32 v216, v216, v220\n\t"
"v_mfma_f32_16x16x32_bf16 v[24:27], a[12:15], a[168:171], v[24:27]\n\t"
"ds_write_addtid_b32 v216 offset:10368\n\t"
"v_cvt_pk_bf16_f32 v217, v217, v221\n\t"
"v_mfma_f32_16x16x32_bf16 v[40:43], a[16:19], a[152:155], v[40:43]\n\t"
"ds_read_b128 a[152:155], v224 offset:18080\n\t"
"v_cvt_pk_bf16_f32 v218, v218, v222\n\t"
"v_mfma_f32_16x16x32_bf16 v[40:43], a[20:23], a[168:171], v[40:43]\n\t"
"s_mov_b32 s78, 245760\n\t"
"buffer_load_dwordx4 a[120:123], off, s[92:95], s78 offset:0 sc1\n\t"
"v_mfma_f32_16x16x32_bf16 v[44:47], a[16:19], a[156:159], v[44:47]\n\t"
"ds_read_b128 a[168:171], v224 offset:20128\n\t"
"v_cvt_pk_bf16_f32 v210, -v210, -v214\n\t"
"v_mfma_f32_16x16x32_bf16 v[44:47], a[20:23], a[172:175], v[44:47]\n\t"
"ds_write_addtid_b32 v219 offset:15520\n\t"
"v_cvt_pk_bf16_f32 v195, -v195, -v199\n\t"
"v_mfma_f32_16x16x32_bf16 v[28:31], a[8:11], a[156:159], v[28:31]\n\t"
"s_mov_b32 s55, 106496\n\t"
"ds_write_addtid_b32 v192 offset:0\n\t"
"ds_write_addtid_b32 v218 offset:11392\n\t"
"v_mfma_f32_16x16x32_bf16 v[28:31], a[12:15], a[172:175], v[28:31]\n\t"
"buffer_load_dwordx4 a[124:127], off, s[92:95], s78 offset:1024 sc1\n\t"
"v_mfma_f32_16x16x32_bf16 v[12:15], a[0:3], a[156:159], v[12:15]\n\t"
"s_mov_b32 s51, 73728\n\t"
"ds_write_addtid_b32 v208 offset:2048\n\t"
"ds_write_addtid_b32 v211 offset:7200\n\t"
"v_mfma_f32_16x16x32_bf16 v[12:15], a[4:7], a[172:175], v[12:15]\n\t"
"s_waitcnt vmcnt(24) lgkmcnt(14)\n\t"
"ds_write_addtid_b32 v195 offset:5152\n\t"
"v_cvt_pk_bf16_f32 v194, -v194, -v198\n\t"
"v_mfma_f32_16x16x32_bf16 v[96:99], a[24:27], a[144:147], v[96:99]\n\t"
"s_mov_b32 s58, 131072\n\t"
"ds_write_addtid_b32 v194 offset:1024\n\t"
"ds_write_addtid_b32 v217 offset:14496\n\t"
"v_mfma_f32_16x16x32_bf16 v[96:99], a[28:31], a[160:163], v[96:99]\n\t"
"s_waitcnt vmcnt(22)\n\t"
"ds_write_addtid_b32 v210 offset:3072\n\t"
"v_cvt_pk_bf16_f32 v209, -v209, -v213\n\t"
"v_mfma_f32_16x16x32_bf16 v[112:115], a[32:35], a[144:147], v[112:115]\n\t"
"s_mov_b32 s59, 139264\n\t"
"ds_write_addtid_b32 v203 offset:13472\n\t"
"ds_write_addtid_b32 v209 offset:6176\n\t"
"v_mfma_f32_16x16x32_bf16 v[112:115], a[36:39], a[160:163], v[112:115]\n\t"
"s_waitcnt vmcnt(20)\n\t"
"buffer_load_dwordx4 a[128:131], off, s[92:95], s78 offset:2048 sc1\n\t"
"v_mfma_f32_16x16x32_bf16 v[128:131], a[40:43], a[144:147], v[128:131]\n\t"
"ds_read_b128 a[172:175], v224 offset:20384\n\t"
"v_mfma_f32_16x16x32_bf16 v[128:131], a[44:47], a[160:163], v[128:131]\n\t"
"s_waitcnt lgkmcnt(15)\n\t"
"buffer_load_dwordx4 a[132:135], off, s[92:95], s78 offset:3072 sc1\n\t"
"v_mfma_f32_16x16x32_bf16 v[132:135], a[40:43], a[148:151], v[132:135]\n\t"
"ds_read_b128 a[156:159], v224 offset:18336\n\t"
"v_mfma_f32_16x16x32_bf16 v[132:135], a[44:47], a[164:167], v[132:135]\n\t"
"buffer_load_dwordx4 a[136:139], off, s[96:99], s78 offset:0 sc1\n\t"
"v_mfma_f32_16x16x32_bf16 v[116:119], a[32:35], a[148:151], v[116:119]\n\t"
"s_mov_b32 s56, 114688\n\t"
"ds_read_b64_tr_b16 a[196:197], v251 offset:20992\n\t"
"ds_read_b64_tr_b16 a[204:205], v250 offset:20992\n\t"
"v_mfma_f32_16x16x32_bf16 v[116:119], a[36:39], a[164:167], v[116:119]\n\t"
"buffer_load_dwordx4 a[140:143], off, s[96:99], s78 offset:1024 sc1\n\t"
"v_mfma_f32_16x16x32_bf16 v[100:103], a[24:27], a[148:151], v[100:103]\n\t"
"v_mfma_f32_16x16x32_bf16 v[100:103], a[28:31], a[164:167], v[100:103]\n\t"
"s_waitcnt lgkmcnt(2)\n\t"
"s_add_u32 s71, s71, 1\n\t"
"v_mov_b32 v215, s71\n\t"
"s_mov_b64 exec, 1\n\t"
"buffer_store_dword v215, off, s[8:11], s70 sc1\n\t"
"s_mov_b64 exec, -1\n\t"
"s_barrier\n\t"
"v_mfma_f32_16x16x32_bf16 v[104:107], a[24:27], a[152:155], v[104:107]\n\t"
"s_nop 0\n\t"
"ds_read_b128 a[144:147], v224 offset:0\n\t"
"ds_read_b128 a[148:151], v224 offset:256\n\t"
"v_mfma_f32_16x16x32_bf16 v[104:107], a[28:31], a[168:171], v[104:107]\n\t"
"buffer_load_dwordx4 a[248:251], v247, s[12:15], 0 offen offset:256 nt\n\t"
"v_mfma_f32_16x16x32_bf16 v[120:123], a[32:35], a[152:155], v[120:123]\n\t"
"ds_read_b128 a[160:163], v224 offset:2048\n\t"
"v_mfma_f32_16x16x32_bf16 v[120:123], a[36:39], a[168:171], v[120:123]\n\t"
"ds_read_b128 a[164:167], v224 offset:2304\n\t"
"v_mfma_f32_16x16x32_bf16 v[136:139], a[40:43], a[152:155], v[136:139]\n\t"
"v_mfma_f32_16x16x32_bf16 v[136:139], a[44:47], a[168:171], v[136:139]\n\t"
"v_mfma_f32_16x16x32_bf16 v[140:143], a[40:43], a[156:159], v[140:143]\n\t"
"v_mfma_f32_16x16x32_bf16 v[140:143], a[44:47], a[172:175], v[140:143]\n\t"
"buffer_load_dwordx4 a[0:3], off, s[96:99], s58 offset:0 sc1\n\t"
"v_mfma_f32_16x16x32_bf16 v[124:127], a[32:35], a[156:159], v[124:127]\n\t"
"ds_read_b128 a[168:171], v224 offset:2560\n\t"
"v_mfma_f32_16x16x32_bf16 v[124:127], a[36:39], a[172:175], v[124:127]\n\t"
"v_accvgpr_read_b32 v248, a208\n\t"
"v_mfma_f32_16x16x32_bf16 v[108:111], a[24:27], a[156:159], v[108:111]\n\t"
"v_mfma_f32_16x16x32_bf16 v[108:111], a[28:31], a[172:175], v[108:111]\n\t"
"s_waitcnt lgkmcnt(5)\n\t"
"buffer_load_dwordx4 a[4:7], off, s[96:99], s58 offset:1024 sc1\n\t"
"v_mfma_f32_4x4x4_16b_bf16 v[200:203], a[252:253], a[178:179], 0\n\t"
"ds_read_b128 a[172:175], v224 offset:2816\n\t"
"v_mfma_f32_4x4x4_16b_bf16 v[204:207], a[252:253], a[186:187], 0\n\t"
"v_mfma_f32_4x4x4_16b_bf16 v[216:219], a[252:253], a[194:195], 0\n\t"
"v_add_u32 v248, 101632, v248\n\t"
"v_mfma_f32_4x4x4_16b_bf16 v[220:223], a[252:253], a[202:203], 0\n\t"
".p2align 3\n\t"
"buffer_load_dwordx4 a[8:11], off, s[96:99], s58 offset:2048 sc1\n\t"
"v_pk_add_f32 v[96:97], v[0:1], v[96:97] neg_lo:[0,1] neg_hi:[0,1]\n\t"
"v_pk_add_f32 v[98:99], v[2:3], v[98:99] neg_lo:[0,1] neg_hi:[0,1]\n\t"
"v_pk_fma_f32 v[0:1], v[96:97], s[90:91], v[0:1] neg_lo:[1,0,0] neg_hi:[1,0,0]\n\t"
"v_pk_fma_f32 v[2:3], v[98:99], s[90:91], v[2:3] neg_lo:[1,0,0] neg_hi:[1,0,0]\n\t"
"v_pk_add_f32 v[112:113], v[16:17], v[112:113] neg_lo:[0,1] neg_hi:[0,1]\n\t"
"v_pk_add_f32 v[114:115], v[18:19], v[114:115] neg_lo:[0,1] neg_hi:[0,1]\n\t"
"v_pk_fma_f32 v[16:17], v[112:113], s[90:91], v[16:17] neg_lo:[1,0,0] neg_hi:[1,0,0]\n\t"
"v_pk_fma_f32 v[18:19], v[114:115], s[90:91], v[18:19] neg_lo:[1,0,0] neg_hi:[1,0,0]\n\t"
"v_pk_add_f32 v[128:129], v[32:33], v[128:129] neg_lo:[0,1] neg_hi:[0,1]\n\t"
"v_pk_add_f32 v[130:131], v[34:35], v[130:131] neg_lo:[0,1] neg_hi:[0,1]\n\t"
"v_pk_fma_f32 v[32:33], v[128:129], s[90:91], v[32:33] neg_lo:[1,0,0] neg_hi:[1,0,0]\n\t"
"v_pk_fma_f32 v[34:35], v[130:131], s[90:91], v[34:35] neg_lo:[1,0,0] neg_hi:[1,0,0]\n\t"
"v_pk_add_f32 v[132:133], v[36:37], v[132:133] neg_lo:[0,1] neg_hi:[0,1]\n\t"
"v_pk_add_f32 v[134:135], v[38:39], v[134:135] neg_lo:[0,1] neg_hi:[0,1]\n\t"
"v_pk_fma_f32 v[36:37], v[132:133], s[90:91], v[36:37] neg_lo:[1,0,0] neg_hi:[1,0,0]\n\t"
"v_pk_fma_f32 v[38:39], v[134:135], s[90:91], v[38:39] neg_lo:[1,0,0] neg_hi:[1,0,0]\n\t"
"v_pk_add_f32 v[116:117], v[20:21], v[116:117] neg_lo:[0,1] neg_hi:[0,1]\n\t"
"v_pk_add_f32 v[118:119], v[22:23], v[118:119] neg_lo:[0,1] neg_hi:[0,1]\n\t"
"v_pk_fma_f32 v[20:21], v[116:117], s[90:91], v[20:21] neg_lo:[1,0,0] neg_hi:[1,0,0]\n\t"
"v_pk_fma_f32 v[22:23], v[118:119], s[90:91], v[22:23] neg_lo:[1,0,0] neg_hi:[1,0,0]\n\t"
"v_pk_add_f32 v[100:101], v[4:5], v[100:101] neg_lo:[0,1] neg_hi:[0,1]\n\t"
"v_pk_add_f32 v[102:103], v[6:7], v[102:103] neg_lo:[0,1] neg_hi:[0,1]\n\t"
"v_pk_fma_f32 v[4:5], v[100:101], s[90:91], v[4:5] neg_lo:[1,0,0] neg_hi:[1,0,0]\n\t"
"v_pk_fma_f32 v[6:7], v[102:103], s[90:91], v[6:7] neg_lo:[1,0,0] neg_hi:[1,0,0]\n\t"
"v_pk_add_f32 v[104:105], v[8:9], v[104:105] neg_lo:[0,1] neg_hi:[0,1]\n\t"
"v_pk_add_f32 v[106:107], v[10:11], v[106:107] neg_lo:[0,1] neg_hi:[0,1]\n\t"
"v_pk_fma_f32 v[8:9], v[104:105], s[90:91], v[8:9] neg_lo:[1,0,0] neg_hi:[1,0,0]\n\t"
"v_pk_fma_f32 v[10:11], v[106:107], s[90:91], v[10:11] neg_lo:[1,0,0] neg_hi:[1,0,0]\n\t"
"v_pk_add_f32 v[120:121], v[24:25], v[120:121] neg_lo:[0,1] neg_hi:[0,1]\n\t"
"v_pk_add_f32 v[122:123], v[26:27], v[122:123] neg_lo:[0,1] neg_hi:[0,1]\n\t"
"v_pk_fma_f32 v[24:25], v[120:121], s[90:91], v[24:25] neg_lo:[1,0,0] neg_hi:[1,0,0]\n\t"
"v_pk_fma_f32 v[26:27], v[122:123], s[90:91], v[26:27] neg_lo:[1,0,0] neg_hi:[1,0,0]\n\t"
"v_pk_add_f32 v[136:137], v[40:41], v[136:137] neg_lo:[0,1] neg_hi:[0,1]\n\t"
"v_pk_add_f32 v[138:139], v[42:43], v[138:139] neg_lo:[0,1] neg_hi:[0,1]\n\t"
"v_pk_fma_f32 v[40:41], v[136:137], s[90:91], v[40:41] neg_lo:[1,0,0] neg_hi:[1,0,0]\n\t"
"v_pk_fma_f32 v[42:43], v[138:139], s[90:91], v[42:43] neg_lo:[1,0,0] neg_hi:[1,0,0]\n\t"
"v_pk_add_f32 v[140:141], v[44:45], v[140:141] neg_lo:[0,1] neg_hi:[0,1]\n\t"
"v_pk_add_f32 v[142:143], v[46:47], v[142:143] neg_lo:[0,1] neg_hi:[0,1]\n\t"
"v_pk_fma_f32 v[44:45], v[140:141], s[90:91], v[44:45] neg_lo:[1,0,0] neg_hi:[1,0,0]\n\t"
"v_pk_fma_f32 v[46:47], v[142:143], s[90:91], v[46:47] neg_lo:[1,0,0] neg_hi:[1,0,0]\n\t"
"v_pk_add_f32 v[124:125], v[28:29], v[124:125] neg_lo:[0,1] neg_hi:[0,1]\n\t"
"v_pk_add_f32 v[126:127], v[30:31], v[126:127] neg_lo:[0,1] neg_hi:[0,1]\n\t"
"v_pk_fma_f32 v[28:29], v[124:125], s[90:91], v[28:29] neg_lo:[1,0,0] neg_hi:[1,0,0]\n\t"
"v_pk_fma_f32 v[30:31], v[126:127], s[90:91], v[30:31] neg_lo:[1,0,0] neg_hi:[1,0,0]\n\t"
"v_pk_add_f32 v[108:109], v[12:13], v[108:109] neg_lo:[0,1] neg_hi:[0,1]\n\t"
"v_pk_add_f32 v[110:111], v[14:15], v[110:111] neg_lo:[0,1] neg_hi:[0,1]\n\t"
"v_pk_fma_f32 v[12:13], v[108:109], s[90:91], v[12:13] neg_lo:[1,0,0] neg_hi:[1,0,0]\n\t"
"v_pk_fma_f32 v[14:15], v[110:111], s[90:91], v[14:15] neg_lo:[1,0,0] neg_hi:[1,0,0]\n\t"
"v_mfma_f32_4x4x4_16b_bf16 v[192:195], a[254:255], a[176:177], v[200:203]\n\t"
"ds_read_b128 a[152:155], v224 offset:512\n\t"
"v_mfma_f32_4x4x4_16b_bf16 v[196:199], a[254:255], a[184:185], v[204:207]\n\t"
"v_mfma_f32_4x4x4_16b_bf16 v[200:203], a[254:255], a[180:181], v[200:203]\n\t"
"v_mfma_f32_4x4x4_16b_bf16 v[204:207], a[254:255], a[188:189], v[204:207]\n\t"
"v_xor_b32 v249, 32, v248\n\t"
"v_mfma_f32_4x4x4_16b_bf16 v[208:211], a[254:255], a[192:193], v[216:219]\n\t"
"ds_read_b128 a[156:159], v224 offset:768\n\t"
"v_mfma_f32_4x4x4_16b_bf16 v[212:215], a[254:255], a[200:201], v[220:223]\n\t"
"v_mfma_f32_4x4x4_16b_bf16 v[216:219], a[254:255], a[196:197], v[216:219]\n\t"
"v_mfma_f32_4x4x4_16b_bf16 v[220:223], a[254:255], a[204:205], v[220:223]\n\t"
"s_waitcnt vmcnt(27) lgkmcnt(5)\n\t"
"buffer_load_dwordx4 a[12:15], off, s[96:99], s58 offset:3072 sc1\n\t"
"v_mfma_f32_16x16x32_bf16 v[0:3], a[48:51], a[144:147], v[0:3]\n\t"
"s_nop 0\n\t"
"v_cvt_pk_bf16_f32 v194, -v194, -v198\n\t"
"v_cvt_pk_bf16_f32 v195, -v195, -v199\n\t"
"v_mfma_f32_16x16x32_bf16 v[0:3], a[52:55], a[160:163], v[0:3]\n\t"
"s_waitcnt vmcnt(26)\n\t"
"buffer_load_dwordx4 a[16:19], off, s[92:95], s59 offset:0 sc1\n\t"
"v_mfma_f32_16x16x32_bf16 v[16:19], a[56:59], a[144:147], v[16:19]\n\t"
"s_nop 0\n\t"
"ds_write_addtid_b32 v194 offset:17568\n\t"
"v_cvt_pk_bf16_f32 v202, v202, v206\n\t"
"v_mfma_f32_16x16x32_bf16 v[16:19], a[60:63], a[160:163], v[16:19]\n\t"
"s_waitcnt vmcnt(25)\n\t"
"buffer_load_dwordx4 a[20:23], off, s[92:95], s59 offset:1024 sc1\n\t"
"v_mfma_f32_16x16x32_bf16 v[32:35], a[64:67], a[144:147], v[32:35]\n\t"
"s_nop 0\n\t"
"ds_read_b128 a[144:147], v224 offset:1024\n\t"
"v_cvt_pk_bf16_f32 v211, -v211, -v215\n\t"
"v_mfma_f32_16x16x32_bf16 v[32:35], a[68:71], a[160:163], v[32:35]\n\t"
"s_waitcnt lgkmcnt(6)\n\t"
"buffer_load_dwordx4 a[24:27], off, s[96:99], s57 offset:2048 sc1\n\t"
"v_mfma_f32_16x16x32_bf16 v[36:39], a[64:67], a[148:151], v[36:39]\n\t"
"s_nop 0\n\t"
"ds_read_b128 a[160:163], v224 offset:3072\n\t"
"v_cvt_pk_bf16_f32 v201, v201, v205\n\t"
"v_mfma_f32_16x16x32_bf16 v[36:39], a[68:71], a[164:167], v[36:39]\n\t"
"s_nop 0\n\t"
"ds_write_addtid_b32 v211 offset:23744\n\t"
"v_cvt_pk_bf16_f32 v203, v203, v207\n\t"
"v_mfma_f32_16x16x32_bf16 v[20:23], a[56:59], a[148:151], v[20:23]\n\t"
"s_nop 0\n\t"
"ds_write_addtid_b32 v203 offset:30016\n\t"
"v_cvt_pk_bf16_f32 v217, v217, v221\n\t"
"v_mfma_f32_16x16x32_bf16 v[20:23], a[60:63], a[164:167], v[20:23]\n\t"
"buffer_load_dwordx4 a[28:31], off, s[96:99], s57 offset:3072 sc1\n\t"
"v_mfma_f32_16x16x32_bf16 v[4:7], a[48:51], a[148:151], v[4:7]\n\t"
"s_nop 0\n\t"
"ds_read_b128 a[148:151], v224 offset:1280\n\t"
"v_cvt_pk_bf16_f32 v192, -v192, -v196\n\t"
"v_mfma_f32_16x16x32_bf16 v[4:7], a[52:55], a[164:167], v[4:7]\n\t"
"s_waitcnt lgkmcnt(7)\n\t"
"ds_write_addtid_b32 v201 offset:28992\n\t"
"v_cvt_pk_bf16_f32 v193, -v193, -v197\n\t"
"s_mov_b32 s65, 163840\n\t"
"v_mfma_f32_16x16x32_bf16 v[8:11], a[48:51], a[152:155], v[8:11]\n\t"
"s_nop 0\n\t"
"ds_read_b128 a[164:167], v224 offset:3328\n\t"
"v_cvt_pk_bf16_f32 v219, v219, v223\n\t"
"v_mfma_f32_16x16x32_bf16 v[8:11], a[52:55], a[168:171], v[8:11]\n\t"
"buffer_load_dwordx4 a[32:35], off, s[92:95], s58 offset:0 sc1\n\t"
"v_mfma_f32_16x16x32_bf16 v[24:27], a[56:59], a[152:155], v[24:27]\n\t"
"s_mov_b32 s68, 188416\n\t"
"ds_write_addtid_b32 v219 offset:32064\n\t"
"v_cvt_pk_bf16_f32 v208, -v208, -v212\n\t"
"v_mfma_f32_16x16x32_bf16 v[24:27], a[60:63], a[168:171], v[24:27]\n\t"
"s_nop 0\n\t"
"ds_write_addtid_b32 v193 offset:20672\n\t"
"v_cvt_pk_bf16_f32 v216, v216, v220\n\t"
"v_mfma_f32_16x16x32_bf16 v[40:43], a[64:67], a[152:155], v[40:43]\n\t"
"s_nop 0\n\t"
"ds_read_b128 a[152:155], v224 offset:1536\n\t"
"v_cvt_pk_bf16_f32 v218, v218, v222\n\t"
"v_mfma_f32_16x16x32_bf16 v[40:43], a[68:71], a[168:171], v[40:43]\n\t"
"s_waitcnt lgkmcnt(11)\n\t"
"buffer_load_dwordx4 a[36:39], off, s[92:95], s58 offset:1024 sc1\n\t"
"v_mfma_f32_16x16x32_bf16 v[44:47], a[64:67], a[156:159], v[44:47]\n\t"
"s_nop 0\n\t"
"ds_read_b128 a[168:171], v224 offset:3584\n\t"
"v_cvt_pk_bf16_f32 v200, v200, v204\n\t"
"v_mfma_f32_16x16x32_bf16 v[44:47], a[68:71], a[172:175], v[44:47]\n\t"
"s_nop 0\n\t"
"ds_write_addtid_b32 v200 offset:24864\n\t"
"v_cvt_pk_bf16_f32 v210, -v210, -v214\n\t"
"v_mfma_f32_16x16x32_bf16 v[28:31], a[56:59], a[156:159], v[28:31]\n\t"
"s_mov_b32 s64, 155648\n\t"
"ds_write_addtid_b32 v195 offset:21696\n\t"
"ds_write_addtid_b32 v217 offset:31040\n\t"
"v_mfma_f32_16x16x32_bf16 v[28:31], a[60:63], a[172:175], v[28:31]\n\t"
"buffer_load_dwordx4 a[40:43], off, s[92:95], s58 offset:2048 sc1\n\t"
"v_mfma_f32_16x16x32_bf16 v[12:15], a[48:51], a[156:159], v[12:15]\n\t"
"s_mov_b32 s72, 204800\n\t"
"ds_write_addtid_b32 v192 offset:16544\n\t"
"ds_write_addtid_b32 v202 offset:25888\n\t"
"v_mfma_f32_16x16x32_bf16 v[12:15], a[52:55], a[172:175], v[12:15]\n\t"
"s_waitcnt vmcnt(29) lgkmcnt(14)\n\t"
"ds_write_addtid_b32 v216 offset:26912\n\t"
"v_cvt_pk_bf16_f32 v209, -v209, -v213\n\t"
"v_mfma_f32_16x16x32_bf16 v[96:99], a[72:75], a[144:147], v[96:99]\n\t"
"s_mov_b32 s74, 212992\n\t"
"ds_write_addtid_b32 v208 offset:18592\n\t"
"ds_write_addtid_b32 v210 offset:19616\n\t"
"v_mfma_f32_16x16x32_bf16 v[96:99], a[76:79], a[160:163], v[96:99]\n\t"
"s_waitcnt vmcnt(27)\n\t"
"buffer_load_dwordx4 a[44:47], off, s[92:95], s58 offset:3072 sc1\n\t"
"v_mfma_f32_16x16x32_bf16 v[112:115], a[80:83], a[144:147], v[112:115]\n\t"
"s_mov_b32 s67, 180224\n\t"
"ds_write_addtid_b32 v209 offset:22720\n\t"
"ds_write_addtid_b32 v218 offset:27936\n\t"
"v_mfma_f32_16x16x32_bf16 v[112:115], a[84:87], a[160:163], v[112:115]\n\t"
"s_waitcnt vmcnt(26)\n\t"
"v_mfma_f32_16x16x32_bf16 v[128:131], a[88:91], a[144:147], v[128:131]\n\t"
"s_add_u32 m0, s43, 33280\n\t"
"ds_read_b128 a[156:159], v224 offset:1792\n\t"
"v_xor_b32 v250, 64, v248\n\t"
"v_mfma_f32_16x16x32_bf16 v[128:131], a[92:95], a[160:163], v[128:131]\n\t"
"s_waitcnt lgkmcnt(15)\n\t"
"s_mov_b64 exec, s[44:45]\n\t"
"buffer_load_dwordx4 v247, s[12:15], 0 offen offset:0 nt lds\n\t"
"s_mov_b64 exec, -1\n\t"
"v_mfma_f32_16x16x32_bf16 v[132:135], a[88:91], a[148:151], v[132:135]\n\t"
"s_add_u32 m0, s26, 35584\n\t"
"ds_read_b128 a[172:175], v224 offset:3840\n\t"
"v_mfma_f32_16x16x32_bf16 v[132:135], a[92:95], a[164:167], v[132:135]\n\t"
"s_nop 0\n\t"
"v_mfma_f32_16x16x32_bf16 v[116:119], a[80:83], a[148:151], v[116:119]\n\t"
"s_nop 0\n\t"
"v_mfma_f32_16x16x32_bf16 v[116:119], a[84:87], a[164:167], v[116:119]\n\t"
"buffer_load_dwordx4 v236, s[12:15], s22 offen offset:0 nt lds\n\t"
"v_mfma_f32_16x16x32_bf16 v[100:103], a[72:75], a[148:151], v[100:103]\n\t"
"s_lshl_b32 m0, s73, 8\n\t"
"v_xor_b32 v251, 96, v248\n\t"
"v_mfma_f32_16x16x32_bf16 v[100:103], a[76:79], a[164:167], v[100:103]\n\t"
"s_waitcnt lgkmcnt(0)\n\t"
"s_barrier\n\t"
"v_mfma_f32_16x16x32_bf16 v[104:107], a[72:75], a[152:155], v[104:107]\n\t"
"s_add_u32 m0, s26, 35584\n\t"
"ds_read_b128 a[144:147], v224 offset:16544\n\t"
"ds_read_b128 a[164:167], v224 offset:18848\n\t"
"v_mfma_f32_16x16x32_bf16 v[104:107], a[76:79], a[168:171], v[104:107]\n\t"
"s_nop 0\n\t"
"ds_read_b128 a[160:163], v224 offset:18592\n\t"
"v_mfma_f32_16x16x32_bf16 v[120:123], a[80:83], a[152:155], v[120:123]\n\t"
"s_nop 0\n\t"
"ds_read_b128 a[148:151], v224 offset:16800\n\t"
"v_mfma_f32_16x16x32_bf16 v[120:123], a[84:87], a[168:171], v[120:123]\n\t"
"s_nop 0\n\t"
"ds_read_b64_tr_b16 a[176:177], v248 offset:20736\n\t"
"v_mfma_f32_16x16x32_bf16 v[136:139], a[88:91], a[152:155], v[136:139]\n\t"
"s_nop 0\n\t"
"ds_read_b64_tr_b16 a[178:179], v249 offset:20736\n\t"
"v_mfma_f32_16x16x32_bf16 v[136:139], a[92:95], a[168:171], v[136:139]\n\t"
"s_nop 0\n\t"
"ds_read_b64_tr_b16 a[180:181], v248 offset:21248\n\t"
"ds_read_b64_tr_b16 a[182:183], v249 offset:21248\n\t"
"v_mfma_f32_16x16x32_bf16 v[140:143], a[88:91], a[156:159], v[140:143]\n\t"
"s_nop 0\n\t"
"ds_read_b64_tr_b16 a[184:185], v250 offset:23040\n\t"
"v_mfma_f32_16x16x32_bf16 v[140:143], a[92:95], a[172:175], v[140:143]\n\t"
"buffer_load_dwordx4 v237, s[12:15], s22 offen offset:1024 nt lds\n\t"
"v_mfma_f32_16x16x32_bf16 v[124:127], a[80:83], a[156:159], v[124:127]\n\t"
"s_nop 0\n\t"
"ds_read_b128 a[152:155], v224 offset:17056\n\t"
"v_mfma_f32_16x16x32_bf16 v[124:127], a[84:87], a[172:175], v[124:127]\n\t"
"s_waitcnt lgkmcnt(2)\n\t"
"ds_read_b128 a[168:171], v224 offset:19104\n\t"
"v_mfma_f32_4x4x4_16b_bf16 v[192:195], a[210:211], a[176:177], 0\n\t"
"ds_read_b64_tr_b16 a[186:187], v251 offset:23040\n\t"
"v_mfma_f32_4x4x4_16b_bf16 v[196:199], a[210:211], a[178:179], 0\n\t"
"ds_read_b64_tr_b16 a[188:189], v250 offset:23552\n\t"
"v_mfma_f32_4x4x4_16b_bf16 v[200:203], a[210:211], a[180:181], 0\n\t"
"ds_read_b64_tr_b16 a[190:191], v251 offset:23552\n\t"
"v_mfma_f32_4x4x4_16b_bf16 v[204:207], a[210:211], a[182:183], 0\n\t"
"ds_read_b64_tr_b16 a[192:193], v250 offset:20736\n\t"
"v_mfma_f32_16x16x32_bf16 v[108:111], a[72:75], a[156:159], v[108:111]\n\t"
"s_mov_b32 s76, 229376\n\t"
"ds_read_b64_tr_b16 a[194:195], v251 offset:20736\n\t"
"ds_read_b64_tr_b16 a[196:197], v250 offset:21248\n\t"
"v_mfma_f32_16x16x32_bf16 v[108:111], a[76:79], a[172:175], v[108:111]\n\t"
"s_waitcnt vmcnt(27)\n\t"
"buffer_load_dwordx4 v238, s[12:15], s22 offen offset:2048 nt lds\n\t"
"v_mfma_f32_16x16x32_bf16 v[0:3], a[96:99], a[144:147], v[0:3]\n\t"
"s_nop 0\n\t"
"ds_read_b128 a[156:159], v224 offset:17312\n\t"
"v_cvt_pk_bf16_f32 v196, v192, v196\n\t"
"v_mfma_f32_16x16x32_bf16 v[0:3], a[100:103], a[160:163], v[0:3]\n\t"
"s_waitcnt vmcnt(26)\n\t"
"ds_read_b64_tr_b16 a[198:199], v251 offset:21248\n\t"
"v_cvt_pk_bf16_f32 v194, v194, v198\n\t"
"v_mfma_f32_16x16x32_bf16 v[16:19], a[104:107], a[144:147], v[16:19]\n\t"
"s_nop 0\n\t"
"ds_read_b64_tr_b16 a[200:201], v248 offset:23040\n\t"
"v_cvt_pk_bf16_f32 v195, v195, v199\n\t"
"v_mfma_f32_16x16x32_bf16 v[16:19], a[108:111], a[160:163], v[16:19]\n\t"
"s_waitcnt vmcnt(24)\n\t"
"ds_read_b64_tr_b16 a[202:203], v249 offset:23040\n\t"
"v_cvt_pk_bf16_f32 v192, v202, v206\n\t"
"v_mfma_f32_16x16x32_bf16 v[32:35], a[112:115], a[144:147], v[32:35]\n\t"
"s_nop 0\n\t"
"ds_read_b64_tr_b16 a[204:205], v248 offset:23552\n\t"
"v_cvt_pk_bf16_f32 v199, v201, v205\n\t"
"v_mfma_f32_16x16x32_bf16 v[32:35], a[116:119], a[160:163], v[32:35]\n\t"
"s_waitcnt lgkmcnt(8)\n\t"
"ds_read_b128 a[144:147], v224 offset:17568\n\t"
"v_cvt_pk_bf16_f32 v197, v193, v197\n\t"
"v_mfma_f32_4x4x4_16b_bf16 v[208:211], a[210:211], a[184:185], 0\n\t"
"buffer_load_dwordx4 v239, s[12:15], s22 offen offset:3072 nt lds\n\t"
"v_mfma_f32_4x4x4_16b_bf16 v[212:215], a[210:211], a[186:187], 0\n\t"
"ds_read_b128 a[172:175], v224 offset:19360\n\t"
"v_mfma_f32_4x4x4_16b_bf16 v[216:219], a[210:211], a[188:189], 0\n\t"
"s_lshl_b32 m0, s73, 8\n\t"
"ds_read_b64_tr_b16 a[206:207], v249 offset:23552\n\t"
"v_mfma_f32_4x4x4_16b_bf16 v[220:223], a[210:211], a[190:191], 0\n\t"
"ds_write_addtid_b32 v199 offset:9344\n\t"
"v_cvt_pk_bf16_f32 v193, v203, v207\n\t"
"v_mfma_f32_16x16x32_bf16 v[36:39], a[112:115], a[148:151], v[36:39]\n\t"
"s_nop 0\n\t"
"ds_write_addtid_b32 v196 offset:0\n\t"
"v_cvt_pk_bf16_f32 v198, v200, v204\n\t"
"v_mfma_f32_16x16x32_bf16 v[36:39], a[116:119], a[164:167], v[36:39]\n\t"
"s_nop 0\n\t"
"ds_write_addtid_b32 v198 offset:8320\n\t"
"ds_write_addtid_b32 v197 offset:1024\n\t"
"v_mfma_f32_16x16x32_bf16 v[20:23], a[104:107], a[148:151], v[20:23]\n\t"
"s_add_u32 m0, s26, 35584\n\t"
"v_cvt_pk_bf16_f32 v213, v209, v213\n\t"
"v_cvt_pk_bf16_f32 v211, v211, v215\n\t"
"v_mfma_f32_16x16x32_bf16 v[20:23], a[108:111], a[164:167], v[20:23]\n\t"
"s_nop 0\n\t"
"v_cvt_pk_bf16_f32 v212, v208, v212\n\t"
"v_cvt_pk_bf16_f32 v210, v210, v214\n\t"
"v_mfma_f32_16x16x32_bf16 v[4:7], a[96:99], a[148:151], v[4:7]\n\t"
"s_nop 0\n\t"
"ds_read_b128 a[148:151], v224 offset:17824\n\t"
"v_cvt_pk_bf16_f32 v209, v219, v223\n\t"
"v_mfma_f32_16x16x32_bf16 v[4:7], a[100:103], a[164:167], v[4:7]\n\t"
"s_waitcnt lgkmcnt(11)\n\t"
"s_mov_b64 exec, s[36:37]\n\t"
"buffer_load_dwordx4 v240, s[12:15], s22 offen offset:3584 nt lds\n\t"
"s_mov_b64 exec, -1\n\t"
"v_mfma_f32_4x4x4_16b_bf16 v[200:203], a[210:211], a[192:193], 0\n\t"
"ds_read_b128 a[160:163], v224 offset:19616\n\t"
"v_mfma_f32_4x4x4_16b_bf16 v[204:207], a[210:211], a[194:195], 0\n\t"
"v_cvt_pk_bf16_f32 v208, v218, v222\n\t"
"v_cvt_pk_bf16_f32 v215, v217, v221\n\t"
"v_mfma_f32_4x4x4_16b_bf16 v[196:199], a[210:211], a[196:197], 0\n\t"
"s_lshl_b32 m0, s73, 8\n\t"
"v_cvt_pk_bf16_f32 v214, v216, v220\n\t"
"v_mfma_f32_4x4x4_16b_bf16 v[216:219], a[210:211], a[198:199], 0\n\t"
"s_mov_b32 s80, 262144\n\t"
"ds_write_addtid_b32 v212 offset:4128\n\t"
"ds_write_addtid_b32 v213 offset:5152\n\t"
"v_mfma_f32_16x16x32_bf16 v[8:11], a[96:99], a[152:155], v[8:11]\n\t"
"s_nop 0\n\t"
"ds_write_addtid_b32 v214 offset:12448\n\t"
"ds_write_addtid_b32 v215 offset:13472\n\t"
"v_mfma_f32_16x16x32_bf16 v[8:11], a[100:103], a[168:171], v[8:11]\n\t"
"buffer_load_dwordx4 a[48:51], off, s[96:99], s64 offset:0 sc1\n\t"
"v_mfma_f32_16x16x32_bf16 v[24:27], a[104:107], a[152:155], v[24:27]\n\t"
"s_nop 0\n\t"
"v_cvt_pk_bf16_f32 v204, v200, v204\n\t"
"v_cvt_pk_bf16_f32 v203, v203, v207\n\t"
"v_mfma_f32_16x16x32_bf16 v[24:27], a[108:111], a[168:171], v[24:27]\n\t"
"buffer_load_dwordx4 a[52:55], off, s[96:99], s64 offset:1024 sc1\n\t"
"v_mfma_f32_16x16x32_bf16 v[40:43], a[112:115], a[152:155], v[40:43]\n\t"
"s_nop 0\n\t"
"ds_read_b128 a[164:167], v224 offset:19872\n\t"
"v_cvt_pk_bf16_f32 v202, v202, v206\n\t"
"v_mfma_f32_16x16x32_bf16 v[40:43], a[116:119], a[168:171], v[40:43]\n\t"
"s_waitcnt lgkmcnt(12)\n\t"
"v_cvt_pk_bf16_f32 v200, v198, v218\n\t"
"v_cvt_pk_bf16_f32 v206, v196, v216\n\t"
"v_mfma_f32_16x16x32_bf16 v[44:47], a[112:115], a[156:159], v[44:47]\n\t"
"s_nop 0\n\t"
"v_cvt_pk_bf16_f32 v205, v201, v205\n\t"
"v_cvt_pk_bf16_f32 v201, v199, v219\n\t"
"v_mfma_f32_16x16x32_bf16 v[44:47], a[116:119], a[172:175], v[44:47]\n\t"
"Lpad2_%=:\n\t"
"s_cbranch_execz Lpad6_%=\n\t"
"s_waitcnt lgkmcnt(11)\n\t"
"buffer_load_dwordx4 a[56:59], off, s[96:99], s64 offset:2048 sc1\n\t"
"v_mfma_f32_4x4x4_16b_bf16 v[220:223], a[210:211], a[200:201], 0\n\t"
"ds_write_addtid_b32 v205 offset:3072\n\t"
"v_cvt_pk_bf16_f32 v207, v197, v217\n\t"
"v_mfma_f32_4x4x4_16b_bf16 v[212:215], a[210:211], a[202:203], 0\n\t"
"buffer_load_dwordx4 a[60:63], off, s[96:99], s64 offset:3072 sc1\n\t"
"v_mfma_f32_4x4x4_16b_bf16 v[196:199], a[210:211], a[204:205], 0\n\t"
"ds_write_addtid_b32 v204 offset:2048\n\t"
"v_mfma_f32_4x4x4_16b_bf16 v[216:219], a[210:211], a[206:207], 0\n\t"
"buffer_load_dwordx4 a[64:67], off, s[92:95], s65 offset:0 sc1\n\t"
"v_mfma_f32_16x16x32_bf16 v[28:31], a[104:107], a[156:159], v[28:31]\n\t"
"s_mov_b32 s79, 253952\n\t"
"ds_write_addtid_b32 v206 offset:10368\n\t"
"ds_write_addtid_b32 v207 offset:11392\n\t"
"v_mfma_f32_16x16x32_bf16 v[28:31], a[108:111], a[172:175], v[28:31]\n\t"
"buffer_load_dwordx4 a[68:71], off, s[92:95], s65 offset:1024 sc1\n\t"
"v_mfma_f32_16x16x32_bf16 v[12:15], a[96:99], a[156:159], v[12:15]\n\t"
"v_cvt_pk_bf16_f32 v213, v221, v213\n\t"
"v_cvt_pk_bf16_f32 v223, v223, v215\n\t"
"v_mfma_f32_16x16x32_bf16 v[12:15], a[100:103], a[172:175], v[12:15]\n\t"
"s_waitcnt vmcnt(30) lgkmcnt(9)\n\t"
"v_cvt_pk_bf16_f32 v212, v220, v212\n\t"
"v_cvt_pk_bf16_f32 v222, v222, v214\n\t"
"v_mfma_f32_16x16x32_bf16 v[96:99], a[120:123], a[144:147], v[96:99]\n\t"
"ds_write_addtid_b32 v212 offset:6176\n\t"
"v_cvt_pk_bf16_f32 v214, v196, v216\n\t"
"v_mfma_f32_16x16x32_bf16 v[96:99], a[124:127], a[160:163], v[96:99]\n\t"
"s_waitcnt vmcnt(28)\n\t"
"ds_write_addtid_b32 v213 offset:7200\n\t"
"buffer_load_dwordx4 a[72:75], off, s[96:99], s63 offset:2048 sc1\n\t"
"v_cvt_pk_bf16_f32 v215, v197, v217\n\t"
"v_mfma_f32_16x16x32_bf16 v[112:115], a[128:131], a[144:147], v[112:115]\n\t"
"s_mov_b32 s82, 278528\n\t"
"ds_write_addtid_b32 v214 offset:14496\n\t"
"ds_write_addtid_b32 v215 offset:15520\n\t"
"v_mfma_f32_16x16x32_bf16 v[112:115], a[132:135], a[160:163], v[112:115]\n\t"
"s_waitcnt vmcnt(27)\n\t"
"buffer_load_dwordx4 a[76:79], off, s[96:99], s63 offset:3072 sc1\n\t"
"buffer_load_dwordx4 a[80:83], off, s[92:95], s64 offset:0 sc1\n\t"
"v_mfma_f32_16x16x32_bf16 v[128:131], a[136:139], a[144:147], v[128:131]\n\t"
"s_mov_b32 s83, 286720\n\t"
"ds_read_b128 a[152:155], v224 offset:18080\n\t"
"ds_read_b128 a[172:175], v224 offset:20384\n\t"
"v_mfma_f32_16x16x32_bf16 v[128:131], a[140:143], a[160:163], v[128:131]\n\t"
"s_waitcnt lgkmcnt(10)\n\t"
"buffer_load_dwordx4 a[84:87], off, s[92:95], s64 offset:1024 sc1\n\t"
"v_mfma_f32_16x16x32_bf16 v[132:135], a[136:139], a[148:151], v[132:135]\n\t"
"s_mov_b32 s77, 237568\n\t"
"ds_read_b128 a[168:171], v224 offset:20128\n\t"
"ds_read_b128 a[156:159], v224 offset:18336\n\t"
"v_mfma_f32_16x16x32_bf16 v[132:135], a[140:143], a[164:167], v[132:135]\n\t"
"buffer_load_dwordx4 a[88:91], off, s[92:95], s64 offset:2048 sc1\n\t"
"buffer_load_dwordx4 a[92:95], off, s[92:95], s64 offset:3072 sc1\n\t"
"v_mfma_f32_16x16x32_bf16 v[116:119], a[128:131], a[148:151], v[116:119]\n\t"
"v_accvgpr_read_b32 v224, a213\n\t"
"v_cvt_pk_bf16_f32 v221, v199, v219\n\t"
"v_mfma_f32_16x16x32_bf16 v[116:119], a[132:135], a[164:167], v[116:119]\n\t"
"buffer_load_dwordx4 a[108:111], off, s[96:99], s46 offset:3072 sc1\n\t"
"v_mfma_f32_16x16x32_bf16 v[100:103], a[120:123], a[148:151], v[100:103]\n\t"
"v_cvt_pk_bf16_f32 v220, v198, v218\n\t"
"v_mfma_f32_16x16x32_bf16 v[100:103], a[124:127], a[164:167], v[100:103]\n\t"
"buffer_load_dwordx4 a[112:115], off, s[92:95], s47 offset:0 sc1\n\t"
"buffer_load_dwordx4 a[116:119], off, s[92:95], s47 offset:1024 sc1\n\t"
"v_add_u32 v246, 8, v228\n\t"
"v_add_u32 v247, 16, v228\n\t"
"s_waitcnt lgkmcnt(0)\n\t"
"s_barrier\n\t"
"Lunit_%=:\n\t"
"v_mfma_f32_16x16x32_bf16 v[104:107], a[120:123], a[152:155], v[104:107]\n\t"
"s_nop 0\n\t"
"ds_read_b128 a[144:147], v224 offset:1024\n\t"
"ds_read_b128 a[148:151], v224 offset:1280\n\t"
"v_mfma_f32_16x16x32_bf16 v[104:107], a[124:127], a[168:171], v[104:107]\n\t"
"buffer_load_dwordx4 a[104:107], off, s[96:99], s46 offset:2048 sc1\n\t"
"v_mfma_f32_16x16x32_bf16 v[120:123], a[128:131], a[152:155], v[120:123]\n\t"
"ds_read_b128 a[160:163], v224 offset:3072\n\t"
"v_mfma_f32_16x16x32_bf16 v[120:123], a[132:135], a[168:171], v[120:123]\n\t"
"ds_read_b128 a[164:167], v224 offset:3328\n\t"
"v_mfma_f32_16x16x32_bf16 v[136:139], a[136:139], a[152:155], v[136:139]\n\t"
"ds_write_addtid_b32 v208 offset:28992\n\t"
"v_mfma_f32_16x16x32_bf16 v[136:139], a[140:143], a[168:171], v[136:139]\n\t"
"buffer_load_dwordx4 a[96:99], off, s[96:99], s46 offset:0 sc1\n\t"
"v_mfma_f32_16x16x32_bf16 v[140:143], a[136:139], a[156:159], v[140:143]\n\t"
"ds_read_b128 a[168:171], v224 offset:3584\n\t"
"v_mfma_f32_16x16x32_bf16 v[140:143], a[140:143], a[172:175], v[140:143]\n\t"
"ds_read_b128 a[152:155], v224 offset:1536\n\t"
"v_mfma_f32_16x16x32_bf16 v[124:127], a[128:131], a[156:159], v[124:127]\n\t"
"ds_write_addtid_b32 v203 offset:19616\n\t"
"v_mfma_f32_16x16x32_bf16 v[124:127], a[132:135], a[172:175], v[124:127]\n\t"
"buffer_load_dwordx4 a[100:103], off, s[96:99], s46 offset:1024 sc1\n\t"
"v_mfma_f32_16x16x32_bf16 v[108:111], a[120:123], a[156:159], v[108:111]\n\t"
"ds_read_b128 a[156:159], v224 offset:1792\n\t"
"v_mfma_f32_16x16x32_bf16 v[108:111], a[124:127], a[172:175], v[108:111]\n\t"
"s_waitcnt vmcnt(34) lgkmcnt(6)\n\t"
"ds_write_addtid_b32 v194 offset:16544\n\t"
"v_mfma_f32_16x16x32_bf16 v[48:51], a[0:3], a[144:147], v[0:3]\n\t"
"ds_read_b128 a[172:175], v224 offset:3840\n\t"
"v_mfma_f32_16x16x32_bf16 v[48:51], a[4:7], a[160:163], v[48:51]\n\t"
"s_waitcnt vmcnt(32) lgkmcnt(7)\n\t"
"buffer_load_dwordx4 a[136:139], off, s[92:95], s46 offset:2048 sc1\n\t"
"v_mfma_f32_16x16x32_bf16 v[64:67], a[8:11], a[144:147], v[16:19]\n\t"
"ds_write_addtid_b32 v195 offset:17568\n\t"
"v_mfma_f32_16x16x32_bf16 v[64:67], a[12:15], a[160:163], v[64:67]\n\t"
"s_waitcnt vmcnt(31) lgkmcnt(7)\n\t"
"ds_write_addtid_b32 v202 offset:18592\n\t"
"v_mfma_f32_16x16x32_bf16 v[80:83], a[16:19], a[144:147], v[32:35]\n\t"
"ds_read_b128 a[144:147], v224 offset:0\n\t"
"v_mfma_f32_16x16x32_bf16 v[80:83], a[20:23], a[160:163], v[80:83]\n\t"
"buffer_load_dwordx4 a[140:143], off, s[92:95], s46 offset:3072 sc1\n\t"
"v_mfma_f32_16x16x32_bf16 v[84:87], a[16:19], a[148:151], v[36:39]\n\t"
"ds_read_b128 a[160:163], v224 offset:2048\n\t"
"v_mfma_f32_16x16x32_bf16 v[84:87], a[20:23], a[164:167], v[84:87]\n\t"
"ds_write_addtid_b32 v223 offset:23744\n\t"
"v_mfma_f32_16x16x32_bf16 v[68:71], a[8:11], a[148:151], v[20:23]\n\t"
"ds_write_addtid_b32 v201 offset:27936\n\t"
"v_mfma_f32_16x16x32_bf16 v[68:71], a[12:15], a[164:167], v[68:71]\n\t"
"buffer_load_dwordx4 a[132:135], off, s[92:95], s46 offset:1024 sc1\n\t"
"v_mfma_f32_16x16x32_bf16 v[52:55], a[0:3], a[148:151], v[4:7]\n\t"
"ds_read_b128 a[148:151], v224 offset:256\n\t"
"v_mfma_f32_16x16x32_bf16 v[52:55], a[4:7], a[164:167], v[52:55]\n\t"
"s_waitcnt lgkmcnt(7)\n\t"
"ds_write_addtid_b32 v221 offset:32064\n\t"
"v_mfma_f32_16x16x32_bf16 v[56:59], a[0:3], a[152:155], v[8:11]\n\t"
"ds_read_b128 a[164:167], v224 offset:2304\n\t"
"v_mfma_f32_16x16x32_bf16 v[56:59], a[4:7], a[168:171], v[56:59]\n\t"
"buffer_load_dwordx4 a[128:131], off, s[92:95], s46 offset:0 sc1\n\t"
"v_mfma_f32_16x16x32_bf16 v[72:75], a[8:11], a[152:155], v[24:27]\n\t"
"ds_write_addtid_b32 v211 offset:21696\n\t"
"v_mfma_f32_16x16x32_bf16 v[72:75], a[12:15], a[168:171], v[72:75]\n\t"
"ds_write_addtid_b32 v193 offset:25888\n\t"
"v_mfma_f32_16x16x32_bf16 v[88:91], a[16:19], a[152:155], v[40:43]\n\t"
"v_mfma_f32_16x16x32_bf16 v[88:91], a[20:23], a[168:171], v[88:91]\n\t"
"ds_write_b128 v254, a[248:251] offset:41472\n\t"
"v_mfma_f32_16x16x32_bf16 v[92:95], a[16:19], a[156:159], v[44:47]\n\t"
"v_mfma_f32_16x16x32_bf16 v[92:95], a[20:23], a[172:175], v[92:95]\n\t"
"buffer_load_dwordx4 a[120:123], off, s[96:99], s42 offset:2048 sc1\n\t"
"v_mfma_f32_16x16x32_bf16 v[76:79], a[8:11], a[156:159], v[28:31]\n\t"
"v_mfma_f32_16x16x32_bf16 v[76:79], a[12:15], a[172:175], v[76:79]\n\t"
"s_nop 0\n\t"
"ds_read_b128 a[168:171], v224 offset:2560\n\t"
"ds_write_addtid_b32 v209 offset:30016\n\t"
"v_mfma_f32_16x16x32_bf16 v[60:63], a[0:3], a[156:159], v[12:15]\n\t"
"ds_write_addtid_b32 v210 offset:20672\n\t"
"v_mfma_f32_16x16x32_bf16 v[60:63], a[4:7], a[172:175], v[60:63]\n\t"
"s_waitcnt vmcnt(33) lgkmcnt(6)\n\t"
"buffer_load_dwordx4 a[124:127], off, s[96:99], s42 offset:3072 sc1\n\t"
"v_mfma_f32_16x16x32_bf16 v[0:3], a[24:27], a[144:147], v[0:3]\n\t"
"ds_write_addtid_b32 v222 offset:22720\n\t"
"v_mfma_f32_16x16x32_bf16 v[0:3], a[28:31], a[160:163], v[0:3]\n\t"
"s_waitcnt vmcnt(32) lgkmcnt(5)\n\t"
"ds_write_addtid_b32 v192 offset:24864\n\t"
"ds_write_addtid_b32 v200 offset:26912\n\t"
"v_mfma_f32_16x16x32_bf16 v[16:19], a[32:35], a[144:147], v[16:19]\n\t"
"ds_write_addtid_b32 v220 offset:31040\n\t"
"v_mfma_f32_16x16x32_bf16 v[16:19], a[36:39], a[160:163], v[16:19]\n\t"
"s_waitcnt vmcnt(30) lgkmcnt(7)\n\t"
"buffer_load_dwordx4 a[16:19], off, s[92:95], s50 offset:0 sc1\n\t"
"v_mfma_f32_16x16x32_bf16 v[32:35], a[40:43], a[144:147], v[32:35]\n\t"
"ds_read_b128 a[156:159], v224 offset:768\n\t"
"v_mfma_f32_16x16x32_bf16 v[32:35], a[44:47], a[160:163], v[32:35]\n\t"
"ds_read_b128 a[172:175], v224 offset:2816\n\t"
"v_mfma_f32_16x16x32_bf16 v[36:39], a[40:43], a[148:151], v[36:39]\n\t"
"ds_read_b128 a[152:155], v224 offset:512\n\t"
"v_mfma_f32_16x16x32_bf16 v[36:39], a[44:47], a[164:167], v[36:39]\n\t"
"buffer_load_dwordx4 a[20:23], off, s[92:95], s50 offset:1024 sc1\n\t"
"v_mfma_f32_16x16x32_bf16 v[20:23], a[32:35], a[148:151], v[20:23]\n\t"
"v_mfma_f32_16x16x32_bf16 v[20:23], a[36:39], a[164:167], v[20:23]\n\t"
"v_mfma_f32_16x16x32_bf16 v[4:7], a[24:27], a[148:151], v[4:7]\n\t"
"buffer_load_dwordx4 a[12:15], off, s[96:99], s49 offset:3072 sc1\n\t"
"v_mfma_f32_16x16x32_bf16 v[4:7], a[28:31], a[164:167], v[4:7]\n\t"
"s_waitcnt vmcnt(27) lgkmcnt(4)\n\t"
"s_waitcnt lgkmcnt(0)\n\t"
"s_barrier\n\t"
"v_mfma_f32_16x16x32_bf16 v[8:11], a[24:27], a[152:155], v[8:11]\n\t"
"s_add_u32 m0, s26, 76800\n\t"
"ds_read_b128 a[144:147], v224 offset:17568\n\t"
"ds_read_b128 a[160:163], v224 offset:19616\n\t"
"v_mfma_f32_16x16x32_bf16 v[8:11], a[28:31], a[168:171], v[8:11]\n\t"
"ds_read_b128 a[164:167], v224 offset:19872\n\t"
"v_mfma_f32_16x16x32_bf16 v[24:27], a[32:35], a[152:155], v[24:27]\n\t"
"ds_read_b128 a[148:151], v224 offset:17824\n\t"
"v_mfma_f32_16x16x32_bf16 v[24:27], a[36:39], a[168:171], v[24:27]\n\t"
"buffer_load_dwordx4 v236, s[12:15], s22 offen offset:256 nt lds\n\t"
"v_mfma_f32_16x16x32_bf16 v[40:43], a[40:43], a[152:155], v[40:43]\n\t"
"s_add_u32 s24, s24, 3145728\n\t"
"ds_read_b64_tr_b16 a[176:177], v248 offset:0\n\t"
"ds_read_b64_tr_b16 a[178:179], v249 offset:0\n\t"
"v_mfma_f32_16x16x32_bf16 v[40:43], a[44:47], a[168:171], v[40:43]\n\t"
"s_add_u32 s20, s20, 3145728\n\t"
"ds_read_b64_tr_b16 a[180:181], v248 offset:512\n\t"
"ds_read_b64_tr_b16 a[182:183], v249 offset:512\n\t"
"v_mfma_f32_16x16x32_bf16 v[44:47], a[40:43], a[156:159], v[44:47]\n\t"
"ds_read_b64_tr_b16 a[184:185], v250 offset:2304\n\t"
"v_mfma_f32_16x16x32_bf16 v[44:47], a[44:47], a[172:175], v[44:47]\n\t"
"buffer_load_dwordx4 v237, s[12:15], s22 offen offset:1280 nt lds\n\t"
"v_mfma_f32_16x16x32_bf16 v[28:31], a[32:35], a[156:159], v[28:31]\n\t"
"ds_read_b128 a[152:155], v224 offset:18080\n\t"
"v_mfma_f32_16x16x32_bf16 v[28:31], a[36:39], a[172:175], v[28:31]\n\t"
"s_waitcnt lgkmcnt(2)\n\t"
"ds_read_b64_tr_b16 a[186:187], v251 offset:2304\n\t"
"v_mfma_f32_4x4x4_16b_bf16 v[192:195], a[210:211], a[176:177], 0\n\t"
"ds_read_b128 a[168:171], v224 offset:20128\n\t"
"v_mfma_f32_4x4x4_16b_bf16 v[196:199], a[210:211], a[178:179], 0\n\t"
"ds_read_b64_tr_b16 a[188:189], v250 offset:2816\n\t"
"v_mfma_f32_4x4x4_16b_bf16 v[200:203], a[210:211], a[180:181], 0\n\t"
"ds_read_b64_tr_b16 a[190:191], v251 offset:2816\n\t"
"v_mfma_f32_4x4x4_16b_bf16 v[204:207], a[210:211], a[182:183], 0\n\t"
"buffer_load_dwordx4 v238, s[12:15], s22 offen offset:2304 nt lds\n\t"
"v_mfma_f32_16x16x32_bf16 v[12:15], a[24:27], a[156:159], v[12:15]\n\t"
"s_add_u32 s27, s27, 3145728\n\t"
"ds_read_b64_tr_b16 a[192:193], v250 offset:0\n\t"
"ds_read_b64_tr_b16 a[194:195], v251 offset:0\n\t"
"v_mfma_f32_16x16x32_bf16 v[12:15], a[28:31], a[172:175], v[12:15]\n\t"
"s_waitcnt vmcnt(28)\n\t"
"buffer_load_dwordx4 a[8:11], off, s[96:99], s49 offset:2048 sc1\n\t"
"v_mfma_f32_16x16x32_bf16 v[144:147], a[48:51], a[144:147], v[96:99]\n\t"
"ds_read_b128 a[156:159], v224 offset:18336\n\t"
"v_cvt_pk_bf16_f32 v194, v194, v198\n\t"
"v_mfma_f32_16x16x32_bf16 v[144:147], a[52:55], a[160:163], v[144:147]\n\t"
"s_waitcnt vmcnt(27) lgkmcnt(8)\n\t"
"v_cvt_pk_bf16_f32 v197, v193, v197\n\t"
"v_cvt_pk_bf16_f32 v195, v195, v199\n\t"
"v_mfma_f32_16x16x32_bf16 v[160:163], a[56:59], a[144:147], v[112:115]\n\t"
"ds_read_b128 a[172:175], v224 offset:20384\n\t"
"v_cvt_pk_bf16_f32 v196, v192, v196\n\t"
"v_mfma_f32_16x16x32_bf16 v[160:163], a[60:63], a[160:163], v[160:163]\n\t"
"s_waitcnt vmcnt(25) lgkmcnt(7)\n\t"
"buffer_load_dwordx4 v239, s[12:15], s22 offen offset:3328 nt lds\n\t"
"v_mfma_f32_16x16x32_bf16 v[176:179], a[64:67], a[144:147], v[128:131]\n\t"
"ds_read_b128 a[144:147], v224 offset:16544\n\t"
"v_cvt_pk_bf16_f32 v199, v201, v205\n\t"
"v_mfma_f32_16x16x32_bf16 v[176:179], a[68:71], a[160:163], v[176:179]\n\t"
"s_waitcnt lgkmcnt(5)\n\t"
"v_cvt_pk_bf16_f32 v192, v202, v206\n\t"
"v_cvt_pk_bf16_f32 v198, v200, v204\n\t"
"v_mfma_f32_4x4x4_16b_bf16 v[208:211], a[210:211], a[184:185], 0\n\t"
"ds_read_b64_tr_b16 a[196:197], v250 offset:512\n\t"
"v_mfma_f32_4x4x4_16b_bf16 v[212:215], a[210:211], a[186:187], 0\n\t"
"ds_read_b64_tr_b16 a[198:199], v251 offset:512\n\t"
"v_mfma_f32_4x4x4_16b_bf16 v[216:219], a[210:211], a[188:189], 0\n\t"
"ds_read_b64_tr_b16 a[200:201], v248 offset:2304\n\t"
"v_mfma_f32_4x4x4_16b_bf16 v[220:223], a[210:211], a[190:191], 0\n\t"
"s_mov_b64 exec, s[36:37]\n\t"
"buffer_load_dwordx4 v240, s[12:15], s22 offen offset:3840 nt lds\n\t"
"s_mov_b64 exec, -1\n\t"
"v_mfma_f32_16x16x32_bf16 v[180:183], a[64:67], a[148:151], v[132:135]\n\t"
"s_lshl_b32 m0, s73, 8\n\t"
"ds_read_b128 a[160:163], v224 offset:18592\n\t"
"v_cvt_pk_bf16_f32 v193, v203, v207\n\t"
"v_mfma_f32_16x16x32_bf16 v[180:183], a[68:71], a[164:167], v[180:183]\n\t"
"s_nop 0\n\t"
"ds_write_addtid_b32 v198 offset:8320\n\t"
"ds_write_addtid_b32 v197 offset:1024\n\t"
"v_mfma_f32_16x16x32_bf16 v[164:167], a[56:59], a[148:151], v[116:119]\n\t"
"v_cvt_pk_bf16_f32 v213, v209, v213\n\t"
"v_cvt_pk_bf16_f32 v211, v211, v215\n\t"
"v_mfma_f32_16x16x32_bf16 v[164:167], a[60:63], a[164:167], v[164:167]\n\t"
"v_cvt_pk_bf16_f32 v212, v208, v212\n\t"
"v_cvt_pk_bf16_f32 v210, v210, v214\n\t"
"v_mfma_f32_16x16x32_bf16 v[148:151], a[48:51], a[148:151], v[100:103]\n\t"
"ds_write_addtid_b32 v199 offset:9344\n\t"
"v_cvt_pk_bf16_f32 v209, v219, v223\n\t"
"v_mfma_f32_16x16x32_bf16 v[148:151], a[52:55], a[164:167], v[148:151]\n\t"
"s_waitcnt lgkmcnt(5)\n\t"
"ds_read_b128 a[148:151], v224 offset:16800\n\t"
"v_cvt_pk_bf16_f32 v214, v216, v220\n\t"
"v_mfma_f32_4x4x4_16b_bf16 v[200:203], a[210:211], a[192:193], 0\n\t"
"v_cvt_pk_bf16_f32 v208, v218, v222\n\t"
"v_mfma_f32_4x4x4_16b_bf16 v[204:207], a[210:211], a[194:195], 0\n\t"
"ds_write_addtid_b32 v196 offset:0\n\t"
"v_mfma_f32_4x4x4_16b_bf16 v[196:199], a[210:211], a[196:197], 0\n\t"
"v_cvt_pk_bf16_f32 v215, v217, v221\n\t"
"v_mfma_f32_4x4x4_16b_bf16 v[216:219], a[210:211], a[198:199], 0\n\t"
"ds_read_b128 a[164:167], v224 offset:18848\n\t"
"v_mfma_f32_16x16x32_bf16 v[152:155], a[48:51], a[152:155], v[104:107]\n\t"
"ds_read_b64_tr_b16 a[202:203], v249 offset:2304\n\t"
"v_mfma_f32_16x16x32_bf16 v[152:155], a[52:55], a[168:171], v[152:155]\n\t"
"ds_read_b64_tr_b16 a[204:205], v248 offset:2816\n\t"
"v_mfma_f32_16x16x32_bf16 v[168:171], a[56:59], a[152:155], v[120:123]\n\t"
"v_cvt_pk_bf16_f32 v202, v202, v206\n\t"
"v_cvt_pk_bf16_f32 v203, v203, v207\n\t"
"v_mfma_f32_16x16x32_bf16 v[168:171], a[60:63], a[168:171], v[168:171]\n\t"
"ds_read_b64_tr_b16 a[206:207], v249 offset:2816\n\t"
"v_cvt_pk_bf16_f32 v205, v201, v205\n\t"
"v_mfma_f32_16x16x32_bf16 v[184:187], a[64:67], a[152:155], v[136:139]\n\t"
"ds_read_b128 a[152:155], v224 offset:17056\n\t"
"v_cvt_pk_bf16_f32 v204, v200, v204\n\t"
"v_mfma_f32_16x16x32_bf16 v[184:187], a[68:71], a[168:171], v[184:187]\n\t"
"ds_write_addtid_b32 v213 offset:5152\n\t"
"v_cvt_pk_bf16_f32 v207, v197, v217\n\t"
"v_mfma_f32_16x16x32_bf16 v[188:191], a[64:67], a[156:159], v[140:143]\n\t"
"ds_write_addtid_b32 v215 offset:13472\n\t"
"v_cvt_pk_bf16_f32 v201, v199, v219\n\t"
"v_mfma_f32_16x16x32_bf16 v[188:191], a[68:71], a[172:175], v[188:191]\n\t"
"s_waitcnt lgkmcnt(3)\n\t"
"ds_write_addtid_b32 v212 offset:4128\n\t"
"v_cvt_pk_bf16_f32 v200, v198, v218\n\t"
"v_mfma_f32_4x4x4_16b_bf16 v[220:223], a[210:211], a[200:201], 0\n\t"
"ds_write_addtid_b32 v214 offset:12448\n\t"
"v_mfma_f32_4x4x4_16b_bf16 v[212:215], a[210:211], a[202:203], 0\n\t"
"v_cvt_pk_bf16_f32 v206, v196, v216\n\t"
"v_mfma_f32_4x4x4_16b_bf16 v[196:199], a[210:211], a[204:205], 0\n\t"
"ds_read_b128 a[168:171], v224 offset:19104\n\t"
"v_mfma_f32_4x4x4_16b_bf16 v[216:219], a[210:211], a[206:207], 0\n\t"
"ds_write_addtid_b32 v206 offset:10368\n\t"
"v_mfma_f32_16x16x32_bf16 v[172:175], a[56:59], a[156:159], v[124:127]\n\t"
"s_nop 0\n\t"
"ds_write_addtid_b32 v205 offset:3072\n\t"
"ds_write_addtid_b32 v207 offset:11392\n\t"
"v_mfma_f32_16x16x32_bf16 v[172:175], a[60:63], a[172:175], v[172:175]\n\t"
"ds_write_addtid_b32 v204 offset:2048\n\t"
"v_mfma_f32_16x16x32_bf16 v[156:159], a[48:51], a[156:159], v[108:111]\n\t"
"v_cvt_pk_bf16_f32 v212, v220, v212\n\t"
"v_cvt_pk_bf16_f32 v222, v222, v214\n\t"
"v_mfma_f32_16x16x32_bf16 v[156:159], a[52:55], a[172:175], v[156:159]\n\t"
"s_waitcnt vmcnt(22) lgkmcnt(8)\n\t"
"v_cvt_pk_bf16_f32 v213, v221, v213\n\t"
"v_cvt_pk_bf16_f32 v223, v223, v215\n\t"
"v_mfma_f32_16x16x32_bf16 v[96:99], a[72:75], a[144:147], v[96:99]\n\t"
"ds_write_addtid_b32 v212 offset:6176\n\t"
"v_cvt_pk_bf16_f32 v214, v196, v216\n\t"
"v_mfma_f32_16x16x32_bf16 v[96:99], a[76:79], a[160:163], v[96:99]\n\t"
"ds_write_addtid_b32 v213 offset:7200\n\t"
"v_cvt_pk_bf16_f32 v215, v197, v217\n\t"
"v_mfma_f32_16x16x32_bf16 v[112:115], a[80:83], a[144:147], v[112:115]\n\t"
"s_add_u32 s25, s25, 3145728\n\t"
"ds_write_addtid_b32 v214 offset:14496\n\t"
"ds_write_addtid_b32 v215 offset:15520\n\t"
"v_mfma_f32_16x16x32_bf16 v[112:115], a[84:87], a[160:163], v[112:115]\n\t"
"s_waitcnt vmcnt(21) lgkmcnt(9)\n\t"
"v_mfma_f32_16x16x32_bf16 v[128:131], a[88:91], a[144:147], v[128:131]\n\t"
"v_mfma_f32_16x16x32_bf16 v[128:131], a[92:95], a[160:163], v[128:131]\n\t"
"ds_read_b128 a[156:159], v224 offset:17312\n\t"
"v_mfma_f32_16x16x32_bf16 v[132:135], a[88:91], a[148:151], v[132:135]\n\t"
"ds_read_b128 a[172:175], v224 offset:19360\n\t"
"v_cvt_pk_bf16_f32 v221, v199, v219\n\t"
"v_mfma_f32_16x16x32_bf16 v[132:135], a[92:95], a[164:167], v[132:135]\n\t"
"v_mfma_f32_16x16x32_bf16 v[116:119], a[80:83], a[148:151], v[116:119]\n\t"
"v_cvt_pk_bf16_f32 v220, v198, v218\n\t"
"v_mfma_f32_16x16x32_bf16 v[116:119], a[84:87], a[164:167], v[116:119]\n\t"
"buffer_load_dwordx4 a[0:3], off, s[96:99], s49 offset:0 sc1\n\t"
"v_mfma_f32_16x16x32_bf16 v[100:103], a[72:75], a[148:151], v[100:103]\n\t"
"s_waitcnt lgkmcnt(0)\n\t"
"v_mfma_f32_16x16x32_bf16 v[100:103], a[76:79], a[164:167], v[100:103]\n\t"
"s_barrier\n\t"
"v_mfma_f32_16x16x32_bf16 v[104:107], a[72:75], a[152:155], v[104:107]\n\t"
"s_nop 0\n\t"
"ds_read_b128 a[144:147], v224 offset:1024\n\t"
"ds_read_b128 a[160:163], v224 offset:3072\n\t"
"v_mfma_f32_16x16x32_bf16 v[104:107], a[76:79], a[168:171], v[104:107]\n\t"
"buffer_load_dwordx4 a[4:7], off, s[96:99], s49 offset:1024 sc1\n\t"
"v_mfma_f32_16x16x32_bf16 v[120:123], a[80:83], a[152:155], v[120:123]\n\t"
"ds_read_b128 a[148:151], v224 offset:1280\n\t"
"v_mfma_f32_16x16x32_bf16 v[120:123], a[84:87], a[168:171], v[120:123]\n\t"
"ds_read_b128 a[164:167], v224 offset:3328\n\t"
"v_mfma_f32_16x16x32_bf16 v[136:139], a[88:91], a[152:155], v[136:139]\n\t"
"ds_read_b128 a[248:251], v255 offset:59904\n\t"
"v_mfma_f32_16x16x32_bf16 v[136:139], a[92:95], a[168:171], v[136:139]\n\t"
"buffer_load_dwordx4 a[40:43], off, s[92:95], s49 offset:2048 sc1\n\t"
"v_mfma_f32_16x16x32_bf16 v[140:143], a[88:91], a[156:159], v[140:143]\n\t"
"v_mfma_f32_16x16x32_bf16 v[140:143], a[92:95], a[172:175], v[140:143]\n\t"
"ds_read_b128 a[152:155], v224 offset:1536\n\t"
"v_mfma_f32_16x16x32_bf16 v[124:127], a[80:83], a[156:159], v[124:127]\n\t"
"ds_read_b128 a[168:171], v224 offset:3584\n\t"
"v_mfma_f32_16x16x32_bf16 v[124:127], a[84:87], a[172:175], v[124:127]\n\t"
"buffer_load_dwordx4 a[44:47], off, s[92:95], s49 offset:3072 sc1\n\t"
"v_mfma_f32_16x16x32_bf16 v[108:111], a[72:75], a[156:159], v[108:111]\n\t"
"ds_write_addtid_b32 v223 offset:23744\n\t"
"v_mfma_f32_16x16x32_bf16 v[108:111], a[76:79], a[172:175], v[108:111]\n\t"
"s_waitcnt vmcnt(19) lgkmcnt(6)\n\t"
"ds_read_b128 a[156:159], v224 offset:1792\n\t"
"v_mfma_f32_16x16x32_bf16 v[48:51], a[96:99], a[144:147], v[48:51]\n\t"
"ds_read_b128 a[172:175], v224 offset:3840\n\t"
"v_mfma_f32_16x16x32_bf16 v[48:51], a[100:103], a[160:163], v[48:51]\n\t"
"buffer_load_dwordx4 a[36:39], off, s[92:95], s49 offset:1024 sc1\n\t"
"v_mfma_f32_16x16x32_bf16 v[64:67], a[104:107], a[144:147], v[64:67]\n\t"
"ds_write_addtid_b32 v192 offset:24864\n\t"
"v_mfma_f32_16x16x32_bf16 v[64:67], a[108:111], a[160:163], v[64:67]\n\t"
"s_nop 0\n\t"
"ds_write_addtid_b32 v200 offset:26912\n\t"
"v_mfma_f32_16x16x32_bf16 v[80:83], a[112:115], a[144:147], v[80:83]\n\t"
"s_nop 0\n\t"
"ds_read_b128 a[144:147], v224 offset:0\n\t"
"v_mfma_f32_16x16x32_bf16 v[80:83], a[116:119], a[160:163], v[80:83]\n\t"
"s_waitcnt lgkmcnt(7)\n\t"
"buffer_load_dwordx4 a[32:35], off, s[92:95], s49 offset:0 sc1\n\t"
"v_mfma_f32_16x16x32_bf16 v[84:87], a[112:115], a[148:151], v[84:87]\n\t"
"s_nop 0\n\t"
"ds_read_b128 a[160:163], v224 offset:2048\n\t"
"v_mfma_f32_16x16x32_bf16 v[84:87], a[116:119], a[164:167], v[84:87]\n\t"
"s_nop 0\n\t"
"ds_write_addtid_b32 v221 offset:32064\n\t"
"v_mfma_f32_16x16x32_bf16 v[68:71], a[104:107], a[148:151], v[68:71]\n\t"
"s_nop 0\n\t"
"ds_write_addtid_b32 v209 offset:30016\n\t"
"v_xor_b32 v226, 64, v226\n\t"
"v_mfma_f32_16x16x32_bf16 v[68:71], a[108:111], a[164:167], v[68:71]\n\t"
"buffer_load_dwordx4 a[24:27], off, s[96:99], s48 offset:2048 sc1\n\t"
"v_mfma_f32_16x16x32_bf16 v[52:55], a[96:99], a[148:151], v[52:55]\n\t"
"s_nop 0\n\t"
"ds_read_b128 a[148:151], v224 offset:256\n\t"
"v_mfma_f32_16x16x32_bf16 v[52:55], a[100:103], a[164:167], v[52:55]\n\t"
"s_waitcnt lgkmcnt(7)\n\t"
"ds_write_addtid_b32 v193 offset:25888\n\t"
"v_mfma_f32_16x16x32_bf16 v[56:59], a[96:99], a[152:155], v[56:59]\n\t"
"s_nop 0\n\t"
"ds_read_b128 a[164:167], v224 offset:2304\n\t"
"v_mfma_f32_16x16x32_bf16 v[56:59], a[100:103], a[168:171], v[56:59]\n\t"
"buffer_load_dwordx4 a[28:31], off, s[96:99], s48 offset:3072 sc1\n\t"
"v_mfma_f32_16x16x32_bf16 v[72:75], a[104:107], a[152:155], v[72:75]\n\t"
"s_nop 0\n\t"
"ds_write_addtid_b32 v210 offset:20672\n\t"
"v_xor_b32 v227, 64, v227\n\t"
"v_mfma_f32_16x16x32_bf16 v[72:75], a[108:111], a[168:171], v[72:75]\n\t"
"s_nop 0\n\t"
"ds_write_addtid_b32 v195 offset:17568\n\t"
"v_mfma_f32_16x16x32_bf16 v[88:91], a[112:115], a[152:155], v[88:91]\n\t"
"s_nop 0\n\t"
"ds_read_b128 a[152:155], v224 offset:512\n\t"
"v_mfma_f32_16x16x32_bf16 v[88:91], a[116:119], a[168:171], v[88:91]\n\t"
"buffer_load_dwordx4 a[64:67], off, s[92:95], s77 offset:0 sc1\n\t"
"v_mfma_f32_16x16x32_bf16 v[92:95], a[112:115], a[156:159], v[92:95]\n\t"
"s_nop 0\n\t"
"ds_read_b128 a[168:171], v224 offset:2560\n\t"
"v_mfma_f32_16x16x32_bf16 v[92:95], a[116:119], a[172:175], v[92:95]\n\t"
"s_nop 0\n\t"
"ds_write_addtid_b32 v202 offset:18592\n\t"
"v_mfma_f32_16x16x32_bf16 v[76:79], a[104:107], a[156:159], v[76:79]\n\t"
"s_nop 0\n\t"
"ds_write_addtid_b32 v208 offset:28992\n\t"
"v_mfma_f32_16x16x32_bf16 v[76:79], a[108:111], a[172:175], v[76:79]\n\t"
"buffer_load_dwordx4 a[68:71], off, s[92:95], s77 offset:1024 sc1\n\t"
"v_mfma_f32_16x16x32_bf16 v[60:63], a[96:99], a[156:159], v[60:63]\n\t"
"s_nop 0\n\t"
"ds_write_addtid_b32 v211 offset:21696\n\t"
"v_mfma_f32_16x16x32_bf16 v[60:63], a[100:103], a[172:175], v[60:63]\n\t"
"s_waitcnt vmcnt(19) lgkmcnt(7)\n\t"
"ds_write_addtid_b32 v222 offset:22720\n\t"
"v_mfma_f32_16x16x32_bf16 v[0:3], a[120:123], a[144:147], v[0:3]\n\t"
"s_nop 0\n\t"
"ds_write_addtid_b32 v194 offset:16544\n\t"
"v_mfma_f32_16x16x32_bf16 v[0:3], a[124:127], a[160:163], v[0:3]\n\t"
"s_nop 0\n\t"
"ds_write_addtid_b32 v220 offset:31040\n\t"
"v_mfma_f32_16x16x32_bf16 v[16:19], a[128:131], a[144:147], v[16:19]\n\t"
"s_add_u32 s22, s22, 3145728\n\t"
"ds_write_addtid_b32 v203 offset:19616\n\t"
"ds_write_addtid_b32 v201 offset:27936\n\t"
"v_mfma_f32_16x16x32_bf16 v[16:19], a[132:135], a[160:163], v[16:19]\n\t"
"buffer_load_dwordx4 a[60:63], off, s[96:99], s76 offset:3072 sc1\n\t"
"v_mfma_f32_16x16x32_bf16 v[32:35], a[136:139], a[144:147], v[32:35]\n\t"
"s_nop 0\n\t"
"ds_read_b128 a[172:175], v224 offset:2816\n\t"
"v_mfma_f32_16x16x32_bf16 v[32:35], a[140:143], a[160:163], v[32:35]\n\t"
"buffer_load_dwordx4 a[56:59], off, s[96:99], s76 offset:2048 sc1\n\t"
"v_mfma_f32_16x16x32_bf16 v[36:39], a[136:139], a[148:151], v[36:39]\n\t"
"s_nop 0\n\t"
"ds_read_b128 a[156:159], v224 offset:768\n\t"
"v_mfma_f32_16x16x32_bf16 v[36:39], a[140:143], a[164:167], v[36:39]\n\t"
"s_nop 0\n\t"
"v_mfma_f32_16x16x32_bf16 v[20:23], a[128:131], a[148:151], v[20:23]\n\t"
"s_cmp_eq_u32 s21, 1\n\t"
"s_cselect_b32 s22, 2.0, s22\n\t"
"v_mfma_f32_16x16x32_bf16 v[20:23], a[132:135], a[164:167], v[20:23]\n\t"
"buffer_load_dwordx4 a[48:51], off, s[96:99], s76 offset:0 sc1\n\t"
"v_mfma_f32_16x16x32_bf16 v[4:7], a[120:123], a[148:151], v[4:7]\n\t"
"s_nop 0\n\t"
"v_mfma_f32_16x16x32_bf16 v[4:7], a[124:127], a[164:167], v[4:7]\n\t"
"s_waitcnt vmcnt(13) lgkmcnt(4)\n\t"
"s_waitcnt lgkmcnt(0)\n\t"
"s_barrier\n\t"
"v_mfma_f32_16x16x32_bf16 v[8:11], a[120:123], a[152:155], v[8:11]\n\t"
"s_nop 0\n\t"
"ds_read_b128 a[144:147], v224 offset:17568\n\t"
"ds_read_b128 a[160:163], v224 offset:19616\n\t"
"v_mfma_f32_16x16x32_bf16 v[8:11], a[124:127], a[168:171], v[8:11]\n\t"
"buffer_load_dwordx4 a[52:55], off, s[96:99], s76 offset:1024 sc1\n\t"
"v_mfma_f32_16x16x32_bf16 v[24:27], a[128:131], a[152:155], v[24:27]\n\t"
"s_nop 0\n\t"
"ds_read_b128 a[148:151], v224 offset:17824\n\t"
"ds_read_b128 a[164:167], v224 offset:19872\n\t"
"v_mfma_f32_16x16x32_bf16 v[24:27], a[132:135], a[168:171], v[24:27]\n\t"
"buffer_load_dwordx4 a[88:91], off, s[92:95], s76 offset:2048 sc1\n\t"
"v_mfma_f32_16x16x32_bf16 v[40:43], a[136:139], a[152:155], v[40:43]\n\t"
"s_nop 0\n\t"
"ds_read_b64_tr_b16 a[176:177], v248 offset:41472\n\t"
"ds_read_b64_tr_b16 a[178:179], v249 offset:41472\n\t"
"v_mfma_f32_16x16x32_bf16 v[40:43], a[140:143], a[168:171], v[40:43]\n\t"
"s_nop 0\n\t"
"ds_read_b64_tr_b16 a[180:181], v248 offset:41984\n\t"
"ds_read_b64_tr_b16 a[182:183], v249 offset:41984\n\t"
"v_mfma_f32_16x16x32_bf16 v[44:47], a[136:139], a[156:159], v[44:47]\n\t"
"s_nop 0\n\t"
"ds_read_b64_tr_b16 a[184:185], v250 offset:43776\n\t"
"ds_read_b64_tr_b16 a[186:187], v251 offset:43776\n\t"
"v_mfma_f32_16x16x32_bf16 v[44:47], a[140:143], a[172:175], v[44:47]\n\t"
"buffer_load_dwordx4 a[92:95], off, s[92:95], s76 offset:3072 sc1\n\t"
"v_mfma_f32_16x16x32_bf16 v[28:31], a[128:131], a[156:159], v[28:31]\n\t"
"s_nop 0\n\t"
"ds_read_b64_tr_b16 a[188:189], v250 offset:44288\n\t"
"ds_read_b64_tr_b16 a[190:191], v251 offset:44288\n\t"
"v_mfma_f32_16x16x32_bf16 v[28:31], a[132:135], a[172:175], v[28:31]\n\t"
"s_waitcnt lgkmcnt(4)\n\t"
"buffer_load_dwordx4 a[84:87], off, s[92:95], s76 offset:1024 sc1\n\t"
"v_mfma_f32_4x4x4_16b_bf16 v[192:195], a[210:211], a[176:177], 0\n\t"
"ds_read_b128 a[152:155], v224 offset:18080\n\t"
"v_mfma_f32_4x4x4_16b_bf16 v[196:199], a[210:211], a[178:179], 0\n\t"
"ds_read_b128 a[168:171], v224 offset:20128\n\t"
"v_mfma_f32_4x4x4_16b_bf16 v[200:203], a[210:211], a[180:181], 0\n\t"
"ds_read_b64_tr_b16 a[192:193], v250 offset:41472\n\t"
"v_mfma_f32_4x4x4_16b_bf16 v[204:207], a[210:211], a[182:183], 0\n\t"
"buffer_load_dwordx4 a[80:83], off, s[92:95], s76 offset:0 sc1\n\t"
"v_mfma_f32_16x16x32_bf16 v[12:15], a[120:123], a[156:159], v[12:15]\n\t"
"s_nop 0\n\t"
"ds_read_b64_tr_b16 a[194:195], v251 offset:41472\n\t"
"ds_read_b64_tr_b16 a[196:197], v250 offset:41984\n\t"
"v_mfma_f32_16x16x32_bf16 v[12:15], a[124:127], a[172:175], v[12:15]\n\t"
"s_waitcnt vmcnt(16)\n\t"
"ds_read_b64_tr_b16 a[198:199], v251 offset:41984\n\t"
"ds_read_b64_tr_b16 a[200:201], v248 offset:43776\n\t"
"v_mfma_f32_16x16x32_bf16 v[144:147], a[0:3], a[144:147], v[144:147]\n\t"
"s_nop 0\n\t"
"v_cvt_pk_bf16_f32 v196, v192, v196\n\t"
"v_cvt_pk_bf16_f32 v194, v194, v198\n\t"
"v_mfma_f32_16x16x32_bf16 v[144:147], a[4:7], a[160:163], v[144:147]\n\t"
"buffer_load_dwordx4 a[72:75], off, s[96:99], s75 offset:2048 sc1\n\t"
"v_mfma_f32_16x16x32_bf16 v[160:163], a[8:11], a[144:147], v[160:163]\n\t"
"s_nop 0\n\t"
"ds_read_b128 a[156:159], v224 offset:18336\n\t"
"v_cvt_pk_bf16_f32 v197, v193, v197\n\t"
"v_mfma_f32_16x16x32_bf16 v[160:163], a[12:15], a[160:163], v[160:163]\n\t"
"s_nop 0\n\t"
"ds_read_b128 a[172:175], v224 offset:20384\n\t"
"v_cvt_pk_bf16_f32 v195, v195, v199\n\t"
"v_mfma_f32_16x16x32_bf16 v[176:179], a[16:19], a[144:147], v[176:179]\n\t"
"s_nop 0\n\t"
"v_cvt_pk_bf16_f32 v198, v200, v204\n\t"
"v_cvt_pk_bf16_f32 v199, v201, v205\n\t"
"v_mfma_f32_16x16x32_bf16 v[176:179], a[20:23], a[160:163], v[176:179]\n\t"
"s_waitcnt lgkmcnt(7)\n\t"
"buffer_load_dwordx4 a[76:79], off, s[96:99], s75 offset:3072 sc1\n\t"
"v_mfma_f32_4x4x4_16b_bf16 v[208:211], a[210:211], a[184:185], 0\n\t"
"ds_read_b128 a[144:147], v224 offset:16544\n\t"
"v_mfma_f32_4x4x4_16b_bf16 v[212:215], a[210:211], a[186:187], 0\n\t"
"ds_write_addtid_b32 v198 offset:8320\n\t"
"v_mfma_f32_4x4x4_16b_bf16 v[216:219], a[210:211], a[188:189], 0\n\t"
"ds_write_addtid_b32 v197 offset:1024\n\t"
"v_mfma_f32_4x4x4_16b_bf16 v[220:223], a[210:211], a[190:191], 0\n\t"
"ds_write_addtid_b32 v196 offset:0\n\t"
"v_cvt_pk_bf16_f32 v193, v203, v207\n\t"
"v_mfma_f32_16x16x32_bf16 v[180:183], a[16:19], a[148:151], v[180:183]\n\t"
"s_nop 0\n\t"
"ds_read_b128 a[160:163], v224 offset:18592\n\t"
"v_cvt_pk_bf16_f32 v192, v202, v206\n\t"
"v_mfma_f32_16x16x32_bf16 v[180:183], a[20:23], a[164:167], v[180:183]\n\t"
"Lpad3_%=:\n\t"
"s_cbranch_execz Lpad7_%=\n\t"
"buffer_load_dwordx4 a[112:115], off, s[92:95], s80 offset:0 sc1\n\t"
"v_mfma_f32_16x16x32_bf16 v[164:167], a[8:11], a[148:151], v[164:167]\n\t"
"ds_write_addtid_b32 v199 offset:9344\n\t"
"v_cvt_pk_bf16_f32 v210, v210, v214\n\t"
"v_mfma_f32_16x16x32_bf16 v[164:167], a[12:15], a[164:167], v[164:167]\n\t"
"v_cvt_pk_bf16_f32 v212, v208, v212\n\t"
"v_cvt_pk_bf16_f32 v213, v209, v213\n\t"
"v_mfma_f32_16x16x32_bf16 v[148:151], a[0:3], a[148:151], v[148:151]\n\t"
"ds_read_b128 a[148:151], v224 offset:16800\n\t"
"v_cvt_pk_bf16_f32 v211, v211, v215\n\t"
"v_mfma_f32_16x16x32_bf16 v[148:151], a[4:7], a[164:167], v[148:151]\n\t"
"s_waitcnt lgkmcnt(7)\n\t"
"v_cvt_pk_bf16_f32 v214, v216, v220\n\t"
"v_cvt_pk_bf16_f32 v209, v219, v223\n\t"
"v_cvt_pk_bf16_f32 v215, v217, v221\n\t"
"v_mfma_f32_4x4x4_16b_bf16 v[200:203], a[210:211], a[192:193], 0\n\t"
"ds_read_b128 a[164:167], v224 offset:18848\n\t"
"v_mfma_f32_4x4x4_16b_bf16 v[204:207], a[210:211], a[194:195], 0\n\t"
"v_cvt_pk_bf16_f32 v208, v218, v222\n\t"
"v_mfma_f32_4x4x4_16b_bf16 v[196:199], a[210:211], a[196:197], 0\n\t"
"ds_read_b64_tr_b16 a[202:203], v249 offset:43776\n\t"
"v_mfma_f32_4x4x4_16b_bf16 v[216:219], a[210:211], a[198:199], 0\n\t"
"buffer_load_dwordx4 a[116:119], off, s[92:95], s80 offset:1024 sc1\n\t"
"v_mfma_f32_16x16x32_bf16 v[152:155], a[0:3], a[152:155], v[152:155]\n\t"
"s_nop 0\n\t"
"ds_write_addtid_b32 v214 offset:12448\n\t"
"ds_read_b64_tr_b16 a[204:205], v248 offset:44288\n\t"
"v_mfma_f32_16x16x32_bf16 v[152:155], a[4:7], a[168:171], v[152:155]\n\t"
"buffer_load_dwordx4 a[108:111], off, s[96:99], s79 offset:3072 sc1\n\t"
"v_mfma_f32_16x16x32_bf16 v[168:171], a[8:11], a[152:155], v[168:171]\n\t"
"ds_read_b64_tr_b16 a[206:207], v249 offset:44288\n\t"
"v_cvt_pk_bf16_f32 v205, v201, v205\n\t"
"v_mfma_f32_16x16x32_bf16 v[168:171], a[12:15], a[168:171], v[168:171]\n\t"
"v_cvt_pk_bf16_f32 v204, v200, v204\n\t"
"v_cvt_pk_bf16_f32 v203, v203, v207\n\t"
"v_mfma_f32_16x16x32_bf16 v[184:187], a[16:19], a[152:155], v[184:187]\n\t"
"ds_read_b128 a[152:155], v224 offset:17056\n\t"
"v_cvt_pk_bf16_f32 v201, v199, v219\n\t"
"v_mfma_f32_16x16x32_bf16 v[184:187], a[20:23], a[168:171], v[184:187]\n\t"
"buffer_load_dwordx4 a[104:107], off, s[96:99], s79 offset:2048 sc1\n\t"
"v_cvt_pk_bf16_f32 v207, v197, v217\n\t"
"v_mfma_f32_16x16x32_bf16 v[188:191], a[16:19], a[156:159], v[188:191]\n\t"
"ds_write_addtid_b32 v215 offset:13472\n\t"
"v_cvt_pk_bf16_f32 v202, v202, v206\n\t"
"v_mfma_f32_16x16x32_bf16 v[188:191], a[20:23], a[172:175], v[188:191]\n\t"
"s_waitcnt lgkmcnt(2)\n\t"
"ds_write_addtid_b32 v213 offset:5152\n\t"
"v_cvt_pk_bf16_f32 v206, v196, v216\n\t"
"v_mfma_f32_4x4x4_16b_bf16 v[220:223], a[210:211], a[200:201], 0\n\t"
"ds_write_addtid_b32 v212 offset:4128\n\t"
"v_mfma_f32_4x4x4_16b_bf16 v[212:215], a[210:211], a[202:203], 0\n\t"
"v_cvt_pk_bf16_f32 v200, v198, v218\n\t"
"v_mfma_f32_4x4x4_16b_bf16 v[196:199], a[210:211], a[204:205], 0\n\t"
"ds_read_b128 a[168:171], v224 offset:19104\n\t"
"v_mfma_f32_4x4x4_16b_bf16 v[216:219], a[210:211], a[206:207], 0\n\t"
"buffer_load_dwordx4 a[96:99], off, s[96:99], s79 offset:0 sc1\n\t"
"v_mfma_f32_16x16x32_bf16 v[172:175], a[8:11], a[156:159], v[172:175]\n\t"
"s_nop 0\n\t"
"ds_write_addtid_b32 v204 offset:2048\n\t"
"ds_write_addtid_b32 v207 offset:11392\n\t"
"v_mfma_f32_16x16x32_bf16 v[172:175], a[12:15], a[172:175], v[172:175]\n\t"
"s_nop 0\n\t"
"ds_write_addtid_b32 v206 offset:10368\n\t"
"ds_write_addtid_b32 v205 offset:3072\n\t"
"v_mfma_f32_16x16x32_bf16 v[156:159], a[0:3], a[156:159], v[156:159]\n\t"
"v_cvt_pk_bf16_f32 v222, v222, v214\n\t"
"v_cvt_pk_bf16_f32 v213, v221, v213\n\t"
"v_mfma_f32_16x16x32_bf16 v[156:159], a[4:7], a[172:175], v[156:159]\n\t"
"s_waitcnt vmcnt(17) lgkmcnt(8)\n\t"
"v_cvt_pk_bf16_f32 v212, v220, v212\n\t"
"v_cvt_pk_bf16_f32 v223, v223, v215\n\t"
"v_mfma_f32_16x16x32_bf16 v[96:99], a[24:27], a[144:147], v[96:99]\n\t"
"ds_write_addtid_b32 v212 offset:6176\n\t"
"v_cvt_pk_bf16_f32 v214, v196, v216\n\t"
"v_mfma_f32_16x16x32_bf16 v[96:99], a[28:31], a[160:163], v[96:99]\n\t"
"ds_write_addtid_b32 v214 offset:14496\n\t"
"v_cvt_pk_bf16_f32 v215, v197, v217\n\t"
"v_mfma_f32_16x16x32_bf16 v[112:115], a[32:35], a[144:147], v[112:115]\n\t"
"s_nop 0\n\t"
"ds_write_addtid_b32 v213 offset:7200\n\t"
"ds_write_addtid_b32 v215 offset:15520\n\t"
"v_mfma_f32_16x16x32_bf16 v[112:115], a[36:39], a[160:163], v[112:115]\n\t"
"buffer_load_dwordx4 a[100:103], off, s[96:99], s79 offset:1024 sc1\n\t"
"v_mfma_f32_16x16x32_bf16 v[128:131], a[40:43], a[144:147], v[128:131]\n\t"
"ds_read_b128 a[172:175], v224 offset:19360\n\t"
"v_cvt_pk_bf16_f32 v220, v198, v218\n\t"
"v_mfma_f32_16x16x32_bf16 v[128:131], a[44:47], a[160:163], v[128:131]\n\t"
"buffer_load_dwordx4 a[136:139], off, s[92:95], s79 offset:2048 sc1\n\t"
"v_mfma_f32_16x16x32_bf16 v[132:135], a[40:43], a[148:151], v[132:135]\n\t"
"s_nop 0\n\t"
"ds_read_b128 a[156:159], v224 offset:17312\n\t"
"ds_read_b128 a[244:247], v255 offset:39168\n\t"
"v_mfma_f32_16x16x32_bf16 v[132:135], a[44:47], a[164:167], v[132:135]\n\t"
"buffer_load_dwordx4 a[140:143], off, s[92:95], s79 offset:3072 sc1\n\t"
"v_mfma_f32_16x16x32_bf16 v[116:119], a[32:35], a[148:151], v[116:119]\n\t"
"v_mfma_f32_16x16x32_bf16 v[116:119], a[36:39], a[164:167], v[116:119]\n\t"
"buffer_load_dwordx4 a[132:135], off, s[92:95], s79 offset:1024 sc1\n\t"
"v_mfma_f32_16x16x32_bf16 v[100:103], a[24:27], a[148:151], v[100:103]\n\t"
"s_waitcnt lgkmcnt(0)\n\t"
"v_cvt_pk_bf16_f32 v221, v199, v219\n\t"
"v_mfma_f32_16x16x32_bf16 v[100:103], a[28:31], a[164:167], v[100:103]\n\t"
"s_barrier\n\t"
"v_mfma_f32_16x16x32_bf16 v[104:107], a[24:27], a[152:155], v[104:107]\n\t"
"s_nop 0\n\t"
"ds_read_b128 a[144:147], v224 offset:1024\n\t"
"ds_read_b128 a[148:151], v224 offset:1280\n\t"
"v_mfma_f32_16x16x32_bf16 v[104:107], a[28:31], a[168:171], v[104:107]\n\t"
"buffer_load_dwordx4 a[128:131], off, s[92:95], s79 offset:0 sc1\n\t"
"v_mfma_f32_16x16x32_bf16 v[120:123], a[32:35], a[152:155], v[120:123]\n\t"
"ds_read_b128 a[160:163], v224 offset:3072\n\t"
"v_add_u32 v248, 101632, v225\n\t"
"v_mfma_f32_16x16x32_bf16 v[120:123], a[36:39], a[168:171], v[120:123]\n\t"
"ds_read_b128 a[164:167], v224 offset:3328\n\t"
"v_mfma_f32_16x16x32_bf16 v[136:139], a[40:43], a[152:155], v[136:139]\n\t"
"ds_read_b128 a[152:155], v224 offset:1536\n\t"
"v_xor_b32 v249, 32, v248\n\t"
"v_mfma_f32_16x16x32_bf16 v[136:139], a[44:47], a[168:171], v[136:139]\n\t"
"buffer_load_dwordx4 a[120:123], off, s[96:99], s78 offset:2048 sc1\n\t"
"v_mfma_f32_16x16x32_bf16 v[140:143], a[40:43], a[156:159], v[140:143]\n\t"
"ds_read_b128 a[240:243], v255 offset:18432\n\t"
"v_xor_b32 v250, 64, v248\n\t"
"v_mfma_f32_16x16x32_bf16 v[140:143], a[44:47], a[172:175], v[140:143]\n\t"
"ds_read_b128 a[168:171], v224 offset:3584\n\t"
"v_xor_b32 v251, 96, v248\n\t"
"v_mfma_f32_16x16x32_bf16 v[124:127], a[32:35], a[156:159], v[124:127]\n\t"
"ds_read_b64_tr_b16 a[178:179], v250 offset:43776\n\t"
"v_mfma_f32_16x16x32_bf16 v[124:127], a[36:39], a[172:175], v[124:127]\n\t"
"buffer_load_dwordx4 a[124:127], off, s[96:99], s78 offset:3072 sc1\n\t"
"v_mfma_f32_16x16x32_bf16 v[108:111], a[24:27], a[156:159], v[108:111]\n\t"
"ds_read_b128 a[156:159], v224 offset:1792\n\t"
"v_mfma_f32_16x16x32_bf16 v[108:111], a[28:31], a[172:175], v[108:111]\n\t"
"s_waitcnt vmcnt(18) lgkmcnt(6)\n\t"
"ds_read_b128 a[172:175], v224 offset:3840\n\t"
"v_mfma_f32_16x16x32_bf16 v[48:51], a[48:51], a[144:147], v[48:51]\n\t"
"ds_read_b64_tr_b16 a[186:187], v251 offset:43776\n\t"
"v_mfma_f32_16x16x32_bf16 v[48:51], a[52:55], a[160:163], v[48:51]\n\t"
"buffer_load_dwordx4 a[16:19], off, s[92:95], s74 offset:0 sc1\n\t"
"v_mfma_f32_16x16x32_bf16 v[64:67], a[56:59], a[144:147], v[64:67]\n\t"
"ds_read_b64_tr_b16 a[194:195], v248 offset:43776\n\t"
"v_mfma_f32_16x16x32_bf16 v[64:67], a[60:63], a[160:163], v[64:67]\n\t"
"s_nop 0\n\t"
"ds_read_b64_tr_b16 a[202:203], v249 offset:43776\n\t"
"ds_read_b64_tr_b16 a[176:177], v248 offset:41472\n\t"
"v_mfma_f32_16x16x32_bf16 v[80:83], a[64:67], a[144:147], v[80:83]\n\t"
"ds_read_b64_tr_b16 a[184:185], v249 offset:41472\n\t"
"v_mfma_f32_16x16x32_bf16 v[80:83], a[68:71], a[160:163], v[80:83]\n\t"
"s_waitcnt lgkmcnt(8)\n\t"
"buffer_load_dwordx4 a[20:23], off, s[92:95], s74 offset:1024 sc1\n\t"
"v_mfma_f32_16x16x32_bf16 v[84:87], a[64:67], a[148:151], v[84:87]\n\t"
"ds_read_b128 a[144:147], v224 offset:0\n\t"
"v_mfma_f32_16x16x32_bf16 v[84:87], a[68:71], a[164:167], v[84:87]\n\t"
"ds_read_b128 a[160:163], v224 offset:2048\n\t"
"v_mfma_f32_16x16x32_bf16 v[68:71], a[56:59], a[148:151], v[68:71]\n\t"
"ds_read_b64_tr_b16 a[180:181], v248 offset:46080\n\t"
"v_mfma_f32_16x16x32_bf16 v[68:71], a[60:63], a[164:167], v[68:71]\n\t"
"buffer_load_dwordx4 a[12:15], off, s[96:99], s72 offset:3072 sc1\n\t"
"v_mfma_f32_16x16x32_bf16 v[52:55], a[48:51], a[148:151], v[52:55]\n\t"
"ds_read_b128 a[148:151], v224 offset:256\n\t"
"v_mfma_f32_16x16x32_bf16 v[52:55], a[52:55], a[164:167], v[52:55]\n\t"
"ds_read_b128 a[164:167], v224 offset:2304\n\t"
"v_mfma_f32_16x16x32_bf16 v[56:59], a[48:51], a[152:155], v[56:59]\n\t"
"s_nop 0\n\t"
"ds_write_addtid_b32 v209 offset:30016\n\t"
"ds_write_addtid_b32 v201 offset:27936\n\t"
"v_mfma_f32_16x16x32_bf16 v[56:59], a[52:55], a[168:171], v[56:59]\n\t"
"buffer_load_dwordx4 a[8:11], off, s[96:99], s72 offset:2048 sc1\n\t"
"v_mfma_f32_16x16x32_bf16 v[72:75], a[56:59], a[152:155], v[72:75]\n\t"
"s_nop 0\n\t"
"ds_write_addtid_b32 v194 offset:16544\n\t"
"ds_write_addtid_b32 v211 offset:21696\n\t"
"v_mfma_f32_16x16x32_bf16 v[72:75], a[60:63], a[168:171], v[72:75]\n\t"
"s_nop 0\n\t"
"ds_write_addtid_b32 v200 offset:26912\n\t"
"ds_write_addtid_b32 v222 offset:22720\n\t"
"v_mfma_f32_16x16x32_bf16 v[88:91], a[64:67], a[152:155], v[88:91]\n\t"
"ds_read_b128 a[152:155], v224 offset:512\n\t"
"v_mfma_f32_16x16x32_bf16 v[88:91], a[68:71], a[168:171], v[88:91]\n\t"
"s_waitcnt lgkmcnt(10)\n\t"
"buffer_load_dwordx4 a[0:3], off, s[96:99], s72 offset:0 sc1\n\t"
"v_mfma_f32_16x16x32_bf16 v[92:95], a[64:67], a[156:159], v[92:95]\n\t"
"ds_read_b128 a[168:171], v224 offset:2560\n\t"
"v_mfma_f32_16x16x32_bf16 v[92:95], a[68:71], a[172:175], v[92:95]\n\t"
"s_nop 0\n\t"
"ds_write_addtid_b32 v202 offset:18592\n\t"
"ds_write_addtid_b32 v203 offset:19616\n\t"
"v_mfma_f32_16x16x32_bf16 v[76:79], a[56:59], a[156:159], v[76:79]\n\t"
"s_nop 0\n\t"
"ds_write_addtid_b32 v192 offset:24864\n\t"
"ds_write_addtid_b32 v210 offset:20672\n\t"
"v_mfma_f32_16x16x32_bf16 v[76:79], a[60:63], a[172:175], v[76:79]\n\t"
"buffer_load_dwordx4 a[4:7], off, s[96:99], s72 offset:1024 sc1\n\t"
"v_mfma_f32_16x16x32_bf16 v[60:63], a[48:51], a[156:159], v[60:63]\n\t"
"ds_read_b128 a[156:159], v224 offset:768\n\t"
"v_mfma_f32_16x16x32_bf16 v[60:63], a[52:55], a[172:175], v[60:63]\n\t"
"s_waitcnt vmcnt(18) lgkmcnt(11)\n\t"
"ds_write_addtid_b32 v195 offset:17568\n\t"
"ds_write_addtid_b32 v193 offset:25888\n\t"
"v_mfma_f32_16x16x32_bf16 v[0:3], a[72:75], a[144:147], v[0:3]\n\t"
"s_nop 0\n\t"
"ds_write_addtid_b32 v208 offset:28992\n\t"
"ds_write_addtid_b32 v221 offset:32064\n\t"
"v_mfma_f32_16x16x32_bf16 v[0:3], a[76:79], a[160:163], v[0:3]\n\t"
"buffer_load_dwordx4 a[40:43], off, s[92:95], s72 offset:2048 sc1\n\t"
"v_mfma_f32_16x16x32_bf16 v[16:19], a[80:83], a[144:147], v[16:19]\n\t"
"s_nop 0\n\t"
"ds_write_addtid_b32 v223 offset:23744\n\t"
"ds_write_addtid_b32 v220 offset:31040\n\t"
"v_mfma_f32_16x16x32_bf16 v[16:19], a[84:87], a[160:163], v[16:19]\n\t"
"ds_read_b128 a[172:175], v224 offset:2816\n\t"
"v_mfma_f32_16x16x32_bf16 v[32:35], a[88:91], a[144:147], v[32:35]\n\t"
"ds_read_b64_tr_b16 a[188:189], v249 offset:46080\n\t"
"v_mfma_f32_16x16x32_bf16 v[32:35], a[92:95], a[160:163], v[32:35]\n\t"
"buffer_load_dwordx4 a[44:47], off, s[92:95], s72 offset:3072 sc1\n\t"
"v_mfma_f32_16x16x32_bf16 v[36:39], a[88:91], a[148:151], v[36:39]\n\t"
"ds_read_b64_tr_b16 a[192:193], v250 offset:41472\n\t"
"v_mfma_f32_16x16x32_bf16 v[36:39], a[92:95], a[164:167], v[36:39]\n\t"
"ds_read_b64_tr_b16 a[200:201], v251 offset:41472\n\t"
"v_mfma_f32_16x16x32_bf16 v[20:23], a[80:83], a[148:151], v[20:23]\n\t"
"s_nop 0\n\t"
"ds_read_b64_tr_b16 a[196:197], v250 offset:46080\n\t"
"ds_read_b64_tr_b16 a[204:205], v251 offset:46080\n\t"
"v_mfma_f32_16x16x32_bf16 v[20:23], a[84:87], a[164:167], v[20:23]\n\t"
"buffer_load_dwordx4 a[36:39], off, s[92:95], s72 offset:1024 sc1\n\t"
"v_mfma_f32_16x16x32_bf16 v[4:7], a[72:75], a[148:151], v[4:7]\n\t"
"s_waitcnt lgkmcnt(5)\n\t"
"v_mfma_f32_16x16x32_bf16 v[4:7], a[76:79], a[164:167], v[4:7]\n\t"
"s_barrier\n\t"
"v_mfma_f32_16x16x32_bf16 v[8:11], a[72:75], a[152:155], v[8:11]\n\t"
"s_nop 0\n\t"
"ds_read_b128 a[144:147], v224 offset:17568\n\t"
"ds_read_b128 a[160:163], v224 offset:19616\n\t"
"v_mfma_f32_16x16x32_bf16 v[8:11], a[76:79], a[168:171], v[8:11]\n\t"
"buffer_load_dwordx4 a[32:35], off, s[92:95], s72 offset:0 sc1\n\t"
"v_mfma_f32_16x16x32_bf16 v[24:27], a[80:83], a[152:155], v[24:27]\n\t"
"ds_read_b128 a[148:151], v224 offset:17824\n\t"
"v_mfma_f32_16x16x32_bf16 v[24:27], a[84:87], a[168:171], v[24:27]\n\t"
"ds_read_b128 a[164:167], v224 offset:19872\n\t"
"v_mfma_f32_16x16x32_bf16 v[40:43], a[88:91], a[152:155], v[40:43]\n\t"
"v_mfma_f32_16x16x32_bf16 v[40:43], a[92:95], a[168:171], v[40:43]\n\t"
"buffer_load_dwordx4 a[24:27], off, s[96:99], s69 offset:2048 sc1\n\t"
"v_mfma_f32_16x16x32_bf16 v[44:47], a[88:91], a[156:159], v[44:47]\n\t"
"ds_read_b128 a[168:171], v224 offset:20128\n\t"
"v_mfma_f32_16x16x32_bf16 v[44:47], a[92:95], a[172:175], v[44:47]\n\t"
"buffer_load_dwordx4 a[28:31], off, s[96:99], s69 offset:3072 sc1\n\t"
"v_mfma_f32_16x16x32_bf16 v[28:31], a[80:83], a[156:159], v[28:31]\n\t"
"ds_read_b128 a[152:155], v224 offset:18080\n\t"
"v_mfma_f32_16x16x32_bf16 v[28:31], a[84:87], a[172:175], v[28:31]\n\t"
"buffer_load_dwordx4 a[64:67], off, s[92:95], s83 offset:0 sc1\n\t"
"v_mfma_f32_16x16x32_bf16 v[12:15], a[72:75], a[156:159], v[12:15]\n\t"
"ds_read_b128 a[156:159], v224 offset:18336\n\t"
"v_mfma_f32_16x16x32_bf16 v[12:15], a[76:79], a[172:175], v[12:15]\n\t"
"s_waitcnt lgkmcnt(5)\n\t"
"buffer_load_dwordx4 a[68:71], off, s[92:95], s83 offset:1024 sc1\n\t"
"v_mfma_f32_4x4x4_16b_bf16 v[200:203], a[220:221], a[178:179], 0\n\t"
"ds_read_b128 a[172:175], v224 offset:20384\n\t"
"v_mfma_f32_4x4x4_16b_bf16 v[204:207], a[220:221], a[186:187], 0\n\t"
"ds_read_b64_tr_b16 a[178:179], v250 offset:44928\n\t"
"v_mfma_f32_4x4x4_16b_bf16 v[216:219], a[220:221], a[194:195], 0\n\t"
"ds_read_b64_tr_b16 a[186:187], v251 offset:44928\n\t"
"v_mfma_f32_4x4x4_16b_bf16 v[220:223], a[220:221], a[202:203], 0\n\t"
"v_mfma_f32_4x4x4_16b_bf16 v[192:195], a[222:223], a[176:177], v[200:203]\n\t"
"ds_read_b64_tr_b16 a[194:195], v248 offset:44928\n\t"
"v_mfma_f32_4x4x4_16b_bf16 v[196:199], a[222:223], a[184:185], v[204:207]\n\t"
"ds_read_b64_tr_b16 a[202:203], v249 offset:44928\n\t"
"v_mfma_f32_4x4x4_16b_bf16 v[200:203], a[222:223], a[180:181], v[200:203]\n\t"
"ds_read_b64_tr_b16 a[176:177], v248 offset:42624\n\t"
"v_mfma_f32_4x4x4_16b_bf16 v[204:207], a[222:223], a[188:189], v[204:207]\n\t"
"ds_read_b64_tr_b16 a[184:185], v249 offset:42624\n\t"
"v_mfma_f32_4x4x4_16b_bf16 v[208:211], a[222:223], a[192:193], v[216:219]\n\t"
"ds_read_b64_tr_b16 a[180:181], v248 offset:47232\n\t"
"v_mfma_f32_4x4x4_16b_bf16 v[212:215], a[222:223], a[200:201], v[220:223]\n\t"
"ds_read_b64_tr_b16 a[188:189], v249 offset:47232\n\t"
"v_mfma_f32_4x4x4_16b_bf16 v[216:219], a[222:223], a[196:197], v[216:219]\n\t"
"ds_read_b64_tr_b16 a[192:193], v250 offset:42624\n\t"
"v_mfma_f32_4x4x4_16b_bf16 v[220:223], a[222:223], a[204:205], v[220:223]\n\t"
"s_waitcnt vmcnt(20) lgkmcnt(8)\n\t"
"buffer_load_dwordx4 a[60:63], off, s[96:99], s82 offset:3072 sc1\n\t"
"v_mfma_f32_16x16x32_bf16 v[144:147], a[96:99], a[144:147], v[144:147]\n\t"
"ds_read_b64_tr_b16 a[200:201], v251 offset:42624\n\t"
"v_mfma_f32_16x16x32_bf16 v[144:147], a[100:103], a[160:163], v[144:147]\n\t"
"buffer_load_dwordx4 a[56:59], off, s[96:99], s82 offset:2048 sc1\n\t"
"v_mfma_f32_16x16x32_bf16 v[160:163], a[104:107], a[144:147], v[160:163]\n\t"
"ds_read_b64_tr_b16 a[196:197], v250 offset:47232\n\t"
"v_cvt_pk_bf16_f32 v192, -v192, -v196\n\t"
"v_mfma_f32_16x16x32_bf16 v[160:163], a[108:111], a[160:163], v[160:163]\n\t"
"ds_write_addtid_b32 v192 offset:0\n\t"
"v_cvt_pk_bf16_f32 v201, v201, v205\n\t"
"v_mfma_f32_16x16x32_bf16 v[176:179], a[112:115], a[144:147], v[176:179]\n\t"
"ds_read_b128 a[144:147], v224 offset:16544\n\t"
"v_cvt_pk_bf16_f32 v211, -v211, -v215\n\t"
"v_mfma_f32_16x16x32_bf16 v[176:179], a[116:119], a[160:163], v[176:179]\n\t"
"buffer_load_dwordx4 a[48:51], off, s[96:99], s82 offset:0 sc1\n\t"
"v_mfma_f32_16x16x32_bf16 v[180:183], a[112:115], a[148:151], v[180:183]\n\t"
"ds_read_b128 a[160:163], v224 offset:18592\n\t"
"v_cvt_pk_bf16_f32 v202, v202, v206\n\t"
"v_mfma_f32_16x16x32_bf16 v[180:183], a[116:119], a[164:167], v[180:183]\n\t"
"ds_write_addtid_b32 v202 offset:5152\n\t"
"v_cvt_pk_bf16_f32 v210, -v210, -v214\n\t"
"v_mfma_f32_16x16x32_bf16 v[164:167], a[104:107], a[148:151], v[164:167]\n\t"
"ds_write_addtid_b32 v210 offset:3072\n\t"
"v_cvt_pk_bf16_f32 v219, v219, v223\n\t"
"v_mfma_f32_16x16x32_bf16 v[164:167], a[108:111], a[164:167], v[164:167]\n\t"
"buffer_load_dwordx4 a[52:55], off, s[96:99], s82 offset:1024 sc1\n\t"
"v_mfma_f32_16x16x32_bf16 v[148:151], a[96:99], a[148:151], v[148:151]\n\t"
"ds_read_b128 a[148:151], v224 offset:16800\n\t"
"v_cvt_pk_bf16_f32 v208, -v208, -v212\n\t"
"v_mfma_f32_16x16x32_bf16 v[148:151], a[100:103], a[164:167], v[148:151]\n\t"
"ds_write_addtid_b32 v208 offset:2048\n\t"
"v_cvt_pk_bf16_f32 v216, v216, v220\n\t"
"v_mfma_f32_16x16x32_bf16 v[152:155], a[96:99], a[152:155], v[152:155]\n\t"
"ds_read_b128 a[164:167], v224 offset:18848\n\t"
"v_cvt_pk_bf16_f32 v217, v217, v221\n\t"
"v_mfma_f32_16x16x32_bf16 v[152:155], a[100:103], a[168:171], v[152:155]\n\t"
"buffer_load_dwordx4 a[88:91], off, s[92:95], s82 offset:2048 sc1\n\t"
"v_mfma_f32_16x16x32_bf16 v[168:171], a[104:107], a[152:155], v[168:171]\n\t"
"ds_write_addtid_b32 v219 offset:15520\n\t"
"v_cvt_pk_bf16_f32 v203, v203, v207\n\t"
"v_mfma_f32_16x16x32_bf16 v[168:171], a[108:111], a[168:171], v[168:171]\n\t"
"ds_write_addtid_b32 v216 offset:6176\n\t"
"v_cvt_pk_bf16_f32 v218, v218, v222\n\t"
"v_mfma_f32_16x16x32_bf16 v[184:187], a[112:115], a[152:155], v[184:187]\n\t"
"ds_read_b128 a[152:155], v224 offset:17056\n\t"
"v_cvt_pk_bf16_f32 v200, v200, v204\n\t"
"v_mfma_f32_16x16x32_bf16 v[184:187], a[116:119], a[168:171], v[184:187]\n\t"
"buffer_load_dwordx4 a[92:95], off, s[92:95], s82 offset:3072 sc1\n\t"
"v_mfma_f32_16x16x32_bf16 v[188:191], a[112:115], a[156:159], v[188:191]\n\t"
"ds_read_b128 a[168:171], v224 offset:19104\n\t"
"v_cvt_pk_bf16_f32 v194, -v194, -v198\n\t"
"v_mfma_f32_16x16x32_bf16 v[188:191], a[116:119], a[172:175], v[188:191]\n\t"
"ds_write_addtid_b32 v203 offset:13472\n\t"
"v_cvt_pk_bf16_f32 v193, -v193, -v197\n\t"
"v_mfma_f32_16x16x32_bf16 v[172:175], a[104:107], a[156:159], v[172:175]\n\t"
"ds_write_addtid_b32 v200 offset:4128\n\t"
"v_cvt_pk_bf16_f32 v195, -v195, -v199\n\t"
"v_mfma_f32_16x16x32_bf16 v[172:175], a[108:111], a[172:175], v[172:175]\n\t"
"buffer_load_dwordx4 a[84:87], off, s[92:95], s82 offset:1024 sc1\n\t"
"v_mfma_f32_16x16x32_bf16 v[156:159], a[96:99], a[156:159], v[156:159]\n\t"
"s_nop 0\n\t"
"ds_write_addtid_b32 v193 offset:8320\n\t"
"ds_write_addtid_b32 v201 offset:12448\n\t"
"v_mfma_f32_16x16x32_bf16 v[156:159], a[100:103], a[172:175], v[156:159]\n\t"
"s_waitcnt vmcnt(21) lgkmcnt(8)\n\t"
"ds_write_addtid_b32 v217 offset:14496\n\t"
"v_cvt_pk_bf16_f32 v209, -v209, -v213\n\t"
"v_mfma_f32_16x16x32_bf16 v[96:99], a[120:123], a[144:147], v[96:99]\n\t"
"ds_write_addtid_b32 v209 offset:10368\n\t"
"v_mfma_f32_16x16x32_bf16 v[96:99], a[124:127], a[160:163], v[96:99]\n\t"
"s_nop 0\n\t"
"ds_write_addtid_b32 v211 offset:11392\n\t"
"ds_write_addtid_b32 v218 offset:7200\n\t"
"v_mfma_f32_16x16x32_bf16 v[112:115], a[128:131], a[144:147], v[112:115]\n\t"
"s_nop 0\n\t"
"ds_write_addtid_b32 v194 offset:1024\n\t"
"ds_write_addtid_b32 v195 offset:9344\n\t"
"v_mfma_f32_16x16x32_bf16 v[112:115], a[132:135], a[160:163], v[112:115]\n\t"
".p2align 3\n\t"
"buffer_load_dwordx4 a[80:83], off, s[92:95], s82 offset:0 sc1\n\t"
"v_pk_fma_f32 v[48:49], v[144:145], s[90:91], v[48:49]\n\t"
"v_pk_fma_f32 v[50:51], v[146:147], s[90:91], v[50:51]\n\t"
"v_pk_add_f32 v[144:145], v[48:49], v[144:145] neg_lo:[0,1] neg_hi:[0,1]\n\t"
"v_pk_add_f32 v[146:147], v[50:51], v[146:147] neg_lo:[0,1] neg_hi:[0,1]\n\t"
"v_pk_fma_f32 v[64:65], v[160:161], s[90:91], v[64:65]\n\t"
"v_pk_fma_f32 v[66:67], v[162:163], s[90:91], v[66:67]\n\t"
"v_pk_add_f32 v[160:161], v[64:65], v[160:161] neg_lo:[0,1] neg_hi:[0,1]\n\t"
"v_pk_add_f32 v[162:163], v[66:67], v[162:163] neg_lo:[0,1] neg_hi:[0,1]\n\t"
"v_pk_fma_f32 v[80:81], v[176:177], s[90:91], v[80:81]\n\t"
"v_pk_fma_f32 v[82:83], v[178:179], s[90:91], v[82:83]\n\t"
"v_pk_add_f32 v[176:177], v[80:81], v[176:177] neg_lo:[0,1] neg_hi:[0,1]\n\t"
"v_pk_add_f32 v[178:179], v[82:83], v[178:179] neg_lo:[0,1] neg_hi:[0,1]\n\t"
"buffer_load_dwordx4 a[72:75], off, s[96:99], s81 offset:2048 sc1\n\t"
"v_pk_fma_f32 v[84:85], v[180:181], s[90:91], v[84:85]\n\t"
"v_pk_fma_f32 v[86:87], v[182:183], s[90:91], v[86:87]\n\t"
"v_pk_add_f32 v[180:181], v[84:85], v[180:181] neg_lo:[0,1] neg_hi:[0,1]\n\t"
"v_pk_add_f32 v[182:183], v[86:87], v[182:183] neg_lo:[0,1] neg_hi:[0,1]\n\t"
"v_pk_fma_f32 v[68:69], v[164:165], s[90:91], v[68:69]\n\t"
"v_pk_fma_f32 v[70:71], v[166:167], s[90:91], v[70:71]\n\t"
"v_pk_add_f32 v[164:165], v[68:69], v[164:165] neg_lo:[0,1] neg_hi:[0,1]\n\t"
"v_pk_add_f32 v[166:167], v[70:71], v[166:167] neg_lo:[0,1] neg_hi:[0,1]\n\t"
"v_pk_fma_f32 v[52:53], v[148:149], s[90:91], v[52:53]\n\t"
"v_pk_fma_f32 v[54:55], v[150:151], s[90:91], v[54:55]\n\t"
"v_pk_add_f32 v[148:149], v[52:53], v[148:149] neg_lo:[0,1] neg_hi:[0,1]\n\t"
"v_pk_add_f32 v[150:151], v[54:55], v[150:151] neg_lo:[0,1] neg_hi:[0,1]\n\t"
"buffer_load_dwordx4 a[76:79], off, s[96:99], s81 offset:3072 sc1\n\t"
"v_pk_fma_f32 v[56:57], v[152:153], s[90:91], v[56:57]\n\t"
"v_pk_fma_f32 v[58:59], v[154:155], s[90:91], v[58:59]\n\t"
"v_pk_add_f32 v[152:153], v[56:57], v[152:153] neg_lo:[0,1] neg_hi:[0,1]\n\t"
"v_pk_add_f32 v[154:155], v[58:59], v[154:155] neg_lo:[0,1] neg_hi:[0,1]\n\t"
"v_pk_fma_f32 v[72:73], v[168:169], s[90:91], v[72:73]\n\t"
"v_pk_fma_f32 v[74:75], v[170:171], s[90:91], v[74:75]\n\t"
"v_pk_add_f32 v[168:169], v[72:73], v[168:169] neg_lo:[0,1] neg_hi:[0,1]\n\t"
"v_pk_add_f32 v[170:171], v[74:75], v[170:171] neg_lo:[0,1] neg_hi:[0,1]\n\t"
"v_pk_fma_f32 v[88:89], v[184:185], s[90:91], v[88:89]\n\t"
"v_pk_fma_f32 v[90:91], v[186:187], s[90:91], v[90:91]\n\t"
"v_pk_add_f32 v[184:185], v[88:89], v[184:185] neg_lo:[0,1] neg_hi:[0,1]\n\t"
"v_pk_add_f32 v[186:187], v[90:91], v[186:187] neg_lo:[0,1] neg_hi:[0,1]\n\t"
"buffer_load_dwordx4 a[112:115], off, s[92:95], s56 offset:0 sc1\n\t"
"v_pk_fma_f32 v[92:93], v[188:189], s[90:91], v[92:93]\n\t"
"v_pk_fma_f32 v[94:95], v[190:191], s[90:91], v[94:95]\n\t"
"v_pk_add_f32 v[188:189], v[92:93], v[188:189] neg_lo:[0,1] neg_hi:[0,1]\n\t"
"v_pk_add_f32 v[190:191], v[94:95], v[190:191] neg_lo:[0,1] neg_hi:[0,1]\n\t"
"v_pk_fma_f32 v[76:77], v[172:173], s[90:91], v[76:77]\n\t"
"v_pk_fma_f32 v[78:79], v[174:175], s[90:91], v[78:79]\n\t"
"v_pk_add_f32 v[172:173], v[76:77], v[172:173] neg_lo:[0,1] neg_hi:[0,1]\n\t"
"v_pk_add_f32 v[174:175], v[78:79], v[174:175] neg_lo:[0,1] neg_hi:[0,1]\n\t"
"v_pk_fma_f32 v[60:61], v[156:157], s[90:91], v[60:61]\n\t"
"v_pk_fma_f32 v[62:63], v[158:159], s[90:91], v[62:63]\n\t"
"v_pk_add_f32 v[156:157], v[60:61], v[156:157] neg_lo:[0,1] neg_hi:[0,1]\n\t"
"v_pk_add_f32 v[158:159], v[62:63], v[158:159] neg_lo:[0,1] neg_hi:[0,1]\n\t"
"v_mfma_f32_16x16x32_bf16 v[128:131], a[136:139], a[144:147], v[128:131]\n\t"
"ds_read_b128 a[172:175], v224 offset:19360\n\t"
"v_mfma_f32_16x16x32_bf16 v[128:131], a[140:143], a[160:163], v[128:131]\n\t"
"buffer_load_dwordx4 a[116:119], off, s[92:95], s56 offset:1024 sc1\n\t"
"v_mfma_f32_16x16x32_bf16 v[132:135], a[136:139], a[148:151], v[132:135]\n\t"
"ds_read_b128 a[156:159], v224 offset:17312\n\t"
"v_mfma_f32_16x16x32_bf16 v[132:135], a[140:143], a[164:167], v[132:135]\n\t"
"buffer_load_dwordx4 a[108:111], off, s[96:99], s55 offset:3072 sc1\n\t"
"v_mfma_f32_16x16x32_bf16 v[116:119], a[128:131], a[148:151], v[116:119]\n\t"
"ds_read_b64_tr_b16 a[204:205], v251 offset:47232\n\t"
"v_mfma_f32_16x16x32_bf16 v[116:119], a[132:135], a[164:167], v[116:119]\n\t"
"buffer_load_dwordx4 a[104:107], off, s[96:99], s55 offset:2048 sc1\n\t"
"v_mfma_f32_16x16x32_bf16 v[100:103], a[120:123], a[148:151], v[100:103]\n\t"
"v_accvgpr_read_b32 v224, a212\n\t"
"v_mfma_f32_16x16x32_bf16 v[100:103], a[124:127], a[164:167], v[100:103]\n\t"
"s_waitcnt lgkmcnt(0)\n\t"
"s_barrier\n\t"
"v_mfma_f32_16x16x32_bf16 v[104:107], a[120:123], a[152:155], v[104:107]\n\t"
"s_add_u32 m0, s26, 145152\n\t"
"ds_read_b128 a[144:147], v224 offset:1024\n\t"
"v_mfma_f32_16x16x32_bf16 v[104:107], a[124:127], a[168:171], v[104:107]\n\t"
"ds_read_b128 a[160:163], v224 offset:3072\n\t"
"v_mfma_f32_16x16x32_bf16 v[120:123], a[128:131], a[152:155], v[120:123]\n\t"
"ds_read_b128 a[148:151], v224 offset:1280\n\t"
"v_mfma_f32_16x16x32_bf16 v[120:123], a[132:135], a[168:171], v[120:123]\n\t"
"ds_read_b128 a[164:167], v224 offset:3328\n\t"
"v_mfma_f32_16x16x32_bf16 v[136:139], a[136:139], a[152:155], v[136:139]\n\t"
"v_mfma_f32_16x16x32_bf16 v[136:139], a[140:143], a[168:171], v[136:139]\n\t"
"buffer_load_dwordx4 v236, s[12:15], s22 offen offset:256 nt lds\n\t"
"v_mfma_f32_16x16x32_bf16 v[140:143], a[136:139], a[156:159], v[140:143]\n\t"
"ds_read_b128 a[152:155], v224 offset:1536\n\t"
"v_mfma_f32_16x16x32_bf16 v[140:143], a[140:143], a[172:175], v[140:143]\n\t"
"ds_read_b128 a[228:231], v254 offset:59904\n\t"
"v_mfma_f32_16x16x32_bf16 v[124:127], a[128:131], a[156:159], v[124:127]\n\t"
"ds_read_b128 a[168:171], v224 offset:3584\n\t"
"v_mfma_f32_16x16x32_bf16 v[124:127], a[132:135], a[172:175], v[124:127]\n\t"
"buffer_load_dwordx4 v237, s[12:15], s22 offen offset:1280 nt lds\n\t"
"v_mfma_f32_16x16x32_bf16 v[108:111], a[120:123], a[156:159], v[108:111]\n\t"
"s_nop 0\n\t"
"ds_read_b128 a[156:159], v224 offset:1792\n\t"
"v_mfma_f32_16x16x32_bf16 v[108:111], a[124:127], a[172:175], v[108:111]\n\t"
"buffer_load_dwordx4 a[96:99], off, s[96:99], s55 offset:0 sc1\n\t"
"v_mfma_f32_4x4x4_16b_bf16 v[200:203], a[220:221], a[178:179], 0\n\t"
"ds_read_b128 a[172:175], v224 offset:3840\n\t"
"v_mfma_f32_4x4x4_16b_bf16 v[204:207], a[220:221], a[186:187], 0\n\t"
"ds_read_b64_tr_b16 a[178:179], v250 offset:23040\n\t"
"v_mfma_f32_4x4x4_16b_bf16 v[216:219], a[220:221], a[194:195], 0\n\t"
"ds_read_b64_tr_b16 a[186:187], v251 offset:23040\n\t"
"v_mfma_f32_4x4x4_16b_bf16 v[220:223], a[220:221], a[202:203], 0\n\t"
"buffer_load_dwordx4 v238, s[12:15], s22 offen offset:2304 nt lds\n\t"
"v_mfma_f32_4x4x4_16b_bf16 v[192:195], a[222:223], a[176:177], v[200:203]\n\t"
"ds_read_b64_tr_b16 a[194:195], v248 offset:23040\n\t"
"v_mfma_f32_4x4x4_16b_bf16 v[196:199], a[222:223], a[184:185], v[204:207]\n\t"
"ds_read_b64_tr_b16 a[202:203], v249 offset:23040\n\t"
"v_mfma_f32_4x4x4_16b_bf16 v[200:203], a[222:223], a[180:181], v[200:203]\n\t"
"ds_read_b64_tr_b16 a[176:177], v248 offset:20736\n\t"
"v_mfma_f32_4x4x4_16b_bf16 v[204:207], a[222:223], a[188:189], v[204:207]\n\t"
".p2align 3\n\t"
"ds_read_b64_tr_b16 a[184:185], v249 offset:20736\n\t"
"v_pk_fma_f32 v[0:1], v[96:97], s[90:91], v[0:1]\n\t"
"v_pk_fma_f32 v[2:3], v[98:99], s[90:91], v[2:3]\n\t"
"v_pk_add_f32 v[96:97], v[0:1], v[96:97] neg_lo:[0,1] neg_hi:[0,1]\n\t"
"v_pk_add_f32 v[98:99], v[2:3], v[98:99] neg_lo:[0,1] neg_hi:[0,1]\n\t"
"v_pk_fma_f32 v[16:17], v[112:113], s[90:91], v[16:17]\n\t"
"v_pk_fma_f32 v[18:19], v[114:115], s[90:91], v[18:19]\n\t"
"v_pk_add_f32 v[112:113], v[16:17], v[112:113] neg_lo:[0,1] neg_hi:[0,1]\n\t"
"v_pk_add_f32 v[114:115], v[18:19], v[114:115] neg_lo:[0,1] neg_hi:[0,1]\n\t"
"v_pk_fma_f32 v[32:33], v[128:129], s[90:91], v[32:33]\n\t"
"v_pk_fma_f32 v[34:35], v[130:131], s[90:91], v[34:35]\n\t"
"v_pk_add_f32 v[128:129], v[32:33], v[128:129] neg_lo:[0,1] neg_hi:[0,1]\n\t"
"v_pk_add_f32 v[130:131], v[34:35], v[130:131] neg_lo:[0,1] neg_hi:[0,1]\n\t"
"v_pk_fma_f32 v[36:37], v[132:133], s[90:91], v[36:37]\n\t"
"v_pk_fma_f32 v[38:39], v[134:135], s[90:91], v[38:39]\n\t"
"v_pk_add_f32 v[132:133], v[36:37], v[132:133] neg_lo:[0,1] neg_hi:[0,1]\n\t"
"v_pk_add_f32 v[134:135], v[38:39], v[134:135] neg_lo:[0,1] neg_hi:[0,1]\n\t"
"v_pk_fma_f32 v[20:21], v[116:117], s[90:91], v[20:21]\n\t"
"v_pk_fma_f32 v[22:23], v[118:119], s[90:91], v[22:23]\n\t"
"v_pk_add_f32 v[116:117], v[20:21], v[116:117] neg_lo:[0,1] neg_hi:[0,1]\n\t"
"v_pk_add_f32 v[118:119], v[22:23], v[118:119] neg_lo:[0,1] neg_hi:[0,1]\n\t"
"v_pk_fma_f32 v[4:5], v[100:101], s[90:91], v[4:5]\n\t"
"v_pk_fma_f32 v[6:7], v[102:103], s[90:91], v[6:7]\n\t"
"v_pk_add_f32 v[100:101], v[4:5], v[100:101] neg_lo:[0,1] neg_hi:[0,1]\n\t"
"v_pk_add_f32 v[102:103], v[6:7], v[102:103] neg_lo:[0,1] neg_hi:[0,1]\n\t"
"v_pk_fma_f32 v[8:9], v[104:105], s[90:91], v[8:9]\n\t"
"v_pk_fma_f32 v[10:11], v[106:107], s[90:91], v[10:11]\n\t"
"v_pk_add_f32 v[104:105], v[8:9], v[104:105] neg_lo:[0,1] neg_hi:[0,1]\n\t"
"v_pk_add_f32 v[106:107], v[10:11], v[106:107] neg_lo:[0,1] neg_hi:[0,1]\n\t"
"v_pk_fma_f32 v[24:25], v[120:121], s[90:91], v[24:25]\n\t"
"v_pk_fma_f32 v[26:27], v[122:123], s[90:91], v[26:27]\n\t"
"v_pk_add_f32 v[120:121], v[24:25], v[120:121] neg_lo:[0,1] neg_hi:[0,1]\n\t"
"v_pk_add_f32 v[122:123], v[26:27], v[122:123] neg_lo:[0,1] neg_hi:[0,1]\n\t"
"v_pk_fma_f32 v[40:41], v[136:137], s[90:91], v[40:41]\n\t"
"v_pk_fma_f32 v[42:43], v[138:139], s[90:91], v[42:43]\n\t"
"v_pk_add_f32 v[136:137], v[40:41], v[136:137] neg_lo:[0,1] neg_hi:[0,1]\n\t"
"v_pk_add_f32 v[138:139], v[42:43], v[138:139] neg_lo:[0,1] neg_hi:[0,1]\n\t"
"v_pk_fma_f32 v[44:45], v[140:141], s[90:91], v[44:45]\n\t"
"v_pk_fma_f32 v[46:47], v[142:143], s[90:91], v[46:47]\n\t"
"v_pk_add_f32 v[140:141], v[44:45], v[140:141] neg_lo:[0,1] neg_hi:[0,1]\n\t"
"v_pk_add_f32 v[142:143], v[46:47], v[142:143] neg_lo:[0,1] neg_hi:[0,1]\n\t"
"v_pk_fma_f32 v[28:29], v[124:125], s[90:91], v[28:29]\n\t"
"v_pk_fma_f32 v[30:31], v[126:127], s[90:91], v[30:31]\n\t"
"v_pk_add_f32 v[124:125], v[28:29], v[124:125] neg_lo:[0,1] neg_hi:[0,1]\n\t"
"v_pk_add_f32 v[126:127], v[30:31], v[126:127] neg_lo:[0,1] neg_hi:[0,1]\n\t"
"v_pk_fma_f32 v[12:13], v[108:109], s[90:91], v[12:13]\n\t"
"v_pk_fma_f32 v[14:15], v[110:111], s[90:91], v[14:15]\n\t"
"v_pk_add_f32 v[108:109], v[12:13], v[108:109] neg_lo:[0,1] neg_hi:[0,1]\n\t"
"v_pk_add_f32 v[110:111], v[14:15], v[110:111] neg_lo:[0,1] neg_hi:[0,1]\n\t"
"v_mfma_f32_4x4x4_16b_bf16 v[208:211], a[222:223], a[192:193], v[216:219]\n\t"
"ds_read_b64_tr_b16 a[180:181], v248 offset:25344\n\t"
"v_mfma_f32_4x4x4_16b_bf16 v[212:215], a[222:223], a[200:201], v[220:223]\n\t"
"ds_read_b64_tr_b16 a[188:189], v249 offset:25344\n\t"
"v_mfma_f32_4x4x4_16b_bf16 v[216:219], a[222:223], a[196:197], v[216:219]\n\t"
"ds_read_b64_tr_b16 a[192:193], v250 offset:20736\n\t"
"v_mfma_f32_4x4x4_16b_bf16 v[220:223], a[222:223], a[204:205], v[220:223]\n\t"
"s_waitcnt vmcnt(26) lgkmcnt(8)\n\t"
"buffer_load_dwordx4 v239, s[12:15], s22 offen offset:3328 nt lds\n\t"
"v_mfma_f32_16x16x32_bf16 v[48:51], a[0:3], a[144:147], v[48:51]\n\t"
"s_nop 0\n\t"
"v_cvt_pk_bf16_f32 v193, -v193, -v197\n\t"
"v_mfma_f32_16x16x32_bf16 v[48:51], a[4:7], a[160:163], v[48:51]\n\t"
"s_nop 0\n\t"
"ds_write_b128 v255, a[228:231] offset:41472\n\t"
"v_mfma_f32_16x16x32_bf16 v[64:67], a[8:11], a[144:147], v[64:67]\n\t"
"Lpad4_%=:\n\t"
"s_cbranch_execz Lret0_%=\n\t"
"v_mfma_f32_16x16x32_bf16 v[64:67], a[12:15], a[160:163], v[64:67]\n\t"
"s_mov_b64 exec, s[36:37]\n\t"
"buffer_load_dwordx4 v240, s[12:15], s22 offen offset:3840 nt lds\n\t"
"s_mov_b64 exec, -1\n\t"
"v_mfma_f32_16x16x32_bf16 v[80:83], a[16:19], a[144:147], v[80:83]\n\t"
"s_lshl_b32 m0, s73, 8\n\t"
"v_cvt_pk_bf16_f32 v211, -v211, -v215\n\t"
"v_mfma_f32_16x16x32_bf16 v[80:83], a[20:23], a[160:163], v[80:83]\n\t"
"ds_read_b128 a[160:163], v224 offset:2048\n\t"
"v_cvt_pk_bf16_f32 v203, v203, v207\n\t"
"v_mfma_f32_16x16x32_bf16 v[84:87], a[16:19], a[148:151], v[84:87]\n\t"
"ds_read_b128 a[144:147], v224 offset:0\n\t"
"v_cvt_pk_bf16_f32 v216, v216, v220\n\t"
"v_mfma_f32_16x16x32_bf16 v[84:87], a[20:23], a[164:167], v[84:87]\n\t"
"buffer_load_dwordx4 a[100:103], off, s[96:99], s55 offset:1024 sc1\n\t"
"v_mfma_f32_16x16x32_bf16 v[68:71], a[8:11], a[148:151], v[68:71]\n\t"
"ds_read_b64_tr_b16 a[200:201], v251 offset:20736\n\t"
"v_cvt_pk_bf16_f32 v192, -v192, -v196\n\t"
"v_mfma_f32_16x16x32_bf16 v[68:71], a[12:15], a[164:167], v[68:71]\n\t"
"v_cvt_pk_bf16_f32 v210, -v210, -v214\n\t"
"v_cvt_pk_bf16_f32 v217, v217, v221\n\t"
"v_mfma_f32_16x16x32_bf16 v[52:55], a[0:3], a[148:151], v[52:55]\n\t"
"ds_read_b128 a[148:151], v224 offset:256\n\t"
"v_mfma_f32_16x16x32_bf16 v[52:55], a[4:7], a[164:167], v[52:55]\n\t"
"ds_read_b128 a[164:167], v224 offset:2304\n\t"
"v_cvt_pk_bf16_f32 v208, -v208, -v212\n\t"
"v_mfma_f32_16x16x32_bf16 v[56:59], a[0:3], a[152:155], v[56:59]\n\t"
"ds_write_addtid_b32 v211 offset:27936\n\t"
"v_cvt_pk_bf16_f32 v201, v201, v205\n\t"
"v_mfma_f32_16x16x32_bf16 v[56:59], a[4:7], a[168:171], v[56:59]\n\t"
"ds_write_addtid_b32 v208 offset:18592\n\t"
"v_cvt_pk_bf16_f32 v195, -v195, -v199\n\t"
"v_mfma_f32_16x16x32_bf16 v[72:75], a[8:11], a[152:155], v[72:75]\n\t"
"ds_write_addtid_b32 v203 offset:30016\n\t"
"v_cvt_pk_bf16_f32 v200, v200, v204\n\t"
"v_mfma_f32_16x16x32_bf16 v[72:75], a[12:15], a[168:171], v[72:75]\n\t"
"ds_write_addtid_b32 v210 offset:19616\n\t"
"v_cvt_pk_bf16_f32 v194, -v194, -v198\n\t"
"v_mfma_f32_16x16x32_bf16 v[88:91], a[16:19], a[152:155], v[88:91]\n\t"
"ds_read_b128 a[152:155], v224 offset:512\n\t"
"v_cvt_pk_bf16_f32 v202, v202, v206\n\t"
"v_mfma_f32_16x16x32_bf16 v[88:91], a[20:23], a[168:171], v[88:91]\n\t"
"ds_write_addtid_b32 v201 offset:28992\n\t"
"v_cvt_pk_bf16_f32 v218, v218, v222\n\t"
"v_mfma_f32_16x16x32_bf16 v[92:95], a[16:19], a[156:159], v[92:95]\n\t"
"ds_write_addtid_b32 v192 offset:16544\n\t"
"v_mfma_f32_16x16x32_bf16 v[92:95], a[20:23], a[172:175], v[92:95]\n\t"
"ds_write_addtid_b32 v195 offset:25888\n\t"
"v_cvt_pk_bf16_f32 v219, v219, v223\n\t"
"v_mfma_f32_16x16x32_bf16 v[76:79], a[8:11], a[156:159], v[76:79]\n\t"
"s_nop 0\n\t"
"ds_write_addtid_b32 v202 offset:21696\n\t"
"ds_write_addtid_b32 v216 offset:22720\n\t"
"v_mfma_f32_16x16x32_bf16 v[76:79], a[12:15], a[172:175], v[76:79]\n\t"
"ds_write_addtid_b32 v217 offset:31040\n\t"
"v_mfma_f32_16x16x32_bf16 v[60:63], a[0:3], a[156:159], v[60:63]\n\t"
"ds_write_addtid_b32 v194 offset:17568\n\t"
"v_cvt_pk_bf16_f32 v209, -v209, -v213\n\t"
"v_mfma_f32_16x16x32_bf16 v[60:63], a[4:7], a[172:175], v[60:63]\n\t"
"s_waitcnt vmcnt(23) lgkmcnt(10)\n\t"
"ds_write_addtid_b32 v218 offset:23744\n\t"
"v_mfma_f32_16x16x32_bf16 v[0:3], a[24:27], a[144:147], v[0:3]\n\t"
"s_nop 0\n\t"
"ds_write_addtid_b32 v193 offset:24864\n\t"
"ds_write_addtid_b32 v219 offset:32064\n\t"
"v_mfma_f32_16x16x32_bf16 v[0:3], a[28:31], a[160:163], v[0:3]\n\t"
"ds_write_addtid_b32 v209 offset:26912\n\t"
"v_mfma_f32_16x16x32_bf16 v[16:19], a[32:35], a[144:147], v[16:19]\n\t"
"ds_write_addtid_b32 v200 offset:20672\n\t"
"v_mfma_f32_16x16x32_bf16 v[16:19], a[36:39], a[160:163], v[16:19]\n\t"
"ds_read_b128 a[172:175], v224 offset:2816\n\t"
"v_mfma_f32_16x16x32_bf16 v[32:35], a[40:43], a[144:147], v[32:35]\n\t"
"ds_read_b128 a[156:159], v224 offset:768\n\t"
"v_mfma_f32_16x16x32_bf16 v[32:35], a[44:47], a[160:163], v[32:35]\n\t"
"v_mfma_f32_16x16x32_bf16 v[36:39], a[40:43], a[148:151], v[36:39]\n\t"
"ds_read_b128 a[168:171], v224 offset:2560\n\t"
"v_mfma_f32_16x16x32_bf16 v[36:39], a[44:47], a[164:167], v[36:39]\n\t"
"ds_read_b64_tr_b16 a[196:197], v250 offset:25344\n\t"
"v_mfma_f32_16x16x32_bf16 v[20:23], a[32:35], a[148:151], v[20:23]\n\t"
"ds_read_b64_tr_b16 a[204:205], v251 offset:25344\n\t"
"v_mfma_f32_16x16x32_bf16 v[20:23], a[36:39], a[164:167], v[20:23]\n\t"
"v_mfma_f32_16x16x32_bf16 v[4:7], a[24:27], a[148:151], v[4:7]\n\t"
"s_waitcnt lgkmcnt(2)\n\t"
"v_mfma_f32_16x16x32_bf16 v[4:7], a[28:31], a[164:167], v[4:7]\n\t"
"s_barrier\n\t"
"v_mfma_f32_16x16x32_bf16 v[8:11], a[24:27], a[152:155], v[8:11]\n\t"
"s_nop 0\n\t"
"ds_read_b128 a[144:147], v224 offset:17568\n\t"
"ds_read_b128 a[148:151], v224 offset:17824\n\t"
"v_mfma_f32_16x16x32_bf16 v[8:11], a[28:31], a[168:171], v[8:11]\n\t"
"buffer_load_dwordx4 a[136:139], off, s[92:95], s55 offset:2048 sc1\n\t"
"v_mfma_f32_16x16x32_bf16 v[24:27], a[32:35], a[152:155], v[24:27]\n\t"
"ds_read_b128 a[160:163], v224 offset:19616\n\t"
"v_mfma_f32_16x16x32_bf16 v[24:27], a[36:39], a[168:171], v[24:27]\n\t"
"ds_read_b128 a[164:167], v224 offset:19872\n\t"
"v_mfma_f32_16x16x32_bf16 v[40:43], a[40:43], a[152:155], v[40:43]\n\t"
"v_mfma_f32_16x16x32_bf16 v[40:43], a[44:47], a[168:171], v[40:43]\n\t"
"buffer_load_dwordx4 a[140:143], off, s[92:95], s55 offset:3072 sc1\n\t"
"v_mfma_f32_16x16x32_bf16 v[44:47], a[40:43], a[156:159], v[44:47]\n\t"
"ds_read_b128 a[168:171], v224 offset:20128\n\t"
"v_mfma_f32_16x16x32_bf16 v[44:47], a[44:47], a[172:175], v[44:47]\n\t"
"buffer_load_dwordx4 a[132:135], off, s[92:95], s55 offset:1024 sc1\n\t"
"v_mfma_f32_16x16x32_bf16 v[28:31], a[32:35], a[156:159], v[28:31]\n\t"
"ds_read_b128 a[152:155], v224 offset:18080\n\t"
"v_mfma_f32_16x16x32_bf16 v[28:31], a[36:39], a[172:175], v[28:31]\n\t"
"v_mfma_f32_16x16x32_bf16 v[12:15], a[24:27], a[156:159], v[12:15]\n\t"
"ds_read_b128 a[156:159], v224 offset:18336\n\t"
"v_mfma_f32_16x16x32_bf16 v[12:15], a[28:31], a[172:175], v[12:15]\n\t"
"s_waitcnt lgkmcnt(5)\n\t"
"buffer_load_dwordx4 a[128:131], off, s[92:95], s55 offset:0 sc1\n\t"
"v_mfma_f32_4x4x4_16b_bf16 v[200:203], a[220:221], a[178:179], 0\n\t"
"ds_read_b128 a[172:175], v224 offset:20384\n\t"
"v_mfma_f32_4x4x4_16b_bf16 v[204:207], a[220:221], a[186:187], 0\n\t"
"ds_read_b64_tr_b16 a[178:179], v250 offset:2304\n\t"
"v_mfma_f32_4x4x4_16b_bf16 v[216:219], a[220:221], a[194:195], 0\n\t"
"ds_read_b64_tr_b16 a[186:187], v251 offset:2304\n\t"
"v_mfma_f32_4x4x4_16b_bf16 v[220:223], a[220:221], a[202:203], 0\n\t"
"v_mfma_f32_4x4x4_16b_bf16 v[192:195], a[222:223], a[176:177], v[200:203]\n\t"
"ds_read_b64_tr_b16 a[194:195], v248 offset:2304\n\t"
"v_mfma_f32_4x4x4_16b_bf16 v[196:199], a[222:223], a[184:185], v[204:207]\n\t"
"ds_read_b64_tr_b16 a[202:203], v249 offset:2304\n\t"
"v_mfma_f32_4x4x4_16b_bf16 v[200:203], a[222:223], a[180:181], v[200:203]\n\t"
"ds_read_b64_tr_b16 a[176:177], v248 offset:0\n\t"
"v_mfma_f32_4x4x4_16b_bf16 v[204:207], a[222:223], a[188:189], v[204:207]\n\t"
"v_mfma_f32_4x4x4_16b_bf16 v[208:211], a[222:223], a[192:193], v[216:219]\n\t"
"ds_read_b64_tr_b16 a[184:185], v249 offset:0\n\t"
"v_mfma_f32_4x4x4_16b_bf16 v[212:215], a[222:223], a[200:201], v[220:223]\n\t"
"ds_read_b64_tr_b16 a[180:181], v248 offset:4608\n\t"
"v_mfma_f32_4x4x4_16b_bf16 v[216:219], a[222:223], a[196:197], v[216:219]\n\t"
"ds_read_b64_tr_b16 a[188:189], v249 offset:4608\n\t"
"v_mfma_f32_4x4x4_16b_bf16 v[220:223], a[222:223], a[204:205], v[220:223]\n\t"
"s_waitcnt vmcnt(21) lgkmcnt(7)\n\t"
"buffer_load_dwordx4 a[120:123], off, s[96:99], s54 offset:2048 sc1\n\t"
"v_mfma_f32_16x16x32_bf16 v[144:147], a[48:51], a[144:147], v[144:147]\n\t"
"ds_read_b64_tr_b16 a[192:193], v250 offset:0\n\t"
"v_cvt_pk_bf16_f32 v192, -v192, -v196\n\t"
"v_mfma_f32_16x16x32_bf16 v[144:147], a[52:55], a[160:163], v[144:147]\n\t"
"buffer_load_dwordx4 a[124:127], off, s[96:99], s54 offset:3072 sc1\n\t"
"v_mfma_f32_16x16x32_bf16 v[160:163], a[56:59], a[144:147], v[160:163]\n\t"
"ds_read_b64_tr_b16 a[200:201], v251 offset:0\n\t"
"v_cvt_pk_bf16_f32 v193, -v193, -v197\n\t"
"v_mfma_f32_16x16x32_bf16 v[160:163], a[60:63], a[160:163], v[160:163]\n\t"
"ds_write_addtid_b32 v193 offset:8320\n\t"
"v_cvt_pk_bf16_f32 v194, -v194, -v198\n\t"
"v_mfma_f32_16x16x32_bf16 v[176:179], a[64:67], a[144:147], v[176:179]\n\t"
"ds_read_b128 a[144:147], v224 offset:16544\n\t"
"v_cvt_pk_bf16_f32 v211, -v211, -v215\n\t"
"v_mfma_f32_16x16x32_bf16 v[176:179], a[68:71], a[160:163], v[176:179]\n\t"
"buffer_load_dwordx4 a[16:19], off, s[92:95], s41 offset:0 sc1\n\t"
"v_mfma_f32_16x16x32_bf16 v[180:183], a[64:67], a[148:151], v[180:183]\n\t"
"ds_read_b128 a[160:163], v224 offset:18592\n\t"
"v_cvt_pk_bf16_f32 v219, v219, v223\n\t"
"v_mfma_f32_16x16x32_bf16 v[180:183], a[68:71], a[164:167], v[180:183]\n\t"
"ds_write_addtid_b32 v194 offset:1024\n\t"
"v_cvt_pk_bf16_f32 v217, v217, v221\n\t"
"v_mfma_f32_16x16x32_bf16 v[164:167], a[56:59], a[148:151], v[164:167]\n\t"
"ds_write_addtid_b32 v192 offset:0\n\t"
"v_cvt_pk_bf16_f32 v209, -v209, -v213\n\t"
"v_mfma_f32_16x16x32_bf16 v[164:167], a[60:63], a[164:167], v[164:167]\n\t"
"buffer_load_dwordx4 a[20:23], off, s[92:95], s41 offset:1024 sc1\n\t"
"v_mfma_f32_16x16x32_bf16 v[148:151], a[48:51], a[148:151], v[148:151]\n\t"
"ds_read_b128 a[148:151], v224 offset:16800\n\t"
"v_cvt_pk_bf16_f32 v195, -v195, -v199\n\t"
"v_mfma_f32_16x16x32_bf16 v[148:151], a[52:55], a[164:167], v[148:151]\n\t"
"ds_write_addtid_b32 v211 offset:11392\n\t"
"v_cvt_pk_bf16_f32 v201, v201, v205\n\t"
"v_mfma_f32_16x16x32_bf16 v[152:155], a[48:51], a[152:155], v[152:155]\n\t"
"ds_read_b128 a[164:167], v224 offset:18848\n\t"
"v_cvt_pk_bf16_f32 v216, v216, v220\n\t"
"v_mfma_f32_16x16x32_bf16 v[152:155], a[52:55], a[168:171], v[152:155]\n\t"
"buffer_load_dwordx4 a[12:15], off, s[96:99], s40 offset:3072 sc1\n\t"
"v_mfma_f32_16x16x32_bf16 v[168:171], a[56:59], a[152:155], v[168:171]\n\t"
"ds_write_addtid_b32 v195 offset:9344\n\t"
"v_cvt_pk_bf16_f32 v210, -v210, -v214\n\t"
"v_mfma_f32_16x16x32_bf16 v[168:171], a[60:63], a[168:171], v[168:171]\n\t"
"ds_write_addtid_b32 v209 offset:10368\n\t"
"v_cvt_pk_bf16_f32 v218, v218, v222\n\t"
"v_mfma_f32_16x16x32_bf16 v[184:187], a[64:67], a[152:155], v[184:187]\n\t"
"ds_read_b128 a[152:155], v224 offset:17056\n\t"
"v_cvt_pk_bf16_f32 v200, v200, v204\n\t"
"v_mfma_f32_16x16x32_bf16 v[184:187], a[68:71], a[168:171], v[184:187]\n\t"
"buffer_load_dwordx4 a[8:11], off, s[96:99], s40 offset:2048 sc1\n\t"
"v_mfma_f32_16x16x32_bf16 v[188:191], a[64:67], a[156:159], v[188:191]\n\t"
"ds_read_b128 a[168:171], v224 offset:19104\n\t"
"v_cvt_pk_bf16_f32 v203, v203, v207\n\t"
"v_mfma_f32_16x16x32_bf16 v[188:191], a[68:71], a[172:175], v[188:191]\n\t"
"ds_write_addtid_b32 v217 offset:14496\n\t"
"v_cvt_pk_bf16_f32 v202, v202, v206\n\t"
"v_mfma_f32_16x16x32_bf16 v[172:175], a[56:59], a[156:159], v[172:175]\n\t"
"s_nop 0\n\t"
"ds_write_addtid_b32 v202 offset:5152\n\t"
"ds_write_addtid_b32 v216 offset:6176\n\t"
"v_mfma_f32_16x16x32_bf16 v[172:175], a[60:63], a[172:175], v[172:175]\n\t"
"buffer_load_dwordx4 a[0:3], off, s[96:99], s40 offset:0 sc1\n\t"
"v_mfma_f32_16x16x32_bf16 v[156:159], a[48:51], a[156:159], v[156:159]\n\t"
"s_nop 0\n\t"
"ds_write_addtid_b32 v210 offset:3072\n\t"
"ds_write_addtid_b32 v219 offset:15520\n\t"
"v_mfma_f32_16x16x32_bf16 v[156:159], a[52:55], a[172:175], v[156:159]\n\t"
"s_waitcnt vmcnt(22) lgkmcnt(9)\n\t"
"ds_write_addtid_b32 v203 offset:13472\n\t"
"v_mfma_f32_16x16x32_bf16 v[96:99], a[72:75], a[144:147], v[96:99]\n\t"
"ds_write_addtid_b32 v200 offset:4128\n\t"
"v_cvt_pk_bf16_f32 v208, -v208, -v212\n\t"
"v_mfma_f32_16x16x32_bf16 v[96:99], a[76:79], a[160:163], v[96:99]\n\t"
"ds_write_addtid_b32 v218 offset:7200\n\t"
"v_mfma_f32_16x16x32_bf16 v[112:115], a[80:83], a[144:147], v[112:115]\n\t"
"s_nop 0\n\t"
"ds_write_addtid_b32 v201 offset:12448\n\t"
"ds_write_addtid_b32 v208 offset:2048\n\t"
"v_mfma_f32_16x16x32_bf16 v[112:115], a[84:87], a[160:163], v[112:115]\n\t"
"buffer_load_dwordx4 a[4:7], off, s[96:99], s40 offset:1024 sc1\n\t"
"v_mfma_f32_16x16x32_bf16 v[128:131], a[88:91], a[144:147], v[128:131]\n\t"
"ds_read_b128 a[172:175], v224 offset:19360\n\t"
"v_mfma_f32_16x16x32_bf16 v[128:131], a[92:95], a[160:163], v[128:131]\n\t"
"buffer_load_dwordx4 a[40:43], off, s[92:95], s40 offset:2048 sc1\n\t"
"v_mfma_f32_16x16x32_bf16 v[132:135], a[88:91], a[148:151], v[132:135]\n\t"
"ds_read_b128 a[156:159], v224 offset:17312\n\t"
"v_mfma_f32_16x16x32_bf16 v[132:135], a[92:95], a[164:167], v[132:135]\n\t"
"ds_read_b64_tr_b16 a[196:197], v250 offset:4608\n\t"
"v_mfma_f32_16x16x32_bf16 v[116:119], a[80:83], a[148:151], v[116:119]\n\t"
"ds_read_b64_tr_b16 a[204:205], v251 offset:4608\n\t"
"v_mfma_f32_16x16x32_bf16 v[116:119], a[84:87], a[164:167], v[116:119]\n\t"
"buffer_load_dwordx4 a[44:47], off, s[92:95], s40 offset:3072 sc1\n\t"
"v_mfma_f32_16x16x32_bf16 v[100:103], a[72:75], a[148:151], v[100:103]\n\t"
"s_waitcnt lgkmcnt(2)\n\t"
"v_mfma_f32_16x16x32_bf16 v[100:103], a[76:79], a[164:167], v[100:103]\n\t"
"buffer_load_dwordx4 a[36:39], off, s[92:95], s40 offset:1024 sc1\n\t"
"s_barrier\n\t"
"v_mfma_f32_16x16x32_bf16 v[104:107], a[72:75], a[152:155], v[104:107]\n\t"
"ds_read_b128 a[144:147], v224 offset:1024\n\t"
"v_mfma_f32_16x16x32_bf16 v[104:107], a[76:79], a[168:171], v[104:107]\n\t"
"ds_read_b128 a[160:163], v224 offset:3072\n\t"
"v_mfma_f32_16x16x32_bf16 v[120:123], a[80:83], a[152:155], v[120:123]\n\t"
"ds_read_b128 a[148:151], v224 offset:1280\n\t"
"v_mfma_f32_16x16x32_bf16 v[120:123], a[84:87], a[168:171], v[120:123]\n\t"
"ds_read_b128 a[164:167], v224 offset:3328\n\t"
"v_mfma_f32_16x16x32_bf16 v[136:139], a[88:91], a[152:155], v[136:139]\n\t"
"v_mfma_f32_16x16x32_bf16 v[136:139], a[92:95], a[168:171], v[136:139]\n\t"
"buffer_load_dwordx4 a[32:35], off, s[92:95], s40 offset:0 sc1\n\t"
"v_mfma_f32_16x16x32_bf16 v[140:143], a[88:91], a[156:159], v[140:143]\n\t"
"ds_read_b128 a[168:171], v224 offset:3584\n\t"
"v_mfma_f32_16x16x32_bf16 v[140:143], a[92:95], a[172:175], v[140:143]\n\t"
"ds_read_b128 a[152:155], v224 offset:1536\n\t"
"v_mfma_f32_16x16x32_bf16 v[124:127], a[80:83], a[156:159], v[124:127]\n\t"
"v_mfma_f32_16x16x32_bf16 v[124:127], a[84:87], a[172:175], v[124:127]\n\t"
"buffer_load_dwordx4 a[24:27], off, s[96:99], 0 offset:2048 sc1\n\t"
"v_mfma_f32_16x16x32_bf16 v[108:111], a[72:75], a[156:159], v[108:111]\n\t"
"ds_read_b128 a[156:159], v224 offset:1792\n\t"
"v_mfma_f32_16x16x32_bf16 v[108:111], a[76:79], a[172:175], v[108:111]\n\t"
"s_waitcnt lgkmcnt(5)\n\t"
"ds_read_b128 a[172:175], v224 offset:3840\n\t"
"v_mfma_f32_4x4x4_16b_bf16 v[200:203], a[220:221], a[178:179], 0\n\t"
"v_mfma_f32_4x4x4_16b_bf16 v[204:207], a[220:221], a[186:187], 0\n\t"
"ds_read_b64_tr_b16 a[178:179], v250 offset:24192\n\t"
"v_mfma_f32_4x4x4_16b_bf16 v[216:219], a[220:221], a[194:195], 0\n\t"
"ds_read_b64_tr_b16 a[186:187], v251 offset:24192\n\t"
"v_mfma_f32_4x4x4_16b_bf16 v[220:223], a[220:221], a[202:203], 0\n\t"
"ds_read_b64_tr_b16 a[194:195], v248 offset:24192\n\t"
"v_mfma_f32_4x4x4_16b_bf16 v[192:195], a[222:223], a[176:177], v[200:203]\n\t"
"ds_read_b64_tr_b16 a[202:203], v249 offset:24192\n\t"
"v_mfma_f32_4x4x4_16b_bf16 v[196:199], a[222:223], a[184:185], v[204:207]\n\t"
"v_mfma_f32_4x4x4_16b_bf16 v[200:203], a[222:223], a[180:181], v[200:203]\n\t"
"ds_read_b64_tr_b16 a[176:177], v248 offset:21888\n\t"
"v_mfma_f32_4x4x4_16b_bf16 v[204:207], a[222:223], a[188:189], v[204:207]\n\t"
"ds_read_b64_tr_b16 a[184:185], v249 offset:21888\n\t"
"v_mfma_f32_4x4x4_16b_bf16 v[208:211], a[222:223], a[192:193], v[216:219]\n\t"
"ds_read_b64_tr_b16 a[180:181], v248 offset:26496\n\t"
"v_mfma_f32_4x4x4_16b_bf16 v[212:215], a[222:223], a[200:201], v[220:223]\n\t"
"ds_read_b64_tr_b16 a[188:189], v249 offset:26496\n\t"
"v_mfma_f32_4x4x4_16b_bf16 v[216:219], a[222:223], a[196:197], v[216:219]\n\t"
"ds_read_b64_tr_b16 a[192:193], v250 offset:21888\n\t"
"v_mfma_f32_4x4x4_16b_bf16 v[220:223], a[222:223], a[204:205], v[220:223]\n\t"
"s_waitcnt vmcnt(17) lgkmcnt(8)\n\t"
"buffer_load_dwordx4 a[28:31], off, s[96:99], 0 offset:3072 sc1\n\t"
"v_mfma_f32_16x16x32_bf16 v[48:51], a[96:99], a[144:147], v[48:51]\n\t"
"ds_read_b64_tr_b16 a[200:201], v251 offset:21888\n\t"
"v_cvt_pk_bf16_f32 v192, -v192, -v196\n\t"
"v_mfma_f32_16x16x32_bf16 v[48:51], a[100:103], a[160:163], v[48:51]\n\t"
"buffer_load_dwordx4 a[64:67], off, s[92:95], s68 offset:0 sc1\n\t"
"v_mfma_f32_16x16x32_bf16 v[64:67], a[104:107], a[144:147], v[64:67]\n\t"
"ds_read_b64_tr_b16 a[196:197], v250 offset:26496\n\t"
"v_cvt_pk_bf16_f32 v195, -v195, -v199\n\t"
"v_mfma_f32_16x16x32_bf16 v[64:67], a[108:111], a[160:163], v[64:67]\n\t"
"ds_write_addtid_b32 v192 offset:16544\n\t"
"v_cvt_pk_bf16_f32 v202, v202, v206\n\t"
"v_mfma_f32_16x16x32_bf16 v[80:83], a[112:115], a[144:147], v[80:83]\n\t"
"ds_read_b128 a[144:147], v224 offset:0\n\t"
"v_cvt_pk_bf16_f32 v193, -v193, -v197\n\t"
"v_mfma_f32_16x16x32_bf16 v[80:83], a[116:119], a[160:163], v[80:83]\n\t"
"buffer_load_dwordx4 a[68:71], off, s[92:95], s68 offset:1024 sc1\n\t"
"v_mfma_f32_16x16x32_bf16 v[84:87], a[112:115], a[148:151], v[84:87]\n\t"
"ds_read_b128 a[160:163], v224 offset:2048\n\t"
"v_cvt_pk_bf16_f32 v203, v203, v207\n\t"
"v_mfma_f32_16x16x32_bf16 v[84:87], a[116:119], a[164:167], v[84:87]\n\t"
"ds_write_addtid_b32 v202 offset:21696\n\t"
"v_cvt_pk_bf16_f32 v200, v200, v204\n\t"
"v_mfma_f32_16x16x32_bf16 v[68:71], a[104:107], a[148:151], v[68:71]\n\t"
"ds_write_addtid_b32 v200 offset:20672\n\t"
"v_mfma_f32_16x16x32_bf16 v[68:71], a[108:111], a[164:167], v[68:71]\n\t"
"buffer_load_dwordx4 a[60:63], off, s[96:99], s67 offset:3072 sc1\n\t"
"v_mfma_f32_16x16x32_bf16 v[52:55], a[96:99], a[148:151], v[52:55]\n\t"
"ds_read_b128 a[148:151], v224 offset:256\n\t"
"v_cvt_pk_bf16_f32 v217, v217, v221\n\t"
"v_mfma_f32_16x16x32_bf16 v[52:55], a[100:103], a[164:167], v[52:55]\n\t"
"ds_write_addtid_b32 v217 offset:31040\n\t"
"v_cvt_pk_bf16_f32 v216, v216, v220\n\t"
"v_mfma_f32_16x16x32_bf16 v[56:59], a[96:99], a[152:155], v[56:59]\n\t"
"ds_read_b128 a[164:167], v224 offset:2304\n\t"
"v_cvt_pk_bf16_f32 v211, -v211, -v215\n\t"
"v_mfma_f32_16x16x32_bf16 v[56:59], a[100:103], a[168:171], v[56:59]\n\t"
"buffer_load_dwordx4 a[56:59], off, s[96:99], s67 offset:2048 sc1\n\t"
"v_mfma_f32_16x16x32_bf16 v[72:75], a[104:107], a[152:155], v[72:75]\n\t"
"ds_write_addtid_b32 v211 offset:27936\n\t"
"v_cvt_pk_bf16_f32 v208, -v208, -v212\n\t"
"v_mfma_f32_16x16x32_bf16 v[72:75], a[108:111], a[168:171], v[72:75]\n\t"
"ds_write_addtid_b32 v216 offset:22720\n\t"
"v_cvt_pk_bf16_f32 v218, v218, v222\n\t"
"v_mfma_f32_16x16x32_bf16 v[88:91], a[112:115], a[152:155], v[88:91]\n\t"
"ds_read_b128 a[152:155], v224 offset:512\n\t"
"v_cvt_pk_bf16_f32 v209, -v209, -v213\n\t"
"v_mfma_f32_16x16x32_bf16 v[88:91], a[116:119], a[168:171], v[88:91]\n\t"
"buffer_load_dwordx4 a[48:51], off, s[96:99], s67 offset:0 sc1\n\t"
"v_mfma_f32_16x16x32_bf16 v[92:95], a[112:115], a[156:159], v[92:95]\n\t"
"ds_read_b128 a[168:171], v224 offset:2560\n\t"
"v_cvt_pk_bf16_f32 v201, v201, v205\n\t"
"v_mfma_f32_16x16x32_bf16 v[92:95], a[116:119], a[172:175], v[92:95]\n\t"
"ds_write_addtid_b32 v209 offset:26912\n\t"
"v_cvt_pk_bf16_f32 v210, -v210, -v214\n\t"
"v_mfma_f32_16x16x32_bf16 v[76:79], a[104:107], a[156:159], v[76:79]\n\t"
"s_nop 0\n\t"
"ds_write_addtid_b32 v193 offset:24864\n\t"
"ds_write_addtid_b32 v195 offset:25888\n\t"
"v_mfma_f32_16x16x32_bf16 v[76:79], a[108:111], a[172:175], v[76:79]\n\t"
"buffer_load_dwordx4 a[52:55], off, s[96:99], s67 offset:1024 sc1\n\t"
"v_mfma_f32_16x16x32_bf16 v[60:63], a[96:99], a[156:159], v[60:63]\n\t"
"s_nop 0\n\t"
"ds_write_addtid_b32 v203 offset:30016\n\t"
"ds_write_addtid_b32 v210 offset:19616\n\t"
"v_mfma_f32_16x16x32_bf16 v[60:63], a[100:103], a[172:175], v[60:63]\n\t"
"s_waitcnt vmcnt(18) lgkmcnt(9)\n\t"
"ds_write_addtid_b32 v208 offset:18592\n\t"
"v_cvt_pk_bf16_f32 v219, v219, v223\n\t"
"v_mfma_f32_16x16x32_bf16 v[0:3], a[120:123], a[144:147], v[0:3]\n\t"
"ds_write_addtid_b32 v219 offset:32064\n\t"
"v_cvt_pk_bf16_f32 v194, -v194, -v198\n\t"
"v_mfma_f32_16x16x32_bf16 v[0:3], a[124:127], a[160:163], v[0:3]\n\t"
"ds_write_addtid_b32 v194 offset:17568\n\t"
"v_mfma_f32_16x16x32_bf16 v[16:19], a[128:131], a[144:147], v[16:19]\n\t"
"s_nop 0\n\t"
"ds_write_addtid_b32 v201 offset:28992\n\t"
"ds_write_addtid_b32 v218 offset:23744\n\t"
"v_mfma_f32_16x16x32_bf16 v[16:19], a[132:135], a[160:163], v[16:19]\n\t"
"buffer_load_dwordx4 a[88:91], off, s[92:95], s67 offset:2048 sc1\n\t"
"v_mfma_f32_16x16x32_bf16 v[32:35], a[136:139], a[144:147], v[32:35]\n\t"
"ds_read_b128 a[156:159], v224 offset:768\n\t"
"v_mfma_f32_16x16x32_bf16 v[32:35], a[140:143], a[160:163], v[32:35]\n\t"
"buffer_load_dwordx4 a[92:95], off, s[92:95], s67 offset:3072 sc1\n\t"
"v_mfma_f32_16x16x32_bf16 v[36:39], a[136:139], a[148:151], v[36:39]\n\t"
"ds_read_b128 a[172:175], v224 offset:2816\n\t"
"v_mfma_f32_16x16x32_bf16 v[36:39], a[140:143], a[164:167], v[36:39]\n\t"
"buffer_load_dwordx4 a[84:87], off, s[92:95], s67 offset:1024 sc1\n\t"
"v_mfma_f32_16x16x32_bf16 v[20:23], a[128:131], a[148:151], v[20:23]\n\t"
"ds_read_b64_tr_b16 a[204:205], v251 offset:26496\n\t"
"v_mfma_f32_16x16x32_bf16 v[20:23], a[132:135], a[164:167], v[20:23]\n\t"
"v_mfma_f32_16x16x32_bf16 v[4:7], a[120:123], a[148:151], v[4:7]\n\t"
"buffer_load_dwordx4 a[80:83], off, s[92:95], s67 offset:0 sc1\n\t"
"v_mfma_f32_16x16x32_bf16 v[4:7], a[124:127], a[164:167], v[4:7]\n\t"
"s_waitcnt lgkmcnt(0)\n\t"
"s_barrier\n\t"
"v_mfma_f32_16x16x32_bf16 v[8:11], a[120:123], a[152:155], v[8:11]\n\t"
"s_nop 0\n\t"
"ds_read_b128 a[144:147], v224 offset:17568\n\t"
"ds_read_b128 a[160:163], v224 offset:19616\n\t"
"v_mfma_f32_16x16x32_bf16 v[8:11], a[124:127], a[168:171], v[8:11]\n\t"
"buffer_load_dwordx4 a[72:75], off, s[96:99], s66 offset:2048 sc1\n\t"
"v_mfma_f32_16x16x32_bf16 v[24:27], a[128:131], a[152:155], v[24:27]\n\t"
"ds_read_b128 a[148:151], v224 offset:17824\n\t"
"v_mfma_f32_16x16x32_bf16 v[24:27], a[132:135], a[168:171], v[24:27]\n\t"
"ds_read_b128 a[164:167], v224 offset:19872\n\t"
"v_mfma_f32_16x16x32_bf16 v[40:43], a[136:139], a[152:155], v[40:43]\n\t"
"ds_read_b128 a[228:231], v254 offset:39168\n\t"
"v_mfma_f32_16x16x32_bf16 v[40:43], a[140:143], a[168:171], v[40:43]\n\t"
"buffer_load_dwordx4 a[76:79], off, s[96:99], s66 offset:3072 sc1\n\t"
"v_mfma_f32_16x16x32_bf16 v[44:47], a[136:139], a[156:159], v[44:47]\n\t"
"ds_read_b128 a[152:155], v224 offset:18080\n\t"
"v_mfma_f32_16x16x32_bf16 v[44:47], a[140:143], a[172:175], v[44:47]\n\t"
"buffer_load_dwordx4 a[112:115], off, s[92:95], s53 offset:0 sc1\n\t"
"v_mfma_f32_16x16x32_bf16 v[28:31], a[128:131], a[156:159], v[28:31]\n\t"
"ds_read_b128 a[168:171], v224 offset:20128\n\t"
"v_mfma_f32_16x16x32_bf16 v[28:31], a[132:135], a[172:175], v[28:31]\n\t"
"v_mfma_f32_16x16x32_bf16 v[12:15], a[120:123], a[156:159], v[12:15]\n\t"
"ds_read_b128 a[156:159], v224 offset:18336\n\t"
"v_mfma_f32_16x16x32_bf16 v[12:15], a[124:127], a[172:175], v[12:15]\n\t"
"buffer_load_dwordx4 a[116:119], off, s[92:95], s53 offset:1024 sc1\n\t"
"v_mfma_f32_4x4x4_16b_bf16 v[200:203], a[220:221], a[178:179], 0\n\t"
"ds_read_b64_tr_b16 a[178:179], v250 offset:3456\n\t"
"v_mfma_f32_4x4x4_16b_bf16 v[204:207], a[220:221], a[186:187], 0\n\t"
"ds_read_b64_tr_b16 a[186:187], v251 offset:3456\n\t"
"v_mfma_f32_4x4x4_16b_bf16 v[216:219], a[220:221], a[194:195], 0\n\t"
"ds_read_b64_tr_b16 a[194:195], v248 offset:3456\n\t"
"v_mfma_f32_4x4x4_16b_bf16 v[220:223], a[220:221], a[202:203], 0\n\t"
"buffer_load_dwordx4 a[108:111], off, s[96:99], s52 offset:3072 sc1\n\t"
"v_mfma_f32_4x4x4_16b_bf16 v[192:195], a[222:223], a[176:177], v[200:203]\n\t"
"s_waitcnt lgkmcnt(5)\n\t"
"ds_write_b128 v255, a[228:231] offset:20736\n\t"
"v_mfma_f32_4x4x4_16b_bf16 v[196:199], a[222:223], a[184:185], v[204:207]\n\t"
"v_mfma_f32_4x4x4_16b_bf16 v[200:203], a[222:223], a[180:181], v[200:203]\n\t"
"v_mfma_f32_4x4x4_16b_bf16 v[204:207], a[222:223], a[188:189], v[204:207]\n\t"
"v_mfma_f32_4x4x4_16b_bf16 v[208:211], a[222:223], a[192:193], v[216:219]\n\t"
"ds_read_b128 a[172:175], v224 offset:20384\n\t"
"v_mfma_f32_4x4x4_16b_bf16 v[212:215], a[222:223], a[200:201], v[220:223]\n\t"
"ds_read_b64_tr_b16 a[202:203], v249 offset:3456\n\t"
"v_mfma_f32_4x4x4_16b_bf16 v[216:219], a[222:223], a[196:197], v[216:219]\n\t"
"ds_read_b64_tr_b16 a[176:177], v248 offset:1152\n\t"
"v_mfma_f32_4x4x4_16b_bf16 v[220:223], a[222:223], a[204:205], v[220:223]\n\t"
"s_waitcnt vmcnt(21) lgkmcnt(5)\n\t"
"buffer_load_dwordx4 a[104:107], off, s[96:99], s52 offset:2048 sc1\n\t"
"v_mfma_f32_16x16x32_bf16 v[48:51], a[0:3], a[144:147], v[48:51]\n\t"
"s_nop 0\n\t"
"ds_read_b64_tr_b16 a[184:185], v249 offset:1152\n\t"
"v_cvt_pk_bf16_f32 v192, -v192, -v196\n\t"
"v_mfma_f32_16x16x32_bf16 v[48:51], a[4:7], a[160:163], v[48:51]\n\t"
"s_nop 0\n\t"
"ds_write_addtid_b32 v192 offset:0\n\t"
"v_cvt_pk_bf16_f32 v193, -v193, -v197\n\t"
"v_mfma_f32_16x16x32_bf16 v[64:67], a[8:11], a[144:147], v[64:67]\n\t"
"s_nop 0\n\t"
"ds_write_addtid_b32 v193 offset:8320\n\t"
"v_cvt_pk_bf16_f32 v195, -v195, -v199\n\t"
"v_mfma_f32_16x16x32_bf16 v[64:67], a[12:15], a[160:163], v[64:67]\n\t"
"buffer_load_dwordx4 a[96:99], off, s[96:99], s52 offset:0 sc1\n\t"
"v_mfma_f32_16x16x32_bf16 v[80:83], a[16:19], a[144:147], v[80:83]\n\t"
"s_nop 0\n\t"
"ds_read_b128 a[144:147], v224 offset:16544\n\t"
"v_cvt_pk_bf16_f32 v201, v201, v205\n\t"
"v_mfma_f32_16x16x32_bf16 v[80:83], a[20:23], a[160:163], v[80:83]\n\t"
"s_nop 0\n\t"
"ds_write_addtid_b32 v201 offset:12448\n\t"
"v_cvt_pk_bf16_f32 v194, -v194, -v198\n\t"
"v_mfma_f32_16x16x32_bf16 v[84:87], a[16:19], a[148:151], v[84:87]\n\t"
"s_nop 0\n\t"
"ds_read_b128 a[160:163], v224 offset:18592\n\t"
"v_cvt_pk_bf16_f32 v209, -v209, -v213\n\t"
"v_mfma_f32_16x16x32_bf16 v[84:87], a[20:23], a[164:167], v[84:87]\n\t"
"buffer_load_dwordx4 a[100:103], off, s[96:99], s52 offset:1024 sc1\n\t"
"v_mfma_f32_16x16x32_bf16 v[68:71], a[8:11], a[148:151], v[68:71]\n\t"
"s_nop 0\n\t"
"ds_write_addtid_b32 v195 offset:9344\n\t"
"v_cvt_pk_bf16_f32 v211, -v211, -v215\n\t"
"v_mfma_f32_16x16x32_bf16 v[68:71], a[12:15], a[164:167], v[68:71]\n\t"
"s_nop 0\n\t"
"ds_write_addtid_b32 v194 offset:1024\n\t"
"v_cvt_pk_bf16_f32 v210, -v210, -v214\n\t"
"v_mfma_f32_16x16x32_bf16 v[52:55], a[0:3], a[148:151], v[52:55]\n\t"
"s_nop 0\n\t"
"ds_read_b128 a[148:151], v224 offset:16800\n\t"
"v_cvt_pk_bf16_f32 v202, v202, v206\n\t"
"v_mfma_f32_16x16x32_bf16 v[52:55], a[4:7], a[164:167], v[52:55]\n\t"
"buffer_load_dwordx4 a[136:139], off, s[92:95], s52 offset:2048 sc1\n\t"
"v_mfma_f32_16x16x32_bf16 v[56:59], a[0:3], a[152:155], v[56:59]\n\t"
"s_nop 0\n\t"
"ds_read_b128 a[164:167], v224 offset:18848\n\t"
"v_cvt_pk_bf16_f32 v200, v200, v204\n\t"
"v_mfma_f32_16x16x32_bf16 v[56:59], a[4:7], a[168:171], v[56:59]\n\t"
"s_nop 0\n\t"
"ds_write_addtid_b32 v211 offset:11392\n\t"
"v_cvt_pk_bf16_f32 v219, v219, v223\n\t"
"v_mfma_f32_16x16x32_bf16 v[72:75], a[8:11], a[152:155], v[72:75]\n\t"
"s_nop 0\n\t"
"ds_write_addtid_b32 v210 offset:3072\n\t"
"v_cvt_pk_bf16_f32 v217, v217, v221\n\t"
"v_mfma_f32_16x16x32_bf16 v[72:75], a[12:15], a[168:171], v[72:75]\n\t"
"s_nop 0\n\t"
"ds_write_addtid_b32 v200 offset:4128\n\t"
"buffer_load_dwordx4 a[140:143], off, s[92:95], s52 offset:3072 sc1\n\t"
"v_mfma_f32_16x16x32_bf16 v[88:91], a[16:19], a[152:155], v[88:91]\n\t"
"s_nop 0\n\t"
"ds_write_addtid_b32 v202 offset:5152\n\t"
"v_cvt_pk_bf16_f32 v203, v203, v207\n\t"
"v_mfma_f32_16x16x32_bf16 v[88:91], a[20:23], a[168:171], v[88:91]\n\t"
"s_waitcnt lgkmcnt(8)\n\t"
"ds_write_addtid_b32 v209 offset:10368\n\t"
"ds_write_addtid_b32 v217 offset:14496\n\t"
"v_mfma_f32_16x16x32_bf16 v[92:95], a[16:19], a[156:159], v[92:95]\n\t"
"s_nop 0\n\t"
"ds_write_addtid_b32 v203 offset:13472\n\t"
"v_cvt_pk_bf16_f32 v216, v216, v220\n\t"
"v_mfma_f32_16x16x32_bf16 v[92:95], a[20:23], a[172:175], v[92:95]\n\t"
"s_nop 0\n\t"
"ds_write_addtid_b32 v219 offset:15520\n\t"
"v_cvt_pk_bf16_f32 v218, v218, v222\n\t"
"v_mfma_f32_16x16x32_bf16 v[76:79], a[8:11], a[156:159], v[76:79]\n\t"
"s_nop 0\n\t"
"ds_write_addtid_b32 v218 offset:7200\n\t"
"v_cvt_pk_bf16_f32 v208, -v208, -v212\n\t"
"v_mfma_f32_16x16x32_bf16 v[76:79], a[12:15], a[172:175], v[76:79]\n\t"
"s_nop 0\n\t"
"ds_write_addtid_b32 v208 offset:2048\n\t"
"ds_write_addtid_b32 v216 offset:6176\n\t"
"v_mfma_f32_16x16x32_bf16 v[60:63], a[0:3], a[156:159], v[60:63]\n\t"
"s_add_u32 m0, s26, 124544\n\t"
"ds_read_b128 a[152:155], v224 offset:17056\n\t"
"v_mfma_f32_16x16x32_bf16 v[60:63], a[4:7], a[172:175], v[60:63]\n\t"
"s_waitcnt vmcnt(20) lgkmcnt(11)\n\t"
"buffer_load_dwordx4 v236, s[12:15], s22 offen offset:128 nt lds\n\t"
"v_mfma_f32_16x16x32_bf16 v[0:3], a[24:27], a[144:147], v[0:3]\n\t"
"s_nop 0\n\t"
"ds_read_b128 a[156:159], v224 offset:17312\n\t"
"ds_read_b128 a[172:175], v224 offset:19360\n\t"
"v_mfma_f32_16x16x32_bf16 v[0:3], a[28:31], a[160:163], v[0:3]\n\t"
"buffer_load_dwordx4 v237, s[12:15], s22 offen offset:1152 nt lds\n\t"
"v_mfma_f32_16x16x32_bf16 v[16:19], a[32:35], a[144:147], v[16:19]\n\t"
"s_nop 0\n\t"
"ds_read_b128 a[168:171], v224 offset:19104\n\t"
"v_mfma_f32_16x16x32_bf16 v[16:19], a[36:39], a[160:163], v[16:19]\n\t"
"buffer_load_dwordx4 v238, s[12:15], s22 offen offset:2176 nt lds\n\t"
"v_mfma_f32_16x16x32_bf16 v[32:35], a[40:43], a[144:147], v[32:35]\n\t"
"s_nop 0\n\t"
"ds_read_b64_tr_b16 a[180:181], v248 offset:5760\n\t"
"ds_read_b64_tr_b16 a[188:189], v249 offset:5760\n\t"
"v_mfma_f32_16x16x32_bf16 v[32:35], a[44:47], a[160:163], v[32:35]\n\t"
"buffer_load_dwordx4 v239, s[12:15], s22 offen offset:3200 nt lds\n\t"
"v_mfma_f32_16x16x32_bf16 v[36:39], a[40:43], a[148:151], v[36:39]\n\t"
"s_nop 0\n\t"
"ds_read_b64_tr_b16 a[192:193], v250 offset:1152\n\t"
"ds_read_b64_tr_b16 a[200:201], v251 offset:1152\n\t"
"v_mfma_f32_16x16x32_bf16 v[36:39], a[44:47], a[164:167], v[36:39]\n\t"
"buffer_load_dwordx4 a[132:135], off, s[92:95], s52 offset:1024 sc1\n\t"
"v_mfma_f32_16x16x32_bf16 v[20:23], a[32:35], a[148:151], v[20:23]\n\t"
"s_nop 0\n\t"
"ds_read_b64_tr_b16 a[196:197], v250 offset:5760\n\t"
"ds_read_b64_tr_b16 a[204:205], v251 offset:5760\n\t"
"v_mfma_f32_16x16x32_bf16 v[20:23], a[36:39], a[164:167], v[20:23]\n\t"
"s_mov_b64 exec, s[36:37]\n\t"
"buffer_load_dwordx4 v240, s[12:15], s22 offen offset:3712 nt lds\n\t"
"s_mov_b64 exec, -1\n\t"
"v_mfma_f32_16x16x32_bf16 v[4:7], a[24:27], a[148:151], v[4:7]\n\t"
"Lpad5_%=:\n\t"
"s_cbranch_execz Lret0_%=\n\t"
"s_lshl_b32 m0, s73, 8\n\t"
"v_mfma_f32_16x16x32_bf16 v[4:7], a[28:31], a[164:167], v[4:7]\n\t"
"s_waitcnt lgkmcnt(0)\n\t"
"s_barrier\n\t"
"v_mfma_f32_16x16x32_bf16 v[8:11], a[24:27], a[152:155], v[8:11]\n\t"
"ds_read_b128 a[144:147], v224 offset:1024\n\t"
"v_add_u32 v248, 33280, v227\n\t"
"v_mfma_f32_16x16x32_bf16 v[8:11], a[28:31], a[168:171], v[8:11]\n\t"
"ds_read_b128 a[160:163], v224 offset:3072\n\t"
"v_xor_b32 v249, 32, v248\n\t"
"v_mfma_f32_16x16x32_bf16 v[24:27], a[32:35], a[152:155], v[24:27]\n\t"
"ds_read_b128 a[148:151], v224 offset:1280\n\t"
"v_xor_b32 v250, 64, v248\n\t"
"v_mfma_f32_16x16x32_bf16 v[24:27], a[36:39], a[168:171], v[24:27]\n\t"
"ds_read_b128 a[164:167], v224 offset:3328\n\t"
"v_xor_b32 v251, 96, v248\n\t"
"v_mfma_f32_16x16x32_bf16 v[40:43], a[40:43], a[152:155], v[40:43]\n\t"
"ds_read_b64_tr_b16 a[182:183], v249 offset:41984\n\t"
"v_mfma_f32_16x16x32_bf16 v[40:43], a[44:47], a[168:171], v[40:43]\n\t"
"ds_read_b128 a[152:155], v224 offset:1536\n\t"
"v_mfma_f32_16x16x32_bf16 v[44:47], a[40:43], a[156:159], v[44:47]\n\t"
"ds_read_b128 a[168:171], v224 offset:3584\n\t"
"v_mfma_f32_16x16x32_bf16 v[44:47], a[44:47], a[172:175], v[44:47]\n\t"
"ds_read_b64_tr_b16 a[190:191], v251 offset:41984\n\t"
"v_mfma_f32_16x16x32_bf16 v[28:31], a[32:35], a[156:159], v[28:31]\n\t"
"ds_read_b64_tr_b16 a[198:199], v249 offset:21248\n\t"
"v_mfma_f32_16x16x32_bf16 v[28:31], a[36:39], a[172:175], v[28:31]\n\t"
"ds_read_b64_tr_b16 a[206:207], v251 offset:21248\n\t"
"v_mfma_f32_16x16x32_bf16 v[12:15], a[24:27], a[156:159], v[12:15]\n\t"
"ds_read_b128 a[156:159], v224 offset:1792\n\t"
"v_mfma_f32_16x16x32_bf16 v[12:15], a[28:31], a[172:175], v[12:15]\n\t"
"ds_read_b128 a[172:175], v224 offset:3840\n\t"
"v_mfma_f32_4x4x4_16b_bf16 v[200:203], a[220:221], a[178:179], 0\n\t"
"ds_read_b64_tr_b16 a[178:179], v248 offset:41984\n\t"
"v_mfma_f32_4x4x4_16b_bf16 v[204:207], a[220:221], a[186:187], 0\n\t"
"ds_read_b64_tr_b16 a[186:187], v250 offset:41984\n\t"
"v_mfma_f32_4x4x4_16b_bf16 v[216:219], a[220:221], a[194:195], 0\n\t"
"ds_read_b64_tr_b16 a[194:195], v248 offset:21248\n\t"
"v_mfma_f32_4x4x4_16b_bf16 v[220:223], a[220:221], a[202:203], 0\n\t"
"ds_read_b64_tr_b16 a[202:203], v250 offset:21248\n\t"
"v_mfma_f32_4x4x4_16b_bf16 v[192:195], a[222:223], a[176:177], v[200:203]\n\t"
"ds_read_b64_tr_b16 a[176:177], v248 offset:41472\n\t"
"v_mfma_f32_4x4x4_16b_bf16 v[196:199], a[222:223], a[184:185], v[204:207]\n\t"
".p2align 3\n\t"
"ds_read_b64_tr_b16 a[184:185], v250 offset:41472\n\t"
"v_pk_add_f32 v[0:1], v[0:1], v[48:49]\n\t"
"v_pk_add_f32 v[2:3], v[2:3], v[50:51]\n\t"
"v_pk_fma_f32 v[48:49], v[48:49], s[38:39], v[0:1]\n\t"
"v_pk_fma_f32 v[50:51], v[50:51], s[38:39], v[2:3]\n\t"
"v_pk_add_f32 v[16:17], v[16:17], v[64:65]\n\t"
"v_pk_add_f32 v[18:19], v[18:19], v[66:67]\n\t"
"v_pk_fma_f32 v[64:65], v[64:65], s[38:39], v[16:17]\n\t"
"v_pk_fma_f32 v[66:67], v[66:67], s[38:39], v[18:19]\n\t"
"v_pk_add_f32 v[32:33], v[32:33], v[80:81]\n\t"
"v_pk_add_f32 v[34:35], v[34:35], v[82:83]\n\t"
"v_pk_fma_f32 v[80:81], v[80:81], s[38:39], v[32:33]\n\t"
"v_pk_fma_f32 v[82:83], v[82:83], s[38:39], v[34:35]\n\t"
"v_pk_add_f32 v[36:37], v[36:37], v[84:85]\n\t"
"v_pk_add_f32 v[38:39], v[38:39], v[86:87]\n\t"
"v_pk_fma_f32 v[84:85], v[84:85], s[38:39], v[36:37]\n\t"
"v_pk_fma_f32 v[86:87], v[86:87], s[38:39], v[38:39]\n\t"
"v_pk_add_f32 v[20:21], v[20:21], v[68:69]\n\t"
"v_pk_add_f32 v[22:23], v[22:23], v[70:71]\n\t"
"v_pk_fma_f32 v[68:69], v[68:69], s[38:39], v[20:21]\n\t"
"v_pk_fma_f32 v[70:71], v[70:71], s[38:39], v[22:23]\n\t"
"v_pk_add_f32 v[4:5], v[4:5], v[52:53]\n\t"
"v_pk_add_f32 v[6:7], v[6:7], v[54:55]\n\t"
"v_pk_fma_f32 v[52:53], v[52:53], s[38:39], v[4:5]\n\t"
"v_pk_fma_f32 v[54:55], v[54:55], s[38:39], v[6:7]\n\t"
"v_pk_add_f32 v[8:9], v[8:9], v[56:57]\n\t"
"v_pk_add_f32 v[10:11], v[10:11], v[58:59]\n\t"
"v_pk_fma_f32 v[56:57], v[56:57], s[38:39], v[8:9]\n\t"
"v_pk_fma_f32 v[58:59], v[58:59], s[38:39], v[10:11]\n\t"
"v_pk_add_f32 v[24:25], v[24:25], v[72:73]\n\t"
"v_pk_add_f32 v[26:27], v[26:27], v[74:75]\n\t"
"v_pk_fma_f32 v[72:73], v[72:73], s[38:39], v[24:25]\n\t"
"v_pk_fma_f32 v[74:75], v[74:75], s[38:39], v[26:27]\n\t"
"v_pk_add_f32 v[40:41], v[40:41], v[88:89]\n\t"
"v_pk_add_f32 v[42:43], v[42:43], v[90:91]\n\t"
"v_pk_fma_f32 v[88:89], v[88:89], s[38:39], v[40:41]\n\t"
"v_pk_fma_f32 v[90:91], v[90:91], s[38:39], v[42:43]\n\t"
"v_pk_add_f32 v[44:45], v[44:45], v[92:93]\n\t"
"v_pk_add_f32 v[46:47], v[46:47], v[94:95]\n\t"
"v_pk_fma_f32 v[92:93], v[92:93], s[38:39], v[44:45]\n\t"
"v_pk_fma_f32 v[94:95], v[94:95], s[38:39], v[46:47]\n\t"
"v_pk_add_f32 v[28:29], v[28:29], v[76:77]\n\t"
"v_pk_add_f32 v[30:31], v[30:31], v[78:79]\n\t"
"v_pk_fma_f32 v[76:77], v[76:77], s[38:39], v[28:29]\n\t"
"v_pk_fma_f32 v[78:79], v[78:79], s[38:39], v[30:31]\n\t"
"v_pk_add_f32 v[12:13], v[12:13], v[60:61]\n\t"
"v_pk_add_f32 v[14:15], v[14:15], v[62:63]\n\t"
"v_pk_fma_f32 v[60:61], v[60:61], s[38:39], v[12:13]\n\t"
"v_pk_fma_f32 v[62:63], v[62:63], s[38:39], v[14:15]\n\t"
"v_cvt_pk_bf16_f32 v4, v4, v5\n\t"
"v_mfma_f32_4x4x4_16b_bf16 v[200:203], a[222:223], a[180:181], v[200:203]\n\t"
"ds_read_b64_tr_b16 a[180:181], v249 offset:41472\n\t"
"v_mfma_f32_4x4x4_16b_bf16 v[204:207], a[222:223], a[188:189], v[204:207]\n\t"
"ds_read_b64_tr_b16 a[188:189], v251 offset:41472\n\t"
"v_mfma_f32_4x4x4_16b_bf16 v[208:211], a[222:223], a[192:193], v[216:219]\n\t"
"v_cvt_pk_bf16_f32 v0, v0, v1\n\t"
"v_cvt_pk_bf16_f32 v1, v2, v3\n\t"
"v_cvt_pk_bf16_f32 v5, v6, v7\n\t"
"v_cvt_pk_bf16_f32 v24, v24, v25\n\t"
"v_mfma_f32_4x4x4_16b_bf16 v[212:215], a[222:223], a[200:201], v[220:223]\n\t"
"ds_write2st64_b64 v228, v[0:1], v[4:5] offset0:40 offset1:53\n\t"
"v_mfma_f32_4x4x4_16b_bf16 v[216:219], a[222:223], a[196:197], v[216:219]\n\t"
"v_cvt_pk_bf16_f32 v16, v16, v17\n\t"
"v_mfma_f32_4x4x4_16b_bf16 v[220:223], a[222:223], a[204:205], v[220:223]\n\t"
"s_waitcnt vmcnt(20) lgkmcnt(7)\n\t"
"v_cvt_pk_bf16_f32 v17, v18, v19\n\t"
"v_cvt_pk_bf16_f32 v20, v20, v21\n\t"
"v_mfma_f32_16x16x32_bf16 v[144:147], a[48:51], a[144:147], v[144:147]\n\t"
"v_cvt_pk_bf16_f32 v21, v22, v23\n\t"
"v_cvt_pk_bf16_f32 v28, v28, v29\n\t"
"v_mfma_f32_16x16x32_bf16 v[144:147], a[52:55], a[160:163], v[144:147]\n\t"
"ds_write2st64_b64 v246, v[16:17], v[20:21] offset0:40 offset1:53\n\t"
"v_mfma_f32_16x16x32_bf16 v[160:163], a[56:59], a[144:147], v[160:163]\n\t"
"v_cvt_pk_bf16_f32 v32, v32, v33\n\t"
"v_cvt_pk_bf16_f32 v29, v30, v31\n\t"
"v_mfma_f32_16x16x32_bf16 v[160:163], a[60:63], a[160:163], v[160:163]\n\t"
"v_cvt_pk_bf16_f32 v33, v34, v35\n\t"
"v_cvt_pk_bf16_f32 v25, v26, v27\n\t"
"v_mfma_f32_16x16x32_bf16 v[176:179], a[64:67], a[144:147], v[176:179]\n\t"
"v_cvt_pk_bf16_f32 v195, -v195, -v199\n\t"
"v_cvt_pk_bf16_f32 v12, v12, v13\n\t"
"v_mfma_f32_16x16x32_bf16 v[176:179], a[68:71], a[160:163], v[176:179]\n\t"
"s_nop 0\n\t"
"ds_read_b128 a[144:147], v224 offset:0\n\t"
"ds_read_b128 a[160:163], v224 offset:2048\n\t"
"ds_write2st64_b64 v246, v[24:25], v[28:29] offset0:66 offset1:79\n\t"
"v_cvt_pk_bf16_f32 v40, v40, v41\n\t"
"v_mfma_f32_16x16x32_bf16 v[180:183], a[64:67], a[148:151], v[180:183]\n\t"
"v_cvt_pk_bf16_f32 v208, -v208, -v212\n\t"
"v_cvt_pk_bf16_f32 v36, v36, v37\n\t"
"v_mfma_f32_16x16x32_bf16 v[180:183], a[68:71], a[164:167], v[180:183]\n\t"
"v_cvt_pk_bf16_f32 v216, v216, v220\n\t"
"v_cvt_pk_bf16_f32 v37, v38, v39\n\t"
"v_mfma_f32_16x16x32_bf16 v[164:167], a[56:59], a[148:151], v[164:167]\n\t"
"v_cvt_pk_bf16_f32 v8, v8, v9\n\t"
"v_cvt_pk_bf16_f32 v44, v44, v45\n\t"
"v_mfma_f32_16x16x32_bf16 v[164:167], a[60:63], a[164:167], v[164:167]\n\t"
"ds_write_addtid_b32 v195 offset:25888\n\t"
"ds_write2st64_b64 v247, v[32:33], v[36:37] offset0:40 offset1:53\n\t"
"v_mfma_f32_16x16x32_bf16 v[148:151], a[48:51], a[148:151], v[148:151]\n\t"
"v_cvt_pk_bf16_f32 v9, v10, v11\n\t"
"v_cvt_pk_bf16_f32 v45, v46, v47\n\t"
"v_mfma_f32_16x16x32_bf16 v[148:151], a[52:55], a[164:167], v[148:151]\n\t"
"buffer_load_dwordx4 a[128:131], off, s[92:95], s52 offset:0 sc1\n\t"
"v_mfma_f32_16x16x32_bf16 v[152:155], a[48:51], a[152:155], v[152:155]\n\t"
"v_cvt_pk_bf16_f32 v202, v202, v206\n\t"
"v_cvt_pk_bf16_f32 v13, v14, v15\n\t"
"v_mfma_f32_16x16x32_bf16 v[152:155], a[52:55], a[168:171], v[152:155]\n\t"
"s_nop 0\n\t"
"ds_read_b128 a[148:151], v224 offset:256\n\t"
"ds_read_b128 a[164:167], v224 offset:2304\n\t"
"ds_write2st64_b64 v228, v[8:9], v[12:13] offset0:66 offset1:79\n\t"
"v_mfma_f32_16x16x32_bf16 v[168:171], a[56:59], a[152:155], v[168:171]\n\t"
"v_cvt_pk_bf16_f32 v217, v217, v221\n\t"
"v_cvt_pk_bf16_f32 v41, v42, v43\n\t"
"v_mfma_f32_16x16x32_bf16 v[168:171], a[60:63], a[168:171], v[168:171]\n\t"
"v_cvt_pk_bf16_f32 v193, -v193, -v197\n\t"
"v_cvt_pk_bf16_f32 v200, v200, v204\n\t"
"v_mfma_f32_16x16x32_bf16 v[184:187], a[64:67], a[152:155], v[184:187]\n\t"
"v_cvt_pk_bf16_f32 v210, -v210, -v214\n\t"
"v_cvt_pk_bf16_f32 v219, v219, v223\n\t"
"v_mfma_f32_16x16x32_bf16 v[184:187], a[68:71], a[168:171], v[184:187]\n\t"
"ds_write_addtid_b32 v202 offset:21696\n\t"
"ds_write2st64_b64 v247, v[40:41], v[44:45] offset0:66 offset1:79\n\t"
"v_mfma_f32_16x16x32_bf16 v[188:191], a[64:67], a[156:159], v[188:191]\n\t"
"v_cvt_pk_bf16_f32 v201, v201, v205\n\t"
"v_cvt_pk_bf16_f32 v218, v218, v222\n\t"
"v_mfma_f32_16x16x32_bf16 v[188:191], a[68:71], a[172:175], v[188:191]\n\t"
"v_cvt_pk_bf16_f32 v194, -v194, -v198\n\t"
"v_cvt_pk_bf16_f32 v203, v203, v207\n\t"
"v_mfma_f32_16x16x32_bf16 v[172:175], a[56:59], a[156:159], v[172:175]\n\t"
"v_cvt_pk_bf16_f32 v209, -v209, -v213\n\t"
"v_cvt_pk_bf16_f32 v211, -v211, -v215\n\t"
"v_mfma_f32_16x16x32_bf16 v[172:175], a[60:63], a[172:175], v[172:175]\n\t"
"s_nop 0\n\t"
"ds_write_addtid_b32 v193 offset:24864\n\t"
"ds_write_addtid_b32 v201 offset:28992\n\t"
"ds_write_addtid_b32 v203 offset:30016\n\t"
"ds_write_addtid_b32 v209 offset:26912\n\t"
"ds_write_addtid_b32 v210 offset:19616\n\t"
"v_mfma_f32_16x16x32_bf16 v[156:159], a[48:51], a[156:159], v[156:159]\n\t"
"s_nop 0\n\t"
"ds_write_addtid_b32 v208 offset:18592\n\t"
"ds_write_addtid_b32 v216 offset:22720\n\t"
"v_mfma_f32_16x16x32_bf16 v[156:159], a[52:55], a[172:175], v[156:159]\n\t"
"s_waitcnt vmcnt(15) lgkmcnt(9)\n\t"
"ds_write_addtid_b32 v218 offset:23744\n\t"
"v_cvt_pk_bf16_f32 v192, -v192, -v196\n\t"
"v_mfma_f32_16x16x32_bf16 v[96:99], a[72:75], a[144:147], v[96:99]\n\t"
"s_nop 0\n\t"
"ds_write_addtid_b32 v217 offset:31040\n\t"
"ds_write_addtid_b32 v219 offset:32064\n\t"
"v_mfma_f32_16x16x32_bf16 v[96:99], a[76:79], a[160:163], v[96:99]\n\t"
"s_nop 0\n\t"
"ds_write_addtid_b32 v192 offset:16544\n\t"
"ds_write_addtid_b32 v200 offset:20672\n\t"
"v_mfma_f32_16x16x32_bf16 v[112:115], a[80:83], a[144:147], v[112:115]\n\t"
"s_nop 0\n\t"
"ds_write_addtid_b32 v194 offset:17568\n\t"
"ds_write_addtid_b32 v211 offset:27936\n\t"
"v_mfma_f32_16x16x32_bf16 v[112:115], a[84:87], a[160:163], v[112:115]\n\t"
"buffer_load_dwordx4 a[120:123], off, s[96:99], s51 offset:2048 sc1\n\t"
"v_mfma_f32_16x16x32_bf16 v[128:131], a[88:91], a[144:147], v[128:131]\n\t"
"s_nop 0\n\t"
"ds_read_b128 a[168:171], v224 offset:2560\n\t"
"ds_read_b128 a[156:159], v224 offset:768\n\t"
"v_mfma_f32_16x16x32_bf16 v[128:131], a[92:95], a[160:163], v[128:131]\n\t"
"buffer_load_dwordx4 a[124:127], off, s[96:99], s51 offset:3072 sc1\n\t"
"v_mfma_f32_16x16x32_bf16 v[132:135], a[88:91], a[148:151], v[132:135]\n\t"
"s_nop 0\n\t"
"ds_read_b128 a[152:155], v224 offset:512\n\t"
"ds_read_b128 a[172:175], v224 offset:2816\n\t"
"v_mfma_f32_16x16x32_bf16 v[132:135], a[92:95], a[164:167], v[132:135]\n\t"
"s_nop 0\n\t"
"ds_read_b64_tr_b16 a[192:193], v248 offset:20736\n\t"
"ds_read_b64_tr_b16 a[200:201], v250 offset:20736\n\t"
"v_mfma_f32_16x16x32_bf16 v[116:119], a[80:83], a[148:151], v[116:119]\n\t"
"s_nop 0\n\t"
"ds_read_b64_tr_b16 a[196:197], v249 offset:20736\n\t"
"ds_read_b64_tr_b16 a[204:205], v251 offset:20736\n\t"
"v_mfma_f32_16x16x32_bf16 v[116:119], a[84:87], a[164:167], v[116:119]\n\t"
"buffer_load_dwordx4 a[16:19], off, s[28:31], s69 offset:0 sc1\n\t"
"v_mfma_f32_16x16x32_bf16 v[100:103], a[72:75], a[148:151], v[100:103]\n\t"
"s_waitcnt lgkmcnt(4)\n\t"
"v_mfma_f32_16x16x32_bf16 v[100:103], a[76:79], a[164:167], v[100:103]\n\t"
"s_barrier\n\t"
"s_cmp_eq_u32 s21, 1\n\t"
"s_cbranch_scc1 Ltail_%=\n\t"
"v_mfma_f32_16x16x32_bf16 v[104:107], a[72:75], a[152:155], v[104:107]\n\t"
"s_nop 0\n\t"
"ds_read_b128 a[144:147], v224 offset:17568\n\t"
"ds_read_b128 a[160:163], v224 offset:19616\n\t"
"v_mfma_f32_16x16x32_bf16 v[104:107], a[76:79], a[168:171], v[104:107]\n\t"
"buffer_load_dwordx4 a[20:23], off, s[28:31], s69 offset:1024 sc1\n\t"
"v_mfma_f32_16x16x32_bf16 v[120:123], a[80:83], a[152:155], v[120:123]\n\t"
"ds_read_b128 a[148:151], v224 offset:17824\n\t"
"v_mfma_f32_16x16x32_bf16 v[120:123], a[84:87], a[168:171], v[120:123]\n\t"
"ds_read_b128 a[164:167], v224 offset:19872\n\t"
"v_mfma_f32_16x16x32_bf16 v[136:139], a[88:91], a[152:155], v[136:139]\n\t"
"ds_read_b128 a[152:155], v224 offset:18080\n\t"
"v_mfma_f32_16x16x32_bf16 v[136:139], a[92:95], a[168:171], v[136:139]\n\t"
"buffer_load_dwordx4 a[12:15], off, s[84:87], s69 offset:3072 sc1\n\t"
"v_mfma_f32_16x16x32_bf16 v[140:143], a[88:91], a[156:159], v[140:143]\n\t"
"ds_read_b128 a[168:171], v224 offset:20128\n\t"
"v_mfma_f32_16x16x32_bf16 v[140:143], a[92:95], a[172:175], v[140:143]\n\t"
"buffer_load_dwordx4 a[8:11], off, s[84:87], s69 offset:2048 sc1\n\t"
"v_mfma_f32_16x16x32_bf16 v[124:127], a[80:83], a[156:159], v[124:127]\n\t"
"ds_read_b128 v[8:11], v229 offset:20480\n\t"
"v_mfma_f32_16x16x32_bf16 v[124:127], a[84:87], a[172:175], v[124:127]\n\t"
"buffer_load_dwordx4 a[0:3], off, s[84:87], s69 offset:0 sc1\n\t"
"v_mfma_f32_16x16x32_bf16 v[108:111], a[72:75], a[156:159], v[108:111]\n\t"
"ds_read_b128 a[156:159], v224 offset:18336\n\t"
"v_mfma_f32_16x16x32_bf16 v[108:111], a[76:79], a[172:175], v[108:111]\n\t"
"ds_read_b128 a[172:175], v224 offset:20384\n\t"
"v_mfma_f32_16x16x32_bf16 v[192:195], a[224:227], a[176:179], 0\n\t"
"s_nop 0\n\t"
"ds_read_b64_tr_b16 a[176:177], v248 offset:0\n\t"
"ds_read_b64_tr_b16 a[178:179], v248 offset:512\n\t"
"v_mfma_f32_16x16x32_bf16 v[196:199], a[224:227], a[180:183], 0\n\t"
"s_nop 0\n\t"
"ds_read_b64_tr_b16 a[180:181], v249 offset:0\n\t"
"ds_read_b64_tr_b16 a[182:183], v249 offset:512\n\t"
"v_mfma_f32_16x16x32_bf16 v[200:203], a[224:227], a[184:187], 0\n\t"
"buffer_load_dwordx4 a[4:7], off, s[84:87], s69 offset:1024 sc1\n\t"
"v_mfma_f32_16x16x32_bf16 v[204:207], a[224:227], a[188:191], 0\n\t"
"s_waitcnt lgkmcnt(8)\n\t"
"ds_read_b64_tr_b16 a[184:185], v250 offset:0\n\t"
"v_mfma_f32_16x16x32_bf16 v[208:211], a[224:227], a[192:195], 0\n\t"
"s_nop 0\n\t"
"ds_read_b64_tr_b16 a[186:187], v250 offset:512\n\t"
"ds_read_b64_tr_b16 a[188:189], v251 offset:0\n\t"
"v_mfma_f32_16x16x32_bf16 v[212:215], a[224:227], a[196:199], 0\n\t"
"s_nop 0\n\t"
"ds_read_b64_tr_b16 a[190:191], v251 offset:512\n\t"
"ds_read_b64_tr_b16 a[192:193], v248 offset:1152\n\t"
"v_mfma_f32_16x16x32_bf16 v[216:219], a[224:227], a[200:203], 0\n\t"
"s_waitcnt lgkmcnt(11)\n\t"
"buffer_store_dwordx4 v[8:11], v232, s[16:19], s24 offen nt\n\t"
"v_mfma_f32_16x16x32_bf16 v[220:223], a[224:227], a[204:207], 0\n\t"
"s_waitcnt vmcnt(18)\n\t"
"ds_read_b64_tr_b16 a[194:195], v248 offset:1664\n\t"
"v_cvt_pk_bf16_f32 v195, v195, v199\n\t"
"v_mfma_f32_16x16x32_bf16 v[144:147], a[96:99], a[144:147], v[144:147]\n\t"
"ds_read_b64_tr_b16 a[196:197], v249 offset:1152\n\t"
"v_cvt_pk_bf16_f32 v194, v194, v198\n\t"
"v_mfma_f32_16x16x32_bf16 v[144:147], a[100:103], a[160:163], v[144:147]\n\t"
"buffer_load_dwordx4 a[40:43], off, s[28:31], s54 offset:0 sc1\n\t"
"v_mfma_f32_16x16x32_bf16 v[160:163], a[104:107], a[144:147], v[160:163]\n\t"
"s_nop 0\n\t"
"ds_write_addtid_b32 v195 offset:12448\n\t"
"ds_read_b64_tr_b16 a[198:199], v249 offset:1664\n\t"
"v_mfma_f32_16x16x32_bf16 v[160:163], a[108:111], a[160:163], v[160:163]\n\t"
"ds_write_addtid_b32 v194 offset:4128\n\t"
"v_cvt_pk_bf16_f32 v201, v201, v205\n\t"
"v_mfma_f32_16x16x32_bf16 v[176:179], a[112:115], a[144:147], v[176:179]\n\t"
"ds_read_b128 a[144:147], v224 offset:16544\n\t"
"v_cvt_pk_bf16_f32 v211, v211, v215\n\t"
"v_mfma_f32_16x16x32_bf16 v[176:179], a[116:119], a[160:163], v[176:179]\n\t"
"buffer_load_dwordx4 a[44:47], off, s[28:31], s54 offset:1024 sc1\n\t"
"v_mfma_f32_16x16x32_bf16 v[180:183], a[112:115], a[148:151], v[180:183]\n\t"
"ds_read_b128 a[160:163], v224 offset:18592\n\t"
"v_cvt_pk_bf16_f32 v208, v208, v212\n\t"
"v_mfma_f32_16x16x32_bf16 v[180:183], a[116:119], a[164:167], v[180:183]\n\t"
"ds_read_b128 v[24:27], v230 offset:20480\n\t"
"v_cvt_pk_bf16_f32 v202, v202, v206\n\t"
"v_mfma_f32_16x16x32_bf16 v[164:167], a[104:107], a[148:151], v[164:167]\n\t"
"ds_read_b128 v[12:15], v229 offset:33792\n\t"
"v_cvt_pk_bf16_f32 v200, v200, v204\n\t"
"v_mfma_f32_16x16x32_bf16 v[164:167], a[108:111], a[164:167], v[164:167]\n\t"
"buffer_load_dwordx4 a[36:39], off, s[84:87], s54 offset:3072 sc1\n\t"
"v_mfma_f32_16x16x32_bf16 v[148:151], a[96:99], a[148:151], v[148:151]\n\t"
"ds_read_b128 v[28:31], v230 offset:33792\n\t"
"v_cvt_pk_bf16_f32 v216, v216, v220\n\t"
"v_mfma_f32_16x16x32_bf16 v[148:151], a[100:103], a[164:167], v[148:151]\n\t"
"ds_read_b128 a[164:167], v224 offset:18848\n\t"
"v_cvt_pk_bf16_f32 v209, v209, v213\n\t"
"v_mfma_f32_16x16x32_bf16 v[152:155], a[96:99], a[152:155], v[152:155]\n\t"
"ds_read_b128 a[148:151], v224 offset:16800\n\t"
"v_cvt_pk_bf16_f32 v218, v218, v222\n\t"
"v_mfma_f32_16x16x32_bf16 v[152:155], a[100:103], a[168:171], v[152:155]\n\t"
"buffer_load_dwordx4 a[32:35], off, s[84:87], s54 offset:2048 sc1\n\t"
"v_mfma_f32_16x16x32_bf16 v[168:171], a[104:107], a[152:155], v[168:171]\n\t"
"ds_write_addtid_b32 v202 offset:6176\n\t"
"v_cvt_pk_bf16_f32 v203, v203, v207\n\t"
"v_mfma_f32_16x16x32_bf16 v[168:171], a[108:111], a[168:171], v[168:171]\n\t"
"s_nop 0\n\t"
"ds_write_addtid_b32 v200 offset:2048\n\t"
"ds_write_addtid_b32 v203 offset:14496\n\t"
"ds_write_addtid_b32 v211 offset:13472\n\t"
"v_mfma_f32_16x16x32_bf16 v[184:187], a[112:115], a[152:155], v[184:187]\n\t"
"ds_read_b128 a[152:155], v224 offset:17056\n\t"
"v_cvt_pk_bf16_f32 v210, v210, v214\n\t"
"v_mfma_f32_16x16x32_bf16 v[184:187], a[116:119], a[168:171], v[184:187]\n\t"
"s_waitcnt lgkmcnt(9)\n\t"
"buffer_store_dwordx4 v[24:27], v233, s[16:19], s24 offen nt\n\t"
"v_mfma_f32_16x16x32_bf16 v[188:191], a[112:115], a[156:159], v[188:191]\n\t"
"ds_read_b128 a[168:171], v224 offset:19104\n\t"
"v_cvt_pk_bf16_f32 v217, v217, v221\n\t"
"v_mfma_f32_16x16x32_bf16 v[188:191], a[116:119], a[172:175], v[188:191]\n\t"
"ds_read_b128 v[44:47], v231 offset:33792\n\t"
"v_cvt_pk_bf16_f32 v192, v192, v196\n\t"
"v_mfma_f32_16x16x32_bf16 v[172:175], a[104:107], a[156:159], v[172:175]\n\t"
"s_nop 0\n\t"
"ds_write_addtid_b32 v209 offset:9344\n\t"
"ds_write_addtid_b32 v210 offset:5152\n\t"
"v_mfma_f32_16x16x32_bf16 v[172:175], a[108:111], a[172:175], v[172:175]\n\t"
"ds_write_addtid_b32 v216 offset:3072\n\t"
"buffer_load_dwordx4 a[24:27], off, s[84:87], s54 offset:0 sc1\n\t"
"v_mfma_f32_16x16x32_bf16 v[156:159], a[96:99], a[156:159], v[156:159]\n\t"
"ds_read_b128 v[40:43], v231 offset:20480\n\t"
"v_cvt_pk_bf16_f32 v219, v219, v223\n\t"
"v_mfma_f32_16x16x32_bf16 v[156:159], a[100:103], a[172:175], v[156:159]\n\t"
"s_waitcnt vmcnt(13) lgkmcnt(11)\n\t"
"ds_write_addtid_b32 v208 offset:1024\n\t"
"v_cvt_pk_bf16_f32 v193, v193, v197\n\t"
"v_mfma_f32_16x16x32_bf16 v[96:99], a[120:123], a[144:147], v[96:99]\n\t"
"s_nop 0\n\t"
"ds_write_addtid_b32 v217 offset:11392\n\t"
"ds_write_addtid_b32 v218 offset:7200\n\t"
"v_mfma_f32_16x16x32_bf16 v[96:99], a[124:127], a[160:163], v[96:99]\n\t"
"s_nop 0\n\t"
"ds_write_addtid_b32 v193 offset:8320\n\t"
"ds_write_addtid_b32 v201 offset:10368\n\t"
"v_mfma_f32_16x16x32_bf16 v[112:115], a[128:131], a[144:147], v[112:115]\n\t"
"s_nop 0\n\t"
"ds_write_addtid_b32 v192 offset:0\n\t"
"ds_write_addtid_b32 v219 offset:15520\n\t"
"v_mfma_f32_16x16x32_bf16 v[112:115], a[132:135], a[160:163], v[112:115]\n\t"
"buffer_load_dwordx4 a[28:31], off, s[84:87], s54 offset:1024 sc1\n\t"
"v_mfma_f32_16x16x32_bf16 v[128:131], a[136:139], a[144:147], v[128:131]\n\t"
"s_nop 0\n\t"
"ds_read_b128 a[156:159], v224 offset:17312\n\t"
"ds_read_b128 a[172:175], v224 offset:19360\n\t"
"v_mfma_f32_16x16x32_bf16 v[128:131], a[140:143], a[160:163], v[128:131]\n\t"
"buffer_load_dwordx4 a[64:67], off, s[28:31], 0 offset:0 sc1\n\t"
"v_mfma_f32_16x16x32_bf16 v[132:135], a[136:139], a[148:151], v[132:135]\n\t"
"s_nop 0\n\t"
"ds_read_b64_tr_b16 a[200:201], v250 offset:1152\n\t"
"ds_read_b64_tr_b16 a[202:203], v250 offset:1664\n\t"
"v_mfma_f32_16x16x32_bf16 v[132:135], a[140:143], a[164:167], v[132:135]\n\t"
"buffer_load_dwordx4 a[68:71], off, s[28:31], 0 offset:1024 sc1\n\t"
"v_mfma_f32_16x16x32_bf16 v[116:119], a[128:131], a[148:151], v[116:119]\n\t"
"s_nop 0\n\t"
"ds_read_b64_tr_b16 a[204:205], v251 offset:1152\n\t"
"ds_read_b64_tr_b16 a[206:207], v251 offset:1664\n\t"
"v_mfma_f32_16x16x32_bf16 v[116:119], a[132:135], a[164:167], v[116:119]\n\t"
"s_waitcnt lgkmcnt(12)\n\t"
"buffer_store_dwordx4 v[40:43], v234, s[16:19], s24 offen nt\n\t"
"v_mfma_f32_16x16x32_bf16 v[100:103], a[120:123], a[148:151], v[100:103]\n\t"
"v_mfma_f32_16x16x32_bf16 v[100:103], a[124:127], a[164:167], v[100:103]\n\t"
"s_waitcnt lgkmcnt(4)\n\t"
"s_barrier\n\t"
"v_mfma_f32_16x16x32_bf16 v[104:107], a[120:123], a[152:155], v[104:107]\n\t"
"ds_read_b128 a[144:147], v224 offset:0\n\t"
"v_mfma_f32_16x16x32_bf16 v[104:107], a[124:127], a[168:171], v[104:107]\n\t"
"ds_read_b128 a[148:151], v224 offset:256\n\t"
"v_mfma_f32_16x16x32_bf16 v[120:123], a[128:131], a[152:155], v[120:123]\n\t"
"ds_read_b128 a[160:163], v224 offset:2048\n\t"
"v_add_u32 v248, 122368, v227\n\t"
"v_mfma_f32_16x16x32_bf16 v[120:123], a[132:135], a[168:171], v[120:123]\n\t"
"ds_read_b128 a[164:167], v224 offset:2304\n\t"
"v_mfma_f32_16x16x32_bf16 v[136:139], a[136:139], a[152:155], v[136:139]\n\t"
"v_mfma_f32_16x16x32_bf16 v[136:139], a[140:143], a[168:171], v[136:139]\n\t"
"buffer_load_dwordx4 a[60:63], off, s[84:87], 0 offset:3072 sc1\n\t"
"v_mfma_f32_16x16x32_bf16 v[140:143], a[136:139], a[156:159], v[140:143]\n\t"
"ds_read_b128 a[152:155], v224 offset:512\n\t"
"v_xor_b32 v249, 32, v248\n\t"
"v_mfma_f32_16x16x32_bf16 v[140:143], a[140:143], a[172:175], v[140:143]\n\t"
"s_nop 0\n\t"
"ds_read_b128 a[168:171], v224 offset:2560\n\t"
"v_xor_b32 v250, 64, v248\n\t"
"v_mfma_f32_16x16x32_bf16 v[124:127], a[128:131], a[156:159], v[124:127]\n\t"
"s_nop 0\n\t"
"v_mfma_f32_16x16x32_bf16 v[124:127], a[132:135], a[172:175], v[124:127]\n\t"
"buffer_load_dwordx4 a[56:59], off, s[84:87], 0 offset:2048 sc1\n\t"
"v_mfma_f32_16x16x32_bf16 v[108:111], a[120:123], a[156:159], v[108:111]\n\t"
"s_nop 0\n\t"
"ds_read_b128 a[156:159], v224 offset:768\n\t"
"v_mfma_f32_16x16x32_bf16 v[108:111], a[124:127], a[172:175], v[108:111]\n\t"
"s_nop 0\n\t"
"ds_read_b128 a[172:175], v224 offset:2816\n\t"
"v_xor_b32 v251, 96, v248\n\t"
"v_mfma_f32_16x16x32_bf16 v[192:195], a[224:227], a[176:179], 0\n\t"
"s_nop 0\n\t"
"ds_read_b64_tr_b16 a[176:177], v248 offset:20736\n\t"
"ds_read_b64_tr_b16 a[178:179], v248 offset:21248\n\t"
"v_mfma_f32_16x16x32_bf16 v[196:199], a[224:227], a[180:183], 0\n\t"
"buffer_load_dwordx4 a[48:51], off, s[84:87], 0 offset:0 sc1\n\t"
"v_mfma_f32_16x16x32_bf16 v[200:203], a[224:227], a[184:187], 0\n\t"
"s_nop 0\n\t"
"ds_read_b64_tr_b16 a[180:181], v249 offset:20736\n\t"
"ds_read_b64_tr_b16 a[182:183], v249 offset:21248\n\t"
"v_mfma_f32_16x16x32_bf16 v[204:207], a[224:227], a[188:191], 0\n\t"
"buffer_load_dwordx4 a[52:55], off, s[84:87], 0 offset:1024 sc1\n\t"
"v_mfma_f32_16x16x32_bf16 v[208:211], a[224:227], a[192:195], 0\n\t"
"s_nop 0\n\t"
"ds_read_b64_tr_b16 a[184:185], v250 offset:20736\n\t"
"ds_read_b64_tr_b16 a[186:187], v250 offset:21248\n\t"
"v_mfma_f32_16x16x32_bf16 v[212:215], a[224:227], a[196:199], 0\n\t"
"s_waitcnt lgkmcnt(8)\n\t"
"ds_read_b64_tr_b16 a[188:189], v251 offset:20736\n\t"
"ds_read_b64_tr_b16 a[190:191], v251 offset:21248\n\t"
"v_mfma_f32_16x16x32_bf16 v[216:219], a[224:227], a[200:203], 0\n\t"
".p2align 3\n\t"
"ds_read_b64_tr_b16 a[192:193], v248 offset:0\n\t"
"buffer_load_dwordx4 a[88:91], off, s[28:31], s51 offset:0 sc1\n\t"
"v_pk_add_f32 v[96:97], v[96:97], v[144:145]\n\t"
"v_pk_add_f32 v[98:99], v[98:99], v[146:147]\n\t"
"v_pk_fma_f32 v[144:145], v[144:145], s[38:39], v[96:97]\n\t"
"v_pk_fma_f32 v[146:147], v[146:147], s[38:39], v[98:99]\n\t"
"v_pk_add_f32 v[112:113], v[112:113], v[160:161]\n\t"
"v_pk_add_f32 v[114:115], v[114:115], v[162:163]\n\t"
"v_pk_fma_f32 v[160:161], v[160:161], s[38:39], v[112:113]\n\t"
"v_pk_fma_f32 v[162:163], v[162:163], s[38:39], v[114:115]\n\t"
"v_pk_add_f32 v[128:129], v[128:129], v[176:177]\n\t"
"v_pk_add_f32 v[130:131], v[130:131], v[178:179]\n\t"
"v_pk_fma_f32 v[176:177], v[176:177], s[38:39], v[128:129]\n\t"
"v_pk_fma_f32 v[178:179], v[178:179], s[38:39], v[130:131]\n\t"
"buffer_load_dwordx4 a[92:95], off, s[28:31], s51 offset:1024 sc1\n\t"
"v_pk_add_f32 v[132:133], v[132:133], v[180:181]\n\t"
"v_pk_add_f32 v[134:135], v[134:135], v[182:183]\n\t"
"v_pk_fma_f32 v[180:181], v[180:181], s[38:39], v[132:133]\n\t"
"v_pk_fma_f32 v[182:183], v[182:183], s[38:39], v[134:135]\n\t"
"v_pk_add_f32 v[116:117], v[116:117], v[164:165]\n\t"
"v_pk_add_f32 v[118:119], v[118:119], v[166:167]\n\t"
"v_pk_fma_f32 v[164:165], v[164:165], s[38:39], v[116:117]\n\t"
"v_pk_fma_f32 v[166:167], v[166:167], s[38:39], v[118:119]\n\t"
"v_pk_add_f32 v[100:101], v[100:101], v[148:149]\n\t"
"v_pk_add_f32 v[102:103], v[102:103], v[150:151]\n\t"
"v_pk_fma_f32 v[148:149], v[148:149], s[38:39], v[100:101]\n\t"
"v_pk_fma_f32 v[150:151], v[150:151], s[38:39], v[102:103]\n\t"
"buffer_load_dwordx4 a[84:87], off, s[84:87], s51 offset:3072 sc1\n\t"
"v_pk_add_f32 v[104:105], v[104:105], v[152:153]\n\t"
"v_pk_add_f32 v[106:107], v[106:107], v[154:155]\n\t"
"v_pk_fma_f32 v[152:153], v[152:153], s[38:39], v[104:105]\n\t"
"v_pk_fma_f32 v[154:155], v[154:155], s[38:39], v[106:107]\n\t"
"v_pk_add_f32 v[120:121], v[120:121], v[168:169]\n\t"
"v_pk_add_f32 v[122:123], v[122:123], v[170:171]\n\t"
"v_pk_fma_f32 v[168:169], v[168:169], s[38:39], v[120:121]\n\t"
"v_pk_fma_f32 v[170:171], v[170:171], s[38:39], v[122:123]\n\t"
"v_pk_add_f32 v[136:137], v[136:137], v[184:185]\n\t"
"v_pk_add_f32 v[138:139], v[138:139], v[186:187]\n\t"
"v_pk_fma_f32 v[184:185], v[184:185], s[38:39], v[136:137]\n\t"
"v_pk_fma_f32 v[186:187], v[186:187], s[38:39], v[138:139]\n\t"
"buffer_load_dwordx4 a[80:83], off, s[84:87], s51 offset:2048 sc1\n\t"
"v_pk_add_f32 v[140:141], v[140:141], v[188:189]\n\t"
"v_pk_add_f32 v[142:143], v[142:143], v[190:191]\n\t"
"v_pk_fma_f32 v[188:189], v[188:189], s[38:39], v[140:141]\n\t"
"v_pk_fma_f32 v[190:191], v[190:191], s[38:39], v[142:143]\n\t"
"v_pk_add_f32 v[124:125], v[124:125], v[172:173]\n\t"
"v_pk_add_f32 v[126:127], v[126:127], v[174:175]\n\t"
"v_pk_fma_f32 v[172:173], v[172:173], s[38:39], v[124:125]\n\t"
"v_pk_fma_f32 v[174:175], v[174:175], s[38:39], v[126:127]\n\t"
"v_pk_add_f32 v[108:109], v[108:109], v[156:157]\n\t"
"v_pk_add_f32 v[110:111], v[110:111], v[158:159]\n\t"
"v_pk_fma_f32 v[156:157], v[156:157], s[38:39], v[108:109]\n\t"
"v_pk_fma_f32 v[158:159], v[158:159], s[38:39], v[110:111]\n\t"
"v_cvt_pk_bf16_f32 v100, v100, v101\n\t"
"v_cvt_pk_bf16_f32 v101, v102, v103\n\t"
"v_cvt_pk_bf16_f32 v108, v108, v109\n\t"
"v_mfma_f32_16x16x32_bf16 v[220:223], a[224:227], a[204:207], 0\n\t"
"s_waitcnt vmcnt(19)\n\t"
"ds_read_b64_tr_b16 a[194:195], v248 offset:512\n\t"
"buffer_store_dwordx4 v[12:15], v232, s[16:19], s27 offen nt\n\t"
"v_cvt_pk_bf16_f32 v193, v193, v197\n\t"
"v_cvt_pk_bf16_f32 v96, v96, v97\n\t"
"v_cvt_pk_bf16_f32 v116, v116, v117\n\t"
"v_cvt_pk_bf16_f32 v132, v132, v133\n\t"
"v_mfma_f32_16x16x32_bf16 v[48:51], a[0:3], a[144:147], v[48:51]\n\t"
"s_nop 0\n\t"
"v_cvt_pk_bf16_f32 v97, v98, v99\n\t"
"v_cvt_pk_bf16_f32 v128, v128, v129\n\t"
"v_mfma_f32_16x16x32_bf16 v[48:51], a[4:7], a[160:163], v[48:51]\n\t"
"s_nop 0\n\t"
"ds_write_addtid_b32 v193 offset:24864\n\t"
"ds_write2st64_b64 v228, v[96:97], v[100:101] offset0:40 offset1:53\n\t"
"v_cvt_pk_bf16_f32 v117, v118, v119\n\t"
"v_mfma_f32_16x16x32_bf16 v[64:67], a[8:11], a[144:147], v[64:67]\n\t"
"s_nop 0\n\t"
"v_cvt_pk_bf16_f32 v133, v134, v135\n\t"
"v_cvt_pk_bf16_f32 v104, v104, v105\n\t"
"v_mfma_f32_16x16x32_bf16 v[64:67], a[12:15], a[160:163], v[64:67]\n\t"
"buffer_load_dwordx4 a[72:75], off, s[84:87], s51 offset:0 sc1\n\t"
"v_cvt_pk_bf16_f32 v210, v210, v214\n\t"
"v_cvt_pk_bf16_f32 v105, v106, v107\n\t"
"v_mfma_f32_16x16x32_bf16 v[80:83], a[16:19], a[144:147], v[80:83]\n\t"
"s_nop 0\n\t"
"v_cvt_pk_bf16_f32 v112, v112, v113\n\t"
"v_cvt_pk_bf16_f32 v113, v114, v115\n\t"
"v_mfma_f32_16x16x32_bf16 v[80:83], a[20:23], a[160:163], v[80:83]\n\t"
"s_nop 0\n\t"
"ds_read_b128 a[144:147], v224 offset:1024\n\t"
"ds_read_b128 a[160:163], v224 offset:3072\n\t"
"ds_write2st64_b64 v246, v[112:113], v[116:117] offset0:40 offset1:53\n\t"
"v_mfma_f32_16x16x32_bf16 v[84:87], a[16:19], a[148:151], v[84:87]\n\t"
"s_nop 0\n\t"
"v_cvt_pk_bf16_f32 v195, v195, v199\n\t"
"v_cvt_pk_bf16_f32 v218, v218, v222\n\t"
"v_mfma_f32_16x16x32_bf16 v[84:87], a[20:23], a[164:167], v[84:87]\n\t"
"Lpad6_%=:\n\t"
"s_cbranch_execz Lret0_%=\n\t"
"buffer_load_dwordx4 a[76:79], off, s[84:87], s51 offset:1024 sc1\n\t"
"v_mfma_f32_16x16x32_bf16 v[68:71], a[8:11], a[148:151], v[68:71]\n\t"
"v_cvt_pk_bf16_f32 v129, v130, v131\n\t"
"v_cvt_pk_bf16_f32 v109, v110, v111\n\t"
"v_mfma_f32_16x16x32_bf16 v[68:71], a[12:15], a[164:167], v[68:71]\n\t"
"ds_write_addtid_b32 v210 offset:21696\n\t"
"ds_write2st64_b64 v247, v[128:129], v[132:133] offset0:40 offset1:53\n\t"
"v_cvt_pk_bf16_f32 v140, v140, v141\n\t"
"v_mfma_f32_16x16x32_bf16 v[52:55], a[0:3], a[148:151], v[52:55]\n\t"
"v_cvt_pk_bf16_f32 v202, v202, v206\n\t"
"v_cvt_pk_bf16_f32 v124, v124, v125\n\t"
"v_mfma_f32_16x16x32_bf16 v[52:55], a[4:7], a[164:167], v[52:55]\n\t"
"buffer_load_dwordx4 a[112:115], off, s[28:31], s81 offset:0 sc1\n\t"
"v_cvt_pk_bf16_f32 v120, v120, v121\n\t"
"v_mfma_f32_16x16x32_bf16 v[56:59], a[0:3], a[152:155], v[56:59]\n\t"
"v_cvt_pk_bf16_f32 v208, v208, v212\n\t"
"v_cvt_pk_bf16_f32 v219, v219, v223\n\t"
"v_mfma_f32_16x16x32_bf16 v[56:59], a[4:7], a[168:171], v[56:59]\n\t"
"s_nop 0\n\t"
"ds_read_b128 a[148:151], v224 offset:1280\n\t"
"ds_read_b128 a[164:167], v224 offset:3328\n\t"
"ds_write_addtid_b32 v208 offset:17568\n\t"
"ds_write_addtid_b32 v219 offset:32064\n\t"
"v_cvt_pk_bf16_f32 v203, v203, v207\n\t"
"v_mfma_f32_16x16x32_bf16 v[72:75], a[8:11], a[152:155], v[72:75]\n\t"
"v_cvt_pk_bf16_f32 v194, v194, v198\n\t"
"v_cvt_pk_bf16_f32 v201, v201, v205\n\t"
"v_mfma_f32_16x16x32_bf16 v[72:75], a[12:15], a[168:171], v[72:75]\n\t"
"ds_write_addtid_b32 v203 offset:31040\n\t"
"buffer_store_dwordx4 v[28:31], v233, s[16:19], s27 offen nt\n\t"
"ds_write2st64_b64 v228, v[104:105], v[108:109] offset0:66 offset1:79\n\t"
"v_cvt_pk_bf16_f32 v136, v136, v137\n\t"
"v_mfma_f32_16x16x32_bf16 v[88:91], a[16:19], a[152:155], v[88:91]\n\t"
"v_cvt_pk_bf16_f32 v121, v122, v123\n\t"
"v_cvt_pk_bf16_f32 v137, v138, v139\n\t"
"v_mfma_f32_16x16x32_bf16 v[88:91], a[20:23], a[168:171], v[88:91]\n\t"
"s_waitcnt lgkmcnt(8)\n\t"
"v_cvt_pk_bf16_f32 v217, v217, v221\n\t"
"v_cvt_pk_bf16_f32 v125, v126, v127\n\t"
"v_mfma_f32_16x16x32_bf16 v[92:95], a[16:19], a[156:159], v[92:95]\n\t"
"v_cvt_pk_bf16_f32 v200, v200, v204\n\t"
"v_cvt_pk_bf16_f32 v141, v142, v143\n\t"
"v_mfma_f32_16x16x32_bf16 v[92:95], a[20:23], a[172:175], v[92:95]\n\t"
"s_nop 0\n\t"
"ds_write_addtid_b32 v195 offset:28992\n\t"
"ds_write_addtid_b32 v200 offset:18592\n\t"
"ds_write_addtid_b32 v201 offset:26912\n\t"
"ds_write_addtid_b32 v202 offset:22720\n\t"
"ds_write_addtid_b32 v217 offset:27936\n\t"
"ds_write2st64_b64 v246, v[120:121], v[124:125] offset0:66 offset1:79\n\t"
"v_mfma_f32_16x16x32_bf16 v[76:79], a[8:11], a[156:159], v[76:79]\n\t"
"v_cvt_pk_bf16_f32 v209, v209, v213\n\t"
"v_cvt_pk_bf16_f32 v216, v216, v220\n\t"
"v_mfma_f32_16x16x32_bf16 v[76:79], a[12:15], a[172:175], v[76:79]\n\t"
"buffer_load_dwordx4 a[116:119], off, s[28:31], s81 offset:1024 sc1\n\t"
"v_mfma_f32_16x16x32_bf16 v[60:63], a[0:3], a[156:159], v[60:63]\n\t"
"v_cvt_pk_bf16_f32 v192, v192, v196\n\t"
"v_cvt_pk_bf16_f32 v211, v211, v215\n\t"
"v_mfma_f32_16x16x32_bf16 v[60:63], a[4:7], a[172:175], v[60:63]\n\t"
"s_waitcnt vmcnt(17) lgkmcnt(8)\n\t"
"ds_write_addtid_b32 v192 offset:16544\n\t"
"ds_write_addtid_b32 v194 offset:20672\n\t"
"ds_write_addtid_b32 v209 offset:25888\n\t"
"ds_write_addtid_b32 v211 offset:30016\n\t"
"ds_write2st64_b64 v247, v[136:137], v[140:141] offset0:66 offset1:79\n\t"
"v_mfma_f32_16x16x32_bf16 v[48:51], a[24:27], a[144:147], v[48:51]\n\t"
"buffer_load_dwordx4 a[108:111], off, s[84:87], s81 offset:3072 sc1\n\t"
"v_mfma_f32_16x16x32_bf16 v[48:51], a[28:31], a[160:163], v[48:51]\n\t"
"s_nop 0\n\t"
"ds_write_addtid_b32 v216 offset:19616\n\t"
"ds_write_addtid_b32 v218 offset:23744\n\t"
"v_mfma_f32_16x16x32_bf16 v[64:67], a[32:35], a[144:147], v[64:67]\n\t"
"buffer_load_dwordx4 a[104:107], off, s[84:87], s81 offset:2048 sc1\n\t"
"v_mfma_f32_16x16x32_bf16 v[64:67], a[36:39], a[160:163], v[64:67]\n\t"
"s_nop 0\n\t"
"ds_read_b128 a[152:155], v224 offset:1536\n\t"
"ds_read_b128 a[156:159], v224 offset:1792\n\t"
"v_mfma_f32_16x16x32_bf16 v[80:83], a[40:43], a[144:147], v[80:83]\n\t"
"s_nop 0\n\t"
"ds_read_b128 a[168:171], v224 offset:3584\n\t"
"ds_read_b128 a[172:175], v224 offset:3840\n\t"
"v_mfma_f32_16x16x32_bf16 v[80:83], a[44:47], a[160:163], v[80:83]\n\t"
"buffer_load_dwordx4 a[96:99], off, s[84:87], s81 offset:0 sc1\n\t"
"v_mfma_f32_16x16x32_bf16 v[84:87], a[40:43], a[148:151], v[84:87]\n\t"
"s_nop 0\n\t"
"ds_read_b64_tr_b16 a[196:197], v249 offset:0\n\t"
"ds_read_b64_tr_b16 a[198:199], v249 offset:512\n\t"
"v_mfma_f32_16x16x32_bf16 v[84:87], a[44:47], a[164:167], v[84:87]\n\t"
"s_nop 0\n\t"
"ds_read_b64_tr_b16 a[200:201], v250 offset:0\n\t"
"ds_read_b64_tr_b16 a[202:203], v250 offset:512\n\t"
"v_mfma_f32_16x16x32_bf16 v[68:71], a[32:35], a[148:151], v[68:71]\n\t"
"s_nop 0\n\t"
"ds_read_b64_tr_b16 a[204:205], v251 offset:0\n\t"
"ds_read_b64_tr_b16 a[206:207], v251 offset:512\n\t"
"v_mfma_f32_16x16x32_bf16 v[68:71], a[36:39], a[164:167], v[68:71]\n\t"
"buffer_store_dwordx4 v[44:47], v234, s[16:19], s27 offen nt\n\t"
"v_mfma_f32_16x16x32_bf16 v[52:55], a[24:27], a[148:151], v[52:55]\n\t"
"s_waitcnt lgkmcnt(6)\n\t"
"v_mfma_f32_16x16x32_bf16 v[52:55], a[28:31], a[164:167], v[52:55]\n\t"
"buffer_load_dwordx4 a[100:103], off, s[84:87], s81 offset:1024 sc1\n\t"
"s_barrier\n\t"
"v_mfma_f32_16x16x32_bf16 v[56:59], a[24:27], a[152:155], v[56:59]\n\t"
"s_nop 0\n\t"
"ds_read_b128 a[144:147], v224 offset:16544\n\t"
"ds_read_b128 a[164:167], v224 offset:18848\n\t"
"v_mfma_f32_16x16x32_bf16 v[56:59], a[28:31], a[168:171], v[56:59]\n\t"
"buffer_load_dwordx4 a[136:139], off, s[28:31], s66 offset:0 sc1\n\t"
"v_mfma_f32_16x16x32_bf16 v[72:75], a[32:35], a[152:155], v[72:75]\n\t"
"s_nop 0\n\t"
"ds_read_b128 a[160:163], v224 offset:18592\n\t"
"ds_read_b128 a[148:151], v224 offset:16800\n\t"
"v_mfma_f32_16x16x32_bf16 v[72:75], a[36:39], a[168:171], v[72:75]\n\t"
"buffer_load_dwordx4 a[140:143], off, s[28:31], s66 offset:1024 sc1\n\t"
"v_mfma_f32_16x16x32_bf16 v[88:91], a[40:43], a[152:155], v[88:91]\n\t"
"ds_read_b128 v[108:111], v229 offset:33792\n\t"
"v_add_u32 v248, 54016, v227\n\t"
"v_mfma_f32_16x16x32_bf16 v[88:91], a[44:47], a[168:171], v[88:91]\n\t"
"ds_read_b128 v[104:107], v229 offset:20480\n\t"
"v_xor_b32 v249, 32, v248\n\t"
"v_mfma_f32_16x16x32_bf16 v[92:95], a[40:43], a[156:159], v[92:95]\n\t"
"s_nop 0\n\t"
"ds_read_b128 v[136:139], v231 offset:20480\n\t"
"ds_read_b128 a[232:235], v230 offset:33792\n\t"
"v_mfma_f32_16x16x32_bf16 v[92:95], a[44:47], a[172:175], v[92:95]\n\t"
"buffer_load_dwordx4 a[132:135], off, s[84:87], s66 offset:3072 sc1\n\t"
"v_mfma_f32_16x16x32_bf16 v[76:79], a[32:35], a[156:159], v[76:79]\n\t"
"ds_read_b128 a[236:239], v231 offset:33792\n\t"
"v_xor_b32 v250, 64, v248\n\t"
"v_mfma_f32_16x16x32_bf16 v[76:79], a[36:39], a[172:175], v[76:79]\n\t"
"ds_read_b128 a[152:155], v224 offset:17056\n\t"
"v_xor_b32 v251, 96, v248\n\t"
"v_mfma_f32_16x16x32_bf16 v[60:63], a[24:27], a[156:159], v[60:63]\n\t"
"ds_read_b128 v[120:123], v230 offset:20480\n\t"
"v_mfma_f32_16x16x32_bf16 v[60:63], a[28:31], a[172:175], v[60:63]\n\t"
"buffer_load_dwordx4 a[128:131], off, s[84:87], s66 offset:2048 sc1\n\t"
"v_mfma_f32_16x16x32_bf16 v[192:195], a[216:219], a[176:179], 0\n\t"
"s_nop 0\n\t"
"ds_read_b128 a[168:171], v224 offset:19104\n\t"
"ds_read_b64_tr_b16 a[176:177], v248 offset:21888\n\t"
"v_mfma_f32_16x16x32_bf16 v[196:199], a[216:219], a[180:183], 0\n\t"
"ds_read_b64_tr_b16 a[178:179], v248 offset:22400\n\t"
"v_mfma_f32_16x16x32_bf16 v[200:203], a[216:219], a[184:187], 0\n\t"
"s_waitcnt lgkmcnt(8)\n\t"
"buffer_store_dwordx4 v[104:107], v232, s[16:19], s20 offen nt\n\t"
"v_mfma_f32_16x16x32_bf16 v[204:207], a[216:219], a[188:191], 0\n\t"
"s_nop 0\n\t"
"ds_read_b64_tr_b16 a[180:181], v249 offset:21888\n\t"
"ds_read_b64_tr_b16 a[182:183], v249 offset:22400\n\t"
"v_mfma_f32_16x16x32_bf16 v[208:211], a[216:219], a[192:195], 0\n\t"
"s_nop 0\n\t"
"ds_read_b64_tr_b16 a[184:185], v250 offset:21888\n\t"
"ds_read_b64_tr_b16 a[186:187], v250 offset:22400\n\t"
"v_mfma_f32_16x16x32_bf16 v[212:215], a[216:219], a[196:199], 0\n\t"
"s_waitcnt lgkmcnt(7)\n\t"
"s_barrier\n\t"
"v_mfma_f32_16x16x32_bf16 v[216:219], a[216:219], a[200:203], 0\n\t"
"buffer_load_dwordx4 a[120:123], off, s[84:87], s66 offset:0 sc1\n\t"
"v_mfma_f32_16x16x32_bf16 v[220:223], a[216:219], a[204:207], 0\n\t"
"s_waitcnt vmcnt(21)\n\t"
"ds_read_b128 a[156:159], v224 offset:17312\n\t"
"v_cvt_pk_bf16_f32 v195, v195, v199\n\t"
"v_mfma_f32_16x16x32_bf16 v[48:51], a[48:51], a[144:147], v[48:51]\n\t"
"ds_read_b128 a[172:175], v224 offset:19360\n\t"
"v_cvt_pk_bf16_f32 v194, v194, v198\n\t"
"v_mfma_f32_16x16x32_bf16 v[48:51], a[52:55], a[160:163], v[48:51]\n\t"
"ds_read_b64_tr_b16 a[188:189], v251 offset:21888\n\t"
"v_cvt_pk_bf16_f32 v200, v200, v204\n\t"
"v_mfma_f32_16x16x32_bf16 v[64:67], a[56:59], a[144:147], v[64:67]\n\t"
"ds_read_b64_tr_b16 a[190:191], v251 offset:22400\n\t"
"v_cvt_pk_bf16_f32 v203, v203, v207\n\t"
"v_mfma_f32_16x16x32_bf16 v[64:67], a[60:63], a[160:163], v[64:67]\n\t"
"buffer_load_dwordx4 a[124:127], off, s[84:87], s66 offset:1024 sc1\n\t"
"v_mfma_f32_16x16x32_bf16 v[80:83], a[64:67], a[144:147], v[80:83]\n\t"
"ds_read_b128 a[144:147], v224 offset:17568\n\t"
"v_cvt_pk_bf16_f32 v201, v201, v205\n\t"
"v_mfma_f32_16x16x32_bf16 v[80:83], a[68:71], a[160:163], v[80:83]\n\t"
"ds_read_b128 a[160:163], v224 offset:19616\n\t"
"v_cvt_pk_bf16_f32 v208, v208, v212\n\t"
"v_mfma_f32_16x16x32_bf16 v[84:87], a[64:67], a[148:151], v[84:87]\n\t"
"ds_write_addtid_b32 v194 offset:4128\n\t"
"v_cvt_pk_bf16_f32 v219, v219, v223\n\t"
"v_mfma_f32_16x16x32_bf16 v[84:87], a[68:71], a[164:167], v[84:87]\n\t"
"ds_write_addtid_b32 v203 offset:14496\n\t"
"buffer_store_dwordx4 v[120:123], v233, s[16:19], s20 offen nt\n\t"
"v_mfma_f32_16x16x32_bf16 v[68:71], a[56:59], a[148:151], v[68:71]\n\t"
"ds_write_addtid_b32 v195 offset:12448\n\t"
"v_cvt_pk_bf16_f32 v216, v216, v220\n\t"
"v_mfma_f32_16x16x32_bf16 v[68:71], a[60:63], a[164:167], v[68:71]\n\t"
"ds_write_addtid_b32 v208 offset:1024\n\t"
"v_cvt_pk_bf16_f32 v192, v192, v196\n\t"
"v_mfma_f32_16x16x32_bf16 v[52:55], a[48:51], a[148:151], v[52:55]\n\t"
"ds_read_b128 a[148:151], v224 offset:17824\n\t"
"v_cvt_pk_bf16_f32 v210, v210, v214\n\t"
"v_mfma_f32_16x16x32_bf16 v[52:55], a[52:55], a[164:167], v[52:55]\n\t"
"s_waitcnt lgkmcnt(8)\n\t"
"ds_write_addtid_b32 v216 offset:3072\n\t"
"v_cvt_pk_bf16_f32 v202, v202, v206\n\t"
"v_mfma_f32_16x16x32_bf16 v[56:59], a[48:51], a[152:155], v[56:59]\n\t"
"ds_read_b128 a[164:167], v224 offset:19872\n\t"
"v_cvt_pk_bf16_f32 v209, v209, v213\n\t"
"v_mfma_f32_16x16x32_bf16 v[56:59], a[52:55], a[168:171], v[56:59]\n\t"
"ds_write_addtid_b32 v210 offset:5152\n\t"
"v_cvt_pk_bf16_f32 v217, v217, v221\n\t"
"v_mfma_f32_16x16x32_bf16 v[72:75], a[56:59], a[152:155], v[72:75]\n\t"
"s_nop 0\n\t"
"ds_write_addtid_b32 v201 offset:10368\n\t"
"ds_write_addtid_b32 v217 offset:11392\n\t"
"v_mfma_f32_16x16x32_bf16 v[72:75], a[60:63], a[168:171], v[72:75]\n\t"
"buffer_store_dwordx4 v[136:139], v234, s[16:19], s20 offen nt\n\t"
"v_mfma_f32_16x16x32_bf16 v[88:91], a[64:67], a[152:155], v[88:91]\n\t"
"s_nop 0\n\t"
"ds_write_addtid_b32 v202 offset:6176\n\t"
"ds_write_addtid_b32 v209 offset:9344\n\t"
"v_mfma_f32_16x16x32_bf16 v[88:91], a[68:71], a[168:171], v[88:91]\n\t"
"s_nop 0\n\t"
"ds_write_addtid_b32 v200 offset:2048\n\t"
"ds_write_addtid_b32 v219 offset:15520\n\t"
"v_mfma_f32_16x16x32_bf16 v[92:95], a[64:67], a[156:159], v[92:95]\n\t"
"s_add_u32 m0, s26, 103936\n\t"
"ds_read_b128 a[152:155], v224 offset:18080\n\t"
"v_cvt_pk_bf16_f32 v193, v193, v197\n\t"
"v_mfma_f32_16x16x32_bf16 v[92:95], a[68:71], a[172:175], v[92:95]\n\t"
"buffer_load_dwordx4 v236, s[12:15], s22 offen offset:0 nt lds\n\t"
"v_mfma_f32_16x16x32_bf16 v[76:79], a[56:59], a[156:159], v[76:79]\n\t"
"ds_read_b128 a[168:171], v224 offset:20128\n\t"
"v_cvt_pk_bf16_f32 v218, v218, v222\n\t"
"v_mfma_f32_16x16x32_bf16 v[76:79], a[60:63], a[172:175], v[76:79]\n\t"
"buffer_load_dwordx4 v237, s[12:15], s22 offen offset:1024 nt lds\n\t"
"v_mfma_f32_16x16x32_bf16 v[60:63], a[48:51], a[156:159], v[60:63]\n\t"
"s_lshl_b32 m0, s73, 8\n\t"
"ds_read_b128 a[156:159], v224 offset:18336\n\t"
"v_cvt_pk_bf16_f32 v211, v211, v215\n\t"
"v_mfma_f32_16x16x32_bf16 v[60:63], a[52:55], a[172:175], v[60:63]\n\t"
"s_waitcnt vmcnt(19) lgkmcnt(9)\n\t"
"ds_write_addtid_b32 v192 offset:0\n\t"
"ds_write_addtid_b32 v193 offset:8320\n\t"
"v_mfma_f32_16x16x32_bf16 v[144:147], a[72:75], a[144:147], v[144:147]\n\t"
"s_nop 0\n\t"
"ds_write_addtid_b32 v211 offset:13472\n\t"
"ds_write_addtid_b32 v218 offset:7200\n\t"
"v_mfma_f32_16x16x32_bf16 v[144:147], a[76:79], a[160:163], v[144:147]\n\t"
"s_add_u32 m0, s26, 103936\n\t"
"ds_read_b128 a[172:175], v224 offset:20384\n\t"
"v_mfma_f32_16x16x32_bf16 v[160:163], a[80:83], a[144:147], v[160:163]\n\t"
"buffer_load_dwordx4 v238, s[12:15], s22 offen offset:2048 nt lds\n\t"
"v_mfma_f32_16x16x32_bf16 v[160:163], a[84:87], a[160:163], v[160:163]\n\t"
"s_nop 0\n\t"
"ds_read_b64_tr_b16 a[192:193], v248 offset:1152\n\t"
"ds_read_b64_tr_b16 a[194:195], v248 offset:1664\n\t"
"v_mfma_f32_16x16x32_bf16 v[176:179], a[88:91], a[144:147], v[176:179]\n\t"
"s_nop 0\n\t"
"ds_read_b64_tr_b16 a[196:197], v249 offset:1152\n\t"
"ds_read_b64_tr_b16 a[198:199], v249 offset:1664\n\t"
"v_mfma_f32_16x16x32_bf16 v[176:179], a[92:95], a[160:163], v[176:179]\n\t"
"buffer_load_dwordx4 v239, s[12:15], s22 offen offset:3072 nt lds\n\t"
"v_mfma_f32_16x16x32_bf16 v[180:183], a[88:91], a[148:151], v[180:183]\n\t"
"s_nop 0\n\t"
"ds_read_b64_tr_b16 a[200:201], v250 offset:1152\n\t"
"ds_read_b64_tr_b16 a[202:203], v250 offset:1664\n\t"
"v_mfma_f32_16x16x32_bf16 v[180:183], a[92:95], a[164:167], v[180:183]\n\t"
"s_mov_b64 exec, s[36:37]\n\t"
"buffer_load_dwordx4 v240, s[12:15], s22 offen offset:3584 nt lds\n\t"
"s_mov_b64 exec, -1\n\t"
"v_mfma_f32_16x16x32_bf16 v[164:167], a[80:83], a[148:151], v[164:167]\n\t"
"s_lshl_b32 m0, s73, 8\n\t"
"ds_read_b64_tr_b16 a[204:205], v251 offset:1152\n\t"
"ds_read_b64_tr_b16 a[206:207], v251 offset:1664\n\t"
"v_mfma_f32_16x16x32_bf16 v[164:167], a[84:87], a[164:167], v[164:167]\n\t"
"buffer_store_dwordx4 v[108:111], v232, s[16:19], s25 offen nt\n\t"
"v_mfma_f32_16x16x32_bf16 v[148:151], a[72:75], a[148:151], v[148:151]\n\t"
"s_waitcnt lgkmcnt(8)\n\t"
"v_mfma_f32_16x16x32_bf16 v[148:151], a[76:79], a[164:167], v[148:151]\n\t"
"s_barrier\n\t"
"v_mfma_f32_16x16x32_bf16 v[152:155], a[72:75], a[152:155], v[152:155]\n\t"
"ds_read_b128 a[144:147], v224 offset:0\n\t"
"v_mfma_f32_16x16x32_bf16 v[152:155], a[76:79], a[168:171], v[152:155]\n\t"
"ds_read_b128 a[160:163], v224 offset:2048\n\t"
"v_add_u32 v248, 54016, v226\n\t"
"v_mfma_f32_16x16x32_bf16 v[168:171], a[80:83], a[152:155], v[168:171]\n\t"
"ds_read_b128 a[148:151], v224 offset:256\n\t"
"v_mfma_f32_16x16x32_bf16 v[168:171], a[84:87], a[168:171], v[168:171]\n\t"
"ds_read_b128 a[164:167], v224 offset:2304\n\t"
"v_mfma_f32_16x16x32_bf16 v[184:187], a[88:91], a[152:155], v[184:187]\n\t"
"v_mfma_f32_16x16x32_bf16 v[184:187], a[92:95], a[168:171], v[184:187]\n\t"
"ds_read_b128 a[152:155], v224 offset:512\n\t"
"v_xor_b32 v249, 32, v248\n\t"
"v_mfma_f32_16x16x32_bf16 v[188:191], a[88:91], a[156:159], v[188:191]\n\t"
"ds_read_b128 a[228:231], v254 offset:18432\n\t"
"v_mfma_f32_16x16x32_bf16 v[188:191], a[92:95], a[172:175], v[188:191]\n\t"
"ds_read_b128 a[168:171], v224 offset:2560\n\t"
"v_mfma_f32_16x16x32_bf16 v[172:175], a[80:83], a[156:159], v[172:175]\n\t"
"v_mfma_f32_16x16x32_bf16 v[172:175], a[84:87], a[172:175], v[172:175]\n\t"
"v_xor_b32 v250, 64, v248\n\t"
"v_mfma_f32_16x16x32_bf16 v[156:159], a[72:75], a[156:159], v[156:159]\n\t"
"ds_read_b128 a[156:159], v224 offset:768\n\t"
"v_xor_b32 v251, 96, v248\n\t"
"v_mfma_f32_16x16x32_bf16 v[156:159], a[76:79], a[172:175], v[156:159]\n\t"
"ds_read_b128 a[172:175], v224 offset:2816\n\t"
"v_mfma_f32_16x16x32_bf16 v[192:195], a[224:227], a[176:179], 0\n\t"
"ds_read_b64_tr_b16 a[178:179], v248 offset:128\n\t"
"v_mfma_f32_16x16x32_bf16 v[196:199], a[224:227], a[180:183], 0\n\t"
"ds_read_b64_tr_b16 a[176:177], v248 offset:0\n\t"
"v_mfma_f32_16x16x32_bf16 v[200:203], a[224:227], a[184:187], 0\n\t"
"ds_read_b64_tr_b16 a[180:181], v249 offset:256\n\t"
"v_mfma_f32_16x16x32_bf16 v[204:207], a[224:227], a[188:191], 0\n\t"
"s_waitcnt lgkmcnt(7)\n\t"
"v_mfma_f32_16x16x32_bf16 v[208:211], a[224:227], a[192:195], 0\n\t"
"s_waitcnt lgkmcnt(6)\n\t"
"ds_write_b128 v255, a[228:231] offset:0\n\t"
"v_mfma_f32_16x16x32_bf16 v[212:215], a[224:227], a[196:199], 0\n\t"
"v_mfma_f32_16x16x32_bf16 v[216:219], a[224:227], a[200:203], 0\n\t"
"v_mfma_f32_16x16x32_bf16 v[220:223], a[224:227], a[204:207], 0\n\t"
"v_cvt_pk_bf16_f32 v195, v195, v199\n\t"
"v_mfma_f32_16x16x32_bf16 v[0:3], a[0:3], a[144:147], 0\n\t"
"ds_read_b64_tr_b16 a[186:187], v249 offset:128\n\t"
"v_cvt_pk_bf16_f32 v193, v193, v197\n\t"
"v_mfma_f32_16x16x32_bf16 v[0:3], a[4:7], a[160:163], v[0:3]\n\t"
"ds_read_b64_tr_b16 a[184:185], v249 offset:0\n\t"
"v_mfma_f32_16x16x32_bf16 v[16:19], a[8:11], a[144:147], 0\n\t"
"ds_read_b64_tr_b16 a[188:189], v248 offset:256\n\t"
"v_cvt_pk_bf16_f32 v201, v201, v205\n\t"
"v_mfma_f32_16x16x32_bf16 v[16:19], a[12:15], a[160:163], v[16:19]\n\t"
"ds_read_b64_tr_b16 a[194:195], v250 offset:128\n\t"
"v_cvt_pk_bf16_f32 v200, v200, v204\n\t"
"v_mfma_f32_16x16x32_bf16 v[32:35], a[16:19], a[144:147], 0\n\t"
"ds_read_b128 a[144:147], v224 offset:1024\n\t"
"v_mfma_f32_16x16x32_bf16 v[32:35], a[20:23], a[160:163], v[32:35]\n\t"
"ds_write_addtid_b32 v201 offset:26912\n\t"
"v_cvt_pk_bf16_f32 v203, v203, v207\n\t"
"v_mfma_f32_16x16x32_bf16 v[36:39], a[16:19], a[148:151], 0\n\t"
"ds_read_b64_tr_b16 a[192:193], v250 offset:0\n\t"
"v_cvt_pk_bf16_f32 v211, v211, v215\n\t"
"v_mfma_f32_16x16x32_bf16 v[36:39], a[20:23], a[164:167], v[36:39]\n\t"
"ds_write_addtid_b32 v193 offset:24864\n\t"
"v_cvt_pk_bf16_f32 v219, v219, v223\n\t"
"v_mfma_f32_16x16x32_bf16 v[20:23], a[8:11], a[148:151], 0\n\t"
"ds_read_b128 a[160:163], v224 offset:3072\n\t"
"v_cvt_pk_bf16_f32 v209, v209, v213\n\t"
"v_mfma_f32_16x16x32_bf16 v[20:23], a[12:15], a[164:167], v[20:23]\n\t"
"ds_write_addtid_b32 v195 offset:28992\n\t"
"v_mfma_f32_16x16x32_bf16 v[4:7], a[0:3], a[148:151], 0\n\t"
"ds_read_b128 a[148:151], v224 offset:1280\n\t"
"v_cvt_pk_bf16_f32 v216, v216, v220\n\t"
"v_mfma_f32_16x16x32_bf16 v[4:7], a[4:7], a[164:167], v[4:7]\n\t"
"s_waitcnt lgkmcnt(9)\n\t"
"ds_write_addtid_b32 v200 offset:18592\n\t"
"v_cvt_pk_bf16_f32 v202, v202, v206\n\t"
"v_mfma_f32_16x16x32_bf16 v[8:11], a[0:3], a[152:155], 0\n\t"
"ds_read_b128 a[164:167], v224 offset:3328\n\t"
"v_cvt_pk_bf16_f32 v218, v218, v222\n\t"
"v_mfma_f32_16x16x32_bf16 v[8:11], a[4:7], a[168:171], v[8:11]\n\t"
"ds_write_addtid_b32 v219 offset:32064\n\t"
"v_cvt_pk_bf16_f32 v217, v217, v221\n\t"
"v_mfma_f32_16x16x32_bf16 v[24:27], a[8:11], a[152:155], 0\n\t"
"ds_write_addtid_b32 v217 offset:27936\n\t"
"v_cvt_pk_bf16_f32 v194, v194, v198\n\t"
"v_mfma_f32_16x16x32_bf16 v[24:27], a[12:15], a[168:171], v[24:27]\n\t"
"ds_write_addtid_b32 v216 offset:19616\n\t"
"v_cvt_pk_bf16_f32 v210, v210, v214\n\t"
"v_mfma_f32_16x16x32_bf16 v[40:43], a[16:19], a[152:155], 0\n\t"
"ds_read_b128 a[152:155], v224 offset:1536\n\t"
"v_mfma_f32_16x16x32_bf16 v[40:43], a[20:23], a[168:171], v[40:43]\n\t"
"ds_read_b128 a[168:171], v224 offset:3584\n\t"
"v_mfma_f32_16x16x32_bf16 v[44:47], a[16:19], a[156:159], 0\n\t"
"ds_write_addtid_b32 v218 offset:23744\n\t"
"v_mfma_f32_16x16x32_bf16 v[44:47], a[20:23], a[172:175], v[44:47]\n\t"
"ds_write_addtid_b32 v203 offset:31040\n\t"
"v_cvt_pk_bf16_f32 v192, v192, v196\n\t"
"v_mfma_f32_16x16x32_bf16 v[28:31], a[8:11], a[156:159], 0\n\t"
"ds_write_addtid_b32 v210 offset:21696\n\t"
"v_cvt_pk_bf16_f32 v208, v208, v212\n\t"
"v_mfma_f32_16x16x32_bf16 v[28:31], a[12:15], a[172:175], v[28:31]\n\t"
"ds_write_addtid_b32 v208 offset:17568\n\t"
"v_mfma_f32_16x16x32_bf16 v[12:15], a[0:3], a[156:159], 0\n\t"
"ds_write_addtid_b32 v202 offset:22720\n\t"
"v_mfma_f32_16x16x32_bf16 v[12:15], a[4:7], a[172:175], v[12:15]\n\t"
"s_waitcnt lgkmcnt(9)\n\t"
"ds_write_addtid_b32 v192 offset:16544\n\t"
"v_mfma_f32_16x16x32_bf16 v[0:3], a[24:27], a[144:147], v[0:3]\n\t"
"ds_write_addtid_b32 v209 offset:25888\n\t"
"v_mfma_f32_16x16x32_bf16 v[0:3], a[28:31], a[160:163], v[0:3]\n\t"
"ds_write_addtid_b32 v211 offset:30016\n\t"
"v_mfma_f32_16x16x32_bf16 v[16:19], a[32:35], a[144:147], v[16:19]\n\t"
"ds_write_addtid_b32 v194 offset:20672\n\t"
"v_mfma_f32_16x16x32_bf16 v[16:19], a[36:39], a[160:163], v[16:19]\n\t"
"ds_read_b128 a[156:159], v224 offset:1792\n\t"
"v_mfma_f32_16x16x32_bf16 v[32:35], a[40:43], a[144:147], v[32:35]\n\t"
"ds_read_b128 a[172:175], v224 offset:3840\n\t"
"v_mfma_f32_16x16x32_bf16 v[32:35], a[44:47], a[160:163], v[32:35]\n\t"
"ds_read_b64_tr_b16 a[196:197], v251 offset:256\n\t"
"v_mfma_f32_16x16x32_bf16 v[36:39], a[40:43], a[148:151], v[36:39]\n\t"
"ds_read_b64_tr_b16 a[202:203], v251 offset:128\n\t"
"v_mfma_f32_16x16x32_bf16 v[36:39], a[44:47], a[164:167], v[36:39]\n\t"
"ds_read_b64_tr_b16 a[200:201], v251 offset:0\n\t"
"v_mfma_f32_16x16x32_bf16 v[20:23], a[32:35], a[148:151], v[20:23]\n\t"
"ds_read_b64_tr_b16 a[204:205], v250 offset:256\n\t"
"v_mfma_f32_16x16x32_bf16 v[20:23], a[36:39], a[164:167], v[20:23]\n\t"
"v_mfma_f32_16x16x32_bf16 v[4:7], a[24:27], a[148:151], v[4:7]\n\t"
"v_mfma_f32_16x16x32_bf16 v[4:7], a[28:31], a[164:167], v[4:7]\n\t"
"s_waitcnt lgkmcnt(0)\n\t"
"s_barrier\n\t"
"v_mfma_f32_16x16x32_bf16 v[8:11], a[24:27], a[152:155], v[8:11]\n\t"
"ds_read_b128 a[160:163], v224 offset:18592\n\t"
"v_mfma_f32_16x16x32_bf16 v[8:11], a[28:31], a[168:171], v[8:11]\n\t"
"ds_read_b128 a[144:147], v224 offset:16544\n\t"
"v_mfma_f32_16x16x32_bf16 v[24:27], a[32:35], a[152:155], v[24:27]\n\t"
"ds_read_b128 a[148:151], v224 offset:16800\n\t"
"v_swap_b32 v236, v241\n\t"
"v_mfma_f32_16x16x32_bf16 v[24:27], a[36:39], a[168:171], v[24:27]\n\t"
"ds_read_b128 a[164:167], v224 offset:18848\n\t"
"v_mfma_f32_16x16x32_bf16 v[40:43], a[40:43], a[152:155], v[40:43]\n\t"
"v_swap_b32 v237, v242\n\t"
"v_mfma_f32_16x16x32_bf16 v[40:43], a[44:47], a[168:171], v[40:43]\n\t"
"v_swap_b32 v238, v243\n\t"
"v_add_u32 v248, 101632, v227\n\t"
"v_mfma_f32_16x16x32_bf16 v[44:47], a[40:43], a[156:159], v[44:47]\n\t"
"v_swap_b32 v239, v244\n\t"
"v_mfma_f32_16x16x32_bf16 v[44:47], a[44:47], a[172:175], v[44:47]\n\t"
"ds_write_b128 v254, a[244:247] offset:20736\n\t"
"v_mfma_f32_16x16x32_bf16 v[28:31], a[32:35], a[156:159], v[28:31]\n\t"
"v_swap_b32 v240, v245\n\t"
"v_mfma_f32_16x16x32_bf16 v[28:31], a[36:39], a[172:175], v[28:31]\n\t"
"v_mfma_f32_16x16x32_bf16 v[12:15], a[24:27], a[156:159], v[12:15]\n\t"
"v_add_u32 v225, v225, v253\n\t"
"v_mfma_f32_16x16x32_bf16 v[12:15], a[28:31], a[172:175], v[12:15]\n\t"
"ds_read_b128 a[156:159], v224 offset:17312\n\t"
"v_sub_u32 v253, 0, v253\n\t"
"v_mfma_f32_4x4x4_16b_bf16 v[200:203], a[220:221], a[178:179], 0\n\t"
"v_accvgpr_mov_b32 a228, a208\n\t"
"v_mfma_f32_4x4x4_16b_bf16 v[204:207], a[220:221], a[186:187], 0\n\t"
"v_mfma_f32_4x4x4_16b_bf16 v[216:219], a[220:221], a[194:195], 0\n\t"
"v_mfma_f32_4x4x4_16b_bf16 v[220:223], a[220:221], a[202:203], 0\n\t"
"ds_read_b128 a[152:155], v224 offset:17056\n\t"
"v_mfma_f32_4x4x4_16b_bf16 v[192:195], a[222:223], a[176:177], v[200:203]\n\t"
"v_xor_b32 v249, 32, v248\n\t"
"v_mfma_f32_4x4x4_16b_bf16 v[196:199], a[222:223], a[184:185], v[204:207]\n\t"
"v_mfma_f32_4x4x4_16b_bf16 v[200:203], a[222:223], a[180:181], v[200:203]\n\t"
"ds_read_b128 a[168:171], v224 offset:19104\n\t"
"v_mfma_f32_4x4x4_16b_bf16 v[204:207], a[222:223], a[188:189], v[204:207]\n\t"
"v_accvgpr_mov_b32 a208, a209\n\t"
"v_mfma_f32_4x4x4_16b_bf16 v[208:211], a[222:223], a[192:193], v[216:219]\n\t"
"v_xor_b32 v250, 64, v248\n\t"
"v_mfma_f32_4x4x4_16b_bf16 v[212:215], a[222:223], a[200:201], v[220:223]\n\t"
"v_xor_b32 v251, 96, v248\n\t"
"v_mfma_f32_4x4x4_16b_bf16 v[216:219], a[222:223], a[196:197], v[216:219]\n\t"
"ds_read_b128 a[172:175], v224 offset:19360\n\t"
"v_mfma_f32_4x4x4_16b_bf16 v[220:223], a[222:223], a[204:205], v[220:223]\n\t"
"s_waitcnt vmcnt(15) lgkmcnt(3)\n\t"
"buffer_load_dwordx4 a[16:19], off, s[28:31], s57 offset:0 sc1\n\t"
"v_mfma_f32_16x16x32_bf16 v[144:147], a[96:99], a[144:147], v[144:147]\n\t"
"v_cvt_pk_bf16_f32 v193, -v193, -v197\n\t"
"v_cvt_pk_bf16_f32 v194, -v194, -v198\n\t"
"v_mfma_f32_16x16x32_bf16 v[144:147], a[100:103], a[160:163], v[144:147]\n\t"
"ds_write_addtid_b32 v193 offset:4128\n\t"
"v_cvt_pk_bf16_f32 v192, -v192, -v196\n\t"
"v_mfma_f32_16x16x32_bf16 v[160:163], a[104:107], a[144:147], v[160:163]\n\t"
"ds_write_addtid_b32 v194 offset:1024\n\t"
"v_cvt_pk_bf16_f32 v200, v200, v204\n\t"
"v_mfma_f32_16x16x32_bf16 v[160:163], a[108:111], a[160:163], v[160:163]\n\t"
"buffer_load_dwordx4 a[20:23], off, s[28:31], s57 offset:1024 sc1\n\t"
"v_mfma_f32_16x16x32_bf16 v[176:179], a[112:115], a[144:147], v[176:179]\n\t"
"ds_read_b128 a[144:147], v224 offset:17568\n\t"
"v_cvt_pk_bf16_f32 v211, -v211, -v215\n\t"
"v_mfma_f32_16x16x32_bf16 v[176:179], a[116:119], a[160:163], v[176:179]\n\t"
"buffer_load_dwordx4 a[12:15], off, s[84:87], s57 offset:3072 sc1\n\t"
"v_mfma_f32_16x16x32_bf16 v[180:183], a[112:115], a[148:151], v[180:183]\n\t"
"ds_read_b128 a[160:163], v224 offset:19616\n\t"
"v_cvt_pk_bf16_f32 v208, -v208, -v212\n\t"
"v_mfma_f32_16x16x32_bf16 v[180:183], a[116:119], a[164:167], v[180:183]\n\t"
"buffer_load_dwordx4 a[8:11], off, s[84:87], s57 offset:2048 sc1\n\t"
"v_mfma_f32_16x16x32_bf16 v[164:167], a[104:107], a[148:151], v[164:167]\n\t"
"ds_write_addtid_b32 v211 offset:7200\n\t"
"v_cvt_pk_bf16_f32 v201, v201, v205\n\t"
"v_mfma_f32_16x16x32_bf16 v[164:167], a[108:111], a[164:167], v[164:167]\n\t"
"s_nop 0\n\t"
"ds_write_addtid_b32 v208 offset:2048\n\t"
"v_cvt_pk_bf16_f32 v209, -v209, -v213\n\t"
"v_mfma_f32_16x16x32_bf16 v[148:151], a[96:99], a[148:151], v[148:151]\n\t"
"s_nop 0\n\t"
"ds_read_b128 a[148:151], v224 offset:17824\n\t"
"v_cvt_pk_bf16_f32 v218, v218, v222\n\t"
"v_mfma_f32_16x16x32_bf16 v[148:151], a[100:103], a[164:167], v[148:151]\n\t"
"s_waitcnt lgkmcnt(6)\n\t"
"buffer_load_dwordx4 a[0:3], off, s[84:87], s57 offset:0 sc1\n\t"
"v_mfma_f32_16x16x32_bf16 v[152:155], a[96:99], a[152:155], v[152:155]\n\t"
"s_nop 0\n\t"
"ds_read_b128 a[164:167], v224 offset:19872\n\t"
"v_cvt_pk_bf16_f32 v203, v203, v207\n\t"
"v_mfma_f32_16x16x32_bf16 v[152:155], a[100:103], a[168:171], v[152:155]\n\t"
"s_nop 0\n\t"
"ds_write_addtid_b32 v218 offset:11392\n\t"
"v_cvt_pk_bf16_f32 v219, v219, v223\n\t"
"v_mfma_f32_16x16x32_bf16 v[168:171], a[104:107], a[152:155], v[168:171]\n\t"
"s_nop 0\n\t"
"ds_write_addtid_b32 v200 offset:8320\n\t"
"v_cvt_pk_bf16_f32 v216, v216, v220\n\t"
"v_mfma_f32_16x16x32_bf16 v[168:171], a[108:111], a[168:171], v[168:171]\n\t"
"buffer_load_dwordx4 a[4:7], off, s[84:87], s57 offset:1024 sc1\n\t"
"v_mfma_f32_16x16x32_bf16 v[184:187], a[112:115], a[152:155], v[184:187]\n\t"
"s_nop 0\n\t"
"ds_read_b128 a[152:155], v224 offset:18080\n\t"
"v_cvt_pk_bf16_f32 v195, -v195, -v199\n\t"
"v_mfma_f32_16x16x32_bf16 v[184:187], a[116:119], a[168:171], v[184:187]\n\t"
"s_nop 0\n\t"
"ds_read_b128 a[168:171], v224 offset:20128\n\t"
"v_cvt_pk_bf16_f32 v217, v217, v221\n\t"
"v_mfma_f32_16x16x32_bf16 v[188:191], a[112:115], a[156:159], v[188:191]\n\t"
"s_nop 0\n\t"
"ds_write_addtid_b32 v219 offset:15520\n\t"
"v_cvt_pk_bf16_f32 v202, v202, v206\n\t"
"v_mfma_f32_16x16x32_bf16 v[188:191], a[116:119], a[172:175], v[188:191]\n\t"
"buffer_load_dwordx4 a[40:43], off, s[28:31], s63 offset:0 sc1\n\t"
"v_mfma_f32_16x16x32_bf16 v[172:175], a[104:107], a[156:159], v[172:175]\n\t"
"s_nop 0\n\t"
"ds_write_addtid_b32 v209 offset:6176\n\t"
"v_cvt_pk_bf16_f32 v210, -v210, -v214\n\t"
"v_mfma_f32_16x16x32_bf16 v[172:175], a[108:111], a[172:175], v[172:175]\n\t"
"s_nop 0\n\t"
"ds_write_addtid_b32 v202 offset:9344\n\t"
"ds_write_addtid_b32 v216 offset:10368\n\t"
"v_mfma_f32_16x16x32_bf16 v[156:159], a[96:99], a[156:159], v[156:159]\n\t"
"s_nop 0\n\t"
"ds_write_addtid_b32 v203 offset:13472\n\t"
"v_mfma_f32_16x16x32_bf16 v[156:159], a[100:103], a[172:175], v[156:159]\n\t"
"s_waitcnt vmcnt(15) lgkmcnt(8)\n\t"
"buffer_load_dwordx4 a[44:47], off, s[28:31], s63 offset:1024 sc1\n\t"
"v_mfma_f32_16x16x32_bf16 v[144:147], a[120:123], a[144:147], v[144:147]\n\t"
"s_nop 0\n\t"
"ds_write_addtid_b32 v201 offset:12448\n\t"
"ds_write_addtid_b32 v210 offset:3072\n\t"
"v_mfma_f32_16x16x32_bf16 v[144:147], a[124:127], a[160:163], v[144:147]\n\t"
"s_nop 0\n\t"
"ds_write_addtid_b32 v217 offset:14496\n\t"
"v_mfma_f32_16x16x32_bf16 v[160:163], a[128:131], a[144:147], v[160:163]\n\t"
"s_nop 0\n\t"
"ds_write_addtid_b32 v192 offset:0\n\t"
"ds_write_addtid_b32 v195 offset:5152\n\t"
"v_mfma_f32_16x16x32_bf16 v[160:163], a[132:135], a[160:163], v[160:163]\n\t"
"buffer_load_dwordx4 a[36:39], off, s[84:87], s63 offset:3072 sc1\n\t"
"v_mfma_f32_16x16x32_bf16 v[176:179], a[136:139], a[144:147], v[176:179]\n\t"
"s_nop 0\n\t"
"ds_read_b128 a[156:159], v224 offset:18336\n\t"
"v_mfma_f32_16x16x32_bf16 v[176:179], a[140:143], a[160:163], v[176:179]\n\t"
"buffer_load_dwordx4 a[32:35], off, s[84:87], s63 offset:2048 sc1\n\t"
"v_mfma_f32_16x16x32_bf16 v[180:183], a[136:139], a[148:151], v[180:183]\n\t"
"s_nop 0\n\t"
"ds_read_b128 a[172:175], v224 offset:20384\n\t"
"v_mfma_f32_16x16x32_bf16 v[180:183], a[140:143], a[164:167], v[180:183]\n\t"
"buffer_load_dwordx4 a[24:27], off, s[84:87], s63 offset:0 sc1\n\t"
"v_mfma_f32_16x16x32_bf16 v[164:167], a[128:131], a[148:151], v[164:167]\n\t"
"Lpad7_%=:\n\t"
"s_cbranch_execz Lret0_%=\n\t"
"v_mfma_f32_16x16x32_bf16 v[164:167], a[132:135], a[164:167], v[164:167]\n\t"
"buffer_load_dwordx4 a[28:31], off, s[84:87], s63 offset:1024 sc1\n\t"
"v_mfma_f32_16x16x32_bf16 v[148:151], a[120:123], a[148:151], v[148:151]\n\t"
"v_accvgpr_mov_b32 a209, a228\n\t"
"v_mfma_f32_16x16x32_bf16 v[148:151], a[124:127], a[164:167], v[148:151]\n\t"
"s_waitcnt vmcnt(13) lgkmcnt(4)\n\t"
"s_waitcnt lgkmcnt(0)\n\t"
"s_barrier\n\t"
"v_mfma_f32_16x16x32_bf16 v[152:155], a[120:123], a[152:155], v[152:155]\n\t"
"s_add_u32 m0, s26, 56192\n\t"
"ds_read_b128 a[144:147], v224 offset:0\n\t"
"ds_read_b128 a[160:163], v224 offset:2048\n\t"
"v_mfma_f32_16x16x32_bf16 v[152:155], a[124:127], a[168:171], v[152:155]\n\t"
"buffer_load_dwordx4 v236, s[12:15], s22 offen offset:128 nt lds\n\t"
"v_mfma_f32_16x16x32_bf16 v[168:171], a[128:131], a[152:155], v[168:171]\n\t"
"ds_read_b128 a[148:151], v224 offset:256\n\t"
"v_mfma_f32_16x16x32_bf16 v[168:171], a[132:135], a[168:171], v[168:171]\n\t"
"ds_read_b128 a[164:167], v224 offset:2304\n\t"
"v_mfma_f32_16x16x32_bf16 v[184:187], a[136:139], a[152:155], v[184:187]\n\t"
"s_nop 0\n\t"
"ds_read_b64_tr_b16 a[176:177], v248 offset:0\n\t"
"ds_read_b64_tr_b16 a[178:179], v248 offset:512\n\t"
"v_mfma_f32_16x16x32_bf16 v[184:187], a[140:143], a[168:171], v[184:187]\n\t"
"buffer_store_dwordx4 a[232:235], v233, s[16:19], s25 offen nt\n\t"
"v_mfma_f32_16x16x32_bf16 v[188:191], a[136:139], a[156:159], v[188:191]\n\t"
"s_nop 0\n\t"
"ds_read_b128 a[152:155], v224 offset:512\n\t"
"ds_read_b64_tr_b16 a[180:181], v249 offset:0\n\t"
"v_mfma_f32_16x16x32_bf16 v[188:191], a[140:143], a[172:175], v[188:191]\n\t"
"ds_read_b64_tr_b16 a[182:183], v249 offset:512\n\t"
"v_mfma_f32_16x16x32_bf16 v[172:175], a[128:131], a[156:159], v[172:175]\n\t"
"buffer_load_dwordx4 v237, s[12:15], s22 offen offset:1152 nt lds\n\t"
"v_mfma_f32_16x16x32_bf16 v[172:175], a[132:135], a[172:175], v[172:175]\n\t"
"s_nop 0\n\t"
"ds_read_b64_tr_b16 a[184:185], v250 offset:0\n\t"
"ds_read_b64_tr_b16 a[186:187], v250 offset:512\n\t"
"v_mfma_f32_16x16x32_bf16 v[156:159], a[120:123], a[156:159], v[156:159]\n\t"
"s_nop 0\n\t"
"ds_read_b128 a[168:171], v224 offset:2560\n\t"
"ds_read_b128 a[156:159], v224 offset:768\n\t"
"v_mfma_f32_16x16x32_bf16 v[156:159], a[124:127], a[172:175], v[156:159]\n\t"
"s_waitcnt vmcnt(9) lgkmcnt(11)\n\t"
".p2align 3\n\t"
"buffer_store_dwordx4 a[236:239], v234, s[16:19], s25 offen nt\n\t"
"v_pk_add_f32 v[144:145], v[48:49], v[144:145] neg_lo:[0,1] neg_hi:[0,1]\n\t"
"v_pk_add_f32 v[146:147], v[50:51], v[146:147] neg_lo:[0,1] neg_hi:[0,1]\n\t"
"v_pk_fma_f32 v[48:49], v[144:145], s[90:91], v[48:49] neg_lo:[1,0,0] neg_hi:[1,0,0]\n\t"
"v_pk_fma_f32 v[50:51], v[146:147], s[90:91], v[50:51] neg_lo:[1,0,0] neg_hi:[1,0,0]\n\t"
"v_pk_add_f32 v[160:161], v[64:65], v[160:161] neg_lo:[0,1] neg_hi:[0,1]\n\t"
"v_pk_add_f32 v[162:163], v[66:67], v[162:163] neg_lo:[0,1] neg_hi:[0,1]\n\t"
"v_pk_fma_f32 v[64:65], v[160:161], s[90:91], v[64:65] neg_lo:[1,0,0] neg_hi:[1,0,0]\n\t"
"v_pk_fma_f32 v[66:67], v[162:163], s[90:91], v[66:67] neg_lo:[1,0,0] neg_hi:[1,0,0]\n\t"
"v_pk_add_f32 v[176:177], v[80:81], v[176:177] neg_lo:[0,1] neg_hi:[0,1]\n\t"
"v_pk_add_f32 v[178:179], v[82:83], v[178:179] neg_lo:[0,1] neg_hi:[0,1]\n\t"
"v_pk_fma_f32 v[80:81], v[176:177], s[90:91], v[80:81] neg_lo:[1,0,0] neg_hi:[1,0,0]\n\t"
"v_pk_fma_f32 v[82:83], v[178:179], s[90:91], v[82:83] neg_lo:[1,0,0] neg_hi:[1,0,0]\n\t"
"v_pk_add_f32 v[180:181], v[84:85], v[180:181] neg_lo:[0,1] neg_hi:[0,1]\n\t"
"v_pk_add_f32 v[182:183], v[86:87], v[182:183] neg_lo:[0,1] neg_hi:[0,1]\n\t"
"v_pk_fma_f32 v[84:85], v[180:181], s[90:91], v[84:85] neg_lo:[1,0,0] neg_hi:[1,0,0]\n\t"
"v_pk_fma_f32 v[86:87], v[182:183], s[90:91], v[86:87] neg_lo:[1,0,0] neg_hi:[1,0,0]\n\t"
"v_pk_add_f32 v[164:165], v[68:69], v[164:165] neg_lo:[0,1] neg_hi:[0,1]\n\t"
"v_pk_add_f32 v[166:167], v[70:71], v[166:167] neg_lo:[0,1] neg_hi:[0,1]\n\t"
"v_pk_fma_f32 v[68:69], v[164:165], s[90:91], v[68:69] neg_lo:[1,0,0] neg_hi:[1,0,0]\n\t"
"v_pk_fma_f32 v[70:71], v[166:167], s[90:91], v[70:71] neg_lo:[1,0,0] neg_hi:[1,0,0]\n\t"
"v_pk_add_f32 v[148:149], v[52:53], v[148:149] neg_lo:[0,1] neg_hi:[0,1]\n\t"
"v_pk_add_f32 v[150:151], v[54:55], v[150:151] neg_lo:[0,1] neg_hi:[0,1]\n\t"
"v_pk_fma_f32 v[52:53], v[148:149], s[90:91], v[52:53] neg_lo:[1,0,0] neg_hi:[1,0,0]\n\t"
"v_pk_fma_f32 v[54:55], v[150:151], s[90:91], v[54:55] neg_lo:[1,0,0] neg_hi:[1,0,0]\n\t"
"v_pk_add_f32 v[152:153], v[56:57], v[152:153] neg_lo:[0,1] neg_hi:[0,1]\n\t"
"v_pk_add_f32 v[154:155], v[58:59], v[154:155] neg_lo:[0,1] neg_hi:[0,1]\n\t"
"v_pk_fma_f32 v[56:57], v[152:153], s[90:91], v[56:57] neg_lo:[1,0,0] neg_hi:[1,0,0]\n\t"
"v_pk_fma_f32 v[58:59], v[154:155], s[90:91], v[58:59] neg_lo:[1,0,0] neg_hi:[1,0,0]\n\t"
"v_pk_add_f32 v[168:169], v[72:73], v[168:169] neg_lo:[0,1] neg_hi:[0,1]\n\t"
"v_pk_add_f32 v[170:171], v[74:75], v[170:171] neg_lo:[0,1] neg_hi:[0,1]\n\t"
"v_pk_fma_f32 v[72:73], v[168:169], s[90:91], v[72:73] neg_lo:[1,0,0] neg_hi:[1,0,0]\n\t"
"v_pk_fma_f32 v[74:75], v[170:171], s[90:91], v[74:75] neg_lo:[1,0,0] neg_hi:[1,0,0]\n\t"
"v_pk_add_f32 v[184:185], v[88:89], v[184:185] neg_lo:[0,1] neg_hi:[0,1]\n\t"
"v_pk_add_f32 v[186:187], v[90:91], v[186:187] neg_lo:[0,1] neg_hi:[0,1]\n\t"
"v_pk_fma_f32 v[88:89], v[184:185], s[90:91], v[88:89] neg_lo:[1,0,0] neg_hi:[1,0,0]\n\t"
"v_pk_fma_f32 v[90:91], v[186:187], s[90:91], v[90:91] neg_lo:[1,0,0] neg_hi:[1,0,0]\n\t"
"v_pk_add_f32 v[188:189], v[92:93], v[188:189] neg_lo:[0,1] neg_hi:[0,1]\n\t"
"v_pk_add_f32 v[190:191], v[94:95], v[190:191] neg_lo:[0,1] neg_hi:[0,1]\n\t"
"v_pk_fma_f32 v[92:93], v[188:189], s[90:91], v[92:93] neg_lo:[1,0,0] neg_hi:[1,0,0]\n\t"
"v_pk_fma_f32 v[94:95], v[190:191], s[90:91], v[94:95] neg_lo:[1,0,0] neg_hi:[1,0,0]\n\t"
"v_pk_add_f32 v[172:173], v[76:77], v[172:173] neg_lo:[0,1] neg_hi:[0,1]\n\t"
"v_pk_add_f32 v[174:175], v[78:79], v[174:175] neg_lo:[0,1] neg_hi:[0,1]\n\t"
"v_pk_fma_f32 v[76:77], v[172:173], s[90:91], v[76:77] neg_lo:[1,0,0] neg_hi:[1,0,0]\n\t"
"v_pk_fma_f32 v[78:79], v[174:175], s[90:91], v[78:79] neg_lo:[1,0,0] neg_hi:[1,0,0]\n\t"
"v_pk_add_f32 v[156:157], v[60:61], v[156:157] neg_lo:[0,1] neg_hi:[0,1]\n\t"
"v_pk_add_f32 v[158:159], v[62:63], v[158:159] neg_lo:[0,1] neg_hi:[0,1]\n\t"
"v_pk_fma_f32 v[60:61], v[156:157], s[90:91], v[60:61] neg_lo:[1,0,0] neg_hi:[1,0,0]\n\t"
"v_pk_fma_f32 v[62:63], v[158:159], s[90:91], v[62:63] neg_lo:[1,0,0] neg_hi:[1,0,0]\n\t"
"v_mfma_f32_16x16x32_bf16 v[48:51], a[0:3], a[144:147], v[48:51]\n\t"
"s_nop 0\n\t"
"ds_read_b128 a[172:175], v224 offset:2816\n\t"
"ds_read_b64_tr_b16 a[188:189], v251 offset:0\n\t"
"v_mfma_f32_16x16x32_bf16 v[48:51], a[4:7], a[160:163], v[48:51]\n\t"
"s_nop 0\n\t"
"ds_read_b64_tr_b16 a[190:191], v251 offset:512\n\t"
"ds_read_b64_tr_b16 a[192:193], v248 offset:1152\n\t"
"v_mfma_f32_16x16x32_bf16 v[64:67], a[8:11], a[144:147], v[64:67]\n\t"
"ds_read_b64_tr_b16 a[194:195], v248 offset:1664\n\t"
"v_mfma_f32_16x16x32_bf16 v[64:67], a[12:15], a[160:163], v[64:67]\n\t"
"buffer_load_dwordx4 v238, s[12:15], s22 offen offset:2176 nt lds\n\t"
"v_mfma_f32_16x16x32_bf16 v[80:83], a[16:19], a[144:147], v[80:83]\n\t"
"s_nop 0\n\t"
"ds_read_b64_tr_b16 a[196:197], v249 offset:1152\n\t"
"ds_read_b64_tr_b16 a[198:199], v249 offset:1664\n\t"
"v_mfma_f32_16x16x32_bf16 v[80:83], a[20:23], a[160:163], v[80:83]\n\t"
"s_waitcnt lgkmcnt(11)\n\t"
"ds_read_b128 a[160:163], v224 offset:3072\n\t"
"v_mfma_f32_16x16x32_bf16 v[192:195], a[216:219], a[176:179], 0\n\t"
"ds_read_b64_tr_b16 a[200:201], v250 offset:1152\n\t"
"v_mfma_f32_16x16x32_bf16 v[196:199], a[216:219], a[180:183], 0\n\t"
"s_waitcnt lgkmcnt(11)\n\t"
"ds_read_b64_tr_b16 a[202:203], v250 offset:1664\n\t"
"v_mfma_f32_16x16x32_bf16 v[200:203], a[216:219], a[184:187], 0\n\t"
"s_waitcnt lgkmcnt(7)\n\t"
"ds_read_b64_tr_b16 a[204:205], v251 offset:1152\n\t"
"ds_read_b64_tr_b16 a[206:207], v251 offset:1664\n\t"
"v_mfma_f32_16x16x32_bf16 v[204:207], a[216:219], a[188:191], 0\n\t"
"s_waitcnt lgkmcnt(7)\n\t"
"buffer_load_dwordx4 v239, s[12:15], s22 offen offset:3200 nt lds\n\t"
"v_mfma_f32_16x16x32_bf16 v[208:211], a[216:219], a[192:195], 0\n\t"
"s_waitcnt lgkmcnt(5)\n\t"
"ds_read_b64_tr_b16 a[176:177], v248 offset:42624\n\t"
"ds_read_b64_tr_b16 a[178:179], v248 offset:43136\n\t"
"v_mfma_f32_16x16x32_bf16 v[212:215], a[216:219], a[196:199], 0\n\t"
"s_waitcnt lgkmcnt(4)\n\t"
"ds_read_b64_tr_b16 a[180:181], v249 offset:42624\n\t"
"v_mfma_f32_16x16x32_bf16 v[216:219], a[216:219], a[200:203], 0\n\t"
"s_waitcnt lgkmcnt(3)\n\t"
"ds_read_b64_tr_b16 a[182:183], v249 offset:43136\n\t"
"ds_read_b64_tr_b16 a[184:185], v250 offset:42624\n\t"
"v_mfma_f32_16x16x32_bf16 v[220:223], a[216:219], a[204:207], 0\n\t"
"s_mov_b64 exec, s[36:37]\n\t"
"buffer_load_dwordx4 v240, s[12:15], s22 offen offset:3712 nt lds\n\t"
"s_mov_b64 exec, -1\n\t"
"v_mfma_f32_16x16x32_bf16 v[84:87], a[16:19], a[148:151], v[84:87]\n\t"
"s_lshl_b32 m0, s73, 8\n\t"
"ds_read_b64_tr_b16 a[186:187], v250 offset:43136\n\t"
"v_cvt_pk_bf16_f32 v193, v193, v197\n\t"
"v_mfma_f32_16x16x32_bf16 v[84:87], a[20:23], a[164:167], v[84:87]\n\t"
"ds_read_b128 a[144:147], v224 offset:1024\n\t"
"v_cvt_pk_bf16_f32 v201, v201, v205\n\t"
"v_mfma_f32_16x16x32_bf16 v[68:71], a[8:11], a[148:151], v[68:71]\n\t"
"ds_read_b64_tr_b16 a[188:189], v251 offset:42624\n\t"
"v_cvt_pk_bf16_f32 v200, v200, v204\n\t"
"v_mfma_f32_16x16x32_bf16 v[68:71], a[12:15], a[164:167], v[68:71]\n\t"
"ds_read_b64_tr_b16 a[190:191], v251 offset:43136\n\t"
"v_cvt_pk_bf16_f32 v194, v194, v198\n\t"
"v_mfma_f32_16x16x32_bf16 v[52:55], a[0:3], a[148:151], v[52:55]\n\t"
"ds_read_b64_tr_b16 a[192:193], v248 offset:21888\n\t"
"v_cvt_pk_bf16_f32 v210, -v210, -v214\n\t"
"v_mfma_f32_16x16x32_bf16 v[52:55], a[4:7], a[164:167], v[52:55]\n\t"
"ds_read_b128 a[148:151], v224 offset:1280\n\t"
"v_cvt_pk_bf16_f32 v211, -v211, -v215\n\t"
"v_mfma_f32_16x16x32_bf16 v[56:59], a[0:3], a[152:155], v[56:59]\n\t"
"ds_read_b128 a[164:167], v224 offset:3328\n\t"
"v_cvt_pk_bf16_f32 v195, v195, v199\n\t"
"v_mfma_f32_16x16x32_bf16 v[56:59], a[4:7], a[168:171], v[56:59]\n\t"
"ds_write_addtid_b32 v195 offset:28992\n\t"
"v_cvt_pk_bf16_f32 v209, -v209, -v213\n\t"
"v_mfma_f32_16x16x32_bf16 v[72:75], a[8:11], a[152:155], v[72:75]\n\t"
"ds_write_addtid_b32 v211 offset:30016\n\t"
"v_cvt_pk_bf16_f32 v217, -v217, -v221\n\t"
"v_mfma_f32_16x16x32_bf16 v[72:75], a[12:15], a[168:171], v[72:75]\n\t"
"s_nop 0\n\t"
"ds_write_addtid_b32 v193 offset:24864\n\t"
"ds_write_addtid_b32 v210 offset:21696\n\t"
"v_mfma_f32_16x16x32_bf16 v[88:91], a[16:19], a[152:155], v[88:91]\n\t"
"ds_read_b128 a[152:155], v224 offset:1536\n\t"
"v_cvt_pk_bf16_f32 v208, -v208, -v212\n\t"
"v_mfma_f32_16x16x32_bf16 v[88:91], a[20:23], a[168:171], v[88:91]\n\t"
"ds_read_b128 a[168:171], v224 offset:3584\n\t"
"v_cvt_pk_bf16_f32 v202, v202, v206\n\t"
"v_mfma_f32_16x16x32_bf16 v[92:95], a[16:19], a[156:159], v[92:95]\n\t"
"ds_write_addtid_b32 v217 offset:27936\n\t"
"v_cvt_pk_bf16_f32 v218, -v218, -v222\n\t"
"v_mfma_f32_16x16x32_bf16 v[92:95], a[20:23], a[172:175], v[92:95]\n\t"
"ds_write_addtid_b32 v202 offset:22720\n\t"
"v_cvt_pk_bf16_f32 v219, -v219, -v223\n\t"
"v_mfma_f32_16x16x32_bf16 v[76:79], a[8:11], a[156:159], v[76:79]\n\t"
"s_nop 0\n\t"
"ds_write_addtid_b32 v200 offset:18592\n\t"
"ds_write_addtid_b32 v218 offset:23744\n\t"
"v_mfma_f32_16x16x32_bf16 v[76:79], a[12:15], a[172:175], v[76:79]\n\t"
"s_nop 0\n\t"
"ds_write_addtid_b32 v208 offset:17568\n\t"
"ds_write_addtid_b32 v209 offset:25888\n\t"
"v_mfma_f32_16x16x32_bf16 v[60:63], a[0:3], a[156:159], v[60:63]\n\t"
"ds_read_b128 a[156:159], v224 offset:1792\n\t"
"v_cvt_pk_bf16_f32 v216, -v216, -v220\n\t"
"v_mfma_f32_16x16x32_bf16 v[60:63], a[4:7], a[172:175], v[60:63]\n\t"
"s_waitcnt vmcnt(7) lgkmcnt(12)\n\t"
"ds_write_addtid_b32 v219 offset:32064\n\t"
"v_cvt_pk_bf16_f32 v203, v203, v207\n\t"
"v_mfma_f32_16x16x32_bf16 v[144:147], a[24:27], a[144:147], v[144:147]\n\t"
"s_nop 0\n\t"
"ds_write_addtid_b32 v201 offset:26912\n\t"
"ds_write_addtid_b32 v216 offset:19616\n\t"
"v_mfma_f32_16x16x32_bf16 v[144:147], a[28:31], a[160:163], v[144:147]\n\t"
"ds_write_addtid_b32 v203 offset:31040\n\t"
"v_cvt_pk_bf16_f32 v192, v192, v196\n\t"
"v_mfma_f32_16x16x32_bf16 v[160:163], a[32:35], a[144:147], v[160:163]\n\t"
"s_nop 0\n\t"
"ds_write_addtid_b32 v192 offset:16544\n\t"
"ds_write_addtid_b32 v194 offset:20672\n\t"
"v_mfma_f32_16x16x32_bf16 v[160:163], a[36:39], a[160:163], v[160:163]\n\t"
"ds_read_b128 a[172:175], v224 offset:3840\n\t"
"v_mfma_f32_16x16x32_bf16 v[176:179], a[40:43], a[144:147], v[176:179]\n\t"
"ds_read_b64_tr_b16 a[194:195], v248 offset:22400\n\t"
"v_mfma_f32_16x16x32_bf16 v[176:179], a[44:47], a[160:163], v[176:179]\n\t"
"ds_read_b64_tr_b16 a[196:197], v249 offset:21888\n\t"
"v_mfma_f32_16x16x32_bf16 v[180:183], a[40:43], a[148:151], v[180:183]\n\t"
"ds_read_b64_tr_b16 a[198:199], v249 offset:22400\n\t"
"v_mfma_f32_16x16x32_bf16 v[180:183], a[44:47], a[164:167], v[180:183]\n\t"
"s_nop 0\n\t"
"ds_read_b64_tr_b16 a[200:201], v250 offset:21888\n\t"
"ds_read_b64_tr_b16 a[202:203], v250 offset:22400\n\t"
"v_mfma_f32_16x16x32_bf16 v[164:167], a[32:35], a[148:151], v[164:167]\n\t"
"s_nop 0\n\t"
"ds_read_b64_tr_b16 a[204:205], v251 offset:21888\n\t"
"ds_read_b64_tr_b16 a[206:207], v251 offset:22400\n\t"
"v_mfma_f32_16x16x32_bf16 v[164:167], a[36:39], a[164:167], v[164:167]\n\t"
"v_mfma_f32_16x16x32_bf16 v[148:151], a[24:27], a[148:151], v[148:151]\n\t"
"s_waitcnt lgkmcnt(7)\n\t"
"v_mfma_f32_16x16x32_bf16 v[148:151], a[28:31], a[164:167], v[148:151]\n\t"
"s_barrier\n\t"
"v_mfma_f32_16x16x32_bf16 v[152:155], a[24:27], a[152:155], v[152:155]\n\t"
"ds_read_b128 a[148:151], v224 offset:16800\n\t"
"v_add_u32 v248, 33280, v226\n\t"
"v_mfma_f32_16x16x32_bf16 v[152:155], a[28:31], a[168:171], v[152:155]\n\t"
"ds_read_b128 a[144:147], v224 offset:16544\n\t"
"v_xor_b32 v249, 32, v248\n\t"
"v_mfma_f32_16x16x32_bf16 v[168:171], a[32:35], a[152:155], v[168:171]\n\t"
"ds_read_b128 a[160:163], v224 offset:18592\n\t"
"v_mfma_f32_16x16x32_bf16 v[168:171], a[36:39], a[168:171], v[168:171]\n\t"
"ds_read_b128 a[164:167], v224 offset:18848\n\t"
"v_mfma_f32_16x16x32_bf16 v[184:187], a[40:43], a[152:155], v[184:187]\n\t"
"v_mfma_f32_16x16x32_bf16 v[184:187], a[44:47], a[168:171], v[184:187]\n\t"
"v_mfma_f32_16x16x32_bf16 v[188:191], a[40:43], a[156:159], v[188:191]\n\t"
"ds_read_b128 a[152:155], v224 offset:17056\n\t"
"v_mfma_f32_16x16x32_bf16 v[188:191], a[44:47], a[172:175], v[188:191]\n\t"
"ds_read_b128 a[168:171], v224 offset:19104\n\t"
"v_xor_b32 v250, 64, v248\n\t"
"v_mfma_f32_16x16x32_bf16 v[172:175], a[32:35], a[156:159], v[172:175]\n\t"
"v_mfma_f32_16x16x32_bf16 v[172:175], a[36:39], a[172:175], v[172:175]\n\t"
"v_mfma_f32_16x16x32_bf16 v[156:159], a[24:27], a[156:159], v[156:159]\n\t"
"ds_read_b128 a[156:159], v224 offset:17312\n\t"
"v_xor_b32 v251, 96, v248\n\t"
"v_mfma_f32_16x16x32_bf16 v[156:159], a[28:31], a[172:175], v[156:159]\n\t"
"ds_read_b128 a[172:175], v224 offset:19360\n\t"
"v_mfma_f32_16x16x32_bf16 v[192:195], a[216:219], a[176:179], 0\n\t"
"ds_read_b64_tr_b16 a[178:179], v248 offset:128\n\t"
"v_mfma_f32_16x16x32_bf16 v[196:199], a[216:219], a[180:183], 0\n\t"
"v_mfma_f32_16x16x32_bf16 v[200:203], a[216:219], a[184:187], 0\n\t"
"ds_read_b64_tr_b16 a[176:177], v248 offset:0\n\t"
"v_mfma_f32_16x16x32_bf16 v[204:207], a[216:219], a[188:191], 0\n\t"
"s_waitcnt lgkmcnt(6)\n\t"
"ds_read_b64_tr_b16 a[180:181], v249 offset:256\n\t"
"v_mfma_f32_16x16x32_bf16 v[208:211], a[216:219], a[192:195], 0\n\t"
"ds_read_b64_tr_b16 a[186:187], v249 offset:128\n\t"
"v_mfma_f32_16x16x32_bf16 v[212:215], a[216:219], a[196:199], 0\n\t"
"v_mfma_f32_16x16x32_bf16 v[216:219], a[216:219], a[200:203], 0\n\t"
"ds_read_b64_tr_b16 a[184:185], v249 offset:0\n\t"
"v_mfma_f32_16x16x32_bf16 v[220:223], a[216:219], a[204:207], 0\n\t"
"ds_read_b64_tr_b16 a[188:189], v248 offset:256\n\t"
"v_mfma_f32_16x16x32_bf16 v[0:3], a[48:51], a[144:147], v[0:3]\n\t"
"ds_read_b64_tr_b16 a[194:195], v250 offset:128\n\t"
"v_mfma_f32_16x16x32_bf16 v[0:3], a[52:55], a[160:163], v[0:3]\n\t"
"v_cvt_pk_bf16_f32 v195, -v195, -v199\n\t"
"v_cvt_pk_bf16_f32 v201, -v201, -v205\n\t"
"v_mfma_f32_16x16x32_bf16 v[16:19], a[56:59], a[144:147], v[16:19]\n\t"
"ds_read_b64_tr_b16 a[192:193], v250 offset:0\n\t"
"v_mfma_f32_16x16x32_bf16 v[16:19], a[60:63], a[160:163], v[16:19]\n\t"
"ds_read_b64_tr_b16 a[196:197], v251 offset:256\n\t"
"v_cvt_pk_bf16_f32 v200, -v200, -v204\n\t"
"v_mfma_f32_16x16x32_bf16 v[32:35], a[64:67], a[144:147], v[32:35]\n\t"
"ds_read_b128 a[144:147], v224 offset:17568\n\t"
"v_cvt_pk_bf16_f32 v209, -v209, -v213\n\t"
"v_mfma_f32_16x16x32_bf16 v[32:35], a[68:71], a[160:163], v[32:35]\n\t"
"v_cvt_pk_bf16_f32 v203, -v203, -v207\n\t"
"v_cvt_pk_bf16_f32 v217, -v217, -v221\n\t"
"v_mfma_f32_16x16x32_bf16 v[36:39], a[64:67], a[148:151], v[36:39]\n\t"
"ds_read_b128 a[160:163], v224 offset:19616\n\t"
"v_cvt_pk_bf16_f32 v219, -v219, -v223\n\t"
"v_mfma_f32_16x16x32_bf16 v[36:39], a[68:71], a[164:167], v[36:39]\n\t"
"ds_read_b64_tr_b16 a[202:203], v251 offset:128\n\t"
"v_cvt_pk_bf16_f32 v208, -v208, -v212\n\t"
"v_mfma_f32_16x16x32_bf16 v[20:23], a[56:59], a[148:151], v[20:23]\n\t"
"ds_write_addtid_b32 v203 offset:14496\n\t"
"v_cvt_pk_bf16_f32 v194, -v194, -v198\n\t"
"v_mfma_f32_16x16x32_bf16 v[20:23], a[60:63], a[164:167], v[20:23]\n\t"
"ds_write_addtid_b32 v200 offset:2048\n\t"
"v_cvt_pk_bf16_f32 v211, -v211, -v215\n\t"
"v_mfma_f32_16x16x32_bf16 v[4:7], a[48:51], a[148:151], v[4:7]\n\t"
"ds_read_b128 a[148:151], v224 offset:17824\n\t"
"v_mfma_f32_16x16x32_bf16 v[4:7], a[52:55], a[164:167], v[4:7]\n\t"
"s_waitcnt lgkmcnt(8)\n\t"
"ds_write_addtid_b32 v201 offset:10368\n\t"
"v_cvt_pk_bf16_f32 v218, -v218, -v222\n\t"
"v_mfma_f32_16x16x32_bf16 v[8:11], a[48:51], a[152:155], v[8:11]\n\t"
"ds_read_b128 a[164:167], v224 offset:19872\n\t"
"v_cvt_pk_bf16_f32 v210, -v210, -v214\n\t"
"v_mfma_f32_16x16x32_bf16 v[8:11], a[52:55], a[168:171], v[8:11]\n\t"
"ds_write_addtid_b32 v194 offset:4128\n\t"
"v_cvt_pk_bf16_f32 v202, -v202, -v206\n\t"
"v_mfma_f32_16x16x32_bf16 v[24:27], a[56:59], a[152:155], v[24:27]\n\t"
"ds_write_addtid_b32 v209 offset:9344\n\t"
"v_mfma_f32_16x16x32_bf16 v[24:27], a[60:63], a[168:171], v[24:27]\n\t"
"ds_write_addtid_b32 v202 offset:6176\n\t"
"v_mfma_f32_16x16x32_bf16 v[40:43], a[64:67], a[152:155], v[40:43]\n\t"
"ds_read_b128 a[152:155], v224 offset:18080\n\t"
"v_cvt_pk_bf16_f32 v193, -v193, -v197\n\t"
"v_mfma_f32_16x16x32_bf16 v[40:43], a[68:71], a[168:171], v[40:43]\n\t"
"ds_read_b128 a[168:171], v224 offset:20128\n\t"
"v_mfma_f32_16x16x32_bf16 v[44:47], a[64:67], a[156:159], v[44:47]\n\t"
"ds_write_addtid_b32 v210 offset:5152\n\t"
"v_cvt_pk_bf16_f32 v192, -v192, -v196\n\t"
"v_mfma_f32_16x16x32_bf16 v[44:47], a[68:71], a[172:175], v[44:47]\n\t"
"buffer_load_dwordx4 a[64:67], off, s[28:31], s42 offset:0 sc1\n\t"
"v_mfma_f32_16x16x32_bf16 v[28:31], a[56:59], a[156:159], v[28:31]\n\t"
"ds_write_addtid_b32 v217 offset:11392\n\t"
"v_mfma_f32_16x16x32_bf16 v[28:31], a[60:63], a[172:175], v[28:31]\n\t"
"s_xor_b32 s24, s24, 384\n\t"
"ds_write_addtid_b32 v195 offset:12448\n\t"
"ds_write_addtid_b32 v208 offset:1024\n\t"
"v_mfma_f32_16x16x32_bf16 v[12:15], a[48:51], a[156:159], v[12:15]\n\t"
"ds_write_addtid_b32 v219 offset:15520\n\t"
"v_mfma_f32_16x16x32_bf16 v[12:15], a[52:55], a[172:175], v[12:15]\n\t"
"s_waitcnt lgkmcnt(9)\n\t"
"buffer_load_dwordx4 a[68:71], off, s[28:31], s42 offset:1024 sc1\n\t"
"v_mfma_f32_16x16x32_bf16 v[96:99], a[72:75], a[144:147], v[0:3]\n\t"
"s_xor_b32 s20, s20, 384\n\t"
"ds_write_addtid_b32 v193 offset:8320\n\t"
"ds_write_addtid_b32 v211 offset:13472\n\t"
"v_mfma_f32_16x16x32_bf16 v[96:99], a[76:79], a[160:163], v[96:99]\n\t"
"ds_write_addtid_b32 v218 offset:7200\n\t"
"v_cvt_pk_bf16_f32 v216, -v216, -v220\n\t"
"v_mfma_f32_16x16x32_bf16 v[112:115], a[80:83], a[144:147], v[16:19]\n\t"
"s_xor_b32 s27, s27, 384\n\t"
"ds_write_addtid_b32 v192 offset:0\n\t"
"ds_write_addtid_b32 v216 offset:3072\n\t"
"v_mfma_f32_16x16x32_bf16 v[112:115], a[84:87], a[160:163], v[112:115]\n\t"
"buffer_load_dwordx4 a[60:63], off, s[84:87], s42 offset:3072 sc1\n\t"
"v_mfma_f32_16x16x32_bf16 v[128:131], a[88:91], a[144:147], v[32:35]\n\t"
"ds_read_b128 a[156:159], v224 offset:18336\n\t"
"v_mfma_f32_16x16x32_bf16 v[128:131], a[92:95], a[160:163], v[128:131]\n\t"
"buffer_load_dwordx4 a[56:59], off, s[84:87], s42 offset:2048 sc1\n\t"
"v_mfma_f32_16x16x32_bf16 v[132:135], a[88:91], a[148:151], v[36:39]\n\t"
"ds_read_b128 a[172:175], v224 offset:20384\n\t"
"v_mfma_f32_16x16x32_bf16 v[132:135], a[92:95], a[164:167], v[132:135]\n\t"
"buffer_load_dwordx4 a[48:51], off, s[84:87], s42 offset:0 sc1\n\t"
"v_mfma_f32_16x16x32_bf16 v[116:119], a[80:83], a[148:151], v[20:23]\n\t"
"s_xor_b32 s25, s25, 384\n\t"
"ds_read_b64_tr_b16 a[200:201], v251 offset:0\n\t"
"ds_read_b64_tr_b16 a[204:205], v250 offset:256\n\t"
"v_mfma_f32_16x16x32_bf16 v[116:119], a[84:87], a[164:167], v[116:119]\n\t"
"buffer_load_dwordx4 a[52:55], off, s[84:87], s42 offset:1024 sc1\n\t"
"v_mfma_f32_16x16x32_bf16 v[100:103], a[72:75], a[148:151], v[4:7]\n\t"
"v_mfma_f32_16x16x32_bf16 v[100:103], a[76:79], a[164:167], v[100:103]\n\t"
"s_waitcnt lgkmcnt(0)\n\t"
"s_barrier\n\t"
"v_mfma_f32_16x16x32_bf16 v[104:107], a[72:75], a[152:155], v[8:11]\n\t"
"ds_read_b128 a[160:163], v224 offset:2048\n\t"
"v_mfma_f32_16x16x32_bf16 v[104:107], a[76:79], a[168:171], v[104:107]\n\t"
"ds_read_b128 a[144:147], v224 offset:0\n\t"
"v_mfma_f32_16x16x32_bf16 v[120:123], a[80:83], a[152:155], v[24:27]\n\t"
"ds_read_b128 a[148:151], v224 offset:256\n\t"
"v_mfma_f32_16x16x32_bf16 v[120:123], a[84:87], a[168:171], v[120:123]\n\t"
"ds_read_b128 a[164:167], v224 offset:2304\n\t"
"v_mfma_f32_16x16x32_bf16 v[136:139], a[88:91], a[152:155], v[40:43]\n\t"
"v_mfma_f32_16x16x32_bf16 v[136:139], a[92:95], a[168:171], v[136:139]\n\t"
"ds_write_b128 v254, a[240:243] offset:0\n\t"
"v_mfma_f32_16x16x32_bf16 v[140:143], a[88:91], a[156:159], v[44:47]\n\t"
"v_mfma_f32_16x16x32_bf16 v[140:143], a[92:95], a[172:175], v[140:143]\n\t"
"buffer_load_dwordx4 a[88:91], off, s[28:31], s48 offset:0 sc1\n\t"
"v_mfma_f32_16x16x32_bf16 v[124:127], a[80:83], a[156:159], v[28:31]\n\t"
"v_mfma_f32_16x16x32_bf16 v[124:127], a[84:87], a[172:175], v[124:127]\n\t"
"buffer_load_dwordx4 a[92:95], off, s[28:31], s48 offset:1024 sc1\n\t"
"v_mfma_f32_16x16x32_bf16 v[108:111], a[72:75], a[156:159], v[12:15]\n\t"
"ds_read_b128 a[152:155], v224 offset:512\n\t"
"v_mfma_f32_16x16x32_bf16 v[108:111], a[76:79], a[172:175], v[108:111]\n\t"
"buffer_load_dwordx4 a[84:87], off, s[84:87], s48 offset:3072 sc1\n\t"
"v_mfma_f32_4x4x4_16b_bf16 v[200:203], a[220:221], a[178:179], 0\n\t"
"ds_read_b128 a[168:171], v224 offset:2560\n\t"
"v_mfma_f32_4x4x4_16b_bf16 v[204:207], a[220:221], a[186:187], 0\n\t"
"ds_read_b64_tr_b16 a[178:179], v248 offset:41600\n\t"
"v_mfma_f32_4x4x4_16b_bf16 v[216:219], a[220:221], a[194:195], 0\n\t"
"ds_read_b128 a[156:159], v224 offset:768\n\t"
"v_mfma_f32_4x4x4_16b_bf16 v[220:223], a[220:221], a[202:203], 0\n\t"
"v_mfma_f32_4x4x4_16b_bf16 v[192:195], a[222:223], a[176:177], v[200:203]\n\t"
"ds_read_b128 a[172:175], v224 offset:2816\n\t"
"v_mfma_f32_4x4x4_16b_bf16 v[196:199], a[222:223], a[184:185], v[204:207]\n\t"
"ds_read_b64_tr_b16 a[186:187], v249 offset:41600\n\t"
"v_mfma_f32_4x4x4_16b_bf16 v[200:203], a[222:223], a[180:181], v[200:203]\n\t"
"ds_read_b64_tr_b16 a[194:195], v250 offset:41600\n\t"
"v_mfma_f32_4x4x4_16b_bf16 v[204:207], a[222:223], a[188:189], v[204:207]\n\t"
"ds_read_b64_tr_b16 a[202:203], v251 offset:41600\n\t"
"v_mfma_f32_4x4x4_16b_bf16 v[208:211], a[222:223], a[192:193], v[216:219]\n\t"
"ds_read_b64_tr_b16 a[176:177], v248 offset:41472\n\t"
"v_mfma_f32_4x4x4_16b_bf16 v[212:215], a[222:223], a[200:201], v[220:223]\n\t"
"ds_read_b64_tr_b16 a[184:185], v249 offset:41472\n\t"
"v_mfma_f32_4x4x4_16b_bf16 v[216:219], a[222:223], a[196:197], v[216:219]\n\t"
"ds_read_b64_tr_b16 a[180:181], v249 offset:41728\n\t"
"v_mfma_f32_4x4x4_16b_bf16 v[220:223], a[222:223], a[204:205], v[220:223]\n\t"
"s_waitcnt lgkmcnt(8)\n\t"
"buffer_load_dwordx4 a[80:83], off, s[84:87], s48 offset:2048 sc1\n\t"
"v_mfma_f32_16x16x32_bf16 v[96:99], a[96:99], a[144:147], v[96:99]\n\t"
"ds_read_b64_tr_b16 a[188:189], v248 offset:41728\n\t"
"v_cvt_pk_bf16_f32 v195, -v195, -v199\n\t"
"v_mfma_f32_16x16x32_bf16 v[96:99], a[100:103], a[160:163], v[96:99]\n\t"
"ds_read_b64_tr_b16 a[192:193], v250 offset:41472\n\t"
"v_cvt_pk_bf16_f32 v193, -v193, -v197\n\t"
"v_mfma_f32_16x16x32_bf16 v[112:115], a[104:107], a[144:147], v[112:115]\n\t"
"ds_read_b64_tr_b16 a[200:201], v251 offset:41472\n\t"
"v_cvt_pk_bf16_f32 v201, v201, v205\n\t"
"v_mfma_f32_16x16x32_bf16 v[112:115], a[108:111], a[160:163], v[112:115]\n\t"
"buffer_load_dwordx4 a[72:75], off, s[84:87], s48 offset:0 sc1\n\t"
"v_mfma_f32_16x16x32_bf16 v[128:131], a[112:115], a[144:147], v[128:131]\n\t"
"ds_read_b128 a[144:147], v224 offset:1024\n\t"
"v_cvt_pk_bf16_f32 v200, v200, v204\n\t"
"v_mfma_f32_16x16x32_bf16 v[128:131], a[116:119], a[160:163], v[128:131]\n\t"
"ds_read_b64_tr_b16 a[196:197], v251 offset:41728\n\t"
"v_cvt_pk_bf16_f32 v194, -v194, -v198\n\t"
"v_mfma_f32_16x16x32_bf16 v[132:135], a[112:115], a[148:151], v[132:135]\n\t"
"s_nop 0\n\t"
"ds_read_b128 a[160:163], v224 offset:3072\n\t"
"v_cvt_pk_bf16_f32 v217, v217, v221\n\t"
"v_mfma_f32_16x16x32_bf16 v[132:135], a[116:119], a[164:167], v[132:135]\n\t"
"buffer_load_dwordx4 a[76:79], off, s[84:87], s48 offset:1024 sc1\n\t"
"v_mfma_f32_16x16x32_bf16 v[116:119], a[104:107], a[148:151], v[116:119]\n\t"
"s_nop 0\n\t"
"ds_write_addtid_b32 v193 offset:20672\n\t"
"v_cvt_pk_bf16_f32 v219, v219, v223\n\t"
"v_mfma_f32_16x16x32_bf16 v[116:119], a[108:111], a[164:167], v[116:119]\n\t"
"s_nop 0\n\t"
"ds_write_addtid_b32 v194 offset:17568\n\t"
"v_cvt_pk_bf16_f32 v210, -v210, -v214\n\t"
"v_mfma_f32_16x16x32_bf16 v[100:103], a[96:99], a[148:151], v[100:103]\n\t"
"s_nop 0\n\t"
"ds_read_b128 a[148:151], v224 offset:1280\n\t"
"v_cvt_pk_bf16_f32 v211, -v211, -v215\n\t"
"v_mfma_f32_16x16x32_bf16 v[100:103], a[100:103], a[164:167], v[100:103]\n\t"
"s_nop 0\n\t"
"ds_write_addtid_b32 v201 offset:28992\n\t"
"v_cvt_pk_bf16_f32 v218, v218, v222\n\t"
"v_mfma_f32_16x16x32_bf16 v[104:107], a[96:99], a[152:155], v[104:107]\n\t"
"s_nop 0\n\t"
"ds_read_b128 a[164:167], v224 offset:3328\n\t"
"v_cvt_pk_bf16_f32 v203, v203, v207\n\t"
"v_mfma_f32_16x16x32_bf16 v[104:107], a[100:103], a[168:171], v[104:107]\n\t"
"s_nop 0\n\t"
"ds_write_addtid_b32 v218 offset:27936\n\t"
"v_mfma_f32_16x16x32_bf16 v[120:123], a[104:107], a[152:155], v[120:123]\n\t"
"s_nop 0\n\t"
"ds_write_addtid_b32 v217 offset:31040\n\t"
"v_cvt_pk_bf16_f32 v208, -v208, -v212\n\t"
"v_mfma_f32_16x16x32_bf16 v[120:123], a[108:111], a[168:171], v[120:123]\n\t"
"s_nop 0\n\t"
"ds_write_addtid_b32 v208 offset:18592\n\t"
"v_mfma_f32_16x16x32_bf16 v[136:139], a[112:115], a[152:155], v[136:139]\n\t"
"s_nop 0\n\t"
"ds_read_b128 a[152:155], v224 offset:1536\n\t"
"v_mfma_f32_16x16x32_bf16 v[136:139], a[116:119], a[168:171], v[136:139]\n\t"
"s_waitcnt lgkmcnt(9)\n\t"
"ds_read_b128 a[168:171], v224 offset:3584\n\t"
"v_cvt_pk_bf16_f32 v216, v216, v220\n\t"
"v_mfma_f32_16x16x32_bf16 v[140:143], a[112:115], a[156:159], v[140:143]\n\t"
"s_nop 0\n\t"
"ds_write_addtid_b32 v210 offset:19616\n\t"
"v_cvt_pk_bf16_f32 v209, -v209, -v213\n\t"
"v_mfma_f32_16x16x32_bf16 v[140:143], a[116:119], a[172:175], v[140:143]\n\t"
"buffer_load_dwordx4 a[112:115], off, s[28:31], s75 offset:0 sc1\n\t"
"v_mfma_f32_16x16x32_bf16 v[124:127], a[104:107], a[156:159], v[124:127]\n\t"
"s_nop 0\n\t"
"ds_write_addtid_b32 v203 offset:30016\n\t"
"v_cvt_pk_bf16_f32 v202, v202, v206\n\t"
"v_mfma_f32_16x16x32_bf16 v[124:127], a[108:111], a[172:175], v[124:127]\n\t"
"s_nop 0\n\t"
"ds_write_addtid_b32 v202 offset:25888\n\t"
"ds_write_addtid_b32 v216 offset:26912\n\t"
"v_mfma_f32_16x16x32_bf16 v[108:111], a[96:99], a[156:159], v[108:111]\n\t"
"s_nop 0\n\t"
"ds_write_addtid_b32 v219 offset:32064\n\t"
"v_cvt_pk_bf16_f32 v192, -v192, -v196\n\t"
"v_mfma_f32_16x16x32_bf16 v[108:111], a[100:103], a[172:175], v[108:111]\n\t"
"buffer_load_dwordx4 a[116:119], off, s[28:31], s75 offset:1024 sc1\n\t"
"v_mfma_f32_16x16x32_bf16 v[96:99], a[120:123], a[144:147], v[96:99]\n\t"
"s_nop 0\n\t"
"ds_write_addtid_b32 v209 offset:22720\n\t"
"ds_write_addtid_b32 v211 offset:23744\n\t"
"v_mfma_f32_16x16x32_bf16 v[96:99], a[124:127], a[160:163], v[96:99]\n\t"
"s_nop 0\n\t"
"ds_write_addtid_b32 v195 offset:21696\n\t"
"v_mfma_f32_16x16x32_bf16 v[112:115], a[128:131], a[144:147], v[112:115]\n\t"
"s_nop 0\n\t"
"ds_write_addtid_b32 v192 offset:16544\n\t"
"ds_write_addtid_b32 v200 offset:24864\n\t"
"v_mfma_f32_16x16x32_bf16 v[112:115], a[132:135], a[160:163], v[112:115]\n\t"
"buffer_load_dwordx4 a[108:111], off, s[84:87], s75 offset:3072 sc1\n\t"
"v_mfma_f32_16x16x32_bf16 v[128:131], a[136:139], a[144:147], v[128:131]\n\t"
"s_nop 0\n\t"
"ds_read_b128 a[172:175], v224 offset:3840\n\t"
"v_mfma_f32_16x16x32_bf16 v[128:131], a[140:143], a[160:163], v[128:131]\n\t"
"s_waitcnt lgkmcnt(10)\n\t"
"buffer_load_dwordx4 a[104:107], off, s[84:87], s75 offset:2048 sc1\n\t"
"v_mfma_f32_16x16x32_bf16 v[132:135], a[136:139], a[148:151], v[132:135]\n\t"
"s_nop 0\n\t"
"ds_read_b128 a[156:159], v224 offset:1792\n\t"
"v_mfma_f32_16x16x32_bf16 v[132:135], a[140:143], a[164:167], v[132:135]\n\t"
"buffer_load_dwordx4 a[96:99], off, s[84:87], s75 offset:0 sc1\n\t"
"v_mfma_f32_16x16x32_bf16 v[116:119], a[128:131], a[148:151], v[116:119]\n\t"
"s_nop 0\n\t"
"ds_read_b64_tr_b16 a[204:205], v250 offset:41728\n\t"
"v_mfma_f32_16x16x32_bf16 v[116:119], a[132:135], a[164:167], v[116:119]\n\t"
"buffer_load_dwordx4 a[100:103], off, s[84:87], s75 offset:1024 sc1\n\t"
"v_mfma_f32_16x16x32_bf16 v[100:103], a[120:123], a[148:151], v[100:103]\n\t"
"s_nop 0\n\t"
"v_mfma_f32_16x16x32_bf16 v[100:103], a[124:127], a[164:167], v[100:103]\n\t"
"s_waitcnt lgkmcnt(0)\n\t"
"s_barrier\n\t"
"v_mfma_f32_16x16x32_bf16 v[104:107], a[120:123], a[152:155], v[104:107]\n\t"
"s_nop 0\n\t"
"ds_read_b128 a[144:147], v224 offset:16544\n\t"
"v_add_u32 v248, 101632, v226\n\t"
"v_mfma_f32_16x16x32_bf16 v[104:107], a[124:127], a[168:171], v[104:107]\n\t"
"s_nop 0\n\t"
"ds_read_b128 a[148:151], v224 offset:16800\n\t"
"v_mfma_f32_16x16x32_bf16 v[120:123], a[128:131], a[152:155], v[120:123]\n\t"
"s_nop 0\n\t"
"ds_read_b128 a[160:163], v224 offset:18592\n\t"
"v_xor_b32 v249, 32, v248\n\t"
"v_mfma_f32_16x16x32_bf16 v[120:123], a[132:135], a[168:171], v[120:123]\n\t"
"s_nop 0\n\t"
"ds_read_b128 a[164:167], v224 offset:18848\n\t"
"v_mfma_f32_16x16x32_bf16 v[136:139], a[136:139], a[152:155], v[136:139]\n\t"
"s_nop 0\n\t"
"v_xor_b32 v250, 64, v248\n\t"
"v_mfma_f32_16x16x32_bf16 v[136:139], a[140:143], a[168:171], v[136:139]\n\t"
"s_nop 0\n\t"
"v_mfma_f32_16x16x32_bf16 v[140:143], a[136:139], a[156:159], v[140:143]\n\t"
"s_nop 0\n\t"
"ds_read_b128 a[152:155], v224 offset:17056\n\t"
"v_mfma_f32_16x16x32_bf16 v[140:143], a[140:143], a[172:175], v[140:143]\n\t"
"buffer_load_dwordx4 a[136:139], off, s[28:31], s78 offset:0 sc1\n\t"
"v_mfma_f32_16x16x32_bf16 v[124:127], a[128:131], a[156:159], v[124:127]\n\t"
"s_nop 0\n\t"
"ds_read_b128 a[168:171], v224 offset:19104\n\t"
"v_xor_b32 v251, 96, v248\n\t"
"v_mfma_f32_16x16x32_bf16 v[124:127], a[132:135], a[172:175], v[124:127]\n\t"
"Lpad8_%=:\n\t"
"s_cbranch_execz Lpad12_%=\n\t"
"v_mfma_f32_16x16x32_bf16 v[108:111], a[120:123], a[156:159], v[108:111]\n\t"
"ds_read_b128 a[156:159], v224 offset:17312\n\t"
"v_mfma_f32_16x16x32_bf16 v[108:111], a[124:127], a[172:175], v[108:111]\n\t"
"buffer_load_dwordx4 a[140:143], off, s[28:31], s78 offset:1024 sc1\n\t"
"v_mfma_f32_4x4x4_16b_bf16 v[200:203], a[220:221], a[178:179], 0\n\t"
"ds_read_b64_tr_b16 a[178:179], v248 offset:20864\n\t"
"v_mfma_f32_4x4x4_16b_bf16 v[204:207], a[220:221], a[186:187], 0\n\t"
"v_mfma_f32_4x4x4_16b_bf16 v[216:219], a[220:221], a[194:195], 0\n\t"
"ds_read_b64_tr_b16 a[186:187], v249 offset:20864\n\t"
"v_mfma_f32_4x4x4_16b_bf16 v[220:223], a[220:221], a[202:203], 0\n\t"
".p2align 3\n\t"
"ds_read_b128 a[172:175], v224 offset:19360\n\t"
"v_pk_fma_f32 v[0:1], v[96:97], s[90:91], v[0:1] neg_lo:[1,0,0] neg_hi:[1,0,0]\n\t"
"v_pk_fma_f32 v[2:3], v[98:99], s[90:91], v[2:3] neg_lo:[1,0,0] neg_hi:[1,0,0]\n\t"
"v_pk_fma_f32 v[16:17], v[112:113], s[90:91], v[16:17] neg_lo:[1,0,0] neg_hi:[1,0,0]\n\t"
"v_pk_fma_f32 v[18:19], v[114:115], s[90:91], v[18:19] neg_lo:[1,0,0] neg_hi:[1,0,0]\n\t"
"v_pk_fma_f32 v[32:33], v[128:129], s[90:91], v[32:33] neg_lo:[1,0,0] neg_hi:[1,0,0]\n\t"
"v_pk_fma_f32 v[34:35], v[130:131], s[90:91], v[34:35] neg_lo:[1,0,0] neg_hi:[1,0,0]\n\t"
"v_pk_fma_f32 v[36:37], v[132:133], s[90:91], v[36:37] neg_lo:[1,0,0] neg_hi:[1,0,0]\n\t"
"v_pk_fma_f32 v[38:39], v[134:135], s[90:91], v[38:39] neg_lo:[1,0,0] neg_hi:[1,0,0]\n\t"
"v_pk_fma_f32 v[20:21], v[116:117], s[90:91], v[20:21] neg_lo:[1,0,0] neg_hi:[1,0,0]\n\t"
"v_pk_fma_f32 v[22:23], v[118:119], s[90:91], v[22:23] neg_lo:[1,0,0] neg_hi:[1,0,0]\n\t"
"v_pk_fma_f32 v[4:5], v[100:101], s[90:91], v[4:5] neg_lo:[1,0,0] neg_hi:[1,0,0]\n\t"
"v_pk_fma_f32 v[6:7], v[102:103], s[90:91], v[6:7] neg_lo:[1,0,0] neg_hi:[1,0,0]\n\t"
"v_pk_fma_f32 v[8:9], v[104:105], s[90:91], v[8:9] neg_lo:[1,0,0] neg_hi:[1,0,0]\n\t"
"v_pk_fma_f32 v[10:11], v[106:107], s[90:91], v[10:11] neg_lo:[1,0,0] neg_hi:[1,0,0]\n\t"
"v_pk_fma_f32 v[24:25], v[120:121], s[90:91], v[24:25] neg_lo:[1,0,0] neg_hi:[1,0,0]\n\t"
"v_pk_fma_f32 v[26:27], v[122:123], s[90:91], v[26:27] neg_lo:[1,0,0] neg_hi:[1,0,0]\n\t"
"v_pk_fma_f32 v[40:41], v[136:137], s[90:91], v[40:41] neg_lo:[1,0,0] neg_hi:[1,0,0]\n\t"
"v_pk_fma_f32 v[42:43], v[138:139], s[90:91], v[42:43] neg_lo:[1,0,0] neg_hi:[1,0,0]\n\t"
"v_pk_fma_f32 v[44:45], v[140:141], s[90:91], v[44:45] neg_lo:[1,0,0] neg_hi:[1,0,0]\n\t"
"v_pk_fma_f32 v[46:47], v[142:143], s[90:91], v[46:47] neg_lo:[1,0,0] neg_hi:[1,0,0]\n\t"
"v_pk_fma_f32 v[28:29], v[124:125], s[90:91], v[28:29] neg_lo:[1,0,0] neg_hi:[1,0,0]\n\t"
"v_pk_fma_f32 v[30:31], v[126:127], s[90:91], v[30:31] neg_lo:[1,0,0] neg_hi:[1,0,0]\n\t"
"v_pk_fma_f32 v[12:13], v[108:109], s[90:91], v[12:13] neg_lo:[1,0,0] neg_hi:[1,0,0]\n\t"
"v_pk_fma_f32 v[14:15], v[110:111], s[90:91], v[14:15] neg_lo:[1,0,0] neg_hi:[1,0,0]\n\t"
"v_mfma_f32_4x4x4_16b_bf16 v[192:195], a[222:223], a[176:177], v[200:203]\n\t"
"ds_read_b64_tr_b16 a[194:195], v250 offset:20864\n\t"
"v_mfma_f32_4x4x4_16b_bf16 v[196:199], a[222:223], a[184:185], v[204:207]\n\t"
"ds_read_b64_tr_b16 a[202:203], v251 offset:20864\n\t"
"v_mfma_f32_4x4x4_16b_bf16 v[200:203], a[222:223], a[180:181], v[200:203]\n\t"
"ds_read_b64_tr_b16 a[176:177], v248 offset:20736\n\t"
"v_mfma_f32_4x4x4_16b_bf16 v[204:207], a[222:223], a[188:189], v[204:207]\n\t"
"v_mfma_f32_4x4x4_16b_bf16 v[208:211], a[222:223], a[192:193], v[216:219]\n\t"
"ds_read_b64_tr_b16 a[184:185], v249 offset:20736\n\t"
"v_mfma_f32_4x4x4_16b_bf16 v[212:215], a[222:223], a[200:201], v[220:223]\n\t"
"ds_read_b64_tr_b16 a[180:181], v249 offset:20992\n\t"
"v_mfma_f32_4x4x4_16b_bf16 v[216:219], a[222:223], a[196:197], v[216:219]\n\t"
"ds_read_b64_tr_b16 a[188:189], v248 offset:20992\n\t"
"v_mfma_f32_4x4x4_16b_bf16 v[220:223], a[222:223], a[204:205], v[220:223]\n\t"
"s_waitcnt vmcnt(14) lgkmcnt(8)\n\t"
"buffer_load_dwordx4 a[132:135], off, s[84:87], s78 offset:3072 sc1\n\t"
"v_mfma_f32_16x16x32_bf16 v[48:51], a[48:51], a[144:147], v[48:51]\n\t"
"ds_read_b64_tr_b16 a[192:193], v250 offset:20736\n\t"
"v_cvt_pk_bf16_f32 v195, -v195, -v199\n\t"
"v_mfma_f32_16x16x32_bf16 v[48:51], a[52:55], a[160:163], v[48:51]\n\t"
"ds_read_b64_tr_b16 a[200:201], v251 offset:20736\n\t"
"v_cvt_pk_bf16_f32 v193, -v193, -v197\n\t"
"v_mfma_f32_16x16x32_bf16 v[64:67], a[56:59], a[144:147], v[64:67]\n\t"
"ds_write_addtid_b32 v193 offset:4128\n\t"
"v_cvt_pk_bf16_f32 v202, v202, v206\n\t"
"v_mfma_f32_16x16x32_bf16 v[64:67], a[60:63], a[160:163], v[64:67]\n\t"
"buffer_load_dwordx4 a[128:131], off, s[84:87], s78 offset:2048 sc1\n\t"
"v_mfma_f32_16x16x32_bf16 v[80:83], a[64:67], a[144:147], v[80:83]\n\t"
"ds_read_b128 a[144:147], v224 offset:17568\n\t"
"v_mfma_f32_16x16x32_bf16 v[80:83], a[68:71], a[160:163], v[80:83]\n\t"
"ds_write_addtid_b32 v202 offset:9344\n\t"
"v_cvt_pk_bf16_f32 v211, -v211, -v215\n\t"
"v_mfma_f32_16x16x32_bf16 v[84:87], a[64:67], a[148:151], v[84:87]\n\t"
"ds_read_b128 a[160:163], v224 offset:19616\n\t"
"v_cvt_pk_bf16_f32 v203, v203, v207\n\t"
"v_mfma_f32_16x16x32_bf16 v[84:87], a[68:71], a[164:167], v[84:87]\n\t"
"buffer_load_dwordx4 a[120:123], off, s[84:87], s78 offset:0 sc1\n\t"
"v_mfma_f32_16x16x32_bf16 v[68:71], a[56:59], a[148:151], v[68:71]\n\t"
"ds_write_addtid_b32 v195 offset:5152\n\t"
"v_mfma_f32_16x16x32_bf16 v[68:71], a[60:63], a[164:167], v[68:71]\n\t"
"ds_write_addtid_b32 v203 offset:13472\n\t"
"v_cvt_pk_bf16_f32 v192, -v192, -v196\n\t"
"v_mfma_f32_16x16x32_bf16 v[52:55], a[48:51], a[148:151], v[52:55]\n\t"
"ds_read_b128 a[148:151], v224 offset:17824\n\t"
"v_cvt_pk_bf16_f32 v201, v201, v205\n\t"
"v_mfma_f32_16x16x32_bf16 v[52:55], a[52:55], a[164:167], v[52:55]\n\t"
"buffer_load_dwordx4 a[124:127], off, s[84:87], s78 offset:1024 sc1\n\t"
"v_mfma_f32_16x16x32_bf16 v[56:59], a[48:51], a[152:155], v[56:59]\n\t"
"ds_read_b128 a[164:167], v224 offset:19872\n\t"
"v_cvt_pk_bf16_f32 v194, -v194, -v198\n\t"
"v_mfma_f32_16x16x32_bf16 v[56:59], a[52:55], a[168:171], v[56:59]\n\t"
"ds_write_addtid_b32 v194 offset:1024\n\t"
"v_mfma_f32_16x16x32_bf16 v[72:75], a[56:59], a[152:155], v[72:75]\n\t"
"ds_write_addtid_b32 v201 offset:12448\n\t"
"v_cvt_pk_bf16_f32 v218, v218, v222\n\t"
"v_mfma_f32_16x16x32_bf16 v[72:75], a[60:63], a[168:171], v[72:75]\n\t"
"ds_write_addtid_b32 v211 offset:7200\n\t"
"v_mfma_f32_16x16x32_bf16 v[88:91], a[64:67], a[152:155], v[88:91]\n\t"
"ds_read_b128 a[152:155], v224 offset:18080\n\t"
"v_cvt_pk_bf16_f32 v200, v200, v204\n\t"
"v_mfma_f32_16x16x32_bf16 v[88:91], a[68:71], a[168:171], v[88:91]\n\t"
"s_waitcnt lgkmcnt(8)\n\t"
"ds_read_b128 a[168:171], v224 offset:20128\n\t"
"v_cvt_pk_bf16_f32 v208, -v208, -v212\n\t"
"v_mfma_f32_16x16x32_bf16 v[92:95], a[64:67], a[156:159], v[92:95]\n\t"
"ds_write_addtid_b32 v200 offset:8320\n\t"
"v_cvt_pk_bf16_f32 v209, -v209, -v213\n\t"
"v_mfma_f32_16x16x32_bf16 v[92:95], a[68:71], a[172:175], v[92:95]\n\t"
"ds_write_addtid_b32 v209 offset:6176\n\t"
"v_cvt_pk_bf16_f32 v217, v217, v221\n\t"
"v_mfma_f32_16x16x32_bf16 v[76:79], a[56:59], a[156:159], v[76:79]\n\t"
"ds_write_addtid_b32 v192 offset:0\n\t"
"v_cvt_pk_bf16_f32 v210, -v210, -v214\n\t"
"v_mfma_f32_16x16x32_bf16 v[76:79], a[60:63], a[172:175], v[76:79]\n\t"
"ds_write_addtid_b32 v208 offset:2048\n\t"
"v_cvt_pk_bf16_f32 v216, v216, v220\n\t"
"v_mfma_f32_16x16x32_bf16 v[60:63], a[48:51], a[156:159], v[60:63]\n\t"
"ds_write_addtid_b32 v210 offset:3072\n\t"
"v_cvt_pk_bf16_f32 v219, v219, v223\n\t"
"v_mfma_f32_16x16x32_bf16 v[60:63], a[52:55], a[172:175], v[60:63]\n\t"
"s_waitcnt vmcnt(12) lgkmcnt(9)\n\t"
"ds_write_addtid_b32 v219 offset:15520\n\t"
"v_mfma_f32_16x16x32_bf16 v[144:147], a[72:75], a[144:147], v[144:147]\n\t"
"ds_write_addtid_b32 v218 offset:11392\n\t"
"v_mfma_f32_16x16x32_bf16 v[144:147], a[76:79], a[160:163], v[144:147]\n\t"
"ds_write_addtid_b32 v217 offset:14496\n\t"
"v_mfma_f32_16x16x32_bf16 v[160:163], a[80:83], a[144:147], v[160:163]\n\t"
"ds_write_addtid_b32 v216 offset:10368\n\t"
"v_mfma_f32_16x16x32_bf16 v[160:163], a[84:87], a[160:163], v[160:163]\n\t"
"ds_read_b128 a[156:159], v224 offset:18336\n\t"
"v_mfma_f32_16x16x32_bf16 v[176:179], a[88:91], a[144:147], v[176:179]\n\t"
"v_mfma_f32_16x16x32_bf16 v[176:179], a[92:95], a[160:163], v[176:179]\n\t"
"ds_read_b128 a[172:175], v224 offset:20384\n\t"
"v_mfma_f32_16x16x32_bf16 v[180:183], a[88:91], a[148:151], v[180:183]\n\t"
"v_mfma_f32_16x16x32_bf16 v[180:183], a[92:95], a[164:167], v[180:183]\n\t"
"ds_read_b64_tr_b16 a[196:197], v251 offset:20992\n\t"
"v_mfma_f32_16x16x32_bf16 v[164:167], a[80:83], a[148:151], v[164:167]\n\t"
"ds_read_b64_tr_b16 a[204:205], v250 offset:20992\n\t"
"v_mfma_f32_16x16x32_bf16 v[164:167], a[84:87], a[164:167], v[164:167]\n\t"
"v_mfma_f32_16x16x32_bf16 v[148:151], a[72:75], a[148:151], v[148:151]\n\t"
"s_waitcnt lgkmcnt(2)\n\t"
"v_mfma_f32_16x16x32_bf16 v[148:151], a[76:79], a[164:167], v[148:151]\n\t"
"s_barrier\n\t"
"v_mfma_f32_16x16x32_bf16 v[152:155], a[72:75], a[152:155], v[152:155]\n\t"
"s_add_u32 m0, s26, 35584\n\t"
"ds_read_b128 a[144:147], v224 offset:0\n\t"
"ds_read_b128 a[160:163], v224 offset:2048\n\t"
"v_mfma_f32_16x16x32_bf16 v[152:155], a[76:79], a[168:171], v[152:155]\n\t"
"buffer_load_dwordx4 v236, s[12:15], s22 offen offset:0 nt lds\n\t"
"v_mfma_f32_16x16x32_bf16 v[168:171], a[80:83], a[152:155], v[168:171]\n\t"
"ds_read_b128 a[148:151], v224 offset:256\n\t"
"v_mfma_f32_16x16x32_bf16 v[168:171], a[84:87], a[168:171], v[168:171]\n\t"
"ds_read_b128 a[164:167], v224 offset:2304\n\t"
"v_mfma_f32_16x16x32_bf16 v[184:187], a[88:91], a[152:155], v[184:187]\n\t"
"v_mfma_f32_16x16x32_bf16 v[184:187], a[92:95], a[168:171], v[184:187]\n\t"
"buffer_load_dwordx4 v237, s[12:15], s22 offen offset:1024 nt lds\n\t"
"v_mfma_f32_16x16x32_bf16 v[188:191], a[88:91], a[156:159], v[188:191]\n\t"
"ds_read_b128 a[168:171], v224 offset:2560\n\t"
"v_mfma_f32_16x16x32_bf16 v[188:191], a[92:95], a[172:175], v[188:191]\n\t"
"ds_read_b128 a[152:155], v224 offset:512\n\t"
"v_mfma_f32_16x16x32_bf16 v[172:175], a[80:83], a[156:159], v[172:175]\n\t"
"v_mfma_f32_16x16x32_bf16 v[172:175], a[84:87], a[172:175], v[172:175]\n\t"
"buffer_load_dwordx4 v238, s[12:15], s22 offen offset:2048 nt lds\n\t"
"v_mfma_f32_16x16x32_bf16 v[156:159], a[72:75], a[156:159], v[156:159]\n\t"
"ds_read_b128 a[156:159], v224 offset:768\n\t"
"v_mfma_f32_16x16x32_bf16 v[156:159], a[76:79], a[172:175], v[156:159]\n\t"
"s_waitcnt lgkmcnt(5)\n\t"
"ds_read_b128 a[172:175], v224 offset:2816\n\t"
"v_mfma_f32_4x4x4_16b_bf16 v[200:203], a[252:253], a[178:179], 0\n\t"
"v_mfma_f32_4x4x4_16b_bf16 v[204:207], a[252:253], a[186:187], 0\n\t"
"buffer_load_dwordx4 v239, s[12:15], s22 offen offset:3072 nt lds\n\t"
"v_mfma_f32_4x4x4_16b_bf16 v[216:219], a[252:253], a[194:195], 0\n\t"
"ds_read_b64_tr_b16 a[178:179], v248 offset:128\n\t"
"v_mfma_f32_4x4x4_16b_bf16 v[220:223], a[252:253], a[202:203], 0\n\t"
"ds_read_b64_tr_b16 a[186:187], v249 offset:128\n\t"
"v_mfma_f32_4x4x4_16b_bf16 v[192:195], a[254:255], a[176:177], v[200:203]\n\t"
"ds_read_b64_tr_b16 a[194:195], v250 offset:128\n\t"
"v_mfma_f32_4x4x4_16b_bf16 v[196:199], a[254:255], a[184:185], v[204:207]\n\t"
"s_mov_b64 exec, s[36:37]\n\t"
"buffer_load_dwordx4 v240, s[12:15], s22 offen offset:3584 nt lds\n\t"
"s_mov_b64 exec, -1\n\t"
"v_mfma_f32_4x4x4_16b_bf16 v[200:203], a[254:255], a[180:181], v[200:203]\n\t"
"ds_read_b64_tr_b16 a[202:203], v251 offset:128\n\t"
"v_mfma_f32_4x4x4_16b_bf16 v[204:207], a[254:255], a[188:189], v[204:207]\n\t"
"ds_read_b64_tr_b16 a[176:177], v248 offset:0\n\t"
"v_mfma_f32_4x4x4_16b_bf16 v[208:211], a[254:255], a[192:193], v[216:219]\n\t"
"ds_read_b64_tr_b16 a[184:185], v249 offset:0\n\t"
"v_mfma_f32_4x4x4_16b_bf16 v[212:215], a[254:255], a[200:201], v[220:223]\n\t"
"s_lshl_b32 m0, s73, 8\n\t"
"v_mfma_f32_4x4x4_16b_bf16 v[216:219], a[254:255], a[196:197], v[216:219]\n\t"
"ds_read_b64_tr_b16 a[180:181], v249 offset:256\n\t"
"v_mfma_f32_4x4x4_16b_bf16 v[220:223], a[254:255], a[204:205], v[220:223]\n\t"
"s_waitcnt vmcnt(11) lgkmcnt(7)\n\t"
"ds_read_b64_tr_b16 a[188:189], v248 offset:256\n\t"
"v_mfma_f32_16x16x32_bf16 v[48:51], a[96:99], a[144:147], v[48:51]\n\t"
"ds_read_b64_tr_b16 a[192:193], v250 offset:0\n\t"
"v_mfma_f32_16x16x32_bf16 v[48:51], a[100:103], a[160:163], v[48:51]\n\t"
"v_cvt_pk_bf16_f32 v193, -v193, -v197\n\t"
"v_cvt_pk_bf16_f32 v195, -v195, -v199\n\t"
"v_mfma_f32_16x16x32_bf16 v[64:67], a[104:107], a[144:147], v[64:67]\n\t"
"ds_read_b64_tr_b16 a[200:201], v251 offset:0\n\t"
"v_mfma_f32_16x16x32_bf16 v[64:67], a[108:111], a[160:163], v[64:67]\n\t"
"ds_write_addtid_b32 v193 offset:20672\n\t"
"v_cvt_pk_bf16_f32 v194, -v194, -v198\n\t"
"v_mfma_f32_16x16x32_bf16 v[80:83], a[112:115], a[144:147], v[80:83]\n\t"
"ds_read_b128 a[144:147], v224 offset:1024\n\t"
"v_cvt_pk_bf16_f32 v201, v201, v205\n\t"
"v_mfma_f32_16x16x32_bf16 v[80:83], a[116:119], a[160:163], v[80:83]\n\t"
"v_cvt_pk_bf16_f32 v209, -v209, -v213\n\t"
"v_mfma_f32_16x16x32_bf16 v[84:87], a[112:115], a[148:151], v[84:87]\n\t"
"ds_read_b128 a[160:163], v224 offset:3072\n\t"
"v_mfma_f32_16x16x32_bf16 v[84:87], a[116:119], a[164:167], v[84:87]\n\t"
"ds_write_addtid_b32 v201 offset:28992\n\t"
"v_cvt_pk_bf16_f32 v217, v217, v221\n\t"
"v_mfma_f32_16x16x32_bf16 v[68:71], a[104:107], a[148:151], v[68:71]\n\t"
"ds_write_addtid_b32 v195 offset:21696\n\t"
"v_cvt_pk_bf16_f32 v203, v203, v207\n\t"
"v_mfma_f32_16x16x32_bf16 v[68:71], a[108:111], a[164:167], v[68:71]\n\t"
"v_cvt_pk_bf16_f32 v200, v200, v204\n\t"
"v_cvt_pk_bf16_f32 v216, v216, v220\n\t"
"v_mfma_f32_16x16x32_bf16 v[52:55], a[96:99], a[148:151], v[52:55]\n\t"
"ds_read_b128 a[148:151], v224 offset:1280\n\t"
"v_mfma_f32_16x16x32_bf16 v[52:55], a[100:103], a[164:167], v[52:55]\n\t"
"ds_read_b128 a[164:167], v224 offset:3328\n\t"
"v_mfma_f32_16x16x32_bf16 v[56:59], a[96:99], a[152:155], v[56:59]\n\t"
"ds_write_addtid_b32 v194 offset:17568\n\t"
"v_cvt_pk_bf16_f32 v211, -v211, -v215\n\t"
"v_mfma_f32_16x16x32_bf16 v[56:59], a[100:103], a[168:171], v[56:59]\n\t"
"ds_write_addtid_b32 v211 offset:23744\n\t"
"v_cvt_pk_bf16_f32 v219, v219, v223\n\t"
"v_mfma_f32_16x16x32_bf16 v[72:75], a[104:107], a[152:155], v[72:75]\n\t"
"ds_write_addtid_b32 v209 offset:22720\n\t"
"v_cvt_pk_bf16_f32 v208, -v208, -v212\n\t"
"v_mfma_f32_16x16x32_bf16 v[72:75], a[108:111], a[168:171], v[72:75]\n\t"
"ds_write_addtid_b32 v203 offset:30016\n\t"
"v_cvt_pk_bf16_f32 v218, v218, v222\n\t"
"v_mfma_f32_16x16x32_bf16 v[88:91], a[112:115], a[152:155], v[88:91]\n\t"
"ds_read_b128 a[152:155], v224 offset:1536\n\t"
"v_cvt_pk_bf16_f32 v210, -v210, -v214\n\t"
"v_mfma_f32_16x16x32_bf16 v[88:91], a[116:119], a[168:171], v[88:91]\n\t"
"ds_read_b128 a[168:171], v224 offset:3584\n\t"
"v_cvt_pk_bf16_f32 v192, -v192, -v196\n\t"
"v_mfma_f32_16x16x32_bf16 v[92:95], a[112:115], a[156:159], v[92:95]\n\t"
"ds_write_addtid_b32 v217 offset:31040\n\t"
"v_mfma_f32_16x16x32_bf16 v[92:95], a[116:119], a[172:175], v[92:95]\n\t"
"ds_write_addtid_b32 v216 offset:26912\n\t"
"v_mfma_f32_16x16x32_bf16 v[76:79], a[104:107], a[156:159], v[76:79]\n\t"
"ds_write_addtid_b32 v200 offset:24864\n\t"
"v_cvt_pk_bf16_f32 v202, v202, v206\n\t"
"v_mfma_f32_16x16x32_bf16 v[76:79], a[108:111], a[172:175], v[76:79]\n\t"
"ds_write_addtid_b32 v210 offset:19616\n\t"
"v_mfma_f32_16x16x32_bf16 v[60:63], a[96:99], a[156:159], v[60:63]\n\t"
"ds_write_addtid_b32 v208 offset:18592\n\t"
"v_mfma_f32_16x16x32_bf16 v[60:63], a[100:103], a[172:175], v[60:63]\n\t"
"s_waitcnt vmcnt(5) lgkmcnt(9)\n\t"
"ds_write_addtid_b32 v218 offset:27936\n\t"
"v_mfma_f32_16x16x32_bf16 v[144:147], a[120:123], a[144:147], v[144:147]\n\t"
"ds_write_addtid_b32 v219 offset:32064\n\t"
"v_mfma_f32_16x16x32_bf16 v[144:147], a[124:127], a[160:163], v[144:147]\n\t"
"ds_write_addtid_b32 v202 offset:25888\n\t"
"v_mfma_f32_16x16x32_bf16 v[160:163], a[128:131], a[144:147], v[160:163]\n\t"
"ds_write_addtid_b32 v192 offset:16544\n\t"
"v_mfma_f32_16x16x32_bf16 v[160:163], a[132:135], a[160:163], v[160:163]\n\t"
"ds_read_b128 a[156:159], v224 offset:1792\n\t"
"v_mfma_f32_16x16x32_bf16 v[176:179], a[136:139], a[144:147], v[176:179]\n\t"
"v_mfma_f32_16x16x32_bf16 v[176:179], a[140:143], a[160:163], v[176:179]\n\t"
"ds_read_b128 a[172:175], v224 offset:3840\n\t"
"v_mfma_f32_16x16x32_bf16 v[180:183], a[136:139], a[148:151], v[180:183]\n\t"
"v_mfma_f32_16x16x32_bf16 v[180:183], a[140:143], a[164:167], v[180:183]\n\t"
"ds_read_b64_tr_b16 a[196:197], v251 offset:256\n\t"
"v_mfma_f32_16x16x32_bf16 v[164:167], a[128:131], a[148:151], v[164:167]\n\t"
"ds_read_b64_tr_b16 a[204:205], v250 offset:256\n\t"
"v_mfma_f32_16x16x32_bf16 v[164:167], a[132:135], a[164:167], v[164:167]\n\t"
"v_mfma_f32_16x16x32_bf16 v[148:151], a[120:123], a[148:151], v[148:151]\n\t"
"s_waitcnt lgkmcnt(2)\n\t"
"v_mfma_f32_16x16x32_bf16 v[148:151], a[124:127], a[164:167], v[148:151]\n\t"
"s_barrier\n\t"
"v_mfma_f32_16x16x32_bf16 v[152:155], a[120:123], a[152:155], v[152:155]\n\t"
"ds_read_b128 a[160:163], v224 offset:18592\n\t"
"v_mfma_f32_16x16x32_bf16 v[152:155], a[124:127], a[168:171], v[152:155]\n\t"
"ds_read_b128 a[144:147], v224 offset:16544\n\t"
"v_mfma_f32_16x16x32_bf16 v[168:171], a[128:131], a[152:155], v[168:171]\n\t"
"ds_read_b128 a[148:151], v224 offset:16800\n\t"
"v_mfma_f32_16x16x32_bf16 v[168:171], a[132:135], a[168:171], v[168:171]\n\t"
"ds_read_b128 a[164:167], v224 offset:18848\n\t"
"v_mfma_f32_16x16x32_bf16 v[184:187], a[136:139], a[152:155], v[184:187]\n\t"
"v_mfma_f32_16x16x32_bf16 v[184:187], a[140:143], a[168:171], v[184:187]\n\t"
"v_mfma_f32_16x16x32_bf16 v[188:191], a[136:139], a[156:159], v[188:191]\n\t"
"ds_read_b128 a[152:155], v224 offset:17056\n\t"
"v_mfma_f32_16x16x32_bf16 v[188:191], a[140:143], a[172:175], v[188:191]\n\t"
"ds_read_b128 a[168:171], v224 offset:19104\n\t"
"v_mfma_f32_16x16x32_bf16 v[172:175], a[128:131], a[156:159], v[172:175]\n\t"
"v_mfma_f32_16x16x32_bf16 v[172:175], a[132:135], a[172:175], v[172:175]\n\t"
"v_mfma_f32_16x16x32_bf16 v[156:159], a[120:123], a[156:159], v[156:159]\n\t"
"ds_read_b128 a[156:159], v224 offset:17312\n\t"
"v_mfma_f32_16x16x32_bf16 v[156:159], a[124:127], a[172:175], v[156:159]\n\t"
"s_waitcnt lgkmcnt(5)\n\t"
"ds_read_b128 a[172:175], v224 offset:19360\n\t"
"v_mfma_f32_4x4x4_16b_bf16 v[200:203], a[252:253], a[178:179], 0\n\t"
"ds_read_b64_tr_b16 a[178:179], v248 offset:41600\n\t"
"v_mfma_f32_4x4x4_16b_bf16 v[204:207], a[252:253], a[186:187], 0\n\t"
"ds_read_b64_tr_b16 a[186:187], v249 offset:41600\n\t"
"v_mfma_f32_4x4x4_16b_bf16 v[216:219], a[252:253], a[194:195], 0\n\t"
"ds_read_b64_tr_b16 a[194:195], v250 offset:41600\n\t"
"v_mfma_f32_4x4x4_16b_bf16 v[220:223], a[252:253], a[202:203], 0\n\t"
".p2align 3\n\t"
"ds_read_b64_tr_b16 a[202:203], v251 offset:41600\n\t"
"v_pk_fma_f32 v[48:49], v[144:145], s[90:91], v[48:49]\n\t"
"v_pk_fma_f32 v[50:51], v[146:147], s[90:91], v[50:51]\n\t"
"v_pk_add_f32 v[144:145], v[48:49], v[144:145] neg_lo:[0,1] neg_hi:[0,1]\n\t"
"v_pk_add_f32 v[146:147], v[50:51], v[146:147] neg_lo:[0,1] neg_hi:[0,1]\n\t"
"v_pk_fma_f32 v[64:65], v[160:161], s[90:91], v[64:65]\n\t"
"v_pk_fma_f32 v[66:67], v[162:163], s[90:91], v[66:67]\n\t"
"v_pk_add_f32 v[160:161], v[64:65], v[160:161] neg_lo:[0,1] neg_hi:[0,1]\n\t"
"v_pk_add_f32 v[162:163], v[66:67], v[162:163] neg_lo:[0,1] neg_hi:[0,1]\n\t"
"v_pk_fma_f32 v[80:81], v[176:177], s[90:91], v[80:81]\n\t"
"v_pk_fma_f32 v[82:83], v[178:179], s[90:91], v[82:83]\n\t"
"v_pk_add_f32 v[176:177], v[80:81], v[176:177] neg_lo:[0,1] neg_hi:[0,1]\n\t"
"v_pk_add_f32 v[178:179], v[82:83], v[178:179] neg_lo:[0,1] neg_hi:[0,1]\n\t"
"v_pk_fma_f32 v[84:85], v[180:181], s[90:91], v[84:85]\n\t"
"v_pk_fma_f32 v[86:87], v[182:183], s[90:91], v[86:87]\n\t"
"v_pk_add_f32 v[180:181], v[84:85], v[180:181] neg_lo:[0,1] neg_hi:[0,1]\n\t"
"v_pk_add_f32 v[182:183], v[86:87], v[182:183] neg_lo:[0,1] neg_hi:[0,1]\n\t"
"v_pk_fma_f32 v[68:69], v[164:165], s[90:91], v[68:69]\n\t"
"v_pk_fma_f32 v[70:71], v[166:167], s[90:91], v[70:71]\n\t"
"v_pk_add_f32 v[164:165], v[68:69], v[164:165] neg_lo:[0,1] neg_hi:[0,1]\n\t"
"v_pk_add_f32 v[166:167], v[70:71], v[166:167] neg_lo:[0,1] neg_hi:[0,1]\n\t"
"v_pk_fma_f32 v[52:53], v[148:149], s[90:91], v[52:53]\n\t"
"v_pk_fma_f32 v[54:55], v[150:151], s[90:91], v[54:55]\n\t"
"v_pk_add_f32 v[148:149], v[52:53], v[148:149] neg_lo:[0,1] neg_hi:[0,1]\n\t"
"v_pk_add_f32 v[150:151], v[54:55], v[150:151] neg_lo:[0,1] neg_hi:[0,1]\n\t"
"v_pk_fma_f32 v[56:57], v[152:153], s[90:91], v[56:57]\n\t"
"v_pk_fma_f32 v[58:59], v[154:155], s[90:91], v[58:59]\n\t"
"v_pk_add_f32 v[152:153], v[56:57], v[152:153] neg_lo:[0,1] neg_hi:[0,1]\n\t"
"v_pk_add_f32 v[154:155], v[58:59], v[154:155] neg_lo:[0,1] neg_hi:[0,1]\n\t"
"v_pk_fma_f32 v[72:73], v[168:169], s[90:91], v[72:73]\n\t"
"v_pk_fma_f32 v[74:75], v[170:171], s[90:91], v[74:75]\n\t"
"v_pk_add_f32 v[168:169], v[72:73], v[168:169] neg_lo:[0,1] neg_hi:[0,1]\n\t"
"v_pk_add_f32 v[170:171], v[74:75], v[170:171] neg_lo:[0,1] neg_hi:[0,1]\n\t"
"v_pk_fma_f32 v[88:89], v[184:185], s[90:91], v[88:89]\n\t"
"v_pk_fma_f32 v[90:91], v[186:187], s[90:91], v[90:91]\n\t"
"v_pk_add_f32 v[184:185], v[88:89], v[184:185] neg_lo:[0,1] neg_hi:[0,1]\n\t"
"v_pk_add_f32 v[186:187], v[90:91], v[186:187] neg_lo:[0,1] neg_hi:[0,1]\n\t"
"v_pk_fma_f32 v[92:93], v[188:189], s[90:91], v[92:93]\n\t"
"v_pk_fma_f32 v[94:95], v[190:191], s[90:91], v[94:95]\n\t"
"v_pk_add_f32 v[188:189], v[92:93], v[188:189] neg_lo:[0,1] neg_hi:[0,1]\n\t"
"v_pk_add_f32 v[190:191], v[94:95], v[190:191] neg_lo:[0,1] neg_hi:[0,1]\n\t"
"v_pk_fma_f32 v[76:77], v[172:173], s[90:91], v[76:77]\n\t"
"v_pk_fma_f32 v[78:79], v[174:175], s[90:91], v[78:79]\n\t"
"v_pk_add_f32 v[172:173], v[76:77], v[172:173] neg_lo:[0,1] neg_hi:[0,1]\n\t"
"v_pk_add_f32 v[174:175], v[78:79], v[174:175] neg_lo:[0,1] neg_hi:[0,1]\n\t"
"v_pk_fma_f32 v[60:61], v[156:157], s[90:91], v[60:61]\n\t"
"v_pk_fma_f32 v[62:63], v[158:159], s[90:91], v[62:63]\n\t"
"v_pk_add_f32 v[156:157], v[60:61], v[156:157] neg_lo:[0,1] neg_hi:[0,1]\n\t"
"v_pk_add_f32 v[158:159], v[62:63], v[158:159] neg_lo:[0,1] neg_hi:[0,1]\n\t"
"v_cvt_pk_bf16_f32 v64, v64, v65\n\t"
"v_mfma_f32_4x4x4_16b_bf16 v[192:195], a[254:255], a[176:177], v[200:203]\n\t"
"ds_read_b64_tr_b16 a[176:177], v248 offset:41472\n\t"
"v_cvt_pk_bf16_f32 v48, v48, v49\n\t"
"v_cvt_pk_bf16_f32 v68, v68, v69\n\t"
"v_mfma_f32_4x4x4_16b_bf16 v[196:199], a[254:255], a[184:185], v[204:207]\n\t"
"ds_read_b64_tr_b16 a[184:185], v249 offset:41472\n\t"
"v_cvt_pk_bf16_f32 v52, v52, v53\n\t"
"v_cvt_pk_bf16_f32 v65, v66, v67\n\t"
"v_mfma_f32_4x4x4_16b_bf16 v[200:203], a[254:255], a[180:181], v[200:203]\n\t"
"ds_read_b64_tr_b16 a[180:181], v249 offset:41728\n\t"
"v_cvt_pk_bf16_f32 v53, v54, v55\n\t"
"v_mfma_f32_4x4x4_16b_bf16 v[204:207], a[254:255], a[188:189], v[204:207]\n\t"
"v_cvt_pk_bf16_f32 v49, v50, v51\n\t"
"v_mfma_f32_4x4x4_16b_bf16 v[208:211], a[254:255], a[192:193], v[216:219]\n\t"
"ds_write2st64_b64 v228, v[48:49], v[52:53] offset0:0 offset1:13\n\t"
"v_mfma_f32_4x4x4_16b_bf16 v[212:215], a[254:255], a[200:201], v[220:223]\n\t"
"v_cvt_pk_bf16_f32 v69, v70, v71\n\t"
"v_mfma_f32_4x4x4_16b_bf16 v[216:219], a[254:255], a[196:197], v[216:219]\n\t"
"v_cvt_pk_bf16_f32 v56, v56, v57\n\t"
"v_mfma_f32_4x4x4_16b_bf16 v[220:223], a[254:255], a[204:205], v[220:223]\n\t"
"ds_write2st64_b64 v246, v[64:65], v[68:69] offset0:0 offset1:13\n\t"
"v_mfma_f32_16x16x32_bf16 v[0:3], a[0:3], a[144:147], v[0:3]\n\t"
"v_cvt_pk_bf16_f32 v84, v84, v85\n\t"
"v_cvt_pk_bf16_f32 v60, v60, v61\n\t"
"v_mfma_f32_16x16x32_bf16 v[0:3], a[4:7], a[160:163], v[0:3]\n\t"
"v_cvt_pk_bf16_f32 v80, v80, v81\n\t"
"v_cvt_pk_bf16_f32 v61, v62, v63\n\t"
"v_mfma_f32_16x16x32_bf16 v[16:19], a[8:11], a[144:147], v[16:19]\n\t"
"v_cvt_pk_bf16_f32 v195, -v195, -v199\n\t"
"v_cvt_pk_bf16_f32 v57, v58, v59\n\t"
"v_mfma_f32_16x16x32_bf16 v[16:19], a[12:15], a[160:163], v[16:19]\n\t"
"ds_read_b64_tr_b16 a[188:189], v248 offset:41728\n\t"
"ds_write2st64_b64 v228, v[56:57], v[60:61] offset0:26 offset1:39\n\t"
"v_mfma_f32_16x16x32_bf16 v[32:35], a[16:19], a[144:147], v[32:35]\n\t"
"v_cvt_pk_bf16_f32 v208, -v208, -v212\n\t"
"v_cvt_pk_bf16_f32 v81, v82, v83\n\t"
"v_mfma_f32_16x16x32_bf16 v[32:35], a[20:23], a[160:163], v[32:35]\n\t"
"s_waitcnt lgkmcnt(4)\n\t"
"v_cvt_pk_bf16_f32 v201, v201, v205\n\t"
"v_cvt_pk_bf16_f32 v76, v76, v77\n\t"
"v_mfma_f32_16x16x32_bf16 v[36:39], a[16:19], a[148:151], v[36:39]\n\t"
"v_cvt_pk_bf16_f32 v202, v202, v206\n\t"
"v_cvt_pk_bf16_f32 v85, v86, v87\n\t"
"v_mfma_f32_16x16x32_bf16 v[36:39], a[20:23], a[164:167], v[36:39]\n\t"
"s_nop 0\n\t"
"ds_read_b128 a[144:147], v224 offset:17568\n\t"
"ds_read_b128 a[160:163], v224 offset:19616\n\t"
"ds_write2st64_b64 v247, v[80:81], v[84:85] offset0:0 offset1:13\n\t"
"v_mfma_f32_16x16x32_bf16 v[20:23], a[8:11], a[148:151], v[20:23]\n\t"
"v_cvt_pk_bf16_f32 v72, v72, v73\n\t"
"v_cvt_pk_bf16_f32 v73, v74, v75\n\t"
"v_mfma_f32_16x16x32_bf16 v[20:23], a[12:15], a[164:167], v[20:23]\n\t"
"v_cvt_pk_bf16_f32 v211, -v211, -v215\n\t"
"v_cvt_pk_bf16_f32 v219, v219, v223\n\t"
"v_mfma_f32_16x16x32_bf16 v[4:7], a[0:3], a[148:151], v[4:7]\n\t"
"v_cvt_pk_bf16_f32 v200, v200, v204\n\t"
"v_cvt_pk_bf16_f32 v77, v78, v79\n\t"
"v_mfma_f32_16x16x32_bf16 v[4:7], a[4:7], a[164:167], v[4:7]\n\t"
"s_nop 0\n\t"
"ds_read_b128 a[148:151], v224 offset:17824\n\t"
"ds_read_b128 a[164:167], v224 offset:19872\n\t"
"ds_write_addtid_b32 v195 offset:5152\n\t"
"ds_write_addtid_b32 v211 offset:7200\n\t"
"v_mfma_f32_16x16x32_bf16 v[8:11], a[0:3], a[152:155], v[8:11]\n\t"
"ds_write_addtid_b32 v201 offset:12448\n\t"
"v_cvt_pk_bf16_f32 v209, -v209, -v213\n\t"
"v_mfma_f32_16x16x32_bf16 v[8:11], a[4:7], a[168:171], v[8:11]\n\t"
"ds_write_addtid_b32 v209 offset:6176\n\t"
"v_cvt_pk_bf16_f32 v194, -v194, -v198\n\t"
"v_mfma_f32_16x16x32_bf16 v[24:27], a[8:11], a[152:155], v[24:27]\n\t"
"ds_write_addtid_b32 v194 offset:1024\n\t"
"v_cvt_pk_bf16_f32 v92, v92, v93\n\t"
"v_mfma_f32_16x16x32_bf16 v[24:27], a[12:15], a[168:171], v[24:27]\n\t"
"s_nop 0\n\t"
"ds_write_addtid_b32 v202 offset:9344\n\t"
"ds_write_addtid_b32 v208 offset:2048\n\t"
"ds_write_addtid_b32 v219 offset:15520\n\t"
"v_mfma_f32_16x16x32_bf16 v[40:43], a[16:19], a[152:155], v[40:43]\n\t"
"v_cvt_pk_bf16_f32 v192, -v192, -v196\n\t"
"v_cvt_pk_bf16_f32 v93, v94, v95\n\t"
"v_mfma_f32_16x16x32_bf16 v[40:43], a[20:23], a[168:171], v[40:43]\n\t"
"ds_read_b128 a[152:155], v224 offset:18080\n\t"
"ds_write2st64_b64 v246, v[72:73], v[76:77] offset0:26 offset1:39\n\t"
"v_mfma_f32_16x16x32_bf16 v[44:47], a[16:19], a[156:159], v[44:47]\n\t"
"v_cvt_pk_bf16_f32 v218, v218, v222\n\t"
"v_cvt_pk_bf16_f32 v88, v88, v89\n\t"
"v_mfma_f32_16x16x32_bf16 v[44:47], a[20:23], a[172:175], v[44:47]\n\t"
"buffer_load_dwordx4 a[16:19], off, s[92:95], s59 offset:0 sc1\n\t"
"v_mfma_f32_16x16x32_bf16 v[28:31], a[8:11], a[156:159], v[28:31]\n\t"
"v_cvt_pk_bf16_f32 v216, v216, v220\n\t"
"v_cvt_pk_bf16_f32 v89, v90, v91\n\t"
"v_mfma_f32_16x16x32_bf16 v[28:31], a[12:15], a[172:175], v[28:31]\n\t"
"ds_read_b128 a[168:171], v224 offset:20128\n\t"
"ds_write2st64_b64 v247, v[88:89], v[92:93] offset0:26 offset1:39\n\t"
"v_mfma_f32_16x16x32_bf16 v[12:15], a[0:3], a[156:159], v[12:15]\n\t"
"v_cvt_pk_bf16_f32 v210, -v210, -v214\n\t"
"v_cvt_pk_bf16_f32 v217, v217, v221\n\t"
"v_mfma_f32_16x16x32_bf16 v[12:15], a[4:7], a[172:175], v[12:15]\n\t"
"s_waitcnt lgkmcnt(8)\n\t"
"buffer_load_dwordx4 a[20:23], off, s[92:95], s59 offset:1024 sc1\n\t"
"v_mfma_f32_16x16x32_bf16 v[96:99], a[24:27], a[144:147], v[96:99]\n\t"
"v_cvt_pk_bf16_f32 v193, -v193, -v197\n\t"
"v_cvt_pk_bf16_f32 v203, v203, v207\n\t"
"v_mfma_f32_16x16x32_bf16 v[96:99], a[28:31], a[160:163], v[96:99]\n\t"
"s_nop 0\n\t"
"ds_write_addtid_b32 v192 offset:0\n\t"
"ds_write_addtid_b32 v193 offset:4128\n\t"
"ds_write_addtid_b32 v200 offset:8320\n\t"
"ds_write_addtid_b32 v210 offset:3072\n\t"
"ds_write_addtid_b32 v216 offset:10368\n\t"
"ds_write_addtid_b32 v218 offset:11392\n\t"
"v_mfma_f32_16x16x32_bf16 v[112:115], a[32:35], a[144:147], v[112:115]\n\t"
"s_nop 0\n\t"
"ds_write_addtid_b32 v203 offset:13472\n\t"
"ds_write_addtid_b32 v217 offset:14496\n\t"
"v_mfma_f32_16x16x32_bf16 v[112:115], a[36:39], a[160:163], v[112:115]\n\t"
"buffer_load_dwordx4 a[12:15], off, s[96:99], s58 offset:3072 sc1\n\t"
"v_mfma_f32_16x16x32_bf16 v[128:131], a[40:43], a[144:147], v[128:131]\n\t"
"s_nop 0\n\t"
"ds_read_b128 a[156:159], v224 offset:18336\n\t"
"ds_read_b128 a[172:175], v224 offset:20384\n\t"
"v_mfma_f32_16x16x32_bf16 v[128:131], a[44:47], a[160:163], v[128:131]\n\t"
"buffer_load_dwordx4 a[8:11], off, s[96:99], s58 offset:2048 sc1\n\t"
"v_mfma_f32_16x16x32_bf16 v[132:135], a[40:43], a[148:151], v[132:135]\n\t"
"s_nop 0\n\t"
"ds_read_b64_tr_b16 a[192:193], v250 offset:41472\n\t"
"ds_read_b64_tr_b16 a[200:201], v251 offset:41472\n\t"
"v_mfma_f32_16x16x32_bf16 v[132:135], a[44:47], a[164:167], v[132:135]\n\t"
"buffer_load_dwordx4 a[0:3], off, s[96:99], s58 offset:0 sc1\n\t"
"v_mfma_f32_16x16x32_bf16 v[116:119], a[32:35], a[148:151], v[116:119]\n\t"
"s_nop 0\n\t"
"ds_read_b64_tr_b16 a[196:197], v251 offset:41728\n\t"
"ds_read_b64_tr_b16 a[204:205], v250 offset:41728\n\t"
"v_mfma_f32_16x16x32_bf16 v[116:119], a[36:39], a[164:167], v[116:119]\n\t"
"buffer_load_dwordx4 a[4:7], off, s[96:99], s58 offset:1024 sc1\n\t"
"v_mfma_f32_16x16x32_bf16 v[100:103], a[24:27], a[148:151], v[100:103]\n\t"
"s_waitcnt lgkmcnt(4)\n\t"
"v_mfma_f32_16x16x32_bf16 v[100:103], a[28:31], a[164:167], v[100:103]\n\t"
"s_barrier\n\t"
"v_mfma_f32_16x16x32_bf16 v[104:107], a[24:27], a[152:155], v[104:107]\n\t"
"ds_read_b128 a[144:147], v224 offset:0\n\t"
"v_cvt_pk_bf16_f32 v144, v144, v145\n\t"
"v_mfma_f32_16x16x32_bf16 v[104:107], a[28:31], a[168:171], v[104:107]\n\t"
"ds_read_b128 a[148:151], v224 offset:256\n\t"
"v_cvt_pk_bf16_f32 v180, v180, v181\n\t"
"v_mfma_f32_16x16x32_bf16 v[120:123], a[32:35], a[152:155], v[120:123]\n\t"
"ds_read_b128 a[160:163], v224 offset:2048\n\t"
"v_cvt_pk_bf16_f32 v160, v160, v161\n\t"
"v_mfma_f32_16x16x32_bf16 v[120:123], a[36:39], a[168:171], v[120:123]\n\t"
"s_nop 0\n\t"
"ds_read_b128 a[164:167], v224 offset:2304\n\t"
"v_cvt_pk_bf16_f32 v161, v162, v163\n\t"
"v_mfma_f32_16x16x32_bf16 v[136:139], a[40:43], a[152:155], v[136:139]\n\t"
"s_nop 0\n\t"
"v_cvt_pk_bf16_f32 v145, v146, v147\n\t"
"v_cvt_pk_bf16_f32 v172, v172, v173\n\t"
"v_mfma_f32_16x16x32_bf16 v[136:139], a[44:47], a[168:171], v[136:139]\n\t"
"s_nop 0\n\t"
"v_cvt_pk_bf16_f32 v176, v176, v177\n\t"
"v_cvt_pk_bf16_f32 v168, v168, v169\n\t"
"v_mfma_f32_16x16x32_bf16 v[140:143], a[40:43], a[156:159], v[140:143]\n\t"
"s_nop 0\n\t"
"ds_read_b128 v[56:59], v229 offset:0\n\t"
"v_cvt_pk_bf16_f32 v148, v148, v149\n\t"
"v_mfma_f32_16x16x32_bf16 v[140:143], a[44:47], a[172:175], v[140:143]\n\t"
"s_nop 0\n\t"
"ds_read_b128 v[72:75], v230 offset:0\n\t"
"buffer_load_dwordx4 a[40:43], off, s[92:95], s58 offset:2048 sc1\n\t"
"v_mfma_f32_16x16x32_bf16 v[124:127], a[32:35], a[156:159], v[124:127]\n\t"
"s_nop 0\n\t"
"v_cvt_pk_bf16_f32 v177, v178, v179\n\t"
"v_cvt_pk_bf16_f32 v156, v156, v157\n\t"
"v_mfma_f32_16x16x32_bf16 v[124:127], a[36:39], a[172:175], v[124:127]\n\t"
"Lpad9_%=:\n\t"
"s_cbranch_execz Lret1_%=\n\t"
"buffer_load_dwordx4 a[44:47], off, s[92:95], s58 offset:3072 sc1\n\t"
"v_mfma_f32_16x16x32_bf16 v[108:111], a[24:27], a[156:159], v[108:111]\n\t"
"ds_read_b128 v[92:95], v231 offset:13312\n\t"
"v_cvt_pk_bf16_f32 v184, v184, v185\n\t"
"v_mfma_f32_16x16x32_bf16 v[108:111], a[28:31], a[172:175], v[108:111]\n\t"
"s_waitcnt lgkmcnt(5)\n\t"
"ds_read_b128 v[76:79], v230 offset:13312\n\t"
"v_cvt_pk_bf16_f32 v164, v164, v165\n\t"
"v_mfma_f32_4x4x4_16b_bf16 v[200:203], a[252:253], a[178:179], 0\n\t"
"buffer_load_dwordx4 a[36:39], off, s[92:95], s58 offset:1024 sc1\n\t"
"v_mfma_f32_4x4x4_16b_bf16 v[204:207], a[252:253], a[186:187], 0\n\t"
"v_cvt_pk_bf16_f32 v157, v158, v159\n\t"
"v_mfma_f32_4x4x4_16b_bf16 v[216:219], a[252:253], a[194:195], 0\n\t"
"ds_read_b128 v[88:91], v231 offset:0\n\t"
"v_mfma_f32_4x4x4_16b_bf16 v[220:223], a[252:253], a[202:203], 0\n\t"
"ds_read_b128 v[60:63], v229 offset:13312\n\t"
"buffer_load_dwordx4 a[32:35], off, s[92:95], s58 offset:0 sc1\n\t"
"v_mfma_f32_4x4x4_16b_bf16 v[192:195], a[254:255], a[176:177], v[200:203]\n\t"
"v_cvt_pk_bf16_f32 v149, v150, v151\n\t"
"v_cvt_pk_bf16_f32 v181, v182, v183\n\t"
"v_mfma_f32_4x4x4_16b_bf16 v[196:199], a[254:255], a[184:185], v[204:207]\n\t"
"ds_read_b128 a[156:159], v224 offset:768\n\t"
"v_mfma_f32_4x4x4_16b_bf16 v[200:203], a[254:255], a[180:181], v[200:203]\n\t"
"ds_read_b128 a[168:171], v224 offset:2560\n\t"
"buffer_load_dwordx4 a[24:27], off, s[96:99], s57 offset:2048 sc1\n\t"
"v_mfma_f32_4x4x4_16b_bf16 v[204:207], a[254:255], a[188:189], v[204:207]\n\t"
"v_cvt_pk_bf16_f32 v152, v152, v153\n\t"
"v_mfma_f32_4x4x4_16b_bf16 v[208:211], a[254:255], a[192:193], v[216:219]\n\t"
"ds_read_b128 a[152:155], v224 offset:512\n\t"
"v_mfma_f32_4x4x4_16b_bf16 v[212:215], a[254:255], a[200:201], v[220:223]\n\t"
"ds_read_b128 a[172:175], v224 offset:2816\n\t"
"s_waitcnt lgkmcnt(6)\n\t"
"buffer_store_dwordx4 v[56:59], v232, s[16:19], s24 offen nt\n\t"
"v_mfma_f32_4x4x4_16b_bf16 v[216:219], a[254:255], a[196:197], v[216:219]\n\t"
"v_cvt_pk_bf16_f32 v165, v166, v167\n\t"
"v_mfma_f32_4x4x4_16b_bf16 v[220:223], a[254:255], a[204:205], v[220:223]\n\t"
"s_waitcnt lgkmcnt(4)\n\t"
"s_barrier\n\t"
"v_mfma_f32_16x16x32_bf16 v[0:3], a[48:51], a[144:147], v[0:3]\n\t"
"ds_write2st64_b64 v228, v[144:145], v[148:149] offset0:0 offset1:13\n\t"
"v_mfma_f32_16x16x32_bf16 v[0:3], a[52:55], a[160:163], v[0:3]\n\t"
"buffer_load_dwordx4 a[28:31], off, s[96:99], s57 offset:3072 sc1\n\t"
"v_mfma_f32_16x16x32_bf16 v[16:19], a[56:59], a[144:147], v[16:19]\n\t"
"v_cvt_pk_bf16_f32 v203, v203, v207\n\t"
"v_cvt_pk_bf16_f32 v153, v154, v155\n\t"
"v_mfma_f32_16x16x32_bf16 v[16:19], a[60:63], a[160:163], v[16:19]\n\t"
"ds_write2st64_b64 v246, v[160:161], v[164:165] offset0:0 offset1:13\n\t"
"v_mfma_f32_16x16x32_bf16 v[32:35], a[64:67], a[144:147], v[32:35]\n\t"
"v_cvt_pk_bf16_f32 v208, -v208, -v212\n\t"
"v_cvt_pk_bf16_f32 v188, v188, v189\n\t"
"v_mfma_f32_16x16x32_bf16 v[32:35], a[68:71], a[160:163], v[32:35]\n\t"
"buffer_store_dwordx4 v[72:75], v233, s[16:19], s24 offen nt\n\t"
"v_mfma_f32_16x16x32_bf16 v[36:39], a[64:67], a[148:151], v[36:39]\n\t"
"v_cvt_pk_bf16_f32 v209, -v209, -v213\n\t"
"v_cvt_pk_bf16_f32 v218, v218, v222\n\t"
"v_mfma_f32_16x16x32_bf16 v[36:39], a[68:71], a[164:167], v[36:39]\n\t"
"s_nop 0\n\t"
"ds_read_b128 a[144:147], v224 offset:1024\n\t"
"ds_read_b128 a[160:163], v224 offset:3072\n\t"
"ds_write2st64_b64 v247, v[176:177], v[180:181] offset0:0 offset1:13\n\t"
"v_mfma_f32_16x16x32_bf16 v[20:23], a[56:59], a[148:151], v[20:23]\n\t"
"v_cvt_pk_bf16_f32 v194, -v194, -v198\n\t"
"v_cvt_pk_bf16_f32 v169, v170, v171\n\t"
"v_mfma_f32_16x16x32_bf16 v[20:23], a[60:63], a[164:167], v[20:23]\n\t"
"buffer_store_dwordx4 v[88:91], v234, s[16:19], s24 offen nt\n\t"
"v_mfma_f32_16x16x32_bf16 v[4:7], a[48:51], a[148:151], v[4:7]\n\t"
"v_cvt_pk_bf16_f32 v219, v219, v223\n\t"
"v_cvt_pk_bf16_f32 v173, v174, v175\n\t"
"v_mfma_f32_16x16x32_bf16 v[4:7], a[52:55], a[164:167], v[4:7]\n\t"
"s_waitcnt lgkmcnt(4)\n\t"
"ds_read_b128 a[148:151], v224 offset:1280\n\t"
"ds_read_b128 a[164:167], v224 offset:3328\n\t"
"ds_write2st64_b64 v246, v[168:169], v[172:173] offset0:26 offset1:39\n\t"
"v_mfma_f32_16x16x32_bf16 v[8:11], a[48:51], a[152:155], v[8:11]\n\t"
"v_cvt_pk_bf16_f32 v201, v201, v205\n\t"
"v_cvt_pk_bf16_f32 v216, v216, v220\n\t"
"v_mfma_f32_16x16x32_bf16 v[8:11], a[52:55], a[168:171], v[8:11]\n\t"
"buffer_store_dwordx4 v[60:63], v232, s[16:19], s27 offen nt\n\t"
"v_mfma_f32_16x16x32_bf16 v[24:27], a[56:59], a[152:155], v[24:27]\n\t"
"v_cvt_pk_bf16_f32 v200, v200, v204\n\t"
"v_cvt_pk_bf16_f32 v211, -v211, -v215\n\t"
"v_mfma_f32_16x16x32_bf16 v[24:27], a[60:63], a[168:171], v[24:27]\n\t"
"s_nop 0\n\t"
"ds_write_addtid_b32 v208 offset:18592\n\t"
"ds_write_addtid_b32 v209 offset:22720\n\t"
"ds_write_addtid_b32 v216 offset:26912\n\t"
"ds_write_addtid_b32 v219 offset:32064\n\t"
"ds_write2st64_b64 v228, v[152:153], v[156:157] offset0:26 offset1:39\n\t"
"v_mfma_f32_16x16x32_bf16 v[40:43], a[64:67], a[152:155], v[40:43]\n\t"
"v_cvt_pk_bf16_f32 v185, v186, v187\n\t"
"v_cvt_pk_bf16_f32 v189, v190, v191\n\t"
"v_mfma_f32_16x16x32_bf16 v[40:43], a[68:71], a[168:171], v[40:43]\n\t"
"v_cvt_pk_bf16_f32 v192, -v192, -v196\n\t"
"v_cvt_pk_bf16_f32 v210, -v210, -v214\n\t"
"v_mfma_f32_16x16x32_bf16 v[44:47], a[64:67], a[156:159], v[44:47]\n\t"
"buffer_load_dwordx4 a[64:67], off, s[92:95], s65 offset:0 sc1\n\t"
"v_mfma_f32_16x16x32_bf16 v[44:47], a[68:71], a[172:175], v[44:47]\n\t"
"ds_write_addtid_b32 v200 offset:24864\n\t"
"ds_write2st64_b64 v247, v[184:185], v[188:189] offset0:26 offset1:39\n\t"
"v_mfma_f32_16x16x32_bf16 v[28:31], a[56:59], a[156:159], v[28:31]\n\t"
"buffer_load_dwordx4 a[68:71], off, s[92:95], s65 offset:1024 sc1\n\t"
"v_mfma_f32_16x16x32_bf16 v[28:31], a[60:63], a[172:175], v[28:31]\n\t"
"v_cvt_pk_bf16_f32 v202, v202, v206\n\t"
"v_cvt_pk_bf16_f32 v217, v217, v221\n\t"
"v_mfma_f32_16x16x32_bf16 v[12:15], a[48:51], a[156:159], v[12:15]\n\t"
"v_cvt_pk_bf16_f32 v193, -v193, -v197\n\t"
"v_cvt_pk_bf16_f32 v195, -v195, -v199\n\t"
"v_mfma_f32_16x16x32_bf16 v[12:15], a[52:55], a[172:175], v[12:15]\n\t"
"s_waitcnt lgkmcnt(7)\n\t"
"ds_write_addtid_b32 v192 offset:16544\n\t"
"ds_write_addtid_b32 v201 offset:28992\n\t"
"ds_write_addtid_b32 v203 offset:30016\n\t"
"ds_write_addtid_b32 v211 offset:23744\n\t"
"ds_write_addtid_b32 v218 offset:27936\n\t"
"buffer_store_dwordx4 v[76:79], v233, s[16:19], s27 offen nt\n\t"
"v_mfma_f32_16x16x32_bf16 v[96:99], a[72:75], a[144:147], v[96:99]\n\t"
"s_nop 0\n\t"
"ds_write_addtid_b32 v210 offset:19616\n\t"
"ds_write_addtid_b32 v217 offset:31040\n\t"
"v_mfma_f32_16x16x32_bf16 v[96:99], a[76:79], a[160:163], v[96:99]\n\t"
"s_nop 0\n\t"
"ds_write_addtid_b32 v193 offset:20672\n\t"
"ds_write_addtid_b32 v194 offset:17568\n\t"
"v_mfma_f32_16x16x32_bf16 v[112:115], a[80:83], a[144:147], v[112:115]\n\t"
"s_nop 0\n\t"
"ds_write_addtid_b32 v195 offset:21696\n\t"
"ds_write_addtid_b32 v202 offset:25888\n\t"
"v_mfma_f32_16x16x32_bf16 v[112:115], a[84:87], a[160:163], v[112:115]\n\t"
"buffer_load_dwordx4 a[60:63], off, s[96:99], s64 offset:3072 sc1\n\t"
"buffer_load_dwordx4 a[56:59], off, s[96:99], s64 offset:2048 sc1\n\t"
"v_mfma_f32_16x16x32_bf16 v[128:131], a[88:91], a[144:147], v[128:131]\n\t"
"ds_read_b128 a[152:155], v224 offset:1536\n\t"
"v_accvgpr_read_b32 v248, a208\n\t"
"v_mfma_f32_16x16x32_bf16 v[128:131], a[92:95], a[160:163], v[128:131]\n\t"
"ds_read_b128 a[168:171], v224 offset:3584\n\t"
"buffer_load_dwordx4 a[48:51], off, s[96:99], s64 offset:0 sc1\n\t"
"v_mfma_f32_16x16x32_bf16 v[132:135], a[88:91], a[148:151], v[132:135]\n\t"
"s_nop 0\n\t"
"ds_read_b128 a[156:159], v224 offset:1792\n\t"
"ds_read_b128 a[172:175], v224 offset:3840\n\t"
"v_mfma_f32_16x16x32_bf16 v[132:135], a[92:95], a[164:167], v[132:135]\n\t"
"buffer_store_dwordx4 v[92:95], v234, s[16:19], s27 offen nt\n\t"
"v_mfma_f32_16x16x32_bf16 v[116:119], a[80:83], a[148:151], v[116:119]\n\t"
"v_add_u32 v248, 101632, v248\n\t"
"v_xor_b32 v249, 32, v248\n\t"
"v_mfma_f32_16x16x32_bf16 v[116:119], a[84:87], a[164:167], v[116:119]\n\t"
"v_xor_b32 v250, 64, v248\n\t"
"v_xor_b32 v251, 96, v248\n\t"
"v_mfma_f32_16x16x32_bf16 v[100:103], a[72:75], a[148:151], v[100:103]\n\t"
"buffer_load_dwordx4 a[52:55], off, s[96:99], s64 offset:1024 sc1\n\t"
"v_mfma_f32_16x16x32_bf16 v[100:103], a[76:79], a[164:167], v[100:103]\n\t"
"s_waitcnt lgkmcnt(0)\n\t"
"s_barrier\n\t"
"v_mfma_f32_16x16x32_bf16 v[104:107], a[72:75], a[152:155], v[104:107]\n\t"
"s_nop 0\n\t"
"ds_read_b128 a[144:147], v224 offset:16544\n\t"
"ds_read_b128 a[160:163], v224 offset:18592\n\t"
"v_mfma_f32_16x16x32_bf16 v[104:107], a[76:79], a[168:171], v[104:107]\n\t"
"s_nop 0\n\t"
"ds_read_b128 a[148:151], v224 offset:16800\n\t"
"ds_read_b128 a[164:167], v224 offset:18848\n\t"
"v_mfma_f32_16x16x32_bf16 v[120:123], a[80:83], a[152:155], v[120:123]\n\t"
"ds_read_b64_tr_b16 a[176:177], v248 offset:20736\n\t"
"v_mfma_f32_16x16x32_bf16 v[120:123], a[84:87], a[168:171], v[120:123]\n\t"
"ds_read_b64_tr_b16 a[178:179], v249 offset:20736\n\t"
"v_mfma_f32_16x16x32_bf16 v[136:139], a[88:91], a[152:155], v[136:139]\n\t"
"s_nop 0\n\t"
"ds_read_b64_tr_b16 a[180:181], v248 offset:21248\n\t"
"ds_read_b64_tr_b16 a[182:183], v249 offset:21248\n\t"
"v_mfma_f32_16x16x32_bf16 v[136:139], a[92:95], a[168:171], v[136:139]\n\t"
"s_nop 0\n\t"
"ds_read_b64_tr_b16 a[184:185], v250 offset:23040\n\t"
"ds_read_b64_tr_b16 a[186:187], v251 offset:23040\n\t"
"v_mfma_f32_16x16x32_bf16 v[140:143], a[88:91], a[156:159], v[140:143]\n\t"
"ds_read_b64_tr_b16 a[188:189], v250 offset:23552\n\t"
"v_mfma_f32_16x16x32_bf16 v[140:143], a[92:95], a[172:175], v[140:143]\n\t"
"s_nop 0\n\t"
"ds_read_b64_tr_b16 a[190:191], v251 offset:23552\n\t"
"ds_read_b64_tr_b16 a[192:193], v250 offset:20736\n\t"
"v_mfma_f32_16x16x32_bf16 v[124:127], a[80:83], a[156:159], v[124:127]\n\t"
"s_nop 0\n\t"
"ds_read_b64_tr_b16 a[194:195], v251 offset:20736\n\t"
"ds_read_b64_tr_b16 a[196:197], v250 offset:21248\n\t"
"v_mfma_f32_16x16x32_bf16 v[124:127], a[84:87], a[172:175], v[124:127]\n\t"
"s_waitcnt lgkmcnt(7)\n\t"
"ds_read_b64_tr_b16 a[198:199], v251 offset:21248\n\t"
"ds_read_b64_tr_b16 a[200:201], v248 offset:23040\n\t"
"v_mfma_f32_4x4x4_16b_bf16 v[192:195], a[210:211], a[176:177], 0\n\t"
"ds_read_b64_tr_b16 a[202:203], v249 offset:23040\n\t"
"v_mfma_f32_4x4x4_16b_bf16 v[196:199], a[210:211], a[178:179], 0\n\t"
"ds_read_b128 v[152:155], v229 offset:0\n\t"
"v_mfma_f32_4x4x4_16b_bf16 v[200:203], a[210:211], a[180:181], 0\n\t"
"ds_read_b128 a[168:171], v224 offset:19104\n\t"
"v_mfma_f32_4x4x4_16b_bf16 v[204:207], a[210:211], a[182:183], 0\n\t"
"buffer_load_dwordx4 a[88:91], off, s[92:95], s64 offset:2048 sc1\n\t"
"v_mfma_f32_16x16x32_bf16 v[108:111], a[72:75], a[156:159], v[108:111]\n\t"
"s_nop 0\n\t"
"ds_read_b128 a[156:159], v224 offset:17312\n\t"
"ds_read_b64_tr_b16 a[204:205], v248 offset:23552\n\t"
"v_mfma_f32_16x16x32_bf16 v[108:111], a[76:79], a[172:175], v[108:111]\n\t"
"buffer_load_dwordx4 a[92:95], off, s[92:95], s64 offset:3072 sc1\n\t"
"v_mfma_f32_16x16x32_bf16 v[0:3], a[96:99], a[144:147], v[0:3]\n\t"
"ds_read_b128 v[168:171], v230 offset:0\n\t"
"v_cvt_pk_bf16_f32 v194, v194, v198\n\t"
"v_mfma_f32_16x16x32_bf16 v[0:3], a[100:103], a[160:163], v[0:3]\n\t"
"v_cvt_pk_bf16_f32 v196, v192, v196\n\t"
"v_cvt_pk_bf16_f32 v195, v195, v199\n\t"
"v_mfma_f32_16x16x32_bf16 v[16:19], a[104:107], a[144:147], v[16:19]\n\t"
"ds_read_b128 a[172:175], v224 offset:19360\n\t"
"v_cvt_pk_bf16_f32 v199, v201, v205\n\t"
"v_mfma_f32_16x16x32_bf16 v[16:19], a[108:111], a[160:163], v[16:19]\n\t"
"s_waitcnt lgkmcnt(5)\n\t"
"buffer_store_dwordx4 v[152:155], v232, s[16:19], s20 offen nt\n\t"
"v_mfma_f32_16x16x32_bf16 v[32:35], a[112:115], a[144:147], v[32:35]\n\t"
"ds_read_b128 a[152:155], v224 offset:17056\n\t"
"v_cvt_pk_bf16_f32 v198, v200, v204\n\t"
"v_mfma_f32_16x16x32_bf16 v[32:35], a[116:119], a[160:163], v[32:35]\n\t"
"ds_read_b128 v[184:187], v231 offset:0\n\t"
"v_cvt_pk_bf16_f32 v197, v193, v197\n\t"
"v_mfma_f32_4x4x4_16b_bf16 v[208:211], a[210:211], a[184:185], 0\n\t"
"ds_write_addtid_b32 v198 offset:8320\n\t"
"v_mfma_f32_4x4x4_16b_bf16 v[212:215], a[210:211], a[186:187], 0\n\t"
"s_waitcnt lgkmcnt(4)\n\t"
"buffer_store_dwordx4 v[168:171], v233, s[16:19], s20 offen nt\n\t"
"v_mfma_f32_4x4x4_16b_bf16 v[216:219], a[210:211], a[188:189], 0\n\t"
"ds_read_b128 v[156:159], v229 offset:13312\n\t"
"v_mfma_f32_4x4x4_16b_bf16 v[220:223], a[210:211], a[190:191], 0\n\t"
"ds_read_b128 a[144:147], v224 offset:17568\n\t"
"v_cvt_pk_bf16_f32 v193, v203, v207\n\t"
"v_mfma_f32_16x16x32_bf16 v[36:39], a[112:115], a[148:151], v[36:39]\n\t"
"ds_write_addtid_b32 v196 offset:0\n\t"
"v_cvt_pk_bf16_f32 v192, v202, v206\n\t"
"v_mfma_f32_16x16x32_bf16 v[36:39], a[116:119], a[164:167], v[36:39]\n\t"
"buffer_load_dwordx4 a[84:87], off, s[92:95], s64 offset:1024 sc1\n\t"
"v_mfma_f32_16x16x32_bf16 v[20:23], a[104:107], a[148:151], v[20:23]\n\t"
"ds_write_addtid_b32 v199 offset:9344\n\t"
"v_cvt_pk_bf16_f32 v212, v208, v212\n\t"
"v_mfma_f32_16x16x32_bf16 v[20:23], a[108:111], a[164:167], v[20:23]\n\t"
"ds_read_b128 v[172:175], v230 offset:13312\n\t"
"v_cvt_pk_bf16_f32 v210, v210, v214\n\t"
"v_mfma_f32_16x16x32_bf16 v[4:7], a[96:99], a[148:151], v[4:7]\n\t"
"v_cvt_pk_bf16_f32 v208, v218, v222\n\t"
"v_cvt_pk_bf16_f32 v213, v209, v213\n\t"
"v_mfma_f32_16x16x32_bf16 v[4:7], a[100:103], a[164:167], v[4:7]\n\t"
"s_waitcnt lgkmcnt(6)\n\t"
"buffer_store_dwordx4 v[184:187], v234, s[16:19], s20 offen nt\n\t"
"v_cvt_pk_bf16_f32 v211, v211, v215\n\t"
"v_mfma_f32_4x4x4_16b_bf16 v[200:203], a[210:211], a[192:193], 0\n\t"
"ds_read_b128 a[148:151], v224 offset:17824\n\t"
"v_cvt_pk_bf16_f32 v215, v217, v221\n\t"
"v_mfma_f32_4x4x4_16b_bf16 v[204:207], a[210:211], a[194:195], 0\n\t"
"ds_write_addtid_b32 v197 offset:1024\n\t"
"v_cvt_pk_bf16_f32 v214, v216, v220\n\t"
"v_mfma_f32_4x4x4_16b_bf16 v[196:199], a[210:211], a[196:197], 0\n\t"
"v_cvt_pk_bf16_f32 v209, v219, v223\n\t"
"v_mfma_f32_4x4x4_16b_bf16 v[216:219], a[210:211], a[198:199], 0\n\t"
"buffer_load_dwordx4 a[80:83], off, s[92:95], s64 offset:0 sc1\n\t"
"v_mfma_f32_16x16x32_bf16 v[8:11], a[96:99], a[152:155], v[8:11]\n\t"
"s_nop 0\n\t"
"ds_read_b128 a[160:163], v224 offset:19616\n\t"
"ds_write_addtid_b32 v215 offset:13472\n\t"
"v_mfma_f32_16x16x32_bf16 v[8:11], a[100:103], a[168:171], v[8:11]\n\t"
"s_nop 0\n\t"
"ds_write_addtid_b32 v212 offset:4128\n\t"
"ds_read_b64_tr_b16 a[206:207], v249 offset:23552\n\t"
"v_mfma_f32_16x16x32_bf16 v[24:27], a[104:107], a[152:155], v[24:27]\n\t"
"v_cvt_pk_bf16_f32 v204, v200, v204\n\t"
"v_cvt_pk_bf16_f32 v202, v202, v206\n\t"
"v_mfma_f32_16x16x32_bf16 v[24:27], a[108:111], a[168:171], v[24:27]\n\t"
"s_waitcnt lgkmcnt(6)\n\t"
"buffer_store_dwordx4 v[156:159], v232, s[16:19], s25 offen nt\n\t"
"v_cvt_pk_bf16_f32 v203, v203, v207\n\t"
"v_mfma_f32_16x16x32_bf16 v[40:43], a[112:115], a[152:155], v[40:43]\n\t"
"ds_read_b128 v[188:191], v231 offset:13312\n\t"
"v_cvt_pk_bf16_f32 v206, v196, v216\n\t"
"v_mfma_f32_16x16x32_bf16 v[40:43], a[116:119], a[168:171], v[40:43]\n\t"
"ds_read_b128 a[164:167], v224 offset:19872\n\t"
"v_cvt_pk_bf16_f32 v207, v197, v217\n\t"
"v_mfma_f32_16x16x32_bf16 v[44:47], a[112:115], a[156:159], v[44:47]\n\t"
"v_cvt_pk_bf16_f32 v200, v198, v218\n\t"
"v_cvt_pk_bf16_f32 v205, v201, v205\n\t"
"v_mfma_f32_16x16x32_bf16 v[44:47], a[116:119], a[172:175], v[44:47]\n\t"
"s_waitcnt lgkmcnt(2)\n\t"
"ds_write_addtid_b32 v213 offset:5152\n\t"
"buffer_load_dwordx4 a[72:75], off, s[96:99], s63 offset:2048 sc1\n\t"
"v_mfma_f32_4x4x4_16b_bf16 v[220:223], a[210:211], a[200:201], 0\n\t"
"ds_write_addtid_b32 v214 offset:12448\n\t"
"v_mfma_f32_4x4x4_16b_bf16 v[212:215], a[210:211], a[202:203], 0\n\t"
"v_cvt_pk_bf16_f32 v201, v199, v219\n\t"
"v_mfma_f32_4x4x4_16b_bf16 v[196:199], a[210:211], a[204:205], 0\n\t"
"buffer_store_dwordx4 v[172:175], v233, s[16:19], s25 offen nt\n\t"
"v_mfma_f32_4x4x4_16b_bf16 v[216:219], a[210:211], a[206:207], 0\n\t"
"s_nop 0\n\t"
"ds_write_addtid_b32 v205 offset:3072\n\t"
"ds_write_addtid_b32 v207 offset:11392\n\t"
"v_mfma_f32_16x16x32_bf16 v[28:31], a[104:107], a[156:159], v[28:31]\n\t"
"s_nop 0\n\t"
"ds_write_addtid_b32 v204 offset:2048\n\t"
"ds_write_addtid_b32 v206 offset:10368\n\t"
"v_mfma_f32_16x16x32_bf16 v[28:31], a[108:111], a[172:175], v[28:31]\n\t"
"s_waitcnt lgkmcnt(7)\n\t"
"buffer_store_dwordx4 v[188:191], v234, s[16:19], s25 offen nt\n\t"
"v_mfma_f32_16x16x32_bf16 v[12:15], a[96:99], a[156:159], v[12:15]\n\t"
"v_cvt_pk_bf16_f32 v222, v222, v214\n\t"
"v_cvt_pk_bf16_f32 v213, v221, v213\n\t"
"v_mfma_f32_16x16x32_bf16 v[12:15], a[100:103], a[172:175], v[12:15]\n\t"
"v_cvt_pk_bf16_f32 v212, v220, v212\n\t"
"v_cvt_pk_bf16_f32 v223, v223, v215\n\t"
"v_mfma_f32_16x16x32_bf16 v[96:99], a[120:123], a[144:147], v[96:99]\n\t"
"ds_write_addtid_b32 v213 offset:7200\n\t"
"v_cvt_pk_bf16_f32 v214, v196, v216\n\t"
"v_mfma_f32_16x16x32_bf16 v[96:99], a[124:127], a[160:163], v[96:99]\n\t"
"ds_write_addtid_b32 v214 offset:14496\n\t"
"v_cvt_pk_bf16_f32 v215, v197, v217\n\t"
"v_mfma_f32_16x16x32_bf16 v[112:115], a[128:131], a[144:147], v[112:115]\n\t"
"s_nop 0\n\t"
"ds_write_addtid_b32 v212 offset:6176\n\t"
"ds_write_addtid_b32 v215 offset:15520\n\t"
"v_mfma_f32_16x16x32_bf16 v[112:115], a[132:135], a[160:163], v[112:115]\n\t"
"buffer_load_dwordx4 a[76:79], off, s[96:99], s63 offset:3072 sc1\n\t"
"v_mfma_f32_16x16x32_bf16 v[128:131], a[136:139], a[144:147], v[128:131]\n\t"
"ds_read_b128 a[152:155], v224 offset:18080\n\t"
"v_cvt_pk_bf16_f32 v221, v199, v219\n\t"
"v_mfma_f32_16x16x32_bf16 v[128:131], a[140:143], a[160:163], v[128:131]\n\t"
"s_waitcnt lgkmcnt(7)\n\t"
"ds_read_b128 a[172:175], v224 offset:20384\n\t"
"v_mfma_f32_16x16x32_bf16 v[132:135], a[136:139], a[148:151], v[132:135]\n\t"
"s_nop 0\n\t"
"ds_read_b128 a[168:171], v224 offset:20128\n\t"
"ds_read_b128 a[156:159], v224 offset:18336\n\t"
"v_mfma_f32_16x16x32_bf16 v[132:135], a[140:143], a[164:167], v[132:135]\n\t"
"buffer_load_dwordx4 a[112:115], off, s[92:95], s47 offset:0 sc1\n\t"
"v_mfma_f32_16x16x32_bf16 v[116:119], a[128:131], a[148:151], v[116:119]\n\t"
"v_cvt_pk_bf16_f32 v220, v198, v218\n\t"
"v_mfma_f32_16x16x32_bf16 v[116:119], a[132:135], a[164:167], v[116:119]\n\t"
"buffer_load_dwordx4 a[116:119], off, s[92:95], s47 offset:1024 sc1\n\t"
"v_mfma_f32_16x16x32_bf16 v[100:103], a[120:123], a[148:151], v[100:103]\n\t"
"s_waitcnt lgkmcnt(0)\n\t"
"v_accvgpr_read_b32 v224, a213\n\t"
"v_mfma_f32_16x16x32_bf16 v[100:103], a[124:127], a[164:167], v[100:103]\n\t"
"buffer_load_dwordx4 a[108:111], off, s[96:99], s46 offset:3072 sc1\n\t"
"s_barrier\n\t"
"s_add_u32 s71, s71, 1\n\t"
"v_mov_b32 v215, s71\n\t"
"s_mov_b64 exec, 1\n\t"
"buffer_store_dword v215, off, s[8:11], s70 sc1\n\t"
"s_mov_b64 exec, -1\n\t"
"s_sub_u32 s21, s21, 1\n\t"
"s_xor_b64 s[84:85], s[84:85], s[88:89]\n\t"
"s_add_u32 s28, s84, 4096\n\t"
"s_addc_u32 s29, s85, 0\n\t"
"s_branch Lunit_%=\n\t"
"Lfb_%=:\n\t"
"v_mbcnt_lo_u32_b32 v162, -1, 0\n\t"
"v_mbcnt_hi_u32_b32 v162, -1, v162\n\t"
"v_bfe_u32 v161, v162, 1, 2\n\t"
"v_lshlrev_b32 v163, 4, v162\n\t"
"v_and_or_b32 v161, v163, 16, v161\n\t"
"v_mul_u32_u24 v161, 0xc0, v161\n\t"
"v_lshrrev_b32 v163, 5, v162\n\t"
"v_mul_u32_u24 v163, 12, v163\n\t"
"v_bfe_u32 v164, v162, 3, 2\n\t"
"v_add3_u32 v161, v161, v163, v164\n\t"
"v_lshlrev_b32 v161, 1, v161\n\t"
"v_lshlrev_b32 v162, 1, v162\n\t"
"v_mov_b32 v152, 0\n\t"
"v_mov_b32 v153, 0\n\t"
"v_mov_b32 v234, 0\n\t"
"v_mov_b32 v233, 0\n\t"
"v_mov_b32 v232, 0\n\t"
"v_mov_b32 v231, 0\n\t"
"v_mov_b32 v230, 0\n\t"
"v_mov_b32 v159, 0\n\t"
"v_mov_b32 v160, 0\n\t"
"s_mov_b32 s67, 0\n\t"
"Lfb0_%=:\n\t"
"s_mov_b32 s69, 0\n\t"
"Lfb1_%=:\n\t"
"s_lshr_b32 s60, s69, 5\n\t"
"s_mul_i32 s56, s60, 12288\n\t"
"s_bfe_u32 s91, s69, 0x20001\n\t"
"s_mul_i32 s91, s91, 1536\n\t"
"s_add_u32 s56, s56, s91\n\t"
"s_bitcmp1_b32 s69, 0\n\t"
"s_cselect_b32 s91, 48, 0\n\t"
"s_add_u32 s56, s56, s91\n\t"
"s_bfe_u32 s90, s69, 0x20003\n\t"
"s_mul_i32 s91, s90, 96\n\t"
"s_add_u32 s56, s56, s91\n\t"
"s_lshl3_add_u32 s56, s67, s56\n\t"
"s_lshr_b32 s60, s69, 5\n\t"
"s_bfe_u32 s90, s69, 0x20003\n\t"
"s_and_b32 s91, s60, 1\n\t"
"s_lshl1_add_u32 s91, s67, s91\n\t"
"s_mul_i32 s90, s90, 288\n\t"
"s_add_u32 s91, s91, s90\n\t"
"s_lshr_b32 s60, s60, 1\n\t"
"s_mul_i32 s90, s60, 96\n\t"
"s_add_u32 s91, s91, s90\n\t"
"s_lshl_b32 s57, s91, 10\n\t"
"s_and_b32 s91, s69, 7\n\t"
"s_lshl_b32 s91, s91, 7\n\t"
"s_add_u32 s57, s57, s91\n\t"
"s_add_u32 s60, s56, 0\n\t"
"buffer_load_short_d16_hi v152, v161, s[52:55], s60 offen\n\t"
"s_add_u32 s60, s56, 73728\n\t"
"buffer_load_short_d16_hi v153, v161, s[52:55], s60 offen\n\t"
"s_add_u32 s60, s56, 147456\n\t"
"buffer_load_short_d16_hi v234, v161, s[52:55], s60 offen\n\t"
"s_add_u32 s60, s56, 221184\n\t"
"buffer_load_short_d16_hi v233, v161, s[52:55], s60 offen\n\t"
"s_add_u32 s60, s56, 294912\n\t"
"buffer_load_short_d16_hi v232, v161, s[52:55], s60 offen\n\t"
"s_add_u32 s60, s56, 368640\n\t"
"buffer_load_short_d16_hi v231, v161, s[52:55], s60 offen\n\t"
"s_add_u32 s60, s56, 442368\n\t"
"buffer_load_short_d16_hi v230, v161, s[52:55], s60 offen\n\t"
"s_add_u32 s60, s56, 516096\n\t"
"buffer_load_short_d16_hi v159, v161, s[52:55], s60 offen\n\t"
"s_add_u32 s60, s56, 589824\n\t"
"buffer_load_short_d16_hi v160, v161, s[52:55], s60 offen\n\t"
"s_waitcnt vmcnt(2)\n\t"
"v_add_f32 v168, v152, v230\n\t"
"s_waitcnt vmcnt(1)\n\t"
"v_add_f32 v169, v153, v159\n\t"
"s_waitcnt vmcnt(0)\n\t"
"v_add_f32 v170, v234, v160\n\t"
"v_add_f32 v171, v168, v233\n\t"
"v_sub_f32 v174, v168, v233\n\t"
"v_add_f32 v172, v169, v232\n\t"
"v_sub_f32 v175, v169, v232\n\t"
"v_add_f32 v173, v170, v231\n\t"
"v_sub_f32 v228, v170, v231\n\t"
"v_add_f32 v177, v152, v234\n\t"
"v_add_f32 v178, v171, v173\n\t"
"v_add_f32 v179, v174, v228\n\t"
"v_add_f32 v180, v230, v160\n\t"
"v_add_f32 v182, v177, v153\n\t"
"v_sub_f32 v183, v177, v153\n\t"
"v_add_f32 v184, v178, v172\n\t"
"v_sub_f32 v185, v178, v172\n\t"
"v_add_f32 v186, v179, v175\n\t"
"v_sub_f32 v187, v179, v175\n\t"
"v_add_f32 v188, v180, v159\n\t"
"v_sub_f32 v189, v180, v159\n\t"
"v_mul_f32 v191, 0.5, v182\n\t"
"v_mul_f32 v225, 0.5, v183\n\t"
"v_mul_f32 v222, 0.5, v171\n\t"
"v_mul_f32 v221, 0x3e800000, v184\n\t"
"v_mul_f32 v196, 0x3e800000, v185\n\t"
"v_mul_f32 v197, -0.5, v173\n\t"
"v_mul_f32 v199, 0.5, v186\n\t"
"v_mul_f32 v200, 0.5, v187\n\t"
"v_mul_f32 v203, -0.5, v188\n\t"
"v_mul_f32 v204, -0.5, v189\n\t"
"v_cvt_pk_bf16_f32 v206, v152, v191\n\t"
"v_cvt_pk_bf16_f32 v207, v225, -v234\n\t"
"v_cvt_pk_bf16_f32 v208, v222, v221\n\t"
"v_cvt_pk_bf16_f32 v209, v196, v197\n\t"
"v_cvt_pk_bf16_f32 v210, v174, v199\n\t"
"v_cvt_pk_bf16_f32 v211, v200, -v228\n\t"
"v_cvt_pk_bf16_f32 v212, -v230, v203\n\t"
"v_cvt_pk_bf16_f32 v213, v204, v160\n\t"
"s_add_u32 s60, s57, 0\n\t"
"buffer_store_short v206, v162, s[8:11], s60 offen sc1\n\t"
"s_add_u32 s60, s57, 6144\n\t"
"buffer_store_short_d16_hi v206, v162, s[8:11], s60 offen sc1\n\t"
"s_add_u32 s60, s57, 12288\n\t"
"buffer_store_short v207, v162, s[8:11], s60 offen sc1\n\t"
"s_add_u32 s60, s57, 18432\n\t"
"buffer_store_short_d16_hi v207, v162, s[8:11], s60 offen sc1\n\t"
"s_add_u32 s60, s57, 24576\n\t"
"buffer_store_short v208, v162, s[8:11], s60 offen sc1\n\t"
"s_add_u32 s60, s57, 30720\n\t"
"buffer_store_short_d16_hi v208, v162, s[8:11], s60 offen sc1\n\t"
"s_add_u32 s60, s57, 36864\n\t"
"buffer_store_short v209, v162, s[8:11], s60 offen sc1\n\t"
"s_add_u32 s60, s57, 43008\n\t"
"buffer_store_short_d16_hi v209, v162, s[8:11], s60 offen sc1\n\t"
"s_add_u32 s60, s57, 49152\n\t"
"buffer_store_short v210, v162, s[8:11], s60 offen sc1\n\t"
"s_add_u32 s60, s57, 55296\n\t"
"buffer_store_short_d16_hi v210, v162, s[8:11], s60 offen sc1\n\t"
"s_add_u32 s60, s57, 61440\n\t"
"buffer_store_short v211, v162, s[8:11], s60 offen sc1\n\t"
"s_add_u32 s60, s57, 67584\n\t"
"buffer_store_short_d16_hi v211, v162, s[8:11], s60 offen sc1\n\t"
"s_add_u32 s60, s57, 73728\n\t"
"buffer_store_short v212, v162, s[8:11], s60 offen sc1\n\t"
"s_add_u32 s60, s57, 79872\n\t"
"buffer_store_short_d16_hi v212, v162, s[8:11], s60 offen sc1\n\t"
"s_add_u32 s60, s57, 86016\n\t"
"buffer_store_short v213, v162, s[8:11], s60 offen sc1\n\t"
"s_add_u32 s60, s57, 92160\n\t"
"buffer_store_short_d16_hi v213, v162, s[8:11], s60 offen sc1\n\t"
"s_waitcnt vmcnt(0)\n\t"
"s_add_u32 s69, s69, 1\n\t"
"s_cmp_lt_u32 s69, 192\n\t"
"s_cbranch_scc1 Lfb1_%=\n\t"
"s_add_u32 s67, s67, 1\n\t"
"s_cmp_lt_u32 s67, 3\n\t"
"s_cbranch_scc1 Lfb0_%=\n\t"
"s_branch Lgo_%=\n\t"
".p2align 6\n\t.fill 448, 4, 0xbf800000\n\t"
"Lpad10_%=:\n\t"
"s_cbranch_execz Lret1_%=\n\t"
"s_nop 0\n\t"
"Ltail_%=:\n\t"
"v_mfma_f32_16x16x32_bf16 v[104:107], a[72:75], a[152:155], v[104:107]\n\t"
"s_nop 0\n\t"
"ds_read_b128 a[144:147], v224 offset:17568\n\t"
"ds_read_b128 a[160:163], v224 offset:19616\n\t"
"v_mfma_f32_16x16x32_bf16 v[104:107], a[76:79], a[168:171], v[104:107]\n\t"
"buffer_load_dwordx4 a[20:23], off, s[28:31], s69 offset:1024 sc1\n\t"
"v_mfma_f32_16x16x32_bf16 v[120:123], a[80:83], a[152:155], v[120:123]\n\t"
"ds_read_b128 a[148:151], v224 offset:17824\n\t"
"v_add_u32 v248, 33280, v226\n\t"
"v_mfma_f32_16x16x32_bf16 v[120:123], a[84:87], a[168:171], v[120:123]\n\t"
"ds_read_b128 a[164:167], v224 offset:19872\n\t"
"v_xor_b32 v249, 32, v248\n\t"
"v_mfma_f32_16x16x32_bf16 v[136:139], a[88:91], a[152:155], v[136:139]\n\t"
"ds_read_b128 a[152:155], v224 offset:18080\n\t"
"v_mfma_f32_16x16x32_bf16 v[136:139], a[92:95], a[168:171], v[136:139]\n\t"
"buffer_load_dwordx4 a[12:15], off, s[84:87], s69 offset:3072 sc1\n\t"
"v_mfma_f32_16x16x32_bf16 v[140:143], a[88:91], a[156:159], v[140:143]\n\t"
"ds_read_b128 v[8:11], v229 offset:20480\n\t"
"v_mfma_f32_16x16x32_bf16 v[140:143], a[92:95], a[172:175], v[140:143]\n\t"
"buffer_load_dwordx4 a[8:11], off, s[84:87], s69 offset:2048 sc1\n\t"
"v_mfma_f32_16x16x32_bf16 v[124:127], a[80:83], a[156:159], v[124:127]\n\t"
"ds_read_b128 a[168:171], v224 offset:20128\n\t"
"v_xor_b32 v250, 64, v248\n\t"
"v_mfma_f32_16x16x32_bf16 v[124:127], a[84:87], a[172:175], v[124:127]\n\t"
"buffer_load_dwordx4 a[0:3], off, s[84:87], s69 offset:0 sc1\n\t"
"v_mfma_f32_16x16x32_bf16 v[108:111], a[72:75], a[156:159], v[108:111]\n\t"
"ds_read_b128 a[156:159], v224 offset:18336\n\t"
"v_xor_b32 v251, 96, v248\n\t"
"v_mfma_f32_16x16x32_bf16 v[108:111], a[76:79], a[172:175], v[108:111]\n\t"
"buffer_load_dwordx4 a[4:7], off, s[84:87], s69 offset:1024 sc1\n\t"
"v_mfma_f32_16x16x32_bf16 v[192:195], a[224:227], a[176:179], 0\n\t"
"s_nop 0\n\t"
"ds_read_b64_tr_b16 a[178:179], v248 offset:20864\n\t"
"ds_read_b64_tr_b16 a[176:177], v248 offset:20736\n\t"
"v_mfma_f32_16x16x32_bf16 v[196:199], a[224:227], a[180:183], 0\n\t"
"ds_read_b64_tr_b16 a[180:181], v249 offset:20992\n\t"
"v_mfma_f32_16x16x32_bf16 v[200:203], a[224:227], a[184:187], 0\n\t"
"s_nop 0\n\t"
"ds_read_b64_tr_b16 a[186:187], v249 offset:20864\n\t"
"ds_read_b64_tr_b16 a[184:185], v249 offset:20736\n\t"
"v_mfma_f32_16x16x32_bf16 v[204:207], a[224:227], a[188:191], 0\n\t"
"s_waitcnt lgkmcnt(8)\n\t"
"s_waitcnt lgkmcnt(7)\n\t"
"buffer_store_dwordx4 v[8:11], v232, s[16:19], s24 offen nt\n\t"
"v_mfma_f32_16x16x32_bf16 v[208:211], a[224:227], a[192:195], 0\n\t"
"ds_read_b128 v[24:27], v230 offset:20480\n\t"
"v_mfma_f32_16x16x32_bf16 v[212:215], a[224:227], a[196:199], 0\n\t"
"ds_read_b64_tr_b16 a[188:189], v248 offset:20992\n\t"
"v_mfma_f32_16x16x32_bf16 v[216:219], a[224:227], a[200:203], 0\n\t"
"s_nop 0\n\t"
"ds_read_b64_tr_b16 a[194:195], v250 offset:20864\n\t"
"ds_read_b64_tr_b16 a[192:193], v250 offset:20736\n\t"
"v_mfma_f32_16x16x32_bf16 v[220:223], a[224:227], a[204:207], 0\n\t"
"s_waitcnt vmcnt(18) lgkmcnt(10)\n\t"
"buffer_load_dwordx4 a[40:43], off, s[28:31], s54 offset:0 sc1\n\t"
"v_mfma_f32_16x16x32_bf16 v[144:147], a[96:99], a[144:147], v[144:147]\n\t"
"ds_read_b64_tr_b16 a[196:197], v251 offset:20992\n\t"
"v_cvt_pk_bf16_f32 v195, v195, v199\n\t"
"v_mfma_f32_16x16x32_bf16 v[144:147], a[100:103], a[160:163], v[144:147]\n\t"
"ds_read_b128 a[172:175], v224 offset:20384\n\t"
"v_cvt_pk_bf16_f32 v194, v194, v198\n\t"
"v_mfma_f32_16x16x32_bf16 v[160:163], a[104:107], a[144:147], v[160:163]\n\t"
"ds_read_b64_tr_b16 a[202:203], v251 offset:20864\n\t"
"v_cvt_pk_bf16_f32 v200, v200, v204\n\t"
"v_mfma_f32_16x16x32_bf16 v[160:163], a[108:111], a[160:163], v[160:163]\n\t"
"buffer_load_dwordx4 a[44:47], off, s[28:31], s54 offset:1024 sc1\n\t"
"v_mfma_f32_16x16x32_bf16 v[176:179], a[112:115], a[144:147], v[176:179]\n\t"
"ds_read_b128 v[12:15], v229 offset:33792\n\t"
"v_cvt_pk_bf16_f32 v209, v209, v213\n\t"
"v_mfma_f32_16x16x32_bf16 v[176:179], a[116:119], a[160:163], v[176:179]\n\t"
"ds_read_b128 a[144:147], v224 offset:16544\n\t"
"v_cvt_pk_bf16_f32 v193, v193, v197\n\t"
"v_mfma_f32_16x16x32_bf16 v[180:183], a[112:115], a[148:151], v[180:183]\n\t"
"ds_read_b128 a[160:163], v224 offset:18592\n\t"
"v_cvt_pk_bf16_f32 v216, v216, v220\n\t"
"v_mfma_f32_16x16x32_bf16 v[180:183], a[116:119], a[164:167], v[180:183]\n\t"
"s_waitcnt lgkmcnt(8)\n\t"
"buffer_store_dwordx4 v[24:27], v233, s[16:19], s24 offen nt\n\t"
"v_mfma_f32_16x16x32_bf16 v[164:167], a[104:107], a[148:151], v[164:167]\n\t"
"ds_write_addtid_b32 v200 offset:2048\n\t"
"v_cvt_pk_bf16_f32 v217, v217, v221\n\t"
"v_mfma_f32_16x16x32_bf16 v[164:167], a[108:111], a[164:167], v[164:167]\n\t"
"s_nop 0\n\t"
"ds_write_addtid_b32 v193 offset:8320\n\t"
"ds_write_addtid_b32 v209 offset:9344\n\t"
"v_mfma_f32_16x16x32_bf16 v[148:151], a[96:99], a[148:151], v[148:151]\n\t"
"ds_read_b128 v[40:43], v231 offset:20480\n\t"
"v_cvt_pk_bf16_f32 v201, v201, v205\n\t"
"v_mfma_f32_16x16x32_bf16 v[148:151], a[100:103], a[164:167], v[148:151]\n\t"
"buffer_load_dwordx4 a[36:39], off, s[84:87], s54 offset:3072 sc1\n\t"
"v_mfma_f32_16x16x32_bf16 v[152:155], a[96:99], a[152:155], v[152:155]\n\t"
"ds_read_b128 v[28:31], v230 offset:33792\n\t"
"v_cvt_pk_bf16_f32 v192, v192, v196\n\t"
"v_mfma_f32_16x16x32_bf16 v[152:155], a[100:103], a[168:171], v[152:155]\n\t"
"ds_read_b128 a[148:151], v224 offset:16800\n\t"
"v_cvt_pk_bf16_f32 v210, v210, v214\n\t"
"v_mfma_f32_16x16x32_bf16 v[168:171], a[104:107], a[152:155], v[168:171]\n\t"
"ds_read_b128 a[164:167], v224 offset:18848\n\t"
"v_cvt_pk_bf16_f32 v219, v219, v223\n\t"
"v_mfma_f32_16x16x32_bf16 v[168:171], a[108:111], a[168:171], v[168:171]\n\t"
"buffer_load_dwordx4 a[32:35], off, s[84:87], s54 offset:2048 sc1\n\t"
"v_mfma_f32_16x16x32_bf16 v[184:187], a[112:115], a[152:155], v[184:187]\n\t"
"ds_read_b128 a[152:155], v224 offset:17056\n\t"
"v_cvt_pk_bf16_f32 v202, v202, v206\n\t"
"v_mfma_f32_16x16x32_bf16 v[184:187], a[116:119], a[168:171], v[184:187]\n\t"
"s_waitcnt lgkmcnt(8)\n\t"
"ds_read_b128 v[44:47], v231 offset:33792\n\t"
"v_cvt_pk_bf16_f32 v208, v208, v212\n\t"
"v_mfma_f32_16x16x32_bf16 v[188:191], a[112:115], a[156:159], v[188:191]\n\t"
"ds_write_addtid_b32 v195 offset:12448\n\t"
"v_cvt_pk_bf16_f32 v203, v203, v207\n\t"
"v_mfma_f32_16x16x32_bf16 v[188:191], a[116:119], a[172:175], v[188:191]\n\t"
"ds_write_addtid_b32 v194 offset:4128\n\t"
"v_cvt_pk_bf16_f32 v211, v211, v215\n\t"
"v_mfma_f32_16x16x32_bf16 v[172:175], a[104:107], a[156:159], v[172:175]\n\t"
"ds_write_addtid_b32 v201 offset:10368\n\t"
"v_cvt_pk_bf16_f32 v218, v218, v222\n\t"
"v_mfma_f32_16x16x32_bf16 v[172:175], a[108:111], a[172:175], v[172:175]\n\t"
"s_nop 0\n\t"
"ds_write_addtid_b32 v218 offset:7200\n\t"
"ds_write_addtid_b32 v219 offset:15520\n\t"
"v_mfma_f32_16x16x32_bf16 v[156:159], a[96:99], a[156:159], v[156:159]\n\t"
"s_nop 0\n\t"
"ds_write_addtid_b32 v202 offset:6176\n\t"
"ds_write_addtid_b32 v210 offset:5152\n\t"
"v_mfma_f32_16x16x32_bf16 v[156:159], a[100:103], a[172:175], v[156:159]\n\t"
"s_waitcnt vmcnt(12) lgkmcnt(10)\n\t"
"buffer_store_dwordx4 v[40:43], v234, s[16:19], s24 offen nt\n\t"
"v_mfma_f32_16x16x32_bf16 v[96:99], a[120:123], a[144:147], v[96:99]\n\t"
"s_nop 0\n\t"
"ds_write_addtid_b32 v192 offset:0\n\t"
"ds_write_addtid_b32 v211 offset:13472\n\t"
"v_mfma_f32_16x16x32_bf16 v[96:99], a[124:127], a[160:163], v[96:99]\n\t"
"s_nop 0\n\t"
"ds_write_addtid_b32 v203 offset:14496\n\t"
"ds_write_addtid_b32 v216 offset:3072\n\t"
"v_mfma_f32_16x16x32_bf16 v[112:115], a[128:131], a[144:147], v[112:115]\n\t"
"s_nop 0\n\t"
"ds_write_addtid_b32 v208 offset:1024\n\t"
"ds_write_addtid_b32 v217 offset:11392\n\t"
"v_mfma_f32_16x16x32_bf16 v[112:115], a[132:135], a[160:163], v[112:115]\n\t"
"buffer_load_dwordx4 a[24:27], off, s[84:87], s54 offset:0 sc1\n\t"
"v_mfma_f32_16x16x32_bf16 v[128:131], a[136:139], a[144:147], v[128:131]\n\t"
"ds_read_b128 a[172:175], v224 offset:19360\n\t"
"v_mfma_f32_16x16x32_bf16 v[128:131], a[140:143], a[160:163], v[128:131]\n\t"
"s_waitcnt lgkmcnt(12)\n\t"
"ds_read_b128 a[168:171], v224 offset:19104\n\t"
"v_mfma_f32_16x16x32_bf16 v[132:135], a[136:139], a[148:151], v[132:135]\n\t"
"ds_read_b128 a[156:159], v224 offset:17312\n\t"
"v_mfma_f32_16x16x32_bf16 v[132:135], a[140:143], a[164:167], v[132:135]\n\t"
"buffer_load_dwordx4 a[28:31], off, s[84:87], s54 offset:1024 sc1\n\t"
"v_mfma_f32_16x16x32_bf16 v[116:119], a[128:131], a[148:151], v[116:119]\n\t"
"s_nop 0\n\t"
"ds_read_b64_tr_b16 a[200:201], v251 offset:20736\n\t"
"ds_read_b64_tr_b16 a[204:205], v250 offset:20992\n\t"
"v_mfma_f32_16x16x32_bf16 v[116:119], a[132:135], a[164:167], v[116:119]\n\t"
"v_mfma_f32_16x16x32_bf16 v[100:103], a[120:123], a[148:151], v[100:103]\n\t"
"buffer_load_dwordx4 a[64:67], off, s[28:31], s57 offset:0 sc1\n\t"
"v_mfma_f32_16x16x32_bf16 v[100:103], a[124:127], a[164:167], v[100:103]\n\t"
"s_waitcnt lgkmcnt(0)\n\t"
"s_barrier\n\t"
"v_mfma_f32_16x16x32_bf16 v[104:107], a[120:123], a[152:155], v[104:107]\n\t"
"s_nop 0\n\t"
"ds_read_b128 a[144:147], v224 offset:0\n\t"
"ds_read_b128 a[148:151], v224 offset:256\n\t"
"v_mfma_f32_16x16x32_bf16 v[104:107], a[124:127], a[168:171], v[104:107]\n\t"
"buffer_store_dwordx4 v[12:15], v232, s[16:19], s27 offen nt\n\t"
"v_mfma_f32_16x16x32_bf16 v[120:123], a[128:131], a[152:155], v[120:123]\n\t"
"ds_read_b128 a[160:163], v224 offset:2048\n\t"
"v_mfma_f32_16x16x32_bf16 v[120:123], a[132:135], a[168:171], v[120:123]\n\t"
"ds_read_b128 a[164:167], v224 offset:2304\n\t"
"v_mfma_f32_16x16x32_bf16 v[136:139], a[136:139], a[152:155], v[136:139]\n\t"
"v_mfma_f32_16x16x32_bf16 v[136:139], a[140:143], a[168:171], v[136:139]\n\t"
"buffer_load_dwordx4 a[68:71], off, s[28:31], s57 offset:1024 sc1\n\t"
"v_mfma_f32_16x16x32_bf16 v[140:143], a[136:139], a[156:159], v[140:143]\n\t"
"ds_read_b128 a[168:171], v224 offset:2560\n\t"
"v_mfma_f32_16x16x32_bf16 v[140:143], a[140:143], a[172:175], v[140:143]\n\t"
"ds_read_b128 a[152:155], v224 offset:512\n\t"
"v_mfma_f32_16x16x32_bf16 v[124:127], a[128:131], a[156:159], v[124:127]\n\t"
"v_mfma_f32_16x16x32_bf16 v[124:127], a[132:135], a[172:175], v[124:127]\n\t"
"v_mfma_f32_16x16x32_bf16 v[108:111], a[120:123], a[156:159], v[108:111]\n\t"
"ds_read_b128 a[156:159], v224 offset:768\n\t"
"v_mfma_f32_16x16x32_bf16 v[108:111], a[124:127], a[172:175], v[108:111]\n\t"
"ds_read_b128 a[172:175], v224 offset:2816\n\t"
"buffer_load_dwordx4 a[60:63], off, s[84:87], s57 offset:3072 sc1\n\t"
"v_mfma_f32_4x4x4_16b_bf16 v[200:203], a[220:221], a[178:179], 0\n\t"
"ds_read_b64_tr_b16 a[178:179], v248 offset:128\n\t"
"v_mfma_f32_4x4x4_16b_bf16 v[204:207], a[220:221], a[186:187], 0\n\t"
"ds_read_b64_tr_b16 a[186:187], v249 offset:128\n\t"
"v_mfma_f32_4x4x4_16b_bf16 v[216:219], a[220:221], a[194:195], 0\n\t"
"ds_read_b64_tr_b16 a[194:195], v250 offset:128\n\t"
"v_mfma_f32_4x4x4_16b_bf16 v[220:223], a[220:221], a[202:203], 0\n\t"
"ds_read_b64_tr_b16 a[202:203], v251 offset:128\n\t"
"buffer_load_dwordx4 a[56:59], off, s[84:87], s57 offset:2048 sc1\n\t"
"v_mfma_f32_4x4x4_16b_bf16 v[192:195], a[222:223], a[176:177], v[200:203]\n\t"
"ds_read_b64_tr_b16 a[176:177], v248 offset:0\n\t"
"v_mfma_f32_4x4x4_16b_bf16 v[196:199], a[222:223], a[184:185], v[204:207]\n\t"
"ds_read_b64_tr_b16 a[184:185], v249 offset:0\n\t"
"v_mfma_f32_4x4x4_16b_bf16 v[200:203], a[222:223], a[180:181], v[200:203]\n\t"
".p2align 3\n\t"
"ds_read_b64_tr_b16 a[180:181], v249 offset:256\n\t"
"buffer_store_dwordx4 v[28:31], v233, s[16:19], s27 offen nt\n\t"
"v_pk_add_f32 v[96:97], v[96:97], v[144:145]\n\t"
"v_pk_add_f32 v[98:99], v[98:99], v[146:147]\n\t"
"v_pk_fma_f32 v[144:145], v[144:145], s[38:39], v[96:97]\n\t"
"v_pk_fma_f32 v[146:147], v[146:147], s[38:39], v[98:99]\n\t"
"v_pk_add_f32 v[112:113], v[112:113], v[160:161]\n\t"
"v_pk_add_f32 v[114:115], v[114:115], v[162:163]\n\t"
"v_pk_fma_f32 v[160:161], v[160:161], s[38:39], v[112:113]\n\t"
"v_pk_fma_f32 v[162:163], v[162:163], s[38:39], v[114:115]\n\t"
"v_pk_add_f32 v[128:129], v[128:129], v[176:177]\n\t"
"v_pk_add_f32 v[130:131], v[130:131], v[178:179]\n\t"
"v_pk_fma_f32 v[176:177], v[176:177], s[38:39], v[128:129]\n\t"
"v_pk_fma_f32 v[178:179], v[178:179], s[38:39], v[130:131]\n\t"
"v_pk_add_f32 v[132:133], v[132:133], v[180:181]\n\t"
"v_pk_add_f32 v[134:135], v[134:135], v[182:183]\n\t"
"v_pk_fma_f32 v[180:181], v[180:181], s[38:39], v[132:133]\n\t"
"v_pk_fma_f32 v[182:183], v[182:183], s[38:39], v[134:135]\n\t"
"v_pk_add_f32 v[116:117], v[116:117], v[164:165]\n\t"
"v_pk_add_f32 v[118:119], v[118:119], v[166:167]\n\t"
"v_pk_fma_f32 v[164:165], v[164:165], s[38:39], v[116:117]\n\t"
"v_pk_fma_f32 v[166:167], v[166:167], s[38:39], v[118:119]\n\t"
"v_pk_add_f32 v[100:101], v[100:101], v[148:149]\n\t"
"v_pk_add_f32 v[102:103], v[102:103], v[150:151]\n\t"
"v_pk_fma_f32 v[148:149], v[148:149], s[38:39], v[100:101]\n\t"
"v_pk_fma_f32 v[150:151], v[150:151], s[38:39], v[102:103]\n\t"
"v_pk_add_f32 v[104:105], v[104:105], v[152:153]\n\t"
"v_pk_add_f32 v[106:107], v[106:107], v[154:155]\n\t"
"v_pk_fma_f32 v[152:153], v[152:153], s[38:39], v[104:105]\n\t"
"v_pk_fma_f32 v[154:155], v[154:155], s[38:39], v[106:107]\n\t"
"v_pk_add_f32 v[120:121], v[120:121], v[168:169]\n\t"
"v_pk_add_f32 v[122:123], v[122:123], v[170:171]\n\t"
"v_pk_fma_f32 v[168:169], v[168:169], s[38:39], v[120:121]\n\t"
"v_pk_fma_f32 v[170:171], v[170:171], s[38:39], v[122:123]\n\t"
"v_pk_add_f32 v[136:137], v[136:137], v[184:185]\n\t"
"v_pk_add_f32 v[138:139], v[138:139], v[186:187]\n\t"
"v_pk_fma_f32 v[184:185], v[184:185], s[38:39], v[136:137]\n\t"
"v_pk_fma_f32 v[186:187], v[186:187], s[38:39], v[138:139]\n\t"
"v_pk_add_f32 v[140:141], v[140:141], v[188:189]\n\t"
"v_pk_add_f32 v[142:143], v[142:143], v[190:191]\n\t"
"v_pk_fma_f32 v[188:189], v[188:189], s[38:39], v[140:141]\n\t"
"v_pk_fma_f32 v[190:191], v[190:191], s[38:39], v[142:143]\n\t"
"v_pk_add_f32 v[124:125], v[124:125], v[172:173]\n\t"
"v_pk_add_f32 v[126:127], v[126:127], v[174:175]\n\t"
"v_pk_fma_f32 v[172:173], v[172:173], s[38:39], v[124:125]\n\t"
"v_pk_fma_f32 v[174:175], v[174:175], s[38:39], v[126:127]\n\t"
"v_pk_add_f32 v[108:109], v[108:109], v[156:157]\n\t"
"v_pk_add_f32 v[110:111], v[110:111], v[158:159]\n\t"
"v_pk_fma_f32 v[156:157], v[156:157], s[38:39], v[108:109]\n\t"
"v_pk_fma_f32 v[158:159], v[158:159], s[38:39], v[110:111]\n\t"
"v_cvt_pk_bf16_f32 v116, v116, v117\n\t"
"v_mfma_f32_4x4x4_16b_bf16 v[204:207], a[222:223], a[188:189], v[204:207]\n\t"
"ds_read_b64_tr_b16 a[188:189], v248 offset:256\n\t"
"v_cvt_pk_bf16_f32 v96, v96, v97\n\t"
"v_mfma_f32_4x4x4_16b_bf16 v[208:211], a[222:223], a[192:193], v[216:219]\n\t"
"v_cvt_pk_bf16_f32 v97, v98, v99\n\t"
"v_cvt_pk_bf16_f32 v100, v100, v101\n\t"
"v_cvt_pk_bf16_f32 v101, v102, v103\n\t"
"v_cvt_pk_bf16_f32 v112, v112, v113\n\t"
"v_mfma_f32_4x4x4_16b_bf16 v[212:215], a[222:223], a[200:201], v[220:223]\n\t"
"buffer_load_dwordx4 a[48:51], off, s[84:87], s57 offset:0 sc1\n\t"
"ds_write2st64_b64 v228, v[96:97], v[100:101] offset0:40 offset1:53\n\t"
"v_cvt_pk_bf16_f32 v117, v118, v119\n\t"
"v_mfma_f32_4x4x4_16b_bf16 v[216:219], a[222:223], a[196:197], v[216:219]\n\t"
"v_cvt_pk_bf16_f32 v124, v124, v125\n\t"
"v_cvt_pk_bf16_f32 v125, v126, v127\n\t"
"v_mfma_f32_4x4x4_16b_bf16 v[220:223], a[222:223], a[204:205], v[220:223]\n\t"
"s_waitcnt vmcnt(16) lgkmcnt(7)\n\t"
"v_cvt_pk_bf16_f32 v113, v114, v115\n\t"
"v_cvt_pk_bf16_f32 v140, v140, v141\n\t"
"v_mfma_f32_16x16x32_bf16 v[48:51], a[0:3], a[144:147], v[48:51]\n\t"
"v_cvt_pk_bf16_f32 v192, -v192, -v196\n\t"
"v_cvt_pk_bf16_f32 v120, v120, v121\n\t"
"v_mfma_f32_16x16x32_bf16 v[48:51], a[4:7], a[160:163], v[48:51]\n\t"
"ds_write_addtid_b32 v192 offset:16544\n\t"
"buffer_load_dwordx4 a[52:55], off, s[84:87], s57 offset:1024 sc1\n\t"
"ds_write2st64_b64 v246, v[112:113], v[116:117] offset0:40 offset1:53\n\t"
"v_cvt_pk_bf16_f32 v136, v136, v137\n\t"
"v_mfma_f32_16x16x32_bf16 v[64:67], a[8:11], a[144:147], v[64:67]\n\t"
"v_cvt_pk_bf16_f32 v104, v104, v105\n\t"
"v_cvt_pk_bf16_f32 v108, v108, v109\n\t"
"v_mfma_f32_16x16x32_bf16 v[64:67], a[12:15], a[160:163], v[64:67]\n\t"
"v_cvt_pk_bf16_f32 v128, v128, v129\n\t"
"v_cvt_pk_bf16_f32 v132, v132, v133\n\t"
"v_cvt_pk_bf16_f32 v133, v134, v135\n\t"
"v_mfma_f32_16x16x32_bf16 v[80:83], a[16:19], a[144:147], v[80:83]\n\t"
"v_cvt_pk_bf16_f32 v203, v203, v207\n\t"
"v_cvt_pk_bf16_f32 v129, v130, v131\n\t"
"v_mfma_f32_16x16x32_bf16 v[80:83], a[20:23], a[160:163], v[80:83]\n\t"
"s_nop 0\n\t"
"ds_read_b128 a[144:147], v224 offset:1024\n\t"
"ds_read_b128 a[160:163], v224 offset:3072\n\t"
"ds_write2st64_b64 v247, v[128:129], v[132:133] offset0:40 offset1:53\n\t"
"v_mfma_f32_16x16x32_bf16 v[84:87], a[16:19], a[148:151], v[84:87]\n\t"
"v_cvt_pk_bf16_f32 v105, v106, v107\n\t"
"v_cvt_pk_bf16_f32 v109, v110, v111\n\t"
"v_mfma_f32_16x16x32_bf16 v[84:87], a[20:23], a[164:167], v[84:87]\n\t"
"buffer_load_dwordx4 a[88:91], off, s[28:31], s63 offset:0 sc1\n\t"
"v_mfma_f32_16x16x32_bf16 v[68:71], a[8:11], a[148:151], v[68:71]\n\t"
"v_cvt_pk_bf16_f32 v195, -v195, -v199\n\t"
"v_cvt_pk_bf16_f32 v217, v217, v221\n\t"
"v_mfma_f32_16x16x32_bf16 v[68:71], a[12:15], a[164:167], v[68:71]\n\t"
"ds_write_addtid_b32 v203 offset:30016\n\t"
"ds_write2st64_b64 v228, v[104:105], v[108:109] offset0:66 offset1:79\n\t"
"v_mfma_f32_16x16x32_bf16 v[52:55], a[0:3], a[148:151], v[52:55]\n\t"
"v_cvt_pk_bf16_f32 v137, v138, v139\n\t"
"v_cvt_pk_bf16_f32 v141, v142, v143\n\t"
"v_mfma_f32_16x16x32_bf16 v[52:55], a[4:7], a[164:167], v[52:55]\n\t"
"buffer_store_dwordx4 v[44:47], v234, s[16:19], s27 offen nt\n\t"
"v_mfma_f32_16x16x32_bf16 v[56:59], a[0:3], a[152:155], v[56:59]\n\t"
"v_cvt_pk_bf16_f32 v211, -v211, -v215\n\t"
"v_cvt_pk_bf16_f32 v121, v122, v123\n\t"
"v_mfma_f32_16x16x32_bf16 v[56:59], a[4:7], a[168:171], v[56:59]\n\t"
"s_nop 0\n\t"
"ds_read_b128 a[148:151], v224 offset:1280\n\t"
"ds_read_b128 a[164:167], v224 offset:3328\n\t"
"ds_write2st64_b64 v247, v[136:137], v[140:141] offset0:66 offset1:79\n\t"
"v_mfma_f32_16x16x32_bf16 v[72:75], a[8:11], a[152:155], v[72:75]\n\t"
"v_cvt_pk_bf16_f32 v208, -v208, -v212\n\t"
"v_cvt_pk_bf16_f32 v210, -v210, -v214\n\t"
"v_mfma_f32_16x16x32_bf16 v[72:75], a[12:15], a[168:171], v[72:75]\n\t"
"buffer_load_dwordx4 a[92:95], off, s[28:31], s63 offset:1024 sc1\n\t"
"v_mfma_f32_16x16x32_bf16 v[88:91], a[16:19], a[152:155], v[88:91]\n\t"
"v_cvt_pk_bf16_f32 v200, v200, v204\n\t"
"v_cvt_pk_bf16_f32 v202, v202, v206\n\t"
"v_mfma_f32_16x16x32_bf16 v[88:91], a[20:23], a[168:171], v[88:91]\n\t"
"ds_write_addtid_b32 v208 offset:18592\n\t"
"ds_write2st64_b64 v246, v[120:121], v[124:125] offset0:66 offset1:79\n\t"
"v_mfma_f32_16x16x32_bf16 v[92:95], a[16:19], a[156:159], v[92:95]\n\t"
"v_cvt_pk_bf16_f32 v193, -v193, -v197\n\t"
"v_cvt_pk_bf16_f32 v201, v201, v205\n\t"
"v_mfma_f32_16x16x32_bf16 v[92:95], a[20:23], a[172:175], v[92:95]\n\t"
"buffer_load_dwordx4 a[84:87], off, s[84:87], s63 offset:3072 sc1\n\t"
"v_mfma_f32_16x16x32_bf16 v[76:79], a[8:11], a[156:159], v[76:79]\n\t"
"v_cvt_pk_bf16_f32 v216, v216, v220\n\t"
"v_cvt_pk_bf16_f32 v219, v219, v223\n\t"
"v_mfma_f32_16x16x32_bf16 v[76:79], a[12:15], a[172:175], v[76:79]\n\t"
"s_nop 0\n\t"
"ds_write_addtid_b32 v193 offset:20672\n\t"
"ds_write_addtid_b32 v210 offset:19616\n\t"
"ds_write_addtid_b32 v211 offset:23744\n\t"
"ds_write_addtid_b32 v216 offset:26912\n\t"
"ds_write_addtid_b32 v219 offset:32064\n\t"
"v_mfma_f32_16x16x32_bf16 v[60:63], a[0:3], a[156:159], v[60:63]\n\t"
"s_nop 0\n\t"
"ds_write_addtid_b32 v217 offset:31040\n\t"
"v_cvt_pk_bf16_f32 v194, -v194, -v198\n\t"
"v_mfma_f32_16x16x32_bf16 v[60:63], a[4:7], a[172:175], v[60:63]\n\t"
"s_waitcnt vmcnt(12) lgkmcnt(8)\n\t"
"ds_write_addtid_b32 v201 offset:28992\n\t"
"v_cvt_pk_bf16_f32 v218, v218, v222\n\t"
"v_mfma_f32_16x16x32_bf16 v[48:51], a[24:27], a[144:147], v[48:51]\n\t"
"s_nop 0\n\t"
"ds_write_addtid_b32 v202 offset:25888\n\t"
"v_cvt_pk_bf16_f32 v209, -v209, -v213\n\t"
"v_mfma_f32_16x16x32_bf16 v[48:51], a[28:31], a[160:163], v[48:51]\n\t"
"s_nop 0\n\t"
"ds_write_addtid_b32 v194 offset:17568\n\t"
"ds_write_addtid_b32 v209 offset:22720\n\t"
"ds_write_addtid_b32 v218 offset:27936\n\t"
"v_mfma_f32_16x16x32_bf16 v[64:67], a[32:35], a[144:147], v[64:67]\n\t"
"s_nop 0\n\t"
"ds_write_addtid_b32 v195 offset:21696\n\t"
"ds_write_addtid_b32 v200 offset:24864\n\t"
"v_mfma_f32_16x16x32_bf16 v[64:67], a[36:39], a[160:163], v[64:67]\n\t"
"buffer_load_dwordx4 a[80:83], off, s[84:87], s63 offset:2048 sc1\n\t"
"v_mfma_f32_16x16x32_bf16 v[80:83], a[40:43], a[144:147], v[80:83]\n\t"
"s_nop 0\n\t"
"ds_read_b128 a[156:159], v224 offset:1792\n\t"
"ds_read_b128 a[172:175], v224 offset:3840\n\t"
"v_mfma_f32_16x16x32_bf16 v[80:83], a[44:47], a[160:163], v[80:83]\n\t"
"buffer_load_dwordx4 a[72:75], off, s[84:87], s63 offset:0 sc1\n\t"
"v_mfma_f32_16x16x32_bf16 v[84:87], a[40:43], a[148:151], v[84:87]\n\t"
"s_nop 0\n\t"
"ds_read_b128 a[152:155], v224 offset:1536\n\t"
"ds_read_b128 a[168:171], v224 offset:3584\n\t"
"v_mfma_f32_16x16x32_bf16 v[84:87], a[44:47], a[164:167], v[84:87]\n\t"
"s_nop 0\n\t"
"ds_read_b64_tr_b16 a[192:193], v250 offset:0\n\t"
"ds_read_b64_tr_b16 a[200:201], v251 offset:0\n\t"
"v_mfma_f32_16x16x32_bf16 v[68:71], a[32:35], a[148:151], v[68:71]\n\t"
"s_nop 0\n\t"
"ds_read_b64_tr_b16 a[196:197], v251 offset:256\n\t"
"ds_read_b64_tr_b16 a[204:205], v250 offset:256\n\t"
"v_mfma_f32_16x16x32_bf16 v[68:71], a[36:39], a[164:167], v[68:71]\n\t"
"Lpad11_%=:\n\t"
"s_cbranch_execz Lret1_%=\n\t"
"v_mfma_f32_16x16x32_bf16 v[52:55], a[24:27], a[148:151], v[52:55]\n\t"
"buffer_load_dwordx4 a[76:79], off, s[84:87], s63 offset:1024 sc1\n\t"
"v_mfma_f32_16x16x32_bf16 v[52:55], a[28:31], a[164:167], v[52:55]\n\t"
"s_waitcnt lgkmcnt(0)\n\t"
"s_barrier\n\t"
"v_mfma_f32_16x16x32_bf16 v[56:59], a[24:27], a[152:155], v[56:59]\n\t"
"ds_read_b128 a[144:147], v224 offset:16544\n\t"
"v_mfma_f32_16x16x32_bf16 v[56:59], a[28:31], a[168:171], v[56:59]\n\t"
"ds_read_b128 a[160:163], v224 offset:18592\n\t"
"v_mfma_f32_16x16x32_bf16 v[72:75], a[32:35], a[152:155], v[72:75]\n\t"
"ds_read_b128 a[148:151], v224 offset:16800\n\t"
"v_mfma_f32_16x16x32_bf16 v[72:75], a[36:39], a[168:171], v[72:75]\n\t"
"ds_read_b128 a[164:167], v224 offset:18848\n\t"
"v_mfma_f32_16x16x32_bf16 v[88:91], a[40:43], a[152:155], v[88:91]\n\t"
"v_mfma_f32_16x16x32_bf16 v[88:91], a[44:47], a[168:171], v[88:91]\n\t"
"buffer_load_dwordx4 a[112:115], off, s[28:31], s42 offset:0 sc1\n\t"
"v_mfma_f32_16x16x32_bf16 v[92:95], a[40:43], a[156:159], v[92:95]\n\t"
"ds_read_b128 a[168:171], v224 offset:19104\n\t"
"v_mfma_f32_16x16x32_bf16 v[92:95], a[44:47], a[172:175], v[92:95]\n\t"
"ds_read_b128 v[104:107], v229 offset:20480\n\t"
"v_mfma_f32_16x16x32_bf16 v[76:79], a[32:35], a[156:159], v[76:79]\n\t"
"v_mfma_f32_16x16x32_bf16 v[76:79], a[36:39], a[172:175], v[76:79]\n\t"
"ds_read_b128 a[152:155], v224 offset:17056\n\t"
"v_mfma_f32_16x16x32_bf16 v[60:63], a[24:27], a[156:159], v[60:63]\n\t"
"ds_read_b128 a[156:159], v224 offset:17312\n\t"
"v_mfma_f32_16x16x32_bf16 v[60:63], a[28:31], a[172:175], v[60:63]\n\t"
"buffer_load_dwordx4 a[116:119], off, s[28:31], s42 offset:1024 sc1\n\t"
"v_mfma_f32_4x4x4_16b_bf16 v[200:203], a[220:221], a[178:179], 0\n\t"
"ds_read_b64_tr_b16 a[178:179], v248 offset:41600\n\t"
"v_mfma_f32_4x4x4_16b_bf16 v[204:207], a[220:221], a[186:187], 0\n\t"
"ds_read_b64_tr_b16 a[186:187], v249 offset:41600\n\t"
"v_mfma_f32_4x4x4_16b_bf16 v[216:219], a[220:221], a[194:195], 0\n\t"
"ds_read_b128 a[172:175], v224 offset:19360\n\t"
"v_mfma_f32_4x4x4_16b_bf16 v[220:223], a[220:221], a[202:203], 0\n\t"
"s_waitcnt lgkmcnt(5)\n\t"
".p2align 3\n\t"
"buffer_store_dwordx4 v[104:107], v232, s[16:19], s20 offen nt\n\t"
"v_pk_add_f32 v[144:145], v[48:49], v[144:145] neg_lo:[0,1] neg_hi:[0,1]\n\t"
"v_pk_add_f32 v[146:147], v[50:51], v[146:147] neg_lo:[0,1] neg_hi:[0,1]\n\t"
"v_pk_fma_f32 v[48:49], v[144:145], s[90:91], v[48:49] neg_lo:[1,0,0] neg_hi:[1,0,0]\n\t"
"v_pk_fma_f32 v[50:51], v[146:147], s[90:91], v[50:51] neg_lo:[1,0,0] neg_hi:[1,0,0]\n\t"
"v_pk_add_f32 v[160:161], v[64:65], v[160:161] neg_lo:[0,1] neg_hi:[0,1]\n\t"
"v_pk_add_f32 v[162:163], v[66:67], v[162:163] neg_lo:[0,1] neg_hi:[0,1]\n\t"
"v_pk_fma_f32 v[64:65], v[160:161], s[90:91], v[64:65] neg_lo:[1,0,0] neg_hi:[1,0,0]\n\t"
"v_pk_fma_f32 v[66:67], v[162:163], s[90:91], v[66:67] neg_lo:[1,0,0] neg_hi:[1,0,0]\n\t"
"v_pk_add_f32 v[176:177], v[80:81], v[176:177] neg_lo:[0,1] neg_hi:[0,1]\n\t"
"v_pk_add_f32 v[178:179], v[82:83], v[178:179] neg_lo:[0,1] neg_hi:[0,1]\n\t"
"v_pk_fma_f32 v[80:81], v[176:177], s[90:91], v[80:81] neg_lo:[1,0,0] neg_hi:[1,0,0]\n\t"
"v_pk_fma_f32 v[82:83], v[178:179], s[90:91], v[82:83] neg_lo:[1,0,0] neg_hi:[1,0,0]\n\t"
"v_pk_add_f32 v[180:181], v[84:85], v[180:181] neg_lo:[0,1] neg_hi:[0,1]\n\t"
"v_pk_add_f32 v[182:183], v[86:87], v[182:183] neg_lo:[0,1] neg_hi:[0,1]\n\t"
"v_pk_fma_f32 v[84:85], v[180:181], s[90:91], v[84:85] neg_lo:[1,0,0] neg_hi:[1,0,0]\n\t"
"v_pk_fma_f32 v[86:87], v[182:183], s[90:91], v[86:87] neg_lo:[1,0,0] neg_hi:[1,0,0]\n\t"
"v_pk_add_f32 v[164:165], v[68:69], v[164:165] neg_lo:[0,1] neg_hi:[0,1]\n\t"
"v_pk_add_f32 v[166:167], v[70:71], v[166:167] neg_lo:[0,1] neg_hi:[0,1]\n\t"
"v_pk_fma_f32 v[68:69], v[164:165], s[90:91], v[68:69] neg_lo:[1,0,0] neg_hi:[1,0,0]\n\t"
"v_pk_fma_f32 v[70:71], v[166:167], s[90:91], v[70:71] neg_lo:[1,0,0] neg_hi:[1,0,0]\n\t"
"v_pk_add_f32 v[148:149], v[52:53], v[148:149] neg_lo:[0,1] neg_hi:[0,1]\n\t"
"v_pk_add_f32 v[150:151], v[54:55], v[150:151] neg_lo:[0,1] neg_hi:[0,1]\n\t"
"v_pk_fma_f32 v[52:53], v[148:149], s[90:91], v[52:53] neg_lo:[1,0,0] neg_hi:[1,0,0]\n\t"
"v_pk_fma_f32 v[54:55], v[150:151], s[90:91], v[54:55] neg_lo:[1,0,0] neg_hi:[1,0,0]\n\t"
"v_pk_add_f32 v[152:153], v[56:57], v[152:153] neg_lo:[0,1] neg_hi:[0,1]\n\t"
"v_pk_add_f32 v[154:155], v[58:59], v[154:155] neg_lo:[0,1] neg_hi:[0,1]\n\t"
"v_pk_fma_f32 v[56:57], v[152:153], s[90:91], v[56:57] neg_lo:[1,0,0] neg_hi:[1,0,0]\n\t"
"v_pk_fma_f32 v[58:59], v[154:155], s[90:91], v[58:59] neg_lo:[1,0,0] neg_hi:[1,0,0]\n\t"
"v_pk_add_f32 v[168:169], v[72:73], v[168:169] neg_lo:[0,1] neg_hi:[0,1]\n\t"
"v_pk_add_f32 v[170:171], v[74:75], v[170:171] neg_lo:[0,1] neg_hi:[0,1]\n\t"
"v_pk_fma_f32 v[72:73], v[168:169], s[90:91], v[72:73] neg_lo:[1,0,0] neg_hi:[1,0,0]\n\t"
"v_pk_fma_f32 v[74:75], v[170:171], s[90:91], v[74:75] neg_lo:[1,0,0] neg_hi:[1,0,0]\n\t"
"v_pk_add_f32 v[184:185], v[88:89], v[184:185] neg_lo:[0,1] neg_hi:[0,1]\n\t"
"v_pk_add_f32 v[186:187], v[90:91], v[186:187] neg_lo:[0,1] neg_hi:[0,1]\n\t"
"v_pk_fma_f32 v[88:89], v[184:185], s[90:91], v[88:89] neg_lo:[1,0,0] neg_hi:[1,0,0]\n\t"
"v_pk_fma_f32 v[90:91], v[186:187], s[90:91], v[90:91] neg_lo:[1,0,0] neg_hi:[1,0,0]\n\t"
"v_pk_add_f32 v[188:189], v[92:93], v[188:189] neg_lo:[0,1] neg_hi:[0,1]\n\t"
"v_pk_add_f32 v[190:191], v[94:95], v[190:191] neg_lo:[0,1] neg_hi:[0,1]\n\t"
"v_pk_fma_f32 v[92:93], v[188:189], s[90:91], v[92:93] neg_lo:[1,0,0] neg_hi:[1,0,0]\n\t"
"v_pk_fma_f32 v[94:95], v[190:191], s[90:91], v[94:95] neg_lo:[1,0,0] neg_hi:[1,0,0]\n\t"
"v_pk_add_f32 v[172:173], v[76:77], v[172:173] neg_lo:[0,1] neg_hi:[0,1]\n\t"
"v_pk_add_f32 v[174:175], v[78:79], v[174:175] neg_lo:[0,1] neg_hi:[0,1]\n\t"
"v_pk_fma_f32 v[76:77], v[172:173], s[90:91], v[76:77] neg_lo:[1,0,0] neg_hi:[1,0,0]\n\t"
"v_pk_fma_f32 v[78:79], v[174:175], s[90:91], v[78:79] neg_lo:[1,0,0] neg_hi:[1,0,0]\n\t"
"v_pk_add_f32 v[156:157], v[60:61], v[156:157] neg_lo:[0,1] neg_hi:[0,1]\n\t"
"v_pk_add_f32 v[158:159], v[62:63], v[158:159] neg_lo:[0,1] neg_hi:[0,1]\n\t"
"v_pk_fma_f32 v[60:61], v[156:157], s[90:91], v[60:61] neg_lo:[1,0,0] neg_hi:[1,0,0]\n\t"
"v_pk_fma_f32 v[62:63], v[158:159], s[90:91], v[62:63] neg_lo:[1,0,0] neg_hi:[1,0,0]\n\t"
"v_mfma_f32_4x4x4_16b_bf16 v[192:195], a[222:223], a[176:177], v[200:203]\n\t"
"ds_read_b128 v[120:123], v230 offset:20480\n\t"
"v_mfma_f32_4x4x4_16b_bf16 v[196:199], a[222:223], a[184:185], v[204:207]\n\t"
"ds_read_b64_tr_b16 a[194:195], v250 offset:41600\n\t"
"v_mfma_f32_4x4x4_16b_bf16 v[200:203], a[222:223], a[180:181], v[200:203]\n\t"
"buffer_load_dwordx4 a[108:111], off, s[84:87], s42 offset:3072 sc1\n\t"
"v_mfma_f32_4x4x4_16b_bf16 v[204:207], a[222:223], a[188:189], v[204:207]\n\t"
"ds_read_b64_tr_b16 a[202:203], v251 offset:41600\n\t"
"v_mfma_f32_4x4x4_16b_bf16 v[208:211], a[222:223], a[192:193], v[216:219]\n\t"
"ds_read_b64_tr_b16 a[176:177], v248 offset:41472\n\t"
"v_mfma_f32_4x4x4_16b_bf16 v[212:215], a[222:223], a[200:201], v[220:223]\n\t"
"ds_read_b64_tr_b16 a[184:185], v249 offset:41472\n\t"
"v_mfma_f32_4x4x4_16b_bf16 v[216:219], a[222:223], a[196:197], v[216:219]\n\t"
"ds_read_b64_tr_b16 a[180:181], v249 offset:41728\n\t"
"v_mfma_f32_4x4x4_16b_bf16 v[220:223], a[222:223], a[204:205], v[220:223]\n\t"
"s_waitcnt vmcnt(11) lgkmcnt(7)\n\t"
"buffer_load_dwordx4 a[104:107], off, s[84:87], s42 offset:2048 sc1\n\t"
"v_mfma_f32_16x16x32_bf16 v[48:51], a[48:51], a[144:147], v[48:51]\n\t"
"ds_read_b64_tr_b16 a[188:189], v248 offset:41728\n\t"
"v_cvt_pk_bf16_f32 v195, -v195, -v199\n\t"
"v_mfma_f32_16x16x32_bf16 v[48:51], a[52:55], a[160:163], v[48:51]\n\t"
"buffer_load_dwordx4 a[96:99], off, s[84:87], s42 offset:0 sc1\n\t"
"v_mfma_f32_16x16x32_bf16 v[64:67], a[56:59], a[144:147], v[64:67]\n\t"
"ds_write_addtid_b32 v195 offset:5152\n\t"
"v_cvt_pk_bf16_f32 v194, -v194, -v198\n\t"
"v_mfma_f32_16x16x32_bf16 v[64:67], a[60:63], a[160:163], v[64:67]\n\t"
"ds_write_addtid_b32 v194 offset:1024\n\t"
"v_cvt_pk_bf16_f32 v203, v203, v207\n\t"
"v_mfma_f32_16x16x32_bf16 v[80:83], a[64:67], a[144:147], v[80:83]\n\t"
"ds_read_b128 a[232:235], v230 offset:33792\n\t"
"v_cvt_pk_bf16_f32 v211, -v211, -v215\n\t"
"v_mfma_f32_16x16x32_bf16 v[80:83], a[68:71], a[160:163], v[80:83]\n\t"
"s_waitcnt lgkmcnt(7)\n\t"
"buffer_store_dwordx4 v[120:123], v233, s[16:19], s20 offen nt\n\t"
"v_mfma_f32_16x16x32_bf16 v[84:87], a[64:67], a[148:151], v[84:87]\n\t"
"ds_read_b128 a[144:147], v224 offset:17568\n\t"
"v_cvt_pk_bf16_f32 v201, v201, v205\n\t"
"v_mfma_f32_16x16x32_bf16 v[84:87], a[68:71], a[164:167], v[84:87]\n\t"
"ds_read_b128 v[136:139], v231 offset:20480\n\t"
"v_cvt_pk_bf16_f32 v202, v202, v206\n\t"
"v_mfma_f32_16x16x32_bf16 v[68:71], a[56:59], a[148:151], v[68:71]\n\t"
"ds_read_b128 a[160:163], v224 offset:19616\n\t"
"v_cvt_pk_bf16_f32 v193, -v193, -v197\n\t"
"v_mfma_f32_16x16x32_bf16 v[68:71], a[60:63], a[164:167], v[68:71]\n\t"
"buffer_load_dwordx4 a[100:103], off, s[84:87], s42 offset:1024 sc1\n\t"
"v_mfma_f32_16x16x32_bf16 v[52:55], a[48:51], a[148:151], v[52:55]\n\t"
"ds_read_b128 a[148:151], v224 offset:17824\n\t"
"v_cvt_pk_bf16_f32 v209, -v209, -v213\n\t"
"v_mfma_f32_16x16x32_bf16 v[52:55], a[52:55], a[164:167], v[52:55]\n\t"
"ds_write_addtid_b32 v211 offset:7200\n\t"
"v_cvt_pk_bf16_f32 v200, v200, v204\n\t"
"v_mfma_f32_16x16x32_bf16 v[56:59], a[48:51], a[152:155], v[56:59]\n\t"
"ds_read_b128 a[236:239], v231 offset:33792\n\t"
"v_cvt_pk_bf16_f32 v216, v216, v220\n\t"
"v_mfma_f32_16x16x32_bf16 v[56:59], a[52:55], a[168:171], v[56:59]\n\t"
"buffer_load_dwordx4 a[136:139], off, s[28:31], s48 offset:0 sc1\n\t"
"v_mfma_f32_16x16x32_bf16 v[72:75], a[56:59], a[152:155], v[72:75]\n\t"
"ds_read_b128 a[164:167], v224 offset:19872\n\t"
"v_cvt_pk_bf16_f32 v210, -v210, -v214\n\t"
"v_mfma_f32_16x16x32_bf16 v[72:75], a[60:63], a[168:171], v[72:75]\n\t"
"ds_write_addtid_b32 v193 offset:4128\n\t"
"v_cvt_pk_bf16_f32 v218, v218, v222\n\t"
"v_mfma_f32_16x16x32_bf16 v[88:91], a[64:67], a[152:155], v[88:91]\n\t"
"s_nop 0\n\t"
"ds_write_addtid_b32 v201 offset:12448\n\t"
"ds_write_addtid_b32 v202 offset:9344\n\t"
"v_mfma_f32_16x16x32_bf16 v[88:91], a[68:71], a[168:171], v[88:91]\n\t"
"s_waitcnt lgkmcnt(8)\n\t"
"buffer_store_dwordx4 v[136:139], v234, s[16:19], s20 offen nt\n\t"
"v_mfma_f32_16x16x32_bf16 v[92:95], a[64:67], a[156:159], v[92:95]\n\t"
"ds_read_b128 v[108:111], v229 offset:33792\n\t"
"v_cvt_pk_bf16_f32 v192, -v192, -v196\n\t"
"v_mfma_f32_16x16x32_bf16 v[92:95], a[68:71], a[172:175], v[92:95]\n\t"
"ds_write_addtid_b32 v210 offset:3072\n\t"
"v_cvt_pk_bf16_f32 v219, v219, v223\n\t"
"v_mfma_f32_16x16x32_bf16 v[76:79], a[56:59], a[156:159], v[76:79]\n\t"
"ds_write_addtid_b32 v219 offset:15520\n\t"
"v_cvt_pk_bf16_f32 v208, -v208, -v212\n\t"
"v_mfma_f32_16x16x32_bf16 v[76:79], a[60:63], a[172:175], v[76:79]\n\t"
"ds_write_addtid_b32 v200 offset:8320\n\t"
"buffer_load_dwordx4 a[140:143], off, s[28:31], s48 offset:1024 sc1\n\t"
"v_mfma_f32_16x16x32_bf16 v[60:63], a[48:51], a[156:159], v[60:63]\n\t"
"s_nop 0\n\t"
"ds_write_addtid_b32 v209 offset:6176\n\t"
"ds_write_addtid_b32 v218 offset:11392\n\t"
"v_mfma_f32_16x16x32_bf16 v[60:63], a[52:55], a[172:175], v[60:63]\n\t"
"s_waitcnt vmcnt(11) lgkmcnt(10)\n\t"
"ds_write_addtid_b32 v192 offset:0\n\t"
"ds_write_addtid_b32 v216 offset:10368\n\t"
"v_mfma_f32_16x16x32_bf16 v[144:147], a[72:75], a[144:147], v[144:147]\n\t"
"ds_write_addtid_b32 v203 offset:13472\n\t"
"v_cvt_pk_bf16_f32 v217, v217, v221\n\t"
"v_mfma_f32_16x16x32_bf16 v[144:147], a[76:79], a[160:163], v[144:147]\n\t"
"buffer_load_dwordx4 a[132:135], off, s[84:87], s48 offset:3072 sc1\n\t"
"v_mfma_f32_16x16x32_bf16 v[160:163], a[80:83], a[144:147], v[160:163]\n\t"
"s_nop 0\n\t"
"ds_write_addtid_b32 v208 offset:2048\n\t"
"ds_write_addtid_b32 v217 offset:14496\n\t"
"v_mfma_f32_16x16x32_bf16 v[160:163], a[84:87], a[160:163], v[160:163]\n\t"
"s_nop 0\n\t"
"ds_read_b128 a[152:155], v224 offset:18080\n\t"
"ds_read_b128 a[156:159], v224 offset:18336\n\t"
"v_mfma_f32_16x16x32_bf16 v[176:179], a[88:91], a[144:147], v[176:179]\n\t"
"s_nop 0\n\t"
"ds_read_b128 a[172:175], v224 offset:20384\n\t"
"ds_read_b64_tr_b16 a[192:193], v250 offset:41472\n\t"
"v_mfma_f32_16x16x32_bf16 v[176:179], a[92:95], a[160:163], v[176:179]\n\t"
"s_waitcnt lgkmcnt(13)\n\t"
"buffer_store_dwordx4 v[108:111], v232, s[16:19], s25 offen nt\n\t"
"v_mfma_f32_16x16x32_bf16 v[180:183], a[88:91], a[148:151], v[180:183]\n\t"
"s_nop 0\n\t"
"ds_read_b128 a[168:171], v224 offset:20128\n\t"
"ds_read_b64_tr_b16 a[200:201], v251 offset:41472\n\t"
"v_mfma_f32_16x16x32_bf16 v[180:183], a[92:95], a[164:167], v[180:183]\n\t"
"s_nop 0\n\t"
"ds_read_b64_tr_b16 a[196:197], v251 offset:41728\n\t"
"ds_read_b64_tr_b16 a[204:205], v250 offset:41728\n\t"
"v_mfma_f32_16x16x32_bf16 v[164:167], a[80:83], a[148:151], v[164:167]\n\t"
"buffer_load_dwordx4 a[128:131], off, s[84:87], s48 offset:2048 sc1\n\t"
"v_mfma_f32_16x16x32_bf16 v[164:167], a[84:87], a[164:167], v[164:167]\n\t"
"v_mfma_f32_16x16x32_bf16 v[148:151], a[72:75], a[148:151], v[148:151]\n\t"
"buffer_load_dwordx4 a[120:123], off, s[84:87], s48 offset:0 sc1\n\t"
"v_mfma_f32_16x16x32_bf16 v[148:151], a[76:79], a[164:167], v[148:151]\n\t"
"s_waitcnt lgkmcnt(0)\n\t"
"s_barrier\n\t"
"v_mfma_f32_16x16x32_bf16 v[152:155], a[72:75], a[152:155], v[152:155]\n\t"
"ds_read_b128 a[144:147], v224 offset:0\n\t"
"v_mfma_f32_16x16x32_bf16 v[152:155], a[76:79], a[168:171], v[152:155]\n\t"
"ds_read_b128 a[160:163], v224 offset:2048\n\t"
"v_mfma_f32_16x16x32_bf16 v[168:171], a[80:83], a[152:155], v[168:171]\n\t"
"ds_read_b128 a[148:151], v224 offset:256\n\t"
"v_add_u32 v248, 33280, v227\n\t"
"v_mfma_f32_16x16x32_bf16 v[168:171], a[84:87], a[168:171], v[168:171]\n\t"
"ds_read_b128 a[164:167], v224 offset:2304\n\t"
"v_xor_b32 v249, 32, v248\n\t"
"v_mfma_f32_16x16x32_bf16 v[184:187], a[88:91], a[152:155], v[184:187]\n\t"
"v_xor_b32 v250, 64, v248\n\t"
"v_xor_b32 v251, 96, v248\n\t"
"v_mfma_f32_16x16x32_bf16 v[184:187], a[92:95], a[168:171], v[184:187]\n\t"
"buffer_load_dwordx4 a[124:127], off, s[84:87], s48 offset:1024 sc1\n\t"
"v_mfma_f32_16x16x32_bf16 v[188:191], a[88:91], a[156:159], v[188:191]\n\t"
"ds_read_b128 a[168:171], v224 offset:2560\n\t"
"v_mfma_f32_16x16x32_bf16 v[188:191], a[92:95], a[172:175], v[188:191]\n\t"
"ds_read_b128 a[152:155], v224 offset:512\n\t"
"v_mfma_f32_16x16x32_bf16 v[172:175], a[80:83], a[156:159], v[172:175]\n\t"
"v_mfma_f32_16x16x32_bf16 v[172:175], a[84:87], a[172:175], v[172:175]\n\t"
"ds_read_b64_tr_b16 a[182:183], v249 offset:512\n\t"
"v_mfma_f32_16x16x32_bf16 v[156:159], a[72:75], a[156:159], v[156:159]\n\t"
"ds_read_b128 a[156:159], v224 offset:768\n\t"
"v_mfma_f32_16x16x32_bf16 v[156:159], a[76:79], a[172:175], v[156:159]\n\t"
"buffer_load_dwordx4 a[16:19], off, s[28:31], s75 offset:0 sc1\n\t"
"v_mfma_f32_4x4x4_16b_bf16 v[200:203], a[220:221], a[178:179], 0\n\t"
"ds_read_b64_tr_b16 a[190:191], v251 offset:512\n\t"
"v_mfma_f32_4x4x4_16b_bf16 v[204:207], a[220:221], a[186:187], 0\n\t"
"ds_read_b64_tr_b16 a[198:199], v249 offset:1664\n\t"
"v_mfma_f32_4x4x4_16b_bf16 v[216:219], a[220:221], a[194:195], 0\n\t"
"ds_read_b128 a[172:175], v224 offset:2816\n\t"
"v_mfma_f32_4x4x4_16b_bf16 v[220:223], a[220:221], a[202:203], 0\n\t"
"buffer_store_dwordx4 a[232:235], v233, s[16:19], s25 offen nt\n\t"
"v_mfma_f32_4x4x4_16b_bf16 v[192:195], a[222:223], a[176:177], v[200:203]\n\t"
"ds_read_b64_tr_b16 a[206:207], v251 offset:1664\n\t"
"v_mfma_f32_4x4x4_16b_bf16 v[196:199], a[222:223], a[184:185], v[204:207]\n\t"
"ds_read_b64_tr_b16 a[178:179], v248 offset:512\n\t"
"v_mfma_f32_4x4x4_16b_bf16 v[200:203], a[222:223], a[180:181], v[200:203]\n\t"
"ds_read_b64_tr_b16 a[186:187], v250 offset:512\n\t"
"v_mfma_f32_4x4x4_16b_bf16 v[204:207], a[222:223], a[188:189], v[204:207]\n\t"
"ds_read_b64_tr_b16 a[194:195], v248 offset:1664\n\t"
"v_mfma_f32_4x4x4_16b_bf16 v[208:211], a[222:223], a[192:193], v[216:219]\n\t"
"ds_read_b64_tr_b16 a[202:203], v250 offset:1664\n\t"
"v_mfma_f32_4x4x4_16b_bf16 v[212:215], a[222:223], a[200:201], v[220:223]\n\t"
"ds_read_b64_tr_b16 a[176:177], v248 offset:0\n\t"
"v_mfma_f32_4x4x4_16b_bf16 v[216:219], a[222:223], a[196:197], v[216:219]\n\t"
"ds_read_b64_tr_b16 a[184:185], v250 offset:0\n\t"
"v_mfma_f32_4x4x4_16b_bf16 v[220:223], a[222:223], a[204:205], v[220:223]\n\t"
"s_waitcnt vmcnt(10) lgkmcnt(8)\n\t"
"buffer_load_dwordx4 a[20:23], off, s[28:31], s75 offset:1024 sc1\n\t"
"v_mfma_f32_16x16x32_bf16 v[48:51], a[96:99], a[144:147], v[48:51]\n\t"
"ds_read_b64_tr_b16 a[180:181], v249 offset:0\n\t"
"v_cvt_pk_bf16_f32 v193, -v193, -v197\n\t"
"v_mfma_f32_16x16x32_bf16 v[48:51], a[100:103], a[160:163], v[48:51]\n\t"
"ds_read_b64_tr_b16 a[188:189], v251 offset:0\n\t"
"v_cvt_pk_bf16_f32 v194, -v194, -v198\n\t"
"v_mfma_f32_16x16x32_bf16 v[64:67], a[104:107], a[144:147], v[64:67]\n\t"
"ds_read_b64_tr_b16 a[192:193], v248 offset:1152\n\t"
"v_cvt_pk_bf16_f32 v201, v201, v205\n\t"
"v_mfma_f32_16x16x32_bf16 v[64:67], a[108:111], a[160:163], v[64:67]\n\t"
"buffer_store_dwordx4 a[236:239], v234, s[16:19], s25 offen nt\n\t"
"v_mfma_f32_16x16x32_bf16 v[80:83], a[112:115], a[144:147], v[80:83]\n\t"
"ds_read_b128 a[144:147], v224 offset:1024\n\t"
"v_cvt_pk_bf16_f32 v211, -v211, -v215\n\t"
"v_mfma_f32_16x16x32_bf16 v[80:83], a[116:119], a[160:163], v[80:83]\n\t"
"ds_read_b64_tr_b16 a[200:201], v250 offset:1152\n\t"
"v_cvt_pk_bf16_f32 v202, v202, v206\n\t"
"v_mfma_f32_16x16x32_bf16 v[84:87], a[112:115], a[148:151], v[84:87]\n\t"
"ds_read_b128 a[160:163], v224 offset:3072\n\t"
"v_cvt_pk_bf16_f32 v208, -v208, -v212\n\t"
"v_mfma_f32_16x16x32_bf16 v[84:87], a[116:119], a[164:167], v[84:87]\n\t"
"buffer_load_dwordx4 a[12:15], off, s[84:87], s75 offset:3072 sc1\n\t"
"v_mfma_f32_16x16x32_bf16 v[68:71], a[104:107], a[148:151], v[68:71]\n\t"
"ds_write_addtid_b32 v201 offset:28992\n\t"
"v_cvt_pk_bf16_f32 v192, -v192, -v196\n\t"
"v_mfma_f32_16x16x32_bf16 v[68:71], a[108:111], a[164:167], v[68:71]\n\t"
"ds_write_addtid_b32 v211 offset:23744\n\t"
"v_cvt_pk_bf16_f32 v203, v203, v207\n\t"
"v_mfma_f32_16x16x32_bf16 v[52:55], a[96:99], a[148:151], v[52:55]\n\t"
"ds_read_b128 a[148:151], v224 offset:1280\n\t"
"v_mfma_f32_16x16x32_bf16 v[52:55], a[100:103], a[164:167], v[52:55]\n\t"
"ds_write_addtid_b32 v193 offset:20672\n\t"
"v_cvt_pk_bf16_f32 v218, v218, v222\n\t"
"v_mfma_f32_16x16x32_bf16 v[56:59], a[96:99], a[152:155], v[56:59]\n\t"
"ds_read_b128 a[164:167], v224 offset:3328\n\t"
"v_cvt_pk_bf16_f32 v217, v217, v221\n\t"
"v_mfma_f32_16x16x32_bf16 v[56:59], a[100:103], a[168:171], v[56:59]\n\t"
"buffer_load_dwordx4 a[8:11], off, s[84:87], s75 offset:2048 sc1\n\t"
"v_mfma_f32_16x16x32_bf16 v[72:75], a[104:107], a[152:155], v[72:75]\n\t"
"ds_write_addtid_b32 v218 offset:27936\n\t"
"v_cvt_pk_bf16_f32 v195, -v195, -v199\n\t"
"v_mfma_f32_16x16x32_bf16 v[72:75], a[108:111], a[168:171], v[72:75]\n\t"
"ds_write_addtid_b32 v217 offset:31040\n\t"
"v_mfma_f32_16x16x32_bf16 v[88:91], a[112:115], a[152:155], v[88:91]\n\t"
"ds_read_b128 a[152:155], v224 offset:1536\n\t"
"v_cvt_pk_bf16_f32 v219, v219, v223\n\t"
"v_mfma_f32_16x16x32_bf16 v[88:91], a[116:119], a[168:171], v[88:91]\n\t"
"s_waitcnt lgkmcnt(8)\n\t"
"buffer_load_dwordx4 a[0:3], off, s[84:87], s75 offset:0 sc1\n\t"
"v_mfma_f32_16x16x32_bf16 v[92:95], a[112:115], a[156:159], v[92:95]\n\t"
"ds_write_addtid_b32 v194 offset:17568\n\t"
"v_cvt_pk_bf16_f32 v210, -v210, -v214\n\t"
"v_mfma_f32_16x16x32_bf16 v[92:95], a[116:119], a[172:175], v[92:95]\n\t"
"s_nop 0\n\t"
"ds_write_addtid_b32 v192 offset:16544\n\t"
"ds_write_addtid_b32 v202 offset:25888\n\t"
"v_mfma_f32_16x16x32_bf16 v[76:79], a[104:107], a[156:159], v[76:79]\n\t"
"ds_write_addtid_b32 v210 offset:19616\n\t"
"v_mfma_f32_16x16x32_bf16 v[76:79], a[108:111], a[172:175], v[76:79]\n\t"
"ds_write_addtid_b32 v208 offset:18592\n\t"
"v_cvt_pk_bf16_f32 v209, -v209, -v213\n\t"
"v_mfma_f32_16x16x32_bf16 v[60:63], a[96:99], a[156:159], v[60:63]\n\t"
"ds_write_addtid_b32 v219 offset:32064\n\t"
"v_cvt_pk_bf16_f32 v216, v216, v220\n\t"
"v_mfma_f32_16x16x32_bf16 v[60:63], a[100:103], a[172:175], v[60:63]\n\t"
"s_waitcnt vmcnt(7) lgkmcnt(9)\n\t"
"ds_write_addtid_b32 v203 offset:30016\n\t"
"ds_write_addtid_b32 v216 offset:26912\n\t"
"v_mfma_f32_16x16x32_bf16 v[144:147], a[120:123], a[144:147], v[144:147]\n\t"
"ds_write_addtid_b32 v195 offset:21696\n\t"
"v_cvt_pk_bf16_f32 v200, v200, v204\n\t"
"v_mfma_f32_16x16x32_bf16 v[144:147], a[124:127], a[160:163], v[144:147]\n\t"
"ds_write_addtid_b32 v200 offset:24864\n\t"
"v_mfma_f32_16x16x32_bf16 v[160:163], a[128:131], a[144:147], v[160:163]\n\t"
"ds_write_addtid_b32 v209 offset:22720\n\t"
"v_mfma_f32_16x16x32_bf16 v[160:163], a[132:135], a[160:163], v[160:163]\n\t"
"ds_read_b128 a[172:175], v224 offset:3840\n\t"
"v_mfma_f32_16x16x32_bf16 v[176:179], a[136:139], a[144:147], v[176:179]\n\t"
"ds_read_b128 a[168:171], v224 offset:3584\n\t"
"v_mfma_f32_16x16x32_bf16 v[176:179], a[140:143], a[160:163], v[176:179]\n\t"
"buffer_load_dwordx4 a[4:7], off, s[84:87], s75 offset:1024 sc1\n\t"
"v_mfma_f32_16x16x32_bf16 v[180:183], a[136:139], a[148:151], v[180:183]\n\t"
"ds_read_b128 a[156:159], v224 offset:1792\n\t"
"v_mfma_f32_16x16x32_bf16 v[180:183], a[140:143], a[164:167], v[180:183]\n\t"
"ds_read_b64_tr_b16 a[196:197], v249 offset:1152\n\t"
"v_mfma_f32_16x16x32_bf16 v[164:167], a[128:131], a[148:151], v[164:167]\n\t"
"ds_read_b64_tr_b16 a[204:205], v251 offset:1152\n\t"
"v_mfma_f32_16x16x32_bf16 v[164:167], a[132:135], a[164:167], v[164:167]\n\t"
"buffer_load_dwordx4 a[40:43], off, s[28:31], s78 offset:0 sc1\n\t"
"v_mfma_f32_16x16x32_bf16 v[148:151], a[120:123], a[148:151], v[148:151]\n\t"
"v_mfma_f32_16x16x32_bf16 v[148:151], a[124:127], a[164:167], v[148:151]\n\t"
"s_waitcnt lgkmcnt(0)\n\t"
"s_barrier\n\t"
"v_mfma_f32_16x16x32_bf16 v[152:155], a[120:123], a[152:155], v[152:155]\n\t"
"ds_read_b128 a[144:147], v224 offset:16544\n\t"
"v_mfma_f32_16x16x32_bf16 v[152:155], a[124:127], a[168:171], v[152:155]\n\t"
"ds_read_b128 a[148:151], v224 offset:16800\n\t"
"v_mfma_f32_16x16x32_bf16 v[168:171], a[128:131], a[152:155], v[168:171]\n\t"
"ds_read_b128 a[160:163], v224 offset:18592\n\t"
"v_mfma_f32_16x16x32_bf16 v[168:171], a[132:135], a[168:171], v[168:171]\n\t"
"ds_read_b128 a[164:167], v224 offset:18848\n\t"
"v_mfma_f32_16x16x32_bf16 v[184:187], a[136:139], a[152:155], v[184:187]\n\t"
"v_mfma_f32_16x16x32_bf16 v[184:187], a[140:143], a[168:171], v[184:187]\n\t"
"buffer_load_dwordx4 a[44:47], off, s[28:31], s78 offset:1024 sc1\n\t"
"v_mfma_f32_16x16x32_bf16 v[188:191], a[136:139], a[156:159], v[188:191]\n\t"
"ds_read_b128 a[168:171], v224 offset:19104\n\t"
"v_mfma_f32_16x16x32_bf16 v[188:191], a[140:143], a[172:175], v[188:191]\n\t"
"buffer_load_dwordx4 a[36:39], off, s[84:87], s78 offset:3072 sc1\n\t"
"v_mfma_f32_16x16x32_bf16 v[172:175], a[128:131], a[156:159], v[172:175]\n\t"
"ds_read_b128 a[152:155], v224 offset:17056\n\t"
"v_mfma_f32_16x16x32_bf16 v[172:175], a[132:135], a[172:175], v[172:175]\n\t"
"buffer_load_dwordx4 a[32:35], off, s[84:87], s78 offset:2048 sc1\n\t"
"v_mfma_f32_16x16x32_bf16 v[156:159], a[120:123], a[156:159], v[156:159]\n\t"
"ds_read_b128 a[156:159], v224 offset:17312\n\t"
"v_mfma_f32_16x16x32_bf16 v[156:159], a[124:127], a[172:175], v[156:159]\n\t"
"ds_read_b128 a[172:175], v224 offset:19360\n\t"
"v_mfma_f32_16x16x32_bf16 v[192:195], a[224:227], a[176:179], 0\n\t"
"ds_read_b64_tr_b16 a[176:177], v248 offset:42624\n\t"
"v_mfma_f32_16x16x32_bf16 v[196:199], a[224:227], a[180:183], 0\n\t"
"buffer_load_dwordx4 a[24:27], off, s[84:87], s78 offset:0 sc1\n\t"
"v_mfma_f32_16x16x32_bf16 v[200:203], a[224:227], a[184:187], 0\n\t"
"ds_read_b64_tr_b16 a[178:179], v248 offset:43136\n\t"
"v_mfma_f32_16x16x32_bf16 v[204:207], a[224:227], a[188:191], 0\n\t"
"ds_read_b64_tr_b16 a[180:181], v249 offset:42624\n\t"
"v_mfma_f32_16x16x32_bf16 v[208:211], a[224:227], a[192:195], 0\n\t"
"ds_read_b64_tr_b16 a[182:183], v249 offset:43136\n\t"
"v_mfma_f32_16x16x32_bf16 v[212:215], a[224:227], a[196:199], 0\n\t"
"buffer_load_dwordx4 a[28:31], off, s[84:87], s78 offset:1024 sc1\n\t"
"v_mfma_f32_16x16x32_bf16 v[216:219], a[224:227], a[200:203], 0\n\t"
"ds_read_b64_tr_b16 a[184:185], v250 offset:42624\n\t"
"v_mfma_f32_16x16x32_bf16 v[220:223], a[224:227], a[204:207], 0\n\t"
"s_waitcnt vmcnt(6) lgkmcnt(7)\n\t"
"ds_read_b64_tr_b16 a[186:187], v250 offset:43136\n\t"
"v_cvt_pk_bf16_f32 v192, v192, v196\n\t"
"v_mfma_f32_16x16x32_bf16 v[48:51], a[0:3], a[144:147], v[48:51]\n\t"
"ds_read_b64_tr_b16 a[188:189], v251 offset:42624\n\t"
"v_cvt_pk_bf16_f32 v194, v194, v198\n\t"
"v_mfma_f32_16x16x32_bf16 v[48:51], a[4:7], a[160:163], v[48:51]\n\t"
"ds_read_b64_tr_b16 a[190:191], v251 offset:43136\n\t"
"v_cvt_pk_bf16_f32 v193, v193, v197\n\t"
"v_mfma_f32_16x16x32_bf16 v[64:67], a[8:11], a[144:147], v[64:67]\n\t"
"ds_read_b64_tr_b16 a[192:193], v248 offset:21888\n\t"
"v_mfma_f32_16x16x32_bf16 v[64:67], a[12:15], a[160:163], v[64:67]\n\t"
"ds_read_b64_tr_b16 a[194:195], v248 offset:22400\n\t"
"v_cvt_pk_bf16_f32 v211, v211, v215\n\t"
"v_mfma_f32_16x16x32_bf16 v[80:83], a[16:19], a[144:147], v[80:83]\n\t"
"ds_read_b128 a[144:147], v224 offset:17568\n\t"
"v_cvt_pk_bf16_f32 v209, v209, v213\n\t"
"v_mfma_f32_16x16x32_bf16 v[80:83], a[20:23], a[160:163], v[80:83]\n\t"
"ds_read_b64_tr_b16 a[196:197], v249 offset:21888\n\t"
"v_cvt_pk_bf16_f32 v219, v219, v223\n\t"
"v_mfma_f32_16x16x32_bf16 v[84:87], a[16:19], a[148:151], v[84:87]\n\t"
"ds_write_addtid_b32 v209 offset:9344\n\t"
"v_mfma_f32_16x16x32_bf16 v[84:87], a[20:23], a[164:167], v[84:87]\n\t"
"ds_read_b64_tr_b16 a[198:199], v249 offset:22400\n\t"
"v_cvt_pk_bf16_f32 v200, v200, v204\n\t"
"v_mfma_f32_16x16x32_bf16 v[68:71], a[8:11], a[148:151], v[68:71]\n\t"
"ds_read_b128 a[160:163], v224 offset:19616\n\t"
"v_cvt_pk_bf16_f32 v208, v208, v212\n\t"
"v_mfma_f32_16x16x32_bf16 v[68:71], a[12:15], a[164:167], v[68:71]\n\t"
"ds_write_addtid_b32 v219 offset:15520\n\t"
"v_cvt_pk_bf16_f32 v217, v217, v221\n\t"
"v_mfma_f32_16x16x32_bf16 v[52:55], a[0:3], a[148:151], v[52:55]\n\t"
"ds_write_addtid_b32 v200 offset:2048\n\t"
"v_cvt_pk_bf16_f32 v218, v218, v222\n\t"
"v_mfma_f32_16x16x32_bf16 v[52:55], a[4:7], a[164:167], v[52:55]\n\t"
"ds_write_addtid_b32 v211 offset:13472\n\t"
"v_mfma_f32_16x16x32_bf16 v[56:59], a[0:3], a[152:155], v[56:59]\n\t"
"ds_read_b128 a[148:151], v224 offset:17824\n\t"
"v_cvt_pk_bf16_f32 v216, v216, v220\n\t"
"v_mfma_f32_16x16x32_bf16 v[56:59], a[4:7], a[168:171], v[56:59]\n\t"
"ds_read_b128 a[164:167], v224 offset:19872\n\t"
"v_cvt_pk_bf16_f32 v195, v195, v199\n\t"
"v_mfma_f32_16x16x32_bf16 v[72:75], a[8:11], a[152:155], v[72:75]\n\t"
"ds_write_addtid_b32 v195 offset:12448\n\t"
"v_cvt_pk_bf16_f32 v210, v210, v214\n\t"
"v_mfma_f32_16x16x32_bf16 v[72:75], a[12:15], a[168:171], v[72:75]\n\t"
"buffer_load_dwordx4 a[64:67], off, s[28:31], 0 offset:0 sc1\n\t"
"v_mfma_f32_16x16x32_bf16 v[88:91], a[16:19], a[152:155], v[88:91]\n\t"
"ds_read_b128 a[152:155], v224 offset:18080\n\t"
"v_cvt_pk_bf16_f32 v201, v201, v205\n\t"
"v_mfma_f32_16x16x32_bf16 v[88:91], a[20:23], a[168:171], v[88:91]\n\t"
"s_waitcnt lgkmcnt(8)\n\t"
"ds_read_b128 a[168:171], v224 offset:20128\n\t"
"v_cvt_pk_bf16_f32 v202, v202, v206\n\t"
"v_mfma_f32_16x16x32_bf16 v[92:95], a[16:19], a[156:159], v[92:95]\n\t"
"ds_write_addtid_b32 v216 offset:3072\n\t"
"v_cvt_pk_bf16_f32 v203, v203, v207\n\t"
"v_mfma_f32_16x16x32_bf16 v[92:95], a[20:23], a[172:175], v[92:95]\n\t"
"ds_write_addtid_b32 v194 offset:4128\n\t"
"v_mfma_f32_16x16x32_bf16 v[76:79], a[8:11], a[156:159], v[76:79]\n\t"
"s_xor_b32 s24, s24, 384\n\t"
"ds_write_addtid_b32 v208 offset:1024\n\t"
"ds_write_addtid_b32 v217 offset:11392\n\t"
"v_mfma_f32_16x16x32_bf16 v[76:79], a[12:15], a[172:175], v[76:79]\n\t"
"buffer_load_dwordx4 a[68:71], off, s[28:31], 0 offset:1024 sc1\n\t"
"v_mfma_f32_16x16x32_bf16 v[60:63], a[0:3], a[156:159], v[60:63]\n\t"
"s_xor_b32 s20, s20, 384\n\t"
"ds_read_b128 a[156:159], v224 offset:18336\n\t"
"ds_write_addtid_b32 v218 offset:7200\n\t"
"v_mfma_f32_16x16x32_bf16 v[60:63], a[4:7], a[172:175], v[60:63]\n\t"
"s_waitcnt vmcnt(2) lgkmcnt(9)\n\t"
"buffer_load_dwordx4 a[60:63], off, s[84:87], 0 offset:3072 sc1\n\t"
"v_mfma_f32_16x16x32_bf16 v[144:147], a[24:27], a[144:147], v[144:147]\n\t"
"s_nop 0\n\t"
"ds_write_addtid_b32 v192 offset:0\n\t"
"ds_write_addtid_b32 v202 offset:6176\n\t"
"v_mfma_f32_16x16x32_bf16 v[144:147], a[28:31], a[160:163], v[144:147]\n\t"
"s_nop 0\n\t"
"ds_write_addtid_b32 v193 offset:8320\n\t"
"ds_write_addtid_b32 v210 offset:5152\n\t"
"v_mfma_f32_16x16x32_bf16 v[160:163], a[32:35], a[144:147], v[160:163]\n\t"
"s_xor_b32 s27, s27, 384\n\t"
"ds_write_addtid_b32 v201 offset:10368\n\t"
"ds_write_addtid_b32 v203 offset:14496\n\t"
"v_mfma_f32_16x16x32_bf16 v[160:163], a[36:39], a[160:163], v[160:163]\n\t"
"buffer_load_dwordx4 a[56:59], off, s[84:87], 0 offset:2048 sc1\n\t"
"v_mfma_f32_16x16x32_bf16 v[176:179], a[40:43], a[144:147], v[176:179]\n\t"
"ds_read_b128 a[172:175], v224 offset:20384\n\t"
"v_mfma_f32_16x16x32_bf16 v[176:179], a[44:47], a[160:163], v[176:179]\n\t"
"buffer_load_dwordx4 a[48:51], off, s[84:87], 0 offset:0 sc1\n\t"
"v_mfma_f32_16x16x32_bf16 v[180:183], a[40:43], a[148:151], v[180:183]\n\t"
"ds_read_b64_tr_b16 a[200:201], v250 offset:21888\n\t"
"v_mfma_f32_16x16x32_bf16 v[180:183], a[44:47], a[164:167], v[180:183]\n\t"
"ds_read_b64_tr_b16 a[202:203], v250 offset:22400\n\t"
"v_mfma_f32_16x16x32_bf16 v[164:167], a[32:35], a[148:151], v[164:167]\n\t"
"s_xor_b32 s25, s25, 384\n\t"
"ds_read_b64_tr_b16 a[204:205], v251 offset:21888\n\t"
"ds_read_b64_tr_b16 a[206:207], v251 offset:22400\n\t"
"v_mfma_f32_16x16x32_bf16 v[164:167], a[36:39], a[164:167], v[164:167]\n\t"
"buffer_load_dwordx4 a[52:55], off, s[84:87], 0 offset:1024 sc1\n\t"
"v_mfma_f32_16x16x32_bf16 v[148:151], a[24:27], a[148:151], v[148:151]\n\t"
"v_mfma_f32_16x16x32_bf16 v[148:151], a[28:31], a[164:167], v[148:151]\n\t"
"s_waitcnt lgkmcnt(0)\n\t"
"s_barrier\n\t"
"v_mfma_f32_16x16x32_bf16 v[152:155], a[24:27], a[152:155], v[152:155]\n\t"
"ds_read_b128 a[160:163], v224 offset:2048\n\t"
"v_mfma_f32_16x16x32_bf16 v[152:155], a[28:31], a[168:171], v[152:155]\n\t"
"ds_read_b128 a[144:147], v224 offset:0\n\t"
"v_mfma_f32_16x16x32_bf16 v[168:171], a[32:35], a[152:155], v[168:171]\n\t"
"ds_read_b128 a[148:151], v224 offset:256\n\t"
"v_mfma_f32_16x16x32_bf16 v[168:171], a[36:39], a[168:171], v[168:171]\n\t"
"s_nop 0\n\t"
"ds_read_b128 a[164:167], v224 offset:2304\n\t"
"v_mfma_f32_16x16x32_bf16 v[184:187], a[40:43], a[152:155], v[184:187]\n\t"
"s_nop 0\n\t"
"v_mfma_f32_16x16x32_bf16 v[184:187], a[44:47], a[168:171], v[184:187]\n\t"
"s_nop 0\n\t"
"v_mfma_f32_16x16x32_bf16 v[188:191], a[40:43], a[156:159], v[188:191]\n\t"
"s_nop 0\n\t"
"ds_read_b128 a[152:155], v224 offset:512\n\t"
"v_mfma_f32_16x16x32_bf16 v[188:191], a[44:47], a[172:175], v[188:191]\n\t"
"s_nop 0\n\t"
"v_mfma_f32_16x16x32_bf16 v[172:175], a[32:35], a[156:159], v[172:175]\n\t"
"s_nop 0\n\t"
"v_mfma_f32_16x16x32_bf16 v[172:175], a[36:39], a[172:175], v[172:175]\n\t"
"buffer_load_dwordx4 a[88:91], off, s[28:31], s51 offset:0 sc1\n\t"
"v_mfma_f32_16x16x32_bf16 v[156:159], a[24:27], a[156:159], v[156:159]\n\t"
"s_nop 0\n\t"
"ds_read_b128 a[168:171], v224 offset:2560\n\t"
"v_mfma_f32_16x16x32_bf16 v[156:159], a[28:31], a[172:175], v[156:159]\n\t"
"buffer_load_dwordx4 a[92:95], off, s[28:31], s51 offset:1024 sc1\n\t"
"v_mfma_f32_16x16x32_bf16 v[192:195], a[224:227], a[176:179], 0\n\t"
"Lpad12_%=:\n\t"
"s_cbranch_execz Lret1_%=\n\t"
"v_mfma_f32_16x16x32_bf16 v[196:199], a[224:227], a[180:183], 0\n\t"
"buffer_load_dwordx4 a[84:87], off, s[84:87], s51 offset:3072 sc1\n\t"
"v_mfma_f32_16x16x32_bf16 v[200:203], a[224:227], a[184:187], 0\n\t"
"ds_read_b128 a[156:159], v224 offset:768\n\t"
"v_mfma_f32_16x16x32_bf16 v[204:207], a[224:227], a[188:191], 0\n\t"
".p2align 3\n\t"
"buffer_load_dwordx4 a[80:83], off, s[84:87], s51 offset:2048 sc1\n\t"
"v_pk_fma_f32 v[48:49], v[144:145], s[90:91], v[48:49]\n\t"
"v_pk_fma_f32 v[50:51], v[146:147], s[90:91], v[50:51]\n\t"
"v_pk_add_f32 v[144:145], v[48:49], v[144:145] neg_lo:[0,1] neg_hi:[0,1]\n\t"
"v_pk_add_f32 v[146:147], v[50:51], v[146:147] neg_lo:[0,1] neg_hi:[0,1]\n\t"
"v_pk_fma_f32 v[64:65], v[160:161], s[90:91], v[64:65]\n\t"
"v_pk_fma_f32 v[66:67], v[162:163], s[90:91], v[66:67]\n\t"
"v_pk_add_f32 v[160:161], v[64:65], v[160:161] neg_lo:[0,1] neg_hi:[0,1]\n\t"
"v_pk_add_f32 v[162:163], v[66:67], v[162:163] neg_lo:[0,1] neg_hi:[0,1]\n\t"
"v_pk_fma_f32 v[80:81], v[176:177], s[90:91], v[80:81]\n\t"
"v_pk_fma_f32 v[82:83], v[178:179], s[90:91], v[82:83]\n\t"
"v_pk_add_f32 v[176:177], v[80:81], v[176:177] neg_lo:[0,1] neg_hi:[0,1]\n\t"
"v_pk_add_f32 v[178:179], v[82:83], v[178:179] neg_lo:[0,1] neg_hi:[0,1]\n\t"
"v_pk_fma_f32 v[84:85], v[180:181], s[90:91], v[84:85]\n\t"
"v_pk_fma_f32 v[86:87], v[182:183], s[90:91], v[86:87]\n\t"
"v_pk_add_f32 v[180:181], v[84:85], v[180:181] neg_lo:[0,1] neg_hi:[0,1]\n\t"
"v_pk_add_f32 v[182:183], v[86:87], v[182:183] neg_lo:[0,1] neg_hi:[0,1]\n\t"
"v_pk_fma_f32 v[68:69], v[164:165], s[90:91], v[68:69]\n\t"
"v_pk_fma_f32 v[70:71], v[166:167], s[90:91], v[70:71]\n\t"
"v_pk_add_f32 v[164:165], v[68:69], v[164:165] neg_lo:[0,1] neg_hi:[0,1]\n\t"
"v_pk_add_f32 v[166:167], v[70:71], v[166:167] neg_lo:[0,1] neg_hi:[0,1]\n\t"
"v_pk_fma_f32 v[52:53], v[148:149], s[90:91], v[52:53]\n\t"
"v_pk_fma_f32 v[54:55], v[150:151], s[90:91], v[54:55]\n\t"
"v_pk_add_f32 v[148:149], v[52:53], v[148:149] neg_lo:[0,1] neg_hi:[0,1]\n\t"
"v_pk_add_f32 v[150:151], v[54:55], v[150:151] neg_lo:[0,1] neg_hi:[0,1]\n\t"
"v_pk_fma_f32 v[56:57], v[152:153], s[90:91], v[56:57]\n\t"
"v_pk_fma_f32 v[58:59], v[154:155], s[90:91], v[58:59]\n\t"
"v_pk_add_f32 v[152:153], v[56:57], v[152:153] neg_lo:[0,1] neg_hi:[0,1]\n\t"
"v_pk_add_f32 v[154:155], v[58:59], v[154:155] neg_lo:[0,1] neg_hi:[0,1]\n\t"
"v_pk_fma_f32 v[72:73], v[168:169], s[90:91], v[72:73]\n\t"
"v_pk_fma_f32 v[74:75], v[170:171], s[90:91], v[74:75]\n\t"
"v_pk_add_f32 v[168:169], v[72:73], v[168:169] neg_lo:[0,1] neg_hi:[0,1]\n\t"
"v_pk_add_f32 v[170:171], v[74:75], v[170:171] neg_lo:[0,1] neg_hi:[0,1]\n\t"
"v_pk_fma_f32 v[88:89], v[184:185], s[90:91], v[88:89]\n\t"
"v_pk_fma_f32 v[90:91], v[186:187], s[90:91], v[90:91]\n\t"
"v_pk_add_f32 v[184:185], v[88:89], v[184:185] neg_lo:[0,1] neg_hi:[0,1]\n\t"
"v_pk_add_f32 v[186:187], v[90:91], v[186:187] neg_lo:[0,1] neg_hi:[0,1]\n\t"
"v_pk_fma_f32 v[92:93], v[188:189], s[90:91], v[92:93]\n\t"
"v_pk_fma_f32 v[94:95], v[190:191], s[90:91], v[94:95]\n\t"
"v_pk_add_f32 v[188:189], v[92:93], v[188:189] neg_lo:[0,1] neg_hi:[0,1]\n\t"
"v_pk_add_f32 v[190:191], v[94:95], v[190:191] neg_lo:[0,1] neg_hi:[0,1]\n\t"
"v_pk_fma_f32 v[76:77], v[172:173], s[90:91], v[76:77]\n\t"
"v_pk_fma_f32 v[78:79], v[174:175], s[90:91], v[78:79]\n\t"
"v_pk_add_f32 v[172:173], v[76:77], v[172:173] neg_lo:[0,1] neg_hi:[0,1]\n\t"
"v_pk_add_f32 v[174:175], v[78:79], v[174:175] neg_lo:[0,1] neg_hi:[0,1]\n\t"
"v_pk_fma_f32 v[60:61], v[156:157], s[90:91], v[60:61]\n\t"
"v_pk_fma_f32 v[62:63], v[158:159], s[90:91], v[62:63]\n\t"
"v_pk_add_f32 v[156:157], v[60:61], v[156:157] neg_lo:[0,1] neg_hi:[0,1]\n\t"
"v_pk_add_f32 v[158:159], v[62:63], v[158:159] neg_lo:[0,1] neg_hi:[0,1]\n\t"
"v_mfma_f32_16x16x32_bf16 v[208:211], a[224:227], a[192:195], 0\n\t"
"buffer_load_dwordx4 a[72:75], off, s[84:87], s51 offset:0 sc1\n\t"
"v_mfma_f32_16x16x32_bf16 v[212:215], a[224:227], a[196:199], 0\n\t"
"ds_read_b128 a[172:175], v224 offset:2816\n\t"
"v_mfma_f32_16x16x32_bf16 v[216:219], a[224:227], a[200:203], 0\n\t"
"buffer_load_dwordx4 a[76:79], off, s[84:87], s51 offset:1024 sc1\n\t"
"v_mfma_f32_16x16x32_bf16 v[220:223], a[224:227], a[204:207], 0\n\t"
"s_waitcnt vmcnt(6) lgkmcnt(3)\n\t"
"v_cvt_pk_bf16_f32 v193, v193, v197\n\t"
"v_cvt_pk_bf16_f32 v194, v194, v198\n\t"
"v_mfma_f32_16x16x32_bf16 v[48:51], a[48:51], a[144:147], v[48:51]\n\t"
"ds_write_addtid_b32 v194 offset:20672\n\t"
"v_cvt_pk_bf16_f32 v195, v195, v199\n\t"
"v_mfma_f32_16x16x32_bf16 v[48:51], a[52:55], a[160:163], v[48:51]\n\t"
"ds_write_addtid_b32 v193 offset:24864\n\t"
"v_cvt_pk_bf16_f32 v201, v201, v205\n\t"
"v_mfma_f32_16x16x32_bf16 v[64:67], a[56:59], a[144:147], v[64:67]\n\t"
"ds_write_addtid_b32 v195 offset:28992\n\t"
"v_cvt_pk_bf16_f32 v192, v192, v196\n\t"
"v_mfma_f32_16x16x32_bf16 v[64:67], a[60:63], a[160:163], v[64:67]\n\t"
"ds_write_addtid_b32 v192 offset:16544\n\t"
"v_cvt_pk_bf16_f32 v209, v209, v213\n\t"
"v_mfma_f32_16x16x32_bf16 v[80:83], a[64:67], a[144:147], v[80:83]\n\t"
"ds_read_b128 a[144:147], v224 offset:1024\n\t"
"v_cvt_pk_bf16_f32 v200, v200, v204\n\t"
"v_mfma_f32_16x16x32_bf16 v[80:83], a[68:71], a[160:163], v[80:83]\n\t"
"ds_write_addtid_b32 v201 offset:26912\n\t"
"v_cvt_pk_bf16_f32 v48, v48, v49\n\t"
"v_mfma_f32_16x16x32_bf16 v[84:87], a[64:67], a[148:151], v[84:87]\n\t"
"ds_read_b128 a[160:163], v224 offset:3072\n\t"
"v_cvt_pk_bf16_f32 v218, v218, v222\n\t"
"v_mfma_f32_16x16x32_bf16 v[84:87], a[68:71], a[164:167], v[84:87]\n\t"
"ds_write_addtid_b32 v209 offset:25888\n\t"
"v_cvt_pk_bf16_f32 v216, v216, v220\n\t"
"v_cvt_pk_bf16_f32 v49, v50, v51\n\t"
"v_mfma_f32_16x16x32_bf16 v[68:71], a[56:59], a[148:151], v[68:71]\n\t"
"ds_write_addtid_b32 v218 offset:23744\n\t"
"v_cvt_pk_bf16_f32 v202, v202, v206\n\t"
"v_mfma_f32_16x16x32_bf16 v[68:71], a[60:63], a[164:167], v[68:71]\n\t"
"ds_write_addtid_b32 v200 offset:18592\n\t"
"v_cvt_pk_bf16_f32 v219, v219, v223\n\t"
"v_mfma_f32_16x16x32_bf16 v[52:55], a[48:51], a[148:151], v[52:55]\n\t"
"ds_read_b128 a[148:151], v224 offset:1280\n\t"
"v_cvt_pk_bf16_f32 v80, v80, v81\n\t"
"v_mfma_f32_16x16x32_bf16 v[52:55], a[52:55], a[164:167], v[52:55]\n\t"
"s_waitcnt lgkmcnt(9)\n\t"
"ds_read_b128 a[164:167], v224 offset:3328\n\t"
"v_cvt_pk_bf16_f32 v208, v208, v212\n\t"
"v_cvt_pk_bf16_f32 v210, v210, v214\n\t"
"v_cvt_pk_bf16_f32 v211, v211, v215\n\t"
"v_cvt_pk_bf16_f32 v217, v217, v221\n\t"
"v_mfma_f32_16x16x32_bf16 v[56:59], a[48:51], a[152:155], v[56:59]\n\t"
"ds_write_addtid_b32 v211 offset:30016\n\t"
"v_cvt_pk_bf16_f32 v203, v203, v207\n\t"
"v_mfma_f32_16x16x32_bf16 v[56:59], a[52:55], a[168:171], v[56:59]\n\t"
"s_nop 0\n\t"
"ds_write_addtid_b32 v202 offset:22720\n\t"
"ds_write_addtid_b32 v203 offset:31040\n\t"
"ds_write_addtid_b32 v208 offset:17568\n\t"
"ds_write_addtid_b32 v216 offset:19616\n\t"
"buffer_load_dwordx4 a[112:115], off, s[28:31], s81 offset:0 sc1\n\t"
"buffer_load_dwordx4 a[116:119], off, s[28:31], s81 offset:1024 sc1\n\t"
"v_cvt_pk_bf16_f32 v84, v84, v85\n\t"
"v_mfma_f32_16x16x32_bf16 v[72:75], a[56:59], a[152:155], v[72:75]\n\t"
"ds_write_addtid_b32 v219 offset:32064\n\t"
"v_cvt_pk_bf16_f32 v81, v82, v83\n\t"
"v_mfma_f32_16x16x32_bf16 v[72:75], a[60:63], a[168:171], v[72:75]\n\t"
"ds_write_addtid_b32 v217 offset:27936\n\t"
"v_cvt_pk_bf16_f32 v52, v52, v53\n\t"
"v_cvt_pk_bf16_f32 v68, v68, v69\n\t"
"v_mfma_f32_16x16x32_bf16 v[88:91], a[64:67], a[152:155], v[88:91]\n\t"
"ds_read_b128 a[152:155], v224 offset:1536\n\t"
"v_cvt_pk_bf16_f32 v53, v54, v55\n\t"
"v_mfma_f32_16x16x32_bf16 v[88:91], a[68:71], a[168:171], v[88:91]\n\t"
"ds_read_b128 a[168:171], v224 offset:3584\n\t"
"buffer_load_dwordx4 a[108:111], off, s[84:87], s81 offset:3072 sc1\n\t"
"buffer_load_dwordx4 a[104:107], off, s[84:87], s81 offset:2048 sc1\n\t"
"v_mfma_f32_16x16x32_bf16 v[92:95], a[64:67], a[156:159], v[92:95]\n\t"
"v_cvt_pk_bf16_f32 v69, v70, v71\n\t"
"v_cvt_pk_bf16_f32 v85, v86, v87\n\t"
"v_mfma_f32_16x16x32_bf16 v[92:95], a[68:71], a[172:175], v[92:95]\n\t"
"ds_write_addtid_b32 v210 offset:21696\n\t"
"ds_write2st64_b64 v247, v[80:81], v[84:85] offset0:0 offset1:13\n\t"
"v_mfma_f32_16x16x32_bf16 v[76:79], a[56:59], a[156:159], v[76:79]\n\t"
"v_cvt_pk_bf16_f32 v64, v64, v65\n\t"
"v_cvt_pk_bf16_f32 v88, v88, v89\n\t"
"v_mfma_f32_16x16x32_bf16 v[76:79], a[60:63], a[172:175], v[76:79]\n\t"
"buffer_load_dwordx4 a[96:99], off, s[84:87], s81 offset:0 sc1\n\t"
"buffer_load_dwordx4 a[100:103], off, s[84:87], s81 offset:1024 sc1\n\t"
"v_cvt_pk_bf16_f32 v89, v90, v91\n\t"
"v_mfma_f32_16x16x32_bf16 v[60:63], a[48:51], a[156:159], v[60:63]\n\t"
"v_cvt_pk_bf16_f32 v65, v66, v67\n\t"
"v_cvt_pk_bf16_f32 v72, v72, v73\n\t"
"v_mfma_f32_16x16x32_bf16 v[60:63], a[52:55], a[172:175], v[60:63]\n\t"
"s_waitcnt vmcnt(6) lgkmcnt(6)\n\t"
"ds_read_b128 a[156:159], v224 offset:1792\n\t"
"ds_read_b128 a[172:175], v224 offset:3840\n\t"
"ds_write2st64_b64 v228, v[48:49], v[52:53] offset0:0 offset1:13\n\t"
"v_cvt_pk_bf16_f32 v73, v74, v75\n\t"
"v_mfma_f32_16x16x32_bf16 v[144:147], a[72:75], a[144:147], v[144:147]\n\t"
"v_cvt_pk_bf16_f32 v56, v56, v57\n\t"
"v_cvt_pk_bf16_f32 v92, v92, v93\n\t"
"v_mfma_f32_16x16x32_bf16 v[144:147], a[76:79], a[160:163], v[144:147]\n\t"
"buffer_load_dwordx4 a[136:139], off, s[28:31], s66 offset:0 sc1\n\t"
"buffer_load_dwordx4 a[140:143], off, s[28:31], s66 offset:1024 sc1\n\t"
"ds_write2st64_b64 v246, v[64:65], v[68:69] offset0:0 offset1:13\n\t"
"v_cvt_pk_bf16_f32 v57, v58, v59\n\t"
"v_cvt_pk_bf16_f32 v93, v94, v95\n\t"
"v_mfma_f32_16x16x32_bf16 v[160:163], a[80:83], a[144:147], v[160:163]\n\t"
"v_cvt_pk_bf16_f32 v60, v60, v61\n\t"
"v_cvt_pk_bf16_f32 v76, v76, v77\n\t"
"v_mfma_f32_16x16x32_bf16 v[160:163], a[84:87], a[160:163], v[160:163]\n\t"
"buffer_load_dwordx4 a[132:135], off, s[84:87], s66 offset:3072 sc1\n\t"
"ds_write2st64_b64 v247, v[88:89], v[92:93] offset0:26 offset1:39\n\t"
"v_mfma_f32_16x16x32_bf16 v[176:179], a[88:91], a[144:147], v[176:179]\n\t"
"v_cvt_pk_bf16_f32 v61, v62, v63\n\t"
"v_cvt_pk_bf16_f32 v77, v78, v79\n\t"
"v_mfma_f32_16x16x32_bf16 v[176:179], a[92:95], a[160:163], v[176:179]\n\t"
"ds_write2st64_b64 v228, v[56:57], v[60:61] offset0:26 offset1:39\n\t"
"v_mfma_f32_16x16x32_bf16 v[180:183], a[88:91], a[148:151], v[180:183]\n\t"
"buffer_load_dwordx4 a[128:131], off, s[84:87], s66 offset:2048 sc1\n\t"
"v_mfma_f32_16x16x32_bf16 v[180:183], a[92:95], a[164:167], v[180:183]\n\t"
"ds_write2st64_b64 v246, v[72:73], v[76:77] offset0:26 offset1:39\n\t"
"v_mfma_f32_16x16x32_bf16 v[164:167], a[80:83], a[148:151], v[164:167]\n\t"
"buffer_load_dwordx4 a[120:123], off, s[84:87], s66 offset:0 sc1\n\t"
"v_mfma_f32_16x16x32_bf16 v[164:167], a[84:87], a[164:167], v[164:167]\n\t"
"v_mfma_f32_16x16x32_bf16 v[148:151], a[72:75], a[148:151], v[148:151]\n\t"
"buffer_load_dwordx4 a[124:127], off, s[84:87], s66 offset:1024 sc1\n\t"
"v_mfma_f32_16x16x32_bf16 v[148:151], a[76:79], a[164:167], v[148:151]\n\t"
"s_waitcnt lgkmcnt(0)\n\t"
"s_barrier\n\t"
"v_mfma_f32_16x16x32_bf16 v[152:155], a[72:75], a[152:155], v[152:155]\n\t"
"s_nop 0\n\t"
"ds_read_b128 a[148:151], v224 offset:16800\n\t"
"ds_read_b128 a[12:15], v229 offset:13312\n\t"
"v_mfma_f32_16x16x32_bf16 v[152:155], a[76:79], a[168:171], v[152:155]\n\t"
"s_nop 0\n\t"
"ds_read_b128 a[144:147], v224 offset:16544\n\t"
"ds_read_b128 a[160:163], v224 offset:18592\n\t"
"v_mfma_f32_16x16x32_bf16 v[168:171], a[80:83], a[152:155], v[168:171]\n\t"
"ds_read_b128 a[0:3], v229 offset:0\n\t"
"v_mfma_f32_16x16x32_bf16 v[168:171], a[84:87], a[168:171], v[168:171]\n\t"
"ds_read_b128 a[164:167], v224 offset:18848\n\t"
"v_mfma_f32_16x16x32_bf16 v[184:187], a[88:91], a[152:155], v[184:187]\n\t"
"s_nop 0\n\t"
"ds_read_b128 a[4:7], v230 offset:0\n\t"
"ds_read_b128 a[16:19], v230 offset:13312\n\t"
"v_mfma_f32_16x16x32_bf16 v[184:187], a[92:95], a[168:171], v[184:187]\n\t"
"s_nop 0\n\t"
"ds_read_b128 a[8:11], v231 offset:0\n\t"
"ds_read_b128 a[20:23], v231 offset:13312\n\t"
"v_mfma_f32_16x16x32_bf16 v[188:191], a[88:91], a[156:159], v[188:191]\n\t"
"s_waitcnt lgkmcnt(8)\n\t"
"buffer_store_dwordx4 a[12:15], v232, s[16:19], s27 offen nt\n\t"
"v_mfma_f32_16x16x32_bf16 v[188:191], a[92:95], a[172:175], v[188:191]\n\t"
"ds_read_b128 a[152:155], v224 offset:17568\n\t"
"v_mfma_f32_16x16x32_bf16 v[172:175], a[80:83], a[156:159], v[172:175]\n\t"
"ds_read_b128 a[168:171], v224 offset:19616\n\t"
"v_mfma_f32_16x16x32_bf16 v[172:175], a[84:87], a[172:175], v[172:175]\n\t"
"s_waitcnt lgkmcnt(7)\n\t"
"buffer_store_dwordx4 a[0:3], v232, s[16:19], s24 offen nt\n\t"
"v_mfma_f32_16x16x32_bf16 v[156:159], a[72:75], a[156:159], v[156:159]\n\t"
"v_mfma_f32_16x16x32_bf16 v[156:159], a[76:79], a[172:175], v[156:159]\n\t"
"s_waitcnt vmcnt(8)\n\t"
"s_waitcnt lgkmcnt(5)\n\t"
"buffer_store_dwordx4 a[4:7], v233, s[16:19], s24 offen nt\n\t"
"v_mfma_f32_16x16x32_bf16 v[144:147], a[96:99], a[144:147], v[144:147]\n\t"
"ds_read_b128 a[156:159], v224 offset:17824\n\t"
"v_mfma_f32_16x16x32_bf16 v[144:147], a[100:103], a[160:163], v[144:147]\n\t"
"s_waitcnt lgkmcnt(5)\n\t"
"buffer_store_dwordx4 a[16:19], v233, s[16:19], s27 offen nt\n\t"
"v_mfma_f32_16x16x32_bf16 v[160:163], a[104:107], a[144:147], v[160:163]\n\t"
"ds_read_b128 a[172:175], v224 offset:19872\n\t"
"v_mfma_f32_16x16x32_bf16 v[160:163], a[108:111], a[160:163], v[160:163]\n\t"
"s_waitcnt lgkmcnt(4)\n\t"
"buffer_store_dwordx4 a[8:11], v234, s[16:19], s24 offen nt\n\t"
"v_mfma_f32_16x16x32_bf16 v[176:179], a[112:115], a[144:147], v[176:179]\n\t"
"ds_read_b128 a[144:147], v224 offset:17056\n\t"
"v_mfma_f32_16x16x32_bf16 v[176:179], a[116:119], a[160:163], v[176:179]\n\t"
"v_mfma_f32_16x16x32_bf16 v[180:183], a[112:115], a[148:151], v[180:183]\n\t"
"buffer_store_dwordx4 a[20:23], v234, s[16:19], s27 offen nt\n\t"
"v_mfma_f32_16x16x32_bf16 v[180:183], a[116:119], a[164:167], v[180:183]\n\t"
"ds_read_b128 a[160:163], v224 offset:19104\n\t"
"v_mfma_f32_16x16x32_bf16 v[164:167], a[104:107], a[148:151], v[164:167]\n\t"
"v_mfma_f32_16x16x32_bf16 v[164:167], a[108:111], a[164:167], v[164:167]\n\t"
"v_mfma_f32_16x16x32_bf16 v[148:151], a[96:99], a[148:151], v[148:151]\n\t"
"ds_read_b128 a[148:151], v224 offset:17312\n\t"
"v_mfma_f32_16x16x32_bf16 v[148:151], a[100:103], a[164:167], v[148:151]\n\t"
"s_waitcnt vmcnt(6) lgkmcnt(4)\n\t"
"v_mfma_f32_16x16x32_bf16 v[144:147], a[120:123], a[152:155], v[144:147]\n\t"
"ds_read_b128 a[164:167], v224 offset:19360\n\t"
"v_mfma_f32_16x16x32_bf16 v[144:147], a[124:127], a[168:171], v[144:147]\n\t"
"v_mfma_f32_16x16x32_bf16 v[160:163], a[128:131], a[152:155], v[160:163]\n\t"
"v_mfma_f32_16x16x32_bf16 v[160:163], a[132:135], a[168:171], v[160:163]\n\t"
"v_mfma_f32_16x16x32_bf16 v[176:179], a[136:139], a[152:155], v[176:179]\n\t"
"ds_read_b128 a[152:155], v224 offset:18080\n\t"
"v_mfma_f32_16x16x32_bf16 v[176:179], a[140:143], a[168:171], v[176:179]\n\t"
"s_waitcnt lgkmcnt(3)\n\t"
"s_nop 1\n\t"
"v_cvt_pk_bf16_f32 v144, v144, v145\n\t"
"v_cvt_pk_bf16_f32 v145, v146, v147\n\t"
"v_mfma_f32_16x16x32_bf16 v[180:183], a[136:139], a[156:159], v[180:183]\n\t"
"ds_read_b128 a[168:171], v224 offset:20128\n\t"
"v_mfma_f32_16x16x32_bf16 v[180:183], a[140:143], a[172:175], v[180:183]\n\t"
"v_mfma_f32_16x16x32_bf16 v[164:167], a[128:131], a[156:159], v[164:167]\n\t"
"v_cvt_pk_bf16_f32 v160, v160, v161\n\t"
"v_cvt_pk_bf16_f32 v176, v176, v177\n\t"
"v_mfma_f32_16x16x32_bf16 v[164:167], a[132:135], a[172:175], v[164:167]\n\t"
"v_cvt_pk_bf16_f32 v161, v162, v163\n\t"
"v_mfma_f32_16x16x32_bf16 v[148:151], a[120:123], a[156:159], v[148:151]\n\t"
"ds_read_b128 a[156:159], v224 offset:18336\n\t"
"s_nop 0\n\t"
"v_cvt_pk_bf16_f32 v180, v180, v181\n\t"
"v_mfma_f32_16x16x32_bf16 v[148:151], a[124:127], a[172:175], v[148:151]\n\t"
"ds_read_b128 a[172:175], v224 offset:20384\n\t"
"v_cvt_pk_bf16_f32 v181, v182, v183\n\t"
"v_mfma_f32_16x16x32_bf16 v[152:155], a[96:99], a[144:147], v[152:155]\n\t"
"v_cvt_pk_bf16_f32 v177, v178, v179\n\t"
"v_mfma_f32_16x16x32_bf16 v[152:155], a[100:103], a[160:163], v[152:155]\n\t"
"ds_write2st64_b64 v247, v[176:177], v[180:181] offset0:52 offset1:65\n\t"
"v_mfma_f32_16x16x32_bf16 v[168:171], a[104:107], a[144:147], v[168:171]\n\t"
"v_cvt_pk_bf16_f32 v164, v164, v165\n\t"
"v_cvt_pk_bf16_f32 v165, v166, v167\n\t"
"v_mfma_f32_16x16x32_bf16 v[168:171], a[108:111], a[160:163], v[168:171]\n\t"
"v_cvt_pk_bf16_f32 v148, v148, v149\n\t"
"v_cvt_pk_bf16_f32 v149, v150, v151\n\t"
"v_mfma_f32_16x16x32_bf16 v[184:187], a[112:115], a[144:147], v[184:187]\n\t"
"ds_write2st64_b64 v228, v[144:145], v[148:149] offset0:52 offset1:65\n\t"
"v_mfma_f32_16x16x32_bf16 v[184:187], a[116:119], a[160:163], v[184:187]\n\t"
"s_waitcnt lgkmcnt(4)\n\t"
"v_mfma_f32_16x16x32_bf16 v[188:191], a[112:115], a[148:151], v[188:191]\n\t"
"v_mfma_f32_16x16x32_bf16 v[188:191], a[116:119], a[164:167], v[188:191]\n\t"
"ds_write2st64_b64 v246, v[160:161], v[164:165] offset0:52 offset1:65\n\t"
"v_mfma_f32_16x16x32_bf16 v[172:175], a[104:107], a[148:151], v[172:175]\n\t"
"v_mfma_f32_16x16x32_bf16 v[172:175], a[108:111], a[164:167], v[172:175]\n\t"
"v_mfma_f32_16x16x32_bf16 v[156:159], a[96:99], a[148:151], v[156:159]\n\t"
"v_mfma_f32_16x16x32_bf16 v[156:159], a[100:103], a[164:167], v[156:159]\n\t"
"s_waitcnt lgkmcnt(0)\n\t"
"s_barrier\n\t"
"v_mfma_f32_16x16x32_bf16 v[152:155], a[120:123], a[152:155], v[152:155]\n\t"
"ds_read_b128 a[32:35], v231 offset:26624\n\t"
"v_mfma_f32_16x16x32_bf16 v[152:155], a[124:127], a[168:171], v[152:155]\n\t"
"ds_read_b128 a[24:27], v229 offset:26624\n\t"
"v_mfma_f32_16x16x32_bf16 v[168:171], a[128:131], a[152:155], v[168:171]\n\t"
"v_mfma_f32_16x16x32_bf16 v[168:171], a[132:135], a[168:171], v[168:171]\n\t"
"v_mfma_f32_16x16x32_bf16 v[184:187], a[136:139], a[152:155], v[184:187]\n\t"
"ds_read_b128 a[28:31], v230 offset:26624\n\t"
"s_nop 2\n\t"
"v_cvt_pk_bf16_f32 v152, v152, v153\n\t"
"v_mfma_f32_16x16x32_bf16 v[184:187], a[140:143], a[168:171], v[184:187]\n\t"
"v_cvt_pk_bf16_f32 v153, v154, v155\n\t"
"v_mfma_f32_16x16x32_bf16 v[188:191], a[136:139], a[156:159], v[188:191]\n\t"
"s_waitcnt lgkmcnt(2)\n\t"
"buffer_store_dwordx4 a[32:35], v234, s[16:19], s20 offen nt\n\t"
"v_mfma_f32_16x16x32_bf16 v[188:191], a[140:143], a[172:175], v[188:191]\n\t"
"v_cvt_pk_bf16_f32 v168, v168, v169\n\t"
"v_cvt_pk_bf16_f32 v169, v170, v171\n\t"
"v_mfma_f32_16x16x32_bf16 v[172:175], a[128:131], a[156:159], v[172:175]\n\t"
"v_cvt_pk_bf16_f32 v184, v184, v185\n\t"
"v_cvt_pk_bf16_f32 v185, v186, v187\n\t"
"v_mfma_f32_16x16x32_bf16 v[172:175], a[132:135], a[172:175], v[172:175]\n\t"
"s_waitcnt lgkmcnt(1)\n\t"
"buffer_store_dwordx4 a[24:27], v232, s[16:19], s20 offen nt\n\t"
"v_mfma_f32_16x16x32_bf16 v[156:159], a[120:123], a[156:159], v[156:159]\n\t"
"v_cvt_pk_bf16_f32 v188, v188, v189\n\t"
"v_cvt_pk_bf16_f32 v189, v190, v191\n\t"
"v_mfma_f32_16x16x32_bf16 v[156:159], a[124:127], a[172:175], v[156:159]\n\t"
"s_waitcnt lgkmcnt(0)\n\t"
"buffer_store_dwordx4 a[28:31], v233, s[16:19], s20 offen nt\n\t"
"s_nop 5\n\t"
"v_cvt_pk_bf16_f32 v156, v156, v157\n\t"
"v_cvt_pk_bf16_f32 v157, v158, v159\n\t"
"ds_write2st64_b64 v228, v[152:153], v[156:157] offset0:78 offset1:91\n\t"
"v_cvt_pk_bf16_f32 v172, v172, v173\n\t"
"v_cvt_pk_bf16_f32 v173, v174, v175\n\t"
"ds_write2st64_b64 v246, v[168:169], v[172:173] offset0:78 offset1:91\n\t"
"ds_write2st64_b64 v247, v[184:185], v[188:189] offset0:78 offset1:91\n\t"
"s_waitcnt lgkmcnt(0)\n\t"
"s_barrier\n\t"
"ds_read_b128 a[36:39], v229 offset:39936\n\t"
"ds_read_b128 a[40:43], v230 offset:39936\n\t"
"ds_read_b128 a[44:47], v231 offset:39936\n\t"
"s_waitcnt lgkmcnt(2)\n\t"
"buffer_store_dwordx4 a[36:39], v232, s[16:19], s25 offen nt\n\t"
"s_waitcnt lgkmcnt(1)\n\t"
"buffer_store_dwordx4 a[40:43], v233, s[16:19], s25 offen nt\n\t"
"s_waitcnt lgkmcnt(0)\n\t"
"buffer_store_dwordx4 a[44:47], v234, s[16:19], s25 offen nt\n\t"
"s_waitcnt vmcnt(0)\n\t"
"Lwino_end:\n\t"
  : "+{v205}"(tid)
  : "s"(__builtin_amdgcn_kernarg_segment_ptr()), "s"(blockIdx.x), "s"((unsigned)(size_t)(__attribute__((address_space(3))) char*)smem)
  : "v0", "v1", "v2", "v3", "v4", "v5", "v6", "v7", "v8", "v9", "v10", "v11", "v12", "v13", "v14", "v15", "v16", "v17", "v18", "v19", "v20", "v21", "v22", "v23", "v24", "v25", "v26", "v27", "v28", "v29", "v30", "v31", "v32", "v33", "v34", "v35", "v36", "v37", "v38", "v39", "v40", "v41", "v42", "v43", "v44", "v45", "v46", "v47", "v48", "v49", "v50", "v51", "v52", "v53", "v54", "v55", "v56", "v57", "v58", "v59", "v60", "v61", "v62", "v63", "v64", "v65", "v66", "v67", "v68", "v69", "v70", "v71", "v72", "v73", "v74", "v75", "v76", "v77", "v78", "v79", "v80", "v81", "v82", "v83", "v84", "v85", "v86", "v87", "v88", "v89", "v90", "v91", "v92", "v93", "v94", "v95", "v96", "v97", "v98", "v99", "v100", "v101", "v102", "v103", "v104", "v105", "v106", "v107", "v108", "v109", "v110", "v111", "v112", "v113", "v114", "v115", "v116", "v117", "v118", "v119", "v120", "v121", "v122", "v123", "v124", "v125", "v126", "v127", "v128", "v129", "v130", "v131", "v132", "v133", "v134", "v135", "v136", "v137", "v138", "v139", "v140", "v141", "v142", "v143", "v144", "v145", "v146", "v147", "v148", "v149", "v150", "v151", "v152", "v153", "v154", "v155", "v156", "v157", "v158", "v159", "v160", "v161", "v162", "v163", "v164", "v165", "v166", "v167", "v168", "v169", "v170", "v171", "v172", "v173", "v174", "v175", "v176", "v177", "v178", "v179", "v180", "v181", "v182", "v183", "v184", "v185", "v186", "v187", "v188", "v189", "v190", "v191", "v192", "v193", "v194", "v195", "v196", "v197", "v198", "v199", "v200", "v201", "v202", "v203", "v204", "v206", "v207", "v208", "v209", "v210", "v211", "v212", "v213", "v214", "v215", "v216", "v217", "v218", "v219", "v220", "v221", "v222", "v223", "v224", "v225", "v226", "v227", "v228", "v229", "v230", "v231", "v232", "v233", "v234", "v235", "v236", "v237", "v238", "v239", "v240", "v241", "v242", "v243", "v244", "v245", "v246", "v247", "v248", "v249", "v250", "v251", "v252", "v253", "v254", "v255", "a0", "a1", "a2", "a3", "a4", "a5", "a6", "a7", "a8", "a9", "a10", "a11", "a12", "a13", "a14", "a15", "a16", "a17", "a18", "a19", "a20", "a21", "a22", "a23", "a24", "a25", "a26", "a27", "a28", "a29", "a30", "a31", "a32", "a33", "a34", "a35", "a36", "a37", "a38", "a39", "a40", "a41", "a42", "a43", "a44", "a45", "a46", "a47", "a48", "a49", "a50", "a51", "a52", "a53", "a54", "a55", "a56", "a57", "a58", "a59", "a60", "a61", "a62", "a63", "a64", "a65", "a66", "a67", "a68", "a69", "a70", "a71", "a72", "a73", "a74", "a75", "a76", "a77", "a78", "a79", "a80", "a81", "a82", "a83", "a84", "a85", "a86", "a87", "a88", "a89", "a90", "a91", "a92", "a93", "a94", "a95", "a96", "a97", "a98", "a99", "a100", "a101", "a102", "a103", "a104", "a105", "a106", "a107", "a108", "a109", "a110", "a111", "a112", "a113", "a114", "a115", "a116", "a117", "a118", "a119", "a120", "a121", "a122", "a123", "a124", "a125", "a126", "a127", "a128", "a129", "a130", "a131", "a132", "a133", "a134", "a135", "a136", "a137", "a138", "a139", "a140", "a141", "a142", "a143", "a144", "a145", "a146", "a147", "a148", "a149", "a150", "a151", "a152", "a153", "a154", "a155", "a156", "a157", "a158", "a159", "a160", "a161", "a162", "a163", "a164", "a165", "a166", "a167", "a168", "a169", "a170", "a171", "a172", "a173", "a174", "a175", "a176", "a177", "a178", "a179", "a180", "a181", "a182", "a183", "a184", "a185", "a186", "a187", "a188", "a189", "a190", "a191", "a192", "a193", "a194", "a195", "a196", "a197", "a198", "a199", "a200", "a201", "a202", "a203", "a204", "a205", "a206", "a207", "a208", "a209", "a210", "a211", "a212", "a213", "a214", "a215", "a216", "a217", "a218", "a219", "a220", "a221", "a222", "a223", "a224", "a225", "a226", "a227", "a228", "a229", "a230", "a231", "a232", "a233", "a234", "a235", "a236", "a237", "a238", "a239", "a240", "a241", "a242", "a243", "a244", "a245", "a246", "a247", "a248", "a249", "a250", "a251", "a252", "a253", "a254", "a255", "s6", "s7", "s8", "s9", "s10", "s11", "s12", "s13", "s14", "s15", "s16", "s17", "s18", "s19", "s20", "s21", "s22", "s23", "s24", "s25", "s26", "s27", "s28", "s29", "s30", "s31", "s32", "s33", "s34", "s35", "s36", "s37", "s38", "s39", "s40", "s41", "s42", "s43", "s44", "s45", "s46", "s47", "s48", "s49", "s50", "s51", "s52", "s53", "s54", "s55", "s56", "s57", "s58", "s59", "s60", "s61", "s62", "s63", "s64", "s65", "s66", "s67", "s68", "s69", "s70", "s71", "s72", "s73", "s74", "s75", "s76", "s77", "s78", "s79", "s80", "s81", "s82", "s83", "s84", "s85", "s86", "s87", "s88", "s89", "s90", "s91", "s92", "s93", "s94", "s95", "s96", "s97", "s98", "s99", "scc", "vcc", "memory");
}

extern "C" void kernel_launch(void* const* d_in, const int* in_sizes, int n_in, void* d_out, int out_size, void* d_ws,
                              size_t ws_size, hipStream_t stream) {
  wino_main_kernel<<<256, 256, 0, stream>>>((const char*)d_in[0], (char*)d_ws, (char*)d_out, (const char*)d_in[1]);
}
```

```cpp
#include <hip/hip_runtime.h>
#include <hip/hip_bf16.h>
#include <stdint.h>

#define U_BYTES 1179648

__global__ __launch_bounds__(256, 1) __attribute__((section(".text.w"))) void wino_main_kernel(const char* __restrict__ x, char* __restrict__ Uq, char* __restrict__ out, const char* __restrict__ wt) {
  __shared__ __attribute__((aligned(1024))) char smem[163840];
  unsigned tid = threadIdx.x;
  asm volatile(
".pushsection .text\n\t"
".p2align 12\n\t"
".fill 576, 4, 0xbf800000\n\t"
".popsection\n\t"
".pushsection .text.x,\"ax\"\n\t"
".fill 256, 4, 0xbf800000\n\t"
".popsection\n\t"
"s_load_dwordx8 s[76:83], %1, 0x0\n\t"
"s_mov_b32 s66, %2\n\t"
"s_getpc_b64 s[6:7]\n\t"
"Lwino_pc:\n\t"
"s_bfe_u32 s42, %2, 0x50003\n\t"
"s_lshl_b32 s42, s42, 12\n\t"
"v_lshl_add_u32 v203, v205, 4, s42\n\t"
"v_min_u32 v203, (Lwino_end-Lwino_pc-16)&-16, v203\n\t"
"global_load_dwordx4 v[196:199], v203, s[6:7]\n\t"
"v_and_b32 v204, 63, v205\n\t"
"v_readfirstlane_b32 s73, v205\n\t"
"v_lshrrev_b32 v120, 3, v204\n\t"
"v_and_b32 v121, 7, v204\n\t"
"s_lshr_b32 s73, s73, 6\n\t"
"s_lshr_b32 s56, s66, 5\n\t"
"s_mul_i32 s56, s56, 12288\n\t"
"s_bfe_u32 s61, s66, 0x20001\n\t"
"s_mul_i32 s61, s61, 1536\n\t"
"s_add_u32 s56, s56, s61\n\t"
"s_bitcmp1_b32 s66, 0\n\t"
"s_cselect_b32 s61, 48, 0\n\t"
"s_add_u32 s56, s56, s61\n\t"
"s_bfe_u32 s62, s66, 0x20003\n\t"
"s_mul_i32 s61, s62, 96\n\t"
"s_add_u32 s56, s56, s61\n\t"
"s_cmp_lt_u32 s66, 192\n\t"
"s_cselect_b32 s56, s56, 2.0\n\t"
"v_bfe_u32 v42, v204, 3, 2\n\t"
"v_lshlrev_b32 v43, 2, v204\n\t"
"v_and_or_b32 v42, v43, 16, v42\n\t"
"v_mul_u32_u24 v42, 0x180, v42\n\t"
"v_lshrrev_b32 v44, 5, v204\n\t"
"v_mul_u32_u24 v43, 0x12000, v44\n\t"
"v_and_b32 v45, 3, v204\n\t"
"v_min_u32 v41, 2, v45\n\t"
"v_lshl_add_u32 v41, v41, 4, v42\n\t"
"v_add_u32 v41, v41, v43\n\t"
"v_lshlrev_b32 v43, 4, v204\n\t"
"v_bfe_u32 v45, v204, 1, 2\n\t"
"v_and_or_b32 v45, v43, 16, v45\n\t"
"v_mul_u32_u24 v45, 0x180, v45\n\t"
"v_lshl_add_u32 v44, v44, 3, v120\n\t"
"v_lshl_add_u32 v45, v44, 1, v45\n\t"
"v_mov_b32 v40, 0\n\t"
"s_mul_i32 s61, s73, 147456\n\t"
"s_add_u32 s61, s61, s56\n\t"
"s_lshl3_add_u32 s62, s73, s56\n\t"
"s_add_u32 s62, s62, 589824\n\t"
"s_lshl_b32 s64, s73, 10\n\t"
"s_and_b32 s60, s66, 7\n\t"
"s_lshr_b32 s65, s66, 3\n\t"
"s_mul_i32 s24, s60, 12582912\n\t"
"s_mul_i32 s63, s65, 6144\n\t"
"s_add_u32 s24, s24, s63\n\t"
"s_add_u32 s22, s24, 196224\n\t"
"s_mul_i32 s63, s73, 786432\n\t"
"s_mul_i32 s26, s73, 4608\n\t"
"s_mov_b32 s48, 189696\n\t"
"s_cmp_eq_u32 s65, 0\n\t"
"s_cselect_b64 s[40:41], 0xff, 0\n\t"
"s_cselect_b64 s[46:47], -1, 0\n\t"
"v_mul_u32_u24 v122, 0x300, v120\n\t"
"v_xor_b32 v123, 4, v121\n\t"
"v_add_u32 v122, s63, v122\n\t"
"v_and_b32 v124, 2, v120\n\t"
"v_xor_b32 v124, v124, v123\n\t"
"v_lshl_add_u32 v128, v124, 4, v122\n\t"
"v_cndmask_b32 v241, v128, 2.0, s[40:41]\n\t"
"v_add_u32 v122, 194816, v122\n\t"
"v_add_u32 v124, -1, v120\n\t"
"v_ashrrev_i32 v125, 31, v124\n\t"
"v_sub_u32 v124, v124, v125\n\t"
"v_and_b32 v124, 2, v124\n\t"
"v_xor_b32 v124, v124, v123\n\t"
"v_mad_i32_i24 v125, v125, s48, v122\n\t"
"v_lshl_add_u32 v129, v124, 4, v125\n\t"
"s_lshl_b64 s[40:41], s[40:41], 8\n\t"
"v_cndmask_b32 v242, v129, 2.0, s[40:41]\n\t"
"v_add_u32 v122, 194816, v122\n\t"
"v_add_u32 v124, -2, v120\n\t"
"v_ashrrev_i32 v125, 31, v124\n\t"
"v_sub_u32 v124, v124, v125\n\t"
"v_and_b32 v124, 2, v124\n\t"
"v_and_or_b32 v124, v125, 4, v124\n\t"
"v_xor_b32 v124, v124, v121\n\t"
"v_mad_i32_i24 v125, v125, s48, v122\n\t"
"v_lshl_add_u32 v130, v124, 4, v125\n\t"
"s_lshl_b64 s[40:41], s[40:41], 8\n\t"
"v_cndmask_b32 v243, v130, 2.0, s[40:41]\n\t"
"v_add_u32 v122, 194816, v122\n\t"
"v_add_u32 v124, -3, v120\n\t"
"v_ashrrev_i32 v125, 31, v124\n\t"
"v_sub_u32 v124, v124, v125\n\t"
"v_and_b32 v124, 2, v124\n\t"
"v_xor_b32 v124, v124, v121\n\t"
"v_mad_i32_i24 v125, v125, s48, v122\n\t"
"v_lshl_add_u32 v131, v124, 4, v125\n\t"
"s_lshl_b64 s[40:41], s[40:41], 8\n\t"
"v_cndmask_b32 v244, v131, 2.0, s[40:41]\n\t"
"v_add_u32 v124, 5, v120\n\t"
"v_and_b32 v124, 2, v124\n\t"
"v_xor_b32 v124, v124, v121\n\t"
"v_add_u32 v122, 2560, v122\n\t"
"v_lshl_add_u32 v245, v124, 4, v122\n\t"
"s_mov_b32 s37, -1\n\t"
"s_mov_b32 s36, 0\n\t"
"s_mul_i32 s67, s73, 48\n\t"
"s_add_u32 s67, s67, 118784\n\t"
"s_mov_b32 s10, 0x200000\n\t"
"s_mov_b32 s11, 0x20000\n\t"
"v_min_u32 v189, 47, v204\n\t"
"v_add_u32 v189, s67, v189\n\t"
"v_mul_u32_u24 v189, 16, v189\n\t"
"s_mov_b32 s54, 663552\n\t"
"s_mov_b32 s55, 0x20000\n\t"
"s_mov_b32 s14, 100663296\n\t"
"s_mov_b32 s15, 0x20000\n\t"
"s_add_u32 m0, s64, 0\n\t"
"s_waitcnt lgkmcnt(0)\n\t"
"s_mov_b32 s52, s82\n\t"
"s_and_b32 s53, s83, 0xffff\n\t"
"buffer_load_dwordx4 v41, s[52:55], s61 offen lds\n\t"
"buffer_load_short_d16_hi v40, v45, s[52:55], s62 offen\n\t"
"s_mov_b32 s8, s78\n\t"
"s_and_b32 s9, s79, 0xffff\n\t"
"buffer_load_dword v188, v189, s[8:11], 0 offen sc1\n\t"
"s_mov_b32 s12, s76\n\t"
"s_and_b32 s13, s77, 0xffff\n\t"
"s_add_u32 m0, s26, 103936\n\t"
"s_nop 0\n\t"
"buffer_load_dwordx4 v241, s[12:15], s22 offen offset:0 nt lds\n\t"
"buffer_load_dwordx4 v242, s[12:15], s22 offen offset:1024 nt lds\n\t"
"buffer_load_dwordx4 v243, s[12:15], s22 offen offset:2048 nt lds\n\t"
"buffer_load_dwordx4 v244, s[12:15], s22 offen offset:3072 nt lds\n\t"
"s_mov_b64 exec, s[36:37]\n\t"
"buffer_load_dwordx4 v245, s[12:15], s22 offen offset:3584 nt lds\n\t"
"s_mov_b64 exec, -1\n\t"
"s_mul_i32 s49, s73, 36\n\t"
"s_mul_i32 s43, s73, 576\n\t"
"s_sub_u32 s42, s22, 203520\n\t"
"s_mov_b32 s44, -1\n\t"
"s_mov_b32 s45, 15\n\t"
"v_min_u32 v124, 35, v204\n\t"
"v_add_u32 v124, s49, v124\n\t"
"v_lshrrev_b32 v119, 3, v124\n\t"
"v_and_b32 v124, 7, v124\n\t"
"v_mul_u32_u24 v127, 0x300, v119\n\t"
"v_cmp_gt_u32 vcc, 9, v119\n\t"
"v_add_u32 v119, -9, v119\n\t"
"v_ashrrev_i32 v125, 31, v119\n\t"
"v_add_u32 v127, s42, v127\n\t"
"v_mad_i32_i24 v127, v125, s48, v127\n\t"
"v_and_b32 v125, 9, v125\n\t"
"v_add_u32 v119, v119, v125\n\t"
"v_and_b32 v125, 2, v119\n\t"
"v_xor_b32 v124, v124, v125\n\t"
"v_lshl_add_u32 v133, v124, 4, v127\n\t"
"s_cmp_eq_u32 s60, 0\n\t"
"s_cselect_b64 s[50:51], vcc, 0\n\t"
"v_cmp_eq_u32 vcc, 0, v119\n\t"
"v_cndmask_b32 v246, v133, 2.0, s[50:51]\n\t"
"s_and_b64 vcc, vcc, s[46:47]\n\t"
"v_cndmask_b32 v246, v246, 2.0, vcc\n\t"
"s_add_u32 m0, s43, 101632\n\t"
"s_mov_b64 exec, s[44:45]\n\t"
"buffer_load_dwordx4 v246, s[12:15], 0 offen offset:0 nt lds\n\t"
"s_mov_b64 exec, -1\n\t"
"s_mul_i32 s70, s66, 16\n\t"
"s_add_u32 s70, s70, 1900544\n\t"
"s_lshl_b32 s61, s73, 8\n\t"
"s_or_b32 s61, s61, s66\n\t"
"s_cmp_lt_u32 s61, 192\n\t"
"s_cselect_b32 s70, s70, 2.0\n\t"
"s_lshr_b32 s61, s66, 3\n\t"
"s_lshl2_add_u32 s61, s61, s73\n\t"
"s_lshl_b32 s60, s61, 8\n\t"
"s_cmp_lt_u32 s61, 96\n\t"
"s_cselect_b32 s60, s60, 2.0\n\t"
"v_lshrrev_b32 v118, 1, v204\n\t"
"v_mul_u32_u24 v118, 220928, v118\n\t"
"v_lshl_add_u32 v118, v204, 7, v118\n\t"
"s_mov_b64 exec, 63\n\t"
"buffer_load_dword v118, v118, s[52:55], s60 offen\n\t"
"s_mov_b64 exec, -1\n\t"
"buffer_load_dword v187, off, s[8:11], s70 sc1\n\t"
"v_add_u32 v107, 0, v204\n\t"
"v_mul_u32_u24 v104, 0xab, v107\n\t"
"v_lshrrev_b32 v104, 10, v104\n\t"
"v_mul_u32_u24 v104, 0x120, v104\n\t"
"v_lshl_add_u32 v104, v107, 4, v104\n\t"
"v_add_u32 v107, 64, v204\n\t"
"v_mul_u32_u24 v105, 0xab, v107\n\t"
"v_lshrrev_b32 v105, 10, v105\n\t"
"v_mul_u32_u24 v105, 0x120, v105\n\t"
"v_lshl_add_u32 v105, v107, 4, v105\n\t"
"v_add_u32 v107, 128, v204\n\t"
"v_mul_u32_u24 v106, 0xab, v107\n\t"
"v_lshrrev_b32 v106, 10, v106\n\t"
"v_mul_u32_u24 v106, 0x120, v106\n\t"
"v_lshl_add_u32 v106, v107, 4, v106\n\t"
"s_mul_i32 s74, s73, 20736\n\t"
"s_mul_i32 s72, s73, 96\n\t"
"s_add_u32 s74, s74, 33280\n\t"
"s_add_u32 s75, s74, 12288\n\t"
"s_cmp_eq_u32 s73, 3\n\t"
"s_cselect_b32 s74, 16544, s74\n\t"
"s_cselect_b32 s75, 95488, s75\n\t"
"s_lshr_b32 s60, s66, 5\n\t"
"s_bfe_u32 s62, s66, 0x20003\n\t"
"s_and_b32 s61, s60, 1\n\t"
"s_lshl1_add_u32 s61, s73, s61\n\t"
"s_mul_i32 s62, s62, 288\n\t"
"s_add_u32 s61, s61, s62\n\t"
"s_lshr_b32 s60, s60, 1\n\t"
"s_mul_i32 s62, s60, 96\n\t"
"s_add_u32 s61, s61, s62\n\t"
"s_lshl_b32 s57, s61, 10\n\t"
"s_and_b32 s61, s66, 7\n\t"
"s_lshl_b32 s61, s61, 7\n\t"
"s_add_u32 s57, s57, s61\n\t"
"s_cmp_lt_u32 s73, 3\n\t"
"s_cselect_b32 s67, s66, 0xff\n\t"
"s_cmp_lt_u32 s67, 192\n\t"
"s_cselect_b32 s57, s57, 2.0\n\t"
"s_lshl_b32 s60, s73, 3\n\t"
"s_add_u32 s60, s60, 0\n\t"
"s_lshl_b32 s61, s73, 11\n\t"
"s_add_u32 s61, s61, 4096\n\t"
"v_lshl_add_u32 v46, v121, 6, s60\n\t"
"v_lshl_add_u32 v46, v44, 1, v46\n\t"
"v_lshl_add_u32 v42, v204, 1, s61\n\t"
"v_lshl_add_u32 v43, v204, 4, s61\n\t"
"v_mul_u32_u24 v44, 6144, v120\n\t"
"v_lshl_add_u32 v44, v121, 4, v44\n\t"
"v_mov_b32 v32, 0\n\t"
"v_mov_b32 v33, 0\n\t"
"v_mov_b32 v34, 0\n\t"
"v_mov_b32 v35, 0\n\t"
"v_mov_b32 v36, 0\n\t"
"v_mov_b32 v37, 0\n\t"
"v_mov_b32 v38, 0\n\t"
"v_mov_b32 v39, 0\n\t"
"s_waitcnt vmcnt(9)\n\t"
"s_barrier\n\t"
".p2align 3\n\t"
"ds_read_u16_d16_hi v32, v46 offset:0\n\t"
"ds_read_u16_d16_hi v33, v46 offset:512\n\t"
"ds_read_u16_d16_hi v34, v46 offset:1024\n\t"
"ds_read_u16_d16_hi v35, v46 offset:1536\n\t"
"ds_read_u16_d16_hi v36, v46 offset:2048\n\t"
"ds_read_u16_d16_hi v37, v46 offset:2560\n\t"
"ds_read_u16_d16_hi v38, v46 offset:3072\n\t"
"ds_read_u16_d16_hi v39, v46 offset:3584\n\t"
"s_waitcnt lgkmcnt(1)\n\t"
"v_add_f32 v48, v32, v38\n\t"
"s_waitcnt lgkmcnt(0)\n\t"
"v_add_f32 v49, v33, v39\n\t"
"v_add_f32 v50, v34, v40\n\t"
"v_add_f32 v51, v48, v35\n\t"
"v_sub_f32 v54, v48, v35\n\t"
"v_add_f32 v52, v49, v36\n\t"
"v_sub_f32 v55, v49, v36\n\t"
"v_add_f32 v53, v50, v37\n\t"
"v_sub_f32 v56, v50, v37\n\t"
"v_add_f32 v57, v32, v34\n\t"
"v_add_f32 v58, v51, v53\n\t"
"v_add_f32 v59, v54, v56\n\t"
"v_add_f32 v60, v38, v40\n\t"
"v_add_f32 v62, v57, v33\n\t"
"v_sub_f32 v63, v57, v33\n\t"
"v_add_f32 v64, v58, v52\n\t"
"v_sub_f32 v65, v58, v52\n\t"
"v_add_f32 v66, v59, v55\n\t"
"v_sub_f32 v67, v59, v55\n\t"
"v_add_f32 v68, v60, v39\n\t"
"v_sub_f32 v69, v60, v39\n\t"
"v_mul_f32 v71, 0.5, v62\n\t"
"v_mul_f32 v72, 0.5, v63\n\t"
"v_mul_f32 v74, 0.5, v51\n\t"
"v_mul_f32 v75, 0x3e800000, v64\n\t"
"v_mul_f32 v76, 0x3e800000, v65\n\t"
"v_mul_f32 v77, -0.5, v53\n\t"
"v_mul_f32 v79, 0.5, v66\n\t"
"v_mul_f32 v80, 0.5, v67\n\t"
"v_mul_f32 v83, -0.5, v68\n\t"
"v_mul_f32 v84, -0.5, v69\n\t"
".p2align 3\n\t"
"v_cvt_pk_bf16_f32 v86, v32, v71\n\t"
"v_cvt_pk_bf16_f32 v87, v72, -v34\n\t"
"v_cvt_pk_bf16_f32 v88, v74, v75\n\t"
"v_cvt_pk_bf16_f32 v89, v76, v77\n\t"
"v_cvt_pk_bf16_f32 v90, v54, v79\n\t"
"v_cvt_pk_bf16_f32 v91, v80, -v56\n\t"
"v_cvt_pk_bf16_f32 v92, -v38, v83\n\t"
"v_cvt_pk_bf16_f32 v93, v84, v40\n\t"
"ds_write_b16 v42, v86 offset:0\n\t"
"ds_write_b16_d16_hi v42, v86 offset:128\n\t"
"ds_write_b16 v42, v87 offset:256\n\t"
"ds_write_b16_d16_hi v42, v87 offset:384\n\t"
"ds_write_b16 v42, v88 offset:512\n\t"
"ds_write_b16_d16_hi v42, v88 offset:640\n\t"
"ds_write_b16 v42, v89 offset:768\n\t"
"ds_write_b16_d16_hi v42, v89 offset:896\n\t"
"ds_write_b16 v42, v90 offset:1024\n\t"
"ds_write_b16_d16_hi v42, v90 offset:1152\n\t"
"ds_write_b16 v42, v91 offset:1280\n\t"
"ds_write_b16_d16_hi v42, v91 offset:1408\n\t"
"ds_write_b16 v42, v92 offset:1536\n\t"
"ds_write_b16_d16_hi v42, v92 offset:1664\n\t"
"ds_write_b16 v42, v93 offset:1792\n\t"
"ds_write_b16_d16_hi v42, v93 offset:1920\n\t"
"ds_read_b128 v[96:99], v43 offset:0\n\t"
"ds_read_b128 v[100:103], v43 offset:1024\n\t"
"s_add_u32 s60, s57, 0\n\t"
"s_waitcnt lgkmcnt(1)\n\t"
"buffer_store_dwordx4 v[96:99], v44, s[8:11], s60 offen sc1\n\t"
"s_add_u32 s60, s57, 49152\n\t"
"s_waitcnt lgkmcnt(0)\n\t"
"buffer_store_dwordx4 v[100:103], v44, s[8:11], s60 offen sc1\n\t"
"v_lshrrev_b32 v123, 4, v204\n\t"
"v_bfe_u32 v124, v204, 2, 2\n\t"
"v_and_b32 v125, 3, v204\n\t"
"v_lshlrev_b32 v125, 3, v125\n\t"
"v_and_b32 v138, 49, v204\n\t"
"v_lshl_or_b32 v138, v124, 1, v138\n\t"
"v_and_b32 v139, 2, v204\n\t"
"v_lshl_or_b32 v138, v139, 2, v138\n\t"
"s_add_u32 s63, s26, 1152\n\t"
"v_and_b32 v126, 48, v204\n\t"
"v_and_b32 v127, 6, v204\n\t"
"v_lshl_add_u32 v126, v127, 5, v126\n\t"
"v_and_b32 v127, 8, v204\n\t"
"s_add_u32 m0, s74, 0\n\t"
"s_add_u32 s27, s72, 0\n\t"
"buffer_load_dwordx4 v104, s[52:55], s27 offen lds\n\t"
"v_mul_u32_u24 v134, 516, v127\n\t"
"v_and_b32 v127, 1, v204\n\t"
"v_mul_u32_u24 v127, 8320, v127\n\t"
"v_add3_u32 v224, v134, v126, v127\n\t"
"v_and_b32 v127, 16, v204\n\t"
"v_lshl_add_u32 v127, v127, 1, v125\n\t"
"v_lshl_add_u32 v127, v123, 8, v127\n\t"
"v_add_u32 v126, 4, v204\n\t"
"v_and_b32 v126, 8, v126\n\t"
"v_lshl_add_u32 v127, v126, 3, v127\n\t"
"v_mul_u32_u24 v126, 1152, v124\n\t"
"v_add3_u32 v226, v126, v127, s63\n\t"
"s_add_u32 m0, s74, 1024\n\t"
"s_add_u32 s27, s72, 0\n\t"
"buffer_load_dwordx4 v105, s[52:55], s27 offen lds\n\t"
"v_min_u32 v126, 2, v124\n\t"
"v_cmp_gt_u32 vcc, 48, v204\n\t"
"s_mov_b32 s48, 384\n\t"
"s_cmp_eq_u32 s65, 31\n\t"
"v_cndmask_b32 v126, v126, v123, vcc\n\t"
"v_cndmask_b32 v127, 4, v124, vcc\n\t"
"v_and_b32 v134, 1, v126\n\t"
"v_lshl_add_u32 v134, v134, 6, v125\n\t"
"v_lshl_add_u32 v134, v127, 7, v134\n\t"
"v_and_b32 v127, 2, v127\n\t"
"v_lshl_add_u32 v134, v127, 4, v134\n\t"
"v_mul_u32_u24 v126, 2304, v126\n\t"
"v_add3_u32 v227, v126, v134, s26\n\t"
"v_lshlrev_b32 v126, 14, v204\n\t"
"v_and_b32 v126, 0x8000, v126\n\t"
"v_or_b32 v134, 0x3f803f80, v126\n\t"
"v_and_b32 v126, 1, v204\n\t"
"v_cmp_eq_u32 vcc, 0, v126\n\t"
"s_cselect_b64 s[40:41], 0xff, 0\n\t"
"s_cselect_b64 s[46:47], -1, 0\n\t"
"s_add_u32 m0, s74, 2048\n\t"
"s_add_u32 s27, s72, 0\n\t"
"buffer_load_dwordx4 v106, s[52:55], s27 offen lds\n\t"
"v_cndmask_b32 v135, v134, 0, vcc\n\t"
"v_cndmask_b32 v134, 0, v134, vcc\n\t"
"v_xor_b32 v136, 0x80008000, v134\n\t"
"v_xor_b32 v137, 0x80008000, v135\n\t"
"s_add_u32 m0, s74, 3072\n\t"
"s_add_u32 s27, s72, 12288\n\t"
"buffer_load_dwordx4 v104, s[52:55], s27 offen lds\n\t"
"v_accvgpr_write_b32 a220, v134\n\t"
"v_accvgpr_write_b32 a221, v135\n\t"
"v_accvgpr_write_b32 a222, v136\n\t"
"v_accvgpr_write_b32 a223, v137\n\t"
"v_xad_u32 v236, v128, 64, s48\n\t"
"v_xad_u32 v237, v129, 64, s48\n\t"
"v_cndmask_b32 v237, v237, 2.0, s[40:41]\n\t"
"s_lshl_b64 s[40:41], s[40:41], 8\n\t"
"v_xad_u32 v238, v130, 64, s48\n\t"
"v_cndmask_b32 v238, v238, 2.0, s[40:41]\n\t"
"s_lshl_b64 s[40:41], s[40:41], 8\n\t"
"v_xad_u32 v239, v131, 64, s48\n\t"
"v_cndmask_b32 v239, v239, 2.0, s[40:41]\n\t"
"s_add_u32 m0, s74, 4096\n\t"
"s_add_u32 s27, s72, 12288\n\t"
"buffer_load_dwordx4 v105, s[52:55], s27 offen lds\n\t"
"s_lshl_b64 s[40:41], s[40:41], 40\n\t"
"v_xad_u32 v240, v245, 64, s48\n\t"
"v_cndmask_b32 v240, v240, 2.0, s[40:41]\n\t"
"v_cmp_eq_u32 vcc, 8, v119\n\t"
"v_xad_u32 v247, v133, 64, s48\n\t"
"s_and_b64 vcc, vcc, s[46:47]\n\t"
"v_cndmask_b32 v247, v247, 2.0, s[50:51]\n\t"
"v_cndmask_b32 v247, v247, 2.0, vcc\n\t"
"v_and_b32 v139, 7, v138\n\t"
"v_bfe_u32 v126, v138, 3, 1\n\t"
"v_lshrrev_b32 v127, 4, v138\n\t"
"v_sub_u32 v127, v127, v126\n\t"
"s_add_u32 m0, s74, 5120\n\t"
"s_add_u32 s27, s72, 12288\n\t"
"buffer_load_dwordx4 v106, s[52:55], s27 offen lds\n\t"
"v_mov_b32 v134, 0xbf803f80\n\t"
"v_mov_b32 v135, 0x3f80bf80\n\t"
"v_cmp_eq_u32 vcc, 0, v127\n\t"
"v_add_u32 v126, 4, v126\n\t"
"s_nop 0\n\t"
"v_cndmask_b32 v134, 0, v134, vcc\n\t"
"v_cmp_eq_u32 vcc, 1, v127\n\t"
"s_nop 1\n\t"
"v_cndmask_b32 v134, v134, v135, vcc\n\t"
"v_cmp_eq_u32 vcc, 7, v139\n\t"
"s_nop 1\n\t"
"v_cndmask_b32 v135, 0, v135, vcc\n\t"
"v_cmp_gt_u32 vcc, 48, v138\n\t"
"s_nop 1\n\t"
"v_cndmask_b32 v134, v135, v134, vcc\n\t"
"v_mov_b32 v135, 0\n\t"
"s_add_u32 m0, s74, 6144\n\t"
"s_add_u32 s27, s72, 442368\n\t"
"buffer_load_dwordx4 v104, s[52:55], s27 offen lds\n\t"
"v_cndmask_b32 v136, v126, v139, vcc\n\t"
"v_and_b32 v137, 1, v136\n\t"
"v_lshlrev_b32 v137, 4, v137\n\t"
"v_lshrrev_b32 v136, 1, v136\n\t"
"v_lshlrev_b64 v[134:135], v137, v[134:135]\n\t"
"v_cmp_eq_u32 vcc, 0, v136\n\t"
"s_nop 1\n\t"
"v_cndmask_b32 v140, 0, v134, vcc\n\t"
"v_accvgpr_write_b32 a224, v140\n\t"
"v_cndmask_b32 v141, 0, v135, vcc\n\t"
"v_cmp_eq_u32 vcc, 1, v136\n\t"
"v_and_b32 v140, 0xff7fff7f, v140\n\t"
"v_xor_b32 v148, 0x80008000, v140\n\t"
"s_add_u32 m0, s74, 7168\n\t"
"s_add_u32 s27, s72, 442368\n\t"
"buffer_load_dwordx4 v105, s[52:55], s27 offen lds\n\t"
"v_cndmask_b32 v141, v141, v134, vcc\n\t"
"v_cndmask_b32 v142, 0, v135, vcc\n\t"
"v_cmp_eq_u32 vcc, 2, v136\n\t"
"v_accvgpr_write_b32 a225, v141\n\t"
"v_accvgpr_write_b32 a216, v140\n\t"
"v_cndmask_b32 v142, v142, v134, vcc\n\t"
"v_cndmask_b32 v143, 0, v135, vcc\n\t"
"v_cmp_eq_u32 vcc, 3, v136\n\t"
"v_accvgpr_write_b32 a226, v142\n\t"
"v_and_b32 v141, 0xff7fff7f, v141\n\t"
"v_cndmask_b32 v143, v143, v134, vcc\n\t"
"v_accvgpr_write_b32 a227, v143\n\t"
"v_xor_b32 v149, 0x80008000, v141\n\t"
"s_add_u32 m0, s74, 8192\n\t"
"s_add_u32 s27, s72, 442368\n\t"
"buffer_load_dwordx4 v106, s[52:55], s27 offen lds\n\t"
"v_accvgpr_write_b32 a217, v141\n\t"
"v_and_b32 v142, 0xff7fff7f, v142\n\t"
"v_xor_b32 v150, 0x80008000, v142\n\t"
"v_accvgpr_write_b32 a218, v142\n\t"
"v_and_b32 v143, 0xff7fff7f, v143\n\t"
"v_xor_b32 v151, 0x80008000, v143\n\t"
"v_accvgpr_write_b32 a219, v143\n\t"
"s_mov_b32 s68, 3000\n\t"
"v_and_b32 v107, 51, v204\n\t"
"v_mul_u32_u24 v107, 24, v107\n\t"
"v_and_b32 v47, 8, v204\n\t"
"v_mad_u32_u24 v107, v47, 12, v107\n\t"
"s_add_u32 m0, s74, 9216\n\t"
"s_add_u32 s27, s72, 454656\n\t"
"buffer_load_dwordx4 v104, s[52:55], s27 offen lds\n\t"
"v_and_b32 v47, 4, v204\n\t"
"v_mul_u32_u24 v47, 0x180, v47\n\t"
"v_add_u32 v107, v47, v107\n\t"
"v_add_u32 v47, s75, v107\n\t"
"v_add_u32 v107, s74, v107\n\t"
"v_lshrrev_b32 v112, 4, v204\n\t"
"v_bfe_u32 v113, v204, 2, 2\n\t"
"v_cmp_eq_u32 vcc, v112, v113\n\t"
"v_lshlrev_b32 v112, 4, v204\n\t"
"v_mov_b32 v113, 0x3e80\n\t"
"v_lshlrev_b32 v113, v112, v113\n\t"
"v_cndmask_b32 v113, 0, v113, vcc\n\t"
"s_add_u32 m0, s74, 10240\n\t"
"s_add_u32 s27, s72, 454656\n\t"
"buffer_load_dwordx4 v105, s[52:55], s27 offen lds\n\t"
"v_and_b32 v112, 2, v204\n\t"
"v_cmp_eq_u32 vcc, 0, v112\n\t"
"v_mov_b32 v108, 0\n\t"
"v_mov_b32 v109, 0\n\t"
"v_cndmask_b32 v110, 0, v113, vcc\n\t"
"v_cndmask_b32 v111, v113, 0, vcc\n\t"
"v_xor_b32 v114, 0x80008000, v110\n\t"
"v_xor_b32 v115, 0x80008000, v111\n\t"
"v_mov_b32 v112, 0\n\t"
"v_mov_b32 v113, 0\n\t"
"v_mov_b32 v116, 0\n\t"
"v_mov_b32 v117, 0\n\t"
"s_add_u32 m0, s74, 11264\n\t"
"s_add_u32 s27, s72, 454656\n\t"
"buffer_load_dwordx4 v106, s[52:55], s27 offen lds\n\t"
"s_waitcnt vmcnt(16)\n\t"
"s_waitcnt vmcnt(12)\n\t"
"v_readfirstlane_b32 s71, v187\n\t"
"s_barrier\n\t"
"s_add_u32 s71, s71, 1\n\t"
"v_mov_b32 v215, s71\n\t"
"s_mov_b64 exec, 1\n\t"
"buffer_store_dword v215, off, s[8:11], s70 sc1\n\t"
"s_mov_b64 exec, -1\n\t"
"v_add_u32 v248, 101632, v226\n\t"
"v_xor_b32 v249, 32, v248\n\t"
"v_xor_b32 v250, 64, v248\n\t"
"v_xor_b32 v251, 96, v248\n\t"
"ds_read_b64_tr_b16 a[178:179], v248 offset:128\n\t"
"ds_read_b64_tr_b16 a[186:187], v249 offset:128\n\t"
"s_add_u32 m0, s75, 0\n\t"
"s_add_u32 s27, s72, 221184\n\t"
"buffer_load_dwordx4 v104, s[52:55], s27 offen lds\n\t"
"ds_read_b64_tr_b16 a[194:195], v250 offset:128\n\t"
"ds_read_b64_tr_b16 a[202:203], v251 offset:128\n\t"
"ds_read_b64_tr_b16 a[176:177], v248 offset:0\n\t"
"s_add_u32 m0, s75, 1024\n\t"
"s_add_u32 s27, s72, 221184\n\t"
"buffer_load_dwordx4 v105, s[52:55], s27 offen lds\n\t"
"ds_read_b64_tr_b16 a[184:185], v249 offset:0\n\t"
"ds_read_b64_tr_b16 a[180:181], v249 offset:256\n\t"
"ds_read_b64_tr_b16 a[188:189], v248 offset:256\n\t"
"s_add_u32 m0, s75, 2048\n\t"
"s_add_u32 s27, s72, 221184\n\t"
"buffer_load_dwordx4 v106, s[52:55], s27 offen lds\n\t"
"ds_read_b64_tr_b16 a[192:193], v250 offset:0\n\t"
"ds_read_b64_tr_b16 a[200:201], v251 offset:0\n\t"
"ds_read_b64_tr_b16 a[196:197], v251 offset:256\n\t"
"s_add_u32 m0, s75, 3072\n\t"
"s_add_u32 s27, s72, 233472\n\t"
"buffer_load_dwordx4 v104, s[52:55], s27 offen lds\n\t"
"ds_read_b64_tr_b16 a[204:205], v250 offset:256\n\t"
"s_waitcnt lgkmcnt(11)\n\t"
"v_mfma_f32_4x4x4_16b_bf16 v[8:11], a[220:221], a[178:179], 0\n\t"
"s_waitcnt lgkmcnt(10)\n\t"
"v_mfma_f32_4x4x4_16b_bf16 v[12:15], a[220:221], a[186:187], 0\n\t"
"s_waitcnt lgkmcnt(9)\n\t"
"v_mfma_f32_4x4x4_16b_bf16 v[24:27], a[220:221], a[194:195], 0\n\t"
"s_waitcnt lgkmcnt(8)\n\t"
"v_mfma_f32_4x4x4_16b_bf16 v[28:31], a[220:221], a[202:203], 0\n\t"
"s_waitcnt lgkmcnt(7)\n\t"
"v_mfma_f32_4x4x4_16b_bf16 v[0:3], a[222:223], a[176:177], v[8:11]\n\t"
"s_waitcnt lgkmcnt(6)\n\t"
"v_mfma_f32_4x4x4_16b_bf16 v[4:7], a[222:223], a[184:185], v[12:15]\n\t"
"s_add_u32 m0, s75, 4096\n\t"
"s_add_u32 s27, s72, 233472\n\t"
"buffer_load_dwordx4 v105, s[52:55], s27 offen lds\n\t"
"s_add_u32 m0, s75, 5120\n\t"
"s_add_u32 s27, s72, 233472\n\t"
"buffer_load_dwordx4 v106, s[52:55], s27 offen lds\n\t"
"s_waitcnt lgkmcnt(5)\n\t"
"v_mfma_f32_4x4x4_16b_bf16 v[8:11], a[222:223], a[180:181], v[8:11]\n\t"
"s_waitcnt lgkmcnt(4)\n\t"
"v_mfma_f32_4x4x4_16b_bf16 v[12:15], a[222:223], a[188:189], v[12:15]\n\t"
"s_waitcnt lgkmcnt(3)\n\t"
"v_mfma_f32_4x4x4_16b_bf16 v[16:19], a[222:223], a[192:193], v[24:27]\n\t"
"s_waitcnt lgkmcnt(2)\n\t"
"v_mfma_f32_4x4x4_16b_bf16 v[20:23], a[222:223], a[200:201], v[28:31]\n\t"
"s_waitcnt lgkmcnt(1)\n\t"
"v_mfma_f32_4x4x4_16b_bf16 v[24:27], a[222:223], a[196:197], v[24:27]\n\t"
"s_waitcnt lgkmcnt(0)\n\t"
"v_mfma_f32_4x4x4_16b_bf16 v[28:31], a[222:223], a[204:205], v[28:31]\n\t"
"s_add_u32 m0, s26, 124544\n\t"
"s_nop 0\n\t"
"buffer_load_dwordx4 v241, s[12:15], s22 offen offset:128 nt lds\n\t"
"buffer_load_dwordx4 v242, s[12:15], s22 offen offset:1152 nt lds\n\t"
"v_cvt_pk_bf16_f32 v0, -v0, -v4\n\t"
"v_cvt_pk_bf16_f32 v1, -v1, -v5\n\t"
"v_cvt_pk_bf16_f32 v2, -v2, -v6\n\t"
"v_cvt_pk_bf16_f32 v3, -v3, -v7\n\t"
"s_lshl_b32 m0, s73, 8\n\t"
"s_nop 0\n\t"
".p2align 3\n\t"
"ds_write_addtid_b32 v0 offset:0\n\t"
"ds_write_addtid_b32 v1 offset:4128\n\t"
"ds_write_addtid_b32 v2 offset:1024\n\t"
"ds_write_addtid_b32 v3 offset:5152\n\t"
"v_cvt_pk_bf16_f32 v8, v8, v12\n\t"
"v_cvt_pk_bf16_f32 v9, v9, v13\n\t"
"v_cvt_pk_bf16_f32 v10, v10, v14\n\t"
"v_cvt_pk_bf16_f32 v11, v11, v15\n\t"
"ds_write_addtid_b32 v8 offset:8320\n\t"
"ds_write_addtid_b32 v9 offset:12448\n\t"
"ds_write_addtid_b32 v10 offset:9344\n\t"
"ds_write_addtid_b32 v11 offset:13472\n\t"
"v_cvt_pk_bf16_f32 v16, -v16, -v20\n\t"
"v_cvt_pk_bf16_f32 v17, -v17, -v21\n\t"
"v_cvt_pk_bf16_f32 v18, -v18, -v22\n\t"
"v_cvt_pk_bf16_f32 v19, -v19, -v23\n\t"
"ds_write_addtid_b32 v16 offset:2048\n\t"
"ds_write_addtid_b32 v17 offset:6176\n\t"
"ds_write_addtid_b32 v18 offset:3072\n\t"
"ds_write_addtid_b32 v19 offset:7200\n\t"
"v_cvt_pk_bf16_f32 v24, v24, v28\n\t"
"v_cvt_pk_bf16_f32 v25, v25, v29\n\t"
"v_cvt_pk_bf16_f32 v26, v26, v30\n\t"
"v_cvt_pk_bf16_f32 v27, v27, v31\n\t"
"ds_write_addtid_b32 v24 offset:10368\n\t"
"ds_write_addtid_b32 v25 offset:14496\n\t"
"ds_write_addtid_b32 v26 offset:11392\n\t"
"ds_write_addtid_b32 v27 offset:15520\n\t"
"v_add_u32 v248, 101632, v227\n\t"
"v_xor_b32 v249, 32, v248\n\t"
"v_xor_b32 v250, 64, v248\n\t"
"v_xor_b32 v251, 96, v248\n\t"
"ds_read_b64_tr_b16 a[176:177], v248 offset:0\n\t"
"ds_read_b64_tr_b16 a[178:179], v248 offset:512\n\t"
"ds_read_b64_tr_b16 a[180:181], v249 offset:0\n\t"
"ds_read_b64_tr_b16 a[182:183], v249 offset:512\n\t"
"s_add_u32 m0, s26, 124544\n\t"
"s_nop 0\n\t"
".p2align 3\n\t"
"buffer_load_dwordx4 v243, s[12:15], s22 offen offset:2176 nt lds\n\t"
"ds_read_b64_tr_b16 a[184:185], v250 offset:0\n\t"
"ds_read_b64_tr_b16 a[186:187], v250 offset:512\n\t"
"ds_read_b64_tr_b16 a[188:189], v251 offset:0\n\t"
"ds_read_b64_tr_b16 a[190:191], v251 offset:512\n\t"
"buffer_load_dwordx4 v244, s[12:15], s22 offen offset:3200 nt lds\n\t"
"ds_read_b64_tr_b16 a[192:193], v248 offset:1152\n\t"
"ds_read_b64_tr_b16 a[194:195], v248 offset:1664\n\t"
"ds_read_b64_tr_b16 a[196:197], v249 offset:1152\n\t"
"ds_read_b64_tr_b16 a[198:199], v249 offset:1664\n\t"
"s_mov_b64 exec, s[36:37]\n\t"
"buffer_load_dwordx4 v245, s[12:15], s22 offen offset:3712 nt lds\n\t"
"s_mov_b64 exec, -1\n\t"
"ds_read_b64_tr_b16 a[200:201], v250 offset:1152\n\t"
"ds_read_b64_tr_b16 a[202:203], v250 offset:1664\n\t"
"ds_read_b64_tr_b16 a[204:205], v251 offset:1152\n\t"
"ds_read_b64_tr_b16 a[206:207], v251 offset:1664\n\t"
"s_add_u32 m0, s43, 122240\n\t"
"s_mov_b64 exec, s[44:45]\n\t"
"buffer_load_dwordx4 v246, s[12:15], 0 offen offset:128 nt lds\n\t"
"s_mov_b64 exec, -1\n\t"
"s_mov_b64 exec, 0\n\t"
"s_cmp_eq_u32 s73, 0\n\t"
"s_cbranch_scc1 Lpad3_%=\n\t"
"s_cmp_eq_u32 s73, 1\n\t"
"s_cbranch_scc1 Lpad2_%=\n\t"
"s_cmp_eq_u32 s73, 2\n\t"
"s_cbranch_scc1 Lpad1_%=\n\t"
"s_branch Lpad0_%=\n\t"
".fill 7, 4, 0xbf800000\n\t"
"Lret0_%=:\n\t"
"s_mov_b64 exec, -1\n\t"
"s_waitcnt vmcnt(19)\n\t"
".p2align 3\n\t"
"ds_read_b64_tr_b16 a[48:49], v107 offset:0\n\t"
"ds_read_b64_tr_b16 a[50:51], v107 offset:192\n\t"
"ds_read_b64_tr_b16 a[52:53], v107 offset:3072\n\t"
"ds_read_b64_tr_b16 a[54:55], v107 offset:3264\n\t"
"ds_read_b64_tr_b16 a[56:57], v107 offset:8\n\t"
"ds_read_b64_tr_b16 a[58:59], v107 offset:200\n\t"
"ds_read_b64_tr_b16 a[60:61], v107 offset:3080\n\t"
"ds_read_b64_tr_b16 a[62:63], v107 offset:3272\n\t"
"ds_read_b64_tr_b16 a[64:65], v107 offset:16\n\t"
"ds_read_b64_tr_b16 a[66:67], v107 offset:208\n\t"
"ds_read_b64_tr_b16 a[68:69], v107 offset:3088\n\t"
"ds_read_b64_tr_b16 a[70:71], v107 offset:3280\n\t"
"s_waitcnt vmcnt(13)\n\t"
".p2align 3\n\t"
"ds_read_b64_tr_b16 a[72:73], v107 offset:6144\n\t"
"ds_read_b64_tr_b16 a[74:75], v107 offset:6336\n\t"
"ds_read_b64_tr_b16 a[76:77], v107 offset:9216\n\t"
"ds_read_b64_tr_b16 a[78:79], v107 offset:9408\n\t"
"ds_read_b64_tr_b16 a[80:81], v107 offset:6152\n\t"
"ds_read_b64_tr_b16 a[82:83], v107 offset:6344\n\t"
"ds_read_b64_tr_b16 a[84:85], v107 offset:9224\n\t"
"ds_read_b64_tr_b16 a[86:87], v107 offset:9416\n\t"
"ds_read_b64_tr_b16 a[88:89], v107 offset:6160\n\t"
"ds_read_b64_tr_b16 a[90:91], v107 offset:6352\n\t"
"ds_read_b64_tr_b16 a[92:93], v107 offset:9232\n\t"
"ds_read_b64_tr_b16 a[94:95], v107 offset:9424\n\t"
"s_waitcnt lgkmcnt(15)\n\t"
"v_mfma_f32_16x16x32_bf16 v[0:3], v[110:113], a[48:51], 0\n\t"
"s_waitcnt lgkmcnt(10)\n\t"
"v_mfma_f32_16x16x32_bf16 v[0:3], v[110:113], a[72:75], v[0:3]\n\t"
"s_nop 0\n\t"
"v_mfma_f32_16x16x32_bf16 v[8:11], v[108:111], a[48:51], 0\n\t"
"s_nop 0\n\t"
"v_mfma_f32_16x16x32_bf16 v[8:11], v[108:111], a[72:75], v[8:11]\n\t"
"s_add_u32 m0, s26, 145152\n\t"
"s_nop 0\n\t"
"buffer_load_dwordx4 v241, s[12:15], s22 offen offset:256 nt lds\n\t"
"v_mfma_f32_16x16x32_bf16 v[16:19], v[110:113], a[52:55], 0\n\t"
"s_waitcnt lgkmcnt(8)\n\t"
"v_mfma_f32_16x16x32_bf16 v[16:19], v[110:113], a[76:79], v[16:19]\n\t"
"s_nop 0\n\t"
"v_mfma_f32_16x16x32_bf16 v[24:27], v[108:111], a[52:55], 0\n\t"
"s_nop 0\n\t"
"v_mfma_f32_16x16x32_bf16 v[24:27], v[108:111], a[76:79], v[24:27]\n\t"
"buffer_load_dwordx4 v242, s[12:15], s22 offen offset:1280 nt lds\n\t"
"s_waitcnt vmcnt(8)\n\t"
".p2align 3\n\t"
"ds_read_b64_tr_b16 a[0:1], v47 offset:0\n\t"
"ds_read_b64_tr_b16 a[2:3], v47 offset:192\n\t"
"ds_read_b64_tr_b16 a[4:5], v47 offset:3072\n\t"
"ds_read_b64_tr_b16 a[6:7], v47 offset:3264\n\t"
"ds_read_b64_tr_b16 a[8:9], v47 offset:8\n\t"
"ds_read_b64_tr_b16 a[10:11], v47 offset:200\n\t"
"ds_read_b64_tr_b16 a[12:13], v47 offset:3080\n\t"
"ds_read_b64_tr_b16 a[14:15], v47 offset:3272\n\t"
"ds_read_b64_tr_b16 a[16:17], v47 offset:16\n\t"
"ds_read_b64_tr_b16 a[18:19], v47 offset:208\n\t"
"ds_read_b64_tr_b16 a[20:21], v47 offset:3088\n\t"
"ds_read_b64_tr_b16 a[22:23], v47 offset:3280\n\t"
"v_mfma_f32_16x16x32_bf16 v[32:35], v[110:113], a[56:59], 0\n\t"
"s_waitcnt lgkmcnt(15)\n\t"
"v_mfma_f32_16x16x32_bf16 v[32:35], v[110:113], a[80:83], v[32:35]\n\t"
"s_nop 0\n\t"
"v_mfma_f32_16x16x32_bf16 v[96:99], v[108:111], a[56:59], 0\n\t"
"s_nop 0\n\t"
"v_mfma_f32_16x16x32_bf16 v[96:99], v[108:111], a[80:83], v[96:99]\n\t"
"buffer_load_dwordx4 v243, s[12:15], s22 offen offset:2304 nt lds\n\t"
"v_mfma_f32_16x16x32_bf16 v[120:123], v[110:113], a[60:63], 0\n\t"
"s_nop 0\n\t"
"v_mfma_f32_16x16x32_bf16 v[120:123], v[110:113], a[84:87], v[120:123]\n\t"
"s_nop 0\n\t"
"v_mfma_f32_16x16x32_bf16 v[128:131], v[108:111], a[60:63], 0\n\t"
"s_nop 0\n\t"
"v_mfma_f32_16x16x32_bf16 v[128:131], v[108:111], a[84:87], v[128:131]\n\t"
"buffer_load_dwordx4 v244, s[12:15], s22 offen offset:3328 nt lds\n\t"
"v_mfma_f32_16x16x32_bf16 v[136:139], v[110:113], a[64:67], 0\n\t"
"s_waitcnt lgkmcnt(14)\n\t"
"v_mfma_f32_16x16x32_bf16 v[136:139], v[110:113], a[88:91], v[136:139]\n\t"
"s_nop 0\n\t"
"v_mfma_f32_16x16x32_bf16 v[152:155], v[108:111], a[64:67], 0\n\t"
"s_nop 0\n\t"
"v_mfma_f32_16x16x32_bf16 v[152:155], v[108:111], a[88:91], v[152:155]\n\t"
"s_mov_b64 exec, s[36:37]\n\t"
"buffer_load_dwordx4 v245, s[12:15], s22 offen offset:3840 nt lds\n\t"
"s_mov_b64 exec, -1\n\t"
"v_mfma_f32_16x16x32_bf16 v[160:163], v[110:113], a[68:71], 0\n\t"
"s_waitcnt lgkmcnt(12)\n\t"
"v_mfma_f32_16x16x32_bf16 v[160:163], v[110:113], a[92:95], v[160:163]\n\t"
"s_nop 0\n\t"
"v_mfma_f32_16x16x32_bf16 v[168:171], v[108:111], a[68:71], 0\n\t"
"s_nop 0\n\t"
"v_mfma_f32_16x16x32_bf16 v[168:171], v[108:111], a[92:95], v[168:171]\n\t"
"s_add_u32 m0, s43, 142848\n\t"
"s_mov_b64 exec, s[44:45]\n\t"
"buffer_load_dwordx4 v246, s[12:15], 0 offen offset:256 nt lds\n\t"
"s_mov_b64 exec, -1\n\t"
"s_waitcnt lgkmcnt(10)\n\t"
"v_mfma_f32_16x16x32_bf16 v[4:7], v[110:113], a[0:3], v[0:3]\n\t"
"s_nop 0\n\t"
"v_mfma_f32_16x16x32_bf16 v[0:3], v[114:117], a[0:3], v[0:3]\n\t"
"s_nop 0\n\t"
"v_mfma_f32_16x16x32_bf16 v[12:15], v[108:111], a[0:3], v[8:11]\n\t"
"s_nop 0\n\t"
"v_mfma_f32_16x16x32_bf16 v[8:11], v[112:115], a[0:3], v[8:11]\n\t"
"s_waitcnt lgkmcnt(8)\n\t"
"v_mfma_f32_16x16x32_bf16 v[20:23], v[110:113], a[4:7], v[16:19]\n\t"
"s_nop 0\n\t"
"v_mfma_f32_16x16x32_bf16 v[16:19], v[114:117], a[4:7], v[16:19]\n\t"
"s_nop 0\n\t"
"v_mfma_f32_16x16x32_bf16 v[28:31], v[108:111], a[4:7], v[24:27]\n\t"
"s_nop 0\n\t"
"v_cvt_pk_bf16_f32 v48, v4, v5\n\t"
"v_cvt_pk_bf16_f32 v49, v6, v7\n\t"
"v_mfma_f32_16x16x32_bf16 v[24:27], v[112:115], a[4:7], v[24:27]\n\t"
"s_nop 0\n\t"
"v_cvt_pk_bf16_f32 v72, v0, v1\n\t"
"v_cvt_pk_bf16_f32 v73, v2, v3\n\t"
"s_waitcnt lgkmcnt(6)\n\t"
"v_mfma_f32_16x16x32_bf16 v[36:39], v[110:113], a[8:11], v[32:35]\n\t"
"s_nop 0\n\t"
"v_cvt_pk_bf16_f32 v50, v12, v13\n\t"
"v_cvt_pk_bf16_f32 v51, v14, v15\n\t"
"v_mfma_f32_16x16x32_bf16 v[32:35], v[114:117], a[8:11], v[32:35]\n\t"
"s_nop 0\n\t"
"v_cvt_pk_bf16_f32 v74, v8, v9\n\t"
"v_cvt_pk_bf16_f32 v75, v10, v11\n\t"
"v_mfma_f32_16x16x32_bf16 v[100:103], v[108:111], a[8:11], v[96:99]\n\t"
"s_nop 0\n\t"
"v_cvt_pk_bf16_f32 v52, v20, v21\n\t"
"v_cvt_pk_bf16_f32 v53, v22, v23\n\t"
"v_mfma_f32_16x16x32_bf16 v[96:99], v[112:115], a[8:11], v[96:99]\n\t"
"s_nop 0\n\t"
"v_cvt_pk_bf16_f32 v76, v16, v17\n\t"
"v_cvt_pk_bf16_f32 v77, v18, v19\n\t"
"s_waitcnt lgkmcnt(4)\n\t"
"v_mfma_f32_16x16x32_bf16 v[124:127], v[110:113], a[12:15], v[120:123]\n\t"
"s_nop 0\n\t"
"v_cvt_pk_bf16_f32 v54, v28, v29\n\t"
"v_cvt_pk_bf16_f32 v55, v30, v31\n\t"
"v_mfma_f32_16x16x32_bf16 v[120:123], v[114:117], a[12:15], v[120:123]\n\t"
"s_nop 0\n\t"
"v_cvt_pk_bf16_f32 v78, v24, v25\n\t"
"v_cvt_pk_bf16_f32 v79, v26, v27\n\t"
"v_mfma_f32_16x16x32_bf16 v[132:135], v[108:111], a[12:15], v[128:131]\n\t"
"s_nop 0\n\t"
"v_cvt_pk_bf16_f32 v56, v36, v37\n\t"
"v_cvt_pk_bf16_f32 v57, v38, v39\n\t"
"v_mfma_f32_16x16x32_bf16 v[128:131], v[112:115], a[12:15], v[128:131]\n\t"
"s_nop 0\n\t"
"v_cvt_pk_bf16_f32 v80, v32, v33\n\t"
"v_cvt_pk_bf16_f32 v81, v34, v35\n\t"
"s_waitcnt lgkmcnt(2)\n\t"
"v_mfma_f32_16x16x32_bf16 v[140:143], v[110:113], a[16:19], v[136:139]\n\t"
"s_nop 0\n\t"
"v_cvt_pk_bf16_f32 v58, v100, v101\n\t"
"v_cvt_pk_bf16_f32 v59, v102, v103\n\t"
"v_mfma_f32_16x16x32_bf16 v[136:139], v[114:117], a[16:19], v[136:139]\n\t"
"s_nop 0\n\t"
"v_cvt_pk_bf16_f32 v82, v96, v97\n\t"
"v_cvt_pk_bf16_f32 v83, v98, v99\n\t"
"v_mfma_f32_16x16x32_bf16 v[156:159], v[108:111], a[16:19], v[152:155]\n\t"
"s_nop 0\n\t"
"v_cvt_pk_bf16_f32 v60, v124, v125\n\t"
"v_cvt_pk_bf16_f32 v61, v126, v127\n\t"
"v_mfma_f32_16x16x32_bf16 v[152:155], v[112:115], a[16:19], v[152:155]\n\t"
"s_nop 0\n\t"
"v_cvt_pk_bf16_f32 v84, v120, v121\n\t"
"v_cvt_pk_bf16_f32 v85, v122, v123\n\t"
"s_waitcnt lgkmcnt(0)\n\t"
"v_mfma_f32_16x16x32_bf16 v[164:167], v[110:113], a[20:23], v[160:163]\n\t"
"s_nop 0\n\t"
"v_cvt_pk_bf16_f32 v62, v132, v133\n\t"
"v_cvt_pk_bf16_f32 v63, v134, v135\n\t"
"v_mfma_f32_16x16x32_bf16 v[160:163], v[114:117], a[20:23], v[160:163]\n\t"
"s_nop 0\n\t"
"v_cvt_pk_bf16_f32 v86, v128, v129\n\t"
"v_cvt_pk_bf16_f32 v87, v130, v131\n\t"
"v_mfma_f32_16x16x32_bf16 v[172:175], v[108:111], a[20:23], v[168:171]\n\t"
"s_nop 0\n\t"
"v_cvt_pk_bf16_f32 v64, v140, v141\n\t"
"v_cvt_pk_bf16_f32 v65, v142, v143\n\t"
"v_mfma_f32_16x16x32_bf16 v[168:171], v[112:115], a[20:23], v[168:171]\n\t"
".p2align 3\n\t"
"v_cvt_pk_bf16_f32 v88, v136, v137\n\t"
"v_cvt_pk_bf16_f32 v89, v138, v139\n\t"
"v_cvt_pk_bf16_f32 v66, v156, v157\n\t"
"v_cvt_pk_bf16_f32 v67, v158, v159\n\t"
"v_cvt_pk_bf16_f32 v90, v152, v153\n\t"
"v_cvt_pk_bf16_f32 v91, v154, v155\n\t"
"v_cvt_pk_bf16_f32 v68, v164, v165\n\t"
"v_cvt_pk_bf16_f32 v69, v166, v167\n\t"
"v_cvt_pk_bf16_f32 v92, v160, v161\n\t"
"v_cvt_pk_bf16_f32 v93, v162, v163\n\t"
"v_cvt_pk_bf16_f32 v70, v172, v173\n\t"
"v_cvt_pk_bf16_f32 v71, v174, v175\n\t"
"v_cvt_pk_bf16_f32 v94, v168, v169\n\t"
"v_cvt_pk_bf16_f32 v95, v170, v171\n\t"
"s_barrier\n\t"
"v_mfma_f32_16x16x32_bf16 v[192:195], a[216:219], a[176:179], 0\n\t"
"s_add_u32 m0, s43, 53888\n\t"
"ds_read_b128 a[144:147], v224 offset:0\n\t"
"ds_read_b128 a[160:163], v224 offset:2048\n\t"
"v_mfma_f32_16x16x32_bf16 v[196:199], a[216:219], a[180:183], 0\n\t"
"s_mov_b64 exec, s[44:45]\n\t"
"buffer_load_dwordx4 v247, s[12:15], 0 offen offset:128 nt lds\n\t"
"s_mov_b64 exec, -1\n\t"
"v_mfma_f32_16x16x32_bf16 v[200:203], a[216:219], a[184:187], 0\n\t"
"s_lshl_b32 m0, s73, 8\n\t"
"ds_read_b128 a[148:151], v224 offset:256\n\t"
"ds_read_b128 a[164:167], v224 offset:2304\n\t"
"s_mul_i32 s92, s73, 294912\n\t"
"v_mfma_f32_16x16x32_bf16 v[204:207], a[216:219], a[188:191], 0\n\t"
"s_add_u32 s92, s92, s8\n\t"
"s_addc_u32 s93, s9, 0x100000\n\t"
"ds_read_b128 a[152:155], v224 offset:512\n\t"
"v_mfma_f32_16x16x32_bf16 v[208:211], v[148:151], a[192:195], 0\n\t"
"s_mov_b32 s94, s10\n\t"
"ds_read_b128 a[168:171], v224 offset:2560\n\t"
"ds_read_b128 a[156:159], v224 offset:768\n\t"
"v_mfma_f32_16x16x32_bf16 v[212:215], v[148:151], a[196:199], 0\n\t"
"s_waitcnt lgkmcnt(5)\n\t"
"ds_read_b128 a[172:175], v224 offset:2816\n\t"
"s_mov_b32 s95, 0x800000\n\t"
"v_mfma_f32_16x16x32_bf16 v[96:99], v[48:51], a[144:147], 0\n\t"
"s_add_u32 s96, s92, 4096\n\t"
"s_addc_u32 s97, s93, 0\n\t"
"v_mfma_f32_16x16x32_bf16 v[96:99], v[52:55], a[160:163], v[96:99]\n\t"
"s_mov_b64 s[98:99], s[94:95]\n\t"
"v_cvt_pk_bf16_f32 v193, v193, v197\n\t"
"v_cvt_pk_bf16_f32 v194, v194, v198\n\t"
"v_mfma_f32_16x16x32_bf16 v[112:115], v[56:59], a[144:147], 0\n\t"
"s_mov_b32 s16, s80\n\t"
"v_cvt_pk_bf16_f32 v192, v192, v196\n\t"
"v_cvt_pk_bf16_f32 v195, v195, v199\n\t"
"v_mfma_f32_16x16x32_bf16 v[112:115], v[60:63], a[160:163], v[112:115]\n\t"
"s_and_b32 s17, s81, 0xffff\n\t"
"ds_write_addtid_b32 v192 offset:16544\n\t"
"ds_write_addtid_b32 v193 offset:24864\n\t"
"ds_write_addtid_b32 v194 offset:20672\n\t"
"ds_write_addtid_b32 v195 offset:28992\n\t"
"v_mfma_f32_16x16x32_bf16 v[128:131], v[64:67], a[144:147], 0\n\t"
"s_mov_b32 s18, 100663296\n\t"
"ds_read_b128 a[144:147], v224 offset:1024\n\t"
"v_cvt_pk_bf16_f32 v202, v202, v206\n\t"
"v_mfma_f32_16x16x32_bf16 v[128:131], v[68:71], a[160:163], v[128:131]\n\t"
"s_waitcnt lgkmcnt(9)\n\t"
"v_cvt_pk_bf16_f32 v200, v200, v204\n\t"
"v_cvt_pk_bf16_f32 v203, v203, v207\n\t"
"s_mov_b32 s19, 0x20000\n\t"
"v_mfma_f32_16x16x32_bf16 v[132:135], v[64:67], a[148:151], 0\n\t"
"s_mov_b32 s21, 4\n\t"
"ds_write_addtid_b32 v200 offset:18592\n\t"
"v_cvt_pk_bf16_f32 v210, v210, v214\n\t"
"v_mfma_f32_16x16x32_bf16 v[132:135], v[68:71], a[164:167], v[132:135]\n\t"
"s_mov_b32 s38, -2.0\n\t"
"ds_read_b128 a[160:163], v224 offset:3072\n\t"
"v_cvt_pk_bf16_f32 v209, v209, v213\n\t"
"v_mfma_f32_16x16x32_bf16 v[116:119], v[56:59], a[148:151], 0\n\t"
"s_mov_b32 s39, -2.0\n\t"
"ds_write_addtid_b32 v203 offset:31040\n\t"
"ds_write_addtid_b32 v209 offset:25888\n\t"
"v_mfma_f32_16x16x32_bf16 v[116:119], v[60:63], a[164:167], v[116:119]\n\t"
"s_nop 0\n\t"
"ds_write_addtid_b32 v210 offset:21696\n\t"
"v_mbcnt_lo_u32_b32 v176, -1, 0\n\t"
"s_waitcnt vmcnt(7)\n\t"
"s_barrier\n\t"
"v_mfma_f32_16x16x32_bf16 v[216:219], v[148:151], a[200:203], 0\n\t"
"Lpad0_%=:\n\t"
"s_cbranch_execz Lpad4_%=\n\t"
"ds_read_b64_tr_b16 a[176:177], v248 offset:20736\n\t"
"v_mbcnt_hi_u32_b32 v176, -1, v176\n\t"
"v_bfe_u32 v156, v176, 2, 2\n\t"
"v_cvt_pk_bf16_f32 v208, v208, v212\n\t"
"v_mfma_f32_16x16x32_bf16 v[100:103], v[48:51], a[148:151], 0\n\t"
"ds_read_b128 a[148:151], v224 offset:1280\n\t"
"v_and_b32 v157, 3, v176\n\t"
"v_mfma_f32_16x16x32_bf16 v[100:103], v[52:55], a[164:167], v[100:103]\n\t"
"s_waitcnt lgkmcnt(14)\n\t"
"ds_read_b64_tr_b16 a[178:179], v248 offset:21248\n\t"
"v_lshlrev_b32 v157, 3, v157\n\t"
"v_and_b32 v158, 48, v176\n\t"
"v_lshl_add_u32 v158, v157, 3, v158\n\t"
"v_and_b32 v159, 8, v176\n\t"
"v_mfma_f32_16x16x32_bf16 v[104:107], v[48:51], a[152:155], 0\n\t"
"ds_read_b128 a[164:167], v224 offset:3328\n\t"
"v_mul_u32_u24 v166, 516, v159\n\t"
"v_mfma_f32_16x16x32_bf16 v[104:107], v[52:55], a[168:171], v[104:107]\n\t"
"ds_read_b64_tr_b16 a[180:181], v249 offset:20736\n\t"
"v_and_b32 v159, 4, v176\n\t"
"v_mul_u32_u24 v159, 2080, v159\n\t"
"v_cvt_pk_bf16_f32 v201, v201, v205\n\t"
"v_mfma_f32_16x16x32_bf16 v[220:223], v[148:151], a[204:207], 0\n\t"
"ds_read_b64_tr_b16 a[182:183], v249 offset:21248\n\t"
"v_add3_u32 v166, v166, v158, v159\n\t"
"v_accvgpr_write_b32 a212, v224\n\t"
"v_accvgpr_write_b32 a213, v166\n\t"
"v_mfma_f32_16x16x32_bf16 v[120:123], v[56:59], a[152:155], 0\n\t"
"ds_read_b64_tr_b16 a[184:185], v250 offset:20736\n\t"
"v_lshlrev_b32 v158, 15, v176\n\t"
"v_mfma_f32_16x16x32_bf16 v[120:123], v[60:63], a[168:171], v[120:123]\n\t"
"ds_read_b64_tr_b16 a[186:187], v250 offset:21248\n\t"
"v_and_b32 v158, 0x8000, v158\n\t"
"v_or_b32 v167, 0x3f803f80, v158\n\t"
"v_mfma_f32_16x16x32_bf16 v[136:139], v[64:67], a[152:155], 0\n\t"
"ds_read_b128 a[152:155], v224 offset:1536\n\t"
"v_and_b32 v158, 2, v176\n\t"
"v_mfma_f32_16x16x32_bf16 v[136:139], v[68:71], a[168:171], v[136:139]\n\t"
"s_waitcnt lgkmcnt(15)\n\t"
"ds_read_b128 a[168:171], v224 offset:3584\n\t"
"buffer_load_dword v190, v189, s[8:11], 0 offen sc1\n\t"
"v_mfma_f32_16x16x32_bf16 v[140:143], v[64:67], a[156:159], 0\n\t"
"ds_read_b64_tr_b16 a[188:189], v251 offset:20736\n\t"
"v_mul_u32_u24 v158, 0x4000, v158\n\t"
"v_mfma_f32_16x16x32_bf16 v[140:143], v[68:71], a[172:175], v[140:143]\n\t"
"ds_read_b64_tr_b16 a[190:191], v251 offset:21248\n\t"
"v_lshl_or_b32 v158, v158, 16, v158\n\t"
"v_mfma_f32_16x16x32_bf16 v[124:127], v[56:59], a[156:159], 0\n\t"
"ds_write_addtid_b32 v202 offset:22720\n\t"
"v_cvt_pk_bf16_f32 v217, v217, v221\n\t"
"v_mfma_f32_16x16x32_bf16 v[124:127], v[60:63], a[172:175], v[124:127]\n\t"
"s_nop 0\n\t"
"ds_write_addtid_b32 v201 offset:26912\n\t"
"ds_write_addtid_b32 v208 offset:17568\n\t"
"ds_write_addtid_b32 v217 offset:27936\n\t"
"v_xor_b32 v166, v167, v158\n\t"
"v_accvgpr_write_b32 a210, v166\n\t"
"v_accvgpr_write_b32 a211, v167\n\t"
"s_add_u32 s61, s26, 4294900096\n\t"
"v_lshrrev_b32 v155, 4, v176\n\t"
"v_cvt_pk_bf16_f32 v211, v211, v215\n\t"
"v_cvt_pk_bf16_f32 v218, v218, v222\n\t"
"v_mfma_f32_16x16x32_bf16 v[108:111], v[48:51], a[156:159], 0\n\t"
"ds_read_b128 a[156:159], v224 offset:1792\n\t"
"v_and_b32 v158, 1, v156\n\t"
"v_mfma_f32_16x16x32_bf16 v[108:111], v[52:55], a[172:175], v[108:111]\n\t"
"s_waitcnt lgkmcnt(15)\n\t"
"ds_read_b128 a[172:175], v224 offset:3840\n\t"
"buffer_load_dword v191, v189, s[8:11], 0 offen sc1\n\t"
"v_lshrrev_b32 v159, 1, v156\n\t"
"v_cvt_pk_bf16_f32 v216, v216, v220\n\t"
"v_cvt_pk_bf16_f32 v219, v219, v223\n\t"
"v_mfma_f32_16x16x32_bf16 v[0:3], v[72:75], a[144:147], v[96:99]\n\t"
"ds_write_addtid_b32 v219 offset:32064\n\t"
"v_mul_u32_u24 v166, 1152, v159\n\t"
"v_mfma_f32_16x16x32_bf16 v[0:3], v[76:79], a[160:163], v[0:3]\n\t"
"s_nop 0\n\t"
"ds_write_addtid_b32 v211 offset:30016\n\t"
"ds_write_addtid_b32 v216 offset:19616\n\t"
"ds_write_addtid_b32 v218 offset:23744\n\t"
"v_mul_u32_u24 v167, 68352, v158\n\t"
"v_mfma_f32_16x16x32_bf16 v[16:19], v[80:83], a[144:147], v[112:115]\n\t"
"s_add_u32 m0, s26, 56192\n\t"
"ds_read_b64_tr_b16 a[192:193], v248 offset:21888\n\t"
"v_add3_u32 v166, v166, v157, s61\n\t"
"v_mfma_f32_16x16x32_bf16 v[16:19], v[84:87], a[160:163], v[16:19]\n\t"
"ds_read_b64_tr_b16 a[194:195], v248 offset:22400\n\t"
"buffer_load_dwordx4 v236, s[12:15], s22 offen offset:128 nt lds\n\t"
"buffer_load_dwordx4 v237, s[12:15], s22 offen offset:1152 nt lds\n\t"
"v_add_u32 v166, v166, v167\n\t"
"v_xor_b32 v159, v159, v158\n\t"
"v_lshl_add_u32 v166, v159, 6, v166\n\t"
"v_mfma_f32_16x16x32_bf16 v[32:35], v[88:91], a[144:147], v[128:131]\n\t"
"ds_read_b64_tr_b16 a[196:197], v249 offset:21888\n\t"
"v_xad_u32 v167, v158, 0, v155\n\t"
"v_mfma_f32_16x16x32_bf16 v[32:35], v[92:95], a[160:163], v[32:35]\n\t"
"s_waitcnt lgkmcnt(15)\n\t"
"ds_read_b64_tr_b16 a[198:199], v249 offset:22400\n\t"
"v_lshl_add_u32 v168, v167, 7, v166\n\t"
"v_mfma_f32_16x16x32_bf16 v[36:39], v[88:91], a[148:151], v[132:135]\n\t"
"ds_read_b64_tr_b16 a[200:201], v250 offset:21888\n\t"
"v_and_b32 v167, 2, v167\n\t"
"v_mfma_f32_16x16x32_bf16 v[36:39], v[92:95], a[164:167], v[36:39]\n\t"
"ds_read_b64_tr_b16 a[202:203], v250 offset:22400\n\t"
"buffer_load_dwordx4 v238, s[12:15], s22 offen offset:2176 nt lds\n\t"
"v_lshl_add_u32 v168, v167, 4, v168\n\t"
"v_xor_b32 v168, 64, v168\n\t"
"v_accvgpr_write_b32 a208, v168\n\t"
"v_xad_u32 v167, v158, 1, v155\n\t"
"v_mfma_f32_16x16x32_bf16 v[20:23], v[80:83], a[148:151], v[116:119]\n\t"
"s_nop 0\n\t"
"ds_read_b64_tr_b16 a[204:205], v251 offset:21888\n\t"
"ds_read_b64_tr_b16 a[206:207], v251 offset:22400\n\t"
"v_mfma_f32_16x16x32_bf16 v[20:23], v[84:87], a[164:167], v[20:23]\n\t"
"buffer_load_dwordx4 v239, s[12:15], s22 offen offset:3200 nt lds\n\t"
"v_lshl_add_u32 v168, v167, 7, v166\n\t"
"v_and_b32 v167, 2, v167\n\t"
"v_mfma_f32_16x16x32_bf16 v[4:7], v[72:75], a[148:151], v[100:103]\n\t"
"s_lshl_b32 m0, s73, 8\n\t"
"v_lshl_add_u32 v168, v167, 4, v168\n\t"
"v_accvgpr_write_b32 a209, v168\n\t"
"v_mfma_f32_16x16x32_bf16 v[4:7], v[76:79], a[164:167], v[4:7]\n\t"
"s_waitcnt lgkmcnt(8)\n\t"
"s_barrier\n\t"
"v_mfma_f32_16x16x32_bf16 v[8:11], v[72:75], a[152:155], v[104:107]\n\t"
"s_add_u32 m0, s26, 56192\n\t"
"ds_read_b128 a[144:147], v224 offset:16544\n\t"
"ds_read_b128 a[160:163], v224 offset:18592\n\t"
"v_mfma_f32_16x16x32_bf16 v[8:11], v[76:79], a[168:171], v[8:11]\n\t"
"s_mov_b64 exec, s[36:37]\n\t"
"buffer_load_dwordx4 v240, s[12:15], s22 offen offset:3712 nt lds\n\t"
"s_mov_b64 exec, -1\n\t"
"v_mfma_f32_16x16x32_bf16 v[24:27], v[80:83], a[152:155], v[120:123]\n\t"
"s_lshl_b32 m0, s73, 8\n\t"
"ds_read_b128 a[148:151], v224 offset:16800\n\t"
"v_mbcnt_lo_u32_b32 v176, -1, 0\n\t"
"v_mfma_f32_16x16x32_bf16 v[24:27], v[84:87], a[168:171], v[24:27]\n\t"
"ds_read_b128 a[164:167], v224 offset:18848\n\t"
"v_mbcnt_hi_u32_b32 v176, -1, v176\n\t"
"v_mfma_f32_16x16x32_bf16 v[40:43], v[88:91], a[152:155], v[136:139]\n\t"
"v_and_b32 v154, 15, v176\n\t"
"v_lshrrev_b32 v155, 4, v176\n\t"
"v_mfma_f32_16x16x32_bf16 v[40:43], v[92:95], a[168:171], v[40:43]\n\t"
"v_bfe_u32 v156, v176, 2, 2\n\t"
"v_and_b32 v157, 3, v176\n\t"
"v_mfma_f32_16x16x32_bf16 v[44:47], v[88:91], a[156:159], v[140:143]\n\t"
"ds_read_b128 a[152:155], v224 offset:17056\n\t"
"v_lshlrev_b32 v157, 3, v157\n\t"
"s_add_u32 s60, s43, 33280\n\t"
"v_mfma_f32_16x16x32_bf16 v[44:47], v[92:95], a[172:175], v[44:47]\n\t"
"s_add_u32 s61, s26, 4294899008\n\t"
"ds_read_b128 a[168:171], v224 offset:19104\n\t"
"s_mul_i32 s62, s73, 96\n\t"
"v_mfma_f32_16x16x32_bf16 v[28:31], v[80:83], a[156:159], v[124:127]\n\t"
"s_add_u32 s62, s62, 75008\n\t"
"v_min_u32 v158, 35, v176\n\t"
"v_lshl_add_u32 v254, v158, 4, s60\n\t"
"v_mfma_f32_16x16x32_bf16 v[28:31], v[84:87], a[172:175], v[28:31]\n\t"
"v_add_u32 v255, 68352, v254\n\t"
"v_and_b32 v158, 1, v156\n\t"
"v_mfma_f32_16x16x32_bf16 v[12:15], v[72:75], a[156:159], v[108:111]\n\t"
"v_lshrrev_b32 v156, 3, v176\n\t"
"v_add_u32 v159, v156, v158\n\t"
"v_mfma_f32_16x16x32_bf16 v[12:15], v[76:79], a[172:175], v[12:15]\n\t"
"v_lshl_add_u32 v166, v159, 7, v157\n\t"
"v_and_b32 v159, 2, v159\n\t"
"v_lshl_add_u32 v166, v159, 4, v166\n\t"
"v_mfma_f32_16x16x32_bf16 v[192:195], a[216:219], a[176:179], 0\n\t"
"v_xad_u32 v167, v158, 1, v156\n\t"
"v_lshl_add_u32 v168, v167, 7, v157\n\t"
"v_mfma_f32_16x16x32_bf16 v[196:199], a[216:219], a[180:183], 0\n\t"
"v_and_b32 v167, 2, v167\n\t"
"v_lshl_add_u32 v168, v167, 4, v168\n\t"
"v_mfma_f32_16x16x32_bf16 v[200:203], a[216:219], a[184:187], 0\n\t"
"v_sub_u32 v168, v168, v166\n\t"
"v_mul_u32_u24 v159, 68288, v158\n\t"
"v_mfma_f32_16x16x32_bf16 v[204:207], a[216:219], a[188:191], 0\n\t"
"s_waitcnt lgkmcnt(12)\n\t"
".p2align 3\n\t"
"ds_read_b128 a[156:159], v224 offset:17312\n\t"
"ds_read_b128 a[172:175], v224 offset:19360\n\t"
"v_pk_fma_f32 v[96:97], v[96:97], s[38:39], v[0:1] neg_lo:[1,0,1] neg_hi:[1,0,1]\n\t"
"v_pk_fma_f32 v[98:99], v[98:99], s[38:39], v[2:3] neg_lo:[1,0,1] neg_hi:[1,0,1]\n\t"
"v_pk_fma_f32 v[112:113], v[112:113], s[38:39], v[16:17] neg_lo:[1,0,1] neg_hi:[1,0,1]\n\t"
"v_pk_fma_f32 v[114:115], v[114:115], s[38:39], v[18:19] neg_lo:[1,0,1] neg_hi:[1,0,1]\n\t"
"v_pk_fma_f32 v[128:129], v[128:129], s[38:39], v[32:33] neg_lo:[1,0,1] neg_hi:[1,0,1]\n\t"
"v_pk_fma_f32 v[130:131], v[130:131], s[38:39], v[34:35] neg_lo:[1,0,1] neg_hi:[1,0,1]\n\t"
"v_pk_fma_f32 v[132:133], v[132:133], s[38:39], v[36:37] neg_lo:[1,0,1] neg_hi:[1,0,1]\n\t"
"v_pk_fma_f32 v[134:135], v[134:135], s[38:39], v[38:39] neg_lo:[1,0,1] neg_hi:[1,0,1]\n\t"
"v_pk_fma_f32 v[116:117], v[116:117], s[38:39], v[20:21] neg_lo:[1,0,1] neg_hi:[1,0,1]\n\t"
"v_pk_fma_f32 v[118:119], v[118:119], s[38:39], v[22:23] neg_lo:[1,0,1] neg_hi:[1,0,1]\n\t"
"v_pk_fma_f32 v[100:101], v[100:101], s[38:39], v[4:5] neg_lo:[1,0,1] neg_hi:[1,0,1]\n\t"
"v_pk_fma_f32 v[102:103], v[102:103], s[38:39], v[6:7] neg_lo:[1,0,1] neg_hi:[1,0,1]\n\t"
"v_pk_fma_f32 v[104:105], v[104:105], s[38:39], v[8:9] neg_lo:[1,0,1] neg_hi:[1,0,1]\n\t"
"v_pk_fma_f32 v[106:107], v[106:107], s[38:39], v[10:11] neg_lo:[1,0,1] neg_hi:[1,0,1]\n\t"
"v_pk_fma_f32 v[120:121], v[120:121], s[38:39], v[24:25] neg_lo:[1,0,1] neg_hi:[1,0,1]\n\t"
"v_pk_fma_f32 v[122:123], v[122:123], s[38:39], v[26:27] neg_lo:[1,0,1] neg_hi:[1,0,1]\n\t"
"v_pk_fma_f32 v[136:137], v[136:137], s[38:39], v[40:41] neg_lo:[1,0,1] neg_hi:[1,0,1]\n\t"
"v_pk_fma_f32 v[138:139], v[138:139], s[38:39], v[42:43] neg_lo:[1,0,1] neg_hi:[1,0,1]\n\t"
"v_pk_fma_f32 v[140:141], v[140:141], s[38:39], v[44:45] neg_lo:[1,0,1] neg_hi:[1,0,1]\n\t"
"v_pk_fma_f32 v[142:143], v[142:143], s[38:39], v[46:47] neg_lo:[1,0,1] neg_hi:[1,0,1]\n\t"
"v_pk_fma_f32 v[124:125], v[124:125], s[38:39], v[28:29] neg_lo:[1,0,1] neg_hi:[1,0,1]\n\t"
"v_pk_fma_f32 v[126:127], v[126:127], s[38:39], v[30:31] neg_lo:[1,0,1] neg_hi:[1,0,1]\n\t"
"v_pk_fma_f32 v[108:109], v[108:109], s[38:39], v[12:13] neg_lo:[1,0,1] neg_hi:[1,0,1]\n\t"
"v_pk_fma_f32 v[110:111], v[110:111], s[38:39], v[14:15] neg_lo:[1,0,1] neg_hi:[1,0,1]\n\t"
"v_mfma_f32_16x16x32_bf16 v[208:211], a[216:219], a[192:195], 0\n\t"
"s_waitcnt lgkmcnt(12)\n\t"
"v_add_u32 v166, v166, v159\n\t"
"v_lshl_add_u32 v168, v158, 7, v168\n\t"
"s_mov_b64 exec, 0\n\t"
"s_cmp_eq_u32 s73, 0\n\t"
"s_cbranch_scc1 Lpad11_%=\n\t"
"s_cmp_eq_u32 s73, 1\n\t"
"s_cbranch_scc1 Lpad10_%=\n\t"
"s_cmp_eq_u32 s73, 2\n\t"
"s_cbranch_scc1 Lpad9_%=\n\t"
"s_branch Lpad8_%=\n\t"
"Lret1_%=:\n\t"
"s_mov_b64 exec, -1\n\t"
"s_waitcnt vmcnt(6)\n\t"
"v_cmp_eq_u32 vcc, v190, v188\n\t"
"s_nop 4\n\t"
"s_cmp_eq_u64 vcc, 0\n\t"
"s_cbranch_scc1 Lgo_%=\n\t"
"s_sub_u32 s68, s68, 1\n\t"
"s_waitcnt vmcnt(5)\n\t"
"Lchk_%=:\n\t"
"v_cmp_eq_u32 vcc, v191, v188\n\t"
"s_nop 4\n\t"
"s_cmp_eq_u64 vcc, 0\n\t"
"s_cbranch_scc1 Lgo_%=\n\t"
"s_sub_u32 s68, s68, 1\n\t"
"s_cmp_eq_u32 s68, 0\n\t"
"s_cbranch_scc1 Lfb_%=\n\t"
"buffer_load_dword v191, v189, s[8:11], 0 offen sc1\n\t"
"s_waitcnt vmcnt(0)\n\t"
"s_branch Lchk_%=\n\t"
"Lgo_%=:\n\t"
"v_mfma_f32_16x16x32_bf16 v[212:215], a[216:219], a[196:199], 0\n\t"
"s_waitcnt lgkmcnt(10)\n\t"
"s_mov_b32 s54, 98304\n\t"
"buffer_load_dwordx4 a[96:99], off, s[92:95], s54 offset:0 sc1\n\t"
"buffer_load_dwordx4 a[100:103], off, s[92:95], s54 offset:1024 sc1\n\t"
"v_mfma_f32_16x16x32_bf16 v[216:219], a[216:219], a[200:203], 0\n\t"
"s_waitcnt lgkmcnt(8)\n\t"
"v_subrev_u32 v253, 64, v168\n\t"
"v_add_u32 v225, s61, v166\n\t"
"v_mfma_f32_16x16x32_bf16 v[220:223], a[216:219], a[204:207], 0\n\t"
"s_waitcnt lgkmcnt(6)\n\t"
"buffer_load_dwordx4 a[104:107], off, s[92:95], s54 offset:2048 sc1\n\t"
"v_mul_u32_u24 v158, 400, v154\n\t"
"v_cvt_pk_bf16_f32 v193, v193, v197\n\t"
"v_cvt_pk_bf16_f32 v195, v195, v199\n\t"
"v_mfma_f32_16x16x32_bf16 v[0:3], a[48:51], a[144:147], v[0:3]\n\t"
"ds_write_addtid_b32 v193 offset:8320\n\t"
"v_mul_u32_u24 v159, 24, v155\n\t"
"v_mfma_f32_16x16x32_bf16 v[0:3], a[52:55], a[160:163], v[0:3]\n\t"
"s_nop 0\n\t"
"ds_write_addtid_b32 v195 offset:12448\n\t"
"buffer_load_dwordx4 a[108:111], off, s[92:95], s54 offset:3072 sc1\n\t"
"v_mfma_f32_16x16x32_bf16 v[16:19], a[56:59], a[144:147], v[16:19]\n\t"
"s_nop 0\n\t"
"v_add3_u32 v228, v158, v159, s62\n\t"
"v_cvt_pk_bf16_f32 v203, v203, v207\n\t"
"v_mfma_f32_16x16x32_bf16 v[16:19], a[60:63], a[160:163], v[16:19]\n\t"
"buffer_load_dwordx4 a[112:115], off, s[96:99], s54 offset:0 sc1\n\t"
"v_cvt_pk_bf16_f32 v194, v194, v198\n\t"
"v_mfma_f32_16x16x32_bf16 v[32:35], a[64:67], a[144:147], v[32:35]\n\t"
"s_mul_i32 s60, s73, 3200\n\t"
"ds_read_b128 a[144:147], v224 offset:17568\n\t"
"ds_write_addtid_b32 v194 offset:4128\n\t"
"v_mfma_f32_16x16x32_bf16 v[32:35], a[68:71], a[160:163], v[32:35]\n\t"
"s_waitcnt lgkmcnt(8)\n\t"
"ds_read_b128 a[160:163], v224 offset:19616\n\t"
"buffer_load_dwordx4 a[116:119], off, s[96:99], s54 offset:1024 sc1\n\t"
"v_cvt_pk_bf16_f32 v216, v216, v220\n\t"
"v_cvt_pk_bf16_f32 v217, v217, v221\n\t"
"v_mfma_f32_16x16x32_bf16 v[36:39], a[64:67], a[148:151], v[36:39]\n\t"
"s_nop 0\n\t"
"ds_write_addtid_b32 v203 offset:14496\n\t"
"v_cvt_pk_bf16_f32 v202, v202, v206\n\t"
"v_mfma_f32_16x16x32_bf16 v[36:39], a[68:71], a[164:167], v[36:39]\n\t"
"s_nop 0\n\t"
"ds_write_addtid_b32 v217 offset:11392\n\t"
"s_mov_b32 s66, 172032\n\t"
"buffer_load_dwordx4 a[120:123], off, s[92:95], s66 offset:0 sc1\n\t"
"v_cvt_pk_bf16_f32 v192, v192, v196\n\t"
"s_barrier\n\t"
"v_mfma_f32_16x16x32_bf16 v[20:23], a[56:59], a[148:151], v[20:23]\n\t"
"s_add_u32 s60, s60, 75008\n\t"
"ds_read_b64_tr_b16 a[176:177], v248 offset:41472\n\t"
"ds_read_b64_tr_b16 a[178:179], v248 offset:41984\n\t"
"v_mfma_f32_16x16x32_bf16 v[20:23], a[60:63], a[164:167], v[20:23]\n\t"
"s_nop 0\n\t"
"ds_read_b64_tr_b16 a[180:181], v249 offset:41472\n\t"
"buffer_load_dwordx4 a[124:127], off, s[92:95], s66 offset:1024 sc1\n\t"
"v_cvt_pk_bf16_f32 v210, v210, v214\n\t"
"v_mfma_f32_16x16x32_bf16 v[4:7], a[48:51], a[148:151], v[4:7]\n\t"
"s_nop 0\n\t"
"ds_read_b64_tr_b16 a[182:183], v249 offset:41984\n\t"
"v_cvt_pk_bf16_f32 v219, v219, v223\n\t"
"v_mfma_f32_16x16x32_bf16 v[4:7], a[52:55], a[164:167], v[4:7]\n\t"
"s_waitcnt lgkmcnt(13)\n\t"
"ds_read_b128 a[148:151], v224 offset:17824\n\t"
"buffer_load_dwordx4 a[128:131], off, s[92:95], s66 offset:2048 sc1\n\t"
"v_cvt_pk_bf16_f32 v218, v218, v222\n\t"
"v_mfma_f32_16x16x32_bf16 v[8:11], a[48:51], a[152:155], v[8:11]\n\t"
"s_and_b32 s61, s73, 2\n\t"
"ds_read_b128 a[164:167], v224 offset:19872\n\t"
"v_cvt_pk_bf16_f32 v211, v211, v215\n\t"
"v_mfma_f32_16x16x32_bf16 v[8:11], a[52:55], a[168:171], v[8:11]\n\t"
"s_lshl_b32 s61, s61, 7\n\t"
"ds_write_addtid_b32 v202 offset:6176\n\t"
"ds_read_b64_tr_b16 a[184:185], v250 offset:41472\n\t"
"v_cvt_pk_bf16_f32 v200, v200, v204\n\t"
"v_mfma_f32_16x16x32_bf16 v[24:27], a[56:59], a[152:155], v[24:27]\n\t"
"s_nop 0\n\t"
"ds_read_b64_tr_b16 a[186:187], v250 offset:41984\n\t"
"v_cvt_pk_bf16_f32 v201, v201, v205\n\t"
"v_mfma_f32_16x16x32_bf16 v[24:27], a[60:63], a[168:171], v[24:27]\n\t"
"s_nop 0\n\t"
"ds_write_addtid_b32 v218 offset:7200\n\t"
"buffer_load_dwordx4 a[132:135], off, s[92:95], s66 offset:3072 sc1\n\t"
"v_mfma_f32_16x16x32_bf16 v[40:43], a[64:67], a[152:155], v[40:43]\n\t"
"s_add_u32 s60, s60, s61\n\t"
"ds_read_b128 a[152:155], v224 offset:18080\n\t"
"v_cvt_pk_bf16_f32 v208, v208, v212\n\t"
"v_mfma_f32_16x16x32_bf16 v[40:43], a[68:71], a[168:171], v[40:43]\n\t"
"s_waitcnt lgkmcnt(15)\n\t"
"ds_write_addtid_b32 v200 offset:2048\n\t"
"ds_write_addtid_b32 v210 offset:5152\n\t"
"ds_write_addtid_b32 v219 offset:15520\n\t"
"v_cvt_pk_bf16_f32 v209, v209, v213\n\t"
"s_mul_i32 s61, s73, 393216\n\t"
"v_mfma_f32_16x16x32_bf16 v[44:47], a[64:67], a[156:159], v[44:47]\n\t"
"s_nop 0\n\t"
"ds_read_b64_tr_b16 a[188:189], v251 offset:41472\n\t"
"v_add_u32 v158, 0, v176\n\t"
"v_mfma_f32_16x16x32_bf16 v[44:47], a[68:71], a[172:175], v[44:47]\n\t"
"s_nop 0\n\t"
"ds_read_b128 a[168:171], v224 offset:20128\n\t"
"buffer_load_dwordx4 a[136:139], off, s[96:99], s66 offset:0 sc1\n\t"
"v_mfma_f32_16x16x32_bf16 v[28:31], a[56:59], a[156:159], v[28:31]\n\t"
"s_nop 0\n\t"
"ds_read_b64_tr_b16 a[190:191], v251 offset:41984\n\t"
"v_mul_u32_u24 v159, 0xab, v158\n\t"
"v_mfma_f32_16x16x32_bf16 v[28:31], a[60:63], a[172:175], v[28:31]\n\t"
"s_nop 0\n\t"
"ds_read_b64_tr_b16 a[192:193], v248 offset:42624\n\t"
"buffer_load_dwordx4 a[140:143], off, s[96:99], s66 offset:1024 sc1\n\t"
"v_lshrrev_b32 v159, 12, v159\n\t"
"v_mfma_f32_16x16x32_bf16 v[12:15], a[48:51], a[156:159], v[12:15]\n\t"
"s_nop 0\n\t"
"ds_read_b128 a[156:159], v224 offset:18336\n\t"
"v_add_lshl_u32 v166, v158, v159, 4\n\t"
"v_mfma_f32_16x16x32_bf16 v[12:15], a[52:55], a[172:175], v[12:15]\n\t"
"s_waitcnt lgkmcnt(15)\n\t"
"ds_read_b64_tr_b16 a[194:195], v248 offset:43136\n\t"
"v_add_u32 v229, s60, v166\n\t"
"v_mad_u32_u24 v158, v159, 24, v158\n\t"
"v_lshl_add_u32 v232, v158, 4, s61\n\t"
"v_mfma_f32_16x16x32_bf16 v[96:99], a[72:75], a[144:147], v[96:99]\n\t"
"s_nop 0\n\t"
"ds_write_addtid_b32 v209 offset:9344\n\t"
"v_add_u32 v158, 64, v176\n\t"
"v_mfma_f32_16x16x32_bf16 v[96:99], a[76:79], a[160:163], v[96:99]\n\t"
"s_nop 0\n\t"
"ds_write_addtid_b32 v192 offset:0\n\t"
"ds_write_addtid_b32 v201 offset:10368\n\t"
"ds_write_addtid_b32 v208 offset:1024\n\t"
"ds_write_addtid_b32 v211 offset:13472\n\t"
"s_mov_b32 s69, 196608\n\t"
"buffer_load_dwordx4 a[0:3], off, s[92:95], s69 offset:0 sc1\n\t"
"v_mul_u32_u24 v159, 0xab, v158\n\t"
"v_lshrrev_b32 v159, 12, v159\n\t"
"v_mfma_f32_16x16x32_bf16 v[112:115], a[80:83], a[144:147], v[112:115]\n\t"
"s_nop 0\n\t"
"ds_write_addtid_b32 v216 offset:3072\n\t"
"v_add_lshl_u32 v166, v158, v159, 4\n\t"
"v_mfma_f32_16x16x32_bf16 v[112:115], a[84:87], a[160:163], v[112:115]\n\t"
"s_nop 0\n\t"
"ds_read_b128 a[172:175], v224 offset:20384\n\t"
"buffer_load_dwordx4 a[4:7], off, s[92:95], s69 offset:1024 sc1\n\t"
"v_mfma_f32_16x16x32_bf16 v[128:131], a[88:91], a[144:147], v[128:131]\n\t"
"s_nop 0\n\t"
"ds_read_b64_tr_b16 a[196:197], v249 offset:42624\n\t"
"v_add_u32 v230, s60, v166\n\t"
"v_mfma_f32_16x16x32_bf16 v[128:131], a[92:95], a[160:163], v[128:131]\n\t"
"s_waitcnt lgkmcnt(15)\n\t"
"ds_read_b64_tr_b16 a[198:199], v249 offset:43136\n\t"
"v_mad_u32_u24 v158, v159, 24, v158\n\t"
"v_mfma_f32_16x16x32_bf16 v[132:135], a[88:91], a[148:151], v[132:135]\n\t"
"s_nop 0\n\t"
"ds_read_b64_tr_b16 a[200:201], v250 offset:42624\n\t"
"v_lshl_add_u32 v233, v158, 4, s61\n\t"
"v_mfma_f32_16x16x32_bf16 v[132:135], a[92:95], a[164:167], v[132:135]\n\t"
"s_nop 0\n\t"
"ds_read_b64_tr_b16 a[202:203], v250 offset:43136\n\t"
"buffer_load_dwordx4 a[8:11], off, s[92:95], s69 offset:2048 sc1\n\t"
"v_add_u32 v158, 128, v176\n\t"
"v_mul_u32_u24 v159, 0xab, v158\n\t"
"v_mfma_f32_16x16x32_bf16 v[116:119], a[80:83], a[148:151], v[116:119]\n\t"
"s_nop 0\n\t"
"ds_read_b64_tr_b16 a[204:205], v251 offset:42624\n\t"
"ds_read_b64_tr_b16 a[206:207], v251 offset:43136\n\t"
"v_mfma_f32_16x16x32_bf16 v[116:119], a[84:87], a[164:167], v[116:119]\n\t"
"s_nop 0\n\t"
"v_lshrrev_b32 v159, 12, v159\n\t"
"v_add_lshl_u32 v166, v158, v159, 4\n\t"
"v_add_u32 v231, s60, v166\n\t"
"v_mfma_f32_16x16x32_bf16 v[100:103], a[72:75], a[148:151], v[100:103]\n\t"
"s_nop 0\n\t"
"v_mad_u32_u24 v158, v159, 24, v158\n\t"
"v_lshl_add_u32 v234, v158, 4, s61\n\t"
"v_mfma_f32_16x16x32_bf16 v[100:103], a[76:79], a[164:167], v[100:103]\n\t"
"buffer_load_dwordx4 a[12:15], off, s[92:95], s69 offset:3072 sc1\n\t"
"s_waitcnt lgkmcnt(6)\n\t"
"s_add_u32 s71, s71, 1\n\t"
"v_mov_b32 v215, s71\n\t"
"s_mov_b64 exec, 1\n\t"
"buffer_store_dword v215, off, s[8:11], s70 sc1\n\t"
"s_mov_b64 exec, -1\n\t"
"s_barrier\n\t"
"v_mfma_f32_16x16x32_bf16 v[104:107], a[72:75], a[152:155], v[104:107]\n\t"
"s_nop 0\n\t"
"ds_read_b128 a[144:147], v224 offset:0\n\t"
"ds_read_b128 a[148:151], v224 offset:256\n\t"
"v_mfma_f32_16x16x32_bf16 v[104:107], a[76:79], a[168:171], v[104:107]\n\t"
"buffer_load_dwordx4 a[16:19], off, s[96:99], s69 offset:0 sc1\n\t"
"v_mfma_f32_16x16x32_bf16 v[120:123], a[80:83], a[152:155], v[120:123]\n\t"
"s_nop 0\n\t"
"ds_read_b128 a[160:163], v224 offset:2048\n\t"
"v_accvgpr_read_b32 v172, a220\n\t"
"v_mfma_f32_16x16x32_bf16 v[120:123], a[84:87], a[168:171], v[120:123]\n\t"
"s_nop 0\n\t"
"ds_read_b128 a[164:167], v224 offset:2304\n\t"
"v_and_b32 v172, 0xff7fff7f, v172\n\t"
"v_mfma_f32_16x16x32_bf16 v[136:139], a[88:91], a[152:155], v[136:139]\n\t"
"s_nop 0\n\t"
"v_accvgpr_write_b32 a252, v172\n\t"
"v_accvgpr_read_b32 v173, a221\n\t"
"v_mfma_f32_16x16x32_bf16 v[136:139], a[92:95], a[168:171], v[136:139]\n\t"
"buffer_load_dwordx4 a[20:23], off, s[96:99], s69 offset:1024 sc1\n\t"
"v_mfma_f32_16x16x32_bf16 v[140:143], a[88:91], a[156:159], v[140:143]\n\t"
"s_nop 0\n\t"
"ds_read_b128 a[168:171], v224 offset:2560\n\t"
"v_and_b32 v173, 0xff7fff7f, v173\n\t"
"v_mfma_f32_16x16x32_bf16 v[140:143], a[92:95], a[172:175], v[140:143]\n\t"
"s_mov_b32 s81, 270336\n\t"
"buffer_load_dwordx4 a[24:27], off, s[92:95], s81 offset:0 sc1\n\t"
"v_mfma_f32_16x16x32_bf16 v[124:127], a[80:83], a[156:159], v[124:127]\n\t"
"s_nop 0\n\t"
"ds_read_b128 a[152:155], v224 offset:512\n\t"
"v_add_u32 v248, 122368, v226\n\t"
"v_mfma_f32_16x16x32_bf16 v[124:127], a[84:87], a[172:175], v[124:127]\n\t"
"s_nop 0\n\t"
"v_accvgpr_write_b32 a253, v173\n\t"
"v_xor_b32 v249, 32, v248\n\t"
"v_mfma_f32_16x16x32_bf16 v[108:111], a[72:75], a[156:159], v[108:111]\n\t"
"s_nop 0\n\t"
"ds_read_b128 a[156:159], v224 offset:768\n\t"
"v_xor_b32 v250, 64, v248\n\t"
"v_mfma_f32_16x16x32_bf16 v[108:111], a[76:79], a[172:175], v[108:111]\n\t"
"buffer_load_dwordx4 a[28:31], off, s[92:95], s81 offset:1024 sc1\n\t"
"v_mfma_f32_16x16x32_bf16 v[192:195], a[216:219], a[176:179], 0\n\t"
"s_nop 0\n\t"
"ds_read_b128 a[172:175], v224 offset:2816\n\t"
"v_xor_b32 v251, 96, v248\n\t"
"v_mfma_f32_16x16x32_bf16 v[196:199], a[216:219], a[180:183], 0\n\t"
"s_nop 0\n\t"
"v_accvgpr_read_b32 v174, a222\n\t"
"v_and_b32 v174, 0xff7fff7f, v174\n\t"
"v_mfma_f32_16x16x32_bf16 v[200:203], a[216:219], a[184:187], 0\n\t"
"s_nop 0\n\t"
"ds_read_b64_tr_b16 a[178:179], v248 offset:128\n\t"
"v_accvgpr_write_b32 a254, v174\n\t"
"v_mfma_f32_16x16x32_bf16 v[204:207], a[216:219], a[188:191], 0\n\t"
"buffer_load_dwordx4 a[32:35], off, s[92:95], s81 offset:2048 sc1\n\t"
"v_mfma_f32_16x16x32_bf16 v[208:211], a[216:219], a[192:195], 0\n\t"
"s_waitcnt lgkmcnt(13)\n\t"
"ds_read_b64_tr_b16 a[176:177], v248 offset:0\n\t"
"ds_read_b64_tr_b16 a[180:181], v249 offset:256\n\t"
"s_mov_b32 s42, 24576\n\t"
"v_mfma_f32_16x16x32_bf16 v[212:215], a[216:219], a[196:199], 0\n\t"
"s_waitcnt lgkmcnt(13)\n\t"
"buffer_load_dwordx4 a[36:39], off, s[92:95], s81 offset:3072 sc1\n\t"
"v_mfma_f32_16x16x32_bf16 v[216:219], a[216:219], a[200:203], 0\n\t"
"s_waitcnt lgkmcnt(11)\n\t"
"ds_read_b64_tr_b16 a[186:187], v249 offset:128\n\t"
"v_accvgpr_read_b32 v175, a223\n\t"
"v_mfma_f32_16x16x32_bf16 v[220:223], a[216:219], a[204:207], 0\n\t"
"s_waitcnt vmcnt(21) lgkmcnt(9)\n\t"
"v_and_b32 v175, 0xff7fff7f, v175\n\t"
"v_accvgpr_write_b32 a255, v175\n\t"
"v_mfma_f32_16x16x32_bf16 v[0:3], a[96:99], a[144:147], v[0:3]\n\t"
"s_nop 0\n\t"
"ds_read_b64_tr_b16 a[184:185], v249 offset:0\n\t"
"v_cvt_pk_bf16_f32 v193, v193, v197\n\t"
"v_mfma_f32_16x16x32_bf16 v[0:3], a[100:103], a[160:163], v[0:3]\n\t"
"s_waitcnt vmcnt(19)\n\t"
"ds_read_b64_tr_b16 a[188:189], v248 offset:256\n\t"
"buffer_load_dwordx4 a[40:43], off, s[96:99], s81 offset:0 sc1\n\t"
"v_mfma_f32_16x16x32_bf16 v[16:19], a[104:107], a[144:147], v[16:19]\n\t"
"s_mov_b32 s41, 16384\n\t"
"ds_read_b64_tr_b16 a[194:195], v250 offset:128\n\t"
"v_cvt_pk_bf16_f32 v192, v192, v196\n\t"
"v_mfma_f32_16x16x32_bf16 v[16:19], a[108:111], a[160:163], v[16:19]\n\t"
"s_waitcnt vmcnt(18)\n\t"
"buffer_load_dwordx4 a[44:47], off, s[96:99], s81 offset:1024 sc1\n\t"
"v_mfma_f32_16x16x32_bf16 v[32:35], a[112:115], a[144:147], v[32:35]\n\t"
"s_mov_b32 s90, 0.5\n\t"
"ds_read_b128 a[144:147], v224 offset:1024\n\t"
"v_cvt_pk_bf16_f32 v210, v210, v214\n\t"
"v_mfma_f32_16x16x32_bf16 v[32:35], a[116:119], a[160:163], v[32:35]\n\t"
"s_waitcnt lgkmcnt(12)\n\t"
"v_cvt_pk_bf16_f32 v194, v194, v198\n\t"
"v_cvt_pk_bf16_f32 v218, v218, v222\n\t"
"v_mfma_f32_16x16x32_bf16 v[36:39], a[112:115], a[148:151], v[36:39]\n\t"
"s_nop 0\n\t"
"ds_read_b128 a[160:163], v224 offset:3072\n\t"
"v_cvt_pk_bf16_f32 v219, v219, v223\n\t"
"v_mfma_f32_16x16x32_bf16 v[36:39], a[116:119], a[164:167], v[36:39]\n\t"
"s_mov_b32 s57, 122880\n\t"
"buffer_load_dwordx4 a[48:51], off, s[92:95], s57 offset:0 sc1\n\t"
"v_mfma_f32_16x16x32_bf16 v[20:23], a[104:107], a[148:151], v[20:23]\n\t"
"s_mov_b32 s91, 0.5\n\t"
"ds_write_addtid_b32 v210 offset:21696\n\t"
"ds_read_b64_tr_b16 a[192:193], v250 offset:0\n\t"
"v_mfma_f32_16x16x32_bf16 v[20:23], a[108:111], a[164:167], v[20:23]\n\t"
"s_nop 0\n\t"
"v_cvt_pk_bf16_f32 v195, v195, v199\n\t"
"v_cvt_pk_bf16_f32 v216, v216, v220\n\t"
"v_mfma_f32_16x16x32_bf16 v[4:7], a[96:99], a[148:151], v[4:7]\n\t"
"s_add_u32 s84, s92, 18432\n\t"
"s_addc_u32 s85, s93, 0\n\t"
"ds_read_b128 a[148:151], v224 offset:1280\n\t"
"v_mfma_f32_16x16x32_bf16 v[4:7], a[100:103], a[164:167], v[4:7]\n\t"
"s_waitcnt lgkmcnt(14)\n\t"
"buffer_load_dwordx4 a[52:55], off, s[92:95], s57 offset:1024 sc1\n\t"
"v_mfma_f32_16x16x32_bf16 v[8:11], a[96:99], a[152:155], v[8:11]\n\t"
"s_mov_b64 s[86:87], s[94:95]\n\t"
"ds_read_b128 a[164:167], v224 offset:3328\n\t"
"v_cvt_pk_bf16_f32 v211, v211, v215\n\t"
"v_mfma_f32_16x16x32_bf16 v[8:11], a[100:103], a[168:171], v[8:11]\n\t"
"s_xor_b64 s[88:89], s[84:85], s[92:93]\n\t"
"ds_write_addtid_b32 v194 offset:20672\n\t"
"ds_write_addtid_b32 v219 offset:32064\n\t"
"v_mfma_f32_16x16x32_bf16 v[24:27], a[104:107], a[152:155], v[24:27]\n\t"
"s_add_u32 s28, s84, 4096\n\t"
"s_addc_u32 s29, s85, 0\n\t"
"v_cvt_pk_bf16_f32 v203, v203, v207\n\t"
"v_mfma_f32_16x16x32_bf16 v[24:27], a[108:111], a[168:171], v[24:27]\n\t"
"s_nop 0\n\t"
"ds_write_addtid_b32 v203 offset:31040\n\t"
"buffer_load_dwordx4 a[56:59], off, s[92:95], s57 offset:2048 sc1\n\t"
"v_cvt_pk_bf16_f32 v201, v201, v205\n\t"
"v_mfma_f32_16x16x32_bf16 v[40:43], a[112:115], a[152:155], v[40:43]\n\t"
"s_nop 0\n\t"
"ds_read_b128 a[152:155], v224 offset:1536\n\t"
"v_cvt_pk_bf16_f32 v209, v209, v213\n\t"
"v_mfma_f32_16x16x32_bf16 v[40:43], a[116:119], a[168:171], v[40:43]\n\t"
"s_waitcnt lgkmcnt(15)\n\t"
"ds_read_b128 a[168:171], v224 offset:3584\n\t"
"v_cvt_pk_bf16_f32 v217, v217, v221\n\t"
"s_mov_b64 s[30:31], s[94:95]\n\t"
"v_mfma_f32_16x16x32_bf16 v[44:47], a[112:115], a[156:159], v[44:47]\n\t"
"s_nop 0\n\t"
"v_cvt_pk_bf16_f32 v200, v200, v204\n\t"
"v_cvt_pk_bf16_f32 v202, v202, v206\n\t"
"v_mfma_f32_16x16x32_bf16 v[44:47], a[116:119], a[172:175], v[44:47]\n\t"
"s_nop 0\n\t"
"ds_write_addtid_b32 v200 offset:18592\n\t"
"buffer_load_dwordx4 a[60:63], off, s[92:95], s57 offset:3072 sc1\n\t"
"v_mfma_f32_16x16x32_bf16 v[28:31], a[104:107], a[156:159], v[28:31]\n\t"
"s_add_u32 s25, s24, 0xffeb0000\n\t"
"ds_write_addtid_b32 v202 offset:22720\n\t"
"ds_write_addtid_b32 v216 offset:19616\n\t"
"v_mfma_f32_16x16x32_bf16 v[28:31], a[108:111], a[172:175], v[28:31]\n\t"
"s_mov_b32 s48, 49152\n\t"
"ds_write_addtid_b32 v209 offset:25888\n\t"
"ds_write_addtid_b32 v217 offset:27936\n\t"
"v_mfma_f32_16x16x32_bf16 v[12:15], a[96:99], a[156:159], v[12:15]\n\t"
"s_nop 0\n\t"
"ds_write_addtid_b32 v201 offset:26912\n\t"
"v_cvt_pk_bf16_f32 v208, v208, v212\n\t"
"v_mfma_f32_16x16x32_bf16 v[12:15], a[100:103], a[172:175], v[12:15]\n\t"
"s_waitcnt vmcnt(21) lgkmcnt(15)\n\t"
"buffer_load_dwordx4 a[64:67], off, s[96:99], s57 offset:0 sc1\n\t"
"v_mfma_f32_16x16x32_bf16 v[96:99], a[120:123], a[144:147], v[96:99]\n\t"
"s_add_u32 s27, s24, 0xffe80000\n\t"
"ds_write_addtid_b32 v192 offset:16544\n\t"
"ds_write_addtid_b32 v208 offset:17568\n\t"
"v_mfma_f32_16x16x32_bf16 v[96:99], a[124:127], a[160:163], v[96:99]\n\t"
"Lpad1_%=:\n\t"
"s_cbranch_execz Lpad5_%=\n\t"
"s_waitcnt vmcnt(20)\n\t"
"ds_write_addtid_b32 v193 offset:24864\n\t"
"ds_write_addtid_b32 v195 offset:28992\n\t"
"s_add_u32 s20, s24, 0xffd30000\n\t"
"v_mfma_f32_16x16x32_bf16 v[112:115], a[128:131], a[144:147], v[112:115]\n\t"
"s_mov_b32 s47, 40960\n\t"
"ds_write_addtid_b32 v211 offset:30016\n\t"
"ds_write_addtid_b32 v218 offset:23744\n\t"
"v_mfma_f32_16x16x32_bf16 v[112:115], a[132:135], a[160:163], v[112:115]\n\t"
"s_waitcnt vmcnt(18)\n\t"
"buffer_load_dwordx4 a[68:71], off, s[96:99], s57 offset:1024 sc1\n\t"
"v_mfma_f32_16x16x32_bf16 v[128:131], a[136:139], a[144:147], v[128:131]\n\t"
"s_mov_b32 s40, 8192\n\t"
"ds_read_b128 a[156:159], v224 offset:1792\n\t"
"ds_read_b128 a[172:175], v224 offset:3840\n\t"
"v_mfma_f32_16x16x32_bf16 v[128:131], a[140:143], a[160:163], v[128:131]\n\t"
"s_waitcnt lgkmcnt(15)\n\t"
"s_mov_b32 s63, 147456\n\t"
"buffer_load_dwordx4 a[72:75], off, s[92:95], s63 offset:0 sc1\n\t"
"v_mfma_f32_16x16x32_bf16 v[132:135], a[136:139], a[148:151], v[132:135]\n\t"
"s_add_u32 s24, s24, 0xffd00000\n\t"
"ds_read_b64_tr_b16 a[196:197], v251 offset:256\n\t"
"ds_read_b64_tr_b16 a[202:203], v251 offset:128\n\t"
"v_mfma_f32_16x16x32_bf16 v[132:135], a[140:143], a[164:167], v[132:135]\n\t"
"buffer_load_dwordx4 a[76:79], off, s[92:95], s63 offset:1024 sc1\n\t"
"v_mfma_f32_16x16x32_bf16 v[116:119], a[128:131], a[148:151], v[116:119]\n\t"
"s_mov_b32 s46, 32768\n\t"
"ds_read_b64_tr_b16 a[200:201], v251 offset:0\n\t"
"ds_read_b64_tr_b16 a[204:205], v250 offset:256\n\t"
"v_mfma_f32_16x16x32_bf16 v[116:119], a[132:135], a[164:167], v[116:119]\n\t"
"buffer_load_dwordx4 a[80:83], off, s[92:95], s63 offset:2048 sc1\n\t"
"v_mfma_f32_16x16x32_bf16 v[100:103], a[120:123], a[148:151], v[100:103]\n\t"
"s_mov_b32 s49, 57344\n\t"
"s_mov_b32 s50, 65536\n\t"
"v_mfma_f32_16x16x32_bf16 v[100:103], a[124:127], a[164:167], v[100:103]\n\t"
"buffer_load_dwordx4 a[84:87], off, s[92:95], s63 offset:3072 sc1\n\t"
"s_waitcnt lgkmcnt(4)\n\t"
"s_barrier\n\t"
"v_mfma_f32_16x16x32_bf16 v[104:107], a[120:123], a[152:155], v[104:107]\n\t"
"ds_read_b128 a[160:163], v224 offset:18592\n\t"
"v_mfma_f32_16x16x32_bf16 v[104:107], a[124:127], a[168:171], v[104:107]\n\t"
"ds_read_b128 a[144:147], v224 offset:16544\n\t"
"v_mfma_f32_16x16x32_bf16 v[120:123], a[128:131], a[152:155], v[120:123]\n\t"
"ds_read_b128 a[148:151], v224 offset:16800\n\t"
"v_mfma_f32_16x16x32_bf16 v[120:123], a[132:135], a[168:171], v[120:123]\n\t"
"ds_read_b128 a[164:167], v224 offset:18848\n\t"
"v_mfma_f32_16x16x32_bf16 v[136:139], a[136:139], a[152:155], v[136:139]\n\t"
"v_mfma_f32_16x16x32_bf16 v[136:139], a[140:143], a[168:171], v[136:139]\n\t"
"buffer_load_dwordx4 a[88:91], off, s[96:99], s63 offset:0 sc1\n\t"
"v_mfma_f32_16x16x32_bf16 v[140:143], a[136:139], a[156:159], v[140:143]\n\t"
"ds_read_b128 a[168:171], v224 offset:19104\n\t"
"v_mfma_f32_16x16x32_bf16 v[140:143], a[140:143], a[172:175], v[140:143]\n\t"
"buffer_load_dwordx4 a[92:95], off, s[96:99], s63 offset:1024 sc1\n\t"
"v_mfma_f32_16x16x32_bf16 v[124:127], a[128:131], a[156:159], v[124:127]\n\t"
"ds_read_b128 a[152:155], v224 offset:17056\n\t"
"v_mfma_f32_16x16x32_bf16 v[124:127], a[132:135], a[172:175], v[124:127]\n\t"
"v_mfma_f32_16x16x32_bf16 v[108:111], a[120:123], a[156:159], v[108:111]\n\t"
"ds_read_b128 a[156:159], v224 offset:17312\n\t"
"v_mfma_f32_16x16x32_bf16 v[108:111], a[124:127], a[172:175], v[108:111]\n\t"
"s_waitcnt lgkmcnt(7)\n\t"
"s_mov_b32 s75, 221184\n\t"
"buffer_load_dwordx4 a[96:99], off, s[92:95], s75 offset:0 sc1\n\t"
"v_mfma_f32_4x4x4_16b_bf16 v[200:203], a[252:253], a[178:179], 0\n\t"
"ds_read_b128 a[172:175], v224 offset:19360\n\t"
"v_mfma_f32_4x4x4_16b_bf16 v[204:207], a[252:253], a[186:187], 0\n\t"
"ds_read_b64_tr_b16 a[178:179], v248 offset:20864\n\t"
"v_mfma_f32_4x4x4_16b_bf16 v[216:219], a[252:253], a[194:195], 0\n\t"
"ds_read_b64_tr_b16 a[186:187], v249 offset:20864\n\t"
"v_mfma_f32_4x4x4_16b_bf16 v[220:223], a[252:253], a[202:203], 0\n\t"
"buffer_load_dwordx4 a[100:103], off, s[92:95], s75 offset:1024 sc1\n\t"
"v_mfma_f32_4x4x4_16b_bf16 v[192:195], a[254:255], a[176:177], v[200:203]\n\t"
"ds_read_b64_tr_b16 a[194:195], v250 offset:20864\n\t"
"v_mfma_f32_4x4x4_16b_bf16 v[196:199], a[254:255], a[184:185], v[204:207]\n\t"
"ds_read_b64_tr_b16 a[202:203], v251 offset:20864\n\t"
"v_mfma_f32_4x4x4_16b_bf16 v[200:203], a[254:255], a[180:181], v[200:203]\n\t"
"ds_read_b64_tr_b16 a[176:177], v248 offset:20736\n\t"
"v_mfma_f32_4x4x4_16b_bf16 v[204:207], a[254:255], a[188:189], v[204:207]\n\t"
"v_mfma_f32_4x4x4_16b_bf16 v[208:211], a[254:255], a[192:193], v[216:219]\n\t"
"ds_read_b64_tr_b16 a[184:185], v249 offset:20736\n\t"
"v_mfma_f32_4x4x4_16b_bf16 v[212:215], a[254:255], a[200:201], v[220:223]\n\t"
"ds_read_b64_tr_b16 a[180:181], v249 offset:20992\n\t"
"v_mfma_f32_4x4x4_16b_bf16 v[216:219], a[254:255], a[196:197], v[216:219]\n\t"
"ds_read_b64_tr_b16 a[188:189], v248 offset:20992\n\t"
"v_mfma_f32_4x4x4_16b_bf16 v[220:223], a[254:255], a[204:205], v[220:223]\n\t"
"s_waitcnt vmcnt(25) lgkmcnt(14)\n\t"
"buffer_load_dwordx4 a[104:107], off, s[92:95], s75 offset:2048 sc1\n\t"
"v_mfma_f32_16x16x32_bf16 v[0:3], a[0:3], a[144:147], v[0:3]\n\t"
"ds_read_b64_tr_b16 a[192:193], v250 offset:20736\n\t"
"v_cvt_pk_bf16_f32 v193, -v193, -v197\n\t"
"v_mfma_f32_16x16x32_bf16 v[0:3], a[4:7], a[160:163], v[0:3]\n\t"
"s_waitcnt vmcnt(24)\n\t"
"buffer_load_dwordx4 a[108:111], off, s[92:95], s75 offset:3072 sc1\n\t"
"v_mfma_f32_16x16x32_bf16 v[16:19], a[8:11], a[144:147], v[16:19]\n\t"
"ds_read_b64_tr_b16 a[200:201], v251 offset:20736\n\t"
"v_cvt_pk_bf16_f32 v192, -v192, -v196\n\t"
"v_mfma_f32_16x16x32_bf16 v[16:19], a[12:15], a[160:163], v[16:19]\n\t"
"s_waitcnt vmcnt(22)\n\t"
"buffer_load_dwordx4 a[112:115], off, s[96:99], s75 offset:0 sc1\n\t"
"v_mfma_f32_16x16x32_bf16 v[32:35], a[16:19], a[144:147], v[32:35]\n\t"
"ds_read_b128 a[144:147], v224 offset:17568\n\t"
"v_cvt_pk_bf16_f32 v203, v203, v207\n\t"
"v_mfma_f32_16x16x32_bf16 v[32:35], a[20:23], a[160:163], v[32:35]\n\t"
"s_waitcnt lgkmcnt(15)\n\t"
"ds_write_addtid_b32 v193 offset:4128\n\t"
"v_cvt_pk_bf16_f32 v194, -v194, -v198\n\t"
"v_mfma_f32_16x16x32_bf16 v[36:39], a[16:19], a[148:151], v[36:39]\n\t"
"ds_read_b128 a[160:163], v224 offset:19616\n\t"
"v_cvt_pk_bf16_f32 v218, v218, v222\n\t"
"v_mfma_f32_16x16x32_bf16 v[36:39], a[20:23], a[164:167], v[36:39]\n\t"
"buffer_load_dwordx4 a[116:119], off, s[96:99], s75 offset:1024 sc1\n\t"
"v_mfma_f32_16x16x32_bf16 v[20:23], a[8:11], a[148:151], v[20:23]\n\t"
"s_mov_b32 s55, 106496\n\t"
"ds_write_addtid_b32 v203 offset:13472\n\t"
"v_cvt_pk_bf16_f32 v208, -v208, -v212\n\t"
"v_mfma_f32_16x16x32_bf16 v[20:23], a[12:15], a[164:167], v[20:23]\n\t"
"ds_write_addtid_b32 v218 offset:11392\n\t"
"v_cvt_pk_bf16_f32 v202, v202, v206\n\t"
"v_mfma_f32_16x16x32_bf16 v[4:7], a[0:3], a[148:151], v[4:7]\n\t"
"ds_read_b128 a[148:151], v224 offset:17824\n\t"
"v_cvt_pk_bf16_f32 v210, -v210, -v214\n\t"
"v_mfma_f32_16x16x32_bf16 v[4:7], a[4:7], a[164:167], v[4:7]\n\t"
"s_waitcnt lgkmcnt(15)\n\t"
"s_mov_b32 s78, 245760\n\t"
"buffer_load_dwordx4 a[120:123], off, s[92:95], s78 offset:0 sc1\n\t"
"v_mfma_f32_16x16x32_bf16 v[8:11], a[0:3], a[152:155], v[8:11]\n\t"
"ds_read_b128 a[164:167], v224 offset:19872\n\t"
"v_cvt_pk_bf16_f32 v201, v201, v205\n\t"
"v_mfma_f32_16x16x32_bf16 v[8:11], a[4:7], a[168:171], v[8:11]\n\t"
"ds_write_addtid_b32 v194 offset:1024\n\t"
"v_cvt_pk_bf16_f32 v211, -v211, -v215\n\t"
"v_mfma_f32_16x16x32_bf16 v[24:27], a[8:11], a[152:155], v[24:27]\n\t"
"ds_write_addtid_b32 v201 offset:12448\n\t"
"v_cvt_pk_bf16_f32 v217, v217, v221\n\t"
"v_mfma_f32_16x16x32_bf16 v[24:27], a[12:15], a[168:171], v[24:27]\n\t"
"buffer_load_dwordx4 a[124:127], off, s[92:95], s78 offset:1024 sc1\n\t"
"v_mfma_f32_16x16x32_bf16 v[40:43], a[16:19], a[152:155], v[40:43]\n\t"
"ds_read_b128 a[152:155], v224 offset:18080\n\t"
"v_cvt_pk_bf16_f32 v209, -v209, -v213\n\t"
"v_mfma_f32_16x16x32_bf16 v[40:43], a[20:23], a[168:171], v[40:43]\n\t"
"ds_read_b128 a[168:171], v224 offset:20128\n\t"
"v_cvt_pk_bf16_f32 v200, v200, v204\n\t"
"v_mfma_f32_16x16x32_bf16 v[44:47], a[16:19], a[156:159], v[44:47]\n\t"
"ds_write_addtid_b32 v202 offset:9344\n\t"
"v_cvt_pk_bf16_f32 v195, -v195, -v199\n\t"
"v_mfma_f32_16x16x32_bf16 v[44:47], a[20:23], a[172:175], v[44:47]\n\t"
"buffer_load_dwordx4 a[128:131], off, s[92:95], s78 offset:2048 sc1\n\t"
"v_mfma_f32_16x16x32_bf16 v[28:31], a[8:11], a[156:159], v[28:31]\n\t"
"s_mov_b32 s58, 131072\n\t"
"ds_write_addtid_b32 v195 offset:5152\n\t"
"ds_write_addtid_b32 v200 offset:8320\n\t"
"v_mfma_f32_16x16x32_bf16 v[28:31], a[12:15], a[172:175], v[28:31]\n\t"
"ds_write_addtid_b32 v209 offset:6176\n\t"
"v_cvt_pk_bf16_f32 v216, v216, v220\n\t"
"v_mfma_f32_16x16x32_bf16 v[12:15], a[0:3], a[156:159], v[12:15]\n\t"
"s_mov_b32 s56, 114688\n\t"
"ds_write_addtid_b32 v211 offset:7200\n\t"
"ds_write_addtid_b32 v217 offset:14496\n\t"
"v_mfma_f32_16x16x32_bf16 v[12:15], a[4:7], a[172:175], v[12:15]\n\t"
"s_waitcnt vmcnt(25) lgkmcnt(14)\n\t"
"buffer_load_dwordx4 a[132:135], off, s[92:95], s78 offset:3072 sc1\n\t"
"v_mfma_f32_16x16x32_bf16 v[96:99], a[24:27], a[144:147], v[96:99]\n\t"
"s_mov_b32 s59, 139264\n\t"
"ds_write_addtid_b32 v192 offset:0\n\t"
"ds_write_addtid_b32 v208 offset:2048\n\t"
"v_mfma_f32_16x16x32_bf16 v[96:99], a[28:31], a[160:163], v[96:99]\n\t"
"s_waitcnt vmcnt(24)\n\t"
"ds_write_addtid_b32 v210 offset:3072\n\t"
"v_cvt_pk_bf16_f32 v219, v219, v223\n\t"
"v_mfma_f32_16x16x32_bf16 v[112:115], a[32:35], a[144:147], v[112:115]\n\t"
"s_mov_b32 s52, 81920\n\t"
"ds_write_addtid_b32 v216 offset:10368\n\t"
"ds_write_addtid_b32 v219 offset:15520\n\t"
"v_mfma_f32_16x16x32_bf16 v[112:115], a[36:39], a[160:163], v[112:115]\n\t"
"s_waitcnt vmcnt(22)\n\t"
"buffer_load_dwordx4 a[136:139], off, s[96:99], s78 offset:0 sc1\n\t"
"v_mfma_f32_16x16x32_bf16 v[128:131], a[40:43], a[144:147], v[128:131]\n\t"
"ds_read_b128 a[172:175], v224 offset:20384\n\t"
"v_mfma_f32_16x16x32_bf16 v[128:131], a[44:47], a[160:163], v[128:131]\n\t"
"s_waitcnt lgkmcnt(15)\n\t"
"buffer_load_dwordx4 a[140:143], off, s[96:99], s78 offset:1024 sc1\n\t"
"v_mfma_f32_16x16x32_bf16 v[132:135], a[40:43], a[148:151], v[132:135]\n\t"
"s_mov_b32 s53, 90112\n\t"
"ds_read_b128 a[156:159], v224 offset:18336\n\t"
"v_mfma_f32_16x16x32_bf16 v[132:135], a[44:47], a[164:167], v[132:135]\n\t"
"buffer_load_dwordx4 a[248:251], v247, s[12:15], 0 offen offset:256 nt\n\t"
"v_mfma_f32_16x16x32_bf16 v[116:119], a[32:35], a[148:151], v[116:119]\n\t"
"s_mov_b32 s51, 73728\n\t"
"ds_read_b64_tr_b16 a[196:197], v251 offset:20992\n\t"
"ds_read_b64_tr_b16 a[204:205], v250 offset:20992\n\t"
"v_mfma_f32_16x16x32_bf16 v[116:119], a[36:39], a[164:167], v[116:119]\n\t"
"v_mfma_f32_16x16x32_bf16 v[100:103], a[24:27], a[148:151], v[100:103]\n\t"
"buffer_load_dwordx4 a[0:3], off, s[96:99], s58 offset:0 sc1\n\t"
"v_mfma_f32_16x16x32_bf16 v[100:103], a[28:31], a[164:167], v[100:103]\n\t"
"s_waitcnt lgkmcnt(2)\n\t"
"s_add_u32 s71, s71, 1\n\t"
"v_mov_b32 v215, s71\n\t"
"s_mov_b64 exec, 1\n\t"
"buffer_store_dword v215, off, s[8:11], s70 sc1\n\t"
"s_mov_b64 exec, -1\n\t"
"s_barrier\n\t"
"v_mfma_f32_16x16x32_bf16 v[104:107], a[24:27], a[152:155], v[104:107]\n\t"
"s_nop 0\n\t"
"ds_read_b128 a[144:147], v224 offset:0\n\t"
"ds_read_b128 a[160:163], v224 offset:2048\n\t"
"v_mfma_f32_16x16x32_bf16 v[104:107], a[28:31], a[168:171], v[104:107]\n\t"
"buffer_load_dwordx4 a[4:7], off, s[96:99], s58 offset:1024 sc1\n\t"
"v_mfma_f32_16x16x32_bf16 v[120:123], a[32:35], a[152:155], v[120:123]\n\t"
"ds_read_b128 a[148:151], v224 offset:256\n\t"
"v_mfma_f32_16x16x32_bf16 v[120:123], a[36:39], a[168:171], v[120:123]\n\t"
"ds_read_b128 a[164:167], v224 offset:2304\n\t"
"v_mfma_f32_16x16x32_bf16 v[136:139], a[40:43], a[152:155], v[136:139]\n\t"
"v_mfma_f32_16x16x32_bf16 v[136:139], a[44:47], a[168:171], v[136:139]\n\t"
"buffer_load_dwordx4 a[8:11], off, s[96:99], s58 offset:2048 sc1\n\t"
"v_mfma_f32_16x16x32_bf16 v[140:143], a[40:43], a[156:159], v[140:143]\n\t"
"v_accvgpr_read_b32 v248, a208\n\t"
"v_mfma_f32_16x16x32_bf16 v[140:143], a[44:47], a[172:175], v[140:143]\n\t"
"ds_read_b128 a[152:155], v224 offset:512\n\t"
"v_mfma_f32_16x16x32_bf16 v[124:127], a[32:35], a[156:159], v[124:127]\n\t"
"v_mfma_f32_16x16x32_bf16 v[124:127], a[36:39], a[172:175], v[124:127]\n\t"
"buffer_load_dwordx4 a[12:15], off, s[96:99], s58 offset:3072 sc1\n\t"
"v_mfma_f32_16x16x32_bf16 v[108:111], a[24:27], a[156:159], v[108:111]\n\t"
"v_mfma_f32_16x16x32_bf16 v[108:111], a[28:31], a[172:175], v[108:111]\n\t"
"s_waitcnt lgkmcnt(5)\n\t"
"buffer_load_dwordx4 a[16:19], off, s[92:95], s59 offset:0 sc1\n\t"
"v_mfma_f32_4x4x4_16b_bf16 v[200:203], a[252:253], a[178:179], 0\n\t"
"ds_read_b128 a[168:171], v224 offset:2560\n\t"
"v_mfma_f32_4x4x4_16b_bf16 v[204:207], a[252:253], a[186:187], 0\n\t"
"v_mfma_f32_4x4x4_16b_bf16 v[216:219], a[252:253], a[194:195], 0\n\t"
"v_mfma_f32_4x4x4_16b_bf16 v[220:223], a[252:253], a[202:203], 0\n\t"
".p2align 3\n\t"
"buffer_load_dwordx4 a[20:23], off, s[92:95], s59 offset:1024 sc1\n\t"
"v_pk_add_f32 v[96:97], v[0:1], v[96:97] neg_lo:[0,1] neg_hi:[0,1]\n\t"
"v_pk_add_f32 v[98:99], v[2:3], v[98:99] neg_lo:[0,1] neg_hi:[0,1]\n\t"
"v_pk_fma_f32 v[0:1], v[96:97], s[90:91], v[0:1] neg_lo:[1,0,0] neg_hi:[1,0,0]\n\t"
"v_pk_fma_f32 v[2:3], v[98:99], s[90:91], v[2:3] neg_lo:[1,0,0] neg_hi:[1,0,0]\n\t"
"v_pk_add_f32 v[112:113], v[16:17], v[112:113] neg_lo:[0,1] neg_hi:[0,1]\n\t"
"v_pk_add_f32 v[114:115], v[18:19], v[114:115] neg_lo:[0,1] neg_hi:[0,1]\n\t"
"v_pk_fma_f32 v[16:17], v[112:113], s[90:91], v[16:17] neg_lo:[1,0,0] neg_hi:[1,0,0]\n\t"
"v_pk_fma_f32 v[18:19], v[114:115], s[90:91], v[18:19] neg_lo:[1,0,0] neg_hi:[1,0,0]\n\t"
"v_pk_add_f32 v[128:129], v[32:33], v[128:129] neg_lo:[0,1] neg_hi:[0,1]\n\t"
"v_pk_add_f32 v[130:131], v[34:35], v[130:131] neg_lo:[0,1] neg_hi:[0,1]\n\t"
"v_pk_fma_f32 v[32:33], v[128:129], s[90:91], v[32:33] neg_lo:[1,0,0] neg_hi:[1,0,0]\n\t"
"v_pk_fma_f32 v[34:35], v[130:131], s[90:91], v[34:35] neg_lo:[1,0,0] neg_hi:[1,0,0]\n\t"
"v_pk_add_f32 v[132:133], v[36:37], v[132:133] neg_lo:[0,1] neg_hi:[0,1]\n\t"
"v_pk_add_f32 v[134:135], v[38:39], v[134:135] neg_lo:[0,1] neg_hi:[0,1]\n\t"
"v_pk_fma_f32 v[36:37], v[132:133], s[90:91], v[36:37] neg_lo:[1,0,0] neg_hi:[1,0,0]\n\t"
"v_pk_fma_f32 v[38:39], v[134:135], s[90:91], v[38:39] neg_lo:[1,0,0] neg_hi:[1,0,0]\n\t"
"v_pk_add_f32 v[116:117], v[20:21], v[116:117] neg_lo:[0,1] neg_hi:[0,1]\n\t"
"v_pk_add_f32 v[118:119], v[22:23], v[118:119] neg_lo:[0,1] neg_hi:[0,1]\n\t"
"v_pk_fma_f32 v[20:21], v[116:117], s[90:91], v[20:21] neg_lo:[1,0,0] neg_hi:[1,0,0]\n\t"
"v_pk_fma_f32 v[22:23], v[118:119], s[90:91], v[22:23] neg_lo:[1,0,0] neg_hi:[1,0,0]\n\t"
"v_pk_add_f32 v[100:101], v[4:5], v[100:101] neg_lo:[0,1] neg_hi:[0,1]\n\t"
"v_pk_add_f32 v[102:103], v[6:7], v[102:103] neg_lo:[0,1] neg_hi:[0,1]\n\t"
"v_pk_fma_f32 v[4:5], v[100:101], s[90:91], v[4:5] neg_lo:[1,0,0] neg_hi:[1,0,0]\n\t"
"v_pk_fma_f32 v[6:7], v[102:103], s[90:91], v[6:7] neg_lo:[1,0,0] neg_hi:[1,0,0]\n\t"
"v_pk_add_f32 v[104:105], v[8:9], v[104:105] neg_lo:[0,1] neg_hi:[0,1]\n\t"
"v_pk_add_f32 v[106:107], v[10:11], v[106:107] neg_lo:[0,1] neg_hi:[0,1]\n\t"
"v_pk_fma_f32 v[8:9], v[104:105], s[90:91], v[8:9] neg_lo:[1,0,0] neg_hi:[1,0,0]\n\t"
"v_pk_fma_f32 v[10:11], v[106:107], s[90:91], v[10:11] neg_lo:[1,0,0] neg_hi:[1,0,0]\n\t"
"v_pk_add_f32 v[120:121], v[24:25], v[120:121] neg_lo:[0,1] neg_hi:[0,1]\n\t"
"v_pk_add_f32 v[122:123], v[26:27], v[122:123] neg_lo:[0,1] neg_hi:[0,1]\n\t"
"v_pk_fma_f32 v[24:25], v[120:121], s[90:91], v[24:25] neg_lo:[1,0,0] neg_hi:[1,0,0]\n\t"
"v_pk_fma_f32 v[26:27], v[122:123], s[90:91], v[26:27] neg_lo:[1,0,0] neg_hi:[1,0,0]\n\t"
"v_pk_add_f32 v[136:137], v[40:41], v[136:137] neg_lo:[0,1] neg_hi:[0,1]\n\t"
"v_pk_add_f32 v[138:139], v[42:43], v[138:139] neg_lo:[0,1] neg_hi:[0,1]\n\t"
"v_pk_fma_f32 v[40:41], v[136:137], s[90:91], v[40:41] neg_lo:[1,0,0] neg_hi:[1,0,0]\n\t"
"v_pk_fma_f32 v[42:43], v[138:139], s[90:91], v[42:43] neg_lo:[1,0,0] neg_hi:[1,0,0]\n\t"
"v_pk_add_f32 v[140:141], v[44:45], v[140:141] neg_lo:[0,1] neg_hi:[0,1]\n\t"
"v_pk_add_f32 v[142:143], v[46:47], v[142:143] neg_lo:[0,1] neg_hi:[0,1]\n\t"
"v_pk_fma_f32 v[44:45], v[140:141], s[90:91], v[44:45] neg_lo:[1,0,0] neg_hi:[1,0,0]\n\t"
"v_pk_fma_f32 v[46:47], v[142:143], s[90:91], v[46:47] neg_lo:[1,0,0] neg_hi:[1,0,0]\n\t"
"v_pk_add_f32 v[124:125], v[28:29], v[124:125] neg_lo:[0,1] neg_hi:[0,1]\n\t"
"v_pk_add_f32 v[126:127], v[30:31], v[126:127] neg_lo:[0,1] neg_hi:[0,1]\n\t"
"v_pk_fma_f32 v[28:29], v[124:125], s[90:91], v[28:29] neg_lo:[1,0,0] neg_hi:[1,0,0]\n\t"
"v_pk_fma_f32 v[30:31], v[126:127], s[90:91], v[30:31] neg_lo:[1,0,0] neg_hi:[1,0,0]\n\t"
"v_pk_add_f32 v[108:109], v[12:13], v[108:109] neg_lo:[0,1] neg_hi:[0,1]\n\t"
"v_pk_add_f32 v[110:111], v[14:15], v[110:111] neg_lo:[0,1] neg_hi:[0,1]\n\t"
"v_pk_fma_f32 v[12:13], v[108:109], s[90:91], v[12:13] neg_lo:[1,0,0] neg_hi:[1,0,0]\n\t"
"v_pk_fma_f32 v[14:15], v[110:111], s[90:91], v[14:15] neg_lo:[1,0,0] neg_hi:[1,0,0]\n\t"
"v_mfma_f32_4x4x4_16b_bf16 v[192:195], a[254:255], a[176:177], v[200:203]\n\t"
"ds_read_b128 a[172:175], v224 offset:2816\n\t"
"v_mfma_f32_4x4x4_16b_bf16 v[196:199], a[254:255], a[184:185], v[204:207]\n\t"
"v_mfma_f32_4x4x4_16b_bf16 v[200:203], a[254:255], a[180:181], v[200:203]\n\t"
"buffer_load_dwordx4 a[24:27], off, s[96:99], s57 offset:2048 sc1\n\t"
"v_mfma_f32_4x4x4_16b_bf16 v[204:207], a[254:255], a[188:189], v[204:207]\n\t"
"v_mfma_f32_4x4x4_16b_bf16 v[208:211], a[254:255], a[192:193], v[216:219]\n\t"
"ds_read_b128 a[156:159], v224 offset:768\n\t"
"v_mfma_f32_4x4x4_16b_bf16 v[212:215], a[254:255], a[200:201], v[220:223]\n\t"
"v_mfma_f32_4x4x4_16b_bf16 v[216:219], a[254:255], a[196:197], v[216:219]\n\t"
"v_mfma_f32_4x4x4_16b_bf16 v[220:223], a[254:255], a[204:205], v[220:223]\n\t"
"s_waitcnt vmcnt(31) lgkmcnt(6)\n\t"
"buffer_load_dwordx4 a[28:31], off, s[96:99], s57 offset:3072 sc1\n\t"
"v_mfma_f32_16x16x32_bf16 v[0:3], a[48:51], a[144:147], v[0:3]\n\t"
"s_nop 0\n\t"
"v_cvt_pk_bf16_f32 v192, -v192, -v196\n\t"
"v_cvt_pk_bf16_f32 v194, -v194, -v198\n\t"
"v_mfma_f32_16x16x32_bf16 v[0:3], a[52:55], a[160:163], v[0:3]\n\t"
"s_waitcnt vmcnt(30)\n\t"
"ds_write_addtid_b32 v192 offset:16544\n\t"
"ds_write_addtid_b32 v194 offset:17568\n\t"
"s_mov_b32 s64, 155648\n\t"
"v_mfma_f32_16x16x32_bf16 v[16:19], a[56:59], a[144:147], v[16:19]\n\t"
"s_nop 0\n\t"
"v_cvt_pk_bf16_f32 v193, -v193, -v197\n\t"
"v_cvt_pk_bf16_f32 v195, -v195, -v199\n\t"
"v_mfma_f32_16x16x32_bf16 v[16:19], a[60:63], a[160:163], v[16:19]\n\t"
"s_waitcnt vmcnt(28)\n\t"
"buffer_load_dwordx4 a[32:35], off, s[92:95], s58 offset:0 sc1\n\t"
"v_mfma_f32_16x16x32_bf16 v[32:35], a[64:67], a[144:147], v[32:35]\n\t"
"s_nop 0\n\t"
"ds_read_b128 a[144:147], v224 offset:1024\n\t"
"v_cvt_pk_bf16_f32 v203, v203, v207\n\t"
"v_mfma_f32_16x16x32_bf16 v[32:35], a[68:71], a[160:163], v[32:35]\n\t"
"s_waitcnt lgkmcnt(7)\n\t"
"buffer_load_dwordx4 a[36:39], off, s[92:95], s58 offset:1024 sc1\n\t"
"v_mfma_f32_16x16x32_bf16 v[36:39], a[64:67], a[148:151], v[36:39]\n\t"
"s_nop 0\n\t"
"ds_read_b128 a[160:163], v224 offset:3072\n\t"
"v_cvt_pk_bf16_f32 v201, v201, v205\n\t"
"v_mfma_f32_16x16x32_bf16 v[36:39], a[68:71], a[164:167], v[36:39]\n\t"
"s_nop 0\n\t"
"v_cvt_pk_bf16_f32 v200, v200, v204\n\t"
"v_cvt_pk_bf16_f32 v217, v217, v221\n\t"
"v_mfma_f32_16x16x32_bf16 v[20:23], a[56:59], a[148:151], v[20:23]\n\t"
"s_nop 0\n\t"
"ds_write_addtid_b32 v201 offset:28992\n\t"
"v_cvt_pk_bf16_f32 v208, -v208, -v212\n\t"
"v_mfma_f32_16x16x32_bf16 v[20:23], a[60:63], a[164:167], v[20:23]\n\t"
"buffer_load_dwordx4 a[40:43], off, s[92:95], s58 offset:2048 sc1\n\t"
"v_mfma_f32_16x16x32_bf16 v[4:7], a[48:51], a[148:151], v[4:7]\n\t"
"s_nop 0\n\t"
"ds_read_b128 a[148:151], v224 offset:1280\n\t"
"v_cvt_pk_bf16_f32 v218, v218, v222\n\t"
"v_mfma_f32_16x16x32_bf16 v[4:7], a[52:55], a[164:167], v[4:7]\n\t"
"s_waitcnt lgkmcnt(8)\n\t"
"ds_read_b128 a[164:167], v224 offset:3328\n\t"
"v_cvt_pk_bf16_f32 v219, v219, v223\n\t"
"v_mfma_f32_16x16x32_bf16 v[8:11], a[48:51], a[152:155], v[8:11]\n\t"
"s_nop 0\n\t"
"ds_write_addtid_b32 v208 offset:18592\n\t"
"v_cvt_pk_bf16_f32 v209, -v209, -v213\n\t"
"v_mfma_f32_16x16x32_bf16 v[8:11], a[52:55], a[168:171], v[8:11]\n\t"
"buffer_load_dwordx4 a[44:47], off, s[92:95], s58 offset:3072 sc1\n\t"
"v_mfma_f32_16x16x32_bf16 v[24:27], a[56:59], a[152:155], v[24:27]\n\t"
"s_mov_b32 s67, 180224\n\t"
"ds_write_addtid_b32 v193 offset:20672\n\t"
"ds_write_addtid_b32 v209 offset:22720\n\t"
"v_mfma_f32_16x16x32_bf16 v[24:27], a[60:63], a[168:171], v[24:27]\n\t"
"s_nop 0\n\t"
"ds_write_addtid_b32 v217 offset:31040\n\t"
"v_mfma_f32_16x16x32_bf16 v[40:43], a[64:67], a[152:155], v[40:43]\n\t"
"s_nop 0\n\t"
"ds_write_addtid_b32 v218 offset:27936\n\t"
"v_cvt_pk_bf16_f32 v211, -v211, -v215\n\t"
"v_mfma_f32_16x16x32_bf16 v[40:43], a[68:71], a[168:171], v[40:43]\n\t"
"s_waitcnt lgkmcnt(12)\n\t"
"ds_write_addtid_b32 v219 offset:32064\n\t"
"v_cvt_pk_bf16_f32 v202, v202, v206\n\t"
"v_mfma_f32_16x16x32_bf16 v[44:47], a[64:67], a[156:159], v[44:47]\n\t"
"s_add_u32 m0, s43, 33280\n\t"
"ds_read_b128 a[168:171], v224 offset:3584\n\t"
"v_mfma_f32_16x16x32_bf16 v[44:47], a[68:71], a[172:175], v[44:47]\n\t"
"s_mov_b64 exec, s[44:45]\n\t"
"buffer_load_dwordx4 v247, s[12:15], 0 offen offset:0 nt lds\n\t"
"s_mov_b64 exec, -1\n\t"
"v_mfma_f32_16x16x32_bf16 v[28:31], a[56:59], a[156:159], v[28:31]\n\t"
"s_add_u32 m0, s26, 35584\n\t"
"ds_read_b128 a[152:155], v224 offset:1536\n\t"
"v_mfma_f32_16x16x32_bf16 v[28:31], a[60:63], a[172:175], v[28:31]\n\t"
"buffer_load_dwordx4 v236, s[12:15], s22 offen offset:0 nt lds\n\t"
"v_mfma_f32_16x16x32_bf16 v[12:15], a[48:51], a[156:159], v[12:15]\n\t"
"s_lshl_b32 m0, s73, 8\n\t"
"v_cvt_pk_bf16_f32 v210, -v210, -v214\n\t"
"v_cvt_pk_bf16_f32 v216, v216, v220\n\t"
"v_mfma_f32_16x16x32_bf16 v[12:15], a[52:55], a[172:175], v[12:15]\n\t"
"s_waitcnt vmcnt(32) lgkmcnt(11)\n\t"
"ds_write_addtid_b32 v210 offset:19616\n\t"
"ds_write_addtid_b32 v216 offset:26912\n\t"
"s_mov_b32 s74, 212992\n\t"
"v_mfma_f32_16x16x32_bf16 v[96:99], a[72:75], a[144:147], v[96:99]\n\t"
"s_nop 0\n\t"
"ds_write_addtid_b32 v195 offset:21696\n\t"
"v_mfma_f32_16x16x32_bf16 v[96:99], a[76:79], a[160:163], v[96:99]\n\t"
"s_waitcnt vmcnt(30)\n\t"
"ds_write_addtid_b32 v200 offset:24864\n\t"
"ds_write_addtid_b32 v203 offset:30016\n\t"
"s_mov_b32 s68, 188416\n\t"
"v_mfma_f32_16x16x32_bf16 v[112:115], a[80:83], a[144:147], v[112:115]\n\t"
"s_mov_b32 s72, 204800\n\t"
"ds_write_addtid_b32 v202 offset:25888\n\t"
"ds_write_addtid_b32 v211 offset:23744\n\t"
"v_mfma_f32_16x16x32_bf16 v[112:115], a[84:87], a[160:163], v[112:115]\n\t"
"s_waitcnt vmcnt(28)\n\t"
"v_mfma_f32_16x16x32_bf16 v[128:131], a[88:91], a[144:147], v[128:131]\n\t"
"s_add_u32 m0, s26, 35584\n\t"
"v_add_u32 v248, 101632, v248\n\t"
"v_xor_b32 v249, 32, v248\n\t"
"v_mfma_f32_16x16x32_bf16 v[128:131], a[92:95], a[160:163], v[128:131]\n\t"
"s_waitcnt lgkmcnt(15)\n\t"
"ds_read_b128 a[172:175], v224 offset:3840\n\t"
"s_mov_b32 s65, 163840\n\t"
"v_mfma_f32_16x16x32_bf16 v[132:135], a[88:91], a[148:151], v[132:135]\n\t"
"s_nop 0\n\t"
"ds_read_b128 a[156:159], v224 offset:1792\n\t"
"v_mfma_f32_16x16x32_bf16 v[132:135], a[92:95], a[164:167], v[132:135]\n\t"
"s_nop 0\n\t"
"v_xor_b32 v250, 64, v248\n\t"
"v_mfma_f32_16x16x32_bf16 v[116:119], a[80:83], a[148:151], v[116:119]\n\t"
"s_nop 0\n\t"
"v_mfma_f32_16x16x32_bf16 v[116:119], a[84:87], a[164:167], v[116:119]\n\t"
"s_nop 0\n\t"
"v_xor_b32 v251, 96, v248\n\t"
"v_mfma_f32_16x16x32_bf16 v[100:103], a[72:75], a[148:151], v[100:103]\n\t"
"buffer_load_dwordx4 v237, s[12:15], s22 offen offset:1024 nt lds\n\t"
"v_mfma_f32_16x16x32_bf16 v[100:103], a[76:79], a[164:167], v[100:103]\n\t"
"s_lshl_b32 m0, s73, 8\n\t"
"s_waitcnt lgkmcnt(0)\n\t"
"s_barrier\n\t"
"v_mfma_f32_16x16x32_bf16 v[104:107], a[72:75], a[152:155], v[104:107]\n\t"
"s_add_u32 m0, s26, 35584\n\t"
"ds_read_b128 a[144:147], v224 offset:16544\n\t"
"ds_read_b128 a[160:163], v224 offset:18592\n\t"
"v_mfma_f32_16x16x32_bf16 v[104:107], a[76:79], a[168:171], v[104:107]\n\t"
"s_nop 0\n\t"
"ds_read_b128 a[164:167], v224 offset:18848\n\t"
"v_mfma_f32_16x16x32_bf16 v[120:123], a[80:83], a[152:155], v[120:123]\n\t"
"s_nop 0\n\t"
"ds_read_b128 a[148:151], v224 offset:16800\n\t"
"v_mfma_f32_16x16x32_bf16 v[120:123], a[84:87], a[168:171], v[120:123]\n\t"
"buffer_load_dwordx4 v238, s[12:15], s22 offen offset:2048 nt lds\n\t"
"v_mfma_f32_16x16x32_bf16 v[136:139], a[88:91], a[152:155], v[136:139]\n\t"
"s_mov_b32 s76, 229376\n\t"
"ds_read_b64_tr_b16 a[176:177], v248 offset:20736\n\t"
"ds_read_b64_tr_b16 a[178:179], v249 offset:20736\n\t"
"v_mfma_f32_16x16x32_bf16 v[136:139], a[92:95], a[168:171], v[136:139]\n\t"
"s_nop 0\n\t"
"ds_read_b64_tr_b16 a[180:181], v248 offset:21248\n\t"
"ds_read_b64_tr_b16 a[182:183], v249 offset:21248\n\t"
"v_mfma_f32_16x16x32_bf16 v[140:143], a[88:91], a[156:159], v[140:143]\n\t"
"s_nop 0\n\t"
"ds_read_b64_tr_b16 a[184:185], v250 offset:23040\n\t"
"v_mfma_f32_16x16x32_bf16 v[140:143], a[92:95], a[172:175], v[140:143]\n\t"
"s_nop 0\n\t"
"ds_read_b64_tr_b16 a[186:187], v251 offset:23040\n\t"
"v_mfma_f32_16x16x32_bf16 v[124:127], a[80:83], a[156:159], v[124:127]\n\t"
"s_nop 0\n\t"
"ds_read_b64_tr_b16 a[188:189], v250 offset:23552\n\t"
"v_mfma_f32_16x16x32_bf16 v[124:127], a[84:87], a[172:175], v[124:127]\n\t"
"s_waitcnt lgkmcnt(3)\n\t"
"ds_read_b64_tr_b16 a[190:191], v251 offset:23552\n\t"
"v_mfma_f32_4x4x4_16b_bf16 v[192:195], a[210:211], a[176:177], 0\n\t"
"ds_read_b64_tr_b16 a[192:193], v250 offset:20736\n\t"
"v_mfma_f32_4x4x4_16b_bf16 v[196:199], a[210:211], a[178:179], 0\n\t"
"ds_read_b128 a[152:155], v224 offset:17056\n\t"
"v_mfma_f32_4x4x4_16b_bf16 v[200:203], a[210:211], a[180:181], 0\n\t"
"ds_read_b64_tr_b16 a[194:195], v251 offset:20736\n\t"
"v_mfma_f32_4x4x4_16b_bf16 v[204:207], a[210:211], a[182:183], 0\n\t"
"buffer_load_dwordx4 v239, s[12:15], s22 offen offset:3072 nt lds\n\t"
"v_mfma_f32_16x16x32_bf16 v[108:111], a[72:75], a[156:159], v[108:111]\n\t"
"s_nop 0\n\t"
"ds_read_b64_tr_b16 a[196:197], v250 offset:21248\n\t"
"ds_read_b64_tr_b16 a[198:199], v251 offset:21248\n\t"
"v_mfma_f32_16x16x32_bf16 v[108:111], a[76:79], a[172:175], v[108:111]\n\t"
"s_waitcnt vmcnt(29)\n\t"
"ds_read_b64_tr_b16 a[200:201], v248 offset:23040\n\t"
"ds_read_b64_tr_b16 a[202:203], v249 offset:23040\n\t"
"s_mov_b32 s77, 237568\n\t"
"v_mfma_f32_16x16x32_bf16 v[0:3], a[96:99], a[144:147], v[0:3]\n\t"
"s_nop 0\n\t"
"v_cvt_pk_bf16_f32 v196, v192, v196\n\t"
"v_cvt_pk_bf16_f32 v194, v194, v198\n\t"
"v_mfma_f32_16x16x32_bf16 v[0:3], a[100:103], a[160:163], v[0:3]\n\t"
"s_waitcnt vmcnt(27)\n\t"
"v_cvt_pk_bf16_f32 v197, v193, v197\n\t"
"v_cvt_pk_bf16_f32 v195, v195, v199\n\t"
"v_mfma_f32_16x16x32_bf16 v[16:19], a[104:107], a[144:147], v[16:19]\n\t"
"s_nop 0\n\t"
"ds_read_b128 a[156:159], v224 offset:17312\n\t"
"v_cvt_pk_bf16_f32 v199, v201, v205\n\t"
"v_mfma_f32_16x16x32_bf16 v[16:19], a[108:111], a[160:163], v[16:19]\n\t"
"s_waitcnt vmcnt(25)\n\t"
"ds_read_b128 a[168:171], v224 offset:19104\n\t"
"v_cvt_pk_bf16_f32 v193, v203, v207\n\t"
"v_mfma_f32_16x16x32_bf16 v[32:35], a[112:115], a[144:147], v[32:35]\n\t"
"s_nop 0\n\t"
"ds_read_b64_tr_b16 a[204:205], v248 offset:23552\n\t"
"v_cvt_pk_bf16_f32 v192, v202, v206\n\t"
"v_mfma_f32_16x16x32_bf16 v[32:35], a[116:119], a[160:163], v[32:35]\n\t"
"s_waitcnt lgkmcnt(10)\n\t"
"s_mov_b64 exec, s[36:37]\n\t"
"buffer_load_dwordx4 v240, s[12:15], s22 offen offset:3584 nt lds\n\t"
"s_mov_b64 exec, -1\n\t"
"v_mfma_f32_4x4x4_16b_bf16 v[208:211], a[210:211], a[184:185], 0\n\t"
"ds_read_b128 a[160:163], v224 offset:19616\n\t"
"v_mfma_f32_4x4x4_16b_bf16 v[212:215], a[210:211], a[186:187], 0\n\t"
"ds_read_b64_tr_b16 a[206:207], v249 offset:23552\n\t"
"v_mfma_f32_4x4x4_16b_bf16 v[216:219], a[210:211], a[188:189], 0\n\t"
"ds_read_b128 a[172:175], v224 offset:19360\n\t"
"v_mfma_f32_4x4x4_16b_bf16 v[220:223], a[210:211], a[190:191], 0\n\t"
"buffer_load_dwordx4 a[48:51], off, s[96:99], s64 offset:0 sc1\n\t"
"v_mfma_f32_16x16x32_bf16 v[36:39], a[112:115], a[148:151], v[36:39]\n\t"
"s_lshl_b32 m0, s73, 8\n\t"
"ds_read_b128 a[144:147], v224 offset:17568\n\t"
"v_cvt_pk_bf16_f32 v198, v200, v204\n\t"
"v_mfma_f32_16x16x32_bf16 v[36:39], a[116:119], a[164:167], v[36:39]\n\t"
"buffer_load_dwordx4 a[52:55], off, s[96:99], s64 offset:1024 sc1\n\t"
"v_mfma_f32_16x16x32_bf16 v[20:23], a[104:107], a[148:151], v[20:23]\n\t"
"s_nop 0\n\t"
"v_cvt_pk_bf16_f32 v212, v208, v212\n\t"
"v_cvt_pk_bf16_f32 v213, v209, v213\n\t"
"v_mfma_f32_16x16x32_bf16 v[20:23], a[108:111], a[164:167], v[20:23]\n\t"
"s_nop 0\n\t"
"ds_write_addtid_b32 v199 offset:9344\n\t"
"buffer_load_dwordx4 a[56:59], off, s[96:99], s64 offset:2048 sc1\n\t"
"v_mfma_f32_16x16x32_bf16 v[4:7], a[96:99], a[148:151], v[4:7]\n\t"
"s_nop 0\n\t"
"ds_write_addtid_b32 v196 offset:0\n\t"
"v_cvt_pk_bf16_f32 v211, v211, v215\n\t"
"v_mfma_f32_16x16x32_bf16 v[4:7], a[100:103], a[164:167], v[4:7]\n\t"
"s_waitcnt lgkmcnt(11)\n\t"
"ds_write_addtid_b32 v197 offset:1024\n\t"
"v_cvt_pk_bf16_f32 v208, v218, v222\n\t"
"v_mfma_f32_4x4x4_16b_bf16 v[200:203], a[210:211], a[192:193], 0\n\t"
"ds_write_addtid_b32 v198 offset:8320\n\t"
"buffer_load_dwordx4 a[60:63], off, s[96:99], s64 offset:3072 sc1\n\t"
"v_cvt_pk_bf16_f32 v215, v217, v221\n\t"
"v_mfma_f32_4x4x4_16b_bf16 v[204:207], a[210:211], a[194:195], 0\n\t"
"ds_read_b128 a[164:167], v224 offset:19872\n\t"
"v_cvt_pk_bf16_f32 v210, v210, v214\n\t"
"v_mfma_f32_4x4x4_16b_bf16 v[196:199], a[210:211], a[196:197], 0\n\t"
"v_cvt_pk_bf16_f32 v214, v216, v220\n\t"
"v_cvt_pk_bf16_f32 v209, v219, v223\n\t"
"v_mfma_f32_4x4x4_16b_bf16 v[216:219], a[210:211], a[198:199], 0\n\t"
"s_waitcnt lgkmcnt(10)\n\t"
"buffer_load_dwordx4 a[64:67], off, s[92:95], s65 offset:0 sc1\n\t"
"v_mfma_f32_16x16x32_bf16 v[8:11], a[96:99], a[152:155], v[8:11]\n\t"
"s_mov_b32 s80, 262144\n\t"
"ds_read_b128 a[148:151], v224 offset:17824\n\t"
"ds_write_addtid_b32 v212 offset:4128\n\t"
"v_mfma_f32_16x16x32_bf16 v[8:11], a[100:103], a[168:171], v[8:11]\n\t"
"buffer_load_dwordx4 a[68:71], off, s[92:95], s65 offset:1024 sc1\n\t"
"v_mfma_f32_16x16x32_bf16 v[24:27], a[104:107], a[152:155], v[24:27]\n\t"
"s_nop 0\n\t"
"v_cvt_pk_bf16_f32 v202, v202, v206\n\t"
"v_cvt_pk_bf16_f32 v203, v203, v207\n\t"
"v_mfma_f32_16x16x32_bf16 v[24:27], a[108:111], a[168:171], v[24:27]\n\t"
"buffer_load_dwordx4 a[72:75], off, s[96:99], s63 offset:2048 sc1\n\t"
"v_mfma_f32_16x16x32_bf16 v[40:43], a[112:115], a[152:155], v[40:43]\n\t"
"s_nop 0\n\t"
"ds_read_b128 a[152:155], v224 offset:18080\n\t"
"v_cvt_pk_bf16_f32 v206, v196, v216\n\t"
"v_mfma_f32_16x16x32_bf16 v[40:43], a[116:119], a[168:171], v[40:43]\n\t"
"s_waitcnt lgkmcnt(9)\n\t"
"buffer_load_dwordx4 a[76:79], off, s[96:99], s63 offset:3072 sc1\n\t"
"v_mfma_f32_16x16x32_bf16 v[44:47], a[112:115], a[156:159], v[44:47]\n\t"
"s_nop 0\n\t"
"ds_write_addtid_b32 v215 offset:13472\n\t"
"v_cvt_pk_bf16_f32 v204, v200, v204\n\t"
"v_mfma_f32_16x16x32_bf16 v[44:47], a[116:119], a[172:175], v[44:47]\n\t"
"Lpad2_%=:\n\t"
"s_cbranch_execz Lpad6_%=\n\t"
"ds_write_addtid_b32 v213 offset:5152\n\t"
"v_cvt_pk_bf16_f32 v205, v201, v205\n\t"
"v_mfma_f32_4x4x4_16b_bf16 v[220:223], a[210:211], a[200:201], 0\n\t"
"ds_write_addtid_b32 v214 offset:12448\n\t"
"buffer_load_dwordx4 a[80:83], off, s[92:95], s64 offset:0 sc1\n\t"
"v_mfma_f32_4x4x4_16b_bf16 v[212:215], a[210:211], a[202:203], 0\n\t"
"v_cvt_pk_bf16_f32 v200, v198, v218\n\t"
"v_cvt_pk_bf16_f32 v201, v199, v219\n\t"
"v_cvt_pk_bf16_f32 v207, v197, v217\n\t"
"v_mfma_f32_4x4x4_16b_bf16 v[196:199], a[210:211], a[204:205], 0\n\t"
"ds_write_addtid_b32 v206 offset:10368\n\t"
"v_mfma_f32_4x4x4_16b_bf16 v[216:219], a[210:211], a[206:207], 0\n\t"
"buffer_load_dwordx4 a[84:87], off, s[92:95], s64 offset:1024 sc1\n\t"
"v_mfma_f32_16x16x32_bf16 v[28:31], a[104:107], a[156:159], v[28:31]\n\t"
"s_mov_b32 s82, 278528\n\t"
"ds_write_addtid_b32 v204 offset:2048\n\t"
"ds_write_addtid_b32 v205 offset:3072\n\t"
"v_mfma_f32_16x16x32_bf16 v[28:31], a[108:111], a[172:175], v[28:31]\n\t"
"buffer_load_dwordx4 a[88:91], off, s[92:95], s64 offset:2048 sc1\n\t"
"v_mfma_f32_16x16x32_bf16 v[12:15], a[96:99], a[156:159], v[12:15]\n\t"
"ds_write_addtid_b32 v207 offset:11392\n\t"
"v_cvt_pk_bf16_f32 v212, v220, v212\n\t"
"v_mfma_f32_16x16x32_bf16 v[12:15], a[100:103], a[172:175], v[12:15]\n\t"
"s_waitcnt vmcnt(35) lgkmcnt(15)\n\t"
"v_cvt_pk_bf16_f32 v222, v222, v214\n\t"
"v_cvt_pk_bf16_f32 v213, v221, v213\n\t"
"v_mfma_f32_16x16x32_bf16 v[96:99], a[120:123], a[144:147], v[96:99]\n\t"
"ds_write_addtid_b32 v213 offset:7200\n\t"
"v_cvt_pk_bf16_f32 v223, v223, v215\n\t"
"v_mfma_f32_16x16x32_bf16 v[96:99], a[124:127], a[160:163], v[96:99]\n\t"
"s_waitcnt vmcnt(33)\n\t"
"ds_write_addtid_b32 v212 offset:6176\n\t"
"v_cvt_pk_bf16_f32 v214, v196, v216\n\t"
"v_cvt_pk_bf16_f32 v215, v197, v217\n\t"
"v_mfma_f32_16x16x32_bf16 v[112:115], a[128:131], a[144:147], v[112:115]\n\t"
"s_mov_b32 s79, 253952\n\t"
"ds_write_addtid_b32 v214 offset:14496\n\t"
"ds_write_addtid_b32 v215 offset:15520\n\t"
"v_mfma_f32_16x16x32_bf16 v[112:115], a[132:135], a[160:163], v[112:115]\n\t"
"s_waitcnt vmcnt(31)\n\t"
"buffer_load_dwordx4 a[92:95], off, s[92:95], s64 offset:3072 sc1\n\t"
"v_mfma_f32_16x16x32_bf16 v[128:131], a[136:139], a[144:147], v[128:131]\n\t"
"ds_read_b128 a[172:175], v224 offset:20384\n\t"
"v_cvt_pk_bf16_f32 v220, v198, v218\n\t"
"v_mfma_f32_16x16x32_bf16 v[128:131], a[140:143], a[160:163], v[128:131]\n\t"
"s_waitcnt lgkmcnt(14)\n\t"
"buffer_load_dwordx4 a[108:111], off, s[96:99], s46 offset:3072 sc1\n\t"
"v_mfma_f32_16x16x32_bf16 v[132:135], a[136:139], a[148:151], v[132:135]\n\t"
"s_mov_b32 s83, 286720\n\t"
"ds_read_b128 a[168:171], v224 offset:20128\n\t"
"ds_read_b128 a[156:159], v224 offset:18336\n\t"
"v_mfma_f32_16x16x32_bf16 v[132:135], a[140:143], a[164:167], v[132:135]\n\t"
"buffer_load_dwordx4 a[112:115], off, s[92:95], s47 offset:0 sc1\n\t"
"v_mfma_f32_16x16x32_bf16 v[116:119], a[128:131], a[148:151], v[116:119]\n\t"
"v_accvgpr_read_b32 v224, a213\n\t"
"v_cvt_pk_bf16_f32 v221, v199, v219\n\t"
"v_mfma_f32_16x16x32_bf16 v[116:119], a[132:135], a[164:167], v[116:119]\n\t"
"buffer_load_dwordx4 a[116:119], off, s[92:95], s47 offset:1024 sc1\n\t"
"v_mfma_f32_16x16x32_bf16 v[100:103], a[120:123], a[148:151], v[100:103]\n\t"
"v_mfma_f32_16x16x32_bf16 v[100:103], a[124:127], a[164:167], v[100:103]\n\t"
"v_add_u32 v246, 8, v228\n\t"
"v_add_u32 v247, 16, v228\n\t"
"s_waitcnt lgkmcnt(0)\n\t"
"s_barrier\n\t"
"Lunit_%=:\n\t"
"v_mfma_f32_16x16x32_bf16 v[104:107], a[120:123], a[152:155], v[104:107]\n\t"
"s_nop 0\n\t"
"ds_read_b128 a[144:147], v224 offset:1024\n\t"
"ds_read_b128 a[148:151], v224 offset:1280\n\t"
"v_mfma_f32_16x16x32_bf16 v[104:107], a[124:127], a[168:171], v[104:107]\n\t"
"buffer_load_dwordx4 a[104:107], off, s[96:99], s46 offset:2048 sc1\n\t"
"v_mfma_f32_16x16x32_bf16 v[120:123], a[128:131], a[152:155], v[120:123]\n\t"
"ds_read_b128 a[160:163], v224 offset:3072\n\t"
"v_mfma_f32_16x16x32_bf16 v[120:123], a[132:135], a[168:171], v[120:123]\n\t"
"ds_read_b128 a[164:167], v224 offset:3328\n\t"
"v_mfma_f32_16x16x32_bf16 v[136:139], a[136:139], a[152:155], v[136:139]\n\t"
"ds_write_addtid_b32 v208 offset:28992\n\t"
"v_mfma_f32_16x16x32_bf16 v[136:139], a[140:143], a[168:171], v[136:139]\n\t"
"buffer_load_dwordx4 a[96:99], off, s[96:99], s46 offset:0 sc1\n\t"
"v_mfma_f32_16x16x32_bf16 v[140:143], a[136:139], a[156:159], v[140:143]\n\t"
"ds_read_b128 a[168:171], v224 offset:3584\n\t"
"v_mfma_f32_16x16x32_bf16 v[140:143], a[140:143], a[172:175], v[140:143]\n\t"
"ds_read_b128 a[152:155], v224 offset:1536\n\t"
"v_mfma_f32_16x16x32_bf16 v[124:127], a[128:131], a[156:159], v[124:127]\n\t"
"ds_write_addtid_b32 v203 offset:19616\n\t"
"v_mfma_f32_16x16x32_bf16 v[124:127], a[132:135], a[172:175], v[124:127]\n\t"
"buffer_load_dwordx4 a[100:103], off, s[96:99], s46 offset:1024 sc1\n\t"
"v_mfma_f32_16x16x32_bf16 v[108:111], a[120:123], a[156:159], v[108:111]\n\t"
"ds_read_b128 a[156:159], v224 offset:1792\n\t"
"v_mfma_f32_16x16x32_bf16 v[108:111], a[124:127], a[172:175], v[108:111]\n\t"
"s_waitcnt vmcnt(34) lgkmcnt(6)\n\t"
"ds_write_addtid_b32 v194 offset:16544\n\t"
"v_mfma_f32_16x16x32_bf16 v[48:51], a[0:3], a[144:147], v[0:3]\n\t"
"ds_read_b128 a[172:175], v224 offset:3840\n\t"
"v_mfma_f32_16x16x32_bf16 v[48:51], a[4:7], a[160:163], v[48:51]\n\t"
"s_waitcnt vmcnt(32) lgkmcnt(7)\n\t"
"buffer_load_dwordx4 a[136:139], off, s[92:95], s46 offset:2048 sc1\n\t"
"v_mfma_f32_16x16x32_bf16 v[64:67], a[8:11], a[144:147], v[16:19]\n\t"
"ds_write_addtid_b32 v195 offset:17568\n\t"
"v_mfma_f32_16x16x32_bf16 v[64:67], a[12:15], a[160:163], v[64:67]\n\t"
"s_waitcnt vmcnt(31) lgkmcnt(7)\n\t"
"ds_write_addtid_b32 v202 offset:18592\n\t"
"v_mfma_f32_16x16x32_bf16 v[80:83], a[16:19], a[144:147], v[32:35]\n\t"
"ds_read_b128 a[144:147], v224 offset:0\n\t"
"v_mfma_f32_16x16x32_bf16 v[80:83], a[20:23], a[160:163], v[80:83]\n\t"
"buffer_load_dwordx4 a[140:143], off, s[92:95], s46 offset:3072 sc1\n\t"
"v_mfma_f32_16x16x32_bf16 v[84:87], a[16:19], a[148:151], v[36:39]\n\t"
"ds_read_b128 a[160:163], v224 offset:2048\n\t"
"v_mfma_f32_16x16x32_bf16 v[84:87], a[20:23], a[164:167], v[84:87]\n\t"
"ds_write_addtid_b32 v223 offset:23744\n\t"
"v_mfma_f32_16x16x32_bf16 v[68:71], a[8:11], a[148:151], v[20:23]\n\t"
"ds_write_addtid_b32 v201 offset:27936\n\t"
"v_mfma_f32_16x16x32_bf16 v[68:71], a[12:15], a[164:167], v[68:71]\n\t"
"buffer_load_dwordx4 a[132:135], off, s[92:95], s46 offset:1024 sc1\n\t"
"v_mfma_f32_16x16x32_bf16 v[52:55], a[0:3], a[148:151], v[4:7]\n\t"
"ds_read_b128 a[148:151], v224 offset:256\n\t"
"v_mfma_f32_16x16x32_bf16 v[52:55], a[4:7], a[164:167], v[52:55]\n\t"
"s_waitcnt lgkmcnt(7)\n\t"
"ds_write_addtid_b32 v221 offset:32064\n\t"
"v_mfma_f32_16x16x32_bf16 v[56:59], a[0:3], a[152:155], v[8:11]\n\t"
"ds_read_b128 a[164:167], v224 offset:2304\n\t"
"v_mfma_f32_16x16x32_bf16 v[56:59], a[4:7], a[168:171], v[56:59]\n\t"
"buffer_load_dwordx4 a[128:131], off, s[92:95], s46 offset:0 sc1\n\t"
"v_mfma_f32_16x16x32_bf16 v[72:75], a[8:11], a[152:155], v[24:27]\n\t"
"ds_write_addtid_b32 v211 offset:21696\n\t"
"v_mfma_f32_16x16x32_bf16 v[72:75], a[12:15], a[168:171], v[72:75]\n\t"
"ds_write_addtid_b32 v193 offset:25888\n\t"
"v_mfma_f32_16x16x32_bf16 v[88:91], a[16:19], a[152:155], v[40:43]\n\t"
"v_mfma_f32_16x16x32_bf16 v[88:91], a[20:23], a[168:171], v[88:91]\n\t"
"ds_write_b128 v254, a[248:251] offset:41472\n\t"
"v_mfma_f32_16x16x32_bf16 v[92:95], a[16:19], a[156:159], v[44:47]\n\t"
"v_mfma_f32_16x16x32_bf16 v[92:95], a[20:23], a[172:175], v[92:95]\n\t"
"buffer_load_dwordx4 a[120:123], off, s[96:99], s42 offset:2048 sc1\n\t"
"v_mfma_f32_16x16x32_bf16 v[76:79], a[8:11], a[156:159], v[28:31]\n\t"
"v_mfma_f32_16x16x32_bf16 v[76:79], a[12:15], a[172:175], v[76:79]\n\t"
"s_nop 0\n\t"
"ds_read_b128 a[168:171], v224 offset:2560\n\t"
"ds_write_addtid_b32 v209 offset:30016\n\t"
"v_mfma_f32_16x16x32_bf16 v[60:63], a[0:3], a[156:159], v[12:15]\n\t"
"ds_write_addtid_b32 v210 offset:20672\n\t"
"v_mfma_f32_16x16x32_bf16 v[60:63], a[4:7], a[172:175], v[60:63]\n\t"
"s_waitcnt vmcnt(33) lgkmcnt(6)\n\t"
"buffer_load_dwordx4 a[124:127], off, s[96:99], s42 offset:3072 sc1\n\t"
"v_mfma_f32_16x16x32_bf16 v[0:3], a[24:27], a[144:147], v[0:3]\n\t"
"ds_write_addtid_b32 v222 offset:22720\n\t"
"v_mfma_f32_16x16x32_bf16 v[0:3], a[28:31], a[160:163], v[0:3]\n\t"
"s_waitcnt vmcnt(32) lgkmcnt(5)\n\t"
"ds_write_addtid_b32 v192 offset:24864\n\t"
"ds_write_addtid_b32 v200 offset:26912\n\t"
"v_mfma_f32_16x16x32_bf16 v[16:19], a[32:35], a[144:147], v[16:19]\n\t"
"ds_write_addtid_b32 v220 offset:31040\n\t"
"v_mfma_f32_16x16x32_bf16 v[16:19], a[36:39], a[160:163], v[16:19]\n\t"
"s_waitcnt vmcnt(30) lgkmcnt(7)\n\t"
"buffer_load_dwordx4 a[16:19], off, s[92:95], s50 offset:0 sc1\n\t"
"v_mfma_f32_16x16x32_bf16 v[32:35], a[40:43], a[144:147], v[32:35]\n\t"
"ds_read_b128 a[156:159], v224 offset:768\n\t"
"v_mfma_f32_16x16x32_bf16 v[32:35], a[44:47], a[160:163], v[32:35]\n\t"
"ds_read_b128 a[172:175], v224 offset:2816\n\t"
"v_mfma_f32_16x16x32_bf16 v[36:39], a[40:43], a[148:151], v[36:39]\n\t"
"ds_read_b128 a[152:155], v224 offset:512\n\t"
"v_mfma_f32_16x16x32_bf16 v[36:39], a[44:47], a[164:167], v[36:39]\n\t"
"buffer_load_dwordx4 a[20:23], off, s[92:95], s50 offset:1024 sc1\n\t"
"v_mfma_f32_16x16x32_bf16 v[20:23], a[32:35], a[148:151], v[20:23]\n\t"
"v_mfma_f32_16x16x32_bf16 v[20:23], a[36:39], a[164:167], v[20:23]\n\t"
"v_mfma_f32_16x16x32_bf16 v[4:7], a[24:27], a[148:151], v[4:7]\n\t"
"buffer_load_dwordx4 a[12:15], off, s[96:99], s49 offset:3072 sc1\n\t"
"v_mfma_f32_16x16x32_bf16 v[4:7], a[28:31], a[164:167], v[4:7]\n\t"
"s_waitcnt vmcnt(27) lgkmcnt(4)\n\t"
"s_waitcnt lgkmcnt(0)\n\t"
"s_barrier\n\t"
"v_mfma_f32_16x16x32_bf16 v[8:11], a[24:27], a[152:155], v[8:11]\n\t"
"s_add_u32 m0, s26, 76800\n\t"
"ds_read_b128 a[144:147], v224 offset:17568\n\t"
"ds_read_b128 a[160:163], v224 offset:19616\n\t"
"v_mfma_f32_16x16x32_bf16 v[8:11], a[28:31], a[168:171], v[8:11]\n\t"
"ds_read_b128 a[164:167], v224 offset:19872\n\t"
"v_mfma_f32_16x16x32_bf16 v[24:27], a[32:35], a[152:155], v[24:27]\n\t"
"ds_read_b128 a[148:151], v224 offset:17824\n\t"
"v_mfma_f32_16x16x32_bf16 v[24:27], a[36:39], a[168:171], v[24:27]\n\t"
"buffer_load_dwordx4 v236, s[12:15], s22 offen offset:256 nt lds\n\t"
"v_mfma_f32_16x16x32_bf16 v[40:43], a[40:43], a[152:155], v[40:43]\n\t"
"s_add_u32 s24, s24, 3145728\n\t"
"ds_read_b64_tr_b16 a[176:177], v248 offset:0\n\t"
"ds_read_b64_tr_b16 a[178:179], v249 offset:0\n\t"
"v_mfma_f32_16x16x32_bf16 v[40:43], a[44:47], a[168:171], v[40:43]\n\t"
"s_add_u32 s20, s20, 3145728\n\t"
"ds_read_b64_tr_b16 a[180:181], v248 offset:512\n\t"
"ds_read_b64_tr_b16 a[182:183], v249 offset:512\n\t"
"v_mfma_f32_16x16x32_bf16 v[44:47], a[40:43], a[156:159], v[44:47]\n\t"
"ds_read_b64_tr_b16 a[184:185], v250 offset:2304\n\t"
"v_mfma_f32_16x16x32_bf16 v[44:47], a[44:47], a[172:175], v[44:47]\n\t"
"buffer_load_dwordx4 v237, s[12:15], s22 offen offset:1280 nt lds\n\t"
"v_mfma_f32_16x16x32_bf16 v[28:31], a[32:35], a[156:159], v[28:31]\n\t"
"ds_read_b128 a[152:155], v224 offset:18080\n\t"
"v_mfma_f32_16x16x32_bf16 v[28:31], a[36:39], a[172:175], v[28:31]\n\t"
"s_waitcnt lgkmcnt(2)\n\t"
"ds_read_b64_tr_b16 a[186:187], v251 offset:2304\n\t"
"v_mfma_f32_4x4x4_16b_bf16 v[192:195], a[210:211], a[176:177], 0\n\t"
"ds_read_b128 a[168:171], v224 offset:20128\n\t"
"v_mfma_f32_4x4x4_16b_bf16 v[196:199], a[210:211], a[178:179], 0\n\t"
"ds_read_b64_tr_b16 a[188:189], v250 offset:2816\n\t"
"v_mfma_f32_4x4x4_16b_bf16 v[200:203], a[210:211], a[180:181], 0\n\t"
"ds_read_b64_tr_b16 a[190:191], v251 offset:2816\n\t"
"v_mfma_f32_4x4x4_16b_bf16 v[204:207], a[210:211], a[182:183], 0\n\t"
"buffer_load_dwordx4 v238, s[12:15], s22 offen offset:2304 nt lds\n\t"
"v_mfma_f32_16x16x32_bf16 v[12:15], a[24:27], a[156:159], v[12:15]\n\t"
"s_add_u32 s27, s27, 3145728\n\t"
"ds_read_b64_tr_b16 a[192:193], v250 offset:0\n\t"
"ds_read_b64_tr_b16 a[194:195], v251 offset:0\n\t"
"v_mfma_f32_16x16x32_bf16 v[12:15], a[28:31], a[172:175], v[12:15]\n\t"
"s_waitcnt vmcnt(28)\n\t"
"buffer_load_dwordx4 a[8:11], off, s[96:99], s49 offset:2048 sc1\n\t"
"v_mfma_f32_16x16x32_bf16 v[144:147], a[48:51], a[144:147], v[96:99]\n\t"
"ds_read_b128 a[156:159], v224 offset:18336\n\t"
"v_cvt_pk_bf16_f32 v194, v194, v198\n\t"
"v_mfma_f32_16x16x32_bf16 v[144:147], a[52:55], a[160:163], v[144:147]\n\t"
"s_waitcnt vmcnt(27) lgkmcnt(8)\n\t"
"v_cvt_pk_bf16_f32 v197, v193, v197\n\t"
"v_cvt_pk_bf16_f32 v195, v195, v199\n\t"
"v_mfma_f32_16x16x32_bf16 v[160:163], a[56:59], a[144:147], v[112:115]\n\t"
"ds_read_b128 a[172:175], v224 offset:20384\n\t"
"v_cvt_pk_bf16_f32 v196, v192, v196\n\t"
"v_mfma_f32_16x16x32_bf16 v[160:163], a[60:63], a[160:163], v[160:163]\n\t"
"s_waitcnt vmcnt(25) lgkmcnt(7)\n\t"
"buffer_load_dwordx4 v239, s[12:15], s22 offen offset:3328 nt lds\n\t"
"v_mfma_f32_16x16x32_bf16 v[176:179], a[64:67], a[144:147], v[128:131]\n\t"
"ds_read_b128 a[144:147], v224 offset:16544\n\t"
"v_cvt_pk_bf16_f32 v199, v201, v205\n\t"
"v_mfma_f32_16x16x32_bf16 v[176:179], a[68:71], a[160:163], v[176:179]\n\t"
"s_waitcnt lgkmcnt(5)\n\t"
"v_cvt_pk_bf16_f32 v192, v202, v206\n\t"
"v_cvt_pk_bf16_f32 v198, v200, v204\n\t"
"v_mfma_f32_4x4x4_16b_bf16 v[208:211], a[210:211], a[184:185], 0\n\t"
"ds_read_b64_tr_b16 a[196:197], v250 offset:512\n\t"
"v_mfma_f32_4x4x4_16b_bf16 v[212:215], a[210:211], a[186:187], 0\n\t"
"ds_read_b64_tr_b16 a[198:199], v251 offset:512\n\t"
"v_mfma_f32_4x4x4_16b_bf16 v[216:219], a[210:211], a[188:189], 0\n\t"
"ds_read_b64_tr_b16 a[200:201], v248 offset:2304\n\t"
"v_mfma_f32_4x4x4_16b_bf16 v[220:223], a[210:211], a[190:191], 0\n\t"
"s_mov_b64 exec, s[36:37]\n\t"
"buffer_load_dwordx4 v240, s[12:15], s22 offen offset:3840 nt lds\n\t"
"s_mov_b64 exec, -1\n\t"
"v_mfma_f32_16x16x32_bf16 v[180:183], a[64:67], a[148:151], v[132:135]\n\t"
"s_lshl_b32 m0, s73, 8\n\t"
"ds_read_b128 a[160:163], v224 offset:18592\n\t"
"v_cvt_pk_bf16_f32 v193, v203, v207\n\t"
"v_mfma_f32_16x16x32_bf16 v[180:183], a[68:71], a[164:167], v[180:183]\n\t"
"s_nop 0\n\t"
"ds_write_addtid_b32 v198 offset:8320\n\t"
"ds_write_addtid_b32 v197 offset:1024\n\t"
"v_mfma_f32_16x16x32_bf16 v[164:167], a[56:59], a[148:151], v[116:119]\n\t"
"v_cvt_pk_bf16_f32 v213, v209, v213\n\t"
"v_cvt_pk_bf16_f32 v211, v211, v215\n\t"
"v_mfma_f32_16x16x32_bf16 v[164:167], a[60:63], a[164:167], v[164:167]\n\t"
"v_cvt_pk_bf16_f32 v212, v208, v212\n\t"
"v_cvt_pk_bf16_f32 v210, v210, v214\n\t"
"v_mfma_f32_16x16x32_bf16 v[148:151], a[48:51], a[148:151], v[100:103]\n\t"
"ds_write_addtid_b32 v199 offset:9344\n\t"
"v_cvt_pk_bf16_f32 v209, v219, v223\n\t"
"v_mfma_f32_16x16x32_bf16 v[148:151], a[52:55], a[164:167], v[148:151]\n\t"
"s_waitcnt lgkmcnt(5)\n\t"
"ds_read_b128 a[148:151], v224 offset:16800\n\t"
"v_cvt_pk_bf16_f32 v214, v216, v220\n\t"
"v_mfma_f32_4x4x4_16b_bf16 v[200:203], a[210:211], a[192:193], 0\n\t"
"v_cvt_pk_bf16_f32 v208, v218, v222\n\t"
"v_mfma_f32_4x4x4_16b_bf16 v[204:207], a[210:211], a[194:195], 0\n\t"
"ds_write_addtid_b32 v196 offset:0\n\t"
"v_mfma_f32_4x4x4_16b_bf16 v[196:199], a[210:211], a[196:197], 0\n\t"
"v_cvt_pk_bf16_f32 v215, v217, v221\n\t"
"v_mfma_f32_4x4x4_16b_bf16 v[216:219], a[210:211], a[198:199], 0\n\t"
"ds_read_b128 a[164:167], v224 offset:18848\n\t"
"v_mfma_f32_16x16x32_bf16 v[152:155], a[48:51], a[152:155], v[104:107]\n\t"
"ds_read_b64_tr_b16 a[202:203], v249 offset:2304\n\t"
"v_mfma_f32_16x16x32_bf16 v[152:155], a[52:55], a[168:171], v[152:155]\n\t"
"ds_read_b64_tr_b16 a[204:205], v248 offset:2816\n\t"
"v_mfma_f32_16x16x32_bf16 v[168:171], a[56:59], a[152:155], v[120:123]\n\t"
"v_cvt_pk_bf16_f32 v202, v202, v206\n\t"
"v_cvt_pk_bf16_f32 v203, v203, v207\n\t"
"v_mfma_f32_16x16x32_bf16 v[168:171], a[60:63], a[168:171], v[168:171]\n\t"
"ds_read_b64_tr_b16 a[206:207], v249 offset:2816\n\t"
"v_cvt_pk_bf16_f32 v205, v201, v205\n\t"
"v_mfma_f32_16x16x32_bf16 v[184:187], a[64:67], a[152:155], v[136:139]\n\t"
"ds_read_b128 a[152:155], v224 offset:17056\n\t"
"v_cvt_pk_bf16_f32 v204, v200, v204\n\t"
"v_mfma_f32_16x16x32_bf16 v[184:187], a[68:71], a[168:171], v[184:187]\n\t"
"ds_write_addtid_b32 v213 offset:5152\n\t"
"v_cvt_pk_bf16_f32 v207, v197, v217\n\t"
"v_mfma_f32_16x16x32_bf16 v[188:191], a[64:67], a[156:159], v[140:143]\n\t"
"ds_write_addtid_b32 v215 offset:13472\n\t"
"v_cvt_pk_bf16_f32 v201, v199, v219\n\t"
"v_mfma_f32_16x16x32_bf16 v[188:191], a[68:71], a[172:175], v[188:191]\n\t"
"s_waitcnt lgkmcnt(3)\n\t"
"ds_write_addtid_b32 v212 offset:4128\n\t"
"v_cvt_pk_bf16_f32 v200, v198, v218\n\t"
"v_mfma_f32_4x4x4_16b_bf16 v[220:223], a[210:211], a[200:201], 0\n\t"
"ds_write_addtid_b32 v214 offset:12448\n\t"
"v_mfma_f32_4x4x4_16b_bf16 v[212:215], a[210:211], a[202:203], 0\n\t"
"v_cvt_pk_bf16_f32 v206, v196, v216\n\t"
"v_mfma_f32_4x4x4_16b_bf16 v[196:199], a[210:211], a[204:205], 0\n\t"
"ds_read_b128 a[168:171], v224 offset:19104\n\t"
"v_mfma_f32_4x4x4_16b_bf16 v[216:219], a[210:211], a[206:207], 0\n\t"
"ds_write_addtid_b32 v206 offset:10368\n\t"
"v_mfma_f32_16x16x32_bf16 v[172:175], a[56:59], a[156:159], v[124:127]\n\t"
"s_nop 0\n\t"
"ds_write_addtid_b32 v205 offset:3072\n\t"
"ds_write_addtid_b32 v207 offset:11392\n\t"
"v_mfma_f32_16x16x32_bf16 v[172:175], a[60:63], a[172:175], v[172:175]\n\t"
"ds_write_addtid_b32 v204 offset:2048\n\t"
"v_mfma_f32_16x16x32_bf16 v[156:159], a[48:51], a[156:159], v[108:111]\n\t"
"v_cvt_pk_bf16_f32 v212, v220, v212\n\t"
"v_cvt_pk_bf16_f32 v222, v222, v214\n\t"
"v_mfma_f32_16x16x32_bf16 v[156:159], a[52:55], a[172:175], v[156:159]\n\t"
"s_waitcnt vmcnt(22) lgkmcnt(8)\n\t"
"v_cvt_pk_bf16_f32 v213, v221, v213\n\t"
"v_cvt_pk_bf16_f32 v223, v223, v215\n\t"
"v_mfma_f32_16x16x32_bf16 v[96:99], a[72:75], a[144:147], v[96:99]\n\t"
"ds_write_addtid_b32 v212 offset:6176\n\t"
"v_cvt_pk_bf16_f32 v214, v196, v216\n\t"
"v_mfma_f32_16x16x32_bf16 v[96:99], a[76:79], a[160:163], v[96:99]\n\t"
"ds_write_addtid_b32 v213 offset:7200\n\t"
"v_cvt_pk_bf16_f32 v215, v197, v217\n\t"
"v_mfma_f32_16x16x32_bf16 v[112:115], a[80:83], a[144:147], v[112:115]\n\t"
"s_add_u32 s25, s25, 3145728\n\t"
"ds_write_addtid_b32 v214 offset:14496\n\t"
"ds_write_addtid_b32 v215 offset:15520\n\t"
"v_mfma_f32_16x16x32_bf16 v[112:115], a[84:87], a[160:163], v[112:115]\n\t"
"s_waitcnt vmcnt(21) lgkmcnt(9)\n\t"
"v_mfma_f32_16x16x32_bf16 v[128:131], a[88:91], a[144:147], v[128:131]\n\t"
"v_mfma_f32_16x16x32_bf16 v[128:131], a[92:95], a[160:163], v[128:131]\n\t"
"ds_read_b128 a[156:159], v224 offset:17312\n\t"
"v_mfma_f32_16x16x32_bf16 v[132:135], a[88:91], a[148:151], v[132:135]\n\t"
"ds_read_b128 a[172:175], v224 offset:19360\n\t"
"v_cvt_pk_bf16_f32 v221, v199, v219\n\t"
"v_mfma_f32_16x16x32_bf16 v[132:135], a[92:95], a[164:167], v[132:135]\n\t"
"v_mfma_f32_16x16x32_bf16 v[116:119], a[80:83], a[148:151], v[116:119]\n\t"
"v_cvt_pk_bf16_f32 v220, v198, v218\n\t"
"v_mfma_f32_16x16x32_bf16 v[116:119], a[84:87], a[164:167], v[116:119]\n\t"
"buffer_load_dwordx4 a[0:3], off, s[96:99], s49 offset:0 sc1\n\t"
"v_mfma_f32_16x16x32_bf16 v[100:103], a[72:75], a[148:151], v[100:103]\n\t"
"s_waitcnt lgkmcnt(0)\n\t"
"v_mfma_f32_16x16x32_bf16 v[100:103], a[76:79], a[164:167], v[100:103]\n\t"
"s_barrier\n\t"
"v_mfma_f32_16x16x32_bf16 v[104:107], a[72:75], a[152:155], v[104:107]\n\t"
"s_nop 0\n\t"
"ds_read_b128 a[144:147], v224 offset:1024\n\t"
"ds_read_b128 a[160:163], v224 offset:3072\n\t"
"v_mfma_f32_16x16x32_bf16 v[104:107], a[76:79], a[168:171], v[104:107]\n\t"
"buffer_load_dwordx4 a[4:7], off, s[96:99], s49 offset:1024 sc1\n\t"
"v_mfma_f32_16x16x32_bf16 v[120:123], a[80:83], a[152:155], v[120:123]\n\t"
"ds_read_b128 a[148:151], v224 offset:1280\n\t"
"v_mfma_f32_16x16x32_bf16 v[120:123], a[84:87], a[168:171], v[120:123]\n\t"
"ds_read_b128 a[164:167], v224 offset:3328\n\t"
"v_mfma_f32_16x16x32_bf16 v[136:139], a[88:91], a[152:155], v[136:139]\n\t"
"ds_read_b128 a[248:251], v255 offset:59904\n\t"
"v_mfma_f32_16x16x32_bf16 v[136:139], a[92:95], a[168:171], v[136:139]\n\t"
"buffer_load_dwordx4 a[40:43], off, s[92:95], s49 offset:2048 sc1\n\t"
"v_mfma_f32_16x16x32_bf16 v[140:143], a[88:91], a[156:159], v[140:143]\n\t"
"v_mfma_f32_16x16x32_bf16 v[140:143], a[92:95], a[172:175], v[140:143]\n\t"
"ds_read_b128 a[152:155], v224 offset:1536\n\t"
"v_mfma_f32_16x16x32_bf16 v[124:127], a[80:83], a[156:159], v[124:127]\n\t"
"ds_read_b128 a[168:171], v224 offset:3584\n\t"
"v_mfma_f32_16x16x32_bf16 v[124:127], a[84:87], a[172:175], v[124:127]\n\t"
"buffer_load_dwordx4 a[44:47], off, s[92:95], s49 offset:3072 sc1\n\t"
"v_mfma_f32_16x16x32_bf16 v[108:111], a[72:75], a[156:159], v[108:111]\n\t"
"ds_write_addtid_b32 v223 offset:23744\n\t"
"v_mfma_f32_16x16x32_bf16 v[108:111], a[76:79], a[172:175], v[108:111]\n\t"
"s_waitcnt vmcnt(19) lgkmcnt(6)\n\t"
"ds_read_b128 a[156:159], v224 offset:1792\n\t"
"v_mfma_f32_16x16x32_bf16 v[48:51], a[96:99], a[144:147], v[48:51]\n\t"
"ds_read_b128 a[172:175], v224 offset:3840\n\t"
"v_mfma_f32_16x16x32_bf16 v[48:51], a[100:103], a[160:163], v[48:51]\n\t"
"buffer_load_dwordx4 a[36:39], off, s[92:95], s49 offset:1024 sc1\n\t"
"v_mfma_f32_16x16x32_bf16 v[64:67], a[104:107], a[144:147], v[64:67]\n\t"
"ds_write_addtid_b32 v192 offset:24864\n\t"
"v_mfma_f32_16x16x32_bf16 v[64:67], a[108:111], a[160:163], v[64:67]\n\t"
"ds_write_addtid_b32 v200 offset:26912\n\t"
"v_mfma_f32_16x16x32_bf16 v[80:83], a[112:115], a[144:147], v[80:83]\n\t"
"ds_read_b128 a[144:147], v224 offset:0\n\t"
"v_mfma_f32_16x16x32_bf16 v[80:83], a[116:119], a[160:163], v[80:83]\n\t"
"s_waitcnt lgkmcnt(7)\n\t"
"buffer_load_dwordx4 a[32:35], off, s[92:95], s49 offset:0 sc1\n\t"
"v_mfma_f32_16x16x32_bf16 v[84:87], a[112:115], a[148:151], v[84:87]\n\t"
"ds_read_b128 a[160:163], v224 offset:2048\n\t"
"v_mfma_f32_16x16x32_bf16 v[84:87], a[116:119], a[164:167], v[84:87]\n\t"
"ds_write_addtid_b32 v221 offset:32064\n\t"
"v_mfma_f32_16x16x32_bf16 v[68:71], a[104:107], a[148:151], v[68:71]\n\t"
"ds_write_addtid_b32 v209 offset:30016\n\t"
"v_xor_b32 v226, 64, v226\n\t"
"v_mfma_f32_16x16x32_bf16 v[68:71], a[108:111], a[164:167], v[68:71]\n\t"
"buffer_load_dwordx4 a[24:27], off, s[96:99], s48 offset:2048 sc1\n\t"
"v_mfma_f32_16x16x32_bf16 v[52:55], a[96:99], a[148:151], v[52:55]\n\t"
"ds_read_b128 a[148:151], v224 offset:256\n\t"
"v_mfma_f32_16x16x32_bf16 v[52:55], a[100:103], a[164:167], v[52:55]\n\t"
"s_waitcnt lgkmcnt(7)\n\t"
"ds_write_addtid_b32 v193 offset:25888\n\t"
"v_mfma_f32_16x16x32_bf16 v[56:59], a[96:99], a[152:155], v[56:59]\n\t"
"ds_read_b128 a[164:167], v224 offset:2304\n\t"
"v_mfma_f32_16x16x32_bf16 v[56:59], a[100:103], a[168:171], v[56:59]\n\t"
"buffer_load_dwordx4 a[28:31], off, s[96:99], s48 offset:3072 sc1\n\t"
"v_mfma_f32_16x16x32_bf16 v[72:75], a[104:107], a[152:155], v[72:75]\n\t"
"s_nop 0\n\t"
"ds_write_addtid_b32 v210 offset:20672\n\t"
"v_xor_b32 v227, 64, v227\n\t"
"v_mfma_f32_16x16x32_bf16 v[72:75], a[108:111], a[168:171], v[72:75]\n\t"
"s_nop 0\n\t"
"ds_write_addtid_b32 v195 offset:17568\n\t"
"v_mfma_f32_16x16x32_bf16 v[88:91], a[112:115], a[152:155], v[88:91]\n\t"
"s_nop 0\n\t"
"ds_read_b128 a[152:155], v224 offset:512\n\t"
"v_mfma_f32_16x16x32_bf16 v[88:91], a[116:119], a[168:171], v[88:91]\n\t"
"buffer_load_dwordx4 a[64:67], off, s[92:95], s77 offset:0 sc1\n\t"
"v_mfma_f32_16x16x32_bf16 v[92:95], a[112:115], a[156:159], v[92:95]\n\t"
"s_nop 0\n\t"
"ds_read_b128 a[168:171], v224 offset:2560\n\t"
"v_mfma_f32_16x16x32_bf16 v[92:95], a[116:119], a[172:175], v[92:95]\n\t"
"s_nop 0\n\t"
"ds_write_addtid_b32 v202 offset:18592\n\t"
"v_mfma_f32_16x16x32_bf16 v[76:79], a[104:107], a[156:159], v[76:79]\n\t"
"s_nop 0\n\t"
"ds_write_addtid_b32 v208 offset:28992\n\t"
"v_mfma_f32_16x16x32_bf16 v[76:79], a[108:111], a[172:175], v[76:79]\n\t"
"buffer_load_dwordx4 a[68:71], off, s[92:95], s77 offset:1024 sc1\n\t"
"v_mfma_f32_16x16x32_bf16 v[60:63], a[96:99], a[156:159], v[60:63]\n\t"
"s_nop 0\n\t"
"ds_write_addtid_b32 v211 offset:21696\n\t"
"v_mfma_f32_16x16x32_bf16 v[60:63], a[100:103], a[172:175], v[60:63]\n\t"
"s_waitcnt vmcnt(19) lgkmcnt(7)\n\t"
"ds_write_addtid_b32 v222 offset:22720\n\t"
"v_mfma_f32_16x16x32_bf16 v[0:3], a[120:123], a[144:147], v[0:3]\n\t"
"s_nop 0\n\t"
"ds_write_addtid_b32 v194 offset:16544\n\t"
"v_mfma_f32_16x16x32_bf16 v[0:3], a[124:127], a[160:163], v[0:3]\n\t"
"s_nop 0\n\t"
"ds_write_addtid_b32 v220 offset:31040\n\t"
"v_mfma_f32_16x16x32_bf16 v[16:19], a[128:131], a[144:147], v[16:19]\n\t"
"s_add_u32 s22, s22, 3145728\n\t"
"ds_write_addtid_b32 v203 offset:19616\n\t"
"ds_write_addtid_b32 v201 offset:27936\n\t"
"v_mfma_f32_16x16x32_bf16 v[16:19], a[132:135], a[160:163], v[16:19]\n\t"
"buffer_load_dwordx4 a[60:63], off, s[96:99], s76 offset:3072 sc1\n\t"
"v_mfma_f32_16x16x32_bf16 v[32:35], a[136:139], a[144:147], v[32:35]\n\t"
"s_nop 0\n\t"
"ds_read_b128 a[172:175], v224 offset:2816\n\t"
"v_mfma_f32_16x16x32_bf16 v[32:35], a[140:143], a[160:163], v[32:35]\n\t"
"buffer_load_dwordx4 a[56:59], off, s[96:99], s76 offset:2048 sc1\n\t"
"v_mfma_f32_16x16x32_bf16 v[36:39], a[136:139], a[148:151], v[36:39]\n\t"
"s_nop 0\n\t"
"ds_read_b128 a[156:159], v224 offset:768\n\t"
"v_mfma_f32_16x16x32_bf16 v[36:39], a[140:143], a[164:167], v[36:39]\n\t"
"s_nop 0\n\t"
"v_mfma_f32_16x16x32_bf16 v[20:23], a[128:131], a[148:151], v[20:23]\n\t"
"s_cmp_eq_u32 s21, 1\n\t"
"s_cselect_b32 s22, 2.0, s22\n\t"
"v_mfma_f32_16x16x32_bf16 v[20:23], a[132:135], a[164:167], v[20:23]\n\t"
"buffer_load_dwordx4 a[48:51], off, s[96:99], s76 offset:0 sc1\n\t"
"v_mfma_f32_16x16x32_bf16 v[4:7], a[120:123], a[148:151], v[4:7]\n\t"
"s_nop 0\n\t"
"v_mfma_f32_16x16x32_bf16 v[4:7], a[124:127], a[164:167], v[4:7]\n\t"
"s_waitcnt vmcnt(13) lgkmcnt(4)\n\t"
"s_waitcnt lgkmcnt(0)\n\t"
"s_barrier\n\t"
"v_mfma_f32_16x16x32_bf16 v[8:11], a[120:123], a[152:155], v[8:11]\n\t"
"s_nop 0\n\t"
"ds_read_b128 a[144:147], v224 offset:17568\n\t"
"ds_read_b128 a[160:163], v224 offset:19616\n\t"
"v_mfma_f32_16x16x32_bf16 v[8:11], a[124:127], a[168:171], v[8:11]\n\t"
"buffer_load_dwordx4 a[52:55], off, s[96:99], s76 offset:1024 sc1\n\t"
"v_mfma_f32_16x16x32_bf16 v[24:27], a[128:131], a[152:155], v[24:27]\n\t"
"s_nop 0\n\t"
"ds_read_b128 a[148:151], v224 offset:17824\n\t"
"ds_read_b128 a[164:167], v224 offset:19872\n\t"
"v_mfma_f32_16x16x32_bf16 v[24:27], a[132:135], a[168:171], v[24:27]\n\t"
"buffer_load_dwordx4 a[88:91], off, s[92:95], s76 offset:2048 sc1\n\t"
"v_mfma_f32_16x16x32_bf16 v[40:43], a[136:139], a[152:155], v[40:43]\n\t"
"s_nop 0\n\t"
"ds_read_b64_tr_b16 a[176:177], v248 offset:41472\n\t"
"ds_read_b64_tr_b16 a[178:179], v249 offset:41472\n\t"
"v_mfma_f32_16x16x32_bf16 v[40:43], a[140:143], a[168:171], v[40:43]\n\t"
"s_nop 0\n\t"
"ds_read_b64_tr_b16 a[180:181], v248 offset:41984\n\t"
"ds_read_b64_tr_b16 a[182:183], v249 offset:41984\n\t"
"v_mfma_f32_16x16x32_bf16 v[44:47], a[136:139], a[156:159], v[44:47]\n\t"
"s_nop 0\n\t"
"ds_read_b64_tr_b16 a[184:185], v250 offset:43776\n\t"
"ds_read_b64_tr_b16 a[186:187], v251 offset:43776\n\t"
"v_mfma_f32_16x16x32_bf16 v[44:47], a[140:143], a[172:175], v[44:47]\n\t"
"buffer_load_dwordx4 a[92:95], off, s[92:95], s76 offset:3072 sc1\n\t"
"v_mfma_f32_16x16x32_bf16 v[28:31], a[128:131], a[156:159], v[28:31]\n\t"
"s_nop 0\n\t"
"ds_read_b64_tr_b16 a[188:189], v250 offset:44288\n\t"
"ds_read_b64_tr_b16 a[190:191], v251 offset:44288\n\t"
"v_mfma_f32_16x16x32_bf16 v[28:31], a[132:135], a[172:175], v[28:31]\n\t"
"s_waitcnt lgkmcnt(4)\n\t"
"buffer_load_dwordx4 a[84:87], off, s[92:95], s76 offset:1024 sc1\n\t"
"v_mfma_f32_4x4x4_16b_bf16 v[192:195], a[210:211], a[176:177], 0\n\t"
"ds_read_b128 a[152:155], v224 offset:18080\n\t"
"v_mfma_f32_4x4x4_16b_bf16 v[196:199], a[210:211], a[178:179], 0\n\t"
"ds_read_b128 a[168:171], v224 offset:20128\n\t"
"v_mfma_f32_4x4x4_16b_bf16 v[200:203], a[210:211], a[180:181], 0\n\t"
"ds_read_b64_tr_b16 a[192:193], v250 offset:41472\n\t"
"v_mfma_f32_4x4x4_16b_bf16 v[204:207], a[210:211], a[182:183], 0\n\t"
"buffer_load_dwordx4 a[80:83], off, s[92:95], s76 offset:0 sc1\n\t"
"v_mfma_f32_16x16x32_bf16 v[12:15], a[120:123], a[156:159], v[12:15]\n\t"
"s_nop 0\n\t"
"ds_read_b64_tr_b16 a[194:195], v251 offset:41472\n\t"
"ds_read_b64_tr_b16 a[196:197], v250 offset:41984\n\t"
"v_mfma_f32_16x16x32_bf16 v[12:15], a[124:127], a[172:175], v[12:15]\n\t"
"s_waitcnt vmcnt(16)\n\t"
"ds_read_b64_tr_b16 a[198:199], v251 offset:41984\n\t"
"ds_read_b64_tr_b16 a[200:201], v248 offset:43776\n\t"
"v_mfma_f32_16x16x32_bf16 v[144:147], a[0:3], a[144:147], v[144:147]\n\t"
"s_nop 0\n\t"
"v_cvt_pk_bf16_f32 v196, v192, v196\n\t"
"v_cvt_pk_bf16_f32 v194, v194, v198\n\t"
"v_mfma_f32_16x16x32_bf16 v[144:147], a[4:7], a[160:163], v[144:147]\n\t"
"buffer_load_dwordx4 a[72:75], off, s[96:99], s75 offset:2048 sc1\n\t"
"v_mfma_f32_16x16x32_bf16 v[160:163], a[8:11], a[144:147], v[160:163]\n\t"
"s_nop 0\n\t"
"ds_read_b128 a[156:159], v224 offset:18336\n\t"
"v_cvt_pk_bf16_f32 v197, v193, v197\n\t"
"v_mfma_f32_16x16x32_bf16 v[160:163], a[12:15], a[160:163], v[160:163]\n\t"
"s_nop 0\n\t"
"ds_read_b128 a[172:175], v224 offset:20384\n\t"
"v_cvt_pk_bf16_f32 v195, v195, v199\n\t"
"v_mfma_f32_16x16x32_bf16 v[176:179], a[16:19], a[144:147], v[176:179]\n\t"
"s_nop 0\n\t"
"v_cvt_pk_bf16_f32 v198, v200, v204\n\t"
"v_cvt_pk_bf16_f32 v199, v201, v205\n\t"
"v_mfma_f32_16x16x32_bf16 v[176:179], a[20:23], a[160:163], v[176:179]\n\t"
"s_waitcnt lgkmcnt(7)\n\t"
"buffer_load_dwordx4 a[76:79], off, s[96:99], s75 offset:3072 sc1\n\t"
"v_mfma_f32_4x4x4_16b_bf16 v[208:211], a[210:211], a[184:185], 0\n\t"
"ds_read_b128 a[144:147], v224 offset:16544\n\t"
"v_mfma_f32_4x4x4_16b_bf16 v[212:215], a[210:211], a[186:187], 0\n\t"
"ds_write_addtid_b32 v198 offset:8320\n\t"
"v_mfma_f32_4x4x4_16b_bf16 v[216:219], a[210:211], a[188:189], 0\n\t"
"ds_write_addtid_b32 v197 offset:1024\n\t"
"v_mfma_f32_4x4x4_16b_bf16 v[220:223], a[210:211], a[190:191], 0\n\t"
"ds_write_addtid_b32 v196 offset:0\n\t"
"v_cvt_pk_bf16_f32 v193, v203, v207\n\t"
"v_mfma_f32_16x16x32_bf16 v[180:183], a[16:19], a[148:151], v[180:183]\n\t"
"s_nop 0\n\t"
"ds_read_b128 a[160:163], v224 offset:18592\n\t"
"v_cvt_pk_bf16_f32 v192, v202, v206\n\t"
"v_mfma_f32_16x16x32_bf16 v[180:183], a[20:23], a[164:167], v[180:183]\n\t"
"Lpad3_%=:\n\t"
"s_cbranch_execz Lpad7_%=\n\t"
"buffer_load_dwordx4 a[112:115], off, s[92:95], s80 offset:0 sc1\n\t"
"v_mfma_f32_16x16x32_bf16 v[164:167], a[8:11], a[148:151], v[164:167]\n\t"
"ds_write_addtid_b32 v199 offset:9344\n\t"
"v_cvt_pk_bf16_f32 v210, v210, v214\n\t"
"v_mfma_f32_16x16x32_bf16 v[164:167], a[12:15], a[164:167], v[164:167]\n\t"
"v_cvt_pk_bf16_f32 v212, v208, v212\n\t"
"v_cvt_pk_bf16_f32 v213, v209, v213\n\t"
"v_mfma_f32_16x16x32_bf16 v[148:151], a[0:3], a[148:151], v[148:151]\n\t"
"ds_read_b128 a[148:151], v224 offset:16800\n\t"
"v_cvt_pk_bf16_f32 v211, v211, v215\n\t"
"v_mfma_f32_16x16x32_bf16 v[148:151], a[4:7], a[164:167], v[148:151]\n\t"
"s_waitcnt lgkmcnt(7)\n\t"
"v_cvt_pk_bf16_f32 v214, v216, v220\n\t"
"v_cvt_pk_bf16_f32 v209, v219, v223\n\t"
"v_cvt_pk_bf16_f32 v215, v217, v221\n\t"
"v_mfma_f32_4x4x4_16b_bf16 v[200:203], a[210:211], a[192:193], 0\n\t"
"ds_read_b128 a[164:167], v224 offset:18848\n\t"
"v_mfma_f32_4x4x4_16b_bf16 v[204:207], a[210:211], a[194:195], 0\n\t"
"v_cvt_pk_bf16_f32 v208, v218, v222\n\t"
"v_mfma_f32_4x4x4_16b_bf16 v[196:199], a[210:211], a[196:197], 0\n\t"
"ds_read_b64_tr_b16 a[202:203], v249 offset:43776\n\t"
"v_mfma_f32_4x4x4_16b_bf16 v[216:219], a[210:211], a[198:199], 0\n\t"
"buffer_load_dwordx4 a[116:119], off, s[92:95], s80 offset:1024 sc1\n\t"
"v_mfma_f32_16x16x32_bf16 v[152:155], a[0:3], a[152:155], v[152:155]\n\t"
"s_nop 0\n\t"
"ds_write_addtid_b32 v214 offset:12448\n\t"
"ds_read_b64_tr_b16 a[204:205], v248 offset:44288\n\t"
"v_mfma_f32_16x16x32_bf16 v[152:155], a[4:7], a[168:171], v[152:155]\n\t"
"buffer_load_dwordx4 a[108:111], off, s[96:99], s79 offset:3072 sc1\n\t"
"v_mfma_f32_16x16x32_bf16 v[168:171], a[8:11], a[152:155], v[168:171]\n\t"
"ds_read_b64_tr_b16 a[206:207], v249 offset:44288\n\t"
"v_cvt_pk_bf16_f32 v205, v201, v205\n\t"
"v_mfma_f32_16x16x32_bf16 v[168:171], a[12:15], a[168:171], v[168:171]\n\t"
"v_cvt_pk_bf16_f32 v204, v200, v204\n\t"
"v_cvt_pk_bf16_f32 v203, v203, v207\n\t"
"v_mfma_f32_16x16x32_bf16 v[184:187], a[16:19], a[152:155], v[184:187]\n\t"
"ds_read_b128 a[152:155], v224 offset:17056\n\t"
"v_cvt_pk_bf16_f32 v201, v199, v219\n\t"
"v_mfma_f32_16x16x32_bf16 v[184:187], a[20:23], a[168:171], v[184:187]\n\t"
"buffer_load_dwordx4 a[104:107], off, s[96:99], s79 offset:2048 sc1\n\t"
"v_cvt_pk_bf16_f32 v207, v197, v217\n\t"
"v_mfma_f32_16x16x32_bf16 v[188:191], a[16:19], a[156:159], v[188:191]\n\t"
"ds_write_addtid_b32 v215 offset:13472\n\t"
"v_cvt_pk_bf16_f32 v202, v202, v206\n\t"
"v_mfma_f32_16x16x32_bf16 v[188:191], a[20:23], a[172:175], v[188:191]\n\t"
"s_waitcnt lgkmcnt(2)\n\t"
"ds_write_addtid_b32 v213 offset:5152\n\t"
"v_cvt_pk_bf16_f32 v206, v196, v216\n\t"
"v_mfma_f32_4x4x4_16b_bf16 v[220:223], a[210:211], a[200:201], 0\n\t"
"ds_write_addtid_b32 v212 offset:4128\n\t"
"v_mfma_f32_4x4x4_16b_bf16 v[212:215], a[210:211], a[202:203], 0\n\t"
"v_cvt_pk_bf16_f32 v200, v198, v218\n\t"
"v_mfma_f32_4x4x4_16b_bf16 v[196:199], a[210:211], a[204:205], 0\n\t"
"ds_read_b128 a[168:171], v224 offset:19104\n\t"
"v_mfma_f32_4x4x4_16b_bf16 v[216:219], a[210:211], a[206:207], 0\n\t"
"buffer_load_dwordx4 a[96:99], off, s[96:99], s79 offset:0 sc1\n\t"
"v_mfma_f32_16x16x32_bf16 v[172:175], a[8:11], a[156:159], v[172:175]\n\t"
"s_nop 0\n\t"
"ds_write_addtid_b32 v204 offset:2048\n\t"
"ds_write_addtid_b32 v207 offset:11392\n\t"
"v_mfma_f32_16x16x32_bf16 v[172:175], a[12:15], a[172:175], v[172:175]\n\t"
"s_nop 0\n\t"
"ds_write_addtid_b32 v206 offset:10368\n\t"
"ds_write_addtid_b32 v205 offset:3072\n\t"
"v_mfma_f32_16x16x32_bf16 v[156:159], a[0:3], a[156:159], v[156:159]\n\t"
"v_cvt_pk_bf16_f32 v222, v222, v214\n\t"
"v_cvt_pk_bf16_f32 v213, v221, v213\n\t"
"v_mfma_f32_16x16x32_bf16 v[156:159], a[4:7], a[172:175], v[156:159]\n\t"
"s_waitcnt vmcnt(17) lgkmcnt(8)\n\t"
"v_cvt_pk_bf16_f32 v212, v220, v212\n\t"
"v_cvt_pk_bf16_f32 v223, v223, v215\n\t"
"v_mfma_f32_16x16x32_bf16 v[96:99], a[24:27], a[144:147], v[96:99]\n\t"
"ds_write_addtid_b32 v212 offset:6176\n\t"
"v_cvt_pk_bf16_f32 v214, v196, v216\n\t"
"v_mfma_f32_16x16x32_bf16 v[96:99], a[28:31], a[160:163], v[96:99]\n\t"
"ds_write_addtid_b32 v214 offset:14496\n\t"
"v_cvt_pk_bf16_f32 v215, v197, v217\n\t"
"v_mfma_f32_16x16x32_bf16 v[112:115], a[32:35], a[144:147], v[112:115]\n\t"
"s_nop 0\n\t"
"ds_write_addtid_b32 v213 offset:7200\n\t"
"ds_write_addtid_b32 v215 offset:15520\n\t"
"v_mfma_f32_16x16x32_bf16 v[112:115], a[36:39], a[160:163], v[112:115]\n\t"
"buffer_load_dwordx4 a[100:103], off, s[96:99], s79 offset:1024 sc1\n\t"
"v_mfma_f32_16x16x32_bf16 v[128:131], a[40:43], a[144:147], v[128:131]\n\t"
"ds_read_b128 a[172:175], v224 offset:19360\n\t"
"v_cvt_pk_bf16_f32 v220, v198, v218\n\t"
"v_mfma_f32_16x16x32_bf16 v[128:131], a[44:47], a[160:163], v[128:131]\n\t"
"buffer_load_dwordx4 a[136:139], off, s[92:95], s79 offset:2048 sc1\n\t"
"v_mfma_f32_16x16x32_bf16 v[132:135], a[40:43], a[148:151], v[132:135]\n\t"
"s_nop 0\n\t"
"ds_read_b128 a[156:159], v224 offset:17312\n\t"
"ds_read_b128 a[244:247], v255 offset:39168\n\t"
"v_mfma_f32_16x16x32_bf16 v[132:135], a[44:47], a[164:167], v[132:135]\n\t"
"buffer_load_dwordx4 a[140:143], off, s[92:95], s79 offset:3072 sc1\n\t"
"v_mfma_f32_16x16x32_bf16 v[116:119], a[32:35], a[148:151], v[116:119]\n\t"
"v_mfma_f32_16x16x32_bf16 v[116:119], a[36:39], a[164:167], v[116:119]\n\t"
"buffer_load_dwordx4 a[132:135], off, s[92:95], s79 offset:1024 sc1\n\t"
"v_mfma_f32_16x16x32_bf16 v[100:103], a[24:27], a[148:151], v[100:103]\n\t"
"s_waitcnt lgkmcnt(0)\n\t"
"v_cvt_pk_bf16_f32 v221, v199, v219\n\t"
"v_mfma_f32_16x16x32_bf16 v[100:103], a[28:31], a[164:167], v[100:103]\n\t"
"s_barrier\n\t"
"v_mfma_f32_16x16x32_bf16 v[104:107], a[24:27], a[152:155], v[104:107]\n\t"
"s_nop 0\n\t"
"ds_read_b128 a[144:147], v224 offset:1024\n\t"
"ds_read_b128 a[148:151], v224 offset:1280\n\t"
"v_mfma_f32_16x16x32_bf16 v[104:107], a[28:31], a[168:171], v[104:107]\n\t"
"buffer_load_dwordx4 a[128:131], off, s[92:95], s79 offset:0 sc1\n\t"
"v_mfma_f32_16x16x32_bf16 v[120:123], a[32:35], a[152:155], v[120:123]\n\t"
"ds_read_b128 a[160:163], v224 offset:3072\n\t"
"v_add_u32 v248, 101632, v225\n\t"
"v_mfma_f32_16x16x32_bf16 v[120:123], a[36:39], a[168:171], v[120:123]\n\t"
"ds_read_b128 a[164:167], v224 offset:3328\n\t"
"v_mfma_f32_16x16x32_bf16 v[136:139], a[40:43], a[152:155], v[136:139]\n\t"
"ds_read_b128 a[152:155], v224 offset:1536\n\t"
"v_xor_b32 v249, 32, v248\n\t"
"v_mfma_f32_16x16x32_bf16 v[136:139], a[44:47], a[168:171], v[136:139]\n\t"
"buffer_load_dwordx4 a[120:123], off, s[96:99], s78 offset:2048 sc1\n\t"
"v_mfma_f32_16x16x32_bf16 v[140:143], a[40:43], a[156:159], v[140:143]\n\t"
"ds_read_b128 a[240:243], v255 offset:18432\n\t"
"v_xor_b32 v250, 64, v248\n\t"
"v_mfma_f32_16x16x32_bf16 v[140:143], a[44:47], a[172:175], v[140:143]\n\t"
"ds_read_b128 a[168:171], v224 offset:3584\n\t"
"v_xor_b32 v251, 96, v248\n\t"
"v_mfma_f32_16x16x32_bf16 v[124:127], a[32:35], a[156:159], v[124:127]\n\t"
"ds_read_b64_tr_b16 a[178:179], v250 offset:43776\n\t"
"v_mfma_f32_16x16x32_bf16 v[124:127], a[36:39], a[172:175], v[124:127]\n\t"
"buffer_load_dwordx4 a[124:127], off, s[96:99], s78 offset:3072 sc1\n\t"
"v_mfma_f32_16x16x32_bf16 v[108:111], a[24:27], a[156:159], v[108:111]\n\t"
"ds_read_b128 a[156:159], v224 offset:1792\n\t"
"v_mfma_f32_16x16x32_bf16 v[108:111], a[28:31], a[172:175], v[108:111]\n\t"
"s_waitcnt vmcnt(18) lgkmcnt(6)\n\t"
"ds_read_b128 a[172:175], v224 offset:3840\n\t"
"v_mfma_f32_16x16x32_bf16 v[48:51], a[48:51], a[144:147], v[48:51]\n\t"
"ds_read_b64_tr_b16 a[186:187], v251 offset:43776\n\t"
"v_mfma_f32_16x16x32_bf16 v[48:51], a[52:55], a[160:163], v[48:51]\n\t"
"buffer_load_dwordx4 a[16:19], off, s[92:95], s74 offset:0 sc1\n\t"
"v_mfma_f32_16x16x32_bf16 v[64:67], a[56:59], a[144:147], v[64:67]\n\t"
"ds_read_b64_tr_b16 a[194:195], v248 offset:43776\n\t"
"v_mfma_f32_16x16x32_bf16 v[64:67], a[60:63], a[160:163], v[64:67]\n\t"
"s_nop 0\n\t"
"ds_read_b64_tr_b16 a[202:203], v249 offset:43776\n\t"
"ds_read_b64_tr_b16 a[176:177], v248 offset:41472\n\t"
"v_mfma_f32_16x16x32_bf16 v[80:83], a[64:67], a[144:147], v[80:83]\n\t"
"ds_read_b64_tr_b16 a[184:185], v249 offset:41472\n\t"
"v_mfma_f32_16x16x32_bf16 v[80:83], a[68:71], a[160:163], v[80:83]\n\t"
"s_waitcnt lgkmcnt(8)\n\t"
"buffer_load_dwordx4 a[20:23], off, s[92:95], s74 offset:1024 sc1\n\t"
"v_mfma_f32_16x16x32_bf16 v[84:87], a[64:67], a[148:151], v[84:87]\n\t"
"ds_read_b128 a[144:147], v224 offset:0\n\t"
"v_mfma_f32_16x16x32_bf16 v[84:87], a[68:71], a[164:167], v[84:87]\n\t"
"ds_read_b128 a[160:163], v224 offset:2048\n\t"
"v_mfma_f32_16x16x32_bf16 v[68:71], a[56:59], a[148:151], v[68:71]\n\t"
"ds_read_b64_tr_b16 a[180:181], v248 offset:46080\n\t"
"v_mfma_f32_16x16x32_bf16 v[68:71], a[60:63], a[164:167], v[68:71]\n\t"
"buffer_load_dwordx4 a[12:15], off, s[96:99], s72 offset:3072 sc1\n\t"
"v_mfma_f32_16x16x32_bf16 v[52:55], a[48:51], a[148:151], v[52:55]\n\t"
"ds_read_b128 a[148:151], v224 offset:256\n\t"
"v_mfma_f32_16x16x32_bf16 v[52:55], a[52:55], a[164:167], v[52:55]\n\t"
"ds_read_b128 a[164:167], v224 offset:2304\n\t"
"v_mfma_f32_16x16x32_bf16 v[56:59], a[48:51], a[152:155], v[56:59]\n\t"
"s_nop 0\n\t"
"ds_write_addtid_b32 v209 offset:30016\n\t"
"ds_write_addtid_b32 v201 offset:27936\n\t"
"v_mfma_f32_16x16x32_bf16 v[56:59], a[52:55], a[168:171], v[56:59]\n\t"
"buffer_load_dwordx4 a[8:11], off, s[96:99], s72 offset:2048 sc1\n\t"
"v_mfma_f32_16x16x32_bf16 v[72:75], a[56:59], a[152:155], v[72:75]\n\t"
"s_nop 0\n\t"
"ds_write_addtid_b32 v194 offset:16544\n\t"
"ds_write_addtid_b32 v211 offset:21696\n\t"
"v_mfma_f32_16x16x32_bf16 v[72:75], a[60:63], a[168:171], v[72:75]\n\t"
"s_nop 0\n\t"
"ds_write_addtid_b32 v200 offset:26912\n\t"
"ds_write_addtid_b32 v222 offset:22720\n\t"
"v_mfma_f32_16x16x32_bf16 v[88:91], a[64:67], a[152:155], v[88:91]\n\t"
"ds_read_b128 a[152:155], v224 offset:512\n\t"
"v_mfma_f32_16x16x32_bf16 v[88:91], a[68:71], a[168:171], v[88:91]\n\t"
"s_waitcnt lgkmcnt(10)\n\t"
"buffer_load_dwordx4 a[0:3], off, s[96:99], s72 offset:0 sc1\n\t"
"v_mfma_f32_16x16x32_bf16 v[92:95], a[64:67], a[156:159], v[92:95]\n\t"
"ds_read_b128 a[168:171], v224 offset:2560\n\t"
"v_mfma_f32_16x16x32_bf16 v[92:95], a[68:71], a[172:175], v[92:95]\n\t"
"s_nop 0\n\t"
"ds_write_addtid_b32 v202 offset:18592\n\t"
"ds_write_addtid_b32 v203 offset:19616\n\t"
"v_mfma_f32_16x16x32_bf16 v[76:79], a[56:59], a[156:159], v[76:79]\n\t"
"s_nop 0\n\t"
"ds_write_addtid_b32 v192 offset:24864\n\t"
"ds_write_addtid_b32 v210 offset:20672\n\t"
"v_mfma_f32_16x16x32_bf16 v[76:79], a[60:63], a[172:175], v[76:79]\n\t"
"buffer_load_dwordx4 a[4:7], off, s[96:99], s72 offset:1024 sc1\n\t"
"v_mfma_f32_16x16x32_bf16 v[60:63], a[48:51], a[156:159], v[60:63]\n\t"
"ds_read_b128 a[156:159], v224 offset:768\n\t"
"v_mfma_f32_16x16x32_bf16 v[60:63], a[52:55], a[172:175], v[60:63]\n\t"
"s_waitcnt vmcnt(18) lgkmcnt(11)\n\t"
"ds_write_addtid_b32 v195 offset:17568\n\t"
"ds_write_addtid_b32 v193 offset:25888\n\t"
"v_mfma_f32_16x16x32_bf16 v[0:3], a[72:75], a[144:147], v[0:3]\n\t"
"s_nop 0\n\t"
"ds_write_addtid_b32 v208 offset:28992\n\t"
"ds_write_addtid_b32 v221 offset:32064\n\t"
"v_mfma_f32_16x16x32_bf16 v[0:3], a[76:79], a[160:163], v[0:3]\n\t"
"buffer_load_dwordx4 a[40:43], off, s[92:95], s72 offset:2048 sc1\n\t"
"v_mfma_f32_16x16x32_bf16 v[16:19], a[80:83], a[144:147], v[16:19]\n\t"
"s_nop 0\n\t"
"ds_write_addtid_b32 v223 offset:23744\n\t"
"ds_write_addtid_b32 v220 offset:31040\n\t"
"v_mfma_f32_16x16x32_bf16 v[16:19], a[84:87], a[160:163], v[16:19]\n\t"
"ds_read_b128 a[172:175], v224 offset:2816\n\t"
"v_mfma_f32_16x16x32_bf16 v[32:35], a[88:91], a[144:147], v[32:35]\n\t"
"ds_read_b64_tr_b16 a[188:189], v249 offset:46080\n\t"
"v_mfma_f32_16x16x32_bf16 v[32:35], a[92:95], a[160:163], v[32:35]\n\t"
"buffer_load_dwordx4 a[44:47], off, s[92:95], s72 offset:3072 sc1\n\t"
"v_mfma_f32_16x16x32_bf16 v[36:39], a[88:91], a[148:151], v[36:39]\n\t"
"ds_read_b64_tr_b16 a[192:193], v250 offset:41472\n\t"
"v_mfma_f32_16x16x32_bf16 v[36:39], a[92:95], a[164:167], v[36:39]\n\t"
"ds_read_b64_tr_b16 a[200:201], v251 offset:41472\n\t"
"v_mfma_f32_16x16x32_bf16 v[20:23], a[80:83], a[148:151], v[20:23]\n\t"
"s_nop 0\n\t"
"ds_read_b64_tr_b16 a[196:197], v250 offset:46080\n\t"
"ds_read_b64_tr_b16 a[204:205], v251 offset:46080\n\t"
"v_mfma_f32_16x16x32_bf16 v[20:23], a[84:87], a[164:167], v[20:23]\n\t"
"buffer_load_dwordx4 a[36:39], off, s[92:95], s72 offset:1024 sc1\n\t"
"v_mfma_f32_16x16x32_bf16 v[4:7], a[72:75], a[148:151], v[4:7]\n\t"
"s_waitcnt lgkmcnt(5)\n\t"
"v_mfma_f32_16x16x32_bf16 v[4:7], a[76:79], a[164:167], v[4:7]\n\t"
"s_barrier\n\t"
"v_mfma_f32_16x16x32_bf16 v[8:11], a[72:75], a[152:155], v[8:11]\n\t"
"s_nop 0\n\t"
"ds_read_b128 a[144:147], v224 offset:17568\n\t"
"ds_read_b128 a[160:163], v224 offset:19616\n\t"
"v_mfma_f32_16x16x32_bf16 v[8:11], a[76:79], a[168:171], v[8:11]\n\t"
"buffer_load_dwordx4 a[32:35], off, s[92:95], s72 offset:0 sc1\n\t"
"v_mfma_f32_16x16x32_bf16 v[24:27], a[80:83], a[152:155], v[24:27]\n\t"
"ds_read_b128 a[148:151], v224 offset:17824\n\t"
"v_mfma_f32_16x16x32_bf16 v[24:27], a[84:87], a[168:171], v[24:27]\n\t"
"ds_read_b128 a[164:167], v224 offset:19872\n\t"
"v_mfma_f32_16x16x32_bf16 v[40:43], a[88:91], a[152:155], v[40:43]\n\t"
"v_mfma_f32_16x16x32_bf16 v[40:43], a[92:95], a[168:171], v[40:43]\n\t"
"buffer_load_dwordx4 a[24:27], off, s[96:99], s69 offset:2048 sc1\n\t"
"v_mfma_f32_16x16x32_bf16 v[44:47], a[88:91], a[156:159], v[44:47]\n\t"
"ds_read_b128 a[168:171], v224 offset:20128\n\t"
"v_mfma_f32_16x16x32_bf16 v[44:47], a[92:95], a[172:175], v[44:47]\n\t"
"buffer_load_dwordx4 a[28:31], off, s[96:99], s69 offset:3072 sc1\n\t"
"v_mfma_f32_16x16x32_bf16 v[28:31], a[80:83], a[156:159], v[28:31]\n\t"
"ds_read_b128 a[152:155], v224 offset:18080\n\t"
"v_mfma_f32_16x16x32_bf16 v[28:31], a[84:87], a[172:175], v[28:31]\n\t"
"buffer_load_dwordx4 a[64:67], off, s[92:95], s83 offset:0 sc1\n\t"
"v_mfma_f32_16x16x32_bf16 v[12:15], a[72:75], a[156:159], v[12:15]\n\t"
"ds_read_b128 a[156:159], v224 offset:18336\n\t"
"v_mfma_f32_16x16x32_bf16 v[12:15], a[76:79], a[172:175], v[12:15]\n\t"
"s_waitcnt lgkmcnt(5)\n\t"
"buffer_load_dwordx4 a[68:71], off, s[92:95], s83 offset:1024 sc1\n\t"
"v_mfma_f32_4x4x4_16b_bf16 v[200:203], a[220:221], a[178:179], 0\n\t"
"ds_read_b128 a[172:175], v224 offset:20384\n\t"
"v_mfma_f32_4x4x4_16b_bf16 v[204:207], a[220:221], a[186:187], 0\n\t"
"ds_read_b64_tr_b16 a[178:179], v250 offset:44928\n\t"
"v_mfma_f32_4x4x4_16b_bf16 v[216:219], a[220:221], a[194:195], 0\n\t"
"ds_read_b64_tr_b16 a[186:187], v251 offset:44928\n\t"
"v_mfma_f32_4x4x4_16b_bf16 v[220:223], a[220:221], a[202:203], 0\n\t"
"v_mfma_f32_4x4x4_16b_bf16 v[192:195], a[222:223], a[176:177], v[200:203]\n\t"
"ds_read_b64_tr_b16 a[194:195], v248 offset:44928\n\t"
"v_mfma_f32_4x4x4_16b_bf16 v[196:199], a[222:223], a[184:185], v[204:207]\n\t"
"ds_read_b64_tr_b16 a[202:203], v249 offset:44928\n\t"
"v_mfma_f32_4x4x4_16b_bf16 v[200:203], a[222:223], a[180:181], v[200:203]\n\t"
"ds_read_b64_tr_b16 a[176:177], v248 offset:42624\n\t"
"v_mfma_f32_4x4x4_16b_bf16 v[204:207], a[222:223], a[188:189], v[204:207]\n\t"
"ds_read_b64_tr_b16 a[184:185], v249 offset:42624\n\t"
"v_mfma_f32_4x4x4_16b_bf16 v[208:211], a[222:223], a[192:193], v[216:219]\n\t"
"ds_read_b64_tr_b16 a[180:181], v248 offset:47232\n\t"
"v_mfma_f32_4x4x4_16b_bf16 v[212:215], a[222:223], a[200:201], v[220:223]\n\t"
"ds_read_b64_tr_b16 a[188:189], v249 offset:47232\n\t"
"v_mfma_f32_4x4x4_16b_bf16 v[216:219], a[222:223], a[196:197], v[216:219]\n\t"
"ds_read_b64_tr_b16 a[192:193], v250 offset:42624\n\t"
"v_mfma_f32_4x4x4_16b_bf16 v[220:223], a[222:223], a[204:205], v[220:223]\n\t"
"s_waitcnt vmcnt(20) lgkmcnt(8)\n\t"
"buffer_load_dwordx4 a[60:63], off, s[96:99], s82 offset:3072 sc1\n\t"
"v_mfma_f32_16x16x32_bf16 v[144:147], a[96:99], a[144:147], v[144:147]\n\t"
"ds_read_b64_tr_b16 a[200:201], v251 offset:42624\n\t"
"v_mfma_f32_16x16x32_bf16 v[144:147], a[100:103], a[160:163], v[144:147]\n\t"
"buffer_load_dwordx4 a[56:59], off, s[96:99], s82 offset:2048 sc1\n\t"
"v_mfma_f32_16x16x32_bf16 v[160:163], a[104:107], a[144:147], v[160:163]\n\t"
"ds_read_b64_tr_b16 a[196:197], v250 offset:47232\n\t"
"v_cvt_pk_bf16_f32 v192, -v192, -v196\n\t"
"v_mfma_f32_16x16x32_bf16 v[160:163], a[108:111], a[160:163], v[160:163]\n\t"
"ds_write_addtid_b32 v192 offset:0\n\t"
"v_cvt_pk_bf16_f32 v201, v201, v205\n\t"
"v_mfma_f32_16x16x32_bf16 v[176:179], a[112:115], a[144:147], v[176:179]\n\t"
"ds_read_b128 a[144:147], v224 offset:16544\n\t"
"v_cvt_pk_bf16_f32 v211, -v211, -v215\n\t"
"v_mfma_f32_16x16x32_bf16 v[176:179], a[116:119], a[160:163], v[176:179]\n\t"
"buffer_load_dwordx4 a[48:51], off, s[96:99], s82 offset:0 sc1\n\t"
"v_mfma_f32_16x16x32_bf16 v[180:183], a[112:115], a[148:151], v[180:183]\n\t"
"ds_read_b128 a[160:163], v224 offset:18592\n\t"
"v_cvt_pk_bf16_f32 v202, v202, v206\n\t"
"v_mfma_f32_16x16x32_bf16 v[180:183], a[116:119], a[164:167], v[180:183]\n\t"
"ds_write_addtid_b32 v202 offset:5152\n\t"
"v_cvt_pk_bf16_f32 v210, -v210, -v214\n\t"
"v_mfma_f32_16x16x32_bf16 v[164:167], a[104:107], a[148:151], v[164:167]\n\t"
"ds_write_addtid_b32 v210 offset:3072\n\t"
"v_cvt_pk_bf16_f32 v219, v219, v223\n\t"
"v_mfma_f32_16x16x32_bf16 v[164:167], a[108:111], a[164:167], v[164:167]\n\t"
"buffer_load_dwordx4 a[52:55], off, s[96:99], s82 offset:1024 sc1\n\t"
"v_mfma_f32_16x16x32_bf16 v[148:151], a[96:99], a[148:151], v[148:151]\n\t"
"ds_read_b128 a[148:151], v224 offset:16800\n\t"
"v_cvt_pk_bf16_f32 v208, -v208, -v212\n\t"
"v_mfma_f32_16x16x32_bf16 v[148:151], a[100:103], a[164:167], v[148:151]\n\t"
"ds_write_addtid_b32 v208 offset:2048\n\t"
"v_cvt_pk_bf16_f32 v216, v216, v220\n\t"
"v_mfma_f32_16x16x32_bf16 v[152:155], a[96:99], a[152:155], v[152:155]\n\t"
"ds_read_b128 a[164:167], v224 offset:18848\n\t"
"v_cvt_pk_bf16_f32 v217, v217, v221\n\t"
"v_mfma_f32_16x16x32_bf16 v[152:155], a[100:103], a[168:171], v[152:155]\n\t"
"buffer_load_dwordx4 a[88:91], off, s[92:95], s82 offset:2048 sc1\n\t"
"v_mfma_f32_16x16x32_bf16 v[168:171], a[104:107], a[152:155], v[168:171]\n\t"
"ds_write_addtid_b32 v219 offset:15520\n\t"
"v_cvt_pk_bf16_f32 v203, v203, v207\n\t"
"v_mfma_f32_16x16x32_bf16 v[168:171], a[108:111], a[168:171], v[168:171]\n\t"
"ds_write_addtid_b32 v216 offset:6176\n\t"
"v_cvt_pk_bf16_f32 v218, v218, v222\n\t"
"v_mfma_f32_16x16x32_bf16 v[184:187], a[112:115], a[152:155], v[184:187]\n\t"
"ds_read_b128 a[152:155], v224 offset:17056\n\t"
"v_cvt_pk_bf16_f32 v200, v200, v204\n\t"
"v_mfma_f32_16x16x32_bf16 v[184:187], a[116:119], a[168:171], v[184:187]\n\t"
"buffer_load_dwordx4 a[92:95], off, s[92:95], s82 offset:3072 sc1\n\t"
"v_mfma_f32_16x16x32_bf16 v[188:191], a[112:115], a[156:159], v[188:191]\n\t"
"ds_read_b128 a[168:171], v224 offset:19104\n\t"
"v_cvt_pk_bf16_f32 v194, -v194, -v198\n\t"
"v_mfma_f32_16x16x32_bf16 v[188:191], a[116:119], a[172:175], v[188:191]\n\t"
"ds_write_addtid_b32 v203 offset:13472\n\t"
"v_cvt_pk_bf16_f32 v193, -v193, -v197\n\t"
"v_mfma_f32_16x16x32_bf16 v[172:175], a[104:107], a[156:159], v[172:175]\n\t"
"ds_write_addtid_b32 v200 offset:4128\n\t"
"v_cvt_pk_bf16_f32 v195, -v195, -v199\n\t"
"v_mfma_f32_16x16x32_bf16 v[172:175], a[108:111], a[172:175], v[172:175]\n\t"
"buffer_load_dwordx4 a[84:87], off, s[92:95], s82 offset:1024 sc1\n\t"
"v_mfma_f32_16x16x32_bf16 v[156:159], a[96:99], a[156:159], v[156:159]\n\t"
"s_nop 0\n\t"
"ds_write_addtid_b32 v193 offset:8320\n\t"
"ds_write_addtid_b32 v201 offset:12448\n\t"
"v_mfma_f32_16x16x32_bf16 v[156:159], a[100:103], a[172:175], v[156:159]\n\t"
"s_waitcnt vmcnt(21) lgkmcnt(8)\n\t"
"ds_write_addtid_b32 v217 offset:14496\n\t"
"v_cvt_pk_bf16_f32 v209, -v209, -v213\n\t"
"v_mfma_f32_16x16x32_bf16 v[96:99], a[120:123], a[144:147], v[96:99]\n\t"
"ds_write_addtid_b32 v209 offset:10368\n\t"
"v_mfma_f32_16x16x32_bf16 v[96:99], a[124:127], a[160:163], v[96:99]\n\t"
"s_nop 0\n\t"
"ds_write_addtid_b32 v211 offset:11392\n\t"
"ds_write_addtid_b32 v218 offset:7200\n\t"
"v_mfma_f32_16x16x32_bf16 v[112:115], a[128:131], a[144:147], v[112:115]\n\t"
"s_nop 0\n\t"
"ds_write_addtid_b32 v194 offset:1024\n\t"
"ds_write_addtid_b32 v195 offset:9344\n\t"
"v_mfma_f32_16x16x32_bf16 v[112:115], a[132:135], a[160:163], v[112:115]\n\t"
".p2align 3\n\t"
"buffer_load_dwordx4 a[80:83], off, s[92:95], s82 offset:0 sc1\n\t"
"v_pk_fma_f32 v[48:49], v[144:145], s[90:91], v[48:49]\n\t"
"v_pk_fma_f32 v[50:51], v[146:147], s[90:91], v[50:51]\n\t"
"v_pk_add_f32 v[144:145], v[48:49], v[144:145] neg_lo:[0,1] neg_hi:[0,1]\n\t"
"v_pk_add_f32 v[146:147], v[50:51], v[146:147] neg_lo:[0,1] neg_hi:[0,1]\n\t"
"v_pk_fma_f32 v[64:65], v[160:161], s[90:91], v[64:65]\n\t"
"v_pk_fma_f32 v[66:67], v[162:163], s[90:91], v[66:67]\n\t"
"v_pk_add_f32 v[160:161], v[64:65], v[160:161] neg_lo:[0,1] neg_hi:[0,1]\n\t"
"v_pk_add_f32 v[162:163], v[66:67], v[162:163] neg_lo:[0,1] neg_hi:[0,1]\n\t"
"v_pk_fma_f32 v[80:81], v[176:177], s[90:91], v[80:81]\n\t"
"v_pk_fma_f32 v[82:83], v[178:179], s[90:91], v[82:83]\n\t"
"v_pk_add_f32 v[176:177], v[80:81], v[176:177] neg_lo:[0,1] neg_hi:[0,1]\n\t"
"v_pk_add_f32 v[178:179], v[82:83], v[178:179] neg_lo:[0,1] neg_hi:[0,1]\n\t"
"buffer_load_dwordx4 a[72:75], off, s[96:99], s81 offset:2048 sc1\n\t"
"v_pk_fma_f32 v[84:85], v[180:181], s[90:91], v[84:85]\n\t"
"v_pk_fma_f32 v[86:87], v[182:183], s[90:91], v[86:87]\n\t"
"v_pk_add_f32 v[180:181], v[84:85], v[180:181] neg_lo:[0,1] neg_hi:[0,1]\n\t"
"v_pk_add_f32 v[182:183], v[86:87], v[182:183] neg_lo:[0,1] neg_hi:[0,1]\n\t"
"v_pk_fma_f32 v[68:69], v[164:165], s[90:91], v[68:69]\n\t"
"v_pk_fma_f32 v[70:71], v[166:167], s[90:91], v[70:71]\n\t"
"v_pk_add_f32 v[164:165], v[68:69], v[164:165] neg_lo:[0,1] neg_hi:[0,1]\n\t"
"v_pk_add_f32 v[166:167], v[70:71], v[166:167] neg_lo:[0,1] neg_hi:[0,1]\n\t"
"v_pk_fma_f32 v[52:53], v[148:149], s[90:91], v[52:53]\n\t"
"v_pk_fma_f32 v[54:55], v[150:151], s[90:91], v[54:55]\n\t"
"v_pk_add_f32 v[148:149], v[52:53], v[148:149] neg_lo:[0,1] neg_hi:[0,1]\n\t"
"v_pk_add_f32 v[150:151], v[54:55], v[150:151] neg_lo:[0,1] neg_hi:[0,1]\n\t"
"buffer_load_dwordx4 a[76:79], off, s[96:99], s81 offset:3072 sc1\n\t"
"v_pk_fma_f32 v[56:57], v[152:153], s[90:91], v[56:57]\n\t"
"v_pk_fma_f32 v[58:59], v[154:155], s[90:91], v[58:59]\n\t"
"v_pk_add_f32 v[152:153], v[56:57], v[152:153] neg_lo:[0,1] neg_hi:[0,1]\n\t"
"v_pk_add_f32 v[154:155], v[58:59], v[154:155] neg_lo:[0,1] neg_hi:[0,1]\n\t"
"v_pk_fma_f32 v[72:73], v[168:169], s[90:91], v[72:73]\n\t"
"v_pk_fma_f32 v[74:75], v[170:171], s[90:91], v[74:75]\n\t"
"v_pk_add_f32 v[168:169], v[72:73], v[168:169] neg_lo:[0,1] neg_hi:[0,1]\n\t"
"v_pk_add_f32 v[170:171], v[74:75], v[170:171] neg_lo:[0,1] neg_hi:[0,1]\n\t"
"v_pk_fma_f32 v[88:89], v[184:185], s[90:91], v[88:89]\n\t"
"v_pk_fma_f32 v[90:91], v[186:187], s[90:91], v[90:91]\n\t"
"v_pk_add_f32 v[184:185], v[88:89], v[184:185] neg_lo:[0,1] neg_hi:[0,1]\n\t"
"v_pk_add_f32 v[186:187], v[90:91], v[186:187] neg_lo:[0,1] neg_hi:[0,1]\n\t"
"buffer_load_dwordx4 a[112:115], off, s[92:95], s56 offset:0 sc1\n\t"
"v_pk_fma_f32 v[92:93], v[188:189], s[90:91], v[92:93]\n\t"
"v_pk_fma_f32 v[94:95], v[190:191], s[90:91], v[94:95]\n\t"
"v_pk_add_f32 v[188:189], v[92:93], v[188:189] neg_lo:[0,1] neg_hi:[0,1]\n\t"
"v_pk_add_f32 v[190:191], v[94:95], v[190:191] neg_lo:[0,1] neg_hi:[0,1]\n\t"
"v_pk_fma_f32 v[76:77], v[172:173], s[90:91], v[76:77]\n\t"
"v_pk_fma_f32 v[78:79], v[174:175], s[90:91], v[78:79]\n\t"
"v_pk_add_f32 v[172:173], v[76:77], v[172:173] neg_lo:[0,1] neg_hi:[0,1]\n\t"
"v_pk_add_f32 v[174:175], v[78:79], v[174:175] neg_lo:[0,1] neg_hi:[0,1]\n\t"
"v_pk_fma_f32 v[60:61], v[156:157], s[90:91], v[60:61]\n\t"
"v_pk_fma_f32 v[62:63], v[158:159], s[90:91], v[62:63]\n\t"
"v_pk_add_f32 v[156:157], v[60:61], v[156:157] neg_lo:[0,1] neg_hi:[0,1]\n\t"
"v_pk_add_f32 v[158:159], v[62:63], v[158:159] neg_lo:[0,1] neg_hi:[0,1]\n\t"
"v_mfma_f32_16x16x32_bf16 v[128:131], a[136:139], a[144:147], v[128:131]\n\t"
"ds_read_b128 a[172:175], v224 offset:19360\n\t"
"v_mfma_f32_16x16x32_bf16 v[128:131], a[140:143], a[160:163], v[128:131]\n\t"
"buffer_load_dwordx4 a[116:119], off, s[92:95], s56 offset:1024 sc1\n\t"
"v_mfma_f32_16x16x32_bf16 v[132:135], a[136:139], a[148:151], v[132:135]\n\t"
"s_nop 0\n\t"
"ds_read_b128 a[156:159], v224 offset:17312\n\t"
"v_mfma_f32_16x16x32_bf16 v[132:135], a[140:143], a[164:167], v[132:135]\n\t"
"buffer_load_dwordx4 a[108:111], off, s[96:99], s55 offset:3072 sc1\n\t"
"v_mfma_f32_16x16x32_bf16 v[116:119], a[128:131], a[148:151], v[116:119]\n\t"
"s_nop 0\n\t"
"ds_read_b64_tr_b16 a[204:205], v251 offset:47232\n\t"
"v_mfma_f32_16x16x32_bf16 v[116:119], a[132:135], a[164:167], v[116:119]\n\t"
"buffer_load_dwordx4 a[104:107], off, s[96:99], s55 offset:2048 sc1\n\t"
"v_mfma_f32_16x16x32_bf16 v[100:103], a[120:123], a[148:151], v[100:103]\n\t"
"s_nop 0\n\t"
"v_accvgpr_read_b32 v224, a212\n\t"
"v_mfma_f32_16x16x32_bf16 v[100:103], a[124:127], a[164:167], v[100:103]\n\t"
"s_waitcnt lgkmcnt(0)\n\t"
"s_barrier\n\t"
"v_mfma_f32_16x16x32_bf16 v[104:107], a[120:123], a[152:155], v[104:107]\n\t"
"s_add_u32 m0, s26, 145152\n\t"
"ds_read_b128 a[144:147], v224 offset:1024\n\t"
"v_mfma_f32_16x16x32_bf16 v[104:107], a[124:127], a[168:171], v[104:107]\n\t"
"s_nop 0\n\t"
"ds_read_b128 a[160:163], v224 offset:3072\n\t"
"v_mfma_f32_16x16x32_bf16 v[120:123], a[128:131], a[152:155], v[120:123]\n\t"
"s_nop 0\n\t"
"ds_read_b128 a[148:151], v224 offset:1280\n\t"
"v_mfma_f32_16x16x32_bf16 v[120:123], a[132:135], a[168:171], v[120:123]\n\t"
"s_nop 0\n\t"
"ds_read_b128 a[164:167], v224 offset:3328\n\t"
"v_mfma_f32_16x16x32_bf16 v[136:139], a[136:139], a[152:155], v[136:139]\n\t"
"s_nop 0\n\t"
"v_mfma_f32_16x16x32_bf16 v[136:139], a[140:143], a[168:171], v[136:139]\n\t"
"buffer_load_dwordx4 v236, s[12:15], s22 offen offset:256 nt lds\n\t"
"v_mfma_f32_16x16x32_bf16 v[140:143], a[136:139], a[156:159], v[140:143]\n\t"
"s_nop 0\n\t"
"ds_read_b128 a[152:155], v224 offset:1536\n\t"
"v_mfma_f32_16x16x32_bf16 v[140:143], a[140:143], a[172:175], v[140:143]\n\t"
"s_nop 0\n\t"
"ds_read_b128 a[228:231], v254 offset:59904\n\t"
"v_mfma_f32_16x16x32_bf16 v[124:127], a[128:131], a[156:159], v[124:127]\n\t"
"s_nop 0\n\t"
"ds_read_b128 a[168:171], v224 offset:3584\n\t"
"v_mfma_f32_16x16x32_bf16 v[124:127], a[132:135], a[172:175], v[124:127]\n\t"
"buffer_load_dwordx4 v237, s[12:15], s22 offen offset:1280 nt lds\n\t"
"v_mfma_f32_16x16x32_bf16 v[108:111], a[120:123], a[156:159], v[108:111]\n\t"
"s_nop 0\n\t"
"ds_read_b128 a[156:159], v224 offset:1792\n\t"
"v_mfma_f32_16x16x32_bf16 v[108:111], a[124:127], a[172:175], v[108:111]\n\t"
"buffer_load_dwordx4 a[96:99], off, s[96:99], s55 offset:0 sc1\n\t"
"v_mfma_f32_4x4x4_16b_bf16 v[200:203], a[220:221], a[178:179], 0\n\t"
"ds_read_b128 a[172:175], v224 offset:3840\n\t"
"v_mfma_f32_4x4x4_16b_bf16 v[204:207], a[220:221], a[186:187], 0\n\t"
"ds_read_b64_tr_b16 a[178:179], v250 offset:23040\n\t"
"v_mfma_f32_4x4x4_16b_bf16 v[216:219], a[220:221], a[194:195], 0\n\t"
"ds_read_b64_tr_b16 a[186:187], v251 offset:23040\n\t"
"v_mfma_f32_4x4x4_16b_bf16 v[220:223], a[220:221], a[202:203], 0\n\t"
"buffer_load_dwordx4 v238, s[12:15], s22 offen offset:2304 nt lds\n\t"
"v_mfma_f32_4x4x4_16b_bf16 v[192:195], a[222:223], a[176:177], v[200:203]\n\t"
"ds_read_b64_tr_b16 a[194:195], v248 offset:23040\n\t"
"v_mfma_f32_4x4x4_16b_bf16 v[196:199], a[222:223], a[184:185], v[204:207]\n\t"
"ds_read_b64_tr_b16 a[202:203], v249 offset:23040\n\t"
"v_mfma_f32_4x4x4_16b_bf16 v[200:203], a[222:223], a[180:181], v[200:203]\n\t"
"ds_read_b64_tr_b16 a[176:177], v248 offset:20736\n\t"
"v_mfma_f32_4x4x4_16b_bf16 v[204:207], a[222:223], a[188:189], v[204:207]\n\t"
".p2align 3\n\t"
"ds_read_b64_tr_b16 a[184:185], v249 offset:20736\n\t"
"v_pk_fma_f32 v[0:1], v[96:97], s[90:91], v[0:1]\n\t"
"v_pk_fma_f32 v[2:3], v[98:99], s[90:91], v[2:3]\n\t"
"v_pk_add_f32 v[96:97], v[0:1], v[96:97] neg_lo:[0,1] neg_hi:[0,1]\n\t"
"v_pk_add_f32 v[98:99], v[2:3], v[98:99] neg_lo:[0,1] neg_hi:[0,1]\n\t"
"v_pk_fma_f32 v[16:17], v[112:113], s[90:91], v[16:17]\n\t"
"v_pk_fma_f32 v[18:19], v[114:115], s[90:91], v[18:19]\n\t"
"v_pk_add_f32 v[112:113], v[16:17], v[112:113] neg_lo:[0,1] neg_hi:[0,1]\n\t"
"v_pk_add_f32 v[114:115], v[18:19], v[114:115] neg_lo:[0,1] neg_hi:[0,1]\n\t"
"v_pk_fma_f32 v[32:33], v[128:129], s[90:91], v[32:33]\n\t"
"v_pk_fma_f32 v[34:35], v[130:131], s[90:91], v[34:35]\n\t"
"v_pk_add_f32 v[128:129], v[32:33], v[128:129] neg_lo:[0,1] neg_hi:[0,1]\n\t"
"v_pk_add_f32 v[130:131], v[34:35], v[130:131] neg_lo:[0,1] neg_hi:[0,1]\n\t"
"v_pk_fma_f32 v[36:37], v[132:133], s[90:91], v[36:37]\n\t"
"v_pk_fma_f32 v[38:39], v[134:135], s[90:91], v[38:39]\n\t"
"v_pk_add_f32 v[132:133], v[36:37], v[132:133] neg_lo:[0,1] neg_hi:[0,1]\n\t"
"v_pk_add_f32 v[134:135], v[38:39], v[134:135] neg_lo:[0,1] neg_hi:[0,1]\n\t"
"v_pk_fma_f32 v[20:21], v[116:117], s[90:91], v[20:21]\n\t"
"v_pk_fma_f32 v[22:23], v[118:119], s[90:91], v[22:23]\n\t"
"v_pk_add_f32 v[116:117], v[20:21], v[116:117] neg_lo:[0,1] neg_hi:[0,1]\n\t"
"v_pk_add_f32 v[118:119], v[22:23], v[118:119] neg_lo:[0,1] neg_hi:[0,1]\n\t"
"v_pk_fma_f32 v[4:5], v[100:101], s[90:91], v[4:5]\n\t"
"v_pk_fma_f32 v[6:7], v[102:103], s[90:91], v[6:7]\n\t"
"v_pk_add_f32 v[100:101], v[4:5], v[100:101] neg_lo:[0,1] neg_hi:[0,1]\n\t"
"v_pk_add_f32 v[102:103], v[6:7], v[102:103] neg_lo:[0,1] neg_hi:[0,1]\n\t"
"v_pk_fma_f32 v[8:9], v[104:105], s[90:91], v[8:9]\n\t"
"v_pk_fma_f32 v[10:11], v[106:107], s[90:91], v[10:11]\n\t"
"v_pk_add_f32 v[104:105], v[8:9], v[104:105] neg_lo:[0,1] neg_hi:[0,1]\n\t"
"v_pk_add_f32 v[106:107], v[10:11], v[106:107] neg_lo:[0,1] neg_hi:[0,1]\n\t"
"v_pk_fma_f32 v[24:25], v[120:121], s[90:91], v[24:25]\n\t"
"v_pk_fma_f32 v[26:27], v[122:123], s[90:91], v[26:27]\n\t"
"v_pk_add_f32 v[120:121], v[24:25], v[120:121] neg_lo:[0,1] neg_hi:[0,1]\n\t"
"v_pk_add_f32 v[122:123], v[26:27], v[122:123] neg_lo:[0,1] neg_hi:[0,1]\n\t"
"v_pk_fma_f32 v[40:41], v[136:137], s[90:91], v[40:41]\n\t"
"v_pk_fma_f32 v[42:43], v[138:139], s[90:91], v[42:43]\n\t"
"v_pk_add_f32 v[136:137], v[40:41], v[136:137] neg_lo:[0,1] neg_hi:[0,1]\n\t"
"v_pk_add_f32 v[138:139], v[42:43], v[138:139] neg_lo:[0,1] neg_hi:[0,1]\n\t"
"v_pk_fma_f32 v[44:45], v[140:141], s[90:91], v[44:45]\n\t"
"v_pk_fma_f32 v[46:47], v[142:143], s[90:91], v[46:47]\n\t"
"v_pk_add_f32 v[140:141], v[44:45], v[140:141] neg_lo:[0,1] neg_hi:[0,1]\n\t"
"v_pk_add_f32 v[142:143], v[46:47], v[142:143] neg_lo:[0,1] neg_hi:[0,1]\n\t"
"v_pk_fma_f32 v[28:29], v[124:125], s[90:91], v[28:29]\n\t"
"v_pk_fma_f32 v[30:31], v[126:127], s[90:91], v[30:31]\n\t"
"v_pk_add_f32 v[124:125], v[28:29], v[124:125] neg_lo:[0,1] neg_hi:[0,1]\n\t"
"v_pk_add_f32 v[126:127], v[30:31], v[126:127] neg_lo:[0,1] neg_hi:[0,1]\n\t"
"v_pk_fma_f32 v[12:13], v[108:109], s[90:91], v[12:13]\n\t"
"v_pk_fma_f32 v[14:15], v[110:111], s[90:91], v[14:15]\n\t"
"v_pk_add_f32 v[108:109], v[12:13], v[108:109] neg_lo:[0,1] neg_hi:[0,1]\n\t"
"v_pk_add_f32 v[110:111], v[14:15], v[110:111] neg_lo:[0,1] neg_hi:[0,1]\n\t"
"v_mfma_f32_4x4x4_16b_bf16 v[208:211], a[222:223], a[192:193], v[216:219]\n\t"
"ds_read_b64_tr_b16 a[180:181], v248 offset:25344\n\t"
"v_mfma_f32_4x4x4_16b_bf16 v[212:215], a[222:223], a[200:201], v[220:223]\n\t"
"ds_read_b64_tr_b16 a[188:189], v249 offset:25344\n\t"
"v_mfma_f32_4x4x4_16b_bf16 v[216:219], a[222:223], a[196:197], v[216:219]\n\t"
"ds_read_b64_tr_b16 a[192:193], v250 offset:20736\n\t"
"v_mfma_f32_4x4x4_16b_bf16 v[220:223], a[222:223], a[204:205], v[220:223]\n\t"
"s_waitcnt vmcnt(26) lgkmcnt(8)\n\t"
"buffer_load_dwordx4 v239, s[12:15], s22 offen offset:3328 nt lds\n\t"
"v_mfma_f32_16x16x32_bf16 v[48:51], a[0:3], a[144:147], v[48:51]\n\t"
"s_nop 0\n\t"
"v_cvt_pk_bf16_f32 v193, -v193, -v197\n\t"
"v_mfma_f32_16x16x32_bf16 v[48:51], a[4:7], a[160:163], v[48:51]\n\t"
"s_nop 0\n\t"
"ds_write_b128 v255, a[228:231] offset:41472\n\t"
"v_mfma_f32_16x16x32_bf16 v[64:67], a[8:11], a[144:147], v[64:67]\n\t"
"Lpad4_%=:\n\t"
"s_cbranch_execz Lret0_%=\n\t"
"v_mfma_f32_16x16x32_bf16 v[64:67], a[12:15], a[160:163], v[64:67]\n\t"
"s_mov_b64 exec, s[36:37]\n\t"
"buffer_load_dwordx4 v240, s[12:15], s22 offen offset:3840 nt lds\n\t"
"s_mov_b64 exec, -1\n\t"
"v_mfma_f32_16x16x32_bf16 v[80:83], a[16:19], a[144:147], v[80:83]\n\t"
"s_lshl_b32 m0, s73, 8\n\t"
"v_cvt_pk_bf16_f32 v211, -v211, -v215\n\t"
"v_mfma_f32_16x16x32_bf16 v[80:83], a[20:23], a[160:163], v[80:83]\n\t"
"ds_read_b128 a[160:163], v224 offset:2048\n\t"
"v_cvt_pk_bf16_f32 v203, v203, v207\n\t"
"v_mfma_f32_16x16x32_bf16 v[84:87], a[16:19], a[148:151], v[84:87]\n\t"
"ds_read_b128 a[144:147], v224 offset:0\n\t"
"v_cvt_pk_bf16_f32 v216, v216, v220\n\t"
"v_mfma_f32_16x16x32_bf16 v[84:87], a[20:23], a[164:167], v[84:87]\n\t"
"buffer_load_dwordx4 a[100:103], off, s[96:99], s55 offset:1024 sc1\n\t"
"v_mfma_f32_16x16x32_bf16 v[68:71], a[8:11], a[148:151], v[68:71]\n\t"
"ds_read_b64_tr_b16 a[200:201], v251 offset:20736\n\t"
"v_cvt_pk_bf16_f32 v192, -v192, -v196\n\t"
"v_mfma_f32_16x16x32_bf16 v[68:71], a[12:15], a[164:167], v[68:71]\n\t"
"v_cvt_pk_bf16_f32 v210, -v210, -v214\n\t"
"v_cvt_pk_bf16_f32 v217, v217, v221\n\t"
"v_mfma_f32_16x16x32_bf16 v[52:55], a[0:3], a[148:151], v[52:55]\n\t"
"ds_read_b128 a[148:151], v224 offset:256\n\t"
"v_mfma_f32_16x16x32_bf16 v[52:55], a[4:7], a[164:167], v[52:55]\n\t"
"ds_read_b128 a[164:167], v224 offset:2304\n\t"
"v_cvt_pk_bf16_f32 v208, -v208, -v212\n\t"
"v_mfma_f32_16x16x32_bf16 v[56:59], a[0:3], a[152:155], v[56:59]\n\t"
"ds_write_addtid_b32 v211 offset:27936\n\t"
"v_cvt_pk_bf16_f32 v201, v201, v205\n\t"
"v_mfma_f32_16x16x32_bf16 v[56:59], a[4:7], a[168:171], v[56:59]\n\t"
"ds_write_addtid_b32 v208 offset:18592\n\t"
"v_cvt_pk_bf16_f32 v195, -v195, -v199\n\t"
"v_mfma_f32_16x16x32_bf16 v[72:75], a[8:11], a[152:155], v[72:75]\n\t"
"ds_write_addtid_b32 v203 offset:30016\n\t"
"v_cvt_pk_bf16_f32 v200, v200, v204\n\t"
"v_mfma_f32_16x16x32_bf16 v[72:75], a[12:15], a[168:171], v[72:75]\n\t"
"ds_write_addtid_b32 v210 offset:19616\n\t"
"v_cvt_pk_bf16_f32 v194, -v194, -v198\n\t"
"v_mfma_f32_16x16x32_bf16 v[88:91], a[16:19], a[152:155], v[88:91]\n\t"
"ds_read_b128 a[152:155], v224 offset:512\n\t"
"v_cvt_pk_bf16_f32 v202, v202, v206\n\t"
"v_mfma_f32_16x16x32_bf16 v[88:91], a[20:23], a[168:171], v[88:91]\n\t"
"ds_write_addtid_b32 v201 offset:28992\n\t"
"v_cvt_pk_bf16_f32 v218, v218, v222\n\t"
"v_mfma_f32_16x16x32_bf16 v[92:95], a[16:19], a[156:159], v[92:95]\n\t"
"ds_write_addtid_b32 v192 offset:16544\n\t"
"v_mfma_f32_16x16x32_bf16 v[92:95], a[20:23], a[172:175], v[92:95]\n\t"
"ds_write_addtid_b32 v195 offset:25888\n\t"
"v_cvt_pk_bf16_f32 v219, v219, v223\n\t"
"v_mfma_f32_16x16x32_bf16 v[76:79], a[8:11], a[156:159], v[76:79]\n\t"
"s_nop 0\n\t"
"ds_write_addtid_b32 v202 offset:21696\n\t"
"ds_write_addtid_b32 v216 offset:22720\n\t"
"v_mfma_f32_16x16x32_bf16 v[76:79], a[12:15], a[172:175], v[76:79]\n\t"
"ds_write_addtid_b32 v217 offset:31040\n\t"
"v_mfma_f32_16x16x32_bf16 v[60:63], a[0:3], a[156:159], v[60:63]\n\t"
"ds_write_addtid_b32 v194 offset:17568\n\t"
"v_cvt_pk_bf16_f32 v209, -v209, -v213\n\t"
"v_mfma_f32_16x16x32_bf16 v[60:63], a[4:7], a[172:175], v[60:63]\n\t"
"s_waitcnt vmcnt(23) lgkmcnt(10)\n\t"
"ds_write_addtid_b32 v218 offset:23744\n\t"
"v_mfma_f32_16x16x32_bf16 v[0:3], a[24:27], a[144:147], v[0:3]\n\t"
"s_nop 0\n\t"
"ds_write_addtid_b32 v193 offset:24864\n\t"
"ds_write_addtid_b32 v219 offset:32064\n\t"
"v_mfma_f32_16x16x32_bf16 v[0:3], a[28:31], a[160:163], v[0:3]\n\t"
"ds_write_addtid_b32 v209 offset:26912\n\t"
"v_mfma_f32_16x16x32_bf16 v[16:19], a[32:35], a[144:147], v[16:19]\n\t"
"ds_write_addtid_b32 v200 offset:20672\n\t"
"v_mfma_f32_16x16x32_bf16 v[16:19], a[36:39], a[160:163], v[16:19]\n\t"
"ds_read_b128 a[172:175], v224 offset:2816\n\t"
"v_mfma_f32_16x16x32_bf16 v[32:35], a[40:43], a[144:147], v[32:35]\n\t"
"ds_read_b128 a[156:159], v224 offset:768\n\t"
"v_mfma_f32_16x16x32_bf16 v[32:35], a[44:47], a[160:163], v[32:35]\n\t"
"v_mfma_f32_16x16x32_bf16 v[36:39], a[40:43], a[148:151], v[36:39]\n\t"
"ds_read_b128 a[168:171], v224 offset:2560\n\t"
"v_mfma_f32_16x16x32_bf16 v[36:39], a[44:47], a[164:167], v[36:39]\n\t"
"ds_read_b64_tr_b16 a[196:197], v250 offset:25344\n\t"
"v_mfma_f32_16x16x32_bf16 v[20:23], a[32:35], a[148:151], v[20:23]\n\t"
"ds_read_b64_tr_b16 a[204:205], v251 offset:25344\n\t"
"v_mfma_f32_16x16x32_bf16 v[20:23], a[36:39], a[164:167], v[20:23]\n\t"
"v_mfma_f32_16x16x32_bf16 v[4:7], a[24:27], a[148:151], v[4:7]\n\t"
"s_waitcnt lgkmcnt(2)\n\t"
"v_mfma_f32_16x16x32_bf16 v[4:7], a[28:31], a[164:167], v[4:7]\n\t"
"s_barrier\n\t"
"v_mfma_f32_16x16x32_bf16 v[8:11], a[24:27], a[152:155], v[8:11]\n\t"
"s_nop 0\n\t"
"ds_read_b128 a[144:147], v224 offset:17568\n\t"
"ds_read_b128 a[148:151], v224 offset:17824\n\t"
"v_mfma_f32_16x16x32_bf16 v[8:11], a[28:31], a[168:171], v[8:11]\n\t"
"buffer_load_dwordx4 a[136:139], off, s[92:95], s55 offset:2048 sc1\n\t"
"v_mfma_f32_16x16x32_bf16 v[24:27], a[32:35], a[152:155], v[24:27]\n\t"
"ds_read_b128 a[160:163], v224 offset:19616\n\t"
"v_mfma_f32_16x16x32_bf16 v[24:27], a[36:39], a[168:171], v[24:27]\n\t"
"ds_read_b128 a[164:167], v224 offset:19872\n\t"
"v_mfma_f32_16x16x32_bf16 v[40:43], a[40:43], a[152:155], v[40:43]\n\t"
"v_mfma_f32_16x16x32_bf16 v[40:43], a[44:47], a[168:171], v[40:43]\n\t"
"buffer_load_dwordx4 a[140:143], off, s[92:95], s55 offset:3072 sc1\n\t"
"v_mfma_f32_16x16x32_bf16 v[44:47], a[40:43], a[156:159], v[44:47]\n\t"
"ds_read_b128 a[168:171], v224 offset:20128\n\t"
"v_mfma_f32_16x16x32_bf16 v[44:47], a[44:47], a[172:175], v[44:47]\n\t"
"buffer_load_dwordx4 a[132:135], off, s[92:95], s55 offset:1024 sc1\n\t"
"v_mfma_f32_16x16x32_bf16 v[28:31], a[32:35], a[156:159], v[28:31]\n\t"
"ds_read_b128 a[152:155], v224 offset:18080\n\t"
"v_mfma_f32_16x16x32_bf16 v[28:31], a[36:39], a[172:175], v[28:31]\n\t"
"v_mfma_f32_16x16x32_bf16 v[12:15], a[24:27], a[156:159], v[12:15]\n\t"
"ds_read_b128 a[156:159], v224 offset:18336\n\t"
"v_mfma_f32_16x16x32_bf16 v[12:15], a[28:31], a[172:175], v[12:15]\n\t"
"s_waitcnt lgkmcnt(5)\n\t"
"buffer_load_dwordx4 a[128:131], off, s[92:95], s55 offset:0 sc1\n\t"
"v_mfma_f32_4x4x4_16b_bf16 v[200:203], a[220:221], a[178:179], 0\n\t"
"ds_read_b128 a[172:175], v224 offset:20384\n\t"
"v_mfma_f32_4x4x4_16b_bf16 v[204:207], a[220:221], a[186:187], 0\n\t"
"ds_read_b64_tr_b16 a[178:179], v250 offset:2304\n\t"
"v_mfma_f32_4x4x4_16b_bf16 v[216:219], a[220:221], a[194:195], 0\n\t"
"ds_read_b64_tr_b16 a[186:187], v251 offset:2304\n\t"
"v_mfma_f32_4x4x4_16b_bf16 v[220:223], a[220:221], a[202:203], 0\n\t"
"v_mfma_f32_4x4x4_16b_bf16 v[192:195], a[222:223], a[176:177], v[200:203]\n\t"
"ds_read_b64_tr_b16 a[194:195], v248 offset:2304\n\t"
"v_mfma_f32_4x4x4_16b_bf16 v[196:199], a[222:223], a[184:185], v[204:207]\n\t"
"ds_read_b64_tr_b16 a[202:203], v249 offset:2304\n\t"
"v_mfma_f32_4x4x4_16b_bf16 v[200:203], a[222:223], a[180:181], v[200:203]\n\t"
"ds_read_b64_tr_b16 a[176:177], v248 offset:0\n\t"
"v_mfma_f32_4x4x4_16b_bf16 v[204:207], a[222:223], a[188:189], v[204:207]\n\t"
"v_mfma_f32_4x4x4_16b_bf16 v[208:211], a[222:223], a[192:193], v[216:219]\n\t"
"ds_read_b64_tr_b16 a[184:185], v249 offset:0\n\t"
"v_mfma_f32_4x4x4_16b_bf16 v[212:215], a[222:223], a[200:201], v[220:223]\n\t"
"ds_read_b64_tr_b16 a[180:181], v248 offset:4608\n\t"
"v_mfma_f32_4x4x4_16b_bf16 v[216:219], a[222:223], a[196:197], v[216:219]\n\t"
"ds_read_b64_tr_b16 a[188:189], v249 offset:4608\n\t"
"v_mfma_f32_4x4x4_16b_bf16 v[220:223], a[222:223], a[204:205], v[220:223]\n\t"
"s_waitcnt vmcnt(21) lgkmcnt(7)\n\t"
"buffer_load_dwordx4 a[120:123], off, s[96:99], s54 offset:2048 sc1\n\t"
"v_mfma_f32_16x16x32_bf16 v[144:147], a[48:51], a[144:147], v[144:147]\n\t"
"ds_read_b64_tr_b16 a[192:193], v250 offset:0\n\t"
"v_cvt_pk_bf16_f32 v192, -v192, -v196\n\t"
"v_mfma_f32_16x16x32_bf16 v[144:147], a[52:55], a[160:163], v[144:147]\n\t"
"buffer_load_dwordx4 a[124:127], off, s[96:99], s54 offset:3072 sc1\n\t"
"v_mfma_f32_16x16x32_bf16 v[160:163], a[56:59], a[144:147], v[160:163]\n\t"
"ds_read_b64_tr_b16 a[200:201], v251 offset:0\n\t"
"v_cvt_pk_bf16_f32 v193, -v193, -v197\n\t"
"v_mfma_f32_16x16x32_bf16 v[160:163], a[60:63], a[160:163], v[160:163]\n\t"
"ds_write_addtid_b32 v193 offset:8320\n\t"
"v_cvt_pk_bf16_f32 v194, -v194, -v198\n\t"
"v_mfma_f32_16x16x32_bf16 v[176:179], a[64:67], a[144:147], v[176:179]\n\t"
"ds_read_b128 a[144:147], v224 offset:16544\n\t"
"v_cvt_pk_bf16_f32 v211, -v211, -v215\n\t"
"v_mfma_f32_16x16x32_bf16 v[176:179], a[68:71], a[160:163], v[176:179]\n\t"
"buffer_load_dwordx4 a[16:19], off, s[92:95], s41 offset:0 sc1\n\t"
"v_mfma_f32_16x16x32_bf16 v[180:183], a[64:67], a[148:151], v[180:183]\n\t"
"ds_read_b128 a[160:163], v224 offset:18592\n\t"
"v_cvt_pk_bf16_f32 v219, v219, v223\n\t"
"v_mfma_f32_16x16x32_bf16 v[180:183], a[68:71], a[164:167], v[180:183]\n\t"
"ds_write_addtid_b32 v194 offset:1024\n\t"
"v_cvt_pk_bf16_f32 v217, v217, v221\n\t"
"v_mfma_f32_16x16x32_bf16 v[164:167], a[56:59], a[148:151], v[164:167]\n\t"
"ds_write_addtid_b32 v192 offset:0\n\t"
"v_cvt_pk_bf16_f32 v209, -v209, -v213\n\t"
"v_mfma_f32_16x16x32_bf16 v[164:167], a[60:63], a[164:167], v[164:167]\n\t"
"buffer_load_dwordx4 a[20:23], off, s[92:95], s41 offset:1024 sc1\n\t"
"v_mfma_f32_16x16x32_bf16 v[148:151], a[48:51], a[148:151], v[148:151]\n\t"
"ds_read_b128 a[148:151], v224 offset:16800\n\t"
"v_cvt_pk_bf16_f32 v195, -v195, -v199\n\t"
"v_mfma_f32_16x16x32_bf16 v[148:151], a[52:55], a[164:167], v[148:151]\n\t"
"ds_write_addtid_b32 v211 offset:11392\n\t"
"v_cvt_pk_bf16_f32 v201, v201, v205\n\t"
"v_mfma_f32_16x16x32_bf16 v[152:155], a[48:51], a[152:155], v[152:155]\n\t"
"ds_read_b128 a[164:167], v224 offset:18848\n\t"
"v_cvt_pk_bf16_f32 v216, v216, v220\n\t"
"v_mfma_f32_16x16x32_bf16 v[152:155], a[52:55], a[168:171], v[152:155]\n\t"
"buffer_load_dwordx4 a[12:15], off, s[96:99], s40 offset:3072 sc1\n\t"
"v_mfma_f32_16x16x32_bf16 v[168:171], a[56:59], a[152:155], v[168:171]\n\t"
"ds_write_addtid_b32 v195 offset:9344\n\t"
"v_cvt_pk_bf16_f32 v210, -v210, -v214\n\t"
"v_mfma_f32_16x16x32_bf16 v[168:171], a[60:63], a[168:171], v[168:171]\n\t"
"ds_write_addtid_b32 v209 offset:10368\n\t"
"v_cvt_pk_bf16_f32 v218, v218, v222\n\t"
"v_mfma_f32_16x16x32_bf16 v[184:187], a[64:67], a[152:155], v[184:187]\n\t"
"ds_read_b128 a[152:155], v224 offset:17056\n\t"
"v_cvt_pk_bf16_f32 v200, v200, v204\n\t"
"v_mfma_f32_16x16x32_bf16 v[184:187], a[68:71], a[168:171], v[184:187]\n\t"
"buffer_load_dwordx4 a[8:11], off, s[96:99], s40 offset:2048 sc1\n\t"
"v_mfma_f32_16x16x32_bf16 v[188:191], a[64:67], a[156:159], v[188:191]\n\t"
"ds_read_b128 a[168:171], v224 offset:19104\n\t"
"v_cvt_pk_bf16_f32 v203, v203, v207\n\t"
"v_mfma_f32_16x16x32_bf16 v[188:191], a[68:71], a[172:175], v[188:191]\n\t"
"ds_write_addtid_b32 v217 offset:14496\n\t"
"v_cvt_pk_bf16_f32 v202, v202, v206\n\t"
"v_mfma_f32_16x16x32_bf16 v[172:175], a[56:59], a[156:159], v[172:175]\n\t"
"s_nop 0\n\t"
"ds_write_addtid_b32 v202 offset:5152\n\t"
"ds_write_addtid_b32 v216 offset:6176\n\t"
"v_mfma_f32_16x16x32_bf16 v[172:175], a[60:63], a[172:175], v[172:175]\n\t"
"buffer_load_dwordx4 a[0:3], off, s[96:99], s40 offset:0 sc1\n\t"
"v_mfma_f32_16x16x32_bf16 v[156:159], a[48:51], a[156:159], v[156:159]\n\t"
"s_nop 0\n\t"
"ds_write_addtid_b32 v210 offset:3072\n\t"
"ds_write_addtid_b32 v219 offset:15520\n\t"
"v_mfma_f32_16x16x32_bf16 v[156:159], a[52:55], a[172:175], v[156:159]\n\t"
"s_waitcnt vmcnt(22) lgkmcnt(9)\n\t"
"ds_write_addtid_b32 v203 offset:13472\n\t"
"v_mfma_f32_16x16x32_bf16 v[96:99], a[72:75], a[144:147], v[96:99]\n\t"
"ds_write_addtid_b32 v200 offset:4128\n\t"
"v_cvt_pk_bf16_f32 v208, -v208, -v212\n\t"
"v_mfma_f32_16x16x32_bf16 v[96:99], a[76:79], a[160:163], v[96:99]\n\t"
"ds_write_addtid_b32 v218 offset:7200\n\t"
"v_mfma_f32_16x16x32_bf16 v[112:115], a[80:83], a[144:147], v[112:115]\n\t"
"s_nop 0\n\t"
"ds_write_addtid_b32 v201 offset:12448\n\t"
"ds_write_addtid_b32 v208 offset:2048\n\t"
"v_mfma_f32_16x16x32_bf16 v[112:115], a[84:87], a[160:163], v[112:115]\n\t"
"buffer_load_dwordx4 a[4:7], off, s[96:99], s40 offset:1024 sc1\n\t"
"v_mfma_f32_16x16x32_bf16 v[128:131], a[88:91], a[144:147], v[128:131]\n\t"
"ds_read_b128 a[172:175], v224 offset:19360\n\t"
"v_mfma_f32_16x16x32_bf16 v[128:131], a[92:95], a[160:163], v[128:131]\n\t"
"buffer_load_dwordx4 a[40:43], off, s[92:95], s40 offset:2048 sc1\n\t"
"v_mfma_f32_16x16x32_bf16 v[132:135], a[88:91], a[148:151], v[132:135]\n\t"
"ds_read_b128 a[156:159], v224 offset:17312\n\t"
"v_mfma_f32_16x16x32_bf16 v[132:135], a[92:95], a[164:167], v[132:135]\n\t"
"ds_read_b64_tr_b16 a[196:197], v250 offset:4608\n\t"
"v_mfma_f32_16x16x32_bf16 v[116:119], a[80:83], a[148:151], v[116:119]\n\t"
"ds_read_b64_tr_b16 a[204:205], v251 offset:4608\n\t"
"v_mfma_f32_16x16x32_bf16 v[116:119], a[84:87], a[164:167], v[116:119]\n\t"
"buffer_load_dwordx4 a[44:47], off, s[92:95], s40 offset:3072 sc1\n\t"
"v_mfma_f32_16x16x32_bf16 v[100:103], a[72:75], a[148:151], v[100:103]\n\t"
"s_waitcnt lgkmcnt(2)\n\t"
"v_mfma_f32_16x16x32_bf16 v[100:103], a[76:79], a[164:167], v[100:103]\n\t"
"buffer_load_dwordx4 a[36:39], off, s[92:95], s40 offset:1024 sc1\n\t"
"s_barrier\n\t"
"v_mfma_f32_16x16x32_bf16 v[104:107], a[72:75], a[152:155], v[104:107]\n\t"
"ds_read_b128 a[144:147], v224 offset:1024\n\t"
"v_mfma_f32_16x16x32_bf16 v[104:107], a[76:79], a[168:171], v[104:107]\n\t"
"ds_read_b128 a[160:163], v224 offset:3072\n\t"
"v_mfma_f32_16x16x32_bf16 v[120:123], a[80:83], a[152:155], v[120:123]\n\t"
"ds_read_b128 a[148:151], v224 offset:1280\n\t"
"v_mfma_f32_16x16x32_bf16 v[120:123], a[84:87], a[168:171], v[120:123]\n\t"
"ds_read_b128 a[164:167], v224 offset:3328\n\t"
"v_mfma_f32_16x16x32_bf16 v[136:139], a[88:91], a[152:155], v[136:139]\n\t"
"v_mfma_f32_16x16x32_bf16 v[136:139], a[92:95], a[168:171], v[136:139]\n\t"
"buffer_load_dwordx4 a[32:35], off, s[92:95], s40 offset:0 sc1\n\t"
"v_mfma_f32_16x16x32_bf16 v[140:143], a[88:91], a[156:159], v[140:143]\n\t"
"ds_read_b128 a[168:171], v224 offset:3584\n\t"
"v_mfma_f32_16x16x32_bf16 v[140:143], a[92:95], a[172:175], v[140:143]\n\t"
"ds_read_b128 a[152:155], v224 offset:1536\n\t"
"v_mfma_f32_16x16x32_bf16 v[124:127], a[80:83], a[156:159], v[124:127]\n\t"
"v_mfma_f32_16x16x32_bf16 v[124:127], a[84:87], a[172:175], v[124:127]\n\t"
"buffer_load_dwordx4 a[24:27], off, s[96:99], 0 offset:2048 sc1\n\t"
"v_mfma_f32_16x16x32_bf16 v[108:111], a[72:75], a[156:159], v[108:111]\n\t"
"ds_read_b128 a[156:159], v224 offset:1792\n\t"
"v_mfma_f32_16x16x32_bf16 v[108:111], a[76:79], a[172:175], v[108:111]\n\t"
"s_waitcnt lgkmcnt(5)\n\t"
"ds_read_b128 a[172:175], v224 offset:3840\n\t"
"v_mfma_f32_4x4x4_16b_bf16 v[200:203], a[220:221], a[178:179], 0\n\t"
"v_mfma_f32_4x4x4_16b_bf16 v[204:207], a[220:221], a[186:187], 0\n\t"
"ds_read_b64_tr_b16 a[178:179], v250 offset:24192\n\t"
"v_mfma_f32_4x4x4_16b_bf16 v[216:219], a[220:221], a[194:195], 0\n\t"
"ds_read_b64_tr_b16 a[186:187], v251 offset:24192\n\t"
"v_mfma_f32_4x4x4_16b_bf16 v[220:223], a[220:221], a[202:203], 0\n\t"
"ds_read_b64_tr_b16 a[194:195], v248 offset:24192\n\t"
"v_mfma_f32_4x4x4_16b_bf16 v[192:195], a[222:223], a[176:177], v[200:203]\n\t"
"ds_read_b64_tr_b16 a[202:203], v249 offset:24192\n\t"
"v_mfma_f32_4x4x4_16b_bf16 v[196:199], a[222:223], a[184:185], v[204:207]\n\t"
"v_mfma_f32_4x4x4_16b_bf16 v[200:203], a[222:223], a[180:181], v[200:203]\n\t"
"ds_read_b64_tr_b16 a[176:177], v248 offset:21888\n\t"
"v_mfma_f32_4x4x4_16b_bf16 v[204:207], a[222:223], a[188:189], v[204:207]\n\t"
"ds_read_b64_tr_b16 a[184:185], v249 offset:21888\n\t"
"v_mfma_f32_4x4x4_16b_bf16 v[208:211], a[222:223], a[192:193], v[216:219]\n\t"
"ds_read_b64_tr_b16 a[180:181], v248 offset:26496\n\t"
"v_mfma_f32_4x4x4_16b_bf16 v[212:215], a[222:223], a[200:201], v[220:223]\n\t"
"ds_read_b64_tr_b16 a[188:189], v249 offset:26496\n\t"
"v_mfma_f32_4x4x4_16b_bf16 v[216:219], a[222:223], a[196:197], v[216:219]\n\t"
"ds_read_b64_tr_b16 a[192:193], v250 offset:21888\n\t"
"v_mfma_f32_4x4x4_16b_bf16 v[220:223], a[222:223], a[204:205], v[220:223]\n\t"
"s_waitcnt vmcnt(17) lgkmcnt(8)\n\t"
"buffer_load_dwordx4 a[28:31], off, s[96:99], 0 offset:3072 sc1\n\t"
"v_mfma_f32_16x16x32_bf16 v[48:51], a[96:99], a[144:147], v[48:51]\n\t"
"ds_read_b64_tr_b16 a[200:201], v251 offset:21888\n\t"
"v_cvt_pk_bf16_f32 v192, -v192, -v196\n\t"
"v_mfma_f32_16x16x32_bf16 v[48:51], a[100:103], a[160:163], v[48:51]\n\t"
"buffer_load_dwordx4 a[64:67], off, s[92:95], s68 offset:0 sc1\n\t"
"v_mfma_f32_16x16x32_bf16 v[64:67], a[104:107], a[144:147], v[64:67]\n\t"
"ds_read_b64_tr_b16 a[196:197], v250 offset:26496\n\t"
"v_cvt_pk_bf16_f32 v195, -v195, -v199\n\t"
"v_mfma_f32_16x16x32_bf16 v[64:67], a[108:111], a[160:163], v[64:67]\n\t"
"ds_write_addtid_b32 v192 offset:16544\n\t"
"v_cvt_pk_bf16_f32 v202, v202, v206\n\t"
"v_mfma_f32_16x16x32_bf16 v[80:83], a[112:115], a[144:147], v[80:83]\n\t"
"ds_read_b128 a[144:147], v224 offset:0\n\t"
"v_cvt_pk_bf16_f32 v193, -v193, -v197\n\t"
"v_mfma_f32_16x16x32_bf16 v[80:83], a[116:119], a[160:163], v[80:83]\n\t"
"buffer_load_dwordx4 a[68:71], off, s[92:95], s68 offset:1024 sc1\n\t"
"v_mfma_f32_16x16x32_bf16 v[84:87], a[112:115], a[148:151], v[84:87]\n\t"
"ds_read_b128 a[160:163], v224 offset:2048\n\t"
"v_cvt_pk_bf16_f32 v203, v203, v207\n\t"
"v_mfma_f32_16x16x32_bf16 v[84:87], a[116:119], a[164:167], v[84:87]\n\t"
"ds_write_addtid_b32 v202 offset:21696\n\t"
"v_cvt_pk_bf16_f32 v200, v200, v204\n\t"
"v_mfma_f32_16x16x32_bf16 v[68:71], a[104:107], a[148:151], v[68:71]\n\t"
"ds_write_addtid_b32 v200 offset:20672\n\t"
"v_mfma_f32_16x16x32_bf16 v[68:71], a[108:111], a[164:167], v[68:71]\n\t"
"buffer_load_dwordx4 a[60:63], off, s[96:99], s67 offset:3072 sc1\n\t"
"v_mfma_f32_16x16x32_bf16 v[52:55], a[96:99], a[148:151], v[52:55]\n\t"
"ds_read_b128 a[148:151], v224 offset:256\n\t"
"v_cvt_pk_bf16_f32 v217, v217, v221\n\t"
"v_mfma_f32_16x16x32_bf16 v[52:55], a[100:103], a[164:167], v[52:55]\n\t"
"ds_write_addtid_b32 v217 offset:31040\n\t"
"v_cvt_pk_bf16_f32 v216, v216, v220\n\t"
"v_mfma_f32_16x16x32_bf16 v[56:59], a[96:99], a[152:155], v[56:59]\n\t"
"ds_read_b128 a[164:167], v224 offset:2304\n\t"
"v_cvt_pk_bf16_f32 v211, -v211, -v215\n\t"
"v_mfma_f32_16x16x32_bf16 v[56:59], a[100:103], a[168:171], v[56:59]\n\t"
"buffer_load_dwordx4 a[56:59], off, s[96:99], s67 offset:2048 sc1\n\t"
"v_mfma_f32_16x16x32_bf16 v[72:75], a[104:107], a[152:155], v[72:75]\n\t"
"ds_write_addtid_b32 v211 offset:27936\n\t"
"v_cvt_pk_bf16_f32 v208, -v208, -v212\n\t"
"v_mfma_f32_16x16x32_bf16 v[72:75], a[108:111], a[168:171], v[72:75]\n\t"
"ds_write_addtid_b32 v216 offset:22720\n\t"
"v_cvt_pk_bf16_f32 v218, v218, v222\n\t"
"v_mfma_f32_16x16x32_bf16 v[88:91], a[112:115], a[152:155], v[88:91]\n\t"
"ds_read_b128 a[152:155], v224 offset:512\n\t"
"v_cvt_pk_bf16_f32 v209, -v209, -v213\n\t"
"v_mfma_f32_16x16x32_bf16 v[88:91], a[116:119], a[168:171], v[88:91]\n\t"
"buffer_load_dwordx4 a[48:51], off, s[96:99], s67 offset:0 sc1\n\t"
"v_mfma_f32_16x16x32_bf16 v[92:95], a[112:115], a[156:159], v[92:95]\n\t"
"ds_read_b128 a[168:171], v224 offset:2560\n\t"
"v_cvt_pk_bf16_f32 v201, v201, v205\n\t"
"v_mfma_f32_16x16x32_bf16 v[92:95], a[116:119], a[172:175], v[92:95]\n\t"
"ds_write_addtid_b32 v209 offset:26912\n\t"
"v_cvt_pk_bf16_f32 v210, -v210, -v214\n\t"
"v_mfma_f32_16x16x32_bf16 v[76:79], a[104:107], a[156:159], v[76:79]\n\t"
"s_nop 0\n\t"
"ds_write_addtid_b32 v193 offset:24864\n\t"
"ds_write_addtid_b32 v195 offset:25888\n\t"
"v_mfma_f32_16x16x32_bf16 v[76:79], a[108:111], a[172:175], v[76:79]\n\t"
"buffer_load_dwordx4 a[52:55], off, s[96:99], s67 offset:1024 sc1\n\t"
"v_mfma_f32_16x16x32_bf16 v[60:63], a[96:99], a[156:159], v[60:63]\n\t"
"s_nop 0\n\t"
"ds_write_addtid_b32 v203 offset:30016\n\t"
"ds_write_addtid_b32 v210 offset:19616\n\t"
"v_mfma_f32_16x16x32_bf16 v[60:63], a[100:103], a[172:175], v[60:63]\n\t"
"s_waitcnt vmcnt(18) lgkmcnt(9)\n\t"
"ds_write_addtid_b32 v208 offset:18592\n\t"
"v_cvt_pk_bf16_f32 v219, v219, v223\n\t"
"v_mfma_f32_16x16x32_bf16 v[0:3], a[120:123], a[144:147], v[0:3]\n\t"
"ds_write_addtid_b32 v219 offset:32064\n\t"
"v_cvt_pk_bf16_f32 v194, -v194, -v198\n\t"
"v_mfma_f32_16x16x32_bf16 v[0:3], a[124:127], a[160:163], v[0:3]\n\t"
"ds_write_addtid_b32 v194 offset:17568\n\t"
"v_mfma_f32_16x16x32_bf16 v[16:19], a[128:131], a[144:147], v[16:19]\n\t"
"s_nop 0\n\t"
"ds_write_addtid_b32 v201 offset:28992\n\t"
"ds_write_addtid_b32 v218 offset:23744\n\t"
"v_mfma_f32_16x16x32_bf16 v[16:19], a[132:135], a[160:163], v[16:19]\n\t"
"buffer_load_dwordx4 a[88:91], off, s[92:95], s67 offset:2048 sc1\n\t"
"v_mfma_f32_16x16x32_bf16 v[32:35], a[136:139], a[144:147], v[32:35]\n\t"
"ds_read_b128 a[156:159], v224 offset:768\n\t"
"v_mfma_f32_16x16x32_bf16 v[32:35], a[140:143], a[160:163], v[32:35]\n\t"
"buffer_load_dwordx4 a[92:95], off, s[92:95], s67 offset:3072 sc1\n\t"
"v_mfma_f32_16x16x32_bf16 v[36:39], a[136:139], a[148:151], v[36:39]\n\t"
"ds_read_b128 a[172:175], v224 offset:2816\n\t"
"v_mfma_f32_16x16x32_bf16 v[36:39], a[140:143], a[164:167], v[36:39]\n\t"
"buffer_load_dwordx4 a[84:87], off, s[92:95], s67 offset:1024 sc1\n\t"
"v_mfma_f32_16x16x32_bf16 v[20:23], a[128:131], a[148:151], v[20:23]\n\t"
"ds_read_b64_tr_b16 a[204:205], v251 offset:26496\n\t"
"v_mfma_f32_16x16x32_bf16 v[20:23], a[132:135], a[164:167], v[20:23]\n\t"
"v_mfma_f32_16x16x32_bf16 v[4:7], a[120:123], a[148:151], v[4:7]\n\t"
"buffer_load_dwordx4 a[80:83], off, s[92:95], s67 offset:0 sc1\n\t"
"v_mfma_f32_16x16x32_bf16 v[4:7], a[124:127], a[164:167], v[4:7]\n\t"
"s_waitcnt lgkmcnt(0)\n\t"
"s_barrier\n\t"
"v_mfma_f32_16x16x32_bf16 v[8:11], a[120:123], a[152:155], v[8:11]\n\t"
"s_nop 0\n\t"
"ds_read_b128 a[144:147], v224 offset:17568\n\t"
"ds_read_b128 a[160:163], v224 offset:19616\n\t"
"v_mfma_f32_16x16x32_bf16 v[8:11], a[124:127], a[168:171], v[8:11]\n\t"
"buffer_load_dwordx4 a[72:75], off, s[96:99], s66 offset:2048 sc1\n\t"
"v_mfma_f32_16x16x32_bf16 v[24:27], a[128:131], a[152:155], v[24:27]\n\t"
"ds_read_b128 a[148:151], v224 offset:17824\n\t"
"v_mfma_f32_16x16x32_bf16 v[24:27], a[132:135], a[168:171], v[24:27]\n\t"
"ds_read_b128 a[164:167], v224 offset:19872\n\t"
"v_mfma_f32_16x16x32_bf16 v[40:43], a[136:139], a[152:155], v[40:43]\n\t"
"ds_read_b128 a[228:231], v254 offset:39168\n\t"
"v_mfma_f32_16x16x32_bf16 v[40:43], a[140:143], a[168:171], v[40:43]\n\t"
"buffer_load_dwordx4 a[76:79], off, s[96:99], s66 offset:3072 sc1\n\t"
"v_mfma_f32_16x16x32_bf16 v[44:47], a[136:139], a[156:159], v[44:47]\n\t"
"ds_read_b128 a[152:155], v224 offset:18080\n\t"
"v_mfma_f32_16x16x32_bf16 v[44:47], a[140:143], a[172:175], v[44:47]\n\t"
"buffer_load_dwordx4 a[112:115], off, s[92:95], s53 offset:0 sc1\n\t"
"v_mfma_f32_16x16x32_bf16 v[28:31], a[128:131], a[156:159], v[28:31]\n\t"
"ds_read_b128 a[168:171], v224 offset:20128\n\t"
"v_mfma_f32_16x16x32_bf16 v[28:31], a[132:135], a[172:175], v[28:31]\n\t"
"v_mfma_f32_16x16x32_bf16 v[12:15], a[120:123], a[156:159], v[12:15]\n\t"
"ds_read_b128 a[156:159], v224 offset:18336\n\t"
"v_mfma_f32_16x16x32_bf16 v[12:15], a[124:127], a[172:175], v[12:15]\n\t"
"buffer_load_dwordx4 a[116:119], off, s[92:95], s53 offset:1024 sc1\n\t"
"v_mfma_f32_4x4x4_16b_bf16 v[200:203], a[220:221], a[178:179], 0\n\t"
"ds_read_b64_tr_b16 a[178:179], v250 offset:3456\n\t"
"v_mfma_f32_4x4x4_16b_bf16 v[204:207], a[220:221], a[186:187], 0\n\t"
"ds_read_b64_tr_b16 a[186:187], v251 offset:3456\n\t"
"v_mfma_f32_4x4x4_16b_bf16 v[216:219], a[220:221], a[194:195], 0\n\t"
"ds_read_b64_tr_b16 a[194:195], v248 offset:3456\n\t"
"v_mfma_f32_4x4x4_16b_bf16 v[220:223], a[220:221], a[202:203], 0\n\t"
"buffer_load_dwordx4 a[108:111], off, s[96:99], s52 offset:3072 sc1\n\t"
"v_mfma_f32_4x4x4_16b_bf16 v[192:195], a[222:223], a[176:177], v[200:203]\n\t"
"s_waitcnt lgkmcnt(5)\n\t"
"ds_write_b128 v255, a[228:231] offset:20736\n\t"
"v_mfma_f32_4x4x4_16b_bf16 v[196:199], a[222:223], a[184:185], v[204:207]\n\t"
"v_mfma_f32_4x4x4_16b_bf16 v[200:203], a[222:223], a[180:181], v[200:203]\n\t"
"v_mfma_f32_4x4x4_16b_bf16 v[204:207], a[222:223], a[188:189], v[204:207]\n\t"
"v_mfma_f32_4x4x4_16b_bf16 v[208:211], a[222:223], a[192:193], v[216:219]\n\t"
"ds_read_b128 a[172:175], v224 offset:20384\n\t"
"v_mfma_f32_4x4x4_16b_bf16 v[212:215], a[222:223], a[200:201], v[220:223]\n\t"
"ds_read_b64_tr_b16 a[202:203], v249 offset:3456\n\t"
"v_mfma_f32_4x4x4_16b_bf16 v[216:219], a[222:223], a[196:197], v[216:219]\n\t"
"ds_read_b64_tr_b16 a[176:177], v248 offset:1152\n\t"
"v_mfma_f32_4x4x4_16b_bf16 v[220:223], a[222:223], a[204:205], v[220:223]\n\t"
"s_waitcnt vmcnt(21) lgkmcnt(5)\n\t"
"buffer_load_dwordx4 a[104:107], off, s[96:99], s52 offset:2048 sc1\n\t"
"v_mfma_f32_16x16x32_bf16 v[48:51], a[0:3], a[144:147], v[48:51]\n\t"
"s_nop 0\n\t"
"ds_read_b64_tr_b16 a[184:185], v249 offset:1152\n\t"
"v_cvt_pk_bf16_f32 v192, -v192, -v196\n\t"
"v_mfma_f32_16x16x32_bf16 v[48:51], a[4:7], a[160:163], v[48:51]\n\t"
"s_nop 0\n\t"
"ds_write_addtid_b32 v192 offset:0\n\t"
"v_cvt_pk_bf16_f32 v193, -v193, -v197\n\t"
"v_mfma_f32_16x16x32_bf16 v[64:67], a[8:11], a[144:147], v[64:67]\n\t"
"s_nop 0\n\t"
"ds_write_addtid_b32 v193 offset:8320\n\t"
"v_cvt_pk_bf16_f32 v195, -v195, -v199\n\t"
"v_mfma_f32_16x16x32_bf16 v[64:67], a[12:15], a[160:163], v[64:67]\n\t"
"buffer_load_dwordx4 a[96:99], off, s[96:99], s52 offset:0 sc1\n\t"
"v_mfma_f32_16x16x32_bf16 v[80:83], a[16:19], a[144:147], v[80:83]\n\t"
"s_nop 0\n\t"
"ds_read_b128 a[144:147], v224 offset:16544\n\t"
"v_cvt_pk_bf16_f32 v201, v201, v205\n\t"
"v_mfma_f32_16x16x32_bf16 v[80:83], a[20:23], a[160:163], v[80:83]\n\t"
"s_nop 0\n\t"
"ds_write_addtid_b32 v201 offset:12448\n\t"
"v_cvt_pk_bf16_f32 v194, -v194, -v198\n\t"
"v_mfma_f32_16x16x32_bf16 v[84:87], a[16:19], a[148:151], v[84:87]\n\t"
"s_nop 0\n\t"
"ds_read_b128 a[160:163], v224 offset:18592\n\t"
"v_cvt_pk_bf16_f32 v209, -v209, -v213\n\t"
"v_mfma_f32_16x16x32_bf16 v[84:87], a[20:23], a[164:167], v[84:87]\n\t"
"buffer_load_dwordx4 a[100:103], off, s[96:99], s52 offset:1024 sc1\n\t"
"v_mfma_f32_16x16x32_bf16 v[68:71], a[8:11], a[148:151], v[68:71]\n\t"
"s_nop 0\n\t"
"ds_write_addtid_b32 v195 offset:9344\n\t"
"v_cvt_pk_bf16_f32 v211, -v211, -v215\n\t"
"v_mfma_f32_16x16x32_bf16 v[68:71], a[12:15], a[164:167], v[68:71]\n\t"
"s_nop 0\n\t"
"ds_write_addtid_b32 v194 offset:1024\n\t"
"v_cvt_pk_bf16_f32 v210, -v210, -v214\n\t"
"v_mfma_f32_16x16x32_bf16 v[52:55], a[0:3], a[148:151], v[52:55]\n\t"
"s_nop 0\n\t"
"ds_read_b128 a[148:151], v224 offset:16800\n\t"
"v_cvt_pk_bf16_f32 v202, v202, v206\n\t"
"v_mfma_f32_16x16x32_bf16 v[52:55], a[4:7], a[164:167], v[52:55]\n\t"
"buffer_load_dwordx4 a[136:139], off, s[92:95], s52 offset:2048 sc1\n\t"
"v_mfma_f32_16x16x32_bf16 v[56:59], a[0:3], a[152:155], v[56:59]\n\t"
"s_nop 0\n\t"
"ds_read_b128 a[164:167], v224 offset:18848\n\t"
"v_cvt_pk_bf16_f32 v200, v200, v204\n\t"
"v_mfma_f32_16x16x32_bf16 v[56:59], a[4:7], a[168:171], v[56:59]\n\t"
"s_nop 0\n\t"
"ds_write_addtid_b32 v211 offset:11392\n\t"
"v_cvt_pk_bf16_f32 v219, v219, v223\n\t"
"v_mfma_f32_16x16x32_bf16 v[72:75], a[8:11], a[152:155], v[72:75]\n\t"
"s_nop 0\n\t"
"ds_write_addtid_b32 v210 offset:3072\n\t"
"v_cvt_pk_bf16_f32 v217, v217, v221\n\t"
"v_mfma_f32_16x16x32_bf16 v[72:75], a[12:15], a[168:171], v[72:75]\n\t"
"s_nop 0\n\t"
"ds_write_addtid_b32 v200 offset:4128\n\t"
"buffer_load_dwordx4 a[140:143], off, s[92:95], s52 offset:3072 sc1\n\t"
"v_mfma_f32_16x16x32_bf16 v[88:91], a[16:19], a[152:155], v[88:91]\n\t"
"s_nop 0\n\t"
"ds_write_addtid_b32 v202 offset:5152\n\t"
"v_cvt_pk_bf16_f32 v203, v203, v207\n\t"
"v_mfma_f32_16x16x32_bf16 v[88:91], a[20:23], a[168:171], v[88:91]\n\t"
"s_waitcnt lgkmcnt(8)\n\t"
"ds_write_addtid_b32 v209 offset:10368\n\t"
"ds_write_addtid_b32 v217 offset:14496\n\t"
"v_mfma_f32_16x16x32_bf16 v[92:95], a[16:19], a[156:159], v[92:95]\n\t"
"s_nop 0\n\t"
"ds_write_addtid_b32 v203 offset:13472\n\t"
"v_cvt_pk_bf16_f32 v216, v216, v220\n\t"
"v_mfma_f32_16x16x32_bf16 v[92:95], a[20:23], a[172:175], v[92:95]\n\t"
"s_nop 0\n\t"
"ds_write_addtid_b32 v219 offset:15520\n\t"
"v_cvt_pk_bf16_f32 v218, v218, v222\n\t"
"v_mfma_f32_16x16x32_bf16 v[76:79], a[8:11], a[156:159], v[76:79]\n\t"
"s_nop 0\n\t"
"ds_write_addtid_b32 v218 offset:7200\n\t"
"v_cvt_pk_bf16_f32 v208, -v208, -v212\n\t"
"v_mfma_f32_16x16x32_bf16 v[76:79], a[12:15], a[172:175], v[76:79]\n\t"
"s_nop 0\n\t"
"ds_write_addtid_b32 v208 offset:2048\n\t"
"ds_write_addtid_b32 v216 offset:6176\n\t"
"v_mfma_f32_16x16x32_bf16 v[60:63], a[0:3], a[156:159], v[60:63]\n\t"
"s_add_u32 m0, s26, 124544\n\t"
"ds_read_b128 a[152:155], v224 offset:17056\n\t"
"v_mfma_f32_16x16x32_bf16 v[60:63], a[4:7], a[172:175], v[60:63]\n\t"
"s_waitcnt vmcnt(20) lgkmcnt(11)\n\t"
"buffer_load_dwordx4 v236, s[12:15], s22 offen offset:128 nt lds\n\t"
"v_mfma_f32_16x16x32_bf16 v[0:3], a[24:27], a[144:147], v[0:3]\n\t"
"s_nop 0\n\t"
"ds_read_b128 a[156:159], v224 offset:17312\n\t"
"ds_read_b128 a[172:175], v224 offset:19360\n\t"
"v_mfma_f32_16x16x32_bf16 v[0:3], a[28:31], a[160:163], v[0:3]\n\t"
"buffer_load_dwordx4 v237, s[12:15], s22 offen offset:1152 nt lds\n\t"
"v_mfma_f32_16x16x32_bf16 v[16:19], a[32:35], a[144:147], v[16:19]\n\t"
"s_nop 0\n\t"
"ds_read_b128 a[168:171], v224 offset:19104\n\t"
"v_mfma_f32_16x16x32_bf16 v[16:19], a[36:39], a[160:163], v[16:19]\n\t"
"buffer_load_dwordx4 v238, s[12:15], s22 offen offset:2176 nt lds\n\t"
"v_mfma_f32_16x16x32_bf16 v[32:35], a[40:43], a[144:147], v[32:35]\n\t"
"s_nop 0\n\t"
"ds_read_b64_tr_b16 a[180:181], v248 offset:5760\n\t"
"ds_read_b64_tr_b16 a[188:189], v249 offset:5760\n\t"
"v_mfma_f32_16x16x32_bf16 v[32:35], a[44:47], a[160:163], v[32:35]\n\t"
"buffer_load_dwordx4 v239, s[12:15], s22 offen offset:3200 nt lds\n\t"
"v_mfma_f32_16x16x32_bf16 v[36:39], a[40:43], a[148:151], v[36:39]\n\t"
"s_nop 0\n\t"
"ds_read_b64_tr_b16 a[192:193], v250 offset:1152\n\t"
"ds_read_b64_tr_b16 a[200:201], v251 offset:1152\n\t"
"v_mfma_f32_16x16x32_bf16 v[36:39], a[44:47], a[164:167], v[36:39]\n\t"
"buffer_load_dwordx4 a[132:135], off, s[92:95], s52 offset:1024 sc1\n\t"
"v_mfma_f32_16x16x32_bf16 v[20:23], a[32:35], a[148:151], v[20:23]\n\t"
"s_nop 0\n\t"
"ds_read_b64_tr_b16 a[196:197], v250 offset:5760\n\t"
"ds_read_b64_tr_b16 a[204:205], v251 offset:5760\n\t"
"v_mfma_f32_16x16x32_bf16 v[20:23], a[36:39], a[164:167], v[20:23]\n\t"
"s_mov_b64 exec, s[36:37]\n\t"
"buffer_load_dwordx4 v240, s[12:15], s22 offen offset:3712 nt lds\n\t"
"s_mov_b64 exec, -1\n\t"
"v_mfma_f32_16x16x32_bf16 v[4:7], a[24:27], a[148:151], v[4:7]\n\t"
"Lpad5_%=:\n\t"
"s_cbranch_execz Lret0_%=\n\t"
"s_lshl_b32 m0, s73, 8\n\t"
"v_mfma_f32_16x16x32_bf16 v[4:7], a[28:31], a[164:167], v[4:7]\n\t"
"s_waitcnt lgkmcnt(0)\n\t"
"s_barrier\n\t"
"v_mfma_f32_16x16x32_bf16 v[8:11], a[24:27], a[152:155], v[8:11]\n\t"
"ds_read_b128 a[144:147], v224 offset:1024\n\t"
"v_add_u32 v248, 33280, v227\n\t"
"v_mfma_f32_16x16x32_bf16 v[8:11], a[28:31], a[168:171], v[8:11]\n\t"
"ds_read_b128 a[160:163], v224 offset:3072\n\t"
"v_xor_b32 v249, 32, v248\n\t"
"v_mfma_f32_16x16x32_bf16 v[24:27], a[32:35], a[152:155], v[24:27]\n\t"
"ds_read_b128 a[148:151], v224 offset:1280\n\t"
"v_xor_b32 v250, 64, v248\n\t"
"v_mfma_f32_16x16x32_bf16 v[24:27], a[36:39], a[168:171], v[24:27]\n\t"
"ds_read_b128 a[164:167], v224 offset:3328\n\t"
"v_xor_b32 v251, 96, v248\n\t"
"v_mfma_f32_16x16x32_bf16 v[40:43], a[40:43], a[152:155], v[40:43]\n\t"
"ds_read_b64_tr_b16 a[182:183], v249 offset:41984\n\t"
"v_mfma_f32_16x16x32_bf16 v[40:43], a[44:47], a[168:171], v[40:43]\n\t"
"ds_read_b128 a[152:155], v224 offset:1536\n\t"
"v_mfma_f32_16x16x32_bf16 v[44:47], a[40:43], a[156:159], v[44:47]\n\t"
"ds_read_b128 a[168:171], v224 offset:3584\n\t"
"v_mfma_f32_16x16x32_bf16 v[44:47], a[44:47], a[172:175], v[44:47]\n\t"
"ds_read_b64_tr_b16 a[190:191], v251 offset:41984\n\t"
"v_mfma_f32_16x16x32_bf16 v[28:31], a[32:35], a[156:159], v[28:31]\n\t"
"ds_read_b64_tr_b16 a[198:199], v249 offset:21248\n\t"
"v_mfma_f32_16x16x32_bf16 v[28:31], a[36:39], a[172:175], v[28:31]\n\t"
"ds_read_b64_tr_b16 a[206:207], v251 offset:21248\n\t"
"v_mfma_f32_16x16x32_bf16 v[12:15], a[24:27], a[156:159], v[12:15]\n\t"
"ds_read_b128 a[156:159], v224 offset:1792\n\t"
"v_mfma_f32_16x16x32_bf16 v[12:15], a[28:31], a[172:175], v[12:15]\n\t"
"ds_read_b128 a[172:175], v224 offset:3840\n\t"
"v_mfma_f32_4x4x4_16b_bf16 v[200:203], a[220:221], a[178:179], 0\n\t"
"ds_read_b64_tr_b16 a[178:179], v248 offset:41984\n\t"
"v_mfma_f32_4x4x4_16b_bf16 v[204:207], a[220:221], a[186:187], 0\n\t"
"ds_read_b64_tr_b16 a[186:187], v250 offset:41984\n\t"
"v_mfma_f32_4x4x4_16b_bf16 v[216:219], a[220:221], a[194:195], 0\n\t"
"ds_read_b64_tr_b16 a[194:195], v248 offset:21248\n\t"
"v_mfma_f32_4x4x4_16b_bf16 v[220:223], a[220:221], a[202:203], 0\n\t"
"ds_read_b64_tr_b16 a[202:203], v250 offset:21248\n\t"
"v_mfma_f32_4x4x4_16b_bf16 v[192:195], a[222:223], a[176:177], v[200:203]\n\t"
"ds_read_b64_tr_b16 a[176:177], v248 offset:41472\n\t"
"v_mfma_f32_4x4x4_16b_bf16 v[196:199], a[222:223], a[184:185], v[204:207]\n\t"
".p2align 3\n\t"
"ds_read_b64_tr_b16 a[184:185], v250 offset:41472\n\t"
"v_pk_add_f32 v[0:1], v[0:1], v[48:49]\n\t"
"v_pk_add_f32 v[2:3], v[2:3], v[50:51]\n\t"
"v_pk_fma_f32 v[48:49], v[48:49], s[38:39], v[0:1]\n\t"
"v_pk_fma_f32 v[50:51], v[50:51], s[38:39], v[2:3]\n\t"
"v_pk_add_f32 v[16:17], v[16:17], v[64:65]\n\t"
"v_pk_add_f32 v[18:19], v[18:19], v[66:67]\n\t"
"v_pk_fma_f32 v[64:65], v[64:65], s[38:39], v[16:17]\n\t"
"v_pk_fma_f32 v[66:67], v[66:67], s[38:39], v[18:19]\n\t"
"v_pk_add_f32 v[32:33], v[32:33], v[80:81]\n\t"
"v_pk_add_f32 v[34:35], v[34:35], v[82:83]\n\t"
"v_pk_fma_f32 v[80:81], v[80:81], s[38:39], v[32:33]\n\t"
"v_pk_fma_f32 v[82:83], v[82:83], s[38:39], v[34:35]\n\t"
"v_pk_add_f32 v[36:37], v[36:37], v[84:85]\n\t"
"v_pk_add_f32 v[38:39], v[38:39], v[86:87]\n\t"
"v_pk_fma_f32 v[84:85], v[84:85], s[38:39], v[36:37]\n\t"
"v_pk_fma_f32 v[86:87], v[86:87], s[38:39], v[38:39]\n\t"
"v_pk_add_f32 v[20:21], v[20:21], v[68:69]\n\t"
"v_pk_add_f32 v[22:23], v[22:23], v[70:71]\n\t"
"v_pk_fma_f32 v[68:69], v[68:69], s[38:39], v[20:21]\n\t"
"v_pk_fma_f32 v[70:71], v[70:71], s[38:39], v[22:23]\n\t"
"v_pk_add_f32 v[4:5], v[4:5], v[52:53]\n\t"
"v_pk_add_f32 v[6:7], v[6:7], v[54:55]\n\t"
"v_pk_fma_f32 v[52:53], v[52:53], s[38:39], v[4:5]\n\t"
"v_pk_fma_f32 v[54:55], v[54:55], s[38:39], v[6:7]\n\t"
"v_pk_add_f32 v[8:9], v[8:9], v[56:57]\n\t"
"v_pk_add_f32 v[10:11], v[10:11], v[58:59]\n\t"
"v_pk_fma_f32 v[56:57], v[56:57], s[38:39], v[8:9]\n\t"
"v_pk_fma_f32 v[58:59], v[58:59], s[38:39], v[10:11]\n\t"
"v_pk_add_f32 v[24:25], v[24:25], v[72:73]\n\t"
"v_pk_add_f32 v[26:27], v[26:27], v[74:75]\n\t"
"v_pk_fma_f32 v[72:73], v[72:73], s[38:39], v[24:25]\n\t"
"v_pk_fma_f32 v[74:75], v[74:75], s[38:39], v[26:27]\n\t"
"v_pk_add_f32 v[40:41], v[40:41], v[88:89]\n\t"
"v_pk_add_f32 v[42:43], v[42:43], v[90:91]\n\t"
"v_pk_fma_f32 v[88:89], v[88:89], s[38:39], v[40:41]\n\t"
"v_pk_fma_f32 v[90:91], v[90:91], s[38:39], v[42:43]\n\t"
"v_pk_add_f32 v[44:45], v[44:45], v[92:93]\n\t"
"v_pk_add_f32 v[46:47], v[46:47], v[94:95]\n\t"
"v_pk_fma_f32 v[92:93], v[92:93], s[38:39], v[44:45]\n\t"
"v_pk_fma_f32 v[94:95], v[94:95], s[38:39], v[46:47]\n\t"
"v_pk_add_f32 v[28:29], v[28:29], v[76:77]\n\t"
"v_pk_add_f32 v[30:31], v[30:31], v[78:79]\n\t"
"v_pk_fma_f32 v[76:77], v[76:77], s[38:39], v[28:29]\n\t"
"v_pk_fma_f32 v[78:79], v[78:79], s[38:39], v[30:31]\n\t"
"v_pk_add_f32 v[12:13], v[12:13], v[60:61]\n\t"
"v_pk_add_f32 v[14:15], v[14:15], v[62:63]\n\t"
"v_pk_fma_f32 v[60:61], v[60:61], s[38:39], v[12:13]\n\t"
"v_pk_fma_f32 v[62:63], v[62:63], s[38:39], v[14:15]\n\t"
"v_cvt_pk_bf16_f32 v4, v4, v5\n\t"
"v_mfma_f32_4x4x4_16b_bf16 v[200:203], a[222:223], a[180:181], v[200:203]\n\t"
"ds_read_b64_tr_b16 a[180:181], v249 offset:41472\n\t"
"v_mfma_f32_4x4x4_16b_bf16 v[204:207], a[222:223], a[188:189], v[204:207]\n\t"
"ds_read_b64_tr_b16 a[188:189], v251 offset:41472\n\t"
"v_mfma_f32_4x4x4_16b_bf16 v[208:211], a[222:223], a[192:193], v[216:219]\n\t"
"v_cvt_pk_bf16_f32 v0, v0, v1\n\t"
"v_cvt_pk_bf16_f32 v1, v2, v3\n\t"
"v_cvt_pk_bf16_f32 v5, v6, v7\n\t"
"v_cvt_pk_bf16_f32 v24, v24, v25\n\t"
"v_mfma_f32_4x4x4_16b_bf16 v[212:215], a[222:223], a[200:201], v[220:223]\n\t"
"ds_write2st64_b64 v228, v[0:1], v[4:5] offset0:40 offset1:53\n\t"
"v_mfma_f32_4x4x4_16b_bf16 v[216:219], a[222:223], a[196:197], v[216:219]\n\t"
"v_cvt_pk_bf16_f32 v16, v16, v17\n\t"
"v_mfma_f32_4x4x4_16b_bf16 v[220:223], a[222:223], a[204:205], v[220:223]\n\t"
"s_waitcnt vmcnt(20) lgkmcnt(7)\n\t"
"v_cvt_pk_bf16_f32 v17, v18, v19\n\t"
"v_cvt_pk_bf16_f32 v20, v20, v21\n\t"
"v_mfma_f32_16x16x32_bf16 v[144:147], a[48:51], a[144:147], v[144:147]\n\t"
"v_cvt_pk_bf16_f32 v21, v22, v23\n\t"
"v_cvt_pk_bf16_f32 v28, v28, v29\n\t"
"v_mfma_f32_16x16x32_bf16 v[144:147], a[52:55], a[160:163], v[144:147]\n\t"
"ds_write2st64_b64 v246, v[16:17], v[20:21] offset0:40 offset1:53\n\t"
"v_mfma_f32_16x16x32_bf16 v[160:163], a[56:59], a[144:147], v[160:163]\n\t"
"v_cvt_pk_bf16_f32 v32, v32, v33\n\t"
"v_cvt_pk_bf16_f32 v29, v30, v31\n\t"
"v_mfma_f32_16x16x32_bf16 v[160:163], a[60:63], a[160:163], v[160:163]\n\t"
"v_cvt_pk_bf16_f32 v33, v34, v35\n\t"
"v_cvt_pk_bf16_f32 v25, v26, v27\n\t"
"v_mfma_f32_16x16x32_bf16 v[176:179], a[64:67], a[144:147], v[176:179]\n\t"
"v_cvt_pk_bf16_f32 v195, -v195, -v199\n\t"
"v_cvt_pk_bf16_f32 v12, v12, v13\n\t"
"v_mfma_f32_16x16x32_bf16 v[176:179], a[68:71], a[160:163], v[176:179]\n\t"
"s_nop 0\n\t"
"ds_read_b128 a[144:147], v224 offset:0\n\t"
"ds_read_b128 a[160:163], v224 offset:2048\n\t"
"ds_write2st64_b64 v246, v[24:25], v[28:29] offset0:66 offset1:79\n\t"
"v_cvt_pk_bf16_f32 v40, v40, v41\n\t"
"v_mfma_f32_16x16x32_bf16 v[180:183], a[64:67], a[148:151], v[180:183]\n\t"
"v_cvt_pk_bf16_f32 v208, -v208, -v212\n\t"
"v_cvt_pk_bf16_f32 v36, v36, v37\n\t"
"v_mfma_f32_16x16x32_bf16 v[180:183], a[68:71], a[164:167], v[180:183]\n\t"
"v_cvt_pk_bf16_f32 v216, v216, v220\n\t"
"v_cvt_pk_bf16_f32 v37, v38, v39\n\t"
"v_mfma_f32_16x16x32_bf16 v[164:167], a[56:59], a[148:151], v[164:167]\n\t"
"v_cvt_pk_bf16_f32 v8, v8, v9\n\t"
"v_cvt_pk_bf16_f32 v44, v44, v45\n\t"
"v_mfma_f32_16x16x32_bf16 v[164:167], a[60:63], a[164:167], v[164:167]\n\t"
"ds_write_addtid_b32 v195 offset:25888\n\t"
"ds_write2st64_b64 v247, v[32:33], v[36:37] offset0:40 offset1:53\n\t"
"v_mfma_f32_16x16x32_bf16 v[148:151], a[48:51], a[148:151], v[148:151]\n\t"
"v_cvt_pk_bf16_f32 v9, v10, v11\n\t"
"v_cvt_pk_bf16_f32 v45, v46, v47\n\t"
"v_mfma_f32_16x16x32_bf16 v[148:151], a[52:55], a[164:167], v[148:151]\n\t"
"buffer_load_dwordx4 a[128:131], off, s[92:95], s52 offset:0 sc1\n\t"
"v_mfma_f32_16x16x32_bf16 v[152:155], a[48:51], a[152:155], v[152:155]\n\t"
"v_cvt_pk_bf16_f32 v202, v202, v206\n\t"
"v_cvt_pk_bf16_f32 v13, v14, v15\n\t"
"v_mfma_f32_16x16x32_bf16 v[152:155], a[52:55], a[168:171], v[152:155]\n\t"
"s_nop 0\n\t"
"ds_read_b128 a[148:151], v224 offset:256\n\t"
"ds_read_b128 a[164:167], v224 offset:2304\n\t"
"ds_write2st64_b64 v228, v[8:9], v[12:13] offset0:66 offset1:79\n\t"
"v_mfma_f32_16x16x32_bf16 v[168:171], a[56:59], a[152:155], v[168:171]\n\t"
"v_cvt_pk_bf16_f32 v217, v217, v221\n\t"
"v_cvt_pk_bf16_f32 v41, v42, v43\n\t"
"v_mfma_f32_16x16x32_bf16 v[168:171], a[60:63], a[168:171], v[168:171]\n\t"
"v_cvt_pk_bf16_f32 v193, -v193, -v197\n\t"
"v_cvt_pk_bf16_f32 v200, v200, v204\n\t"
"v_mfma_f32_16x16x32_bf16 v[184:187], a[64:67], a[152:155], v[184:187]\n\t"
"v_cvt_pk_bf16_f32 v210, -v210, -v214\n\t"
"v_cvt_pk_bf16_f32 v219, v219, v223\n\t"
"v_mfma_f32_16x16x32_bf16 v[184:187], a[68:71], a[168:171], v[184:187]\n\t"
"ds_write_addtid_b32 v202 offset:21696\n\t"
"ds_write2st64_b64 v247, v[40:41], v[44:45] offset0:66 offset1:79\n\t"
"v_mfma_f32_16x16x32_bf16 v[188:191], a[64:67], a[156:159], v[188:191]\n\t"
"v_cvt_pk_bf16_f32 v201, v201, v205\n\t"
"v_cvt_pk_bf16_f32 v218, v218, v222\n\t"
"v_mfma_f32_16x16x32_bf16 v[188:191], a[68:71], a[172:175], v[188:191]\n\t"
"v_cvt_pk_bf16_f32 v194, -v194, -v198\n\t"
"v_cvt_pk_bf16_f32 v203, v203, v207\n\t"
"v_mfma_f32_16x16x32_bf16 v[172:175], a[56:59], a[156:159], v[172:175]\n\t"
"v_cvt_pk_bf16_f32 v209, -v209, -v213\n\t"
"v_cvt_pk_bf16_f32 v211, -v211, -v215\n\t"
"v_mfma_f32_16x16x32_bf16 v[172:175], a[60:63], a[172:175], v[172:175]\n\t"
"s_nop 0\n\t"
"ds_write_addtid_b32 v193 offset:24864\n\t"
"ds_write_addtid_b32 v201 offset:28992\n\t"
"ds_write_addtid_b32 v203 offset:30016\n\t"
"ds_write_addtid_b32 v209 offset:26912\n\t"
"ds_write_addtid_b32 v210 offset:19616\n\t"
"v_mfma_f32_16x16x32_bf16 v[156:159], a[48:51], a[156:159], v[156:159]\n\t"
"s_nop 0\n\t"
"ds_write_addtid_b32 v208 offset:18592\n\t"
"ds_write_addtid_b32 v216 offset:22720\n\t"
"v_mfma_f32_16x16x32_bf16 v[156:159], a[52:55], a[172:175], v[156:159]\n\t"
"s_waitcnt vmcnt(15) lgkmcnt(9)\n\t"
"ds_write_addtid_b32 v218 offset:23744\n\t"
"v_cvt_pk_bf16_f32 v192, -v192, -v196\n\t"
"v_mfma_f32_16x16x32_bf16 v[96:99], a[72:75], a[144:147], v[96:99]\n\t"
"s_nop 0\n\t"
"ds_write_addtid_b32 v217 offset:31040\n\t"
"ds_write_addtid_b32 v219 offset:32064\n\t"
"v_mfma_f32_16x16x32_bf16 v[96:99], a[76:79], a[160:163], v[96:99]\n\t"
"s_nop 0\n\t"
"ds_write_addtid_b32 v192 offset:16544\n\t"
"ds_write_addtid_b32 v200 offset:20672\n\t"
"v_mfma_f32_16x16x32_bf16 v[112:115], a[80:83], a[144:147], v[112:115]\n\t"
"s_nop 0\n\t"
"ds_write_addtid_b32 v194 offset:17568\n\t"
"ds_write_addtid_b32 v211 offset:27936\n\t"
"v_mfma_f32_16x16x32_bf16 v[112:115], a[84:87], a[160:163], v[112:115]\n\t"
"buffer_load_dwordx4 a[120:123], off, s[96:99], s51 offset:2048 sc1\n\t"
"v_mfma_f32_16x16x32_bf16 v[128:131], a[88:91], a[144:147], v[128:131]\n\t"
"s_nop 0\n\t"
"ds_read_b128 a[168:171], v224 offset:2560\n\t"
"ds_read_b128 a[156:159], v224 offset:768\n\t"
"v_mfma_f32_16x16x32_bf16 v[128:131], a[92:95], a[160:163], v[128:131]\n\t"
"buffer_load_dwordx4 a[124:127], off, s[96:99], s51 offset:3072 sc1\n\t"
"v_mfma_f32_16x16x32_bf16 v[132:135], a[88:91], a[148:151], v[132:135]\n\t"
"s_nop 0\n\t"
"ds_read_b128 a[152:155], v224 offset:512\n\t"
"ds_read_b128 a[172:175], v224 offset:2816\n\t"
"v_mfma_f32_16x16x32_bf16 v[132:135], a[92:95], a[164:167], v[132:135]\n\t"
"s_nop 0\n\t"
"ds_read_b64_tr_b16 a[192:193], v248 offset:20736\n\t"
"ds_read_b64_tr_b16 a[200:201], v250 offset:20736\n\t"
"v_mfma_f32_16x16x32_bf16 v[116:119], a[80:83], a[148:151], v[116:119]\n\t"
"s_nop 0\n\t"
"ds_read_b64_tr_b16 a[196:197], v249 offset:20736\n\t"
"ds_read_b64_tr_b16 a[204:205], v251 offset:20736\n\t"
"v_mfma_f32_16x16x32_bf16 v[116:119], a[84:87], a[164:167], v[116:119]\n\t"
"buffer_load_dwordx4 a[16:19], off, s[28:31], s69 offset:0 sc1\n\t"
"v_mfma_f32_16x16x32_bf16 v[100:103], a[72:75], a[148:151], v[100:103]\n\t"
"s_waitcnt lgkmcnt(4)\n\t"
"v_mfma_f32_16x16x32_bf16 v[100:103], a[76:79], a[164:167], v[100:103]\n\t"
"s_barrier\n\t"
"s_cmp_eq_u32 s21, 1\n\t"
"s_cbranch_scc1 Ltail_%=\n\t"
"v_mfma_f32_16x16x32_bf16 v[104:107], a[72:75], a[152:155], v[104:107]\n\t"
"s_nop 0\n\t"
"ds_read_b128 a[144:147], v224 offset:17568\n\t"
"ds_read_b128 a[160:163], v224 offset:19616\n\t"
"v_mfma_f32_16x16x32_bf16 v[104:107], a[76:79], a[168:171], v[104:107]\n\t"
"buffer_load_dwordx4 a[20:23], off, s[28:31], s69 offset:1024 sc1\n\t"
"v_mfma_f32_16x16x32_bf16 v[120:123], a[80:83], a[152:155], v[120:123]\n\t"
"ds_read_b128 a[148:151], v224 offset:17824\n\t"
"v_mfma_f32_16x16x32_bf16 v[120:123], a[84:87], a[168:171], v[120:123]\n\t"
"ds_read_b128 a[164:167], v224 offset:19872\n\t"
"v_mfma_f32_16x16x32_bf16 v[136:139], a[88:91], a[152:155], v[136:139]\n\t"
"ds_read_b128 a[152:155], v224 offset:18080\n\t"
"v_mfma_f32_16x16x32_bf16 v[136:139], a[92:95], a[168:171], v[136:139]\n\t"
"buffer_load_dwordx4 a[12:15], off, s[84:87], s69 offset:3072 sc1\n\t"
"v_mfma_f32_16x16x32_bf16 v[140:143], a[88:91], a[156:159], v[140:143]\n\t"
"ds_read_b128 a[168:171], v224 offset:20128\n\t"
"v_mfma_f32_16x16x32_bf16 v[140:143], a[92:95], a[172:175], v[140:143]\n\t"
"buffer_load_dwordx4 a[8:11], off, s[84:87], s69 offset:2048 sc1\n\t"
"v_mfma_f32_16x16x32_bf16 v[124:127], a[80:83], a[156:159], v[124:127]\n\t"
"ds_read_b128 v[8:11], v229 offset:20480\n\t"
"v_mfma_f32_16x16x32_bf16 v[124:127], a[84:87], a[172:175], v[124:127]\n\t"
"buffer_load_dwordx4 a[0:3], off, s[84:87], s69 offset:0 sc1\n\t"
"v_mfma_f32_16x16x32_bf16 v[108:111], a[72:75], a[156:159], v[108:111]\n\t"
"ds_read_b128 a[156:159], v224 offset:18336\n\t"
"v_mfma_f32_16x16x32_bf16 v[108:111], a[76:79], a[172:175], v[108:111]\n\t"
"ds_read_b128 a[172:175], v224 offset:20384\n\t"
"v_mfma_f32_16x16x32_bf16 v[192:195], a[224:227], a[176:179], 0\n\t"
"s_nop 0\n\t"
"ds_read_b64_tr_b16 a[176:177], v248 offset:0\n\t"
"ds_read_b64_tr_b16 a[178:179], v248 offset:512\n\t"
"v_mfma_f32_16x16x32_bf16 v[196:199], a[224:227], a[180:183], 0\n\t"
"s_nop 0\n\t"
"ds_read_b64_tr_b16 a[180:181], v249 offset:0\n\t"
"ds_read_b64_tr_b16 a[182:183], v249 offset:512\n\t"
"v_mfma_f32_16x16x32_bf16 v[200:203], a[224:227], a[184:187], 0\n\t"
"buffer_load_dwordx4 a[4:7], off, s[84:87], s69 offset:1024 sc1\n\t"
"v_mfma_f32_16x16x32_bf16 v[204:207], a[224:227], a[188:191], 0\n\t"
"s_waitcnt lgkmcnt(8)\n\t"
"ds_read_b64_tr_b16 a[184:185], v250 offset:0\n\t"
"v_mfma_f32_16x16x32_bf16 v[208:211], a[224:227], a[192:195], 0\n\t"
"s_nop 0\n\t"
"ds_read_b64_tr_b16 a[186:187], v250 offset:512\n\t"
"ds_read_b64_tr_b16 a[188:189], v251 offset:0\n\t"
"v_mfma_f32_16x16x32_bf16 v[212:215], a[224:227], a[196:199], 0\n\t"
"s_nop 0\n\t"
"ds_read_b64_tr_b16 a[190:191], v251 offset:512\n\t"
"ds_read_b64_tr_b16 a[192:193], v248 offset:1152\n\t"
"v_mfma_f32_16x16x32_bf16 v[216:219], a[224:227], a[200:203], 0\n\t"
"s_waitcnt lgkmcnt(11)\n\t"
"buffer_store_dwordx4 v[8:11], v232, s[16:19], s24 offen nt\n\t"
"v_mfma_f32_16x16x32_bf16 v[220:223], a[224:227], a[204:207], 0\n\t"
"s_waitcnt vmcnt(18)\n\t"
"ds_read_b64_tr_b16 a[194:195], v248 offset:1664\n\t"
"v_cvt_pk_bf16_f32 v195, v195, v199\n\t"
"v_mfma_f32_16x16x32_bf16 v[144:147], a[96:99], a[144:147], v[144:147]\n\t"
"ds_read_b64_tr_b16 a[196:197], v249 offset:1152\n\t"
"v_cvt_pk_bf16_f32 v194, v194, v198\n\t"
"v_mfma_f32_16x16x32_bf16 v[144:147], a[100:103], a[160:163], v[144:147]\n\t"
"buffer_load_dwordx4 a[40:43], off, s[28:31], s54 offset:0 sc1\n\t"
"v_mfma_f32_16x16x32_bf16 v[160:163], a[104:107], a[144:147], v[160:163]\n\t"
"s_nop 0\n\t"
"ds_write_addtid_b32 v195 offset:12448\n\t"
"ds_read_b64_tr_b16 a[198:199], v249 offset:1664\n\t"
"v_mfma_f32_16x16x32_bf16 v[160:163], a[108:111], a[160:163], v[160:163]\n\t"
"ds_write_addtid_b32 v194 offset:4128\n\t"
"v_cvt_pk_bf16_f32 v201, v201, v205\n\t"
"v_mfma_f32_16x16x32_bf16 v[176:179], a[112:115], a[144:147], v[176:179]\n\t"
"ds_read_b128 a[144:147], v224 offset:16544\n\t"
"v_cvt_pk_bf16_f32 v211, v211, v215\n\t"
"v_mfma_f32_16x16x32_bf16 v[176:179], a[116:119], a[160:163], v[176:179]\n\t"
"buffer_load_dwordx4 a[44:47], off, s[28:31], s54 offset:1024 sc1\n\t"
"v_mfma_f32_16x16x32_bf16 v[180:183], a[112:115], a[148:151], v[180:183]\n\t"
"ds_read_b128 a[160:163], v224 offset:18592\n\t"
"v_cvt_pk_bf16_f32 v208, v208, v212\n\t"
"v_mfma_f32_16x16x32_bf16 v[180:183], a[116:119], a[164:167], v[180:183]\n\t"
"ds_read_b128 v[24:27], v230 offset:20480\n\t"
"v_cvt_pk_bf16_f32 v202, v202, v206\n\t"
"v_mfma_f32_16x16x32_bf16 v[164:167], a[104:107], a[148:151], v[164:167]\n\t"
"ds_read_b128 v[12:15], v229 offset:33792\n\t"
"v_cvt_pk_bf16_f32 v200, v200, v204\n\t"
"v_mfma_f32_16x16x32_bf16 v[164:167], a[108:111], a[164:167], v[164:167]\n\t"
"buffer_load_dwordx4 a[36:39], off, s[84:87], s54 offset:3072 sc1\n\t"
"v_mfma_f32_16x16x32_bf16 v[148:151], a[96:99], a[148:151], v[148:151]\n\t"
"ds_read_b128 v[28:31], v230 offset:33792\n\t"
"v_cvt_pk_bf16_f32 v216, v216, v220\n\t"
"v_mfma_f32_16x16x32_bf16 v[148:151], a[100:103], a[164:167], v[148:151]\n\t"
"ds_read_b128 a[164:167], v224 offset:18848\n\t"
"v_cvt_pk_bf16_f32 v209, v209, v213\n\t"
"v_mfma_f32_16x16x32_bf16 v[152:155], a[96:99], a[152:155], v[152:155]\n\t"
"ds_read_b128 a[148:151], v224 offset:16800\n\t"
"v_cvt_pk_bf16_f32 v218, v218, v222\n\t"
"v_mfma_f32_16x16x32_bf16 v[152:155], a[100:103], a[168:171], v[152:155]\n\t"
"buffer_load_dwordx4 a[32:35], off, s[84:87], s54 offset:2048 sc1\n\t"
"v_mfma_f32_16x16x32_bf16 v[168:171], a[104:107], a[152:155], v[168:171]\n\t"
"ds_write_addtid_b32 v202 offset:6176\n\t"
"v_cvt_pk_bf16_f32 v203, v203, v207\n\t"
"v_mfma_f32_16x16x32_bf16 v[168:171], a[108:111], a[168:171], v[168:171]\n\t"
"s_nop 0\n\t"
"ds_write_addtid_b32 v200 offset:2048\n\t"
"ds_write_addtid_b32 v203 offset:14496\n\t"
"ds_write_addtid_b32 v211 offset:13472\n\t"
"v_mfma_f32_16x16x32_bf16 v[184:187], a[112:115], a[152:155], v[184:187]\n\t"
"ds_read_b128 a[152:155], v224 offset:17056\n\t"
"v_cvt_pk_bf16_f32 v210, v210, v214\n\t"
"v_mfma_f32_16x16x32_bf16 v[184:187], a[116:119], a[168:171], v[184:187]\n\t"
"s_waitcnt lgkmcnt(9)\n\t"
"buffer_store_dwordx4 v[24:27], v233, s[16:19], s24 offen nt\n\t"
"v_mfma_f32_16x16x32_bf16 v[188:191], a[112:115], a[156:159], v[188:191]\n\t"
"ds_read_b128 a[168:171], v224 offset:19104\n\t"
"v_cvt_pk_bf16_f32 v217, v217, v221\n\t"
"v_mfma_f32_16x16x32_bf16 v[188:191], a[116:119], a[172:175], v[188:191]\n\t"
"ds_read_b128 v[44:47], v231 offset:33792\n\t"
"v_cvt_pk_bf16_f32 v192, v192, v196\n\t"
"v_mfma_f32_16x16x32_bf16 v[172:175], a[104:107], a[156:159], v[172:175]\n\t"
"s_nop 0\n\t"
"ds_write_addtid_b32 v209 offset:9344\n\t"
"ds_write_addtid_b32 v210 offset:5152\n\t"
"v_mfma_f32_16x16x32_bf16 v[172:175], a[108:111], a[172:175], v[172:175]\n\t"
"ds_write_addtid_b32 v216 offset:3072\n\t"
"buffer_load_dwordx4 a[24:27], off, s[84:87], s54 offset:0 sc1\n\t"
"v_mfma_f32_16x16x32_bf16 v[156:159], a[96:99], a[156:159], v[156:159]\n\t"
"ds_read_b128 v[40:43], v231 offset:20480\n\t"
"v_cvt_pk_bf16_f32 v219, v219, v223\n\t"
"v_mfma_f32_16x16x32_bf16 v[156:159], a[100:103], a[172:175], v[156:159]\n\t"
"s_waitcnt vmcnt(13) lgkmcnt(11)\n\t"
"ds_write_addtid_b32 v208 offset:1024\n\t"
"v_cvt_pk_bf16_f32 v193, v193, v197\n\t"
"v_mfma_f32_16x16x32_bf16 v[96:99], a[120:123], a[144:147], v[96:99]\n\t"
"s_nop 0\n\t"
"ds_write_addtid_b32 v217 offset:11392\n\t"
"ds_write_addtid_b32 v218 offset:7200\n\t"
"v_mfma_f32_16x16x32_bf16 v[96:99], a[124:127], a[160:163], v[96:99]\n\t"
"s_nop 0\n\t"
"ds_write_addtid_b32 v193 offset:8320\n\t"
"ds_write_addtid_b32 v201 offset:10368\n\t"
"v_mfma_f32_16x16x32_bf16 v[112:115], a[128:131], a[144:147], v[112:115]\n\t"
"s_nop 0\n\t"
"ds_write_addtid_b32 v192 offset:0\n\t"
"ds_write_addtid_b32 v219 offset:15520\n\t"
"v_mfma_f32_16x16x32_bf16 v[112:115], a[132:135], a[160:163], v[112:115]\n\t"
"buffer_load_dwordx4 a[28:31], off, s[84:87], s54 offset:1024 sc1\n\t"
"v_mfma_f32_16x16x32_bf16 v[128:131], a[136:139], a[144:147], v[128:131]\n\t"
"s_nop 0\n\t"
"ds_read_b128 a[156:159], v224 offset:17312\n\t"
"ds_read_b128 a[172:175], v224 offset:19360\n\t"
"v_mfma_f32_16x16x32_bf16 v[128:131], a[140:143], a[160:163], v[128:131]\n\t"
"buffer_load_dwordx4 a[64:67], off, s[28:31], 0 offset:0 sc1\n\t"
"v_mfma_f32_16x16x32_bf16 v[132:135], a[136:139], a[148:151], v[132:135]\n\t"
"s_nop 0\n\t"
"ds_read_b64_tr_b16 a[200:201], v250 offset:1152\n\t"
"ds_read_b64_tr_b16 a[202:203], v250 offset:1664\n\t"
"v_mfma_f32_16x16x32_bf16 v[132:135], a[140:143], a[164:167], v[132:135]\n\t"
"buffer_load_dwordx4 a[68:71], off, s[28:31], 0 offset:1024 sc1\n\t"
"v_mfma_f32_16x16x32_bf16 v[116:119], a[128:131], a[148:151], v[116:119]\n\t"
"s_nop 0\n\t"
"ds_read_b64_tr_b16 a[204:205], v251 offset:1152\n\t"
"ds_read_b64_tr_b16 a[206:207], v251 offset:1664\n\t"
"v_mfma_f32_16x16x32_bf16 v[116:119], a[132:135], a[164:167], v[116:119]\n\t"
"s_waitcnt lgkmcnt(12)\n\t"
"buffer_store_dwordx4 v[40:43], v234, s[16:19], s24 offen nt\n\t"
"v_mfma_f32_16x16x32_bf16 v[100:103], a[120:123], a[148:151], v[100:103]\n\t"
"v_mfma_f32_16x16x32_bf16 v[100:103], a[124:127], a[164:167], v[100:103]\n\t"
"s_waitcnt lgkmcnt(4)\n\t"
"s_barrier\n\t"
"v_mfma_f32_16x16x32_bf16 v[104:107], a[120:123], a[152:155], v[104:107]\n\t"
"ds_read_b128 a[144:147], v224 offset:0\n\t"
"v_mfma_f32_16x16x32_bf16 v[104:107], a[124:127], a[168:171], v[104:107]\n\t"
"ds_read_b128 a[148:151], v224 offset:256\n\t"
"v_mfma_f32_16x16x32_bf16 v[120:123], a[128:131], a[152:155], v[120:123]\n\t"
"ds_read_b128 a[160:163], v224 offset:2048\n\t"
"v_add_u32 v248, 122368, v227\n\t"
"v_mfma_f32_16x16x32_bf16 v[120:123], a[132:135], a[168:171], v[120:123]\n\t"
"ds_read_b128 a[164:167], v224 offset:2304\n\t"
"v_mfma_f32_16x16x32_bf16 v[136:139], a[136:139], a[152:155], v[136:139]\n\t"
"v_mfma_f32_16x16x32_bf16 v[136:139], a[140:143], a[168:171], v[136:139]\n\t"
"buffer_load_dwordx4 a[60:63], off, s[84:87], 0 offset:3072 sc1\n\t"
"v_mfma_f32_16x16x32_bf16 v[140:143], a[136:139], a[156:159], v[140:143]\n\t"
"ds_read_b128 a[152:155], v224 offset:512\n\t"
"v_xor_b32 v249, 32, v248\n\t"
"v_mfma_f32_16x16x32_bf16 v[140:143], a[140:143], a[172:175], v[140:143]\n\t"
"ds_read_b128 a[168:171], v224 offset:2560\n\t"
"v_xor_b32 v250, 64, v248\n\t"
"v_mfma_f32_16x16x32_bf16 v[124:127], a[128:131], a[156:159], v[124:127]\n\t"
"v_mfma_f32_16x16x32_bf16 v[124:127], a[132:135], a[172:175], v[124:127]\n\t"
"buffer_load_dwordx4 a[56:59], off, s[84:87], 0 offset:2048 sc1\n\t"
"v_mfma_f32_16x16x32_bf16 v[108:111], a[120:123], a[156:159], v[108:111]\n\t"
"ds_read_b128 a[156:159], v224 offset:768\n\t"
"v_mfma_f32_16x16x32_bf16 v[108:111], a[124:127], a[172:175], v[108:111]\n\t"
"ds_read_b128 a[172:175], v224 offset:2816\n\t"
"v_xor_b32 v251, 96, v248\n\t"
"v_mfma_f32_16x16x32_bf16 v[192:195], a[224:227], a[176:179], 0\n\t"
"s_nop 0\n\t"
"ds_read_b64_tr_b16 a[176:177], v248 offset:20736\n\t"
"ds_read_b64_tr_b16 a[178:179], v248 offset:21248\n\t"
"v_mfma_f32_16x16x32_bf16 v[196:199], a[224:227], a[180:183], 0\n\t"
"buffer_load_dwordx4 a[48:51], off, s[84:87], 0 offset:0 sc1\n\t"
"v_mfma_f32_16x16x32_bf16 v[200:203], a[224:227], a[184:187], 0\n\t"
"s_nop 0\n\t"
"ds_read_b64_tr_b16 a[180:181], v249 offset:20736\n\t"
"ds_read_b64_tr_b16 a[182:183], v249 offset:21248\n\t"
"v_mfma_f32_16x16x32_bf16 v[204:207], a[224:227], a[188:191], 0\n\t"
"buffer_load_dwordx4 a[52:55], off, s[84:87], 0 offset:1024 sc1\n\t"
"v_mfma_f32_16x16x32_bf16 v[208:211], a[224:227], a[192:195], 0\n\t"
"s_nop 0\n\t"
"ds_read_b64_tr_b16 a[184:185], v250 offset:20736\n\t"
"ds_read_b64_tr_b16 a[186:187], v250 offset:21248\n\t"
"v_mfma_f32_16x16x32_bf16 v[212:215], a[224:227], a[196:199], 0\n\t"
"s_waitcnt lgkmcnt(8)\n\t"
"ds_read_b64_tr_b16 a[188:189], v251 offset:20736\n\t"
"ds_read_b64_tr_b16 a[190:191], v251 offset:21248\n\t"
"v_mfma_f32_16x16x32_bf16 v[216:219], a[224:227], a[200:203], 0\n\t"
".p2align 3\n\t"
"ds_read_b64_tr_b16 a[192:193], v248 offset:0\n\t"
"buffer_load_dwordx4 a[88:91], off, s[28:31], s51 offset:0 sc1\n\t"
"v_pk_add_f32 v[96:97], v[96:97], v[144:145]\n\t"
"v_pk_add_f32 v[98:99], v[98:99], v[146:147]\n\t"
"v_pk_fma_f32 v[144:145], v[144:145], s[38:39], v[96:97]\n\t"
"v_pk_fma_f32 v[146:147], v[146:147], s[38:39], v[98:99]\n\t"
"v_pk_add_f32 v[112:113], v[112:113], v[160:161]\n\t"
"v_pk_add_f32 v[114:115], v[114:115], v[162:163]\n\t"
"v_pk_fma_f32 v[160:161], v[160:161], s[38:39], v[112:113]\n\t"
"v_pk_fma_f32 v[162:163], v[162:163], s[38:39], v[114:115]\n\t"
"v_pk_add_f32 v[128:129], v[128:129], v[176:177]\n\t"
"v_pk_add_f32 v[130:131], v[130:131], v[178:179]\n\t"
"v_pk_fma_f32 v[176:177], v[176:177], s[38:39], v[128:129]\n\t"
"v_pk_fma_f32 v[178:179], v[178:179], s[38:39], v[130:131]\n\t"
"buffer_load_dwordx4 a[92:95], off, s[28:31], s51 offset:1024 sc1\n\t"
"v_pk_add_f32 v[132:133], v[132:133], v[180:181]\n\t"
"v_pk_add_f32 v[134:135], v[134:135], v[182:183]\n\t"
"v_pk_fma_f32 v[180:181], v[180:181], s[38:39], v[132:133]\n\t"
"v_pk_fma_f32 v[182:183], v[182:183], s[38:39], v[134:135]\n\t"
"v_pk_add_f32 v[116:117], v[116:117], v[164:165]\n\t"
"v_pk_add_f32 v[118:119], v[118:119], v[166:167]\n\t"
"v_pk_fma_f32 v[164:165], v[164:165], s[38:39], v[116:117]\n\t"
"v_pk_fma_f32 v[166:167], v[166:167], s[38:39], v[118:119]\n\t"
"v_pk_add_f32 v[100:101], v[100:101], v[148:149]\n\t"
"v_pk_add_f32 v[102:103], v[102:103], v[150:151]\n\t"
"v_pk_fma_f32 v[148:149], v[148:149], s[38:39], v[100:101]\n\t"
"v_pk_fma_f32 v[150:151], v[150:151], s[38:39], v[102:103]\n\t"
"buffer_load_dwordx4 a[84:87], off, s[84:87], s51 offset:3072 sc1\n\t"
"v_pk_add_f32 v[104:105], v[104:105], v[152:153]\n\t"
"v_pk_add_f32 v[106:107], v[106:107], v[154:155]\n\t"
"v_pk_fma_f32 v[152:153], v[152:153], s[38:39], v[104:105]\n\t"
"v_pk_fma_f32 v[154:155], v[154:155], s[38:39], v[106:107]\n\t"
"v_pk_add_f32 v[120:121], v[120:121], v[168:169]\n\t"
"v_pk_add_f32 v[122:123], v[122:123], v[170:171]\n\t"
"v_pk_fma_f32 v[168:169], v[168:169], s[38:39], v[120:121]\n\t"
"v_pk_fma_f32 v[170:171], v[170:171], s[38:39], v[122:123]\n\t"
"v_pk_add_f32 v[136:137], v[136:137], v[184:185]\n\t"
"v_pk_add_f32 v[138:139], v[138:139], v[186:187]\n\t"
"v_pk_fma_f32 v[184:185], v[184:185], s[38:39], v[136:137]\n\t"
"v_pk_fma_f32 v[186:187], v[186:187], s[38:39], v[138:139]\n\t"
"buffer_load_dwordx4 a[80:83], off, s[84:87], s51 offset:2048 sc1\n\t"
"v_pk_add_f32 v[140:141], v[140:141], v[188:189]\n\t"
"v_pk_add_f32 v[142:143], v[142:143], v[190:191]\n\t"
"v_pk_fma_f32 v[188:189], v[188:189], s[38:39], v[140:141]\n\t"
"v_pk_fma_f32 v[190:191], v[190:191], s[38:39], v[142:143]\n\t"
"v_pk_add_f32 v[124:125], v[124:125], v[172:173]\n\t"
"v_pk_add_f32 v[126:127], v[126:127], v[174:175]\n\t"
"v_pk_fma_f32 v[172:173], v[172:173], s[38:39], v[124:125]\n\t"
"v_pk_fma_f32 v[174:175], v[174:175], s[38:39], v[126:127]\n\t"
"v_pk_add_f32 v[108:109], v[108:109], v[156:157]\n\t"
"v_pk_add_f32 v[110:111], v[110:111], v[158:159]\n\t"
"v_pk_fma_f32 v[156:157], v[156:157], s[38:39], v[108:109]\n\t"
"v_pk_fma_f32 v[158:159], v[158:159], s[38:39], v[110:111]\n\t"
"v_cvt_pk_bf16_f32 v100, v100, v101\n\t"
"v_cvt_pk_bf16_f32 v101, v102, v103\n\t"
"v_cvt_pk_bf16_f32 v108, v108, v109\n\t"
"v_mfma_f32_16x16x32_bf16 v[220:223], a[224:227], a[204:207], 0\n\t"
"s_waitcnt vmcnt(19)\n\t"
"ds_read_b64_tr_b16 a[194:195], v248 offset:512\n\t"
"buffer_store_dwordx4 v[12:15], v232, s[16:19], s27 offen nt\n\t"
"v_cvt_pk_bf16_f32 v193, v193, v197\n\t"
"v_cvt_pk_bf16_f32 v96, v96, v97\n\t"
"v_cvt_pk_bf16_f32 v116, v116, v117\n\t"
"v_cvt_pk_bf16_f32 v132, v132, v133\n\t"
"v_mfma_f32_16x16x32_bf16 v[48:51], a[0:3], a[144:147], v[48:51]\n\t"
"v_cvt_pk_bf16_f32 v97, v98, v99\n\t"
"v_cvt_pk_bf16_f32 v128, v128, v129\n\t"
"v_mfma_f32_16x16x32_bf16 v[48:51], a[4:7], a[160:163], v[48:51]\n\t"
"ds_write_addtid_b32 v193 offset:24864\n\t"
"ds_write2st64_b64 v228, v[96:97], v[100:101] offset0:40 offset1:53\n\t"
"v_cvt_pk_bf16_f32 v117, v118, v119\n\t"
"v_mfma_f32_16x16x32_bf16 v[64:67], a[8:11], a[144:147], v[64:67]\n\t"
"s_nop 0\n\t"
"v_cvt_pk_bf16_f32 v133, v134, v135\n\t"
"v_cvt_pk_bf16_f32 v104, v104, v105\n\t"
"v_mfma_f32_16x16x32_bf16 v[64:67], a[12:15], a[160:163], v[64:67]\n\t"
"buffer_load_dwordx4 a[72:75], off, s[84:87], s51 offset:0 sc1\n\t"
"v_cvt_pk_bf16_f32 v210, v210, v214\n\t"
"v_cvt_pk_bf16_f32 v105, v106, v107\n\t"
"v_mfma_f32_16x16x32_bf16 v[80:83], a[16:19], a[144:147], v[80:83]\n\t"
"s_nop 0\n\t"
"v_cvt_pk_bf16_f32 v112, v112, v113\n\t"
"v_cvt_pk_bf16_f32 v113, v114, v115\n\t"
"v_mfma_f32_16x16x32_bf16 v[80:83], a[20:23], a[160:163], v[80:83]\n\t"
"s_nop 0\n\t"
"ds_read_b128 a[144:147], v224 offset:1024\n\t"
"ds_read_b128 a[160:163], v224 offset:3072\n\t"
"ds_write2st64_b64 v246, v[112:113], v[116:117] offset0:40 offset1:53\n\t"
"v_mfma_f32_16x16x32_bf16 v[84:87], a[16:19], a[148:151], v[84:87]\n\t"
"s_nop 0\n\t"
"v_cvt_pk_bf16_f32 v195, v195, v199\n\t"
"v_cvt_pk_bf16_f32 v218, v218, v222\n\t"
"v_mfma_f32_16x16x32_bf16 v[84:87], a[20:23], a[164:167], v[84:87]\n\t"
"Lpad6_%=:\n\t"
"s_cbranch_execz Lret0_%=\n\t"
"buffer_load_dwordx4 a[76:79], off, s[84:87], s51 offset:1024 sc1\n\t"
"v_mfma_f32_16x16x32_bf16 v[68:71], a[8:11], a[148:151], v[68:71]\n\t"
"v_cvt_pk_bf16_f32 v129, v130, v131\n\t"
"v_cvt_pk_bf16_f32 v109, v110, v111\n\t"
"v_mfma_f32_16x16x32_bf16 v[68:71], a[12:15], a[164:167], v[68:71]\n\t"
"ds_write_addtid_b32 v210 offset:21696\n\t"
"ds_write2st64_b64 v247, v[128:129], v[132:133] offset0:40 offset1:53\n\t"
"v_cvt_pk_bf16_f32 v140, v140, v141\n\t"
"v_mfma_f32_16x16x32_bf16 v[52:55], a[0:3], a[148:151], v[52:55]\n\t"
"v_cvt_pk_bf16_f32 v202, v202, v206\n\t"
"v_cvt_pk_bf16_f32 v124, v124, v125\n\t"
"v_mfma_f32_16x16x32_bf16 v[52:55], a[4:7], a[164:167], v[52:55]\n\t"
"buffer_load_dwordx4 a[112:115], off, s[28:31], s81 offset:0 sc1\n\t"
"v_cvt_pk_bf16_f32 v120, v120, v121\n\t"
"v_mfma_f32_16x16x32_bf16 v[56:59], a[0:3], a[152:155], v[56:59]\n\t"
"v_cvt_pk_bf16_f32 v208, v208, v212\n\t"
"v_cvt_pk_bf16_f32 v219, v219, v223\n\t"
"v_mfma_f32_16x16x32_bf16 v[56:59], a[4:7], a[168:171], v[56:59]\n\t"
"s_nop 0\n\t"
"ds_read_b128 a[148:151], v224 offset:1280\n\t"
"ds_read_b128 a[164:167], v224 offset:3328\n\t"
"ds_write_addtid_b32 v208 offset:17568\n\t"
"ds_write_addtid_b32 v219 offset:32064\n\t"
"v_cvt_pk_bf16_f32 v203, v203, v207\n\t"
"v_mfma_f32_16x16x32_bf16 v[72:75], a[8:11], a[152:155], v[72:75]\n\t"
"v_cvt_pk_bf16_f32 v194, v194, v198\n\t"
"v_cvt_pk_bf16_f32 v201, v201, v205\n\t"
"v_mfma_f32_16x16x32_bf16 v[72:75], a[12:15], a[168:171], v[72:75]\n\t"
"ds_write_addtid_b32 v203 offset:31040\n\t"
"buffer_store_dwordx4 v[28:31], v233, s[16:19], s27 offen nt\n\t"
"ds_write2st64_b64 v228, v[104:105], v[108:109] offset0:66 offset1:79\n\t"
"v_cvt_pk_bf16_f32 v136, v136, v137\n\t"
"v_mfma_f32_16x16x32_bf16 v[88:91], a[16:19], a[152:155], v[88:91]\n\t"
"v_cvt_pk_bf16_f32 v121, v122, v123\n\t"
"v_cvt_pk_bf16_f32 v137, v138, v139\n\t"
"v_mfma_f32_16x16x32_bf16 v[88:91], a[20:23], a[168:171], v[88:91]\n\t"
"s_waitcnt lgkmcnt(8)\n\t"
"v_cvt_pk_bf16_f32 v217, v217, v221\n\t"
"v_cvt_pk_bf16_f32 v125, v126, v127\n\t"
"v_mfma_f32_16x16x32_bf16 v[92:95], a[16:19], a[156:159], v[92:95]\n\t"
"v_cvt_pk_bf16_f32 v200, v200, v204\n\t"
"v_cvt_pk_bf16_f32 v141, v142, v143\n\t"
"v_mfma_f32_16x16x32_bf16 v[92:95], a[20:23], a[172:175], v[92:95]\n\t"
"s_nop 0\n\t"
"ds_write_addtid_b32 v195 offset:28992\n\t"
"ds_write_addtid_b32 v200 offset:18592\n\t"
"ds_write_addtid_b32 v201 offset:26912\n\t"
"ds_write_addtid_b32 v202 offset:22720\n\t"
"ds_write_addtid_b32 v217 offset:27936\n\t"
"ds_write2st64_b64 v246, v[120:121], v[124:125] offset0:66 offset1:79\n\t"
"v_mfma_f32_16x16x32_bf16 v[76:79], a[8:11], a[156:159], v[76:79]\n\t"
"v_cvt_pk_bf16_f32 v209, v209, v213\n\t"
"v_cvt_pk_bf16_f32 v216, v216, v220\n\t"
"v_mfma_f32_16x16x32_bf16 v[76:79], a[12:15], a[172:175], v[76:79]\n\t"
"buffer_load_dwordx4 a[116:119], off, s[28:31], s81 offset:1024 sc1\n\t"
"v_mfma_f32_16x16x32_bf16 v[60:63], a[0:3], a[156:159], v[60:63]\n\t"
"v_cvt_pk_bf16_f32 v192, v192, v196\n\t"
"v_cvt_pk_bf16_f32 v211, v211, v215\n\t"
"v_mfma_f32_16x16x32_bf16 v[60:63], a[4:7], a[172:175], v[60:63]\n\t"
"s_waitcnt vmcnt(17) lgkmcnt(8)\n\t"
"ds_write_addtid_b32 v192 offset:16544\n\t"
"ds_write_addtid_b32 v194 offset:20672\n\t"
"ds_write_addtid_b32 v209 offset:25888\n\t"
"ds_write_addtid_b32 v211 offset:30016\n\t"
"ds_write2st64_b64 v247, v[136:137], v[140:141] offset0:66 offset1:79\n\t"
"v_mfma_f32_16x16x32_bf16 v[48:51], a[24:27], a[144:147], v[48:51]\n\t"
"buffer_load_dwordx4 a[108:111], off, s[84:87], s81 offset:3072 sc1\n\t"
"v_mfma_f32_16x16x32_bf16 v[48:51], a[28:31], a[160:163], v[48:51]\n\t"
"s_nop 0\n\t"
"ds_write_addtid_b32 v216 offset:19616\n\t"
"ds_write_addtid_b32 v218 offset:23744\n\t"
"v_mfma_f32_16x16x32_bf16 v[64:67], a[32:35], a[144:147], v[64:67]\n\t"
"buffer_load_dwordx4 a[104:107], off, s[84:87], s81 offset:2048 sc1\n\t"
"v_mfma_f32_16x16x32_bf16 v[64:67], a[36:39], a[160:163], v[64:67]\n\t"
"s_nop 0\n\t"
"ds_read_b128 a[152:155], v224 offset:1536\n\t"
"ds_read_b128 a[156:159], v224 offset:1792\n\t"
"v_mfma_f32_16x16x32_bf16 v[80:83], a[40:43], a[144:147], v[80:83]\n\t"
"s_nop 0\n\t"
"ds_read_b128 a[168:171], v224 offset:3584\n\t"
"ds_read_b128 a[172:175], v224 offset:3840\n\t"
"v_mfma_f32_16x16x32_bf16 v[80:83], a[44:47], a[160:163], v[80:83]\n\t"
"buffer_load_dwordx4 a[96:99], off, s[84:87], s81 offset:0 sc1\n\t"
"v_mfma_f32_16x16x32_bf16 v[84:87], a[40:43], a[148:151], v[84:87]\n\t"
"s_nop 0\n\t"
"ds_read_b64_tr_b16 a[196:197], v249 offset:0\n\t"
"ds_read_b64_tr_b16 a[198:199], v249 offset:512\n\t"
"v_mfma_f32_16x16x32_bf16 v[84:87], a[44:47], a[164:167], v[84:87]\n\t"
"s_nop 0\n\t"
"ds_read_b64_tr_b16 a[200:201], v250 offset:0\n\t"
"ds_read_b64_tr_b16 a[202:203], v250 offset:512\n\t"
"v_mfma_f32_16x16x32_bf16 v[68:71], a[32:35], a[148:151], v[68:71]\n\t"
"s_nop 0\n\t"
"ds_read_b64_tr_b16 a[204:205], v251 offset:0\n\t"
"ds_read_b64_tr_b16 a[206:207], v251 offset:512\n\t"
"v_mfma_f32_16x16x32_bf16 v[68:71], a[36:39], a[164:167], v[68:71]\n\t"
"buffer_store_dwordx4 v[44:47], v234, s[16:19], s27 offen nt\n\t"
"v_mfma_f32_16x16x32_bf16 v[52:55], a[24:27], a[148:151], v[52:55]\n\t"
"s_waitcnt lgkmcnt(6)\n\t"
"v_mfma_f32_16x16x32_bf16 v[52:55], a[28:31], a[164:167], v[52:55]\n\t"
"buffer_load_dwordx4 a[100:103], off, s[84:87], s81 offset:1024 sc1\n\t"
"s_barrier\n\t"
"v_mfma_f32_16x16x32_bf16 v[56:59], a[24:27], a[152:155], v[56:59]\n\t"
"s_nop 0\n\t"
"ds_read_b128 a[144:147], v224 offset:16544\n\t"
"ds_read_b128 a[164:167], v224 offset:18848\n\t"
"v_mfma_f32_16x16x32_bf16 v[56:59], a[28:31], a[168:171], v[56:59]\n\t"
"buffer_load_dwordx4 a[136:139], off, s[28:31], s66 offset:0 sc1\n\t"
"v_mfma_f32_16x16x32_bf16 v[72:75], a[32:35], a[152:155], v[72:75]\n\t"
"s_nop 0\n\t"
"ds_read_b128 a[160:163], v224 offset:18592\n\t"
"ds_read_b128 a[148:151], v224 offset:16800\n\t"
"v_mfma_f32_16x16x32_bf16 v[72:75], a[36:39], a[168:171], v[72:75]\n\t"
"buffer_load_dwordx4 a[140:143], off, s[28:31], s66 offset:1024 sc1\n\t"
"v_mfma_f32_16x16x32_bf16 v[88:91], a[40:43], a[152:155], v[88:91]\n\t"
"ds_read_b128 v[108:111], v229 offset:33792\n\t"
"v_add_u32 v248, 54016, v227\n\t"
"v_mfma_f32_16x16x32_bf16 v[88:91], a[44:47], a[168:171], v[88:91]\n\t"
"ds_read_b128 v[104:107], v229 offset:20480\n\t"
"v_xor_b32 v249, 32, v248\n\t"
"v_mfma_f32_16x16x32_bf16 v[92:95], a[40:43], a[156:159], v[92:95]\n\t"
"s_nop 0\n\t"
"ds_read_b128 v[136:139], v231 offset:20480\n\t"
"ds_read_b128 a[232:235], v230 offset:33792\n\t"
"v_mfma_f32_16x16x32_bf16 v[92:95], a[44:47], a[172:175], v[92:95]\n\t"
"buffer_load_dwordx4 a[132:135], off, s[84:87], s66 offset:3072 sc1\n\t"
"v_mfma_f32_16x16x32_bf16 v[76:79], a[32:35], a[156:159], v[76:79]\n\t"
"ds_read_b128 a[236:239], v231 offset:33792\n\t"
"v_xor_b32 v250, 64, v248\n\t"
"v_mfma_f32_16x16x32_bf16 v[76:79], a[36:39], a[172:175], v[76:79]\n\t"
"ds_read_b128 a[152:155], v224 offset:17056\n\t"
"v_xor_b32 v251, 96, v248\n\t"
"v_mfma_f32_16x16x32_bf16 v[60:63], a[24:27], a[156:159], v[60:63]\n\t"
"ds_read_b128 v[120:123], v230 offset:20480\n\t"
"v_mfma_f32_16x16x32_bf16 v[60:63], a[28:31], a[172:175], v[60:63]\n\t"
"buffer_load_dwordx4 a[128:131], off, s[84:87], s66 offset:2048 sc1\n\t"
"v_mfma_f32_16x16x32_bf16 v[192:195], a[216:219], a[176:179], 0\n\t"
"s_nop 0\n\t"
"ds_read_b128 a[168:171], v224 offset:19104\n\t"
"ds_read_b64_tr_b16 a[176:177], v248 offset:21888\n\t"
"v_mfma_f32_16x16x32_bf16 v[196:199], a[216:219], a[180:183], 0\n\t"
"ds_read_b64_tr_b16 a[178:179], v248 offset:22400\n\t"
"v_mfma_f32_16x16x32_bf16 v[200:203], a[216:219], a[184:187], 0\n\t"
"s_waitcnt lgkmcnt(8)\n\t"
"buffer_store_dwordx4 v[104:107], v232, s[16:19], s20 offen nt\n\t"
"v_mfma_f32_16x16x32_bf16 v[204:207], a[216:219], a[188:191], 0\n\t"
"s_nop 0\n\t"
"ds_read_b64_tr_b16 a[180:181], v249 offset:21888\n\t"
"ds_read_b64_tr_b16 a[182:183], v249 offset:22400\n\t"
"v_mfma_f32_16x16x32_bf16 v[208:211], a[216:219], a[192:195], 0\n\t"
"s_nop 0\n\t"
"ds_read_b64_tr_b16 a[184:185], v250 offset:21888\n\t"
"ds_read_b64_tr_b16 a[186:187], v250 offset:22400\n\t"
"v_mfma_f32_16x16x32_bf16 v[212:215], a[216:219], a[196:199], 0\n\t"
"s_waitcnt lgkmcnt(7)\n\t"
"s_barrier\n\t"
"v_mfma_f32_16x16x32_bf16 v[216:219], a[216:219], a[200:203], 0\n\t"
"buffer_load_dwordx4 a[120:123], off, s[84:87], s66 offset:0 sc1\n\t"
"v_mfma_f32_16x16x32_bf16 v[220:223], a[216:219], a[204:207], 0\n\t"
"s_waitcnt vmcnt(21)\n\t"
"ds_read_b128 a[156:159], v224 offset:17312\n\t"
"v_cvt_pk_bf16_f32 v195, v195, v199\n\t"
"v_mfma_f32_16x16x32_bf16 v[48:51], a[48:51], a[144:147], v[48:51]\n\t"
"ds_read_b128 a[172:175], v224 offset:19360\n\t"
"v_cvt_pk_bf16_f32 v194, v194, v198\n\t"
"v_mfma_f32_16x16x32_bf16 v[48:51], a[52:55], a[160:163], v[48:51]\n\t"
"ds_read_b64_tr_b16 a[188:189], v251 offset:21888\n\t"
"v_cvt_pk_bf16_f32 v200, v200, v204\n\t"
"v_mfma_f32_16x16x32_bf16 v[64:67], a[56:59], a[144:147], v[64:67]\n\t"
"ds_read_b64_tr_b16 a[190:191], v251 offset:22400\n\t"
"v_cvt_pk_bf16_f32 v203, v203, v207\n\t"
"v_mfma_f32_16x16x32_bf16 v[64:67], a[60:63], a[160:163], v[64:67]\n\t"
"buffer_load_dwordx4 a[124:127], off, s[84:87], s66 offset:1024 sc1\n\t"
"v_mfma_f32_16x16x32_bf16 v[80:83], a[64:67], a[144:147], v[80:83]\n\t"
"ds_read_b128 a[144:147], v224 offset:17568\n\t"
"v_cvt_pk_bf16_f32 v201, v201, v205\n\t"
"v_mfma_f32_16x16x32_bf16 v[80:83], a[68:71], a[160:163], v[80:83]\n\t"
"ds_read_b128 a[160:163], v224 offset:19616\n\t"
"v_cvt_pk_bf16_f32 v208, v208, v212\n\t"
"v_mfma_f32_16x16x32_bf16 v[84:87], a[64:67], a[148:151], v[84:87]\n\t"
"ds_write_addtid_b32 v194 offset:4128\n\t"
"v_cvt_pk_bf16_f32 v219, v219, v223\n\t"
"v_mfma_f32_16x16x32_bf16 v[84:87], a[68:71], a[164:167], v[84:87]\n\t"
"ds_write_addtid_b32 v203 offset:14496\n\t"
"buffer_store_dwordx4 v[120:123], v233, s[16:19], s20 offen nt\n\t"
"v_mfma_f32_16x16x32_bf16 v[68:71], a[56:59], a[148:151], v[68:71]\n\t"
"ds_write_addtid_b32 v195 offset:12448\n\t"
"v_cvt_pk_bf16_f32 v216, v216, v220\n\t"
"v_mfma_f32_16x16x32_bf16 v[68:71], a[60:63], a[164:167], v[68:71]\n\t"
"ds_write_addtid_b32 v208 offset:1024\n\t"
"v_cvt_pk_bf16_f32 v192, v192, v196\n\t"
"v_mfma_f32_16x16x32_bf16 v[52:55], a[48:51], a[148:151], v[52:55]\n\t"
"ds_read_b128 a[148:151], v224 offset:17824\n\t"
"v_cvt_pk_bf16_f32 v210, v210, v214\n\t"
"v_mfma_f32_16x16x32_bf16 v[52:55], a[52:55], a[164:167], v[52:55]\n\t"
"s_waitcnt lgkmcnt(8)\n\t"
"ds_write_addtid_b32 v216 offset:3072\n\t"
"v_cvt_pk_bf16_f32 v202, v202, v206\n\t"
"v_mfma_f32_16x16x32_bf16 v[56:59], a[48:51], a[152:155], v[56:59]\n\t"
"ds_read_b128 a[164:167], v224 offset:19872\n\t"
"v_cvt_pk_bf16_f32 v209, v209, v213\n\t"
"v_mfma_f32_16x16x32_bf16 v[56:59], a[52:55], a[168:171], v[56:59]\n\t"
"ds_write_addtid_b32 v210 offset:5152\n\t"
"v_cvt_pk_bf16_f32 v217, v217, v221\n\t"
"v_mfma_f32_16x16x32_bf16 v[72:75], a[56:59], a[152:155], v[72:75]\n\t"
"s_nop 0\n\t"
"ds_write_addtid_b32 v201 offset:10368\n\t"
"ds_write_addtid_b32 v217 offset:11392\n\t"
"v_mfma_f32_16x16x32_bf16 v[72:75], a[60:63], a[168:171], v[72:75]\n\t"
"buffer_store_dwordx4 v[136:139], v234, s[16:19], s20 offen nt\n\t"
"v_mfma_f32_16x16x32_bf16 v[88:91], a[64:67], a[152:155], v[88:91]\n\t"
"s_nop 0\n\t"
"ds_write_addtid_b32 v202 offset:6176\n\t"
"ds_write_addtid_b32 v209 offset:9344\n\t"
"v_mfma_f32_16x16x32_bf16 v[88:91], a[68:71], a[168:171], v[88:91]\n\t"
"s_nop 0\n\t"
"ds_write_addtid_b32 v200 offset:2048\n\t"
"ds_write_addtid_b32 v219 offset:15520\n\t"
"v_mfma_f32_16x16x32_bf16 v[92:95], a[64:67], a[156:159], v[92:95]\n\t"
"s_add_u32 m0, s26, 103936\n\t"
"ds_read_b128 a[152:155], v224 offset:18080\n\t"
"v_cvt_pk_bf16_f32 v193, v193, v197\n\t"
"v_mfma_f32_16x16x32_bf16 v[92:95], a[68:71], a[172:175], v[92:95]\n\t"
"buffer_load_dwordx4 v236, s[12:15], s22 offen offset:0 nt lds\n\t"
"v_mfma_f32_16x16x32_bf16 v[76:79], a[56:59], a[156:159], v[76:79]\n\t"
"ds_read_b128 a[168:171], v224 offset:20128\n\t"
"v_cvt_pk_bf16_f32 v218, v218, v222\n\t"
"v_mfma_f32_16x16x32_bf16 v[76:79], a[60:63], a[172:175], v[76:79]\n\t"
"buffer_load_dwordx4 v237, s[12:15], s22 offen offset:1024 nt lds\n\t"
"v_mfma_f32_16x16x32_bf16 v[60:63], a[48:51], a[156:159], v[60:63]\n\t"
"s_lshl_b32 m0, s73, 8\n\t"
"ds_read_b128 a[156:159], v224 offset:18336\n\t"
"v_cvt_pk_bf16_f32 v211, v211, v215\n\t"
"v_mfma_f32_16x16x32_bf16 v[60:63], a[52:55], a[172:175], v[60:63]\n\t"
"s_waitcnt vmcnt(19) lgkmcnt(9)\n\t"
"ds_write_addtid_b32 v192 offset:0\n\t"
"ds_write_addtid_b32 v193 offset:8320\n\t"
"v_mfma_f32_16x16x32_bf16 v[144:147], a[72:75], a[144:147], v[144:147]\n\t"
"s_nop 0\n\t"
"ds_write_addtid_b32 v211 offset:13472\n\t"
"ds_write_addtid_b32 v218 offset:7200\n\t"
"v_mfma_f32_16x16x32_bf16 v[144:147], a[76:79], a[160:163], v[144:147]\n\t"
"s_add_u32 m0, s26, 103936\n\t"
"ds_read_b128 a[172:175], v224 offset:20384\n\t"
"v_mfma_f32_16x16x32_bf16 v[160:163], a[80:83], a[144:147], v[160:163]\n\t"
"buffer_load_dwordx4 v238, s[12:15], s22 offen offset:2048 nt lds\n\t"
"v_mfma_f32_16x16x32_bf16 v[160:163], a[84:87], a[160:163], v[160:163]\n\t"
"s_nop 0\n\t"
"ds_read_b64_tr_b16 a[192:193], v248 offset:1152\n\t"
"ds_read_b64_tr_b16 a[194:195], v248 offset:1664\n\t"
"v_mfma_f32_16x16x32_bf16 v[176:179], a[88:91], a[144:147], v[176:179]\n\t"
"s_nop 0\n\t"
"ds_read_b64_tr_b16 a[196:197], v249 offset:1152\n\t"
"ds_read_b64_tr_b16 a[198:199], v249 offset:1664\n\t"
"v_mfma_f32_16x16x32_bf16 v[176:179], a[92:95], a[160:163], v[176:179]\n\t"
"buffer_load_dwordx4 v239, s[12:15], s22 offen offset:3072 nt lds\n\t"
"v_mfma_f32_16x16x32_bf16 v[180:183], a[88:91], a[148:151], v[180:183]\n\t"
"s_nop 0\n\t"
"ds_read_b64_tr_b16 a[200:201], v250 offset:1152\n\t"
"ds_read_b64_tr_b16 a[202:203], v250 offset:1664\n\t"
"v_mfma_f32_16x16x32_bf16 v[180:183], a[92:95], a[164:167], v[180:183]\n\t"
"s_mov_b64 exec, s[36:37]\n\t"
"buffer_load_dwordx4 v240, s[12:15], s22 offen offset:3584 nt lds\n\t"
"s_mov_b64 exec, -1\n\t"
"v_mfma_f32_16x16x32_bf16 v[164:167], a[80:83], a[148:151], v[164:167]\n\t"
"s_lshl_b32 m0, s73, 8\n\t"
"ds_read_b64_tr_b16 a[204:205], v251 offset:1152\n\t"
"ds_read_b64_tr_b16 a[206:207], v251 offset:1664\n\t"
"v_mfma_f32_16x16x32_bf16 v[164:167], a[84:87], a[164:167], v[164:167]\n\t"
"buffer_store_dwordx4 v[108:111], v232, s[16:19], s25 offen nt\n\t"
"v_mfma_f32_16x16x32_bf16 v[148:151], a[72:75], a[148:151], v[148:151]\n\t"
"s_waitcnt lgkmcnt(8)\n\t"
"v_mfma_f32_16x16x32_bf16 v[148:151], a[76:79], a[164:167], v[148:151]\n\t"
"s_barrier\n\t"
"v_mfma_f32_16x16x32_bf16 v[152:155], a[72:75], a[152:155], v[152:155]\n\t"
"ds_read_b128 a[144:147], v224 offset:0\n\t"
"v_mfma_f32_16x16x32_bf16 v[152:155], a[76:79], a[168:171], v[152:155]\n\t"
"ds_read_b128 a[160:163], v224 offset:2048\n\t"
"v_add_u32 v248, 54016, v226\n\t"
"v_mfma_f32_16x16x32_bf16 v[168:171], a[80:83], a[152:155], v[168:171]\n\t"
"ds_read_b128 a[148:151], v224 offset:256\n\t"
"v_mfma_f32_16x16x32_bf16 v[168:171], a[84:87], a[168:171], v[168:171]\n\t"
"ds_read_b128 a[164:167], v224 offset:2304\n\t"
"v_mfma_f32_16x16x32_bf16 v[184:187], a[88:91], a[152:155], v[184:187]\n\t"
"v_mfma_f32_16x16x32_bf16 v[184:187], a[92:95], a[168:171], v[184:187]\n\t"
"ds_read_b128 a[152:155], v224 offset:512\n\t"
"v_xor_b32 v249, 32, v248\n\t"
"v_mfma_f32_16x16x32_bf16 v[188:191], a[88:91], a[156:159], v[188:191]\n\t"
"ds_read_b128 a[228:231], v254 offset:18432\n\t"
"v_mfma_f32_16x16x32_bf16 v[188:191], a[92:95], a[172:175], v[188:191]\n\t"
"ds_read_b128 a[168:171], v224 offset:2560\n\t"
"v_mfma_f32_16x16x32_bf16 v[172:175], a[80:83], a[156:159], v[172:175]\n\t"
"v_mfma_f32_16x16x32_bf16 v[172:175], a[84:87], a[172:175], v[172:175]\n\t"
"v_xor_b32 v250, 64, v248\n\t"
"v_mfma_f32_16x16x32_bf16 v[156:159], a[72:75], a[156:159], v[156:159]\n\t"
"ds_read_b128 a[156:159], v224 offset:768\n\t"
"v_xor_b32 v251, 96, v248\n\t"
"v_mfma_f32_16x16x32_bf16 v[156:159], a[76:79], a[172:175], v[156:159]\n\t"
"ds_read_b128 a[172:175], v224 offset:2816\n\t"
"v_mfma_f32_16x16x32_bf16 v[192:195], a[224:227], a[176:179], 0\n\t"
"ds_read_b64_tr_b16 a[178:179], v248 offset:128\n\t"
"v_mfma_f32_16x16x32_bf16 v[196:199], a[224:227], a[180:183], 0\n\t"
"ds_read_b64_tr_b16 a[176:177], v248 offset:0\n\t"
"v_mfma_f32_16x16x32_bf16 v[200:203], a[224:227], a[184:187], 0\n\t"
"ds_read_b64_tr_b16 a[180:181], v249 offset:256\n\t"
"v_mfma_f32_16x16x32_bf16 v[204:207], a[224:227], a[188:191], 0\n\t"
"s_waitcnt lgkmcnt(7)\n\t"
"v_mfma_f32_16x16x32_bf16 v[208:211], a[224:227], a[192:195], 0\n\t"
"s_waitcnt lgkmcnt(6)\n\t"
"ds_write_b128 v255, a[228:231] offset:0\n\t"
"v_mfma_f32_16x16x32_bf16 v[212:215], a[224:227], a[196:199], 0\n\t"
"v_mfma_f32_16x16x32_bf16 v[216:219], a[224:227], a[200:203], 0\n\t"
"v_mfma_f32_16x16x32_bf16 v[220:223], a[224:227], a[204:207], 0\n\t"
"v_cvt_pk_bf16_f32 v195, v195, v199\n\t"
"v_mfma_f32_16x16x32_bf16 v[0:3], a[0:3], a[144:147], 0\n\t"
"ds_read_b64_tr_b16 a[186:187], v249 offset:128\n\t"
"v_cvt_pk_bf16_f32 v193, v193, v197\n\t"
"v_mfma_f32_16x16x32_bf16 v[0:3], a[4:7], a[160:163], v[0:3]\n\t"
"ds_read_b64_tr_b16 a[184:185], v249 offset:0\n\t"
"v_mfma_f32_16x16x32_bf16 v[16:19], a[8:11], a[144:147], 0\n\t"
"ds_read_b64_tr_b16 a[188:189], v248 offset:256\n\t"
"v_cvt_pk_bf16_f32 v201, v201, v205\n\t"
"v_mfma_f32_16x16x32_bf16 v[16:19], a[12:15], a[160:163], v[16:19]\n\t"
"ds_read_b64_tr_b16 a[194:195], v250 offset:128\n\t"
"v_cvt_pk_bf16_f32 v200, v200, v204\n\t"
"v_mfma_f32_16x16x32_bf16 v[32:35], a[16:19], a[144:147], 0\n\t"
"ds_read_b128 a[144:147], v224 offset:1024\n\t"
"v_mfma_f32_16x16x32_bf16 v[32:35], a[20:23], a[160:163], v[32:35]\n\t"
"ds_write_addtid_b32 v201 offset:26912\n\t"
"v_cvt_pk_bf16_f32 v203, v203, v207\n\t"
"v_mfma_f32_16x16x32_bf16 v[36:39], a[16:19], a[148:151], 0\n\t"
"ds_read_b64_tr_b16 a[192:193], v250 offset:0\n\t"
"v_cvt_pk_bf16_f32 v211, v211, v215\n\t"
"v_mfma_f32_16x16x32_bf16 v[36:39], a[20:23], a[164:167], v[36:39]\n\t"
"ds_write_addtid_b32 v193 offset:24864\n\t"
"v_cvt_pk_bf16_f32 v219, v219, v223\n\t"
"v_mfma_f32_16x16x32_bf16 v[20:23], a[8:11], a[148:151], 0\n\t"
"ds_read_b128 a[160:163], v224 offset:3072\n\t"
"v_cvt_pk_bf16_f32 v209, v209, v213\n\t"
"v_mfma_f32_16x16x32_bf16 v[20:23], a[12:15], a[164:167], v[20:23]\n\t"
"ds_write_addtid_b32 v195 offset:28992\n\t"
"v_mfma_f32_16x16x32_bf16 v[4:7], a[0:3], a[148:151], 0\n\t"
"ds_read_b128 a[148:151], v224 offset:1280\n\t"
"v_cvt_pk_bf16_f32 v216, v216, v220\n\t"
"v_mfma_f32_16x16x32_bf16 v[4:7], a[4:7], a[164:167], v[4:7]\n\t"
"s_waitcnt lgkmcnt(9)\n\t"
"ds_write_addtid_b32 v200 offset:18592\n\t"
"v_cvt_pk_bf16_f32 v202, v202, v206\n\t"
"v_mfma_f32_16x16x32_bf16 v[8:11], a[0:3], a[152:155], 0\n\t"
"ds_read_b128 a[164:167], v224 offset:3328\n\t"
"v_cvt_pk_bf16_f32 v218, v218, v222\n\t"
"v_mfma_f32_16x16x32_bf16 v[8:11], a[4:7], a[168:171], v[8:11]\n\t"
"ds_write_addtid_b32 v219 offset:32064\n\t"
"v_cvt_pk_bf16_f32 v217, v217, v221\n\t"
"v_mfma_f32_16x16x32_bf16 v[24:27], a[8:11], a[152:155], 0\n\t"
"ds_write_addtid_b32 v217 offset:27936\n\t"
"v_cvt_pk_bf16_f32 v194, v194, v198\n\t"
"v_mfma_f32_16x16x32_bf16 v[24:27], a[12:15], a[168:171], v[24:27]\n\t"
"ds_write_addtid_b32 v216 offset:19616\n\t"
"v_cvt_pk_bf16_f32 v210, v210, v214\n\t"
"v_mfma_f32_16x16x32_bf16 v[40:43], a[16:19], a[152:155], 0\n\t"
"ds_read_b128 a[152:155], v224 offset:1536\n\t"
"v_mfma_f32_16x16x32_bf16 v[40:43], a[20:23], a[168:171], v[40:43]\n\t"
"ds_read_b128 a[168:171], v224 offset:3584\n\t"
"v_mfma_f32_16x16x32_bf16 v[44:47], a[16:19], a[156:159], 0\n\t"
"ds_write_addtid_b32 v218 offset:23744\n\t"
"v_mfma_f32_16x16x32_bf16 v[44:47], a[20:23], a[172:175], v[44:47]\n\t"
"ds_write_addtid_b32 v203 offset:31040\n\t"
"v_cvt_pk_bf16_f32 v192, v192, v196\n\t"
"v_mfma_f32_16x16x32_bf16 v[28:31], a[8:11], a[156:159], 0\n\t"
"ds_write_addtid_b32 v210 offset:21696\n\t"
"v_cvt_pk_bf16_f32 v208, v208, v212\n\t"
"v_mfma_f32_16x16x32_bf16 v[28:31], a[12:15], a[172:175], v[28:31]\n\t"
"ds_write_addtid_b32 v208 offset:17568\n\t"
"v_mfma_f32_16x16x32_bf16 v[12:15], a[0:3], a[156:159], 0\n\t"
"ds_write_addtid_b32 v202 offset:22720\n\t"
"v_mfma_f32_16x16x32_bf16 v[12:15], a[4:7], a[172:175], v[12:15]\n\t"
"s_waitcnt lgkmcnt(9)\n\t"
"ds_write_addtid_b32 v192 offset:16544\n\t"
"v_mfma_f32_16x16x32_bf16 v[0:3], a[24:27], a[144:147], v[0:3]\n\t"
"ds_write_addtid_b32 v209 offset:25888\n\t"
"v_mfma_f32_16x16x32_bf16 v[0:3], a[28:31], a[160:163], v[0:3]\n\t"
"ds_write_addtid_b32 v211 offset:30016\n\t"
"v_mfma_f32_16x16x32_bf16 v[16:19], a[32:35], a[144:147], v[16:19]\n\t"
"ds_write_addtid_b32 v194 offset:20672\n\t"
"v_mfma_f32_16x16x32_bf16 v[16:19], a[36:39], a[160:163], v[16:19]\n\t"
"ds_read_b128 a[156:159], v224 offset:1792\n\t"
"v_mfma_f32_16x16x32_bf16 v[32:35], a[40:43], a[144:147], v[32:35]\n\t"
"ds_read_b128 a[172:175], v224 offset:3840\n\t"
"v_mfma_f32_16x16x32_bf16 v[32:35], a[44:47], a[160:163], v[32:35]\n\t"
"ds_read_b64_tr_b16 a[196:197], v251 offset:256\n\t"
"v_mfma_f32_16x16x32_bf16 v[36:39], a[40:43], a[148:151], v[36:39]\n\t"
"ds_read_b64_tr_b16 a[202:203], v251 offset:128\n\t"
"v_mfma_f32_16x16x32_bf16 v[36:39], a[44:47], a[164:167], v[36:39]\n\t"
"ds_read_b64_tr_b16 a[200:201], v251 offset:0\n\t"
"v_mfma_f32_16x16x32_bf16 v[20:23], a[32:35], a[148:151], v[20:23]\n\t"
"ds_read_b64_tr_b16 a[204:205], v250 offset:256\n\t"
"v_mfma_f32_16x16x32_bf16 v[20:23], a[36:39], a[164:167], v[20:23]\n\t"
"v_mfma_f32_16x16x32_bf16 v[4:7], a[24:27], a[148:151], v[4:7]\n\t"
"v_mfma_f32_16x16x32_bf16 v[4:7], a[28:31], a[164:167], v[4:7]\n\t"
"s_waitcnt lgkmcnt(0)\n\t"
"s_barrier\n\t"
"v_mfma_f32_16x16x32_bf16 v[8:11], a[24:27], a[152:155], v[8:11]\n\t"
"ds_read_b128 a[160:163], v224 offset:18592\n\t"
"v_mfma_f32_16x16x32_bf16 v[8:11], a[28:31], a[168:171], v[8:11]\n\t"
"ds_read_b128 a[144:147], v224 offset:16544\n\t"
"v_mfma_f32_16x16x32_bf16 v[24:27], a[32:35], a[152:155], v[24:27]\n\t"
"ds_read_b128 a[148:151], v224 offset:16800\n\t"
"v_swap_b32 v236, v241\n\t"
"v_mfma_f32_16x16x32_bf16 v[24:27], a[36:39], a[168:171], v[24:27]\n\t"
"ds_read_b128 a[164:167], v224 offset:18848\n\t"
"v_mfma_f32_16x16x32_bf16 v[40:43], a[40:43], a[152:155], v[40:43]\n\t"
"v_swap_b32 v237, v242\n\t"
"v_mfma_f32_16x16x32_bf16 v[40:43], a[44:47], a[168:171], v[40:43]\n\t"
"v_swap_b32 v238, v243\n\t"
"v_add_u32 v248, 101632, v227\n\t"
"v_mfma_f32_16x16x32_bf16 v[44:47], a[40:43], a[156:159], v[44:47]\n\t"
"v_swap_b32 v239, v244\n\t"
"v_mfma_f32_16x16x32_bf16 v[44:47], a[44:47], a[172:175], v[44:47]\n\t"
"ds_write_b128 v254, a[244:247] offset:20736\n\t"
"v_mfma_f32_16x16x32_bf16 v[28:31], a[32:35], a[156:159], v[28:31]\n\t"
"v_swap_b32 v240, v245\n\t"
"v_mfma_f32_16x16x32_bf16 v[28:31], a[36:39], a[172:175], v[28:31]\n\t"
"v_mfma_f32_16x16x32_bf16 v[12:15], a[24:27], a[156:159], v[12:15]\n\t"
"v_add_u32 v225, v225, v253\n\t"
"v_mfma_f32_16x16x32_bf16 v[12:15], a[28:31], a[172:175], v[12:15]\n\t"
"ds_read_b128 a[156:159], v224 offset:17312\n\t"
"v_sub_u32 v253, 0, v253\n\t"
"v_mfma_f32_4x4x4_16b_bf16 v[200:203], a[220:221], a[178:179], 0\n\t"
"v_accvgpr_mov_b32 a228, a208\n\t"
"v_mfma_f32_4x4x4_16b_bf16 v[204:207], a[220:221], a[186:187], 0\n\t"
"v_mfma_f32_4x4x4_16b_bf16 v[216:219], a[220:221], a[194:195], 0\n\t"
"v_mfma_f32_4x4x4_16b_bf16 v[220:223], a[220:221], a[202:203], 0\n\t"
"ds_read_b128 a[152:155], v224 offset:17056\n\t"
"v_mfma_f32_4x4x4_16b_bf16 v[192:195], a[222:223], a[176:177], v[200:203]\n\t"
"v_xor_b32 v249, 32, v248\n\t"
"v_mfma_f32_4x4x4_16b_bf16 v[196:199], a[222:223], a[184:185], v[204:207]\n\t"
"v_mfma_f32_4x4x4_16b_bf16 v[200:203], a[222:223], a[180:181], v[200:203]\n\t"
"ds_read_b128 a[168:171], v224 offset:19104\n\t"
"v_mfma_f32_4x4x4_16b_bf16 v[204:207], a[222:223], a[188:189], v[204:207]\n\t"
"v_accvgpr_mov_b32 a208, a209\n\t"
"v_mfma_f32_4x4x4_16b_bf16 v[208:211], a[222:223], a[192:193], v[216:219]\n\t"
"v_xor_b32 v250, 64, v248\n\t"
"v_mfma_f32_4x4x4_16b_bf16 v[212:215], a[222:223], a[200:201], v[220:223]\n\t"
"v_xor_b32 v251, 96, v248\n\t"
"v_mfma_f32_4x4x4_16b_bf16 v[216:219], a[222:223], a[196:197], v[216:219]\n\t"
"ds_read_b128 a[172:175], v224 offset:19360\n\t"
"v_mfma_f32_4x4x4_16b_bf16 v[220:223], a[222:223], a[204:205], v[220:223]\n\t"
"s_waitcnt vmcnt(15) lgkmcnt(3)\n\t"
"buffer_load_dwordx4 a[16:19], off, s[28:31], s57 offset:0 sc1\n\t"
"v_mfma_f32_16x16x32_bf16 v[144:147], a[96:99], a[144:147], v[144:147]\n\t"
"s_nop 0\n\t"
"v_cvt_pk_bf16_f32 v193, -v193, -v197\n\t"
"v_cvt_pk_bf16_f32 v194, -v194, -v198\n\t"
"v_mfma_f32_16x16x32_bf16 v[144:147], a[100:103], a[160:163], v[144:147]\n\t"
"s_nop 0\n\t"
"ds_write_addtid_b32 v193 offset:4128\n\t"
"v_cvt_pk_bf16_f32 v192, -v192, -v196\n\t"
"v_mfma_f32_16x16x32_bf16 v[160:163], a[104:107], a[144:147], v[160:163]\n\t"
"s_nop 0\n\t"
"ds_write_addtid_b32 v194 offset:1024\n\t"
"v_cvt_pk_bf16_f32 v200, v200, v204\n\t"
"v_mfma_f32_16x16x32_bf16 v[160:163], a[108:111], a[160:163], v[160:163]\n\t"
"buffer_load_dwordx4 a[20:23], off, s[28:31], s57 offset:1024 sc1\n\t"
"v_mfma_f32_16x16x32_bf16 v[176:179], a[112:115], a[144:147], v[176:179]\n\t"
"s_nop 0\n\t"
"ds_read_b128 a[144:147], v224 offset:17568\n\t"
"v_cvt_pk_bf16_f32 v211, -v211, -v215\n\t"
"v_mfma_f32_16x16x32_bf16 v[176:179], a[116:119], a[160:163], v[176:179]\n\t"
"buffer_load_dwordx4 a[12:15], off, s[84:87], s57 offset:3072 sc1\n\t"
"v_mfma_f32_16x16x32_bf16 v[180:183], a[112:115], a[148:151], v[180:183]\n\t"
"s_nop 0\n\t"
"ds_read_b128 a[160:163], v224 offset:19616\n\t"
"v_cvt_pk_bf16_f32 v208, -v208, -v212\n\t"
"v_mfma_f32_16x16x32_bf16 v[180:183], a[116:119], a[164:167], v[180:183]\n\t"
"buffer_load_dwordx4 a[8:11], off, s[84:87], s57 offset:2048 sc1\n\t"
"v_mfma_f32_16x16x32_bf16 v[164:167], a[104:107], a[148:151], v[164:167]\n\t"
"s_nop 0\n\t"
"ds_write_addtid_b32 v211 offset:7200\n\t"
"v_cvt_pk_bf16_f32 v201, v201, v205\n\t"
"v_mfma_f32_16x16x32_bf16 v[164:167], a[108:111], a[164:167], v[164:167]\n\t"
"s_nop 0\n\t"
"ds_write_addtid_b32 v208 offset:2048\n\t"
"v_cvt_pk_bf16_f32 v209, -v209, -v213\n\t"
"v_mfma_f32_16x16x32_bf16 v[148:151], a[96:99], a[148:151], v[148:151]\n\t"
"s_nop 0\n\t"
"ds_read_b128 a[148:151], v224 offset:17824\n\t"
"v_cvt_pk_bf16_f32 v218, v218, v222\n\t"
"v_mfma_f32_16x16x32_bf16 v[148:151], a[100:103], a[164:167], v[148:151]\n\t"
"s_waitcnt lgkmcnt(6)\n\t"
"buffer_load_dwordx4 a[0:3], off, s[84:87], s57 offset:0 sc1\n\t"
"v_mfma_f32_16x16x32_bf16 v[152:155], a[96:99], a[152:155], v[152:155]\n\t"
"s_nop 0\n\t"
"ds_read_b128 a[164:167], v224 offset:19872\n\t"
"v_cvt_pk_bf16_f32 v203, v203, v207\n\t"
"v_mfma_f32_16x16x32_bf16 v[152:155], a[100:103], a[168:171], v[152:155]\n\t"
"s_nop 0\n\t"
"ds_write_addtid_b32 v218 offset:11392\n\t"
"v_cvt_pk_bf16_f32 v219, v219, v223\n\t"
"v_mfma_f32_16x16x32_bf16 v[168:171], a[104:107], a[152:155], v[168:171]\n\t"
"s_nop 0\n\t"
"ds_write_addtid_b32 v200 offset:8320\n\t"
"v_cvt_pk_bf16_f32 v216, v216, v220\n\t"
"v_mfma_f32_16x16x32_bf16 v[168:171], a[108:111], a[168:171], v[168:171]\n\t"
"buffer_load_dwordx4 a[4:7], off, s[84:87], s57 offset:1024 sc1\n\t"
"v_mfma_f32_16x16x32_bf16 v[184:187], a[112:115], a[152:155], v[184:187]\n\t"
"s_nop 0\n\t"
"ds_read_b128 a[152:155], v224 offset:18080\n\t"
"v_cvt_pk_bf16_f32 v195, -v195, -v199\n\t"
"v_mfma_f32_16x16x32_bf16 v[184:187], a[116:119], a[168:171], v[184:187]\n\t"
"s_nop 0\n\t"
"ds_read_b128 a[168:171], v224 offset:20128\n\t"
"v_cvt_pk_bf16_f32 v217, v217, v221\n\t"
"v_mfma_f32_16x16x32_bf16 v[188:191], a[112:115], a[156:159], v[188:191]\n\t"
"s_nop 0\n\t"
"ds_write_addtid_b32 v219 offset:15520\n\t"
"v_cvt_pk_bf16_f32 v202, v202, v206\n\t"
"v_mfma_f32_16x16x32_bf16 v[188:191], a[116:119], a[172:175], v[188:191]\n\t"
"buffer_load_dwordx4 a[40:43], off, s[28:31], s63 offset:0 sc1\n\t"
"v_mfma_f32_16x16x32_bf16 v[172:175], a[104:107], a[156:159], v[172:175]\n\t"
"s_nop 0\n\t"
"ds_write_addtid_b32 v209 offset:6176\n\t"
"v_cvt_pk_bf16_f32 v210, -v210, -v214\n\t"
"v_mfma_f32_16x16x32_bf16 v[172:175], a[108:111], a[172:175], v[172:175]\n\t"
"s_nop 0\n\t"
"ds_write_addtid_b32 v202 offset:9344\n\t"
"ds_write_addtid_b32 v216 offset:10368\n\t"
"v_mfma_f32_16x16x32_bf16 v[156:159], a[96:99], a[156:159], v[156:159]\n\t"
"s_nop 0\n\t"
"ds_write_addtid_b32 v203 offset:13472\n\t"
"v_mfma_f32_16x16x32_bf16 v[156:159], a[100:103], a[172:175], v[156:159]\n\t"
"s_waitcnt vmcnt(15) lgkmcnt(8)\n\t"
"buffer_load_dwordx4 a[44:47], off, s[28:31], s63 offset:1024 sc1\n\t"
"v_mfma_f32_16x16x32_bf16 v[144:147], a[120:123], a[144:147], v[144:147]\n\t"
"s_nop 0\n\t"
"ds_write_addtid_b32 v201 offset:12448\n\t"
"ds_write_addtid_b32 v210 offset:3072\n\t"
"v_mfma_f32_16x16x32_bf16 v[144:147], a[124:127], a[160:163], v[144:147]\n\t"
"s_nop 0\n\t"
"ds_write_addtid_b32 v217 offset:14496\n\t"
"v_mfma_f32_16x16x32_bf16 v[160:163], a[128:131], a[144:147], v[160:163]\n\t"
"s_nop 0\n\t"
"ds_write_addtid_b32 v192 offset:0\n\t"
"ds_write_addtid_b32 v195 offset:5152\n\t"
"v_mfma_f32_16x16x32_bf16 v[160:163], a[132:135], a[160:163], v[160:163]\n\t"
"buffer_load_dwordx4 a[36:39], off, s[84:87], s63 offset:3072 sc1\n\t"
"v_mfma_f32_16x16x32_bf16 v[176:179], a[136:139], a[144:147], v[176:179]\n\t"
"s_nop 0\n\t"
"ds_read_b128 a[156:159], v224 offset:18336\n\t"
"v_mfma_f32_16x16x32_bf16 v[176:179], a[140:143], a[160:163], v[176:179]\n\t"
"buffer_load_dwordx4 a[32:35], off, s[84:87], s63 offset:2048 sc1\n\t"
"v_mfma_f32_16x16x32_bf16 v[180:183], a[136:139], a[148:151], v[180:183]\n\t"
"s_nop 0\n\t"
"ds_read_b128 a[172:175], v224 offset:20384\n\t"
"v_mfma_f32_16x16x32_bf16 v[180:183], a[140:143], a[164:167], v[180:183]\n\t"
"buffer_load_dwordx4 a[24:27], off, s[84:87], s63 offset:0 sc1\n\t"
"v_mfma_f32_16x16x32_bf16 v[164:167], a[128:131], a[148:151], v[164:167]\n\t"
"Lpad7_%=:\n\t"
"s_cbranch_execz Lret0_%=\n\t"
"v_mfma_f32_16x16x32_bf16 v[164:167], a[132:135], a[164:167], v[164:167]\n\t"
"buffer_load_dwordx4 a[28:31], off, s[84:87], s63 offset:1024 sc1\n\t"
"v_mfma_f32_16x16x32_bf16 v[148:151], a[120:123], a[148:151], v[148:151]\n\t"
"v_accvgpr_mov_b32 a209, a228\n\t"
"v_mfma_f32_16x16x32_bf16 v[148:151], a[124:127], a[164:167], v[148:151]\n\t"
"s_waitcnt vmcnt(13) lgkmcnt(4)\n\t"
"s_waitcnt lgkmcnt(0)\n\t"
"s_barrier\n\t"
"v_mfma_f32_16x16x32_bf16 v[152:155], a[120:123], a[152:155], v[152:155]\n\t"
"s_add_u32 m0, s26, 56192\n\t"
"ds_read_b128 a[144:147], v224 offset:0\n\t"
"ds_read_b128 a[160:163], v224 offset:2048\n\t"
"v_mfma_f32_16x16x32_bf16 v[152:155], a[124:127], a[168:171], v[152:155]\n\t"
"buffer_load_dwordx4 v236, s[12:15], s22 offen offset:128 nt lds\n\t"
"v_mfma_f32_16x16x32_bf16 v[168:171], a[128:131], a[152:155], v[168:171]\n\t"
"ds_read_b128 a[148:151], v224 offset:256\n\t"
"v_mfma_f32_16x16x32_bf16 v[168:171], a[132:135], a[168:171], v[168:171]\n\t"
"ds_read_b128 a[164:167], v224 offset:2304\n\t"
"v_mfma_f32_16x16x32_bf16 v[184:187], a[136:139], a[152:155], v[184:187]\n\t"
"s_nop 0\n\t"
"ds_read_b64_tr_b16 a[176:177], v248 offset:0\n\t"
"ds_read_b64_tr_b16 a[178:179], v248 offset:512\n\t"
"v_mfma_f32_16x16x32_bf16 v[184:187], a[140:143], a[168:171], v[184:187]\n\t"
"buffer_store_dwordx4 a[232:235], v233, s[16:19], s25 offen nt\n\t"
"v_mfma_f32_16x16x32_bf16 v[188:191], a[136:139], a[156:159], v[188:191]\n\t"
"s_nop 0\n\t"
"ds_read_b128 a[152:155], v224 offset:512\n\t"
"ds_read_b64_tr_b16 a[180:181], v249 offset:0\n\t"
"v_mfma_f32_16x16x32_bf16 v[188:191], a[140:143], a[172:175], v[188:191]\n\t"
"ds_read_b64_tr_b16 a[182:183], v249 offset:512\n\t"
"v_mfma_f32_16x16x32_bf16 v[172:175], a[128:131], a[156:159], v[172:175]\n\t"
"buffer_load_dwordx4 v237, s[12:15], s22 offen offset:1152 nt lds\n\t"
"v_mfma_f32_16x16x32_bf16 v[172:175], a[132:135], a[172:175], v[172:175]\n\t"
"s_nop 0\n\t"
"ds_read_b64_tr_b16 a[184:185], v250 offset:0\n\t"
"ds_read_b64_tr_b16 a[186:187], v250 offset:512\n\t"
"v_mfma_f32_16x16x32_bf16 v[156:159], a[120:123], a[156:159], v[156:159]\n\t"
"s_nop 0\n\t"
"ds_read_b128 a[168:171], v224 offset:2560\n\t"
"ds_read_b128 a[156:159], v224 offset:768\n\t"
"v_mfma_f32_16x16x32_bf16 v[156:159], a[124:127], a[172:175], v[156:159]\n\t"
"s_waitcnt vmcnt(9) lgkmcnt(11)\n\t"
".p2align 3\n\t"
"buffer_store_dwordx4 a[236:239], v234, s[16:19], s25 offen nt\n\t"
"v_pk_add_f32 v[144:145], v[48:49], v[144:145] neg_lo:[0,1] neg_hi:[0,1]\n\t"
"v_pk_add_f32 v[146:147], v[50:51], v[146:147] neg_lo:[0,1] neg_hi:[0,1]\n\t"
"v_pk_fma_f32 v[48:49], v[144:145], s[90:91], v[48:49] neg_lo:[1,0,0] neg_hi:[1,0,0]\n\t"
"v_pk_fma_f32 v[50:51], v[146:147], s[90:91], v[50:51] neg_lo:[1,0,0] neg_hi:[1,0,0]\n\t"
"v_pk_add_f32 v[160:161], v[64:65], v[160:161] neg_lo:[0,1] neg_hi:[0,1]\n\t"
"v_pk_add_f32 v[162:163], v[66:67], v[162:163] neg_lo:[0,1] neg_hi:[0,1]\n\t"
"v_pk_fma_f32 v[64:65], v[160:161], s[90:91], v[64:65] neg_lo:[1,0,0] neg_hi:[1,0,0]\n\t"
"v_pk_fma_f32 v[66:67], v[162:163], s[90:91], v[66:67] neg_lo:[1,0,0] neg_hi:[1,0,0]\n\t"
"v_pk_add_f32 v[176:177], v[80:81], v[176:177] neg_lo:[0,1] neg_hi:[0,1]\n\t"
"v_pk_add_f32 v[178:179], v[82:83], v[178:179] neg_lo:[0,1] neg_hi:[0,1]\n\t"
"v_pk_fma_f32 v[80:81], v[176:177], s[90:91], v[80:81] neg_lo:[1,0,0] neg_hi:[1,0,0]\n\t"
"v_pk_fma_f32 v[82:83], v[178:179], s[90:91], v[82:83] neg_lo:[1,0,0] neg_hi:[1,0,0]\n\t"
"v_pk_add_f32 v[180:181], v[84:85], v[180:181] neg_lo:[0,1] neg_hi:[0,1]\n\t"
"v_pk_add_f32 v[182:183], v[86:87], v[182:183] neg_lo:[0,1] neg_hi:[0,1]\n\t"
"v_pk_fma_f32 v[84:85], v[180:181], s[90:91], v[84:85] neg_lo:[1,0,0] neg_hi:[1,0,0]\n\t"
"v_pk_fma_f32 v[86:87], v[182:183], s[90:91], v[86:87] neg_lo:[1,0,0] neg_hi:[1,0,0]\n\t"
"v_pk_add_f32 v[164:165], v[68:69], v[164:165] neg_lo:[0,1] neg_hi:[0,1]\n\t"
"v_pk_add_f32 v[166:167], v[70:71], v[166:167] neg_lo:[0,1] neg_hi:[0,1]\n\t"
"v_pk_fma_f32 v[68:69], v[164:165], s[90:91], v[68:69] neg_lo:[1,0,0] neg_hi:[1,0,0]\n\t"
"v_pk_fma_f32 v[70:71], v[166:167], s[90:91], v[70:71] neg_lo:[1,0,0] neg_hi:[1,0,0]\n\t"
"v_pk_add_f32 v[148:149], v[52:53], v[148:149] neg_lo:[0,1] neg_hi:[0,1]\n\t"
"v_pk_add_f32 v[150:151], v[54:55], v[150:151] neg_lo:[0,1] neg_hi:[0,1]\n\t"
"v_pk_fma_f32 v[52:53], v[148:149], s[90:91], v[52:53] neg_lo:[1,0,0] neg_hi:[1,0,0]\n\t"
"v_pk_fma_f32 v[54:55], v[150:151], s[90:91], v[54:55] neg_lo:[1,0,0] neg_hi:[1,0,0]\n\t"
"v_pk_add_f32 v[152:153], v[56:57], v[152:153] neg_lo:[0,1] neg_hi:[0,1]\n\t"
"v_pk_add_f32 v[154:155], v[58:59], v[154:155] neg_lo:[0,1] neg_hi:[0,1]\n\t"
"v_pk_fma_f32 v[56:57], v[152:153], s[90:91], v[56:57] neg_lo:[1,0,0] neg_hi:[1,0,0]\n\t"
"v_pk_fma_f32 v[58:59], v[154:155], s[90:91], v[58:59] neg_lo:[1,0,0] neg_hi:[1,0,0]\n\t"
"v_pk_add_f32 v[168:169], v[72:73], v[168:169] neg_lo:[0,1] neg_hi:[0,1]\n\t"
"v_pk_add_f32 v[170:171], v[74:75], v[170:171] neg_lo:[0,1] neg_hi:[0,1]\n\t"
"v_pk_fma_f32 v[72:73], v[168:169], s[90:91], v[72:73] neg_lo:[1,0,0] neg_hi:[1,0,0]\n\t"
"v_pk_fma_f32 v[74:75], v[170:171], s[90:91], v[74:75] neg_lo:[1,0,0] neg_hi:[1,0,0]\n\t"
"v_pk_add_f32 v[184:185], v[88:89], v[184:185] neg_lo:[0,1] neg_hi:[0,1]\n\t"
"v_pk_add_f32 v[186:187], v[90:91], v[186:187] neg_lo:[0,1] neg_hi:[0,1]\n\t"
"v_pk_fma_f32 v[88:89], v[184:185], s[90:91], v[88:89] neg_lo:[1,0,0] neg_hi:[1,0,0]\n\t"
"v_pk_fma_f32 v[90:91], v[186:187], s[90:91], v[90:91] neg_lo:[1,0,0] neg_hi:[1,0,0]\n\t"
"v_pk_add_f32 v[188:189], v[92:93], v[188:189] neg_lo:[0,1] neg_hi:[0,1]\n\t"
"v_pk_add_f32 v[190:191], v[94:95], v[190:191] neg_lo:[0,1] neg_hi:[0,1]\n\t"
"v_pk_fma_f32 v[92:93], v[188:189], s[90:91], v[92:93] neg_lo:[1,0,0] neg_hi:[1,0,0]\n\t"
"v_pk_fma_f32 v[94:95], v[190:191], s[90:91], v[94:95] neg_lo:[1,0,0] neg_hi:[1,0,0]\n\t"
"v_pk_add_f32 v[172:173], v[76:77], v[172:173] neg_lo:[0,1] neg_hi:[0,1]\n\t"
"v_pk_add_f32 v[174:175], v[78:79], v[174:175] neg_lo:[0,1] neg_hi:[0,1]\n\t"
"v_pk_fma_f32 v[76:77], v[172:173], s[90:91], v[76:77] neg_lo:[1,0,0] neg_hi:[1,0,0]\n\t"
"v_pk_fma_f32 v[78:79], v[174:175], s[90:91], v[78:79] neg_lo:[1,0,0] neg_hi:[1,0,0]\n\t"
"v_pk_add_f32 v[156:157], v[60:61], v[156:157] neg_lo:[0,1] neg_hi:[0,1]\n\t"
"v_pk_add_f32 v[158:159], v[62:63], v[158:159] neg_lo:[0,1] neg_hi:[0,1]\n\t"
"v_pk_fma_f32 v[60:61], v[156:157], s[90:91], v[60:61] neg_lo:[1,0,0] neg_hi:[1,0,0]\n\t"
"v_pk_fma_f32 v[62:63], v[158:159], s[90:91], v[62:63] neg_lo:[1,0,0] neg_hi:[1,0,0]\n\t"
"v_mfma_f32_16x16x32_bf16 v[48:51], a[0:3], a[144:147], v[48:51]\n\t"
"s_nop 0\n\t"
"ds_read_b128 a[172:175], v224 offset:2816\n\t"
"ds_read_b64_tr_b16 a[188:189], v251 offset:0\n\t"
"v_mfma_f32_16x16x32_bf16 v[48:51], a[4:7], a[160:163], v[48:51]\n\t"
"s_nop 0\n\t"
"ds_read_b64_tr_b16 a[190:191], v251 offset:512\n\t"
"ds_read_b64_tr_b16 a[192:193], v248 offset:1152\n\t"
"v_mfma_f32_16x16x32_bf16 v[64:67], a[8:11], a[144:147], v[64:67]\n\t"
"ds_read_b64_tr_b16 a[194:195], v248 offset:1664\n\t"
"v_mfma_f32_16x16x32_bf16 v[64:67], a[12:15], a[160:163], v[64:67]\n\t"
"buffer_load_dwordx4 v238, s[12:15], s22 offen offset:2176 nt lds\n\t"
"v_mfma_f32_16x16x32_bf16 v[80:83], a[16:19], a[144:147], v[80:83]\n\t"
"s_nop 0\n\t"
"ds_read_b64_tr_b16 a[196:197], v249 offset:1152\n\t"
"ds_read_b64_tr_b16 a[198:199], v249 offset:1664\n\t"
"v_mfma_f32_16x16x32_bf16 v[80:83], a[20:23], a[160:163], v[80:83]\n\t"
"s_waitcnt lgkmcnt(11)\n\t"
"ds_read_b128 a[160:163], v224 offset:3072\n\t"
"v_mfma_f32_16x16x32_bf16 v[192:195], a[216:219], a[176:179], 0\n\t"
"ds_read_b64_tr_b16 a[200:201], v250 offset:1152\n\t"
"v_mfma_f32_16x16x32_bf16 v[196:199], a[216:219], a[180:183], 0\n\t"
"s_waitcnt lgkmcnt(11)\n\t"
"ds_read_b64_tr_b16 a[202:203], v250 offset:1664\n\t"
"v_mfma_f32_16x16x32_bf16 v[200:203], a[216:219], a[184:187], 0\n\t"
"s_waitcnt lgkmcnt(7)\n\t"
"ds_read_b64_tr_b16 a[204:205], v251 offset:1152\n\t"
"ds_read_b64_tr_b16 a[206:207], v251 offset:1664\n\t"
"v_mfma_f32_16x16x32_bf16 v[204:207], a[216:219], a[188:191], 0\n\t"
"s_waitcnt lgkmcnt(7)\n\t"
"buffer_load_dwordx4 v239, s[12:15], s22 offen offset:3200 nt lds\n\t"
"v_mfma_f32_16x16x32_bf16 v[208:211], a[216:219], a[192:195], 0\n\t"
"s_waitcnt lgkmcnt(5)\n\t"
"ds_read_b64_tr_b16 a[176:177], v248 offset:42624\n\t"
"ds_read_b64_tr_b16 a[178:179], v248 offset:43136\n\t"
"v_mfma_f32_16x16x32_bf16 v[212:215], a[216:219], a[196:199], 0\n\t"
"s_waitcnt lgkmcnt(4)\n\t"
"ds_read_b64_tr_b16 a[180:181], v249 offset:42624\n\t"
"v_mfma_f32_16x16x32_bf16 v[216:219], a[216:219], a[200:203], 0\n\t"
"s_waitcnt lgkmcnt(3)\n\t"
"ds_read_b64_tr_b16 a[182:183], v249 offset:43136\n\t"
"ds_read_b64_tr_b16 a[184:185], v250 offset:42624\n\t"
"v_mfma_f32_16x16x32_bf16 v[220:223], a[216:219], a[204:207], 0\n\t"
"s_mov_b64 exec, s[36:37]\n\t"
"buffer_load_dwordx4 v240, s[12:15], s22 offen offset:3712 nt lds\n\t"
"s_mov_b64 exec, -1\n\t"
"v_mfma_f32_16x16x32_bf16 v[84:87], a[16:19], a[148:151], v[84:87]\n\t"
"s_lshl_b32 m0, s73, 8\n\t"
"ds_read_b64_tr_b16 a[186:187], v250 offset:43136\n\t"
"v_cvt_pk_bf16_f32 v193, v193, v197\n\t"
"v_mfma_f32_16x16x32_bf16 v[84:87], a[20:23], a[164:167], v[84:87]\n\t"
"ds_read_b128 a[144:147], v224 offset:1024\n\t"
"v_cvt_pk_bf16_f32 v201, v201, v205\n\t"
"v_mfma_f32_16x16x32_bf16 v[68:71], a[8:11], a[148:151], v[68:71]\n\t"
"ds_read_b64_tr_b16 a[188:189], v251 offset:42624\n\t"
"v_cvt_pk_bf16_f32 v200, v200, v204\n\t"
"v_mfma_f32_16x16x32_bf16 v[68:71], a[12:15], a[164:167], v[68:71]\n\t"
"ds_read_b64_tr_b16 a[190:191], v251 offset:43136\n\t"
"v_cvt_pk_bf16_f32 v194, v194, v198\n\t"
"v_mfma_f32_16x16x32_bf16 v[52:55], a[0:3], a[148:151], v[52:55]\n\t"
"ds_read_b64_tr_b16 a[192:193], v248 offset:21888\n\t"
"v_cvt_pk_bf16_f32 v210, -v210, -v214\n\t"
"v_mfma_f32_16x16x32_bf16 v[52:55], a[4:7], a[164:167], v[52:55]\n\t"
"ds_read_b128 a[148:151], v224 offset:1280\n\t"
"v_cvt_pk_bf16_f32 v211, -v211, -v215\n\t"
"v_mfma_f32_16x16x32_bf16 v[56:59], a[0:3], a[152:155], v[56:59]\n\t"
"ds_read_b128 a[164:167], v224 offset:3328\n\t"
"v_cvt_pk_bf16_f32 v195, v195, v199\n\t"
"v_mfma_f32_16x16x32_bf16 v[56:59], a[4:7], a[168:171], v[56:59]\n\t"
"ds_write_addtid_b32 v195 offset:28992\n\t"
"v_cvt_pk_bf16_f32 v209, -v209, -v213\n\t"
"v_mfma_f32_16x16x32_bf16 v[72:75], a[8:11], a[152:155], v[72:75]\n\t"
"ds_write_addtid_b32 v211 offset:30016\n\t"
"v_cvt_pk_bf16_f32 v217, -v217, -v221\n\t"
"v_mfma_f32_16x16x32_bf16 v[72:75], a[12:15], a[168:171], v[72:75]\n\t"
"s_nop 0\n\t"
"ds_write_addtid_b32 v193 offset:24864\n\t"
"ds_write_addtid_b32 v210 offset:21696\n\t"
"v_mfma_f32_16x16x32_bf16 v[88:91], a[16:19], a[152:155], v[88:91]\n\t"
"ds_read_b128 a[152:155], v224 offset:1536\n\t"
"v_cvt_pk_bf16_f32 v208, -v208, -v212\n\t"
"v_mfma_f32_16x16x32_bf16 v[88:91], a[20:23], a[168:171], v[88:91]\n\t"
"ds_read_b128 a[168:171], v224 offset:3584\n\t"
"v_cvt_pk_bf16_f32 v202, v202, v206\n\t"
"v_mfma_f32_16x16x32_bf16 v[92:95], a[16:19], a[156:159], v[92:95]\n\t"
"ds_write_addtid_b32 v217 offset:27936\n\t"
"v_cvt_pk_bf16_f32 v218, -v218, -v222\n\t"
"v_mfma_f32_16x16x32_bf16 v[92:95], a[20:23], a[172:175], v[92:95]\n\t"
"ds_write_addtid_b32 v202 offset:22720\n\t"
"v_cvt_pk_bf16_f32 v219, -v219, -v223\n\t"
"v_mfma_f32_16x16x32_bf16 v[76:79], a[8:11], a[156:159], v[76:79]\n\t"
"s_nop 0\n\t"
"ds_write_addtid_b32 v200 offset:18592\n\t"
"ds_write_addtid_b32 v218 offset:23744\n\t"
"v_mfma_f32_16x16x32_bf16 v[76:79], a[12:15], a[172:175], v[76:79]\n\t"
"s_nop 0\n\t"
"ds_write_addtid_b32 v208 offset:17568\n\t"
"ds_write_addtid_b32 v209 offset:25888\n\t"
"v_mfma_f32_16x16x32_bf16 v[60:63], a[0:3], a[156:159], v[60:63]\n\t"
"ds_read_b128 a[156:159], v224 offset:1792\n\t"
"v_cvt_pk_bf16_f32 v216, -v216, -v220\n\t"
"v_mfma_f32_16x16x32_bf16 v[60:63], a[4:7], a[172:175], v[60:63]\n\t"
"s_waitcnt vmcnt(7) lgkmcnt(12)\n\t"
"ds_write_addtid_b32 v219 offset:32064\n\t"
"v_cvt_pk_bf16_f32 v203, v203, v207\n\t"
"v_mfma_f32_16x16x32_bf16 v[144:147], a[24:27], a[144:147], v[144:147]\n\t"
"s_nop 0\n\t"
"ds_write_addtid_b32 v201 offset:26912\n\t"
"ds_write_addtid_b32 v216 offset:19616\n\t"
"v_mfma_f32_16x16x32_bf16 v[144:147], a[28:31], a[160:163], v[144:147]\n\t"
"ds_write_addtid_b32 v203 offset:31040\n\t"
"v_cvt_pk_bf16_f32 v192, v192, v196\n\t"
"v_mfma_f32_16x16x32_bf16 v[160:163], a[32:35], a[144:147], v[160:163]\n\t"
"s_nop 0\n\t"
"ds_write_addtid_b32 v192 offset:16544\n\t"
"ds_write_addtid_b32 v194 offset:20672\n\t"
"v_mfma_f32_16x16x32_bf16 v[160:163], a[36:39], a[160:163], v[160:163]\n\t"
"ds_read_b128 a[172:175], v224 offset:3840\n\t"
"v_mfma_f32_16x16x32_bf16 v[176:179], a[40:43], a[144:147], v[176:179]\n\t"
"ds_read_b64_tr_b16 a[194:195], v248 offset:22400\n\t"
"v_mfma_f32_16x16x32_bf16 v[176:179], a[44:47], a[160:163], v[176:179]\n\t"
"ds_read_b64_tr_b16 a[196:197], v249 offset:21888\n\t"
"v_mfma_f32_16x16x32_bf16 v[180:183], a[40:43], a[148:151], v[180:183]\n\t"
"ds_read_b64_tr_b16 a[198:199], v249 offset:22400\n\t"
"v_mfma_f32_16x16x32_bf16 v[180:183], a[44:47], a[164:167], v[180:183]\n\t"
"s_nop 0\n\t"
"ds_read_b64_tr_b16 a[200:201], v250 offset:21888\n\t"
"ds_read_b64_tr_b16 a[202:203], v250 offset:22400\n\t"
"v_mfma_f32_16x16x32_bf16 v[164:167], a[32:35], a[148:151], v[164:167]\n\t"
"s_nop 0\n\t"
"ds_read_b64_tr_b16 a[204:205], v251 offset:21888\n\t"
"ds_read_b64_tr_b16 a[206:207], v251 offset:22400\n\t"
"v_mfma_f32_16x16x32_bf16 v[164:167], a[36:39], a[164:167], v[164:167]\n\t"
"v_mfma_f32_16x16x32_bf16 v[148:151], a[24:27], a[148:151], v[148:151]\n\t"
"s_waitcnt lgkmcnt(7)\n\t"
"v_mfma_f32_16x16x32_bf16 v[148:151], a[28:31], a[164:167], v[148:151]\n\t"
"s_barrier\n\t"
"v_mfma_f32_16x16x32_bf16 v[152:155], a[24:27], a[152:155], v[152:155]\n\t"
"ds_read_b128 a[148:151], v224 offset:16800\n\t"
"v_add_u32 v248, 33280, v226\n\t"
"v_mfma_f32_16x16x32_bf16 v[152:155], a[28:31], a[168:171], v[152:155]\n\t"
"ds_read_b128 a[144:147], v224 offset:16544\n\t"
"v_xor_b32 v249, 32, v248\n\t"
"v_mfma_f32_16x16x32_bf16 v[168:171], a[32:35], a[152:155], v[168:171]\n\t"
"ds_read_b128 a[160:163], v224 offset:18592\n\t"
"v_mfma_f32_16x16x32_bf16 v[168:171], a[36:39], a[168:171], v[168:171]\n\t"
"ds_read_b128 a[164:167], v224 offset:18848\n\t"
"v_mfma_f32_16x16x32_bf16 v[184:187], a[40:43], a[152:155], v[184:187]\n\t"
"v_mfma_f32_16x16x32_bf16 v[184:187], a[44:47], a[168:171], v[184:187]\n\t"
"v_mfma_f32_16x16x32_bf16 v[188:191], a[40:43], a[156:159], v[188:191]\n\t"
"ds_read_b128 a[152:155], v224 offset:17056\n\t"
"v_mfma_f32_16x16x32_bf16 v[188:191], a[44:47], a[172:175], v[188:191]\n\t"
"ds_read_b128 a[168:171], v224 offset:19104\n\t"
"v_xor_b32 v250, 64, v248\n\t"
"v_mfma_f32_16x16x32_bf16 v[172:175], a[32:35], a[156:159], v[172:175]\n\t"
"v_mfma_f32_16x16x32_bf16 v[172:175], a[36:39], a[172:175], v[172:175]\n\t"
"v_mfma_f32_16x16x32_bf16 v[156:159], a[24:27], a[156:159], v[156:159]\n\t"
"ds_read_b128 a[156:159], v224 offset:17312\n\t"
"v_xor_b32 v251, 96, v248\n\t"
"v_mfma_f32_16x16x32_bf16 v[156:159], a[28:31], a[172:175], v[156:159]\n\t"
"ds_read_b128 a[172:175], v224 offset:19360\n\t"
"v_mfma_f32_16x16x32_bf16 v[192:195], a[216:219], a[176:179], 0\n\t"
"ds_read_b64_tr_b16 a[178:179], v248 offset:128\n\t"
"v_mfma_f32_16x16x32_bf16 v[196:199], a[216:219], a[180:183], 0\n\t"
"v_mfma_f32_16x16x32_bf16 v[200:203], a[216:219], a[184:187], 0\n\t"
"ds_read_b64_tr_b16 a[176:177], v248 offset:0\n\t"
"v_mfma_f32_16x16x32_bf16 v[204:207], a[216:219], a[188:191], 0\n\t"
"s_waitcnt lgkmcnt(6)\n\t"
"ds_read_b64_tr_b16 a[180:181], v249 offset:256\n\t"
"v_mfma_f32_16x16x32_bf16 v[208:211], a[216:219], a[192:195], 0\n\t"
"ds_read_b64_tr_b16 a[186:187], v249 offset:128\n\t"
"v_mfma_f32_16x16x32_bf16 v[212:215], a[216:219], a[196:199], 0\n\t"
"v_mfma_f32_16x16x32_bf16 v[216:219], a[216:219], a[200:203], 0\n\t"
"ds_read_b64_tr_b16 a[184:185], v249 offset:0\n\t"
"v_mfma_f32_16x16x32_bf16 v[220:223], a[216:219], a[204:207], 0\n\t"
"ds_read_b64_tr_b16 a[188:189], v248 offset:256\n\t"
"v_mfma_f32_16x16x32_bf16 v[0:3], a[48:51], a[144:147], v[0:3]\n\t"
"ds_read_b64_tr_b16 a[194:195], v250 offset:128\n\t"
"v_mfma_f32_16x16x32_bf16 v[0:3], a[52:55], a[160:163], v[0:3]\n\t"
"v_cvt_pk_bf16_f32 v195, -v195, -v199\n\t"
"v_cvt_pk_bf16_f32 v201, -v201, -v205\n\t"
"v_mfma_f32_16x16x32_bf16 v[16:19], a[56:59], a[144:147], v[16:19]\n\t"
"ds_read_b64_tr_b16 a[192:193], v250 offset:0\n\t"
"v_mfma_f32_16x16x32_bf16 v[16:19], a[60:63], a[160:163], v[16:19]\n\t"
"ds_read_b64_tr_b16 a[196:197], v251 offset:256\n\t"
"v_cvt_pk_bf16_f32 v200, -v200, -v204\n\t"
"v_mfma_f32_16x16x32_bf16 v[32:35], a[64:67], a[144:147], v[32:35]\n\t"
"ds_read_b128 a[144:147], v224 offset:17568\n\t"
"v_cvt_pk_bf16_f32 v209, -v209, -v213\n\t"
"v_mfma_f32_16x16x32_bf16 v[32:35], a[68:71], a[160:163], v[32:35]\n\t"
"v_cvt_pk_bf16_f32 v203, -v203, -v207\n\t"
"v_cvt_pk_bf16_f32 v217, -v217, -v221\n\t"
"v_mfma_f32_16x16x32_bf16 v[36:39], a[64:67], a[148:151], v[36:39]\n\t"
"ds_read_b128 a[160:163], v224 offset:19616\n\t"
"v_cvt_pk_bf16_f32 v219, -v219, -v223\n\t"
"v_mfma_f32_16x16x32_bf16 v[36:39], a[68:71], a[164:167], v[36:39]\n\t"
"ds_read_b64_tr_b16 a[202:203], v251 offset:128\n\t"
"v_cvt_pk_bf16_f32 v208, -v208, -v212\n\t"
"v_mfma_f32_16x16x32_bf16 v[20:23], a[56:59], a[148:151], v[20:23]\n\t"
"ds_write_addtid_b32 v203 offset:14496\n\t"
"v_cvt_pk_bf16_f32 v194, -v194, -v198\n\t"
"v_mfma_f32_16x16x32_bf16 v[20:23], a[60:63], a[164:167], v[20:23]\n\t"
"ds_write_addtid_b32 v200 offset:2048\n\t"
"v_cvt_pk_bf16_f32 v211, -v211, -v215\n\t"
"v_mfma_f32_16x16x32_bf16 v[4:7], a[48:51], a[148:151], v[4:7]\n\t"
"ds_read_b128 a[148:151], v224 offset:17824\n\t"
"v_mfma_f32_16x16x32_bf16 v[4:7], a[52:55], a[164:167], v[4:7]\n\t"
"s_waitcnt lgkmcnt(8)\n\t"
"ds_write_addtid_b32 v201 offset:10368\n\t"
"v_cvt_pk_bf16_f32 v218, -v218, -v222\n\t"
"v_mfma_f32_16x16x32_bf16 v[8:11], a[48:51], a[152:155], v[8:11]\n\t"
"ds_read_b128 a[164:167], v224 offset:19872\n\t"
"v_cvt_pk_bf16_f32 v210, -v210, -v214\n\t"
"v_mfma_f32_16x16x32_bf16 v[8:11], a[52:55], a[168:171], v[8:11]\n\t"
"ds_write_addtid_b32 v194 offset:4128\n\t"
"v_cvt_pk_bf16_f32 v202, -v202, -v206\n\t"
"v_mfma_f32_16x16x32_bf16 v[24:27], a[56:59], a[152:155], v[24:27]\n\t"
"ds_write_addtid_b32 v209 offset:9344\n\t"
"v_mfma_f32_16x16x32_bf16 v[24:27], a[60:63], a[168:171], v[24:27]\n\t"
"ds_write_addtid_b32 v202 offset:6176\n\t"
"v_mfma_f32_16x16x32_bf16 v[40:43], a[64:67], a[152:155], v[40:43]\n\t"
"ds_read_b128 a[152:155], v224 offset:18080\n\t"
"v_cvt_pk_bf16_f32 v193, -v193, -v197\n\t"
"v_mfma_f32_16x16x32_bf16 v[40:43], a[68:71], a[168:171], v[40:43]\n\t"
"ds_read_b128 a[168:171], v224 offset:20128\n\t"
"v_mfma_f32_16x16x32_bf16 v[44:47], a[64:67], a[156:159], v[44:47]\n\t"
"ds_write_addtid_b32 v210 offset:5152\n\t"
"v_cvt_pk_bf16_f32 v192, -v192, -v196\n\t"
"v_mfma_f32_16x16x32_bf16 v[44:47], a[68:71], a[172:175], v[44:47]\n\t"
"buffer_load_dwordx4 a[64:67], off, s[28:31], s42 offset:0 sc1\n\t"
"v_mfma_f32_16x16x32_bf16 v[28:31], a[56:59], a[156:159], v[28:31]\n\t"
"ds_write_addtid_b32 v217 offset:11392\n\t"
"v_mfma_f32_16x16x32_bf16 v[28:31], a[60:63], a[172:175], v[28:31]\n\t"
"s_xor_b32 s24, s24, 384\n\t"
"ds_write_addtid_b32 v195 offset:12448\n\t"
"ds_write_addtid_b32 v208 offset:1024\n\t"
"v_mfma_f32_16x16x32_bf16 v[12:15], a[48:51], a[156:159], v[12:15]\n\t"
"ds_write_addtid_b32 v219 offset:15520\n\t"
"v_mfma_f32_16x16x32_bf16 v[12:15], a[52:55], a[172:175], v[12:15]\n\t"
"s_waitcnt lgkmcnt(9)\n\t"
"buffer_load_dwordx4 a[68:71], off, s[28:31], s42 offset:1024 sc1\n\t"
"v_mfma_f32_16x16x32_bf16 v[96:99], a[72:75], a[144:147], v[0:3]\n\t"
"s_xor_b32 s20, s20, 384\n\t"
"ds_write_addtid_b32 v193 offset:8320\n\t"
"ds_write_addtid_b32 v211 offset:13472\n\t"
"v_mfma_f32_16x16x32_bf16 v[96:99], a[76:79], a[160:163], v[96:99]\n\t"
"ds_write_addtid_b32 v218 offset:7200\n\t"
"v_cvt_pk_bf16_f32 v216, -v216, -v220\n\t"
"v_mfma_f32_16x16x32_bf16 v[112:115], a[80:83], a[144:147], v[16:19]\n\t"
"s_xor_b32 s27, s27, 384\n\t"
"ds_write_addtid_b32 v192 offset:0\n\t"
"ds_write_addtid_b32 v216 offset:3072\n\t"
"v_mfma_f32_16x16x32_bf16 v[112:115], a[84:87], a[160:163], v[112:115]\n\t"
"buffer_load_dwordx4 a[60:63], off, s[84:87], s42 offset:3072 sc1\n\t"
"v_mfma_f32_16x16x32_bf16 v[128:131], a[88:91], a[144:147], v[32:35]\n\t"
"ds_read_b128 a[156:159], v224 offset:18336\n\t"
"v_mfma_f32_16x16x32_bf16 v[128:131], a[92:95], a[160:163], v[128:131]\n\t"
"buffer_load_dwordx4 a[56:59], off, s[84:87], s42 offset:2048 sc1\n\t"
"v_mfma_f32_16x16x32_bf16 v[132:135], a[88:91], a[148:151], v[36:39]\n\t"
"ds_read_b128 a[172:175], v224 offset:20384\n\t"
"v_mfma_f32_16x16x32_bf16 v[132:135], a[92:95], a[164:167], v[132:135]\n\t"
"buffer_load_dwordx4 a[48:51], off, s[84:87], s42 offset:0 sc1\n\t"
"v_mfma_f32_16x16x32_bf16 v[116:119], a[80:83], a[148:151], v[20:23]\n\t"
"s_xor_b32 s25, s25, 384\n\t"
"ds_read_b64_tr_b16 a[200:201], v251 offset:0\n\t"
"ds_read_b64_tr_b16 a[204:205], v250 offset:256\n\t"
"v_mfma_f32_16x16x32_bf16 v[116:119], a[84:87], a[164:167], v[116:119]\n\t"
"buffer_load_dwordx4 a[52:55], off, s[84:87], s42 offset:1024 sc1\n\t"
"v_mfma_f32_16x16x32_bf16 v[100:103], a[72:75], a[148:151], v[4:7]\n\t"
"v_mfma_f32_16x16x32_bf16 v[100:103], a[76:79], a[164:167], v[100:103]\n\t"
"s_waitcnt lgkmcnt(0)\n\t"
"s_barrier\n\t"
"v_mfma_f32_16x16x32_bf16 v[104:107], a[72:75], a[152:155], v[8:11]\n\t"
"ds_read_b128 a[160:163], v224 offset:2048\n\t"
"v_mfma_f32_16x16x32_bf16 v[104:107], a[76:79], a[168:171], v[104:107]\n\t"
"ds_read_b128 a[144:147], v224 offset:0\n\t"
"v_mfma_f32_16x16x32_bf16 v[120:123], a[80:83], a[152:155], v[24:27]\n\t"
"ds_read_b128 a[148:151], v224 offset:256\n\t"
"v_mfma_f32_16x16x32_bf16 v[120:123], a[84:87], a[168:171], v[120:123]\n\t"
"ds_read_b128 a[164:167], v224 offset:2304\n\t"
"v_mfma_f32_16x16x32_bf16 v[136:139], a[88:91], a[152:155], v[40:43]\n\t"
"v_mfma_f32_16x16x32_bf16 v[136:139], a[92:95], a[168:171], v[136:139]\n\t"
"ds_write_b128 v254, a[240:243] offset:0\n\t"
"v_mfma_f32_16x16x32_bf16 v[140:143], a[88:91], a[156:159], v[44:47]\n\t"
"v_mfma_f32_16x16x32_bf16 v[140:143], a[92:95], a[172:175], v[140:143]\n\t"
"buffer_load_dwordx4 a[88:91], off, s[28:31], s48 offset:0 sc1\n\t"
"v_mfma_f32_16x16x32_bf16 v[124:127], a[80:83], a[156:159], v[28:31]\n\t"
"v_mfma_f32_16x16x32_bf16 v[124:127], a[84:87], a[172:175], v[124:127]\n\t"
"buffer_load_dwordx4 a[92:95], off, s[28:31], s48 offset:1024 sc1\n\t"
"v_mfma_f32_16x16x32_bf16 v[108:111], a[72:75], a[156:159], v[12:15]\n\t"
"ds_read_b128 a[152:155], v224 offset:512\n\t"
"v_mfma_f32_16x16x32_bf16 v[108:111], a[76:79], a[172:175], v[108:111]\n\t"
"buffer_load_dwordx4 a[84:87], off, s[84:87], s48 offset:3072 sc1\n\t"
"v_mfma_f32_4x4x4_16b_bf16 v[200:203], a[220:221], a[178:179], 0\n\t"
"ds_read_b128 a[168:171], v224 offset:2560\n\t"
"v_mfma_f32_4x4x4_16b_bf16 v[204:207], a[220:221], a[186:187], 0\n\t"
"ds_read_b64_tr_b16 a[178:179], v248 offset:41600\n\t"
"v_mfma_f32_4x4x4_16b_bf16 v[216:219], a[220:221], a[194:195], 0\n\t"
"ds_read_b128 a[156:159], v224 offset:768\n\t"
"v_mfma_f32_4x4x4_16b_bf16 v[220:223], a[220:221], a[202:203], 0\n\t"
"v_mfma_f32_4x4x4_16b_bf16 v[192:195], a[222:223], a[176:177], v[200:203]\n\t"
"ds_read_b128 a[172:175], v224 offset:2816\n\t"
"v_mfma_f32_4x4x4_16b_bf16 v[196:199], a[222:223], a[184:185], v[204:207]\n\t"
"ds_read_b64_tr_b16 a[186:187], v249 offset:41600\n\t"
"v_mfma_f32_4x4x4_16b_bf16 v[200:203], a[222:223], a[180:181], v[200:203]\n\t"
"ds_read_b64_tr_b16 a[194:195], v250 offset:41600\n\t"
"v_mfma_f32_4x4x4_16b_bf16 v[204:207], a[222:223], a[188:189], v[204:207]\n\t"
"ds_read_b64_tr_b16 a[202:203], v251 offset:41600\n\t"
"v_mfma_f32_4x4x4_16b_bf16 v[208:211], a[222:223], a[192:193], v[216:219]\n\t"
"ds_read_b64_tr_b16 a[176:177], v248 offset:41472\n\t"
"v_mfma_f32_4x4x4_16b_bf16 v[212:215], a[222:223], a[200:201], v[220:223]\n\t"
"ds_read_b64_tr_b16 a[184:185], v249 offset:41472\n\t"
"v_mfma_f32_4x4x4_16b_bf16 v[216:219], a[222:223], a[196:197], v[216:219]\n\t"
"ds_read_b64_tr_b16 a[180:181], v249 offset:41728\n\t"
"v_mfma_f32_4x4x4_16b_bf16 v[220:223], a[222:223], a[204:205], v[220:223]\n\t"
"s_waitcnt lgkmcnt(8)\n\t"
"buffer_load_dwordx4 a[80:83], off, s[84:87], s48 offset:2048 sc1\n\t"
"v_mfma_f32_16x16x32_bf16 v[96:99], a[96:99], a[144:147], v[96:99]\n\t"
"ds_read_b64_tr_b16 a[188:189], v248 offset:41728\n\t"
"v_cvt_pk_bf16_f32 v195, -v195, -v199\n\t"
"v_mfma_f32_16x16x32_bf16 v[96:99], a[100:103], a[160:163], v[96:99]\n\t"
"ds_read_b64_tr_b16 a[192:193], v250 offset:41472\n\t"
"v_cvt_pk_bf16_f32 v193, -v193, -v197\n\t"
"v_mfma_f32_16x16x32_bf16 v[112:115], a[104:107], a[144:147], v[112:115]\n\t"
"ds_read_b64_tr_b16 a[200:201], v251 offset:41472\n\t"
"v_cvt_pk_bf16_f32 v201, v201, v205\n\t"
"v_mfma_f32_16x16x32_bf16 v[112:115], a[108:111], a[160:163], v[112:115]\n\t"
"buffer_load_dwordx4 a[72:75], off, s[84:87], s48 offset:0 sc1\n\t"
"v_mfma_f32_16x16x32_bf16 v[128:131], a[112:115], a[144:147], v[128:131]\n\t"
"ds_read_b128 a[144:147], v224 offset:1024\n\t"
"v_cvt_pk_bf16_f32 v200, v200, v204\n\t"
"v_mfma_f32_16x16x32_bf16 v[128:131], a[116:119], a[160:163], v[128:131]\n\t"
"ds_read_b64_tr_b16 a[196:197], v251 offset:41728\n\t"
"v_cvt_pk_bf16_f32 v194, -v194, -v198\n\t"
"v_mfma_f32_16x16x32_bf16 v[132:135], a[112:115], a[148:151], v[132:135]\n\t"
"ds_read_b128 a[160:163], v224 offset:3072\n\t"
"v_cvt_pk_bf16_f32 v217, v217, v221\n\t"
"v_mfma_f32_16x16x32_bf16 v[132:135], a[116:119], a[164:167], v[132:135]\n\t"
"buffer_load_dwordx4 a[76:79], off, s[84:87], s48 offset:1024 sc1\n\t"
"v_mfma_f32_16x16x32_bf16 v[116:119], a[104:107], a[148:151], v[116:119]\n\t"
"ds_write_addtid_b32 v193 offset:20672\n\t"
"v_cvt_pk_bf16_f32 v219, v219, v223\n\t"
"v_mfma_f32_16x16x32_bf16 v[116:119], a[108:111], a[164:167], v[116:119]\n\t"
"ds_write_addtid_b32 v194 offset:17568\n\t"
"v_cvt_pk_bf16_f32 v210, -v210, -v214\n\t"
"v_mfma_f32_16x16x32_bf16 v[100:103], a[96:99], a[148:151], v[100:103]\n\t"
"ds_read_b128 a[148:151], v224 offset:1280\n\t"
"v_cvt_pk_bf16_f32 v211, -v211, -v215\n\t"
"v_mfma_f32_16x16x32_bf16 v[100:103], a[100:103], a[164:167], v[100:103]\n\t"
"ds_write_addtid_b32 v201 offset:28992\n\t"
"v_cvt_pk_bf16_f32 v218, v218, v222\n\t"
"v_mfma_f32_16x16x32_bf16 v[104:107], a[96:99], a[152:155], v[104:107]\n\t"
"ds_read_b128 a[164:167], v224 offset:3328\n\t"
"v_cvt_pk_bf16_f32 v203, v203, v207\n\t"
"v_mfma_f32_16x16x32_bf16 v[104:107], a[100:103], a[168:171], v[104:107]\n\t"
"ds_write_addtid_b32 v218 offset:27936\n\t"
"v_mfma_f32_16x16x32_bf16 v[120:123], a[104:107], a[152:155], v[120:123]\n\t"
"ds_write_addtid_b32 v217 offset:31040\n\t"
"v_cvt_pk_bf16_f32 v208, -v208, -v212\n\t"
"v_mfma_f32_16x16x32_bf16 v[120:123], a[108:111], a[168:171], v[120:123]\n\t"
"ds_write_addtid_b32 v208 offset:18592\n\t"
"v_mfma_f32_16x16x32_bf16 v[136:139], a[112:115], a[152:155], v[136:139]\n\t"
"ds_read_b128 a[152:155], v224 offset:1536\n\t"
"v_mfma_f32_16x16x32_bf16 v[136:139], a[116:119], a[168:171], v[136:139]\n\t"
"s_waitcnt lgkmcnt(9)\n\t"
"ds_read_b128 a[168:171], v224 offset:3584\n\t"
"v_cvt_pk_bf16_f32 v216, v216, v220\n\t"
"v_mfma_f32_16x16x32_bf16 v[140:143], a[112:115], a[156:159], v[140:143]\n\t"
"s_nop 0\n\t"
"ds_write_addtid_b32 v210 offset:19616\n\t"
"v_cvt_pk_bf16_f32 v209, -v209, -v213\n\t"
"v_mfma_f32_16x16x32_bf16 v[140:143], a[116:119], a[172:175], v[140:143]\n\t"
"buffer_load_dwordx4 a[112:115], off, s[28:31], s75 offset:0 sc1\n\t"
"v_mfma_f32_16x16x32_bf16 v[124:127], a[104:107], a[156:159], v[124:127]\n\t"
"s_nop 0\n\t"
"ds_write_addtid_b32 v203 offset:30016\n\t"
"v_cvt_pk_bf16_f32 v202, v202, v206\n\t"
"v_mfma_f32_16x16x32_bf16 v[124:127], a[108:111], a[172:175], v[124:127]\n\t"
"s_nop 0\n\t"
"ds_write_addtid_b32 v202 offset:25888\n\t"
"ds_write_addtid_b32 v216 offset:26912\n\t"
"v_mfma_f32_16x16x32_bf16 v[108:111], a[96:99], a[156:159], v[108:111]\n\t"
"s_nop 0\n\t"
"ds_write_addtid_b32 v219 offset:32064\n\t"
"v_cvt_pk_bf16_f32 v192, -v192, -v196\n\t"
"v_mfma_f32_16x16x32_bf16 v[108:111], a[100:103], a[172:175], v[108:111]\n\t"
"buffer_load_dwordx4 a[116:119], off, s[28:31], s75 offset:1024 sc1\n\t"
"v_mfma_f32_16x16x32_bf16 v[96:99], a[120:123], a[144:147], v[96:99]\n\t"
"s_nop 0\n\t"
"ds_write_addtid_b32 v209 offset:22720\n\t"
"ds_write_addtid_b32 v211 offset:23744\n\t"
"v_mfma_f32_16x16x32_bf16 v[96:99], a[124:127], a[160:163], v[96:99]\n\t"
"s_nop 0\n\t"
"ds_write_addtid_b32 v195 offset:21696\n\t"
"v_mfma_f32_16x16x32_bf16 v[112:115], a[128:131], a[144:147], v[112:115]\n\t"
"s_nop 0\n\t"
"ds_write_addtid_b32 v192 offset:16544\n\t"
"ds_write_addtid_b32 v200 offset:24864\n\t"
"v_mfma_f32_16x16x32_bf16 v[112:115], a[132:135], a[160:163], v[112:115]\n\t"
"buffer_load_dwordx4 a[108:111], off, s[84:87], s75 offset:3072 sc1\n\t"
"v_mfma_f32_16x16x32_bf16 v[128:131], a[136:139], a[144:147], v[128:131]\n\t"
"s_nop 0\n\t"
"ds_read_b128 a[172:175], v224 offset:3840\n\t"
"v_mfma_f32_16x16x32_bf16 v[128:131], a[140:143], a[160:163], v[128:131]\n\t"
"s_waitcnt lgkmcnt(10)\n\t"
"buffer_load_dwordx4 a[104:107], off, s[84:87], s75 offset:2048 sc1\n\t"
"v_mfma_f32_16x16x32_bf16 v[132:135], a[136:139], a[148:151], v[132:135]\n\t"
"s_nop 0\n\t"
"ds_read_b128 a[156:159], v224 offset:1792\n\t"
"v_mfma_f32_16x16x32_bf16 v[132:135], a[140:143], a[164:167], v[132:135]\n\t"
"buffer_load_dwordx4 a[96:99], off, s[84:87], s75 offset:0 sc1\n\t"
"v_mfma_f32_16x16x32_bf16 v[116:119], a[128:131], a[148:151], v[116:119]\n\t"
"s_nop 0\n\t"
"ds_read_b64_tr_b16 a[204:205], v250 offset:41728\n\t"
"v_mfma_f32_16x16x32_bf16 v[116:119], a[132:135], a[164:167], v[116:119]\n\t"
"buffer_load_dwordx4 a[100:103], off, s[84:87], s75 offset:1024 sc1\n\t"
"v_mfma_f32_16x16x32_bf16 v[100:103], a[120:123], a[148:151], v[100:103]\n\t"
"s_nop 0\n\t"
"v_mfma_f32_16x16x32_bf16 v[100:103], a[124:127], a[164:167], v[100:103]\n\t"
"s_waitcnt lgkmcnt(0)\n\t"
"s_barrier\n\t"
"v_mfma_f32_16x16x32_bf16 v[104:107], a[120:123], a[152:155], v[104:107]\n\t"
"s_nop 0\n\t"
"ds_read_b128 a[144:147], v224 offset:16544\n\t"
"v_add_u32 v248, 101632, v226\n\t"
"v_mfma_f32_16x16x32_bf16 v[104:107], a[124:127], a[168:171], v[104:107]\n\t"
"s_nop 0\n\t"
"ds_read_b128 a[148:151], v224 offset:16800\n\t"
"v_mfma_f32_16x16x32_bf16 v[120:123], a[128:131], a[152:155], v[120:123]\n\t"
"s_nop 0\n\t"
"ds_read_b128 a[160:163], v224 offset:18592\n\t"
"v_xor_b32 v249, 32, v248\n\t"
"v_mfma_f32_16x16x32_bf16 v[120:123], a[132:135], a[168:171], v[120:123]\n\t"
"s_nop 0\n\t"
"ds_read_b128 a[164:167], v224 offset:18848\n\t"
"v_mfma_f32_16x16x32_bf16 v[136:139], a[136:139], a[152:155], v[136:139]\n\t"
"s_nop 0\n\t"
"v_xor_b32 v250, 64, v248\n\t"
"v_mfma_f32_16x16x32_bf16 v[136:139], a[140:143], a[168:171], v[136:139]\n\t"
"s_nop 0\n\t"
"v_mfma_f32_16x16x32_bf16 v[140:143], a[136:139], a[156:159], v[140:143]\n\t"
"s_nop 0\n\t"
"ds_read_b128 a[152:155], v224 offset:17056\n\t"
"v_mfma_f32_16x16x32_bf16 v[140:143], a[140:143], a[172:175], v[140:143]\n\t"
"buffer_load_dwordx4 a[136:139], off, s[28:31], s78 offset:0 sc1\n\t"
"v_mfma_f32_16x16x32_bf16 v[124:127], a[128:131], a[156:159], v[124:127]\n\t"
"s_nop 0\n\t"
"ds_read_b128 a[168:171], v224 offset:19104\n\t"
"v_xor_b32 v251, 96, v248\n\t"
"v_mfma_f32_16x16x32_bf16 v[124:127], a[132:135], a[172:175], v[124:127]\n\t"
"Lpad8_%=:\n\t"
"s_cbranch_execz Lpad12_%=\n\t"
"v_mfma_f32_16x16x32_bf16 v[108:111], a[120:123], a[156:159], v[108:111]\n\t"
"ds_read_b128 a[156:159], v224 offset:17312\n\t"
"v_mfma_f32_16x16x32_bf16 v[108:111], a[124:127], a[172:175], v[108:111]\n\t"
"buffer_load_dwordx4 a[140:143], off, s[28:31], s78 offset:1024 sc1\n\t"
"v_mfma_f32_4x4x4_16b_bf16 v[200:203], a[220:221], a[178:179], 0\n\t"
"ds_read_b64_tr_b16 a[178:179], v248 offset:20864\n\t"
"v_mfma_f32_4x4x4_16b_bf16 v[204:207], a[220:221], a[186:187], 0\n\t"
"v_mfma_f32_4x4x4_16b_bf16 v[216:219], a[220:221], a[194:195], 0\n\t"
"ds_read_b64_tr_b16 a[186:187], v249 offset:20864\n\t"
"v_mfma_f32_4x4x4_16b_bf16 v[220:223], a[220:221], a[202:203], 0\n\t"
".p2align 3\n\t"
"ds_read_b128 a[172:175], v224 offset:19360\n\t"
"v_pk_fma_f32 v[0:1], v[96:97], s[90:91], v[0:1] neg_lo:[1,0,0] neg_hi:[1,0,0]\n\t"
"v_pk_fma_f32 v[2:3], v[98:99], s[90:91], v[2:3] neg_lo:[1,0,0] neg_hi:[1,0,0]\n\t"
"v_pk_fma_f32 v[16:17], v[112:113], s[90:91], v[16:17] neg_lo:[1,0,0] neg_hi:[1,0,0]\n\t"
"v_pk_fma_f32 v[18:19], v[114:115], s[90:91], v[18:19] neg_lo:[1,0,0] neg_hi:[1,0,0]\n\t"
"v_pk_fma_f32 v[32:33], v[128:129], s[90:91], v[32:33] neg_lo:[1,0,0] neg_hi:[1,0,0]\n\t"
"v_pk_fma_f32 v[34:35], v[130:131], s[90:91], v[34:35] neg_lo:[1,0,0] neg_hi:[1,0,0]\n\t"
"v_pk_fma_f32 v[36:37], v[132:133], s[90:91], v[36:37] neg_lo:[1,0,0] neg_hi:[1,0,0]\n\t"
"v_pk_fma_f32 v[38:39], v[134:135], s[90:91], v[38:39] neg_lo:[1,0,0] neg_hi:[1,0,0]\n\t"
"v_pk_fma_f32 v[20:21], v[116:117], s[90:91], v[20:21] neg_lo:[1,0,0] neg_hi:[1,0,0]\n\t"
"v_pk_fma_f32 v[22:23], v[118:119], s[90:91], v[22:23] neg_lo:[1,0,0] neg_hi:[1,0,0]\n\t"
"v_pk_fma_f32 v[4:5], v[100:101], s[90:91], v[4:5] neg_lo:[1,0,0] neg_hi:[1,0,0]\n\t"
"v_pk_fma_f32 v[6:7], v[102:103], s[90:91], v[6:7] neg_lo:[1,0,0] neg_hi:[1,0,0]\n\t"
"v_pk_fma_f32 v[8:9], v[104:105], s[90:91], v[8:9] neg_lo:[1,0,0] neg_hi:[1,0,0]\n\t"
"v_pk_fma_f32 v[10:11], v[106:107], s[90:91], v[10:11] neg_lo:[1,0,0] neg_hi:[1,0,0]\n\t"
"v_pk_fma_f32 v[24:25], v[120:121], s[90:91], v[24:25] neg_lo:[1,0,0] neg_hi:[1,0,0]\n\t"
"v_pk_fma_f32 v[26:27], v[122:123], s[90:91], v[26:27] neg_lo:[1,0,0] neg_hi:[1,0,0]\n\t"
"v_pk_fma_f32 v[40:41], v[136:137], s[90:91], v[40:41] neg_lo:[1,0,0] neg_hi:[1,0,0]\n\t"
"v_pk_fma_f32 v[42:43], v[138:139], s[90:91], v[42:43] neg_lo:[1,0,0] neg_hi:[1,0,0]\n\t"
"v_pk_fma_f32 v[44:45], v[140:141], s[90:91], v[44:45] neg_lo:[1,0,0] neg_hi:[1,0,0]\n\t"
"v_pk_fma_f32 v[46:47], v[142:143], s[90:91], v[46:47] neg_lo:[1,0,0] neg_hi:[1,0,0]\n\t"
"v_pk_fma_f32 v[28:29], v[124:125], s[90:91], v[28:29] neg_lo:[1,0,0] neg_hi:[1,0,0]\n\t"
"v_pk_fma_f32 v[30:31], v[126:127], s[90:91], v[30:31] neg_lo:[1,0,0] neg_hi:[1,0,0]\n\t"
"v_pk_fma_f32 v[12:13], v[108:109], s[90:91], v[12:13] neg_lo:[1,0,0] neg_hi:[1,0,0]\n\t"
"v_pk_fma_f32 v[14:15], v[110:111], s[90:91], v[14:15] neg_lo:[1,0,0] neg_hi:[1,0,0]\n\t"
"v_mfma_f32_4x4x4_16b_bf16 v[192:195], a[222:223], a[176:177], v[200:203]\n\t"
"ds_read_b64_tr_b16 a[194:195], v250 offset:20864\n\t"
"v_mfma_f32_4x4x4_16b_bf16 v[196:199], a[222:223], a[184:185], v[204:207]\n\t"
"ds_read_b64_tr_b16 a[202:203], v251 offset:20864\n\t"
"v_mfma_f32_4x4x4_16b_bf16 v[200:203], a[222:223], a[180:181], v[200:203]\n\t"
"ds_read_b64_tr_b16 a[176:177], v248 offset:20736\n\t"
"v_mfma_f32_4x4x4_16b_bf16 v[204:207], a[222:223], a[188:189], v[204:207]\n\t"
"v_mfma_f32_4x4x4_16b_bf16 v[208:211], a[222:223], a[192:193], v[216:219]\n\t"
"ds_read_b64_tr_b16 a[184:185], v249 offset:20736\n\t"
"v_mfma_f32_4x4x4_16b_bf16 v[212:215], a[222:223], a[200:201], v[220:223]\n\t"
"ds_read_b64_tr_b16 a[180:181], v249 offset:20992\n\t"
"v_mfma_f32_4x4x4_16b_bf16 v[216:219], a[222:223], a[196:197], v[216:219]\n\t"
"ds_read_b64_tr_b16 a[188:189], v248 offset:20992\n\t"
"v_mfma_f32_4x4x4_16b_bf16 v[220:223], a[222:223], a[204:205], v[220:223]\n\t"
"s_waitcnt vmcnt(14) lgkmcnt(8)\n\t"
"buffer_load_dwordx4 a[132:135], off, s[84:87], s78 offset:3072 sc1\n\t"
"v_mfma_f32_16x16x32_bf16 v[48:51], a[48:51], a[144:147], v[48:51]\n\t"
"ds_read_b64_tr_b16 a[192:193], v250 offset:20736\n\t"
"v_cvt_pk_bf16_f32 v195, -v195, -v199\n\t"
"v_mfma_f32_16x16x32_bf16 v[48:51], a[52:55], a[160:163], v[48:51]\n\t"
"ds_read_b64_tr_b16 a[200:201], v251 offset:20736\n\t"
"v_cvt_pk_bf16_f32 v193, -v193, -v197\n\t"
"v_mfma_f32_16x16x32_bf16 v[64:67], a[56:59], a[144:147], v[64:67]\n\t"
"ds_write_addtid_b32 v193 offset:4128\n\t"
"v_cvt_pk_bf16_f32 v202, v202, v206\n\t"
"v_mfma_f32_16x16x32_bf16 v[64:67], a[60:63], a[160:163], v[64:67]\n\t"
"buffer_load_dwordx4 a[128:131], off, s[84:87], s78 offset:2048 sc1\n\t"
"v_mfma_f32_16x16x32_bf16 v[80:83], a[64:67], a[144:147], v[80:83]\n\t"
"ds_read_b128 a[144:147], v224 offset:17568\n\t"
"v_mfma_f32_16x16x32_bf16 v[80:83], a[68:71], a[160:163], v[80:83]\n\t"
"ds_write_addtid_b32 v202 offset:9344\n\t"
"v_cvt_pk_bf16_f32 v211, -v211, -v215\n\t"
"v_mfma_f32_16x16x32_bf16 v[84:87], a[64:67], a[148:151], v[84:87]\n\t"
"ds_read_b128 a[160:163], v224 offset:19616\n\t"
"v_cvt_pk_bf16_f32 v203, v203, v207\n\t"
"v_mfma_f32_16x16x32_bf16 v[84:87], a[68:71], a[164:167], v[84:87]\n\t"
"buffer_load_dwordx4 a[120:123], off, s[84:87], s78 offset:0 sc1\n\t"
"v_mfma_f32_16x16x32_bf16 v[68:71], a[56:59], a[148:151], v[68:71]\n\t"
"ds_write_addtid_b32 v195 offset:5152\n\t"
"v_mfma_f32_16x16x32_bf16 v[68:71], a[60:63], a[164:167], v[68:71]\n\t"
"ds_write_addtid_b32 v203 offset:13472\n\t"
"v_cvt_pk_bf16_f32 v192, -v192, -v196\n\t"
"v_mfma_f32_16x16x32_bf16 v[52:55], a[48:51], a[148:151], v[52:55]\n\t"
"ds_read_b128 a[148:151], v224 offset:17824\n\t"
"v_cvt_pk_bf16_f32 v201, v201, v205\n\t"
"v_mfma_f32_16x16x32_bf16 v[52:55], a[52:55], a[164:167], v[52:55]\n\t"
"buffer_load_dwordx4 a[124:127], off, s[84:87], s78 offset:1024 sc1\n\t"
"v_mfma_f32_16x16x32_bf16 v[56:59], a[48:51], a[152:155], v[56:59]\n\t"
"ds_read_b128 a[164:167], v224 offset:19872\n\t"
"v_cvt_pk_bf16_f32 v194, -v194, -v198\n\t"
"v_mfma_f32_16x16x32_bf16 v[56:59], a[52:55], a[168:171], v[56:59]\n\t"
"ds_write_addtid_b32 v194 offset:1024\n\t"
"v_mfma_f32_16x16x32_bf16 v[72:75], a[56:59], a[152:155], v[72:75]\n\t"
"ds_write_addtid_b32 v201 offset:12448\n\t"
"v_cvt_pk_bf16_f32 v218, v218, v222\n\t"
"v_mfma_f32_16x16x32_bf16 v[72:75], a[60:63], a[168:171], v[72:75]\n\t"
"ds_write_addtid_b32 v211 offset:7200\n\t"
"v_mfma_f32_16x16x32_bf16 v[88:91], a[64:67], a[152:155], v[88:91]\n\t"
"ds_read_b128 a[152:155], v224 offset:18080\n\t"
"v_cvt_pk_bf16_f32 v200, v200, v204\n\t"
"v_mfma_f32_16x16x32_bf16 v[88:91], a[68:71], a[168:171], v[88:91]\n\t"
"s_waitcnt lgkmcnt(8)\n\t"
"ds_read_b128 a[168:171], v224 offset:20128\n\t"
"v_cvt_pk_bf16_f32 v208, -v208, -v212\n\t"
"v_mfma_f32_16x16x32_bf16 v[92:95], a[64:67], a[156:159], v[92:95]\n\t"
"ds_write_addtid_b32 v200 offset:8320\n\t"
"v_cvt_pk_bf16_f32 v209, -v209, -v213\n\t"
"v_mfma_f32_16x16x32_bf16 v[92:95], a[68:71], a[172:175], v[92:95]\n\t"
"ds_write_addtid_b32 v209 offset:6176\n\t"
"v_cvt_pk_bf16_f32 v217, v217, v221\n\t"
"v_mfma_f32_16x16x32_bf16 v[76:79], a[56:59], a[156:159], v[76:79]\n\t"
"ds_write_addtid_b32 v192 offset:0\n\t"
"v_cvt_pk_bf16_f32 v210, -v210, -v214\n\t"
"v_mfma_f32_16x16x32_bf16 v[76:79], a[60:63], a[172:175], v[76:79]\n\t"
"ds_write_addtid_b32 v208 offset:2048\n\t"
"v_cvt_pk_bf16_f32 v216, v216, v220\n\t"
"v_mfma_f32_16x16x32_bf16 v[60:63], a[48:51], a[156:159], v[60:63]\n\t"
"ds_write_addtid_b32 v210 offset:3072\n\t"
"v_cvt_pk_bf16_f32 v219, v219, v223\n\t"
"v_mfma_f32_16x16x32_bf16 v[60:63], a[52:55], a[172:175], v[60:63]\n\t"
"s_waitcnt vmcnt(12) lgkmcnt(9)\n\t"
"ds_write_addtid_b32 v219 offset:15520\n\t"
"v_mfma_f32_16x16x32_bf16 v[144:147], a[72:75], a[144:147], v[144:147]\n\t"
"ds_write_addtid_b32 v218 offset:11392\n\t"
"v_mfma_f32_16x16x32_bf16 v[144:147], a[76:79], a[160:163], v[144:147]\n\t"
"ds_write_addtid_b32 v217 offset:14496\n\t"
"v_mfma_f32_16x16x32_bf16 v[160:163], a[80:83], a[144:147], v[160:163]\n\t"
"ds_write_addtid_b32 v216 offset:10368\n\t"
"v_mfma_f32_16x16x32_bf16 v[160:163], a[84:87], a[160:163], v[160:163]\n\t"
"ds_read_b128 a[156:159], v224 offset:18336\n\t"
"v_mfma_f32_16x16x32_bf16 v[176:179], a[88:91], a[144:147], v[176:179]\n\t"
"v_mfma_f32_16x16x32_bf16 v[176:179], a[92:95], a[160:163], v[176:179]\n\t"
"ds_read_b128 a[172:175], v224 offset:20384\n\t"
"v_mfma_f32_16x16x32_bf16 v[180:183], a[88:91], a[148:151], v[180:183]\n\t"
"v_mfma_f32_16x16x32_bf16 v[180:183], a[92:95], a[164:167], v[180:183]\n\t"
"ds_read_b64_tr_b16 a[196:197], v251 offset:20992\n\t"
"v_mfma_f32_16x16x32_bf16 v[164:167], a[80:83], a[148:151], v[164:167]\n\t"
"ds_read_b64_tr_b16 a[204:205], v250 offset:20992\n\t"
"v_mfma_f32_16x16x32_bf16 v[164:167], a[84:87], a[164:167], v[164:167]\n\t"
"v_mfma_f32_16x16x32_bf16 v[148:151], a[72:75], a[148:151], v[148:151]\n\t"
"s_waitcnt lgkmcnt(2)\n\t"
"v_mfma_f32_16x16x32_bf16 v[148:151], a[76:79], a[164:167], v[148:151]\n\t"
"s_barrier\n\t"
"v_mfma_f32_16x16x32_bf16 v[152:155], a[72:75], a[152:155], v[152:155]\n\t"
"s_add_u32 m0, s26, 35584\n\t"
"ds_read_b128 a[144:147], v224 offset:0\n\t"
"ds_read_b128 a[160:163], v224 offset:2048\n\t"
"v_mfma_f32_16x16x32_bf16 v[152:155], a[76:79], a[168:171], v[152:155]\n\t"
"buffer_load_dwordx4 v236, s[12:15], s22 offen offset:0 nt lds\n\t"
"v_mfma_f32_16x16x32_bf16 v[168:171], a[80:83], a[152:155], v[168:171]\n\t"
"ds_read_b128 a[148:151], v224 offset:256\n\t"
"v_mfma_f32_16x16x32_bf16 v[168:171], a[84:87], a[168:171], v[168:171]\n\t"
"ds_read_b128 a[164:167], v224 offset:2304\n\t"
"v_mfma_f32_16x16x32_bf16 v[184:187], a[88:91], a[152:155], v[184:187]\n\t"
"v_mfma_f32_16x16x32_bf16 v[184:187], a[92:95], a[168:171], v[184:187]\n\t"
"buffer_load_dwordx4 v237, s[12:15], s22 offen offset:1024 nt lds\n\t"
"v_mfma_f32_16x16x32_bf16 v[188:191], a[88:91], a[156:159], v[188:191]\n\t"
"ds_read_b128 a[168:171], v224 offset:2560\n\t"
"v_mfma_f32_16x16x32_bf16 v[188:191], a[92:95], a[172:175], v[188:191]\n\t"
"ds_read_b128 a[152:155], v224 offset:512\n\t"
"v_mfma_f32_16x16x32_bf16 v[172:175], a[80:83], a[156:159], v[172:175]\n\t"
"v_mfma_f32_16x16x32_bf16 v[172:175], a[84:87], a[172:175], v[172:175]\n\t"
"buffer_load_dwordx4 v238, s[12:15], s22 offen offset:2048 nt lds\n\t"
"v_mfma_f32_16x16x32_bf16 v[156:159], a[72:75], a[156:159], v[156:159]\n\t"
"ds_read_b128 a[156:159], v224 offset:768\n\t"
"v_mfma_f32_16x16x32_bf16 v[156:159], a[76:79], a[172:175], v[156:159]\n\t"
"s_waitcnt lgkmcnt(5)\n\t"
"ds_read_b128 a[172:175], v224 offset:2816\n\t"
"v_mfma_f32_4x4x4_16b_bf16 v[200:203], a[252:253], a[178:179], 0\n\t"
"v_mfma_f32_4x4x4_16b_bf16 v[204:207], a[252:253], a[186:187], 0\n\t"
"buffer_load_dwordx4 v239, s[12:15], s22 offen offset:3072 nt lds\n\t"
"v_mfma_f32_4x4x4_16b_bf16 v[216:219], a[252:253], a[194:195], 0\n\t"
"ds_read_b64_tr_b16 a[178:179], v248 offset:128\n\t"
"v_mfma_f32_4x4x4_16b_bf16 v[220:223], a[252:253], a[202:203], 0\n\t"
"ds_read_b64_tr_b16 a[186:187], v249 offset:128\n\t"
"v_mfma_f32_4x4x4_16b_bf16 v[192:195], a[254:255], a[176:177], v[200:203]\n\t"
"ds_read_b64_tr_b16 a[194:195], v250 offset:128\n\t"
"v_mfma_f32_4x4x4_16b_bf16 v[196:199], a[254:255], a[184:185], v[204:207]\n\t"
"s_mov_b64 exec, s[36:37]\n\t"
"buffer_load_dwordx4 v240, s[12:15], s22 offen offset:3584 nt lds\n\t"
"s_mov_b64 exec, -1\n\t"
"v_mfma_f32_4x4x4_16b_bf16 v[200:203], a[254:255], a[180:181], v[200:203]\n\t"
"ds_read_b64_tr_b16 a[202:203], v251 offset:128\n\t"
"v_mfma_f32_4x4x4_16b_bf16 v[204:207], a[254:255], a[188:189], v[204:207]\n\t"
"ds_read_b64_tr_b16 a[176:177], v248 offset:0\n\t"
"v_mfma_f32_4x4x4_16b_bf16 v[208:211], a[254:255], a[192:193], v[216:219]\n\t"
"ds_read_b64_tr_b16 a[184:185], v249 offset:0\n\t"
"v_mfma_f32_4x4x4_16b_bf16 v[212:215], a[254:255], a[200:201], v[220:223]\n\t"
"s_lshl_b32 m0, s73, 8\n\t"
"v_mfma_f32_4x4x4_16b_bf16 v[216:219], a[254:255], a[196:197], v[216:219]\n\t"
"ds_read_b64_tr_b16 a[180:181], v249 offset:256\n\t"
"v_mfma_f32_4x4x4_16b_bf16 v[220:223], a[254:255], a[204:205], v[220:223]\n\t"
"s_waitcnt vmcnt(11) lgkmcnt(7)\n\t"
"ds_read_b64_tr_b16 a[188:189], v248 offset:256\n\t"
"v_mfma_f32_16x16x32_bf16 v[48:51], a[96:99], a[144:147], v[48:51]\n\t"
"ds_read_b64_tr_b16 a[192:193], v250 offset:0\n\t"
"v_mfma_f32_16x16x32_bf16 v[48:51], a[100:103], a[160:163], v[48:51]\n\t"
"v_cvt_pk_bf16_f32 v193, -v193, -v197\n\t"
"v_cvt_pk_bf16_f32 v195, -v195, -v199\n\t"
"v_mfma_f32_16x16x32_bf16 v[64:67], a[104:107], a[144:147], v[64:67]\n\t"
"ds_read_b64_tr_b16 a[200:201], v251 offset:0\n\t"
"v_mfma_f32_16x16x32_bf16 v[64:67], a[108:111], a[160:163], v[64:67]\n\t"
"ds_write_addtid_b32 v193 offset:20672\n\t"
"v_cvt_pk_bf16_f32 v194, -v194, -v198\n\t"
"v_mfma_f32_16x16x32_bf16 v[80:83], a[112:115], a[144:147], v[80:83]\n\t"
"ds_read_b128 a[144:147], v224 offset:1024\n\t"
"v_cvt_pk_bf16_f32 v201, v201, v205\n\t"
"v_mfma_f32_16x16x32_bf16 v[80:83], a[116:119], a[160:163], v[80:83]\n\t"
"v_cvt_pk_bf16_f32 v209, -v209, -v213\n\t"
"v_mfma_f32_16x16x32_bf16 v[84:87], a[112:115], a[148:151], v[84:87]\n\t"
"ds_read_b128 a[160:163], v224 offset:3072\n\t"
"v_mfma_f32_16x16x32_bf16 v[84:87], a[116:119], a[164:167], v[84:87]\n\t"
"ds_write_addtid_b32 v201 offset:28992\n\t"
"v_cvt_pk_bf16_f32 v217, v217, v221\n\t"
"v_mfma_f32_16x16x32_bf16 v[68:71], a[104:107], a[148:151], v[68:71]\n\t"
"ds_write_addtid_b32 v195 offset:21696\n\t"
"v_cvt_pk_bf16_f32 v203, v203, v207\n\t"
"v_mfma_f32_16x16x32_bf16 v[68:71], a[108:111], a[164:167], v[68:71]\n\t"
"v_cvt_pk_bf16_f32 v200, v200, v204\n\t"
"v_cvt_pk_bf16_f32 v216, v216, v220\n\t"
"v_mfma_f32_16x16x32_bf16 v[52:55], a[96:99], a[148:151], v[52:55]\n\t"
"ds_read_b128 a[148:151], v224 offset:1280\n\t"
"v_mfma_f32_16x16x32_bf16 v[52:55], a[100:103], a[164:167], v[52:55]\n\t"
"ds_read_b128 a[164:167], v224 offset:3328\n\t"
"v_mfma_f32_16x16x32_bf16 v[56:59], a[96:99], a[152:155], v[56:59]\n\t"
"ds_write_addtid_b32 v194 offset:17568\n\t"
"v_cvt_pk_bf16_f32 v211, -v211, -v215\n\t"
"v_mfma_f32_16x16x32_bf16 v[56:59], a[100:103], a[168:171], v[56:59]\n\t"
"ds_write_addtid_b32 v211 offset:23744\n\t"
"v_cvt_pk_bf16_f32 v219, v219, v223\n\t"
"v_mfma_f32_16x16x32_bf16 v[72:75], a[104:107], a[152:155], v[72:75]\n\t"
"ds_write_addtid_b32 v209 offset:22720\n\t"
"v_cvt_pk_bf16_f32 v208, -v208, -v212\n\t"
"v_mfma_f32_16x16x32_bf16 v[72:75], a[108:111], a[168:171], v[72:75]\n\t"
"ds_write_addtid_b32 v203 offset:30016\n\t"
"v_cvt_pk_bf16_f32 v218, v218, v222\n\t"
"v_mfma_f32_16x16x32_bf16 v[88:91], a[112:115], a[152:155], v[88:91]\n\t"
"ds_read_b128 a[152:155], v224 offset:1536\n\t"
"v_cvt_pk_bf16_f32 v210, -v210, -v214\n\t"
"v_mfma_f32_16x16x32_bf16 v[88:91], a[116:119], a[168:171], v[88:91]\n\t"
"ds_read_b128 a[168:171], v224 offset:3584\n\t"
"v_cvt_pk_bf16_f32 v192, -v192, -v196\n\t"
"v_mfma_f32_16x16x32_bf16 v[92:95], a[112:115], a[156:159], v[92:95]\n\t"
"ds_write_addtid_b32 v217 offset:31040\n\t"
"v_mfma_f32_16x16x32_bf16 v[92:95], a[116:119], a[172:175], v[92:95]\n\t"
"ds_write_addtid_b32 v216 offset:26912\n\t"
"v_mfma_f32_16x16x32_bf16 v[76:79], a[104:107], a[156:159], v[76:79]\n\t"
"ds_write_addtid_b32 v200 offset:24864\n\t"
"v_cvt_pk_bf16_f32 v202, v202, v206\n\t"
"v_mfma_f32_16x16x32_bf16 v[76:79], a[108:111], a[172:175], v[76:79]\n\t"
"ds_write_addtid_b32 v210 offset:19616\n\t"
"v_mfma_f32_16x16x32_bf16 v[60:63], a[96:99], a[156:159], v[60:63]\n\t"
"ds_write_addtid_b32 v208 offset:18592\n\t"
"v_mfma_f32_16x16x32_bf16 v[60:63], a[100:103], a[172:175], v[60:63]\n\t"
"s_waitcnt vmcnt(5) lgkmcnt(9)\n\t"
"ds_write_addtid_b32 v218 offset:27936\n\t"
"v_mfma_f32_16x16x32_bf16 v[144:147], a[120:123], a[144:147], v[144:147]\n\t"
"ds_write_addtid_b32 v219 offset:32064\n\t"
"v_mfma_f32_16x16x32_bf16 v[144:147], a[124:127], a[160:163], v[144:147]\n\t"
"ds_write_addtid_b32 v202 offset:25888\n\t"
"v_mfma_f32_16x16x32_bf16 v[160:163], a[128:131], a[144:147], v[160:163]\n\t"
"ds_write_addtid_b32 v192 offset:16544\n\t"
"v_mfma_f32_16x16x32_bf16 v[160:163], a[132:135], a[160:163], v[160:163]\n\t"
"ds_read_b128 a[156:159], v224 offset:1792\n\t"
"v_mfma_f32_16x16x32_bf16 v[176:179], a[136:139], a[144:147], v[176:179]\n\t"
"v_mfma_f32_16x16x32_bf16 v[176:179], a[140:143], a[160:163], v[176:179]\n\t"
"ds_read_b128 a[172:175], v224 offset:3840\n\t"
"v_mfma_f32_16x16x32_bf16 v[180:183], a[136:139], a[148:151], v[180:183]\n\t"
"v_mfma_f32_16x16x32_bf16 v[180:183], a[140:143], a[164:167], v[180:183]\n\t"
"ds_read_b64_tr_b16 a[196:197], v251 offset:256\n\t"
"v_mfma_f32_16x16x32_bf16 v[164:167], a[128:131], a[148:151], v[164:167]\n\t"
"ds_read_b64_tr_b16 a[204:205], v250 offset:256\n\t"
"v_mfma_f32_16x16x32_bf16 v[164:167], a[132:135], a[164:167], v[164:167]\n\t"
"v_mfma_f32_16x16x32_bf16 v[148:151], a[120:123], a[148:151], v[148:151]\n\t"
"s_waitcnt lgkmcnt(2)\n\t"
"v_mfma_f32_16x16x32_bf16 v[148:151], a[124:127], a[164:167], v[148:151]\n\t"
"s_barrier\n\t"
"v_mfma_f32_16x16x32_bf16 v[152:155], a[120:123], a[152:155], v[152:155]\n\t"
"ds_read_b128 a[160:163], v224 offset:18592\n\t"
"v_mfma_f32_16x16x32_bf16 v[152:155], a[124:127], a[168:171], v[152:155]\n\t"
"ds_read_b128 a[144:147], v224 offset:16544\n\t"
"v_mfma_f32_16x16x32_bf16 v[168:171], a[128:131], a[152:155], v[168:171]\n\t"
"ds_read_b128 a[148:151], v224 offset:16800\n\t"
"v_mfma_f32_16x16x32_bf16 v[168:171], a[132:135], a[168:171], v[168:171]\n\t"
"ds_read_b128 a[164:167], v224 offset:18848\n\t"
"v_mfma_f32_16x16x32_bf16 v[184:187], a[136:139], a[152:155], v[184:187]\n\t"
"v_mfma_f32_16x16x32_bf16 v[184:187], a[140:143], a[168:171], v[184:187]\n\t"
"v_mfma_f32_16x16x32_bf16 v[188:191], a[136:139], a[156:159], v[188:191]\n\t"
"ds_read_b128 a[152:155], v224 offset:17056\n\t"
"v_mfma_f32_16x16x32_bf16 v[188:191], a[140:143], a[172:175], v[188:191]\n\t"
"ds_read_b128 a[168:171], v224 offset:19104\n\t"
"v_mfma_f32_16x16x32_bf16 v[172:175], a[128:131], a[156:159], v[172:175]\n\t"
"v_mfma_f32_16x16x32_bf16 v[172:175], a[132:135], a[172:175], v[172:175]\n\t"
"v_mfma_f32_16x16x32_bf16 v[156:159], a[120:123], a[156:159], v[156:159]\n\t"
"ds_read_b128 a[156:159], v224 offset:17312\n\t"
"v_mfma_f32_16x16x32_bf16 v[156:159], a[124:127], a[172:175], v[156:159]\n\t"
"s_waitcnt lgkmcnt(5)\n\t"
"ds_read_b128 a[172:175], v224 offset:19360\n\t"
"v_mfma_f32_4x4x4_16b_bf16 v[200:203], a[252:253], a[178:179], 0\n\t"
"ds_read_b64_tr_b16 a[178:179], v248 offset:41600\n\t"
"v_mfma_f32_4x4x4_16b_bf16 v[204:207], a[252:253], a[186:187], 0\n\t"
"ds_read_b64_tr_b16 a[186:187], v249 offset:41600\n\t"
"v_mfma_f32_4x4x4_16b_bf16 v[216:219], a[252:253], a[194:195], 0\n\t"
"ds_read_b64_tr_b16 a[194:195], v250 offset:41600\n\t"
"v_mfma_f32_4x4x4_16b_bf16 v[220:223], a[252:253], a[202:203], 0\n\t"
".p2align 3\n\t"
"ds_read_b64_tr_b16 a[202:203], v251 offset:41600\n\t"
"v_pk_fma_f32 v[48:49], v[144:145], s[90:91], v[48:49]\n\t"
"v_pk_fma_f32 v[50:51], v[146:147], s[90:91], v[50:51]\n\t"
"v_pk_add_f32 v[144:145], v[48:49], v[144:145] neg_lo:[0,1] neg_hi:[0,1]\n\t"
"v_pk_add_f32 v[146:147], v[50:51], v[146:147] neg_lo:[0,1] neg_hi:[0,1]\n\t"
"v_pk_fma_f32 v[64:65], v[160:161], s[90:91], v[64:65]\n\t"
"v_pk_fma_f32 v[66:67], v[162:163], s[90:91], v[66:67]\n\t"
"v_pk_add_f32 v[160:161], v[64:65], v[160:161] neg_lo:[0,1] neg_hi:[0,1]\n\t"
"v_pk_add_f32 v[162:163], v[66:67], v[162:163] neg_lo:[0,1] neg_hi:[0,1]\n\t"
"v_pk_fma_f32 v[80:81], v[176:177], s[90:91], v[80:81]\n\t"
"v_pk_fma_f32 v[82:83], v[178:179], s[90:91], v[82:83]\n\t"
"v_pk_add_f32 v[176:177], v[80:81], v[176:177] neg_lo:[0,1] neg_hi:[0,1]\n\t"
"v_pk_add_f32 v[178:179], v[82:83], v[178:179] neg_lo:[0,1] neg_hi:[0,1]\n\t"
"v_pk_fma_f32 v[84:85], v[180:181], s[90:91], v[84:85]\n\t"
"v_pk_fma_f32 v[86:87], v[182:183], s[90:91], v[86:87]\n\t"
"v_pk_add_f32 v[180:181], v[84:85], v[180:181] neg_lo:[0,1] neg_hi:[0,1]\n\t"
"v_pk_add_f32 v[182:183], v[86:87], v[182:183] neg_lo:[0,1] neg_hi:[0,1]\n\t"
"v_pk_fma_f32 v[68:69], v[164:165], s[90:91], v[68:69]\n\t"
"v_pk_fma_f32 v[70:71], v[166:167], s[90:91], v[70:71]\n\t"
"v_pk_add_f32 v[164:165], v[68:69], v[164:165] neg_lo:[0,1] neg_hi:[0,1]\n\t"
"v_pk_add_f32 v[166:167], v[70:71], v[166:167] neg_lo:[0,1] neg_hi:[0,1]\n\t"
"v_pk_fma_f32 v[52:53], v[148:149], s[90:91], v[52:53]\n\t"
"v_pk_fma_f32 v[54:55], v[150:151], s[90:91], v[54:55]\n\t"
"v_pk_add_f32 v[148:149], v[52:53], v[148:149] neg_lo:[0,1] neg_hi:[0,1]\n\t"
"v_pk_add_f32 v[150:151], v[54:55], v[150:151] neg_lo:[0,1] neg_hi:[0,1]\n\t"
"v_pk_fma_f32 v[56:57], v[152:153], s[90:91], v[56:57]\n\t"
"v_pk_fma_f32 v[58:59], v[154:155], s[90:91], v[58:59]\n\t"
"v_pk_add_f32 v[152:153], v[56:57], v[152:153] neg_lo:[0,1] neg_hi:[0,1]\n\t"
"v_pk_add_f32 v[154:155], v[58:59], v[154:155] neg_lo:[0,1] neg_hi:[0,1]\n\t"
"v_pk_fma_f32 v[72:73], v[168:169], s[90:91], v[72:73]\n\t"
"v_pk_fma_f32 v[74:75], v[170:171], s[90:91], v[74:75]\n\t"
"v_pk_add_f32 v[168:169], v[72:73], v[168:169] neg_lo:[0,1] neg_hi:[0,1]\n\t"
"v_pk_add_f32 v[170:171], v[74:75], v[170:171] neg_lo:[0,1] neg_hi:[0,1]\n\t"
"v_pk_fma_f32 v[88:89], v[184:185], s[90:91], v[88:89]\n\t"
"v_pk_fma_f32 v[90:91], v[186:187], s[90:91], v[90:91]\n\t"
"v_pk_add_f32 v[184:185], v[88:89], v[184:185] neg_lo:[0,1] neg_hi:[0,1]\n\t"
"v_pk_add_f32 v[186:187], v[90:91], v[186:187] neg_lo:[0,1] neg_hi:[0,1]\n\t"
"v_pk_fma_f32 v[92:93], v[188:189], s[90:91], v[92:93]\n\t"
"v_pk_fma_f32 v[94:95], v[190:191], s[90:91], v[94:95]\n\t"
"v_pk_add_f32 v[188:189], v[92:93], v[188:189] neg_lo:[0,1] neg_hi:[0,1]\n\t"
"v_pk_add_f32 v[190:191], v[94:95], v[190:191] neg_lo:[0,1] neg_hi:[0,1]\n\t"
"v_pk_fma_f32 v[76:77], v[172:173], s[90:91], v[76:77]\n\t"
"v_pk_fma_f32 v[78:79], v[174:175], s[90:91], v[78:79]\n\t"
"v_pk_add_f32 v[172:173], v[76:77], v[172:173] neg_lo:[0,1] neg_hi:[0,1]\n\t"
"v_pk_add_f32 v[174:175], v[78:79], v[174:175] neg_lo:[0,1] neg_hi:[0,1]\n\t"
"v_pk_fma_f32 v[60:61], v[156:157], s[90:91], v[60:61]\n\t"
"v_pk_fma_f32 v[62:63], v[158:159], s[90:91], v[62:63]\n\t"
"v_pk_add_f32 v[156:157], v[60:61], v[156:157] neg_lo:[0,1] neg_hi:[0,1]\n\t"
"v_pk_add_f32 v[158:159], v[62:63], v[158:159] neg_lo:[0,1] neg_hi:[0,1]\n\t"
"v_cvt_pk_bf16_f32 v64, v64, v65\n\t"
"v_mfma_f32_4x4x4_16b_bf16 v[192:195], a[254:255], a[176:177], v[200:203]\n\t"
"ds_read_b64_tr_b16 a[176:177], v248 offset:41472\n\t"
"v_cvt_pk_bf16_f32 v48, v48, v49\n\t"
"v_cvt_pk_bf16_f32 v68, v68, v69\n\t"
"v_mfma_f32_4x4x4_16b_bf16 v[196:199], a[254:255], a[184:185], v[204:207]\n\t"
"ds_read_b64_tr_b16 a[184:185], v249 offset:41472\n\t"
"v_cvt_pk_bf16_f32 v52, v52, v53\n\t"
"v_cvt_pk_bf16_f32 v65, v66, v67\n\t"
"v_mfma_f32_4x4x4_16b_bf16 v[200:203], a[254:255], a[180:181], v[200:203]\n\t"
"ds_read_b64_tr_b16 a[180:181], v249 offset:41728\n\t"
"v_cvt_pk_bf16_f32 v53, v54, v55\n\t"
"v_mfma_f32_4x4x4_16b_bf16 v[204:207], a[254:255], a[188:189], v[204:207]\n\t"
"v_cvt_pk_bf16_f32 v49, v50, v51\n\t"
"v_mfma_f32_4x4x4_16b_bf16 v[208:211], a[254:255], a[192:193], v[216:219]\n\t"
"ds_write2st64_b64 v228, v[48:49], v[52:53] offset0:0 offset1:13\n\t"
"v_mfma_f32_4x4x4_16b_bf16 v[212:215], a[254:255], a[200:201], v[220:223]\n\t"
"v_cvt_pk_bf16_f32 v69, v70, v71\n\t"
"v_mfma_f32_4x4x4_16b_bf16 v[216:219], a[254:255], a[196:197], v[216:219]\n\t"
"v_cvt_pk_bf16_f32 v56, v56, v57\n\t"
"v_mfma_f32_4x4x4_16b_bf16 v[220:223], a[254:255], a[204:205], v[220:223]\n\t"
"ds_write2st64_b64 v246, v[64:65], v[68:69] offset0:0 offset1:13\n\t"
"v_mfma_f32_16x16x32_bf16 v[0:3], a[0:3], a[144:147], v[0:3]\n\t"
"v_cvt_pk_bf16_f32 v84, v84, v85\n\t"
"v_cvt_pk_bf16_f32 v60, v60, v61\n\t"
"v_mfma_f32_16x16x32_bf16 v[0:3], a[4:7], a[160:163], v[0:3]\n\t"
"v_cvt_pk_bf16_f32 v80, v80, v81\n\t"
"v_cvt_pk_bf16_f32 v61, v62, v63\n\t"
"v_mfma_f32_16x16x32_bf16 v[16:19], a[8:11], a[144:147], v[16:19]\n\t"
"v_cvt_pk_bf16_f32 v195, -v195, -v199\n\t"
"v_cvt_pk_bf16_f32 v57, v58, v59\n\t"
"v_mfma_f32_16x16x32_bf16 v[16:19], a[12:15], a[160:163], v[16:19]\n\t"
"ds_read_b64_tr_b16 a[188:189], v248 offset:41728\n\t"
"ds_write2st64_b64 v228, v[56:57], v[60:61] offset0:26 offset1:39\n\t"
"v_mfma_f32_16x16x32_bf16 v[32:35], a[16:19], a[144:147], v[32:35]\n\t"
"v_cvt_pk_bf16_f32 v208, -v208, -v212\n\t"
"v_cvt_pk_bf16_f32 v81, v82, v83\n\t"
"v_mfma_f32_16x16x32_bf16 v[32:35], a[20:23], a[160:163], v[32:35]\n\t"
"s_waitcnt lgkmcnt(4)\n\t"
"v_cvt_pk_bf16_f32 v201, v201, v205\n\t"
"v_cvt_pk_bf16_f32 v76, v76, v77\n\t"
"v_mfma_f32_16x16x32_bf16 v[36:39], a[16:19], a[148:151], v[36:39]\n\t"
"v_cvt_pk_bf16_f32 v202, v202, v206\n\t"
"v_cvt_pk_bf16_f32 v85, v86, v87\n\t"
"v_mfma_f32_16x16x32_bf16 v[36:39], a[20:23], a[164:167], v[36:39]\n\t"
"s_nop 0\n\t"
"ds_read_b128 a[144:147], v224 offset:17568\n\t"
"ds_read_b128 a[160:163], v224 offset:19616\n\t"
"ds_write2st64_b64 v247, v[80:81], v[84:85] offset0:0 offset1:13\n\t"
"v_mfma_f32_16x16x32_bf16 v[20:23], a[8:11], a[148:151], v[20:23]\n\t"
"v_cvt_pk_bf16_f32 v72, v72, v73\n\t"
"v_cvt_pk_bf16_f32 v73, v74, v75\n\t"
"v_mfma_f32_16x16x32_bf16 v[20:23], a[12:15], a[164:167], v[20:23]\n\t"
"v_cvt_pk_bf16_f32 v211, -v211, -v215\n\t"
"v_cvt_pk_bf16_f32 v219, v219, v223\n\t"
"v_mfma_f32_16x16x32_bf16 v[4:7], a[0:3], a[148:151], v[4:7]\n\t"
"v_cvt_pk_bf16_f32 v200, v200, v204\n\t"
"v_cvt_pk_bf16_f32 v77, v78, v79\n\t"
"v_mfma_f32_16x16x32_bf16 v[4:7], a[4:7], a[164:167], v[4:7]\n\t"
"s_nop 0\n\t"
"ds_read_b128 a[148:151], v224 offset:17824\n\t"
"ds_read_b128 a[164:167], v224 offset:19872\n\t"
"ds_write_addtid_b32 v195 offset:5152\n\t"
"ds_write_addtid_b32 v211 offset:7200\n\t"
"v_mfma_f32_16x16x32_bf16 v[8:11], a[0:3], a[152:155], v[8:11]\n\t"
"ds_write_addtid_b32 v201 offset:12448\n\t"
"v_cvt_pk_bf16_f32 v209, -v209, -v213\n\t"
"v_mfma_f32_16x16x32_bf16 v[8:11], a[4:7], a[168:171], v[8:11]\n\t"
"ds_write_addtid_b32 v209 offset:6176\n\t"
"v_cvt_pk_bf16_f32 v194, -v194, -v198\n\t"
"v_mfma_f32_16x16x32_bf16 v[24:27], a[8:11], a[152:155], v[24:27]\n\t"
"ds_write_addtid_b32 v194 offset:1024\n\t"
"v_cvt_pk_bf16_f32 v92, v92, v93\n\t"
"v_mfma_f32_16x16x32_bf16 v[24:27], a[12:15], a[168:171], v[24:27]\n\t"
"s_nop 0\n\t"
"ds_write_addtid_b32 v202 offset:9344\n\t"
"ds_write_addtid_b32 v208 offset:2048\n\t"
"ds_write_addtid_b32 v219 offset:15520\n\t"
"v_mfma_f32_16x16x32_bf16 v[40:43], a[16:19], a[152:155], v[40:43]\n\t"
"v_cvt_pk_bf16_f32 v192, -v192, -v196\n\t"
"v_cvt_pk_bf16_f32 v93, v94, v95\n\t"
"v_mfma_f32_16x16x32_bf16 v[40:43], a[20:23], a[168:171], v[40:43]\n\t"
"ds_read_b128 a[152:155], v224 offset:18080\n\t"
"ds_write2st64_b64 v246, v[72:73], v[76:77] offset0:26 offset1:39\n\t"
"v_mfma_f32_16x16x32_bf16 v[44:47], a[16:19], a[156:159], v[44:47]\n\t"
"v_cvt_pk_bf16_f32 v218, v218, v222\n\t"
"v_cvt_pk_bf16_f32 v88, v88, v89\n\t"
"v_mfma_f32_16x16x32_bf16 v[44:47], a[20:23], a[172:175], v[44:47]\n\t"
"buffer_load_dwordx4 a[16:19], off, s[92:95], s59 offset:0 sc1\n\t"
"v_mfma_f32_16x16x32_bf16 v[28:31], a[8:11], a[156:159], v[28:31]\n\t"
"v_cvt_pk_bf16_f32 v216, v216, v220\n\t"
"v_cvt_pk_bf16_f32 v89, v90, v91\n\t"
"v_mfma_f32_16x16x32_bf16 v[28:31], a[12:15], a[172:175], v[28:31]\n\t"
"ds_read_b128 a[168:171], v224 offset:20128\n\t"
"ds_write2st64_b64 v247, v[88:89], v[92:93] offset0:26 offset1:39\n\t"
"v_mfma_f32_16x16x32_bf16 v[12:15], a[0:3], a[156:159], v[12:15]\n\t"
"v_cvt_pk_bf16_f32 v210, -v210, -v214\n\t"
"v_cvt_pk_bf16_f32 v217, v217, v221\n\t"
"v_mfma_f32_16x16x32_bf16 v[12:15], a[4:7], a[172:175], v[12:15]\n\t"
"s_waitcnt lgkmcnt(8)\n\t"
"buffer_load_dwordx4 a[20:23], off, s[92:95], s59 offset:1024 sc1\n\t"
"v_mfma_f32_16x16x32_bf16 v[96:99], a[24:27], a[144:147], v[96:99]\n\t"
"s_nop 0\n\t"
"v_cvt_pk_bf16_f32 v193, -v193, -v197\n\t"
"v_cvt_pk_bf16_f32 v203, v203, v207\n\t"
"v_mfma_f32_16x16x32_bf16 v[96:99], a[28:31], a[160:163], v[96:99]\n\t"
"s_nop 0\n\t"
"ds_write_addtid_b32 v192 offset:0\n\t"
"ds_write_addtid_b32 v193 offset:4128\n\t"
"ds_write_addtid_b32 v200 offset:8320\n\t"
"ds_write_addtid_b32 v210 offset:3072\n\t"
"ds_write_addtid_b32 v216 offset:10368\n\t"
"ds_write_addtid_b32 v218 offset:11392\n\t"
"v_mfma_f32_16x16x32_bf16 v[112:115], a[32:35], a[144:147], v[112:115]\n\t"
"s_nop 0\n\t"
"ds_write_addtid_b32 v203 offset:13472\n\t"
"ds_write_addtid_b32 v217 offset:14496\n\t"
"v_mfma_f32_16x16x32_bf16 v[112:115], a[36:39], a[160:163], v[112:115]\n\t"
"buffer_load_dwordx4 a[12:15], off, s[96:99], s58 offset:3072 sc1\n\t"
"v_mfma_f32_16x16x32_bf16 v[128:131], a[40:43], a[144:147], v[128:131]\n\t"
"s_nop 0\n\t"
"ds_read_b128 a[156:159], v224 offset:18336\n\t"
"ds_read_b128 a[172:175], v224 offset:20384\n\t"
"v_mfma_f32_16x16x32_bf16 v[128:131], a[44:47], a[160:163], v[128:131]\n\t"
"buffer_load_dwordx4 a[8:11], off, s[96:99], s58 offset:2048 sc1\n\t"
"v_mfma_f32_16x16x32_bf16 v[132:135], a[40:43], a[148:151], v[132:135]\n\t"
"s_nop 0\n\t"
"ds_read_b64_tr_b16 a[192:193], v250 offset:41472\n\t"
"ds_read_b64_tr_b16 a[200:201], v251 offset:41472\n\t"
"v_mfma_f32_16x16x32_bf16 v[132:135], a[44:47], a[164:167], v[132:135]\n\t"
"buffer_load_dwordx4 a[0:3], off, s[96:99], s58 offset:0 sc1\n\t"
"v_mfma_f32_16x16x32_bf16 v[116:119], a[32:35], a[148:151], v[116:119]\n\t"
"s_nop 0\n\t"
"ds_read_b64_tr_b16 a[196:197], v251 offset:41728\n\t"
"ds_read_b64_tr_b16 a[204:205], v250 offset:41728\n\t"
"v_mfma_f32_16x16x32_bf16 v[116:119], a[36:39], a[164:167], v[116:119]\n\t"
"buffer_load_dwordx4 a[4:7], off, s[96:99], s58 offset:1024 sc1\n\t"
"v_mfma_f32_16x16x32_bf16 v[100:103], a[24:27], a[148:151], v[100:103]\n\t"
"s_waitcnt lgkmcnt(4)\n\t"
"v_mfma_f32_16x16x32_bf16 v[100:103], a[28:31], a[164:167], v[100:103]\n\t"
"s_barrier\n\t"
"v_mfma_f32_16x16x32_bf16 v[104:107], a[24:27], a[152:155], v[104:107]\n\t"
"s_nop 0\n\t"
"ds_read_b128 a[144:147], v224 offset:0\n\t"
"v_cvt_pk_bf16_f32 v144, v144, v145\n\t"
"v_mfma_f32_16x16x32_bf16 v[104:107], a[28:31], a[168:171], v[104:107]\n\t"
"s_nop 0\n\t"
"ds_read_b128 a[148:151], v224 offset:256\n\t"
"v_cvt_pk_bf16_f32 v180, v180, v181\n\t"
"v_mfma_f32_16x16x32_bf16 v[120:123], a[32:35], a[152:155], v[120:123]\n\t"
"s_nop 0\n\t"
"ds_read_b128 a[160:163], v224 offset:2048\n\t"
"v_cvt_pk_bf16_f32 v160, v160, v161\n\t"
"v_mfma_f32_16x16x32_bf16 v[120:123], a[36:39], a[168:171], v[120:123]\n\t"
"s_nop 0\n\t"
"ds_read_b128 a[164:167], v224 offset:2304\n\t"
"v_cvt_pk_bf16_f32 v161, v162, v163\n\t"
"v_mfma_f32_16x16x32_bf16 v[136:139], a[40:43], a[152:155], v[136:139]\n\t"
"s_nop 0\n\t"
"v_cvt_pk_bf16_f32 v145, v146, v147\n\t"
"v_cvt_pk_bf16_f32 v172, v172, v173\n\t"
"v_mfma_f32_16x16x32_bf16 v[136:139], a[44:47], a[168:171], v[136:139]\n\t"
"s_nop 0\n\t"
"v_cvt_pk_bf16_f32 v176, v176, v177\n\t"
"v_cvt_pk_bf16_f32 v168, v168, v169\n\t"
"v_mfma_f32_16x16x32_bf16 v[140:143], a[40:43], a[156:159], v[140:143]\n\t"
"s_nop 0\n\t"
"ds_read_b128 v[56:59], v229 offset:0\n\t"
"v_cvt_pk_bf16_f32 v148, v148, v149\n\t"
"v_mfma_f32_16x16x32_bf16 v[140:143], a[44:47], a[172:175], v[140:143]\n\t"
"s_nop 0\n\t"
"ds_read_b128 v[72:75], v230 offset:0\n\t"
"buffer_load_dwordx4 a[40:43], off, s[92:95], s58 offset:2048 sc1\n\t"
"v_mfma_f32_16x16x32_bf16 v[124:127], a[32:35], a[156:159], v[124:127]\n\t"
"s_nop 0\n\t"
"v_cvt_pk_bf16_f32 v177, v178, v179\n\t"
"v_cvt_pk_bf16_f32 v156, v156, v157\n\t"
"v_mfma_f32_16x16x32_bf16 v[124:127], a[36:39], a[172:175], v[124:127]\n\t"
"Lpad9_%=:\n\t"
"s_cbranch_execz Lret1_%=\n\t"
"buffer_load_dwordx4 a[44:47], off, s[92:95], s58 offset:3072 sc1\n\t"
"v_mfma_f32_16x16x32_bf16 v[108:111], a[24:27], a[156:159], v[108:111]\n\t"
"ds_read_b128 v[92:95], v231 offset:13312\n\t"
"v_cvt_pk_bf16_f32 v184, v184, v185\n\t"
"v_mfma_f32_16x16x32_bf16 v[108:111], a[28:31], a[172:175], v[108:111]\n\t"
"s_waitcnt lgkmcnt(5)\n\t"
"ds_read_b128 v[76:79], v230 offset:13312\n\t"
"v_cvt_pk_bf16_f32 v164, v164, v165\n\t"
"v_mfma_f32_4x4x4_16b_bf16 v[200:203], a[252:253], a[178:179], 0\n\t"
"buffer_load_dwordx4 a[36:39], off, s[92:95], s58 offset:1024 sc1\n\t"
"v_mfma_f32_4x4x4_16b_bf16 v[204:207], a[252:253], a[186:187], 0\n\t"
"v_cvt_pk_bf16_f32 v157, v158, v159\n\t"
"v_mfma_f32_4x4x4_16b_bf16 v[216:219], a[252:253], a[194:195], 0\n\t"
"ds_read_b128 v[88:91], v231 offset:0\n\t"
"v_mfma_f32_4x4x4_16b_bf16 v[220:223], a[252:253], a[202:203], 0\n\t"
"ds_read_b128 v[60:63], v229 offset:13312\n\t"
"buffer_load_dwordx4 a[32:35], off, s[92:95], s58 offset:0 sc1\n\t"
"v_mfma_f32_4x4x4_16b_bf16 v[192:195], a[254:255], a[176:177], v[200:203]\n\t"
"v_cvt_pk_bf16_f32 v149, v150, v151\n\t"
"v_cvt_pk_bf16_f32 v181, v182, v183\n\t"
"v_mfma_f32_4x4x4_16b_bf16 v[196:199], a[254:255], a[184:185], v[204:207]\n\t"
"ds_read_b128 a[156:159], v224 offset:768\n\t"
"v_mfma_f32_4x4x4_16b_bf16 v[200:203], a[254:255], a[180:181], v[200:203]\n\t"
"ds_read_b128 a[168:171], v224 offset:2560\n\t"
"buffer_load_dwordx4 a[24:27], off, s[96:99], s57 offset:2048 sc1\n\t"
"v_mfma_f32_4x4x4_16b_bf16 v[204:207], a[254:255], a[188:189], v[204:207]\n\t"
"v_cvt_pk_bf16_f32 v152, v152, v153\n\t"
"v_mfma_f32_4x4x4_16b_bf16 v[208:211], a[254:255], a[192:193], v[216:219]\n\t"
"ds_read_b128 a[152:155], v224 offset:512\n\t"
"v_mfma_f32_4x4x4_16b_bf16 v[212:215], a[254:255], a[200:201], v[220:223]\n\t"
"ds_read_b128 a[172:175], v224 offset:2816\n\t"
"s_waitcnt lgkmcnt(6)\n\t"
"buffer_store_dwordx4 v[56:59], v232, s[16:19], s24 offen nt\n\t"
"v_mfma_f32_4x4x4_16b_bf16 v[216:219], a[254:255], a[196:197], v[216:219]\n\t"
"v_cvt_pk_bf16_f32 v165, v166, v167\n\t"
"v_mfma_f32_4x4x4_16b_bf16 v[220:223], a[254:255], a[204:205], v[220:223]\n\t"
"s_waitcnt lgkmcnt(4)\n\t"
"s_barrier\n\t"
"v_mfma_f32_16x16x32_bf16 v[0:3], a[48:51], a[144:147], v[0:3]\n\t"
"ds_write2st64_b64 v228, v[144:145], v[148:149] offset0:0 offset1:13\n\t"
"v_mfma_f32_16x16x32_bf16 v[0:3], a[52:55], a[160:163], v[0:3]\n\t"
"buffer_load_dwordx4 a[28:31], off, s[96:99], s57 offset:3072 sc1\n\t"
"v_mfma_f32_16x16x32_bf16 v[16:19], a[56:59], a[144:147], v[16:19]\n\t"
"v_cvt_pk_bf16_f32 v203, v203, v207\n\t"
"v_cvt_pk_bf16_f32 v153, v154, v155\n\t"
"v_mfma_f32_16x16x32_bf16 v[16:19], a[60:63], a[160:163], v[16:19]\n\t"
"ds_write2st64_b64 v246, v[160:161], v[164:165] offset0:0 offset1:13\n\t"
"v_mfma_f32_16x16x32_bf16 v[32:35], a[64:67], a[144:147], v[32:35]\n\t"
"v_cvt_pk_bf16_f32 v208, -v208, -v212\n\t"
"v_cvt_pk_bf16_f32 v188, v188, v189\n\t"
"v_mfma_f32_16x16x32_bf16 v[32:35], a[68:71], a[160:163], v[32:35]\n\t"
"buffer_store_dwordx4 v[72:75], v233, s[16:19], s24 offen nt\n\t"
"v_mfma_f32_16x16x32_bf16 v[36:39], a[64:67], a[148:151], v[36:39]\n\t"
"v_cvt_pk_bf16_f32 v209, -v209, -v213\n\t"
"v_cvt_pk_bf16_f32 v218, v218, v222\n\t"
"v_mfma_f32_16x16x32_bf16 v[36:39], a[68:71], a[164:167], v[36:39]\n\t"
"s_nop 0\n\t"
"ds_read_b128 a[144:147], v224 offset:1024\n\t"
"ds_read_b128 a[160:163], v224 offset:3072\n\t"
"ds_write2st64_b64 v247, v[176:177], v[180:181] offset0:0 offset1:13\n\t"
"v_mfma_f32_16x16x32_bf16 v[20:23], a[56:59], a[148:151], v[20:23]\n\t"
"v_cvt_pk_bf16_f32 v194, -v194, -v198\n\t"
"v_cvt_pk_bf16_f32 v169, v170, v171\n\t"
"v_mfma_f32_16x16x32_bf16 v[20:23], a[60:63], a[164:167], v[20:23]\n\t"
"buffer_store_dwordx4 v[88:91], v234, s[16:19], s24 offen nt\n\t"
"v_mfma_f32_16x16x32_bf16 v[4:7], a[48:51], a[148:151], v[4:7]\n\t"
"v_cvt_pk_bf16_f32 v219, v219, v223\n\t"
"v_cvt_pk_bf16_f32 v173, v174, v175\n\t"
"v_mfma_f32_16x16x32_bf16 v[4:7], a[52:55], a[164:167], v[4:7]\n\t"
"s_waitcnt lgkmcnt(4)\n\t"
"ds_read_b128 a[148:151], v224 offset:1280\n\t"
"ds_read_b128 a[164:167], v224 offset:3328\n\t"
"ds_write2st64_b64 v246, v[168:169], v[172:173] offset0:26 offset1:39\n\t"
"v_mfma_f32_16x16x32_bf16 v[8:11], a[48:51], a[152:155], v[8:11]\n\t"
"v_cvt_pk_bf16_f32 v201, v201, v205\n\t"
"v_cvt_pk_bf16_f32 v216, v216, v220\n\t"
"v_mfma_f32_16x16x32_bf16 v[8:11], a[52:55], a[168:171], v[8:11]\n\t"
"buffer_store_dwordx4 v[60:63], v232, s[16:19], s27 offen nt\n\t"
"v_mfma_f32_16x16x32_bf16 v[24:27], a[56:59], a[152:155], v[24:27]\n\t"
"v_cvt_pk_bf16_f32 v200, v200, v204\n\t"
"v_cvt_pk_bf16_f32 v211, -v211, -v215\n\t"
"v_mfma_f32_16x16x32_bf16 v[24:27], a[60:63], a[168:171], v[24:27]\n\t"
"s_nop 0\n\t"
"ds_write_addtid_b32 v208 offset:18592\n\t"
"ds_write_addtid_b32 v209 offset:22720\n\t"
"ds_write_addtid_b32 v216 offset:26912\n\t"
"ds_write_addtid_b32 v219 offset:32064\n\t"
"ds_write2st64_b64 v228, v[152:153], v[156:157] offset0:26 offset1:39\n\t"
"v_mfma_f32_16x16x32_bf16 v[40:43], a[64:67], a[152:155], v[40:43]\n\t"
"v_cvt_pk_bf16_f32 v185, v186, v187\n\t"
"v_cvt_pk_bf16_f32 v189, v190, v191\n\t"
"v_mfma_f32_16x16x32_bf16 v[40:43], a[68:71], a[168:171], v[40:43]\n\t"
"v_cvt_pk_bf16_f32 v192, -v192, -v196\n\t"
"v_cvt_pk_bf16_f32 v210, -v210, -v214\n\t"
"v_mfma_f32_16x16x32_bf16 v[44:47], a[64:67], a[156:159], v[44:47]\n\t"
"buffer_load_dwordx4 a[64:67], off, s[92:95], s65 offset:0 sc1\n\t"
"v_mfma_f32_16x16x32_bf16 v[44:47], a[68:71], a[172:175], v[44:47]\n\t"
"ds_write_addtid_b32 v200 offset:24864\n\t"
"ds_write2st64_b64 v247, v[184:185], v[188:189] offset0:26 offset1:39\n\t"
"v_mfma_f32_16x16x32_bf16 v[28:31], a[56:59], a[156:159], v[28:31]\n\t"
"buffer_load_dwordx4 a[68:71], off, s[92:95], s65 offset:1024 sc1\n\t"
"v_mfma_f32_16x16x32_bf16 v[28:31], a[60:63], a[172:175], v[28:31]\n\t"
"v_cvt_pk_bf16_f32 v202, v202, v206\n\t"
"v_cvt_pk_bf16_f32 v217, v217, v221\n\t"
"v_mfma_f32_16x16x32_bf16 v[12:15], a[48:51], a[156:159], v[12:15]\n\t"
"v_cvt_pk_bf16_f32 v193, -v193, -v197\n\t"
"v_cvt_pk_bf16_f32 v195, -v195, -v199\n\t"
"v_mfma_f32_16x16x32_bf16 v[12:15], a[52:55], a[172:175], v[12:15]\n\t"
"s_waitcnt lgkmcnt(7)\n\t"
"ds_write_addtid_b32 v192 offset:16544\n\t"
"ds_write_addtid_b32 v201 offset:28992\n\t"
"ds_write_addtid_b32 v203 offset:30016\n\t"
"ds_write_addtid_b32 v211 offset:23744\n\t"
"ds_write_addtid_b32 v218 offset:27936\n\t"
"buffer_store_dwordx4 v[76:79], v233, s[16:19], s27 offen nt\n\t"
"v_mfma_f32_16x16x32_bf16 v[96:99], a[72:75], a[144:147], v[96:99]\n\t"
"s_nop 0\n\t"
"ds_write_addtid_b32 v210 offset:19616\n\t"
"ds_write_addtid_b32 v217 offset:31040\n\t"
"v_mfma_f32_16x16x32_bf16 v[96:99], a[76:79], a[160:163], v[96:99]\n\t"
"s_nop 0\n\t"
"ds_write_addtid_b32 v193 offset:20672\n\t"
"ds_write_addtid_b32 v194 offset:17568\n\t"
"v_mfma_f32_16x16x32_bf16 v[112:115], a[80:83], a[144:147], v[112:115]\n\t"
"s_nop 0\n\t"
"ds_write_addtid_b32 v195 offset:21696\n\t"
"ds_write_addtid_b32 v202 offset:25888\n\t"
"v_mfma_f32_16x16x32_bf16 v[112:115], a[84:87], a[160:163], v[112:115]\n\t"
"buffer_load_dwordx4 a[60:63], off, s[96:99], s64 offset:3072 sc1\n\t"
"buffer_load_dwordx4 a[56:59], off, s[96:99], s64 offset:2048 sc1\n\t"
"v_mfma_f32_16x16x32_bf16 v[128:131], a[88:91], a[144:147], v[128:131]\n\t"
"ds_read_b128 a[152:155], v224 offset:1536\n\t"
"v_accvgpr_read_b32 v248, a208\n\t"
"v_mfma_f32_16x16x32_bf16 v[128:131], a[92:95], a[160:163], v[128:131]\n\t"
"ds_read_b128 a[168:171], v224 offset:3584\n\t"
"buffer_load_dwordx4 a[48:51], off, s[96:99], s64 offset:0 sc1\n\t"
"v_mfma_f32_16x16x32_bf16 v[132:135], a[88:91], a[148:151], v[132:135]\n\t"
"s_nop 0\n\t"
"ds_read_b128 a[156:159], v224 offset:1792\n\t"
"ds_read_b128 a[172:175], v224 offset:3840\n\t"
"v_mfma_f32_16x16x32_bf16 v[132:135], a[92:95], a[164:167], v[132:135]\n\t"
"buffer_store_dwordx4 v[92:95], v234, s[16:19], s27 offen nt\n\t"
"v_mfma_f32_16x16x32_bf16 v[116:119], a[80:83], a[148:151], v[116:119]\n\t"
"v_add_u32 v248, 101632, v248\n\t"
"v_xor_b32 v249, 32, v248\n\t"
"v_mfma_f32_16x16x32_bf16 v[116:119], a[84:87], a[164:167], v[116:119]\n\t"
"v_xor_b32 v250, 64, v248\n\t"
"v_xor_b32 v251, 96, v248\n\t"
"v_mfma_f32_16x16x32_bf16 v[100:103], a[72:75], a[148:151], v[100:103]\n\t"
"buffer_load_dwordx4 a[52:55], off, s[96:99], s64 offset:1024 sc1\n\t"
"v_mfma_f32_16x16x32_bf16 v[100:103], a[76:79], a[164:167], v[100:103]\n\t"
"s_waitcnt lgkmcnt(0)\n\t"
"s_barrier\n\t"
"v_mfma_f32_16x16x32_bf16 v[104:107], a[72:75], a[152:155], v[104:107]\n\t"
"s_nop 0\n\t"
"ds_read_b128 a[144:147], v224 offset:16544\n\t"
"ds_read_b128 a[160:163], v224 offset:18592\n\t"
"v_mfma_f32_16x16x32_bf16 v[104:107], a[76:79], a[168:171], v[104:107]\n\t"
"s_nop 0\n\t"
"ds_read_b128 a[148:151], v224 offset:16800\n\t"
"ds_read_b128 a[164:167], v224 offset:18848\n\t"
"v_mfma_f32_16x16x32_bf16 v[120:123], a[80:83], a[152:155], v[120:123]\n\t"
"ds_read_b64_tr_b16 a[176:177], v248 offset:20736\n\t"
"v_mfma_f32_16x16x32_bf16 v[120:123], a[84:87], a[168:171], v[120:123]\n\t"
"ds_read_b64_tr_b16 a[178:179], v249 offset:20736\n\t"
"v_mfma_f32_16x16x32_bf16 v[136:139], a[88:91], a[152:155], v[136:139]\n\t"
"s_nop 0\n\t"
"ds_read_b64_tr_b16 a[180:181], v248 offset:21248\n\t"
"ds_read_b64_tr_b16 a[182:183], v249 offset:21248\n\t"
"v_mfma_f32_16x16x32_bf16 v[136:139], a[92:95], a[168:171], v[136:139]\n\t"
"s_nop 0\n\t"
"ds_read_b64_tr_b16 a[184:185], v250 offset:23040\n\t"
"ds_read_b64_tr_b16 a[186:187], v251 offset:23040\n\t"
"v_mfma_f32_16x16x32_bf16 v[140:143], a[88:91], a[156:159], v[140:143]\n\t"
"ds_read_b64_tr_b16 a[188:189], v250 offset:23552\n\t"
"v_mfma_f32_16x16x32_bf16 v[140:143], a[92:95], a[172:175], v[140:143]\n\t"
"s_nop 0\n\t"
"ds_read_b64_tr_b16 a[190:191], v251 offset:23552\n\t"
"ds_read_b64_tr_b16 a[192:193], v250 offset:20736\n\t"
"v_mfma_f32_16x16x32_bf16 v[124:127], a[80:83], a[156:159], v[124:127]\n\t"
"s_nop 0\n\t"
"ds_read_b64_tr_b16 a[194:195], v251 offset:20736\n\t"
"ds_read_b64_tr_b16 a[196:197], v250 offset:21248\n\t"
"v_mfma_f32_16x16x32_bf16 v[124:127], a[84:87], a[172:175], v[124:127]\n\t"
"s_waitcnt lgkmcnt(7)\n\t"
"ds_read_b64_tr_b16 a[198:199], v251 offset:21248\n\t"
"ds_read_b64_tr_b16 a[200:201], v248 offset:23040\n\t"
"v_mfma_f32_4x4x4_16b_bf16 v[192:195], a[210:211], a[176:177], 0\n\t"
"ds_read_b64_tr_b16 a[202:203], v249 offset:23040\n\t"
"v_mfma_f32_4x4x4_16b_bf16 v[196:199], a[210:211], a[178:179], 0\n\t"
"ds_read_b128 v[152:155], v229 offset:0\n\t"
"v_mfma_f32_4x4x4_16b_bf16 v[200:203], a[210:211], a[180:181], 0\n\t"
"ds_read_b128 a[168:171], v224 offset:19104\n\t"
"v_mfma_f32_4x4x4_16b_bf16 v[204:207], a[210:211], a[182:183], 0\n\t"
"buffer_load_dwordx4 a[88:91], off, s[92:95], s64 offset:2048 sc1\n\t"
"v_mfma_f32_16x16x32_bf16 v[108:111], a[72:75], a[156:159], v[108:111]\n\t"
"s_nop 0\n\t"
"ds_read_b128 a[156:159], v224 offset:17312\n\t"
"ds_read_b64_tr_b16 a[204:205], v248 offset:23552\n\t"
"v_mfma_f32_16x16x32_bf16 v[108:111], a[76:79], a[172:175], v[108:111]\n\t"
"buffer_load_dwordx4 a[92:95], off, s[92:95], s64 offset:3072 sc1\n\t"
"v_mfma_f32_16x16x32_bf16 v[0:3], a[96:99], a[144:147], v[0:3]\n\t"
"ds_read_b128 v[168:171], v230 offset:0\n\t"
"v_cvt_pk_bf16_f32 v194, v194, v198\n\t"
"v_mfma_f32_16x16x32_bf16 v[0:3], a[100:103], a[160:163], v[0:3]\n\t"
"v_cvt_pk_bf16_f32 v196, v192, v196\n\t"
"v_cvt_pk_bf16_f32 v195, v195, v199\n\t"
"v_mfma_f32_16x16x32_bf16 v[16:19], a[104:107], a[144:147], v[16:19]\n\t"
"ds_read_b128 a[172:175], v224 offset:19360\n\t"
"v_cvt_pk_bf16_f32 v199, v201, v205\n\t"
"v_mfma_f32_16x16x32_bf16 v[16:19], a[108:111], a[160:163], v[16:19]\n\t"
"s_waitcnt lgkmcnt(5)\n\t"
"buffer_store_dwordx4 v[152:155], v232, s[16:19], s20 offen nt\n\t"
"v_mfma_f32_16x16x32_bf16 v[32:35], a[112:115], a[144:147], v[32:35]\n\t"
"ds_read_b128 a[152:155], v224 offset:17056\n\t"
"v_cvt_pk_bf16_f32 v198, v200, v204\n\t"
"v_mfma_f32_16x16x32_bf16 v[32:35], a[116:119], a[160:163], v[32:35]\n\t"
"ds_read_b128 v[184:187], v231 offset:0\n\t"
"v_cvt_pk_bf16_f32 v197, v193, v197\n\t"
"v_mfma_f32_4x4x4_16b_bf16 v[208:211], a[210:211], a[184:185], 0\n\t"
"ds_write_addtid_b32 v198 offset:8320\n\t"
"v_mfma_f32_4x4x4_16b_bf16 v[212:215], a[210:211], a[186:187], 0\n\t"
"s_waitcnt lgkmcnt(4)\n\t"
"buffer_store_dwordx4 v[168:171], v233, s[16:19], s20 offen nt\n\t"
"v_mfma_f32_4x4x4_16b_bf16 v[216:219], a[210:211], a[188:189], 0\n\t"
"ds_read_b128 v[156:159], v229 offset:13312\n\t"
"v_mfma_f32_4x4x4_16b_bf16 v[220:223], a[210:211], a[190:191], 0\n\t"
"ds_read_b128 a[144:147], v224 offset:17568\n\t"
"v_cvt_pk_bf16_f32 v193, v203, v207\n\t"
"v_mfma_f32_16x16x32_bf16 v[36:39], a[112:115], a[148:151], v[36:39]\n\t"
"ds_write_addtid_b32 v196 offset:0\n\t"
"v_cvt_pk_bf16_f32 v192, v202, v206\n\t"
"v_mfma_f32_16x16x32_bf16 v[36:39], a[116:119], a[164:167], v[36:39]\n\t"
"buffer_load_dwordx4 a[84:87], off, s[92:95], s64 offset:1024 sc1\n\t"
"v_mfma_f32_16x16x32_bf16 v[20:23], a[104:107], a[148:151], v[20:23]\n\t"
"ds_write_addtid_b32 v199 offset:9344\n\t"
"v_cvt_pk_bf16_f32 v212, v208, v212\n\t"
"v_mfma_f32_16x16x32_bf16 v[20:23], a[108:111], a[164:167], v[20:23]\n\t"
"ds_read_b128 v[172:175], v230 offset:13312\n\t"
"v_cvt_pk_bf16_f32 v210, v210, v214\n\t"
"v_mfma_f32_16x16x32_bf16 v[4:7], a[96:99], a[148:151], v[4:7]\n\t"
"v_cvt_pk_bf16_f32 v208, v218, v222\n\t"
"v_cvt_pk_bf16_f32 v213, v209, v213\n\t"
"v_mfma_f32_16x16x32_bf16 v[4:7], a[100:103], a[164:167], v[4:7]\n\t"
"s_waitcnt lgkmcnt(6)\n\t"
"buffer_store_dwordx4 v[184:187], v234, s[16:19], s20 offen nt\n\t"
"v_cvt_pk_bf16_f32 v211, v211, v215\n\t"
"v_mfma_f32_4x4x4_16b_bf16 v[200:203], a[210:211], a[192:193], 0\n\t"
"ds_read_b128 a[148:151], v224 offset:17824\n\t"
"v_cvt_pk_bf16_f32 v215, v217, v221\n\t"
"v_mfma_f32_4x4x4_16b_bf16 v[204:207], a[210:211], a[194:195], 0\n\t"
"ds_write_addtid_b32 v197 offset:1024\n\t"
"v_cvt_pk_bf16_f32 v214, v216, v220\n\t"
"v_mfma_f32_4x4x4_16b_bf16 v[196:199], a[210:211], a[196:197], 0\n\t"
"v_cvt_pk_bf16_f32 v209, v219, v223\n\t"
"v_mfma_f32_4x4x4_16b_bf16 v[216:219], a[210:211], a[198:199], 0\n\t"
"buffer_load_dwordx4 a[80:83], off, s[92:95], s64 offset:0 sc1\n\t"
"v_mfma_f32_16x16x32_bf16 v[8:11], a[96:99], a[152:155], v[8:11]\n\t"
"s_nop 0\n\t"
"ds_read_b128 a[160:163], v224 offset:19616\n\t"
"ds_write_addtid_b32 v215 offset:13472\n\t"
"v_mfma_f32_16x16x32_bf16 v[8:11], a[100:103], a[168:171], v[8:11]\n\t"
"s_nop 0\n\t"
"ds_write_addtid_b32 v212 offset:4128\n\t"
"ds_read_b64_tr_b16 a[206:207], v249 offset:23552\n\t"
"v_mfma_f32_16x16x32_bf16 v[24:27], a[104:107], a[152:155], v[24:27]\n\t"
"v_cvt_pk_bf16_f32 v204, v200, v204\n\t"
"v_cvt_pk_bf16_f32 v202, v202, v206\n\t"
"v_mfma_f32_16x16x32_bf16 v[24:27], a[108:111], a[168:171], v[24:27]\n\t"
"s_waitcnt lgkmcnt(6)\n\t"
"buffer_store_dwordx4 v[156:159], v232, s[16:19], s25 offen nt\n\t"
"v_cvt_pk_bf16_f32 v203, v203, v207\n\t"
"v_mfma_f32_16x16x32_bf16 v[40:43], a[112:115], a[152:155], v[40:43]\n\t"
"ds_read_b128 v[188:191], v231 offset:13312\n\t"
"v_cvt_pk_bf16_f32 v206, v196, v216\n\t"
"v_mfma_f32_16x16x32_bf16 v[40:43], a[116:119], a[168:171], v[40:43]\n\t"
"ds_read_b128 a[164:167], v224 offset:19872\n\t"
"v_cvt_pk_bf16_f32 v207, v197, v217\n\t"
"v_mfma_f32_16x16x32_bf16 v[44:47], a[112:115], a[156:159], v[44:47]\n\t"
"v_cvt_pk_bf16_f32 v200, v198, v218\n\t"
"v_cvt_pk_bf16_f32 v205, v201, v205\n\t"
"v_mfma_f32_16x16x32_bf16 v[44:47], a[116:119], a[172:175], v[44:47]\n\t"
"s_waitcnt lgkmcnt(2)\n\t"
"ds_write_addtid_b32 v213 offset:5152\n\t"
"buffer_load_dwordx4 a[72:75], off, s[96:99], s63 offset:2048 sc1\n\t"
"v_mfma_f32_4x4x4_16b_bf16 v[220:223], a[210:211], a[200:201], 0\n\t"
"ds_write_addtid_b32 v214 offset:12448\n\t"
"v_mfma_f32_4x4x4_16b_bf16 v[212:215], a[210:211], a[202:203], 0\n\t"
"v_cvt_pk_bf16_f32 v201, v199, v219\n\t"
"v_mfma_f32_4x4x4_16b_bf16 v[196:199], a[210:211], a[204:205], 0\n\t"
"buffer_store_dwordx4 v[172:175], v233, s[16:19], s25 offen nt\n\t"
"v_mfma_f32_4x4x4_16b_bf16 v[216:219], a[210:211], a[206:207], 0\n\t"
"s_nop 0\n\t"
"ds_write_addtid_b32 v205 offset:3072\n\t"
"ds_write_addtid_b32 v207 offset:11392\n\t"
"v_mfma_f32_16x16x32_bf16 v[28:31], a[104:107], a[156:159], v[28:31]\n\t"
"s_nop 0\n\t"
"ds_write_addtid_b32 v204 offset:2048\n\t"
"ds_write_addtid_b32 v206 offset:10368\n\t"
"v_mfma_f32_16x16x32_bf16 v[28:31], a[108:111], a[172:175], v[28:31]\n\t"
"s_waitcnt lgkmcnt(7)\n\t"
"buffer_store_dwordx4 v[188:191], v234, s[16:19], s25 offen nt\n\t"
"v_mfma_f32_16x16x32_bf16 v[12:15], a[96:99], a[156:159], v[12:15]\n\t"
"v_cvt_pk_bf16_f32 v222, v222, v214\n\t"
"v_cvt_pk_bf16_f32 v213, v221, v213\n\t"
"v_mfma_f32_16x16x32_bf16 v[12:15], a[100:103], a[172:175], v[12:15]\n\t"
"v_cvt_pk_bf16_f32 v212, v220, v212\n\t"
"v_cvt_pk_bf16_f32 v223, v223, v215\n\t"
"v_mfma_f32_16x16x32_bf16 v[96:99], a[120:123], a[144:147], v[96:99]\n\t"
"ds_write_addtid_b32 v213 offset:7200\n\t"
"v_cvt_pk_bf16_f32 v214, v196, v216\n\t"
"v_mfma_f32_16x16x32_bf16 v[96:99], a[124:127], a[160:163], v[96:99]\n\t"
"ds_write_addtid_b32 v214 offset:14496\n\t"
"v_cvt_pk_bf16_f32 v215, v197, v217\n\t"
"v_mfma_f32_16x16x32_bf16 v[112:115], a[128:131], a[144:147], v[112:115]\n\t"
"s_nop 0\n\t"
"ds_write_addtid_b32 v212 offset:6176\n\t"
"ds_write_addtid_b32 v215 offset:15520\n\t"
"v_mfma_f32_16x16x32_bf16 v[112:115], a[132:135], a[160:163], v[112:115]\n\t"
"buffer_load_dwordx4 a[76:79], off, s[96:99], s63 offset:3072 sc1\n\t"
"v_mfma_f32_16x16x32_bf16 v[128:131], a[136:139], a[144:147], v[128:131]\n\t"
"ds_read_b128 a[152:155], v224 offset:18080\n\t"
"v_cvt_pk_bf16_f32 v221, v199, v219\n\t"
"v_mfma_f32_16x16x32_bf16 v[128:131], a[140:143], a[160:163], v[128:131]\n\t"
"s_waitcnt lgkmcnt(7)\n\t"
"ds_read_b128 a[172:175], v224 offset:20384\n\t"
"v_mfma_f32_16x16x32_bf16 v[132:135], a[136:139], a[148:151], v[132:135]\n\t"
"s_nop 0\n\t"
"ds_read_b128 a[168:171], v224 offset:20128\n\t"
"ds_read_b128 a[156:159], v224 offset:18336\n\t"
"v_mfma_f32_16x16x32_bf16 v[132:135], a[140:143], a[164:167], v[132:135]\n\t"
"buffer_load_dwordx4 a[112:115], off, s[92:95], s47 offset:0 sc1\n\t"
"v_mfma_f32_16x16x32_bf16 v[116:119], a[128:131], a[148:151], v[116:119]\n\t"
"v_cvt_pk_bf16_f32 v220, v198, v218\n\t"
"v_mfma_f32_16x16x32_bf16 v[116:119], a[132:135], a[164:167], v[116:119]\n\t"
"buffer_load_dwordx4 a[116:119], off, s[92:95], s47 offset:1024 sc1\n\t"
"v_mfma_f32_16x16x32_bf16 v[100:103], a[120:123], a[148:151], v[100:103]\n\t"
"s_waitcnt lgkmcnt(0)\n\t"
"v_accvgpr_read_b32 v224, a213\n\t"
"v_mfma_f32_16x16x32_bf16 v[100:103], a[124:127], a[164:167], v[100:103]\n\t"
"buffer_load_dwordx4 a[108:111], off, s[96:99], s46 offset:3072 sc1\n\t"
"s_barrier\n\t"
"s_add_u32 s71, s71, 1\n\t"
"v_mov_b32 v215, s71\n\t"
"s_mov_b64 exec, 1\n\t"
"buffer_store_dword v215, off, s[8:11], s70 sc1\n\t"
"s_mov_b64 exec, -1\n\t"
"s_sub_u32 s21, s21, 1\n\t"
"s_xor_b64 s[84:85], s[84:85], s[88:89]\n\t"
"s_add_u32 s28, s84, 4096\n\t"
"s_addc_u32 s29, s85, 0\n\t"
"s_branch Lunit_%=\n\t"
"Lfb_%=:\n\t"
"v_mbcnt_lo_u32_b32 v162, -1, 0\n\t"
"v_mbcnt_hi_u32_b32 v162, -1, v162\n\t"
"v_bfe_u32 v161, v162, 1, 2\n\t"
"v_lshlrev_b32 v163, 4, v162\n\t"
"v_and_or_b32 v161, v163, 16, v161\n\t"
"v_mul_u32_u24 v161, 0xc0, v161\n\t"
"v_lshrrev_b32 v163, 5, v162\n\t"
"v_mul_u32_u24 v163, 12, v163\n\t"
"v_bfe_u32 v164, v162, 3, 2\n\t"
"v_add3_u32 v161, v161, v163, v164\n\t"
"v_lshlrev_b32 v161, 1, v161\n\t"
"v_lshlrev_b32 v162, 1, v162\n\t"
"v_mov_b32 v152, 0\n\t"
"v_mov_b32 v153, 0\n\t"
"v_mov_b32 v234, 0\n\t"
"v_mov_b32 v233, 0\n\t"
"v_mov_b32 v156, 0\n\t"
"v_mov_b32 v157, 0\n\t"
"v_mov_b32 v158, 0\n\t"
"v_mov_b32 v159, 0\n\t"
"v_mov_b32 v160, 0\n\t"
"s_mov_b32 s67, 0\n\t"
"Lfb0_%=:\n\t"
"s_mov_b32 s69, 0\n\t"
"Lfb1_%=:\n\t"
"s_lshr_b32 s60, s69, 5\n\t"
"s_mul_i32 s56, s60, 12288\n\t"
"s_bfe_u32 s91, s69, 0x20001\n\t"
"s_mul_i32 s91, s91, 1536\n\t"
"s_add_u32 s56, s56, s91\n\t"
"s_bitcmp1_b32 s69, 0\n\t"
"s_cselect_b32 s91, 48, 0\n\t"
"s_add_u32 s56, s56, s91\n\t"
"s_bfe_u32 s90, s69, 0x20003\n\t"
"s_mul_i32 s91, s90, 96\n\t"
"s_add_u32 s56, s56, s91\n\t"
"s_lshl3_add_u32 s56, s67, s56\n\t"
"s_lshr_b32 s60, s69, 5\n\t"
"s_bfe_u32 s90, s69, 0x20003\n\t"
"s_and_b32 s91, s60, 1\n\t"
"s_lshl1_add_u32 s91, s67, s91\n\t"
"s_mul_i32 s90, s90, 288\n\t"
"s_add_u32 s91, s91, s90\n\t"
"s_lshr_b32 s60, s60, 1\n\t"
"s_mul_i32 s90, s60, 96\n\t"
"s_add_u32 s91, s91, s90\n\t"
"s_lshl_b32 s57, s91, 10\n\t"
"s_and_b32 s91, s69, 7\n\t"
"s_lshl_b32 s91, s91, 7\n\t"
"s_add_u32 s57, s57, s91\n\t"
"s_add_u32 s60, s56, 0\n\t"
"buffer_load_short_d16_hi v152, v161, s[52:55], s60 offen\n\t"
"s_add_u32 s60, s56, 73728\n\t"
"buffer_load_short_d16_hi v153, v161, s[52:55], s60 offen\n\t"
"s_add_u32 s60, s56, 147456\n\t"
"buffer_load_short_d16_hi v234, v161, s[52:55], s60 offen\n\t"
"s_add_u32 s60, s56, 221184\n\t"
"buffer_load_short_d16_hi v233, v161, s[52:55], s60 offen\n\t"
"s_add_u32 s60, s56, 294912\n\t"
"buffer_load_short_d16_hi v156, v161, s[52:55], s60 offen\n\t"
"s_add_u32 s60, s56, 368640\n\t"
"buffer_load_short_d16_hi v157, v161, s[52:55], s60 offen\n\t"
"s_add_u32 s60, s56, 442368\n\t"
"buffer_load_short_d16_hi v158, v161, s[52:55], s60 offen\n\t"
"s_add_u32 s60, s56, 516096\n\t"
"buffer_load_short_d16_hi v159, v161, s[52:55], s60 offen\n\t"
"s_add_u32 s60, s56, 589824\n\t"
"buffer_load_short_d16_hi v160, v161, s[52:55], s60 offen\n\t"
"s_waitcnt vmcnt(2)\n\t"
"v_add_f32 v231, v152, v158\n\t"
"s_waitcnt vmcnt(1)\n\t"
"v_add_f32 v169, v153, v159\n\t"
"s_waitcnt vmcnt(0)\n\t"
"v_add_f32 v170, v234, v160\n\t"
"v_add_f32 v171, v231, v233\n\t"
"v_sub_f32 v174, v231, v233\n\t"
"v_add_f32 v172, v169, v156\n\t"
"v_sub_f32 v175, v169, v156\n\t"
"v_add_f32 v173, v170, v157\n\t"
"v_sub_f32 v230, v170, v157\n\t"
"v_add_f32 v177, v152, v234\n\t"
"v_add_f32 v178, v171, v173\n\t"
"v_add_f32 v179, v174, v230\n\t"
"v_add_f32 v180, v158, v160\n\t"
"v_add_f32 v182, v177, v153\n\t"
"v_sub_f32 v183, v177, v153\n\t"
"v_add_f32 v184, v178, v172\n\t"
"v_sub_f32 v185, v178, v172\n\t"
"v_add_f32 v186, v179, v175\n\t"
"v_sub_f32 v187, v179, v175\n\t"
"v_add_f32 v188, v180, v159\n\t"
"v_sub_f32 v189, v180, v159\n\t"
"v_mul_f32 v191, 0.5, v182\n\t"
"v_mul_f32 v229, 0.5, v183\n\t"
"v_mul_f32 v225, 0.5, v171\n\t"
"v_mul_f32 v223, 0x3e800000, v184\n\t"
"v_mul_f32 v222, 0x3e800000, v185\n\t"
"v_mul_f32 v221, -0.5, v173\n\t"
"v_mul_f32 v219, 0.5, v186\n\t"
"v_mul_f32 v218, 0.5, v187\n\t"
"v_mul_f32 v215, -0.5, v188\n\t"
"v_mul_f32 v214, -0.5, v189\n\t"
"v_cvt_pk_bf16_f32 v150, v152, v191\n\t"
"v_cvt_pk_bf16_f32 v149, v229, -v234\n\t"
"v_cvt_pk_bf16_f32 v148, v225, v223\n\t"
"v_cvt_pk_bf16_f32 v147, v222, v221\n\t"
"v_cvt_pk_bf16_f32 v146, v174, v219\n\t"
"v_cvt_pk_bf16_f32 v145, v218, -v230\n\t"
"v_cvt_pk_bf16_f32 v212, -v158, v215\n\t"
"v_cvt_pk_bf16_f32 v213, v214, v160\n\t"
"s_add_u32 s60, s57, 0\n\t"
"buffer_store_short v150, v162, s[8:11], s60 offen sc1\n\t"
"s_add_u32 s60, s57, 6144\n\t"
"buffer_store_short_d16_hi v150, v162, s[8:11], s60 offen sc1\n\t"
"s_add_u32 s60, s57, 12288\n\t"
"buffer_store_short v149, v162, s[8:11], s60 offen sc1\n\t"
"s_add_u32 s60, s57, 18432\n\t"
"buffer_store_short_d16_hi v149, v162, s[8:11], s60 offen sc1\n\t"
"s_add_u32 s60, s57, 24576\n\t"
"buffer_store_short v148, v162, s[8:11], s60 offen sc1\n\t"
"s_add_u32 s60, s57, 30720\n\t"
"buffer_store_short_d16_hi v148, v162, s[8:11], s60 offen sc1\n\t"
"s_add_u32 s60, s57, 36864\n\t"
"buffer_store_short v147, v162, s[8:11], s60 offen sc1\n\t"
"s_add_u32 s60, s57, 43008\n\t"
"buffer_store_short_d16_hi v147, v162, s[8:11], s60 offen sc1\n\t"
"s_add_u32 s60, s57, 49152\n\t"
"buffer_store_short v146, v162, s[8:11], s60 offen sc1\n\t"
"s_add_u32 s60, s57, 55296\n\t"
"buffer_store_short_d16_hi v146, v162, s[8:11], s60 offen sc1\n\t"
"s_add_u32 s60, s57, 61440\n\t"
"buffer_store_short v145, v162, s[8:11], s60 offen sc1\n\t"
"s_add_u32 s60, s57, 67584\n\t"
"buffer_store_short_d16_hi v145, v162, s[8:11], s60 offen sc1\n\t"
"s_add_u32 s60, s57, 73728\n\t"
"buffer_store_short v212, v162, s[8:11], s60 offen sc1\n\t"
"s_add_u32 s60, s57, 79872\n\t"
"buffer_store_short_d16_hi v212, v162, s[8:11], s60 offen sc1\n\t"
"s_add_u32 s60, s57, 86016\n\t"
"buffer_store_short v213, v162, s[8:11], s60 offen sc1\n\t"
"s_add_u32 s60, s57, 92160\n\t"
"buffer_store_short_d16_hi v213, v162, s[8:11], s60 offen sc1\n\t"
"s_waitcnt vmcnt(0)\n\t"
"s_add_u32 s69, s69, 1\n\t"
"s_cmp_lt_u32 s69, 192\n\t"
"s_cbranch_scc1 Lfb1_%=\n\t"
"s_add_u32 s67, s67, 1\n\t"
"s_cmp_lt_u32 s67, 3\n\t"
"s_cbranch_scc1 Lfb0_%=\n\t"
"s_branch Lgo_%=\n\t"
".p2align 6\n\t.fill 448, 4, 0xbf800000\n\t"
"Lpad10_%=:\n\t"
"s_cbranch_execz Lret1_%=\n\t"
"s_nop 0\n\t"
"Ltail_%=:\n\t"
"v_mfma_f32_16x16x32_bf16 v[104:107], a[72:75], a[152:155], v[104:107]\n\t"
"s_nop 0\n\t"
"ds_read_b128 a[144:147], v224 offset:17568\n\t"
"ds_read_b128 a[160:163], v224 offset:19616\n\t"
"v_mfma_f32_16x16x32_bf16 v[104:107], a[76:79], a[168:171], v[104:107]\n\t"
"buffer_load_dwordx4 a[20:23], off, s[28:31], s69 offset:1024 sc1\n\t"
"v_mfma_f32_16x16x32_bf16 v[120:123], a[80:83], a[152:155], v[120:123]\n\t"
"ds_read_b128 a[148:151], v224 offset:17824\n\t"
"v_add_u32 v248, 33280, v226\n\t"
"v_mfma_f32_16x16x32_bf16 v[120:123], a[84:87], a[168:171], v[120:123]\n\t"
"ds_read_b128 a[164:167], v224 offset:19872\n\t"
"v_xor_b32 v249, 32, v248\n\t"
"v_mfma_f32_16x16x32_bf16 v[136:139], a[88:91], a[152:155], v[136:139]\n\t"
"ds_read_b128 a[152:155], v224 offset:18080\n\t"
"v_mfma_f32_16x16x32_bf16 v[136:139], a[92:95], a[168:171], v[136:139]\n\t"
"buffer_load_dwordx4 a[12:15], off, s[84:87], s69 offset:3072 sc1\n\t"
"v_mfma_f32_16x16x32_bf16 v[140:143], a[88:91], a[156:159], v[140:143]\n\t"
"ds_read_b128 v[8:11], v229 offset:20480\n\t"
"v_mfma_f32_16x16x32_bf16 v[140:143], a[92:95], a[172:175], v[140:143]\n\t"
"buffer_load_dwordx4 a[8:11], off, s[84:87], s69 offset:2048 sc1\n\t"
"v_mfma_f32_16x16x32_bf16 v[124:127], a[80:83], a[156:159], v[124:127]\n\t"
"ds_read_b128 a[168:171], v224 offset:20128\n\t"
"v_xor_b32 v250, 64, v248\n\t"
"v_mfma_f32_16x16x32_bf16 v[124:127], a[84:87], a[172:175], v[124:127]\n\t"
"buffer_load_dwordx4 a[0:3], off, s[84:87], s69 offset:0 sc1\n\t"
"v_mfma_f32_16x16x32_bf16 v[108:111], a[72:75], a[156:159], v[108:111]\n\t"
"ds_read_b128 a[156:159], v224 offset:18336\n\t"
"v_xor_b32 v251, 96, v248\n\t"
"v_mfma_f32_16x16x32_bf16 v[108:111], a[76:79], a[172:175], v[108:111]\n\t"
"buffer_load_dwordx4 a[4:7], off, s[84:87], s69 offset:1024 sc1\n\t"
"v_mfma_f32_16x16x32_bf16 v[192:195], a[224:227], a[176:179], 0\n\t"
"s_nop 0\n\t"
"ds_read_b64_tr_b16 a[178:179], v248 offset:20864\n\t"
"ds_read_b64_tr_b16 a[176:177], v248 offset:20736\n\t"
"v_mfma_f32_16x16x32_bf16 v[196:199], a[224:227], a[180:183], 0\n\t"
"ds_read_b64_tr_b16 a[180:181], v249 offset:20992\n\t"
"v_mfma_f32_16x16x32_bf16 v[200:203], a[224:227], a[184:187], 0\n\t"
"s_nop 0\n\t"
"ds_read_b64_tr_b16 a[186:187], v249 offset:20864\n\t"
"ds_read_b64_tr_b16 a[184:185], v249 offset:20736\n\t"
"v_mfma_f32_16x16x32_bf16 v[204:207], a[224:227], a[188:191], 0\n\t"
"s_waitcnt lgkmcnt(8)\n\t"
"s_waitcnt lgkmcnt(7)\n\t"
"buffer_store_dwordx4 v[8:11], v232, s[16:19], s24 offen nt\n\t"
"v_mfma_f32_16x16x32_bf16 v[208:211], a[224:227], a[192:195], 0\n\t"
"ds_read_b128 v[24:27], v230 offset:20480\n\t"
"v_mfma_f32_16x16x32_bf16 v[212:215], a[224:227], a[196:199], 0\n\t"
"ds_read_b64_tr_b16 a[188:189], v248 offset:20992\n\t"
"v_mfma_f32_16x16x32_bf16 v[216:219], a[224:227], a[200:203], 0\n\t"
"s_nop 0\n\t"
"ds_read_b64_tr_b16 a[194:195], v250 offset:20864\n\t"
"ds_read_b64_tr_b16 a[192:193], v250 offset:20736\n\t"
"v_mfma_f32_16x16x32_bf16 v[220:223], a[224:227], a[204:207], 0\n\t"
"s_waitcnt vmcnt(18) lgkmcnt(10)\n\t"
"buffer_load_dwordx4 a[40:43], off, s[28:31], s54 offset:0 sc1\n\t"
"v_mfma_f32_16x16x32_bf16 v[144:147], a[96:99], a[144:147], v[144:147]\n\t"
"ds_read_b64_tr_b16 a[196:197], v251 offset:20992\n\t"
"v_cvt_pk_bf16_f32 v195, v195, v199\n\t"
"v_mfma_f32_16x16x32_bf16 v[144:147], a[100:103], a[160:163], v[144:147]\n\t"
"ds_read_b128 a[172:175], v224 offset:20384\n\t"
"v_cvt_pk_bf16_f32 v194, v194, v198\n\t"
"v_mfma_f32_16x16x32_bf16 v[160:163], a[104:107], a[144:147], v[160:163]\n\t"
"ds_read_b64_tr_b16 a[202:203], v251 offset:20864\n\t"
"v_cvt_pk_bf16_f32 v200, v200, v204\n\t"
"v_mfma_f32_16x16x32_bf16 v[160:163], a[108:111], a[160:163], v[160:163]\n\t"
"buffer_load_dwordx4 a[44:47], off, s[28:31], s54 offset:1024 sc1\n\t"
"v_mfma_f32_16x16x32_bf16 v[176:179], a[112:115], a[144:147], v[176:179]\n\t"
"ds_read_b128 v[12:15], v229 offset:33792\n\t"
"v_cvt_pk_bf16_f32 v209, v209, v213\n\t"
"v_mfma_f32_16x16x32_bf16 v[176:179], a[116:119], a[160:163], v[176:179]\n\t"
"ds_read_b128 a[144:147], v224 offset:16544\n\t"
"v_cvt_pk_bf16_f32 v193, v193, v197\n\t"
"v_mfma_f32_16x16x32_bf16 v[180:183], a[112:115], a[148:151], v[180:183]\n\t"
"ds_read_b128 a[160:163], v224 offset:18592\n\t"
"v_cvt_pk_bf16_f32 v216, v216, v220\n\t"
"v_mfma_f32_16x16x32_bf16 v[180:183], a[116:119], a[164:167], v[180:183]\n\t"
"s_waitcnt lgkmcnt(8)\n\t"
"buffer_store_dwordx4 v[24:27], v233, s[16:19], s24 offen nt\n\t"
"v_mfma_f32_16x16x32_bf16 v[164:167], a[104:107], a[148:151], v[164:167]\n\t"
"ds_write_addtid_b32 v200 offset:2048\n\t"
"v_cvt_pk_bf16_f32 v217, v217, v221\n\t"
"v_mfma_f32_16x16x32_bf16 v[164:167], a[108:111], a[164:167], v[164:167]\n\t"
"s_nop 0\n\t"
"ds_write_addtid_b32 v193 offset:8320\n\t"
"ds_write_addtid_b32 v209 offset:9344\n\t"
"v_mfma_f32_16x16x32_bf16 v[148:151], a[96:99], a[148:151], v[148:151]\n\t"
"ds_read_b128 v[40:43], v231 offset:20480\n\t"
"v_cvt_pk_bf16_f32 v201, v201, v205\n\t"
"v_mfma_f32_16x16x32_bf16 v[148:151], a[100:103], a[164:167], v[148:151]\n\t"
"buffer_load_dwordx4 a[36:39], off, s[84:87], s54 offset:3072 sc1\n\t"
"v_mfma_f32_16x16x32_bf16 v[152:155], a[96:99], a[152:155], v[152:155]\n\t"
"ds_read_b128 v[28:31], v230 offset:33792\n\t"
"v_cvt_pk_bf16_f32 v192, v192, v196\n\t"
"v_mfma_f32_16x16x32_bf16 v[152:155], a[100:103], a[168:171], v[152:155]\n\t"
"ds_read_b128 a[148:151], v224 offset:16800\n\t"
"v_cvt_pk_bf16_f32 v210, v210, v214\n\t"
"v_mfma_f32_16x16x32_bf16 v[168:171], a[104:107], a[152:155], v[168:171]\n\t"
"ds_read_b128 a[164:167], v224 offset:18848\n\t"
"v_cvt_pk_bf16_f32 v219, v219, v223\n\t"
"v_mfma_f32_16x16x32_bf16 v[168:171], a[108:111], a[168:171], v[168:171]\n\t"
"buffer_load_dwordx4 a[32:35], off, s[84:87], s54 offset:2048 sc1\n\t"
"v_mfma_f32_16x16x32_bf16 v[184:187], a[112:115], a[152:155], v[184:187]\n\t"
"ds_read_b128 a[152:155], v224 offset:17056\n\t"
"v_cvt_pk_bf16_f32 v202, v202, v206\n\t"
"v_mfma_f32_16x16x32_bf16 v[184:187], a[116:119], a[168:171], v[184:187]\n\t"
"s_waitcnt lgkmcnt(8)\n\t"
"ds_read_b128 v[44:47], v231 offset:33792\n\t"
"v_cvt_pk_bf16_f32 v208, v208, v212\n\t"
"v_mfma_f32_16x16x32_bf16 v[188:191], a[112:115], a[156:159], v[188:191]\n\t"
"ds_write_addtid_b32 v195 offset:12448\n\t"
"v_cvt_pk_bf16_f32 v203, v203, v207\n\t"
"v_mfma_f32_16x16x32_bf16 v[188:191], a[116:119], a[172:175], v[188:191]\n\t"
"ds_write_addtid_b32 v194 offset:4128\n\t"
"v_cvt_pk_bf16_f32 v211, v211, v215\n\t"
"v_mfma_f32_16x16x32_bf16 v[172:175], a[104:107], a[156:159], v[172:175]\n\t"
"ds_write_addtid_b32 v201 offset:10368\n\t"
"v_cvt_pk_bf16_f32 v218, v218, v222\n\t"
"v_mfma_f32_16x16x32_bf16 v[172:175], a[108:111], a[172:175], v[172:175]\n\t"
"s_nop 0\n\t"
"ds_write_addtid_b32 v218 offset:7200\n\t"
"ds_write_addtid_b32 v219 offset:15520\n\t"
"v_mfma_f32_16x16x32_bf16 v[156:159], a[96:99], a[156:159], v[156:159]\n\t"
"s_nop 0\n\t"
"ds_write_addtid_b32 v202 offset:6176\n\t"
"ds_write_addtid_b32 v210 offset:5152\n\t"
"v_mfma_f32_16x16x32_bf16 v[156:159], a[100:103], a[172:175], v[156:159]\n\t"
"s_waitcnt vmcnt(12) lgkmcnt(10)\n\t"
"buffer_store_dwordx4 v[40:43], v234, s[16:19], s24 offen nt\n\t"
"v_mfma_f32_16x16x32_bf16 v[96:99], a[120:123], a[144:147], v[96:99]\n\t"
"s_nop 0\n\t"
"ds_write_addtid_b32 v192 offset:0\n\t"
"ds_write_addtid_b32 v211 offset:13472\n\t"
"v_mfma_f32_16x16x32_bf16 v[96:99], a[124:127], a[160:163], v[96:99]\n\t"
"s_nop 0\n\t"
"ds_write_addtid_b32 v203 offset:14496\n\t"
"ds_write_addtid_b32 v216 offset:3072\n\t"
"v_mfma_f32_16x16x32_bf16 v[112:115], a[128:131], a[144:147], v[112:115]\n\t"
"s_nop 0\n\t"
"ds_write_addtid_b32 v208 offset:1024\n\t"
"ds_write_addtid_b32 v217 offset:11392\n\t"
"v_mfma_f32_16x16x32_bf16 v[112:115], a[132:135], a[160:163], v[112:115]\n\t"
"buffer_load_dwordx4 a[24:27], off, s[84:87], s54 offset:0 sc1\n\t"
"v_mfma_f32_16x16x32_bf16 v[128:131], a[136:139], a[144:147], v[128:131]\n\t"
"ds_read_b128 a[172:175], v224 offset:19360\n\t"
"v_mfma_f32_16x16x32_bf16 v[128:131], a[140:143], a[160:163], v[128:131]\n\t"
"s_waitcnt lgkmcnt(12)\n\t"
"ds_read_b128 a[168:171], v224 offset:19104\n\t"
"v_mfma_f32_16x16x32_bf16 v[132:135], a[136:139], a[148:151], v[132:135]\n\t"
"ds_read_b128 a[156:159], v224 offset:17312\n\t"
"v_mfma_f32_16x16x32_bf16 v[132:135], a[140:143], a[164:167], v[132:135]\n\t"
"buffer_load_dwordx4 a[28:31], off, s[84:87], s54 offset:1024 sc1\n\t"
"v_mfma_f32_16x16x32_bf16 v[116:119], a[128:131], a[148:151], v[116:119]\n\t"
"s_nop 0\n\t"
"ds_read_b64_tr_b16 a[200:201], v251 offset:20736\n\t"
"ds_read_b64_tr_b16 a[204:205], v250 offset:20992\n\t"
"v_mfma_f32_16x16x32_bf16 v[116:119], a[132:135], a[164:167], v[116:119]\n\t"
"v_mfma_f32_16x16x32_bf16 v[100:103], a[120:123], a[148:151], v[100:103]\n\t"
"buffer_load_dwordx4 a[64:67], off, s[28:31], s57 offset:0 sc1\n\t"
"v_mfma_f32_16x16x32_bf16 v[100:103], a[124:127], a[164:167], v[100:103]\n\t"
"s_waitcnt lgkmcnt(0)\n\t"
"s_barrier\n\t"
"v_mfma_f32_16x16x32_bf16 v[104:107], a[120:123], a[152:155], v[104:107]\n\t"
"s_nop 0\n\t"
"ds_read_b128 a[144:147], v224 offset:0\n\t"
"ds_read_b128 a[148:151], v224 offset:256\n\t"
"v_mfma_f32_16x16x32_bf16 v[104:107], a[124:127], a[168:171], v[104:107]\n\t"
"buffer_store_dwordx4 v[12:15], v232, s[16:19], s27 offen nt\n\t"
"v_mfma_f32_16x16x32_bf16 v[120:123], a[128:131], a[152:155], v[120:123]\n\t"
"ds_read_b128 a[160:163], v224 offset:2048\n\t"
"v_mfma_f32_16x16x32_bf16 v[120:123], a[132:135], a[168:171], v[120:123]\n\t"
"ds_read_b128 a[164:167], v224 offset:2304\n\t"
"v_mfma_f32_16x16x32_bf16 v[136:139], a[136:139], a[152:155], v[136:139]\n\t"
"v_mfma_f32_16x16x32_bf16 v[136:139], a[140:143], a[168:171], v[136:139]\n\t"
"buffer_load_dwordx4 a[68:71], off, s[28:31], s57 offset:1024 sc1\n\t"
"v_mfma_f32_16x16x32_bf16 v[140:143], a[136:139], a[156:159], v[140:143]\n\t"
"ds_read_b128 a[168:171], v224 offset:2560\n\t"
"v_mfma_f32_16x16x32_bf16 v[140:143], a[140:143], a[172:175], v[140:143]\n\t"
"ds_read_b128 a[152:155], v224 offset:512\n\t"
"v_mfma_f32_16x16x32_bf16 v[124:127], a[128:131], a[156:159], v[124:127]\n\t"
"v_mfma_f32_16x16x32_bf16 v[124:127], a[132:135], a[172:175], v[124:127]\n\t"
"v_mfma_f32_16x16x32_bf16 v[108:111], a[120:123], a[156:159], v[108:111]\n\t"
"ds_read_b128 a[156:159], v224 offset:768\n\t"
"v_mfma_f32_16x16x32_bf16 v[108:111], a[124:127], a[172:175], v[108:111]\n\t"
"ds_read_b128 a[172:175], v224 offset:2816\n\t"
"buffer_load_dwordx4 a[60:63], off, s[84:87], s57 offset:3072 sc1\n\t"
"v_mfma_f32_4x4x4_16b_bf16 v[200:203], a[220:221], a[178:179], 0\n\t"
"ds_read_b64_tr_b16 a[178:179], v248 offset:128\n\t"
"v_mfma_f32_4x4x4_16b_bf16 v[204:207], a[220:221], a[186:187], 0\n\t"
"ds_read_b64_tr_b16 a[186:187], v249 offset:128\n\t"
"v_mfma_f32_4x4x4_16b_bf16 v[216:219], a[220:221], a[194:195], 0\n\t"
"ds_read_b64_tr_b16 a[194:195], v250 offset:128\n\t"
"v_mfma_f32_4x4x4_16b_bf16 v[220:223], a[220:221], a[202:203], 0\n\t"
"ds_read_b64_tr_b16 a[202:203], v251 offset:128\n\t"
"buffer_load_dwordx4 a[56:59], off, s[84:87], s57 offset:2048 sc1\n\t"
"v_mfma_f32_4x4x4_16b_bf16 v[192:195], a[222:223], a[176:177], v[200:203]\n\t"
"ds_read_b64_tr_b16 a[176:177], v248 offset:0\n\t"
"v_mfma_f32_4x4x4_16b_bf16 v[196:199], a[222:223], a[184:185], v[204:207]\n\t"
"ds_read_b64_tr_b16 a[184:185], v249 offset:0\n\t"
"v_mfma_f32_4x4x4_16b_bf16 v[200:203], a[222:223], a[180:181], v[200:203]\n\t"
".p2align 3\n\t"
"ds_read_b64_tr_b16 a[180:181], v249 offset:256\n\t"
"buffer_store_dwordx4 v[28:31], v233, s[16:19], s27 offen nt\n\t"
"v_pk_add_f32 v[96:97], v[96:97], v[144:145]\n\t"
"v_pk_add_f32 v[98:99], v[98:99], v[146:147]\n\t"
"v_pk_fma_f32 v[144:145], v[144:145], s[38:39], v[96:97]\n\t"
"v_pk_fma_f32 v[146:147], v[146:147], s[38:39], v[98:99]\n\t"
"v_pk_add_f32 v[112:113], v[112:113], v[160:161]\n\t"
"v_pk_add_f32 v[114:115], v[114:115], v[162:163]\n\t"
"v_pk_fma_f32 v[160:161], v[160:161], s[38:39], v[112:113]\n\t"
"v_pk_fma_f32 v[162:163], v[162:163], s[38:39], v[114:115]\n\t"
"v_pk_add_f32 v[128:129], v[128:129], v[176:177]\n\t"
"v_pk_add_f32 v[130:131], v[130:131], v[178:179]\n\t"
"v_pk_fma_f32 v[176:177], v[176:177], s[38:39], v[128:129]\n\t"
"v_pk_fma_f32 v[178:179], v[178:179], s[38:39], v[130:131]\n\t"
"v_pk_add_f32 v[132:133], v[132:133], v[180:181]\n\t"
"v_pk_add_f32 v[134:135], v[134:135], v[182:183]\n\t"
"v_pk_fma_f32 v[180:181], v[180:181], s[38:39], v[132:133]\n\t"
"v_pk_fma_f32 v[182:183], v[182:183], s[38:39], v[134:135]\n\t"
"v_pk_add_f32 v[116:117], v[116:117], v[164:165]\n\t"
"v_pk_add_f32 v[118:119], v[118:119], v[166:167]\n\t"
"v_pk_fma_f32 v[164:165], v[164:165], s[38:39], v[116:117]\n\t"
"v_pk_fma_f32 v[166:167], v[166:167], s[38:39], v[118:119]\n\t"
"v_pk_add_f32 v[100:101], v[100:101], v[148:149]\n\t"
"v_pk_add_f32 v[102:103], v[102:103], v[150:151]\n\t"
"v_pk_fma_f32 v[148:149], v[148:149], s[38:39], v[100:101]\n\t"
"v_pk_fma_f32 v[150:151], v[150:151], s[38:39], v[102:103]\n\t"
"v_pk_add_f32 v[104:105], v[104:105], v[152:153]\n\t"
"v_pk_add_f32 v[106:107], v[106:107], v[154:155]\n\t"
"v_pk_fma_f32 v[152:153], v[152:153], s[38:39], v[104:105]\n\t"
"v_pk_fma_f32 v[154:155], v[154:155], s[38:39], v[106:107]\n\t"
"v_pk_add_f32 v[120:121], v[120:121], v[168:169]\n\t"
"v_pk_add_f32 v[122:123], v[122:123], v[170:171]\n\t"
"v_pk_fma_f32 v[168:169], v[168:169], s[38:39], v[120:121]\n\t"
"v_pk_fma_f32 v[170:171], v[170:171], s[38:39], v[122:123]\n\t"
"v_pk_add_f32 v[136:137], v[136:137], v[184:185]\n\t"
"v_pk_add_f32 v[138:139], v[138:139], v[186:187]\n\t"
"v_pk_fma_f32 v[184:185], v[184:185], s[38:39], v[136:137]\n\t"
"v_pk_fma_f32 v[186:187], v[186:187], s[38:39], v[138:139]\n\t"
"v_pk_add_f32 v[140:141], v[140:141], v[188:189]\n\t"
"v_pk_add_f32 v[142:143], v[142:143], v[190:191]\n\t"
"v_pk_fma_f32 v[188:189], v[188:189], s[38:39], v[140:141]\n\t"
"v_pk_fma_f32 v[190:191], v[190:191], s[38:39], v[142:143]\n\t"
"v_pk_add_f32 v[124:125], v[124:125], v[172:173]\n\t"
"v_pk_add_f32 v[126:127], v[126:127], v[174:175]\n\t"
"v_pk_fma_f32 v[172:173], v[172:173], s[38:39], v[124:125]\n\t"
"v_pk_fma_f32 v[174:175], v[174:175], s[38:39], v[126:127]\n\t"
"v_pk_add_f32 v[108:109], v[108:109], v[156:157]\n\t"
"v_pk_add_f32 v[110:111], v[110:111], v[158:159]\n\t"
"v_pk_fma_f32 v[156:157], v[156:157], s[38:39], v[108:109]\n\t"
"v_pk_fma_f32 v[158:159], v[158:159], s[38:39], v[110:111]\n\t"
"v_cvt_pk_bf16_f32 v116, v116, v117\n\t"
"v_mfma_f32_4x4x4_16b_bf16 v[204:207], a[222:223], a[188:189], v[204:207]\n\t"
"ds_read_b64_tr_b16 a[188:189], v248 offset:256\n\t"
"v_cvt_pk_bf16_f32 v96, v96, v97\n\t"
"v_mfma_f32_4x4x4_16b_bf16 v[208:211], a[222:223], a[192:193], v[216:219]\n\t"
"v_cvt_pk_bf16_f32 v97, v98, v99\n\t"
"v_cvt_pk_bf16_f32 v100, v100, v101\n\t"
"v_cvt_pk_bf16_f32 v101, v102, v103\n\t"
"v_cvt_pk_bf16_f32 v112, v112, v113\n\t"
"v_mfma_f32_4x4x4_16b_bf16 v[212:215], a[222:223], a[200:201], v[220:223]\n\t"
"buffer_load_dwordx4 a[48:51], off, s[84:87], s57 offset:0 sc1\n\t"
"ds_write2st64_b64 v228, v[96:97], v[100:101] offset0:40 offset1:53\n\t"
"v_cvt_pk_bf16_f32 v117, v118, v119\n\t"
"v_mfma_f32_4x4x4_16b_bf16 v[216:219], a[222:223], a[196:197], v[216:219]\n\t"
"v_cvt_pk_bf16_f32 v124, v124, v125\n\t"
"v_cvt_pk_bf16_f32 v125, v126, v127\n\t"
"v_mfma_f32_4x4x4_16b_bf16 v[220:223], a[222:223], a[204:205], v[220:223]\n\t"
"s_waitcnt vmcnt(16) lgkmcnt(7)\n\t"
"v_cvt_pk_bf16_f32 v113, v114, v115\n\t"
"v_cvt_pk_bf16_f32 v140, v140, v141\n\t"
"v_mfma_f32_16x16x32_bf16 v[48:51], a[0:3], a[144:147], v[48:51]\n\t"
"v_cvt_pk_bf16_f32 v192, -v192, -v196\n\t"
"v_cvt_pk_bf16_f32 v120, v120, v121\n\t"
"v_mfma_f32_16x16x32_bf16 v[48:51], a[4:7], a[160:163], v[48:51]\n\t"
"ds_write_addtid_b32 v192 offset:16544\n\t"
"buffer_load_dwordx4 a[52:55], off, s[84:87], s57 offset:1024 sc1\n\t"
"ds_write2st64_b64 v246, v[112:113], v[116:117] offset0:40 offset1:53\n\t"
"v_cvt_pk_bf16_f32 v136, v136, v137\n\t"
"v_mfma_f32_16x16x32_bf16 v[64:67], a[8:11], a[144:147], v[64:67]\n\t"
"v_cvt_pk_bf16_f32 v104, v104, v105\n\t"
"v_cvt_pk_bf16_f32 v108, v108, v109\n\t"
"v_mfma_f32_16x16x32_bf16 v[64:67], a[12:15], a[160:163], v[64:67]\n\t"
"v_cvt_pk_bf16_f32 v128, v128, v129\n\t"
"v_cvt_pk_bf16_f32 v132, v132, v133\n\t"
"v_cvt_pk_bf16_f32 v133, v134, v135\n\t"
"v_mfma_f32_16x16x32_bf16 v[80:83], a[16:19], a[144:147], v[80:83]\n\t"
"v_cvt_pk_bf16_f32 v203, v203, v207\n\t"
"v_cvt_pk_bf16_f32 v129, v130, v131\n\t"
"v_mfma_f32_16x16x32_bf16 v[80:83], a[20:23], a[160:163], v[80:83]\n\t"
"s_nop 0\n\t"
"ds_read_b128 a[144:147], v224 offset:1024\n\t"
"ds_read_b128 a[160:163], v224 offset:3072\n\t"
"ds_write2st64_b64 v247, v[128:129], v[132:133] offset0:40 offset1:53\n\t"
"v_mfma_f32_16x16x32_bf16 v[84:87], a[16:19], a[148:151], v[84:87]\n\t"
"v_cvt_pk_bf16_f32 v105, v106, v107\n\t"
"v_cvt_pk_bf16_f32 v109, v110, v111\n\t"
"v_mfma_f32_16x16x32_bf16 v[84:87], a[20:23], a[164:167], v[84:87]\n\t"
"buffer_load_dwordx4 a[88:91], off, s[28:31], s63 offset:0 sc1\n\t"
"v_mfma_f32_16x16x32_bf16 v[68:71], a[8:11], a[148:151], v[68:71]\n\t"
"v_cvt_pk_bf16_f32 v195, -v195, -v199\n\t"
"v_cvt_pk_bf16_f32 v217, v217, v221\n\t"
"v_mfma_f32_16x16x32_bf16 v[68:71], a[12:15], a[164:167], v[68:71]\n\t"
"ds_write_addtid_b32 v203 offset:30016\n\t"
"ds_write2st64_b64 v228, v[104:105], v[108:109] offset0:66 offset1:79\n\t"
"v_mfma_f32_16x16x32_bf16 v[52:55], a[0:3], a[148:151], v[52:55]\n\t"
"v_cvt_pk_bf16_f32 v137, v138, v139\n\t"
"v_cvt_pk_bf16_f32 v141, v142, v143\n\t"
"v_mfma_f32_16x16x32_bf16 v[52:55], a[4:7], a[164:167], v[52:55]\n\t"
"buffer_store_dwordx4 v[44:47], v234, s[16:19], s27 offen nt\n\t"
"v_mfma_f32_16x16x32_bf16 v[56:59], a[0:3], a[152:155], v[56:59]\n\t"
"v_cvt_pk_bf16_f32 v211, -v211, -v215\n\t"
"v_cvt_pk_bf16_f32 v121, v122, v123\n\t"
"v_mfma_f32_16x16x32_bf16 v[56:59], a[4:7], a[168:171], v[56:59]\n\t"
"s_nop 0\n\t"
"ds_read_b128 a[148:151], v224 offset:1280\n\t"
"ds_read_b128 a[164:167], v224 offset:3328\n\t"
"ds_write2st64_b64 v247, v[136:137], v[140:141] offset0:66 offset1:79\n\t"
"v_mfma_f32_16x16x32_bf16 v[72:75], a[8:11], a[152:155], v[72:75]\n\t"
"v_cvt_pk_bf16_f32 v208, -v208, -v212\n\t"
"v_cvt_pk_bf16_f32 v210, -v210, -v214\n\t"
"v_mfma_f32_16x16x32_bf16 v[72:75], a[12:15], a[168:171], v[72:75]\n\t"
"buffer_load_dwordx4 a[92:95], off, s[28:31], s63 offset:1024 sc1\n\t"
"v_mfma_f32_16x16x32_bf16 v[88:91], a[16:19], a[152:155], v[88:91]\n\t"
"v_cvt_pk_bf16_f32 v200, v200, v204\n\t"
"v_cvt_pk_bf16_f32 v202, v202, v206\n\t"
"v_mfma_f32_16x16x32_bf16 v[88:91], a[20:23], a[168:171], v[88:91]\n\t"
"ds_write_addtid_b32 v208 offset:18592\n\t"
"ds_write2st64_b64 v246, v[120:121], v[124:125] offset0:66 offset1:79\n\t"
"v_mfma_f32_16x16x32_bf16 v[92:95], a[16:19], a[156:159], v[92:95]\n\t"
"v_cvt_pk_bf16_f32 v193, -v193, -v197\n\t"
"v_cvt_pk_bf16_f32 v201, v201, v205\n\t"
"v_mfma_f32_16x16x32_bf16 v[92:95], a[20:23], a[172:175], v[92:95]\n\t"
"buffer_load_dwordx4 a[84:87], off, s[84:87], s63 offset:3072 sc1\n\t"
"v_mfma_f32_16x16x32_bf16 v[76:79], a[8:11], a[156:159], v[76:79]\n\t"
"v_cvt_pk_bf16_f32 v216, v216, v220\n\t"
"v_cvt_pk_bf16_f32 v219, v219, v223\n\t"
"v_mfma_f32_16x16x32_bf16 v[76:79], a[12:15], a[172:175], v[76:79]\n\t"
"s_nop 0\n\t"
"ds_write_addtid_b32 v193 offset:20672\n\t"
"ds_write_addtid_b32 v210 offset:19616\n\t"
"ds_write_addtid_b32 v211 offset:23744\n\t"
"ds_write_addtid_b32 v216 offset:26912\n\t"
"ds_write_addtid_b32 v219 offset:32064\n\t"
"v_mfma_f32_16x16x32_bf16 v[60:63], a[0:3], a[156:159], v[60:63]\n\t"
"s_nop 0\n\t"
"ds_write_addtid_b32 v217 offset:31040\n\t"
"v_cvt_pk_bf16_f32 v194, -v194, -v198\n\t"
"v_mfma_f32_16x16x32_bf16 v[60:63], a[4:7], a[172:175], v[60:63]\n\t"
"s_waitcnt vmcnt(12) lgkmcnt(8)\n\t"
"ds_write_addtid_b32 v201 offset:28992\n\t"
"v_cvt_pk_bf16_f32 v218, v218, v222\n\t"
"v_mfma_f32_16x16x32_bf16 v[48:51], a[24:27], a[144:147], v[48:51]\n\t"
"s_nop 0\n\t"
"ds_write_addtid_b32 v202 offset:25888\n\t"
"v_cvt_pk_bf16_f32 v209, -v209, -v213\n\t"
"v_mfma_f32_16x16x32_bf16 v[48:51], a[28:31], a[160:163], v[48:51]\n\t"
"s_nop 0\n\t"
"ds_write_addtid_b32 v194 offset:17568\n\t"
"ds_write_addtid_b32 v209 offset:22720\n\t"
"ds_write_addtid_b32 v218 offset:27936\n\t"
"v_mfma_f32_16x16x32_bf16 v[64:67], a[32:35], a[144:147], v[64:67]\n\t"
"s_nop 0\n\t"
"ds_write_addtid_b32 v195 offset:21696\n\t"
"ds_write_addtid_b32 v200 offset:24864\n\t"
"v_mfma_f32_16x16x32_bf16 v[64:67], a[36:39], a[160:163], v[64:67]\n\t"
"buffer_load_dwordx4 a[80:83], off, s[84:87], s63 offset:2048 sc1\n\t"
"v_mfma_f32_16x16x32_bf16 v[80:83], a[40:43], a[144:147], v[80:83]\n\t"
"s_nop 0\n\t"
"ds_read_b128 a[156:159], v224 offset:1792\n\t"
"ds_read_b128 a[172:175], v224 offset:3840\n\t"
"v_mfma_f32_16x16x32_bf16 v[80:83], a[44:47], a[160:163], v[80:83]\n\t"
"buffer_load_dwordx4 a[72:75], off, s[84:87], s63 offset:0 sc1\n\t"
"v_mfma_f32_16x16x32_bf16 v[84:87], a[40:43], a[148:151], v[84:87]\n\t"
"s_nop 0\n\t"
"ds_read_b128 a[152:155], v224 offset:1536\n\t"
"ds_read_b128 a[168:171], v224 offset:3584\n\t"
"v_mfma_f32_16x16x32_bf16 v[84:87], a[44:47], a[164:167], v[84:87]\n\t"
"s_nop 0\n\t"
"ds_read_b64_tr_b16 a[192:193], v250 offset:0\n\t"
"ds_read_b64_tr_b16 a[200:201], v251 offset:0\n\t"
"v_mfma_f32_16x16x32_bf16 v[68:71], a[32:35], a[148:151], v[68:71]\n\t"
"s_nop 0\n\t"
"ds_read_b64_tr_b16 a[196:197], v251 offset:256\n\t"
"ds_read_b64_tr_b16 a[204:205], v250 offset:256\n\t"
"v_mfma_f32_16x16x32_bf16 v[68:71], a[36:39], a[164:167], v[68:71]\n\t"
"Lpad11_%=:\n\t"
"s_cbranch_execz Lret1_%=\n\t"
"v_mfma_f32_16x16x32_bf16 v[52:55], a[24:27], a[148:151], v[52:55]\n\t"
"buffer_load_dwordx4 a[76:79], off, s[84:87], s63 offset:1024 sc1\n\t"
"v_mfma_f32_16x16x32_bf16 v[52:55], a[28:31], a[164:167], v[52:55]\n\t"
"s_waitcnt lgkmcnt(0)\n\t"
"s_barrier\n\t"
"v_mfma_f32_16x16x32_bf16 v[56:59], a[24:27], a[152:155], v[56:59]\n\t"
"ds_read_b128 a[144:147], v224 offset:16544\n\t"
"v_mfma_f32_16x16x32_bf16 v[56:59], a[28:31], a[168:171], v[56:59]\n\t"
"ds_read_b128 a[160:163], v224 offset:18592\n\t"
"v_mfma_f32_16x16x32_bf16 v[72:75], a[32:35], a[152:155], v[72:75]\n\t"
"ds_read_b128 a[148:151], v224 offset:16800\n\t"
"v_mfma_f32_16x16x32_bf16 v[72:75], a[36:39], a[168:171], v[72:75]\n\t"
"ds_read_b128 a[164:167], v224 offset:18848\n\t"
"v_mfma_f32_16x16x32_bf16 v[88:91], a[40:43], a[152:155], v[88:91]\n\t"
"v_mfma_f32_16x16x32_bf16 v[88:91], a[44:47], a[168:171], v[88:91]\n\t"
"buffer_load_dwordx4 a[112:115], off, s[28:31], s42 offset:0 sc1\n\t"
"v_mfma_f32_16x16x32_bf16 v[92:95], a[40:43], a[156:159], v[92:95]\n\t"
"ds_read_b128 a[168:171], v224 offset:19104\n\t"
"v_mfma_f32_16x16x32_bf16 v[92:95], a[44:47], a[172:175], v[92:95]\n\t"
"ds_read_b128 v[104:107], v229 offset:20480\n\t"
"v_mfma_f32_16x16x32_bf16 v[76:79], a[32:35], a[156:159], v[76:79]\n\t"
"v_mfma_f32_16x16x32_bf16 v[76:79], a[36:39], a[172:175], v[76:79]\n\t"
"ds_read_b128 a[152:155], v224 offset:17056\n\t"
"v_mfma_f32_16x16x32_bf16 v[60:63], a[24:27], a[156:159], v[60:63]\n\t"
"ds_read_b128 a[156:159], v224 offset:17312\n\t"
"v_mfma_f32_16x16x32_bf16 v[60:63], a[28:31], a[172:175], v[60:63]\n\t"
"buffer_load_dwordx4 a[116:119], off, s[28:31], s42 offset:1024 sc1\n\t"
"v_mfma_f32_4x4x4_16b_bf16 v[200:203], a[220:221], a[178:179], 0\n\t"
"ds_read_b64_tr_b16 a[178:179], v248 offset:41600\n\t"
"v_mfma_f32_4x4x4_16b_bf16 v[204:207], a[220:221], a[186:187], 0\n\t"
"ds_read_b64_tr_b16 a[186:187], v249 offset:41600\n\t"
"v_mfma_f32_4x4x4_16b_bf16 v[216:219], a[220:221], a[194:195], 0\n\t"
"ds_read_b128 a[172:175], v224 offset:19360\n\t"
"v_mfma_f32_4x4x4_16b_bf16 v[220:223], a[220:221], a[202:203], 0\n\t"
"s_waitcnt lgkmcnt(5)\n\t"
".p2align 3\n\t"
"buffer_store_dwordx4 v[104:107], v232, s[16:19], s20 offen nt\n\t"
"v_pk_add_f32 v[144:145], v[48:49], v[144:145] neg_lo:[0,1] neg_hi:[0,1]\n\t"
"v_pk_add_f32 v[146:147], v[50:51], v[146:147] neg_lo:[0,1] neg_hi:[0,1]\n\t"
"v_pk_fma_f32 v[48:49], v[144:145], s[90:91], v[48:49] neg_lo:[1,0,0] neg_hi:[1,0,0]\n\t"
"v_pk_fma_f32 v[50:51], v[146:147], s[90:91], v[50:51] neg_lo:[1,0,0] neg_hi:[1,0,0]\n\t"
"v_pk_add_f32 v[160:161], v[64:65], v[160:161] neg_lo:[0,1] neg_hi:[0,1]\n\t"
"v_pk_add_f32 v[162:163], v[66:67], v[162:163] neg_lo:[0,1] neg_hi:[0,1]\n\t"
"v_pk_fma_f32 v[64:65], v[160:161], s[90:91], v[64:65] neg_lo:[1,0,0] neg_hi:[1,0,0]\n\t"
"v_pk_fma_f32 v[66:67], v[162:163], s[90:91], v[66:67] neg_lo:[1,0,0] neg_hi:[1,0,0]\n\t"
"v_pk_add_f32 v[176:177], v[80:81], v[176:177] neg_lo:[0,1] neg_hi:[0,1]\n\t"
"v_pk_add_f32 v[178:179], v[82:83], v[178:179] neg_lo:[0,1] neg_hi:[0,1]\n\t"
"v_pk_fma_f32 v[80:81], v[176:177], s[90:91], v[80:81] neg_lo:[1,0,0] neg_hi:[1,0,0]\n\t"
"v_pk_fma_f32 v[82:83], v[178:179], s[90:91], v[82:83] neg_lo:[1,0,0] neg_hi:[1,0,0]\n\t"
"v_pk_add_f32 v[180:181], v[84:85], v[180:181] neg_lo:[0,1] neg_hi:[0,1]\n\t"
"v_pk_add_f32 v[182:183], v[86:87], v[182:183] neg_lo:[0,1] neg_hi:[0,1]\n\t"
"v_pk_fma_f32 v[84:85], v[180:181], s[90:91], v[84:85] neg_lo:[1,0,0] neg_hi:[1,0,0]\n\t"
"v_pk_fma_f32 v[86:87], v[182:183], s[90:91], v[86:87] neg_lo:[1,0,0] neg_hi:[1,0,0]\n\t"
"v_pk_add_f32 v[164:165], v[68:69], v[164:165] neg_lo:[0,1] neg_hi:[0,1]\n\t"
"v_pk_add_f32 v[166:167], v[70:71], v[166:167] neg_lo:[0,1] neg_hi:[0,1]\n\t"
"v_pk_fma_f32 v[68:69], v[164:165], s[90:91], v[68:69] neg_lo:[1,0,0] neg_hi:[1,0,0]\n\t"
"v_pk_fma_f32 v[70:71], v[166:167], s[90:91], v[70:71] neg_lo:[1,0,0] neg_hi:[1,0,0]\n\t"
"v_pk_add_f32 v[148:149], v[52:53], v[148:149] neg_lo:[0,1] neg_hi:[0,1]\n\t"
"v_pk_add_f32 v[150:151], v[54:55], v[150:151] neg_lo:[0,1] neg_hi:[0,1]\n\t"
"v_pk_fma_f32 v[52:53], v[148:149], s[90:91], v[52:53] neg_lo:[1,0,0] neg_hi:[1,0,0]\n\t"
"v_pk_fma_f32 v[54:55], v[150:151], s[90:91], v[54:55] neg_lo:[1,0,0] neg_hi:[1,0,0]\n\t"
"v_pk_add_f32 v[152:153], v[56:57], v[152:153] neg_lo:[0,1] neg_hi:[0,1]\n\t"
"v_pk_add_f32 v[154:155], v[58:59], v[154:155] neg_lo:[0,1] neg_hi:[0,1]\n\t"
"v_pk_fma_f32 v[56:57], v[152:153], s[90:91], v[56:57] neg_lo:[1,0,0] neg_hi:[1,0,0]\n\t"
"v_pk_fma_f32 v[58:59], v[154:155], s[90:91], v[58:59] neg_lo:[1,0,0] neg_hi:[1,0,0]\n\t"
"v_pk_add_f32 v[168:169], v[72:73], v[168:169] neg_lo:[0,1] neg_hi:[0,1]\n\t"
"v_pk_add_f32 v[170:171], v[74:75], v[170:171] neg_lo:[0,1] neg_hi:[0,1]\n\t"
"v_pk_fma_f32 v[72:73], v[168:169], s[90:91], v[72:73] neg_lo:[1,0,0] neg_hi:[1,0,0]\n\t"
"v_pk_fma_f32 v[74:75], v[170:171], s[90:91], v[74:75] neg_lo:[1,0,0] neg_hi:[1,0,0]\n\t"
"v_pk_add_f32 v[184:185], v[88:89], v[184:185] neg_lo:[0,1] neg_hi:[0,1]\n\t"
"v_pk_add_f32 v[186:187], v[90:91], v[186:187] neg_lo:[0,1] neg_hi:[0,1]\n\t"
"v_pk_fma_f32 v[88:89], v[184:185], s[90:91], v[88:89] neg_lo:[1,0,0] neg_hi:[1,0,0]\n\t"
"v_pk_fma_f32 v[90:91], v[186:187], s[90:91], v[90:91] neg_lo:[1,0,0] neg_hi:[1,0,0]\n\t"
"v_pk_add_f32 v[188:189], v[92:93], v[188:189] neg_lo:[0,1] neg_hi:[0,1]\n\t"
"v_pk_add_f32 v[190:191], v[94:95], v[190:191] neg_lo:[0,1] neg_hi:[0,1]\n\t"
"v_pk_fma_f32 v[92:93], v[188:189], s[90:91], v[92:93] neg_lo:[1,0,0] neg_hi:[1,0,0]\n\t"
"v_pk_fma_f32 v[94:95], v[190:191], s[90:91], v[94:95] neg_lo:[1,0,0] neg_hi:[1,0,0]\n\t"
"v_pk_add_f32 v[172:173], v[76:77], v[172:173] neg_lo:[0,1] neg_hi:[0,1]\n\t"
"v_pk_add_f32 v[174:175], v[78:79], v[174:175] neg_lo:[0,1] neg_hi:[0,1]\n\t"
"v_pk_fma_f32 v[76:77], v[172:173], s[90:91], v[76:77] neg_lo:[1,0,0] neg_hi:[1,0,0]\n\t"
"v_pk_fma_f32 v[78:79], v[174:175], s[90:91], v[78:79] neg_lo:[1,0,0] neg_hi:[1,0,0]\n\t"
"v_pk_add_f32 v[156:157], v[60:61], v[156:157] neg_lo:[0,1] neg_hi:[0,1]\n\t"
"v_pk_add_f32 v[158:159], v[62:63], v[158:159] neg_lo:[0,1] neg_hi:[0,1]\n\t"
"v_pk_fma_f32 v[60:61], v[156:157], s[90:91], v[60:61] neg_lo:[1,0,0] neg_hi:[1,0,0]\n\t"
"v_pk_fma_f32 v[62:63], v[158:159], s[90:91], v[62:63] neg_lo:[1,0,0] neg_hi:[1,0,0]\n\t"
"v_mfma_f32_4x4x4_16b_bf16 v[192:195], a[222:223], a[176:177], v[200:203]\n\t"
"ds_read_b128 v[120:123], v230 offset:20480\n\t"
"v_mfma_f32_4x4x4_16b_bf16 v[196:199], a[222:223], a[184:185], v[204:207]\n\t"
"ds_read_b64_tr_b16 a[194:195], v250 offset:41600\n\t"
"v_mfma_f32_4x4x4_16b_bf16 v[200:203], a[222:223], a[180:181], v[200:203]\n\t"
"buffer_load_dwordx4 a[108:111], off, s[84:87], s42 offset:3072 sc1\n\t"
"v_mfma_f32_4x4x4_16b_bf16 v[204:207], a[222:223], a[188:189], v[204:207]\n\t"
"ds_read_b64_tr_b16 a[202:203], v251 offset:41600\n\t"
"v_mfma_f32_4x4x4_16b_bf16 v[208:211], a[222:223], a[192:193], v[216:219]\n\t"
"ds_read_b64_tr_b16 a[176:177], v248 offset:41472\n\t"
"v_mfma_f32_4x4x4_16b_bf16 v[212:215], a[222:223], a[200:201], v[220:223]\n\t"
"ds_read_b64_tr_b16 a[184:185], v249 offset:41472\n\t"
"v_mfma_f32_4x4x4_16b_bf16 v[216:219], a[222:223], a[196:197], v[216:219]\n\t"
"ds_read_b64_tr_b16 a[180:181], v249 offset:41728\n\t"
"v_mfma_f32_4x4x4_16b_bf16 v[220:223], a[222:223], a[204:205], v[220:223]\n\t"
"s_waitcnt vmcnt(11) lgkmcnt(7)\n\t"
"buffer_load_dwordx4 a[104:107], off, s[84:87], s42 offset:2048 sc1\n\t"
"v_mfma_f32_16x16x32_bf16 v[48:51], a[48:51], a[144:147], v[48:51]\n\t"
"ds_read_b64_tr_b16 a[188:189], v248 offset:41728\n\t"
"v_cvt_pk_bf16_f32 v195, -v195, -v199\n\t"
"v_mfma_f32_16x16x32_bf16 v[48:51], a[52:55], a[160:163], v[48:51]\n\t"
"buffer_load_dwordx4 a[96:99], off, s[84:87], s42 offset:0 sc1\n\t"
"v_mfma_f32_16x16x32_bf16 v[64:67], a[56:59], a[144:147], v[64:67]\n\t"
"ds_write_addtid_b32 v195 offset:5152\n\t"
"v_cvt_pk_bf16_f32 v194, -v194, -v198\n\t"
"v_mfma_f32_16x16x32_bf16 v[64:67], a[60:63], a[160:163], v[64:67]\n\t"
"ds_write_addtid_b32 v194 offset:1024\n\t"
"v_cvt_pk_bf16_f32 v203, v203, v207\n\t"
"v_mfma_f32_16x16x32_bf16 v[80:83], a[64:67], a[144:147], v[80:83]\n\t"
"ds_read_b128 a[232:235], v230 offset:33792\n\t"
"v_cvt_pk_bf16_f32 v211, -v211, -v215\n\t"
"v_mfma_f32_16x16x32_bf16 v[80:83], a[68:71], a[160:163], v[80:83]\n\t"
"s_waitcnt lgkmcnt(7)\n\t"
"buffer_store_dwordx4 v[120:123], v233, s[16:19], s20 offen nt\n\t"
"v_mfma_f32_16x16x32_bf16 v[84:87], a[64:67], a[148:151], v[84:87]\n\t"
"ds_read_b128 a[144:147], v224 offset:17568\n\t"
"v_cvt_pk_bf16_f32 v201, v201, v205\n\t"
"v_mfma_f32_16x16x32_bf16 v[84:87], a[68:71], a[164:167], v[84:87]\n\t"
"ds_read_b128 v[136:139], v231 offset:20480\n\t"
"v_cvt_pk_bf16_f32 v202, v202, v206\n\t"
"v_mfma_f32_16x16x32_bf16 v[68:71], a[56:59], a[148:151], v[68:71]\n\t"
"ds_read_b128 a[160:163], v224 offset:19616\n\t"
"v_cvt_pk_bf16_f32 v193, -v193, -v197\n\t"
"v_mfma_f32_16x16x32_bf16 v[68:71], a[60:63], a[164:167], v[68:71]\n\t"
"buffer_load_dwordx4 a[100:103], off, s[84:87], s42 offset:1024 sc1\n\t"
"v_mfma_f32_16x16x32_bf16 v[52:55], a[48:51], a[148:151], v[52:55]\n\t"
"ds_read_b128 a[148:151], v224 offset:17824\n\t"
"v_cvt_pk_bf16_f32 v209, -v209, -v213\n\t"
"v_mfma_f32_16x16x32_bf16 v[52:55], a[52:55], a[164:167], v[52:55]\n\t"
"ds_write_addtid_b32 v211 offset:7200\n\t"
"v_cvt_pk_bf16_f32 v200, v200, v204\n\t"
"v_mfma_f32_16x16x32_bf16 v[56:59], a[48:51], a[152:155], v[56:59]\n\t"
"ds_read_b128 a[236:239], v231 offset:33792\n\t"
"v_cvt_pk_bf16_f32 v216, v216, v220\n\t"
"v_mfma_f32_16x16x32_bf16 v[56:59], a[52:55], a[168:171], v[56:59]\n\t"
"buffer_load_dwordx4 a[136:139], off, s[28:31], s48 offset:0 sc1\n\t"
"v_mfma_f32_16x16x32_bf16 v[72:75], a[56:59], a[152:155], v[72:75]\n\t"
"ds_read_b128 a[164:167], v224 offset:19872\n\t"
"v_cvt_pk_bf16_f32 v210, -v210, -v214\n\t"
"v_mfma_f32_16x16x32_bf16 v[72:75], a[60:63], a[168:171], v[72:75]\n\t"
"ds_write_addtid_b32 v193 offset:4128\n\t"
"v_cvt_pk_bf16_f32 v218, v218, v222\n\t"
"v_mfma_f32_16x16x32_bf16 v[88:91], a[64:67], a[152:155], v[88:91]\n\t"
"s_nop 0\n\t"
"ds_write_addtid_b32 v201 offset:12448\n\t"
"ds_write_addtid_b32 v202 offset:9344\n\t"
"v_mfma_f32_16x16x32_bf16 v[88:91], a[68:71], a[168:171], v[88:91]\n\t"
"s_waitcnt lgkmcnt(8)\n\t"
"buffer_store_dwordx4 v[136:139], v234, s[16:19], s20 offen nt\n\t"
"v_mfma_f32_16x16x32_bf16 v[92:95], a[64:67], a[156:159], v[92:95]\n\t"
"ds_read_b128 v[108:111], v229 offset:33792\n\t"
"v_cvt_pk_bf16_f32 v192, -v192, -v196\n\t"
"v_mfma_f32_16x16x32_bf16 v[92:95], a[68:71], a[172:175], v[92:95]\n\t"
"ds_write_addtid_b32 v210 offset:3072\n\t"
"v_cvt_pk_bf16_f32 v219, v219, v223\n\t"
"v_mfma_f32_16x16x32_bf16 v[76:79], a[56:59], a[156:159], v[76:79]\n\t"
"ds_write_addtid_b32 v219 offset:15520\n\t"
"v_cvt_pk_bf16_f32 v208, -v208, -v212\n\t"
"v_mfma_f32_16x16x32_bf16 v[76:79], a[60:63], a[172:175], v[76:79]\n\t"
"ds_write_addtid_b32 v200 offset:8320\n\t"
"buffer_load_dwordx4 a[140:143], off, s[28:31], s48 offset:1024 sc1\n\t"
"v_mfma_f32_16x16x32_bf16 v[60:63], a[48:51], a[156:159], v[60:63]\n\t"
"s_nop 0\n\t"
"ds_write_addtid_b32 v209 offset:6176\n\t"
"ds_write_addtid_b32 v218 offset:11392\n\t"
"v_mfma_f32_16x16x32_bf16 v[60:63], a[52:55], a[172:175], v[60:63]\n\t"
"s_waitcnt vmcnt(11) lgkmcnt(10)\n\t"
"ds_write_addtid_b32 v192 offset:0\n\t"
"ds_write_addtid_b32 v216 offset:10368\n\t"
"v_mfma_f32_16x16x32_bf16 v[144:147], a[72:75], a[144:147], v[144:147]\n\t"
"ds_write_addtid_b32 v203 offset:13472\n\t"
"v_cvt_pk_bf16_f32 v217, v217, v221\n\t"
"v_mfma_f32_16x16x32_bf16 v[144:147], a[76:79], a[160:163], v[144:147]\n\t"
"buffer_load_dwordx4 a[132:135], off, s[84:87], s48 offset:3072 sc1\n\t"
"v_mfma_f32_16x16x32_bf16 v[160:163], a[80:83], a[144:147], v[160:163]\n\t"
"s_nop 0\n\t"
"ds_write_addtid_b32 v208 offset:2048\n\t"
"ds_write_addtid_b32 v217 offset:14496\n\t"
"v_mfma_f32_16x16x32_bf16 v[160:163], a[84:87], a[160:163], v[160:163]\n\t"
"s_nop 0\n\t"
"ds_read_b128 a[152:155], v224 offset:18080\n\t"
"ds_read_b128 a[156:159], v224 offset:18336\n\t"
"v_mfma_f32_16x16x32_bf16 v[176:179], a[88:91], a[144:147], v[176:179]\n\t"
"s_nop 0\n\t"
"ds_read_b128 a[172:175], v224 offset:20384\n\t"
"ds_read_b64_tr_b16 a[192:193], v250 offset:41472\n\t"
"v_mfma_f32_16x16x32_bf16 v[176:179], a[92:95], a[160:163], v[176:179]\n\t"
"s_waitcnt lgkmcnt(13)\n\t"
"buffer_store_dwordx4 v[108:111], v232, s[16:19], s25 offen nt\n\t"
"v_mfma_f32_16x16x32_bf16 v[180:183], a[88:91], a[148:151], v[180:183]\n\t"
"s_nop 0\n\t"
"ds_read_b128 a[168:171], v224 offset:20128\n\t"
"ds_read_b64_tr_b16 a[200:201], v251 offset:41472\n\t"
"v_mfma_f32_16x16x32_bf16 v[180:183], a[92:95], a[164:167], v[180:183]\n\t"
"s_nop 0\n\t"
"ds_read_b64_tr_b16 a[196:197], v251 offset:41728\n\t"
"ds_read_b64_tr_b16 a[204:205], v250 offset:41728\n\t"
"v_mfma_f32_16x16x32_bf16 v[164:167], a[80:83], a[148:151], v[164:167]\n\t"
"buffer_load_dwordx4 a[128:131], off, s[84:87], s48 offset:2048 sc1\n\t"
"v_mfma_f32_16x16x32_bf16 v[164:167], a[84:87], a[164:167], v[164:167]\n\t"
"v_mfma_f32_16x16x32_bf16 v[148:151], a[72:75], a[148:151], v[148:151]\n\t"
"buffer_load_dwordx4 a[120:123], off, s[84:87], s48 offset:0 sc1\n\t"
"v_mfma_f32_16x16x32_bf16 v[148:151], a[76:79], a[164:167], v[148:151]\n\t"
"s_waitcnt lgkmcnt(0)\n\t"
"s_barrier\n\t"
"v_mfma_f32_16x16x32_bf16 v[152:155], a[72:75], a[152:155], v[152:155]\n\t"
"ds_read_b128 a[144:147], v224 offset:0\n\t"
"v_mfma_f32_16x16x32_bf16 v[152:155], a[76:79], a[168:171], v[152:155]\n\t"
"ds_read_b128 a[160:163], v224 offset:2048\n\t"
"v_mfma_f32_16x16x32_bf16 v[168:171], a[80:83], a[152:155], v[168:171]\n\t"
"ds_read_b128 a[148:151], v224 offset:256\n\t"
"v_add_u32 v248, 33280, v227\n\t"
"v_mfma_f32_16x16x32_bf16 v[168:171], a[84:87], a[168:171], v[168:171]\n\t"
"ds_read_b128 a[164:167], v224 offset:2304\n\t"
"v_xor_b32 v249, 32, v248\n\t"
"v_mfma_f32_16x16x32_bf16 v[184:187], a[88:91], a[152:155], v[184:187]\n\t"
"v_xor_b32 v250, 64, v248\n\t"
"v_xor_b32 v251, 96, v248\n\t"
"v_mfma_f32_16x16x32_bf16 v[184:187], a[92:95], a[168:171], v[184:187]\n\t"
"buffer_load_dwordx4 a[124:127], off, s[84:87], s48 offset:1024 sc1\n\t"
"v_mfma_f32_16x16x32_bf16 v[188:191], a[88:91], a[156:159], v[188:191]\n\t"
"ds_read_b128 a[168:171], v224 offset:2560\n\t"
"v_mfma_f32_16x16x32_bf16 v[188:191], a[92:95], a[172:175], v[188:191]\n\t"
"ds_read_b128 a[152:155], v224 offset:512\n\t"
"v_mfma_f32_16x16x32_bf16 v[172:175], a[80:83], a[156:159], v[172:175]\n\t"
"v_mfma_f32_16x16x32_bf16 v[172:175], a[84:87], a[172:175], v[172:175]\n\t"
"ds_read_b64_tr_b16 a[182:183], v249 offset:512\n\t"
"v_mfma_f32_16x16x32_bf16 v[156:159], a[72:75], a[156:159], v[156:159]\n\t"
"ds_read_b128 a[156:159], v224 offset:768\n\t"
"v_mfma_f32_16x16x32_bf16 v[156:159], a[76:79], a[172:175], v[156:159]\n\t"
"buffer_load_dwordx4 a[16:19], off, s[28:31], s75 offset:0 sc1\n\t"
"v_mfma_f32_4x4x4_16b_bf16 v[200:203], a[220:221], a[178:179], 0\n\t"
"ds_read_b64_tr_b16 a[190:191], v251 offset:512\n\t"
"v_mfma_f32_4x4x4_16b_bf16 v[204:207], a[220:221], a[186:187], 0\n\t"
"ds_read_b64_tr_b16 a[198:199], v249 offset:1664\n\t"
"v_mfma_f32_4x4x4_16b_bf16 v[216:219], a[220:221], a[194:195], 0\n\t"
"ds_read_b128 a[172:175], v224 offset:2816\n\t"
"v_mfma_f32_4x4x4_16b_bf16 v[220:223], a[220:221], a[202:203], 0\n\t"
"buffer_store_dwordx4 a[232:235], v233, s[16:19], s25 offen nt\n\t"
"v_mfma_f32_4x4x4_16b_bf16 v[192:195], a[222:223], a[176:177], v[200:203]\n\t"
"ds_read_b64_tr_b16 a[206:207], v251 offset:1664\n\t"
"v_mfma_f32_4x4x4_16b_bf16 v[196:199], a[222:223], a[184:185], v[204:207]\n\t"
"ds_read_b64_tr_b16 a[178:179], v248 offset:512\n\t"
"v_mfma_f32_4x4x4_16b_bf16 v[200:203], a[222:223], a[180:181], v[200:203]\n\t"
"ds_read_b64_tr_b16 a[186:187], v250 offset:512\n\t"
"v_mfma_f32_4x4x4_16b_bf16 v[204:207], a[222:223], a[188:189], v[204:207]\n\t"
"ds_read_b64_tr_b16 a[194:195], v248 offset:1664\n\t"
"v_mfma_f32_4x4x4_16b_bf16 v[208:211], a[222:223], a[192:193], v[216:219]\n\t"
"ds_read_b64_tr_b16 a[202:203], v250 offset:1664\n\t"
"v_mfma_f32_4x4x4_16b_bf16 v[212:215], a[222:223], a[200:201], v[220:223]\n\t"
"ds_read_b64_tr_b16 a[176:177], v248 offset:0\n\t"
"v_mfma_f32_4x4x4_16b_bf16 v[216:219], a[222:223], a[196:197], v[216:219]\n\t"
"ds_read_b64_tr_b16 a[184:185], v250 offset:0\n\t"
"v_mfma_f32_4x4x4_16b_bf16 v[220:223], a[222:223], a[204:205], v[220:223]\n\t"
"s_waitcnt vmcnt(10) lgkmcnt(8)\n\t"
"buffer_load_dwordx4 a[20:23], off, s[28:31], s75 offset:1024 sc1\n\t"
"v_mfma_f32_16x16x32_bf16 v[48:51], a[96:99], a[144:147], v[48:51]\n\t"
"ds_read_b64_tr_b16 a[180:181], v249 offset:0\n\t"
"v_cvt_pk_bf16_f32 v193, -v193, -v197\n\t"
"v_mfma_f32_16x16x32_bf16 v[48:51], a[100:103], a[160:163], v[48:51]\n\t"
"ds_read_b64_tr_b16 a[188:189], v251 offset:0\n\t"
"v_cvt_pk_bf16_f32 v194, -v194, -v198\n\t"
"v_mfma_f32_16x16x32_bf16 v[64:67], a[104:107], a[144:147], v[64:67]\n\t"
"ds_read_b64_tr_b16 a[192:193], v248 offset:1152\n\t"
"v_cvt_pk_bf16_f32 v201, v201, v205\n\t"
"v_mfma_f32_16x16x32_bf16 v[64:67], a[108:111], a[160:163], v[64:67]\n\t"
"buffer_store_dwordx4 a[236:239], v234, s[16:19], s25 offen nt\n\t"
"v_mfma_f32_16x16x32_bf16 v[80:83], a[112:115], a[144:147], v[80:83]\n\t"
"ds_read_b128 a[144:147], v224 offset:1024\n\t"
"v_cvt_pk_bf16_f32 v211, -v211, -v215\n\t"
"v_mfma_f32_16x16x32_bf16 v[80:83], a[116:119], a[160:163], v[80:83]\n\t"
"ds_read_b64_tr_b16 a[200:201], v250 offset:1152\n\t"
"v_cvt_pk_bf16_f32 v202, v202, v206\n\t"
"v_mfma_f32_16x16x32_bf16 v[84:87], a[112:115], a[148:151], v[84:87]\n\t"
"ds_read_b128 a[160:163], v224 offset:3072\n\t"
"v_cvt_pk_bf16_f32 v208, -v208, -v212\n\t"
"v_mfma_f32_16x16x32_bf16 v[84:87], a[116:119], a[164:167], v[84:87]\n\t"
"buffer_load_dwordx4 a[12:15], off, s[84:87], s75 offset:3072 sc1\n\t"
"v_mfma_f32_16x16x32_bf16 v[68:71], a[104:107], a[148:151], v[68:71]\n\t"
"ds_write_addtid_b32 v201 offset:28992\n\t"
"v_cvt_pk_bf16_f32 v192, -v192, -v196\n\t"
"v_mfma_f32_16x16x32_bf16 v[68:71], a[108:111], a[164:167], v[68:71]\n\t"
"ds_write_addtid_b32 v211 offset:23744\n\t"
"v_cvt_pk_bf16_f32 v203, v203, v207\n\t"
"v_mfma_f32_16x16x32_bf16 v[52:55], a[96:99], a[148:151], v[52:55]\n\t"
"ds_read_b128 a[148:151], v224 offset:1280\n\t"
"v_mfma_f32_16x16x32_bf16 v[52:55], a[100:103], a[164:167], v[52:55]\n\t"
"ds_write_addtid_b32 v193 offset:20672\n\t"
"v_cvt_pk_bf16_f32 v218, v218, v222\n\t"
"v_mfma_f32_16x16x32_bf16 v[56:59], a[96:99], a[152:155], v[56:59]\n\t"
"ds_read_b128 a[164:167], v224 offset:3328\n\t"
"v_cvt_pk_bf16_f32 v217, v217, v221\n\t"
"v_mfma_f32_16x16x32_bf16 v[56:59], a[100:103], a[168:171], v[56:59]\n\t"
"buffer_load_dwordx4 a[8:11], off, s[84:87], s75 offset:2048 sc1\n\t"
"v_mfma_f32_16x16x32_bf16 v[72:75], a[104:107], a[152:155], v[72:75]\n\t"
"ds_write_addtid_b32 v218 offset:27936\n\t"
"v_cvt_pk_bf16_f32 v195, -v195, -v199\n\t"
"v_mfma_f32_16x16x32_bf16 v[72:75], a[108:111], a[168:171], v[72:75]\n\t"
"ds_write_addtid_b32 v217 offset:31040\n\t"
"v_mfma_f32_16x16x32_bf16 v[88:91], a[112:115], a[152:155], v[88:91]\n\t"
"ds_read_b128 a[152:155], v224 offset:1536\n\t"
"v_cvt_pk_bf16_f32 v219, v219, v223\n\t"
"v_mfma_f32_16x16x32_bf16 v[88:91], a[116:119], a[168:171], v[88:91]\n\t"
"s_waitcnt lgkmcnt(8)\n\t"
"buffer_load_dwordx4 a[0:3], off, s[84:87], s75 offset:0 sc1\n\t"
"v_mfma_f32_16x16x32_bf16 v[92:95], a[112:115], a[156:159], v[92:95]\n\t"
"ds_write_addtid_b32 v194 offset:17568\n\t"
"v_cvt_pk_bf16_f32 v210, -v210, -v214\n\t"
"v_mfma_f32_16x16x32_bf16 v[92:95], a[116:119], a[172:175], v[92:95]\n\t"
"s_nop 0\n\t"
"ds_write_addtid_b32 v192 offset:16544\n\t"
"ds_write_addtid_b32 v202 offset:25888\n\t"
"v_mfma_f32_16x16x32_bf16 v[76:79], a[104:107], a[156:159], v[76:79]\n\t"
"ds_write_addtid_b32 v210 offset:19616\n\t"
"v_mfma_f32_16x16x32_bf16 v[76:79], a[108:111], a[172:175], v[76:79]\n\t"
"ds_write_addtid_b32 v208 offset:18592\n\t"
"v_cvt_pk_bf16_f32 v209, -v209, -v213\n\t"
"v_mfma_f32_16x16x32_bf16 v[60:63], a[96:99], a[156:159], v[60:63]\n\t"
"ds_write_addtid_b32 v219 offset:32064\n\t"
"v_cvt_pk_bf16_f32 v216, v216, v220\n\t"
"v_mfma_f32_16x16x32_bf16 v[60:63], a[100:103], a[172:175], v[60:63]\n\t"
"s_waitcnt vmcnt(7) lgkmcnt(9)\n\t"
"ds_write_addtid_b32 v203 offset:30016\n\t"
"ds_write_addtid_b32 v216 offset:26912\n\t"
"v_mfma_f32_16x16x32_bf16 v[144:147], a[120:123], a[144:147], v[144:147]\n\t"
"ds_write_addtid_b32 v195 offset:21696\n\t"
"v_cvt_pk_bf16_f32 v200, v200, v204\n\t"
"v_mfma_f32_16x16x32_bf16 v[144:147], a[124:127], a[160:163], v[144:147]\n\t"
"ds_write_addtid_b32 v200 offset:24864\n\t"
"v_mfma_f32_16x16x32_bf16 v[160:163], a[128:131], a[144:147], v[160:163]\n\t"
"ds_write_addtid_b32 v209 offset:22720\n\t"
"v_mfma_f32_16x16x32_bf16 v[160:163], a[132:135], a[160:163], v[160:163]\n\t"
"ds_read_b128 a[172:175], v224 offset:3840\n\t"
"v_mfma_f32_16x16x32_bf16 v[176:179], a[136:139], a[144:147], v[176:179]\n\t"
"ds_read_b128 a[168:171], v224 offset:3584\n\t"
"v_mfma_f32_16x16x32_bf16 v[176:179], a[140:143], a[160:163], v[176:179]\n\t"
"buffer_load_dwordx4 a[4:7], off, s[84:87], s75 offset:1024 sc1\n\t"
"v_mfma_f32_16x16x32_bf16 v[180:183], a[136:139], a[148:151], v[180:183]\n\t"
"ds_read_b128 a[156:159], v224 offset:1792\n\t"
"v_mfma_f32_16x16x32_bf16 v[180:183], a[140:143], a[164:167], v[180:183]\n\t"
"ds_read_b64_tr_b16 a[196:197], v249 offset:1152\n\t"
"v_mfma_f32_16x16x32_bf16 v[164:167], a[128:131], a[148:151], v[164:167]\n\t"
"ds_read_b64_tr_b16 a[204:205], v251 offset:1152\n\t"
"v_mfma_f32_16x16x32_bf16 v[164:167], a[132:135], a[164:167], v[164:167]\n\t"
"buffer_load_dwordx4 a[40:43], off, s[28:31], s78 offset:0 sc1\n\t"
"v_mfma_f32_16x16x32_bf16 v[148:151], a[120:123], a[148:151], v[148:151]\n\t"
"v_mfma_f32_16x16x32_bf16 v[148:151], a[124:127], a[164:167], v[148:151]\n\t"
"s_waitcnt lgkmcnt(0)\n\t"
"s_barrier\n\t"
"v_mfma_f32_16x16x32_bf16 v[152:155], a[120:123], a[152:155], v[152:155]\n\t"
"ds_read_b128 a[144:147], v224 offset:16544\n\t"
"v_mfma_f32_16x16x32_bf16 v[152:155], a[124:127], a[168:171], v[152:155]\n\t"
"ds_read_b128 a[148:151], v224 offset:16800\n\t"
"v_mfma_f32_16x16x32_bf16 v[168:171], a[128:131], a[152:155], v[168:171]\n\t"
"ds_read_b128 a[160:163], v224 offset:18592\n\t"
"v_mfma_f32_16x16x32_bf16 v[168:171], a[132:135], a[168:171], v[168:171]\n\t"
"ds_read_b128 a[164:167], v224 offset:18848\n\t"
"v_mfma_f32_16x16x32_bf16 v[184:187], a[136:139], a[152:155], v[184:187]\n\t"
"v_mfma_f32_16x16x32_bf16 v[184:187], a[140:143], a[168:171], v[184:187]\n\t"
"buffer_load_dwordx4 a[44:47], off, s[28:31], s78 offset:1024 sc1\n\t"
"v_mfma_f32_16x16x32_bf16 v[188:191], a[136:139], a[156:159], v[188:191]\n\t"
"ds_read_b128 a[168:171], v224 offset:19104\n\t"
"v_mfma_f32_16x16x32_bf16 v[188:191], a[140:143], a[172:175], v[188:191]\n\t"
"buffer_load_dwordx4 a[36:39], off, s[84:87], s78 offset:3072 sc1\n\t"
"v_mfma_f32_16x16x32_bf16 v[172:175], a[128:131], a[156:159], v[172:175]\n\t"
"ds_read_b128 a[152:155], v224 offset:17056\n\t"
"v_mfma_f32_16x16x32_bf16 v[172:175], a[132:135], a[172:175], v[172:175]\n\t"
"buffer_load_dwordx4 a[32:35], off, s[84:87], s78 offset:2048 sc1\n\t"
"v_mfma_f32_16x16x32_bf16 v[156:159], a[120:123], a[156:159], v[156:159]\n\t"
"ds_read_b128 a[156:159], v224 offset:17312\n\t"
"v_mfma_f32_16x16x32_bf16 v[156:159], a[124:127], a[172:175], v[156:159]\n\t"
"ds_read_b128 a[172:175], v224 offset:19360\n\t"
"v_mfma_f32_16x16x32_bf16 v[192:195], a[224:227], a[176:179], 0\n\t"
"ds_read_b64_tr_b16 a[176:177], v248 offset:42624\n\t"
"v_mfma_f32_16x16x32_bf16 v[196:199], a[224:227], a[180:183], 0\n\t"
"buffer_load_dwordx4 a[24:27], off, s[84:87], s78 offset:0 sc1\n\t"
"v_mfma_f32_16x16x32_bf16 v[200:203], a[224:227], a[184:187], 0\n\t"
"ds_read_b64_tr_b16 a[178:179], v248 offset:43136\n\t"
"v_mfma_f32_16x16x32_bf16 v[204:207], a[224:227], a[188:191], 0\n\t"
"ds_read_b64_tr_b16 a[180:181], v249 offset:42624\n\t"
"v_mfma_f32_16x16x32_bf16 v[208:211], a[224:227], a[192:195], 0\n\t"
"ds_read_b64_tr_b16 a[182:183], v249 offset:43136\n\t"
"v_mfma_f32_16x16x32_bf16 v[212:215], a[224:227], a[196:199], 0\n\t"
"buffer_load_dwordx4 a[28:31], off, s[84:87], s78 offset:1024 sc1\n\t"
"v_mfma_f32_16x16x32_bf16 v[216:219], a[224:227], a[200:203], 0\n\t"
"ds_read_b64_tr_b16 a[184:185], v250 offset:42624\n\t"
"v_mfma_f32_16x16x32_bf16 v[220:223], a[224:227], a[204:207], 0\n\t"
"s_waitcnt vmcnt(6) lgkmcnt(7)\n\t"
"ds_read_b64_tr_b16 a[186:187], v250 offset:43136\n\t"
"v_cvt_pk_bf16_f32 v192, v192, v196\n\t"
"v_mfma_f32_16x16x32_bf16 v[48:51], a[0:3], a[144:147], v[48:51]\n\t"
"ds_read_b64_tr_b16 a[188:189], v251 offset:42624\n\t"
"v_cvt_pk_bf16_f32 v194, v194, v198\n\t"
"v_mfma_f32_16x16x32_bf16 v[48:51], a[4:7], a[160:163], v[48:51]\n\t"
"ds_read_b64_tr_b16 a[190:191], v251 offset:43136\n\t"
"v_cvt_pk_bf16_f32 v193, v193, v197\n\t"
"v_mfma_f32_16x16x32_bf16 v[64:67], a[8:11], a[144:147], v[64:67]\n\t"
"ds_read_b64_tr_b16 a[192:193], v248 offset:21888\n\t"
"v_mfma_f32_16x16x32_bf16 v[64:67], a[12:15], a[160:163], v[64:67]\n\t"
"ds_read_b64_tr_b16 a[194:195], v248 offset:22400\n\t"
"v_cvt_pk_bf16_f32 v211, v211, v215\n\t"
"v_mfma_f32_16x16x32_bf16 v[80:83], a[16:19], a[144:147], v[80:83]\n\t"
"ds_read_b128 a[144:147], v224 offset:17568\n\t"
"v_cvt_pk_bf16_f32 v209, v209, v213\n\t"
"v_mfma_f32_16x16x32_bf16 v[80:83], a[20:23], a[160:163], v[80:83]\n\t"
"ds_read_b64_tr_b16 a[196:197], v249 offset:21888\n\t"
"v_cvt_pk_bf16_f32 v219, v219, v223\n\t"
"v_mfma_f32_16x16x32_bf16 v[84:87], a[16:19], a[148:151], v[84:87]\n\t"
"ds_write_addtid_b32 v209 offset:9344\n\t"
"v_mfma_f32_16x16x32_bf16 v[84:87], a[20:23], a[164:167], v[84:87]\n\t"
"ds_read_b64_tr_b16 a[198:199], v249 offset:22400\n\t"
"v_cvt_pk_bf16_f32 v200, v200, v204\n\t"
"v_mfma_f32_16x16x32_bf16 v[68:71], a[8:11], a[148:151], v[68:71]\n\t"
"ds_read_b128 a[160:163], v224 offset:19616\n\t"
"v_cvt_pk_bf16_f32 v208, v208, v212\n\t"
"v_mfma_f32_16x16x32_bf16 v[68:71], a[12:15], a[164:167], v[68:71]\n\t"
"ds_write_addtid_b32 v219 offset:15520\n\t"
"v_cvt_pk_bf16_f32 v217, v217, v221\n\t"
"v_mfma_f32_16x16x32_bf16 v[52:55], a[0:3], a[148:151], v[52:55]\n\t"
"ds_write_addtid_b32 v200 offset:2048\n\t"
"v_cvt_pk_bf16_f32 v218, v218, v222\n\t"
"v_mfma_f32_16x16x32_bf16 v[52:55], a[4:7], a[164:167], v[52:55]\n\t"
"ds_write_addtid_b32 v211 offset:13472\n\t"
"v_mfma_f32_16x16x32_bf16 v[56:59], a[0:3], a[152:155], v[56:59]\n\t"
"ds_read_b128 a[148:151], v224 offset:17824\n\t"
"v_cvt_pk_bf16_f32 v216, v216, v220\n\t"
"v_mfma_f32_16x16x32_bf16 v[56:59], a[4:7], a[168:171], v[56:59]\n\t"
"ds_read_b128 a[164:167], v224 offset:19872\n\t"
"v_cvt_pk_bf16_f32 v195, v195, v199\n\t"
"v_mfma_f32_16x16x32_bf16 v[72:75], a[8:11], a[152:155], v[72:75]\n\t"
"ds_write_addtid_b32 v195 offset:12448\n\t"
"v_cvt_pk_bf16_f32 v210, v210, v214\n\t"
"v_mfma_f32_16x16x32_bf16 v[72:75], a[12:15], a[168:171], v[72:75]\n\t"
"buffer_load_dwordx4 a[64:67], off, s[28:31], 0 offset:0 sc1\n\t"
"v_mfma_f32_16x16x32_bf16 v[88:91], a[16:19], a[152:155], v[88:91]\n\t"
"ds_read_b128 a[152:155], v224 offset:18080\n\t"
"v_cvt_pk_bf16_f32 v201, v201, v205\n\t"
"v_mfma_f32_16x16x32_bf16 v[88:91], a[20:23], a[168:171], v[88:91]\n\t"
"s_waitcnt lgkmcnt(8)\n\t"
"ds_read_b128 a[168:171], v224 offset:20128\n\t"
"v_cvt_pk_bf16_f32 v202, v202, v206\n\t"
"v_mfma_f32_16x16x32_bf16 v[92:95], a[16:19], a[156:159], v[92:95]\n\t"
"ds_write_addtid_b32 v216 offset:3072\n\t"
"v_cvt_pk_bf16_f32 v203, v203, v207\n\t"
"v_mfma_f32_16x16x32_bf16 v[92:95], a[20:23], a[172:175], v[92:95]\n\t"
"ds_write_addtid_b32 v194 offset:4128\n\t"
"v_mfma_f32_16x16x32_bf16 v[76:79], a[8:11], a[156:159], v[76:79]\n\t"
"s_xor_b32 s24, s24, 384\n\t"
"ds_write_addtid_b32 v208 offset:1024\n\t"
"ds_write_addtid_b32 v217 offset:11392\n\t"
"v_mfma_f32_16x16x32_bf16 v[76:79], a[12:15], a[172:175], v[76:79]\n\t"
"buffer_load_dwordx4 a[68:71], off, s[28:31], 0 offset:1024 sc1\n\t"
"v_mfma_f32_16x16x32_bf16 v[60:63], a[0:3], a[156:159], v[60:63]\n\t"
"s_xor_b32 s20, s20, 384\n\t"
"ds_read_b128 a[156:159], v224 offset:18336\n\t"
"ds_write_addtid_b32 v218 offset:7200\n\t"
"v_mfma_f32_16x16x32_bf16 v[60:63], a[4:7], a[172:175], v[60:63]\n\t"
"s_waitcnt vmcnt(2) lgkmcnt(9)\n\t"
"buffer_load_dwordx4 a[60:63], off, s[84:87], 0 offset:3072 sc1\n\t"
"v_mfma_f32_16x16x32_bf16 v[144:147], a[24:27], a[144:147], v[144:147]\n\t"
"s_nop 0\n\t"
"ds_write_addtid_b32 v192 offset:0\n\t"
"ds_write_addtid_b32 v202 offset:6176\n\t"
"v_mfma_f32_16x16x32_bf16 v[144:147], a[28:31], a[160:163], v[144:147]\n\t"
"s_nop 0\n\t"
"ds_write_addtid_b32 v193 offset:8320\n\t"
"ds_write_addtid_b32 v210 offset:5152\n\t"
"v_mfma_f32_16x16x32_bf16 v[160:163], a[32:35], a[144:147], v[160:163]\n\t"
"s_xor_b32 s27, s27, 384\n\t"
"ds_write_addtid_b32 v201 offset:10368\n\t"
"ds_write_addtid_b32 v203 offset:14496\n\t"
"v_mfma_f32_16x16x32_bf16 v[160:163], a[36:39], a[160:163], v[160:163]\n\t"
"buffer_load_dwordx4 a[56:59], off, s[84:87], 0 offset:2048 sc1\n\t"
"v_mfma_f32_16x16x32_bf16 v[176:179], a[40:43], a[144:147], v[176:179]\n\t"
"ds_read_b128 a[172:175], v224 offset:20384\n\t"
"v_mfma_f32_16x16x32_bf16 v[176:179], a[44:47], a[160:163], v[176:179]\n\t"
"buffer_load_dwordx4 a[48:51], off, s[84:87], 0 offset:0 sc1\n\t"
"v_mfma_f32_16x16x32_bf16 v[180:183], a[40:43], a[148:151], v[180:183]\n\t"
"ds_read_b64_tr_b16 a[200:201], v250 offset:21888\n\t"
"v_mfma_f32_16x16x32_bf16 v[180:183], a[44:47], a[164:167], v[180:183]\n\t"
"ds_read_b64_tr_b16 a[202:203], v250 offset:22400\n\t"
"v_mfma_f32_16x16x32_bf16 v[164:167], a[32:35], a[148:151], v[164:167]\n\t"
"s_xor_b32 s25, s25, 384\n\t"
"ds_read_b64_tr_b16 a[204:205], v251 offset:21888\n\t"
"ds_read_b64_tr_b16 a[206:207], v251 offset:22400\n\t"
"v_mfma_f32_16x16x32_bf16 v[164:167], a[36:39], a[164:167], v[164:167]\n\t"
"buffer_load_dwordx4 a[52:55], off, s[84:87], 0 offset:1024 sc1\n\t"
"v_mfma_f32_16x16x32_bf16 v[148:151], a[24:27], a[148:151], v[148:151]\n\t"
"v_mfma_f32_16x16x32_bf16 v[148:151], a[28:31], a[164:167], v[148:151]\n\t"
"s_waitcnt lgkmcnt(0)\n\t"
"s_barrier\n\t"
"v_mfma_f32_16x16x32_bf16 v[152:155], a[24:27], a[152:155], v[152:155]\n\t"
"ds_read_b128 a[160:163], v224 offset:2048\n\t"
"v_mfma_f32_16x16x32_bf16 v[152:155], a[28:31], a[168:171], v[152:155]\n\t"
"ds_read_b128 a[144:147], v224 offset:0\n\t"
"v_mfma_f32_16x16x32_bf16 v[168:171], a[32:35], a[152:155], v[168:171]\n\t"
"ds_read_b128 a[148:151], v224 offset:256\n\t"
"v_mfma_f32_16x16x32_bf16 v[168:171], a[36:39], a[168:171], v[168:171]\n\t"
"s_nop 0\n\t"
"ds_read_b128 a[164:167], v224 offset:2304\n\t"
"v_mfma_f32_16x16x32_bf16 v[184:187], a[40:43], a[152:155], v[184:187]\n\t"
"s_nop 0\n\t"
"v_mfma_f32_16x16x32_bf16 v[184:187], a[44:47], a[168:171], v[184:187]\n\t"
"s_nop 0\n\t"
"v_mfma_f32_16x16x32_bf16 v[188:191], a[40:43], a[156:159], v[188:191]\n\t"
"s_nop 0\n\t"
"ds_read_b128 a[152:155], v224 offset:512\n\t"
"v_mfma_f32_16x16x32_bf16 v[188:191], a[44:47], a[172:175], v[188:191]\n\t"
"s_nop 0\n\t"
"v_mfma_f32_16x16x32_bf16 v[172:175], a[32:35], a[156:159], v[172:175]\n\t"
"s_nop 0\n\t"
"v_mfma_f32_16x16x32_bf16 v[172:175], a[36:39], a[172:175], v[172:175]\n\t"
"buffer_load_dwordx4 a[88:91], off, s[28:31], s51 offset:0 sc1\n\t"
"v_mfma_f32_16x16x32_bf16 v[156:159], a[24:27], a[156:159], v[156:159]\n\t"
"s_nop 0\n\t"
"ds_read_b128 a[168:171], v224 offset:2560\n\t"
"v_mfma_f32_16x16x32_bf16 v[156:159], a[28:31], a[172:175], v[156:159]\n\t"
"buffer_load_dwordx4 a[92:95], off, s[28:31], s51 offset:1024 sc1\n\t"
"v_mfma_f32_16x16x32_bf16 v[192:195], a[224:227], a[176:179], 0\n\t"
"Lpad12_%=:\n\t"
"s_cbranch_execz Lret1_%=\n\t"
"v_mfma_f32_16x16x32_bf16 v[196:199], a[224:227], a[180:183], 0\n\t"
"buffer_load_dwordx4 a[84:87], off, s[84:87], s51 offset:3072 sc1\n\t"
"v_mfma_f32_16x16x32_bf16 v[200:203], a[224:227], a[184:187], 0\n\t"
"ds_read_b128 a[156:159], v224 offset:768\n\t"
"v_mfma_f32_16x16x32_bf16 v[204:207], a[224:227], a[188:191], 0\n\t"
".p2align 3\n\t"
"buffer_load_dwordx4 a[80:83], off, s[84:87], s51 offset:2048 sc1\n\t"
"v_pk_fma_f32 v[48:49], v[144:145], s[90:91], v[48:49]\n\t"
"v_pk_fma_f32 v[50:51], v[146:147], s[90:91], v[50:51]\n\t"
"v_pk_add_f32 v[144:145], v[48:49], v[144:145] neg_lo:[0,1] neg_hi:[0,1]\n\t"
"v_pk_add_f32 v[146:147], v[50:51], v[146:147] neg_lo:[0,1] neg_hi:[0,1]\n\t"
"v_pk_fma_f32 v[64:65], v[160:161], s[90:91], v[64:65]\n\t"
"v_pk_fma_f32 v[66:67], v[162:163], s[90:91], v[66:67]\n\t"
"v_pk_add_f32 v[160:161], v[64:65], v[160:161] neg_lo:[0,1] neg_hi:[0,1]\n\t"
"v_pk_add_f32 v[162:163], v[66:67], v[162:163] neg_lo:[0,1] neg_hi:[0,1]\n\t"
"v_pk_fma_f32 v[80:81], v[176:177], s[90:91], v[80:81]\n\t"
"v_pk_fma_f32 v[82:83], v[178:179], s[90:91], v[82:83]\n\t"
"v_pk_add_f32 v[176:177], v[80:81], v[176:177] neg_lo:[0,1] neg_hi:[0,1]\n\t"
"v_pk_add_f32 v[178:179], v[82:83], v[178:179] neg_lo:[0,1] neg_hi:[0,1]\n\t"
"v_pk_fma_f32 v[84:85], v[180:181], s[90:91], v[84:85]\n\t"
"v_pk_fma_f32 v[86:87], v[182:183], s[90:91], v[86:87]\n\t"
"v_pk_add_f32 v[180:181], v[84:85], v[180:181] neg_lo:[0,1] neg_hi:[0,1]\n\t"
"v_pk_add_f32 v[182:183], v[86:87], v[182:183] neg_lo:[0,1] neg_hi:[0,1]\n\t"
"v_pk_fma_f32 v[68:69], v[164:165], s[90:91], v[68:69]\n\t"
"v_pk_fma_f32 v[70:71], v[166:167], s[90:91], v[70:71]\n\t"
"v_pk_add_f32 v[164:165], v[68:69], v[164:165] neg_lo:[0,1] neg_hi:[0,1]\n\t"
"v_pk_add_f32 v[166:167], v[70:71], v[166:167] neg_lo:[0,1] neg_hi:[0,1]\n\t"
"v_pk_fma_f32 v[52:53], v[148:149], s[90:91], v[52:53]\n\t"
"v_pk_fma_f32 v[54:55], v[150:151], s[90:91], v[54:55]\n\t"
"v_pk_add_f32 v[148:149], v[52:53], v[148:149] neg_lo:[0,1] neg_hi:[0,1]\n\t"
"v_pk_add_f32 v[150:151], v[54:55], v[150:151] neg_lo:[0,1] neg_hi:[0,1]\n\t"
"v_pk_fma_f32 v[56:57], v[152:153], s[90:91], v[56:57]\n\t"
"v_pk_fma_f32 v[58:59], v[154:155], s[90:91], v[58:59]\n\t"
"v_pk_add_f32 v[152:153], v[56:57], v[152:153] neg_lo:[0,1] neg_hi:[0,1]\n\t"
"v_pk_add_f32 v[154:155], v[58:59], v[154:155] neg_lo:[0,1] neg_hi:[0,1]\n\t"
"v_pk_fma_f32 v[72:73], v[168:169], s[90:91], v[72:73]\n\t"
"v_pk_fma_f32 v[74:75], v[170:171], s[90:91], v[74:75]\n\t"
"v_pk_add_f32 v[168:169], v[72:73], v[168:169] neg_lo:[0,1] neg_hi:[0,1]\n\t"
"v_pk_add_f32 v[170:171], v[74:75], v[170:171] neg_lo:[0,1] neg_hi:[0,1]\n\t"
"v_pk_fma_f32 v[88:89], v[184:185], s[90:91], v[88:89]\n\t"
"v_pk_fma_f32 v[90:91], v[186:187], s[90:91], v[90:91]\n\t"
"v_pk_add_f32 v[184:185], v[88:89], v[184:185] neg_lo:[0,1] neg_hi:[0,1]\n\t"
"v_pk_add_f32 v[186:187], v[90:91], v[186:187] neg_lo:[0,1] neg_hi:[0,1]\n\t"
"v_pk_fma_f32 v[92:93], v[188:189], s[90:91], v[92:93]\n\t"
"v_pk_fma_f32 v[94:95], v[190:191], s[90:91], v[94:95]\n\t"
"v_pk_add_f32 v[188:189], v[92:93], v[188:189] neg_lo:[0,1] neg_hi:[0,1]\n\t"
"v_pk_add_f32 v[190:191], v[94:95], v[190:191] neg_lo:[0,1] neg_hi:[0,1]\n\t"
"v_pk_fma_f32 v[76:77], v[172:173], s[90:91], v[76:77]\n\t"
"v_pk_fma_f32 v[78:79], v[174:175], s[90:91], v[78:79]\n\t"
"v_pk_add_f32 v[172:173], v[76:77], v[172:173] neg_lo:[0,1] neg_hi:[0,1]\n\t"
"v_pk_add_f32 v[174:175], v[78:79], v[174:175] neg_lo:[0,1] neg_hi:[0,1]\n\t"
"v_pk_fma_f32 v[60:61], v[156:157], s[90:91], v[60:61]\n\t"
"v_pk_fma_f32 v[62:63], v[158:159], s[90:91], v[62:63]\n\t"
"v_pk_add_f32 v[156:157], v[60:61], v[156:157] neg_lo:[0,1] neg_hi:[0,1]\n\t"
"v_pk_add_f32 v[158:159], v[62:63], v[158:159] neg_lo:[0,1] neg_hi:[0,1]\n\t"
"v_mfma_f32_16x16x32_bf16 v[208:211], a[224:227], a[192:195], 0\n\t"
"buffer_load_dwordx4 a[72:75], off, s[84:87], s51 offset:0 sc1\n\t"
"v_mfma_f32_16x16x32_bf16 v[212:215], a[224:227], a[196:199], 0\n\t"
"ds_read_b128 a[172:175], v224 offset:2816\n\t"
"v_mfma_f32_16x16x32_bf16 v[216:219], a[224:227], a[200:203], 0\n\t"
"buffer_load_dwordx4 a[76:79], off, s[84:87], s51 offset:1024 sc1\n\t"
"v_mfma_f32_16x16x32_bf16 v[220:223], a[224:227], a[204:207], 0\n\t"
"s_waitcnt vmcnt(6) lgkmcnt(3)\n\t"
"v_cvt_pk_bf16_f32 v193, v193, v197\n\t"
"v_cvt_pk_bf16_f32 v194, v194, v198\n\t"
"v_mfma_f32_16x16x32_bf16 v[48:51], a[48:51], a[144:147], v[48:51]\n\t"
"ds_write_addtid_b32 v194 offset:20672\n\t"
"v_cvt_pk_bf16_f32 v195, v195, v199\n\t"
"v_mfma_f32_16x16x32_bf16 v[48:51], a[52:55], a[160:163], v[48:51]\n\t"
"ds_write_addtid_b32 v193 offset:24864\n\t"
"v_cvt_pk_bf16_f32 v201, v201, v205\n\t"
"v_mfma_f32_16x16x32_bf16 v[64:67], a[56:59], a[144:147], v[64:67]\n\t"
"ds_write_addtid_b32 v195 offset:28992\n\t"
"v_cvt_pk_bf16_f32 v192, v192, v196\n\t"
"v_mfma_f32_16x16x32_bf16 v[64:67], a[60:63], a[160:163], v[64:67]\n\t"
"ds_write_addtid_b32 v192 offset:16544\n\t"
"v_cvt_pk_bf16_f32 v209, v209, v213\n\t"
"v_mfma_f32_16x16x32_bf16 v[80:83], a[64:67], a[144:147], v[80:83]\n\t"
"ds_read_b128 a[144:147], v224 offset:1024\n\t"
"v_cvt_pk_bf16_f32 v200, v200, v204\n\t"
"v_mfma_f32_16x16x32_bf16 v[80:83], a[68:71], a[160:163], v[80:83]\n\t"
"ds_write_addtid_b32 v201 offset:26912\n\t"
"v_cvt_pk_bf16_f32 v48, v48, v49\n\t"
"v_mfma_f32_16x16x32_bf16 v[84:87], a[64:67], a[148:151], v[84:87]\n\t"
"ds_read_b128 a[160:163], v224 offset:3072\n\t"
"v_cvt_pk_bf16_f32 v218, v218, v222\n\t"
"v_mfma_f32_16x16x32_bf16 v[84:87], a[68:71], a[164:167], v[84:87]\n\t"
"ds_write_addtid_b32 v209 offset:25888\n\t"
"v_cvt_pk_bf16_f32 v216, v216, v220\n\t"
"v_cvt_pk_bf16_f32 v49, v50, v51\n\t"
"v_mfma_f32_16x16x32_bf16 v[68:71], a[56:59], a[148:151], v[68:71]\n\t"
"ds_write_addtid_b32 v218 offset:23744\n\t"
"v_cvt_pk_bf16_f32 v202, v202, v206\n\t"
"v_mfma_f32_16x16x32_bf16 v[68:71], a[60:63], a[164:167], v[68:71]\n\t"
"ds_write_addtid_b32 v200 offset:18592\n\t"
"v_cvt_pk_bf16_f32 v219, v219, v223\n\t"
"v_mfma_f32_16x16x32_bf16 v[52:55], a[48:51], a[148:151], v[52:55]\n\t"
"ds_read_b128 a[148:151], v224 offset:1280\n\t"
"v_cvt_pk_bf16_f32 v80, v80, v81\n\t"
"v_mfma_f32_16x16x32_bf16 v[52:55], a[52:55], a[164:167], v[52:55]\n\t"
"s_waitcnt lgkmcnt(9)\n\t"
"ds_read_b128 a[164:167], v224 offset:3328\n\t"
"v_cvt_pk_bf16_f32 v208, v208, v212\n\t"
"v_cvt_pk_bf16_f32 v210, v210, v214\n\t"
"v_cvt_pk_bf16_f32 v211, v211, v215\n\t"
"v_cvt_pk_bf16_f32 v217, v217, v221\n\t"
"v_mfma_f32_16x16x32_bf16 v[56:59], a[48:51], a[152:155], v[56:59]\n\t"
"ds_write_addtid_b32 v211 offset:30016\n\t"
"v_cvt_pk_bf16_f32 v203, v203, v207\n\t"
"v_mfma_f32_16x16x32_bf16 v[56:59], a[52:55], a[168:171], v[56:59]\n\t"
"s_nop 0\n\t"
"ds_write_addtid_b32 v202 offset:22720\n\t"
"ds_write_addtid_b32 v203 offset:31040\n\t"
"ds_write_addtid_b32 v208 offset:17568\n\t"
"ds_write_addtid_b32 v216 offset:19616\n\t"
"buffer_load_dwordx4 a[112:115], off, s[28:31], s81 offset:0 sc1\n\t"
"buffer_load_dwordx4 a[116:119], off, s[28:31], s81 offset:1024 sc1\n\t"
"v_cvt_pk_bf16_f32 v84, v84, v85\n\t"
"v_mfma_f32_16x16x32_bf16 v[72:75], a[56:59], a[152:155], v[72:75]\n\t"
"ds_write_addtid_b32 v219 offset:32064\n\t"
"v_cvt_pk_bf16_f32 v81, v82, v83\n\t"
"v_mfma_f32_16x16x32_bf16 v[72:75], a[60:63], a[168:171], v[72:75]\n\t"
"ds_write_addtid_b32 v217 offset:27936\n\t"
"v_cvt_pk_bf16_f32 v52, v52, v53\n\t"
"v_cvt_pk_bf16_f32 v68, v68, v69\n\t"
"v_mfma_f32_16x16x32_bf16 v[88:91], a[64:67], a[152:155], v[88:91]\n\t"
"ds_read_b128 a[152:155], v224 offset:1536\n\t"
"v_cvt_pk_bf16_f32 v53, v54, v55\n\t"
"v_mfma_f32_16x16x32_bf16 v[88:91], a[68:71], a[168:171], v[88:91]\n\t"
"ds_read_b128 a[168:171], v224 offset:3584\n\t"
"buffer_load_dwordx4 a[108:111], off, s[84:87], s81 offset:3072 sc1\n\t"
"buffer_load_dwordx4 a[104:107], off, s[84:87], s81 offset:2048 sc1\n\t"
"v_mfma_f32_16x16x32_bf16 v[92:95], a[64:67], a[156:159], v[92:95]\n\t"
"v_cvt_pk_bf16_f32 v69, v70, v71\n\t"
"v_cvt_pk_bf16_f32 v85, v86, v87\n\t"
"v_mfma_f32_16x16x32_bf16 v[92:95], a[68:71], a[172:175], v[92:95]\n\t"
"ds_write_addtid_b32 v210 offset:21696\n\t"
"ds_write2st64_b64 v247, v[80:81], v[84:85] offset0:0 offset1:13\n\t"
"v_mfma_f32_16x16x32_bf16 v[76:79], a[56:59], a[156:159], v[76:79]\n\t"
"v_cvt_pk_bf16_f32 v64, v64, v65\n\t"
"v_cvt_pk_bf16_f32 v88, v88, v89\n\t"
"v_mfma_f32_16x16x32_bf16 v[76:79], a[60:63], a[172:175], v[76:79]\n\t"
"buffer_load_dwordx4 a[96:99], off, s[84:87], s81 offset:0 sc1\n\t"
"buffer_load_dwordx4 a[100:103], off, s[84:87], s81 offset:1024 sc1\n\t"
"v_cvt_pk_bf16_f32 v89, v90, v91\n\t"
"v_mfma_f32_16x16x32_bf16 v[60:63], a[48:51], a[156:159], v[60:63]\n\t"
"v_cvt_pk_bf16_f32 v65, v66, v67\n\t"
"v_cvt_pk_bf16_f32 v72, v72, v73\n\t"
"v_mfma_f32_16x16x32_bf16 v[60:63], a[52:55], a[172:175], v[60:63]\n\t"
"s_waitcnt vmcnt(6) lgkmcnt(6)\n\t"
"ds_read_b128 a[156:159], v224 offset:1792\n\t"
"ds_read_b128 a[172:175], v224 offset:3840\n\t"
"ds_write2st64_b64 v228, v[48:49], v[52:53] offset0:0 offset1:13\n\t"
"v_cvt_pk_bf16_f32 v73, v74, v75\n\t"
"v_mfma_f32_16x16x32_bf16 v[144:147], a[72:75], a[144:147], v[144:147]\n\t"
"v_cvt_pk_bf16_f32 v56, v56, v57\n\t"
"v_cvt_pk_bf16_f32 v92, v92, v93\n\t"
"v_mfma_f32_16x16x32_bf16 v[144:147], a[76:79], a[160:163], v[144:147]\n\t"
"buffer_load_dwordx4 a[136:139], off, s[28:31], s66 offset:0 sc1\n\t"
"buffer_load_dwordx4 a[140:143], off, s[28:31], s66 offset:1024 sc1\n\t"
"ds_write2st64_b64 v246, v[64:65], v[68:69] offset0:0 offset1:13\n\t"
"v_cvt_pk_bf16_f32 v57, v58, v59\n\t"
"v_cvt_pk_bf16_f32 v93, v94, v95\n\t"
"v_mfma_f32_16x16x32_bf16 v[160:163], a[80:83], a[144:147], v[160:163]\n\t"
"v_cvt_pk_bf16_f32 v60, v60, v61\n\t"
"v_cvt_pk_bf16_f32 v76, v76, v77\n\t"
"v_mfma_f32_16x16x32_bf16 v[160:163], a[84:87], a[160:163], v[160:163]\n\t"
"buffer_load_dwordx4 a[132:135], off, s[84:87], s66 offset:3072 sc1\n\t"
"ds_write2st64_b64 v247, v[88:89], v[92:93] offset0:26 offset1:39\n\t"
"v_mfma_f32_16x16x32_bf16 v[176:179], a[88:91], a[144:147], v[176:179]\n\t"
"v_cvt_pk_bf16_f32 v61, v62, v63\n\t"
"v_cvt_pk_bf16_f32 v77, v78, v79\n\t"
"v_mfma_f32_16x16x32_bf16 v[176:179], a[92:95], a[160:163], v[176:179]\n\t"
"ds_write2st64_b64 v228, v[56:57], v[60:61] offset0:26 offset1:39\n\t"
"v_mfma_f32_16x16x32_bf16 v[180:183], a[88:91], a[148:151], v[180:183]\n\t"
"buffer_load_dwordx4 a[128:131], off, s[84:87], s66 offset:2048 sc1\n\t"
"v_mfma_f32_16x16x32_bf16 v[180:183], a[92:95], a[164:167], v[180:183]\n\t"
"ds_write2st64_b64 v246, v[72:73], v[76:77] offset0:26 offset1:39\n\t"
"v_mfma_f32_16x16x32_bf16 v[164:167], a[80:83], a[148:151], v[164:167]\n\t"
"buffer_load_dwordx4 a[120:123], off, s[84:87], s66 offset:0 sc1\n\t"
"v_mfma_f32_16x16x32_bf16 v[164:167], a[84:87], a[164:167], v[164:167]\n\t"
"v_mfma_f32_16x16x32_bf16 v[148:151], a[72:75], a[148:151], v[148:151]\n\t"
"buffer_load_dwordx4 a[124:127], off, s[84:87], s66 offset:1024 sc1\n\t"
"v_mfma_f32_16x16x32_bf16 v[148:151], a[76:79], a[164:167], v[148:151]\n\t"
"s_waitcnt lgkmcnt(0)\n\t"
"s_barrier\n\t"
"v_mfma_f32_16x16x32_bf16 v[152:155], a[72:75], a[152:155], v[152:155]\n\t"
"s_nop 0\n\t"
"ds_read_b128 a[148:151], v224 offset:16800\n\t"
"ds_read_b128 a[12:15], v229 offset:13312\n\t"
"v_mfma_f32_16x16x32_bf16 v[152:155], a[76:79], a[168:171], v[152:155]\n\t"
"s_nop 0\n\t"
"ds_read_b128 a[144:147], v224 offset:16544\n\t"
"ds_read_b128 a[160:163], v224 offset:18592\n\t"
"v_mfma_f32_16x16x32_bf16 v[168:171], a[80:83], a[152:155], v[168:171]\n\t"
"ds_read_b128 a[0:3], v229 offset:0\n\t"
"v_mfma_f32_16x16x32_bf16 v[168:171], a[84:87], a[168:171], v[168:171]\n\t"
"ds_read_b128 a[164:167], v224 offset:18848\n\t"
"v_mfma_f32_16x16x32_bf16 v[184:187], a[88:91], a[152:155], v[184:187]\n\t"
"s_nop 0\n\t"
"ds_read_b128 a[4:7], v230 offset:0\n\t"
"ds_read_b128 a[16:19], v230 offset:13312\n\t"
"v_mfma_f32_16x16x32_bf16 v[184:187], a[92:95], a[168:171], v[184:187]\n\t"
"s_nop 0\n\t"
"ds_read_b128 a[8:11], v231 offset:0\n\t"
"ds_read_b128 a[20:23], v231 offset:13312\n\t"
"v_mfma_f32_16x16x32_bf16 v[188:191], a[88:91], a[156:159], v[188:191]\n\t"
"s_waitcnt lgkmcnt(8)\n\t"
"buffer_store_dwordx4 a[12:15], v232, s[16:19], s27 offen nt\n\t"
"v_mfma_f32_16x16x32_bf16 v[188:191], a[92:95], a[172:175], v[188:191]\n\t"
"ds_read_b128 a[152:155], v224 offset:17568\n\t"
"v_mfma_f32_16x16x32_bf16 v[172:175], a[80:83], a[156:159], v[172:175]\n\t"
"ds_read_b128 a[168:171], v224 offset:19616\n\t"
"v_mfma_f32_16x16x32_bf16 v[172:175], a[84:87], a[172:175], v[172:175]\n\t"
"s_waitcnt lgkmcnt(7)\n\t"
"buffer_store_dwordx4 a[0:3], v232, s[16:19], s24 offen nt\n\t"
"v_mfma_f32_16x16x32_bf16 v[156:159], a[72:75], a[156:159], v[156:159]\n\t"
"v_mfma_f32_16x16x32_bf16 v[156:159], a[76:79], a[172:175], v[156:159]\n\t"
"s_waitcnt vmcnt(8)\n\t"
"s_waitcnt lgkmcnt(5)\n\t"
"buffer_store_dwordx4 a[4:7], v233, s[16:19], s24 offen nt\n\t"
"v_mfma_f32_16x16x32_bf16 v[144:147], a[96:99], a[144:147], v[144:147]\n\t"
"ds_read_b128 a[156:159], v224 offset:17824\n\t"
"v_mfma_f32_16x16x32_bf16 v[144:147], a[100:103], a[160:163], v[144:147]\n\t"
"s_waitcnt lgkmcnt(5)\n\t"
"buffer_store_dwordx4 a[16:19], v233, s[16:19], s27 offen nt\n\t"
"v_mfma_f32_16x16x32_bf16 v[160:163], a[104:107], a[144:147], v[160:163]\n\t"
"ds_read_b128 a[172:175], v224 offset:19872\n\t"
"v_mfma_f32_16x16x32_bf16 v[160:163], a[108:111], a[160:163], v[160:163]\n\t"
"s_waitcnt lgkmcnt(4)\n\t"
"buffer_store_dwordx4 a[8:11], v234, s[16:19], s24 offen nt\n\t"
"v_mfma_f32_16x16x32_bf16 v[176:179], a[112:115], a[144:147], v[176:179]\n\t"
"ds_read_b128 a[144:147], v224 offset:17056\n\t"
"v_mfma_f32_16x16x32_bf16 v[176:179], a[116:119], a[160:163], v[176:179]\n\t"
"v_mfma_f32_16x16x32_bf16 v[180:183], a[112:115], a[148:151], v[180:183]\n\t"
"buffer_store_dwordx4 a[20:23], v234, s[16:19], s27 offen nt\n\t"
"v_mfma_f32_16x16x32_bf16 v[180:183], a[116:119], a[164:167], v[180:183]\n\t"
"ds_read_b128 a[160:163], v224 offset:19104\n\t"
"v_mfma_f32_16x16x32_bf16 v[164:167], a[104:107], a[148:151], v[164:167]\n\t"
"v_mfma_f32_16x16x32_bf16 v[164:167], a[108:111], a[164:167], v[164:167]\n\t"
"v_mfma_f32_16x16x32_bf16 v[148:151], a[96:99], a[148:151], v[148:151]\n\t"
"ds_read_b128 a[148:151], v224 offset:17312\n\t"
"v_mfma_f32_16x16x32_bf16 v[148:151], a[100:103], a[164:167], v[148:151]\n\t"
"s_waitcnt vmcnt(6) lgkmcnt(4)\n\t"
"v_mfma_f32_16x16x32_bf16 v[144:147], a[120:123], a[152:155], v[144:147]\n\t"
"ds_read_b128 a[164:167], v224 offset:19360\n\t"
"v_mfma_f32_16x16x32_bf16 v[144:147], a[124:127], a[168:171], v[144:147]\n\t"
"v_mfma_f32_16x16x32_bf16 v[160:163], a[128:131], a[152:155], v[160:163]\n\t"
"v_mfma_f32_16x16x32_bf16 v[160:163], a[132:135], a[168:171], v[160:163]\n\t"
"v_mfma_f32_16x16x32_bf16 v[176:179], a[136:139], a[152:155], v[176:179]\n\t"
"ds_read_b128 a[152:155], v224 offset:18080\n\t"
"v_mfma_f32_16x16x32_bf16 v[176:179], a[140:143], a[168:171], v[176:179]\n\t"
"s_waitcnt lgkmcnt(3)\n\t"
"s_nop 1\n\t"
"v_cvt_pk_bf16_f32 v144, v144, v145\n\t"
"v_cvt_pk_bf16_f32 v145, v146, v147\n\t"
"v_mfma_f32_16x16x32_bf16 v[180:183], a[136:139], a[156:159], v[180:183]\n\t"
"ds_read_b128 a[168:171], v224 offset:20128\n\t"
"v_mfma_f32_16x16x32_bf16 v[180:183], a[140:143], a[172:175], v[180:183]\n\t"
"v_mfma_f32_16x16x32_bf16 v[164:167], a[128:131], a[156:159], v[164:167]\n\t"
"v_cvt_pk_bf16_f32 v160, v160, v161\n\t"
"v_cvt_pk_bf16_f32 v176, v176, v177\n\t"
"v_mfma_f32_16x16x32_bf16 v[164:167], a[132:135], a[172:175], v[164:167]\n\t"
"v_cvt_pk_bf16_f32 v161, v162, v163\n\t"
"v_mfma_f32_16x16x32_bf16 v[148:151], a[120:123], a[156:159], v[148:151]\n\t"
"ds_read_b128 a[156:159], v224 offset:18336\n\t"
"s_nop 0\n\t"
"v_cvt_pk_bf16_f32 v180, v180, v181\n\t"
"v_mfma_f32_16x16x32_bf16 v[148:151], a[124:127], a[172:175], v[148:151]\n\t"
"ds_read_b128 a[172:175], v224 offset:20384\n\t"
"v_cvt_pk_bf16_f32 v181, v182, v183\n\t"
"v_mfma_f32_16x16x32_bf16 v[152:155], a[96:99], a[144:147], v[152:155]\n\t"
"v_cvt_pk_bf16_f32 v177, v178, v179\n\t"
"v_mfma_f32_16x16x32_bf16 v[152:155], a[100:103], a[160:163], v[152:155]\n\t"
"ds_write2st64_b64 v247, v[176:177], v[180:181] offset0:52 offset1:65\n\t"
"v_mfma_f32_16x16x32_bf16 v[168:171], a[104:107], a[144:147], v[168:171]\n\t"
"v_cvt_pk_bf16_f32 v164, v164, v165\n\t"
"v_cvt_pk_bf16_f32 v165, v166, v167\n\t"
"v_mfma_f32_16x16x32_bf16 v[168:171], a[108:111], a[160:163], v[168:171]\n\t"
"v_cvt_pk_bf16_f32 v148, v148, v149\n\t"
"v_cvt_pk_bf16_f32 v149, v150, v151\n\t"
"v_mfma_f32_16x16x32_bf16 v[184:187], a[112:115], a[144:147], v[184:187]\n\t"
"ds_write2st64_b64 v228, v[144:145], v[148:149] offset0:52 offset1:65\n\t"
"v_mfma_f32_16x16x32_bf16 v[184:187], a[116:119], a[160:163], v[184:187]\n\t"
"s_waitcnt lgkmcnt(4)\n\t"
"v_mfma_f32_16x16x32_bf16 v[188:191], a[112:115], a[148:151], v[188:191]\n\t"
"v_mfma_f32_16x16x32_bf16 v[188:191], a[116:119], a[164:167], v[188:191]\n\t"
"ds_write2st64_b64 v246, v[160:161], v[164:165] offset0:52 offset1:65\n\t"
"v_mfma_f32_16x16x32_bf16 v[172:175], a[104:107], a[148:151], v[172:175]\n\t"
"v_mfma_f32_16x16x32_bf16 v[172:175], a[108:111], a[164:167], v[172:175]\n\t"
"v_mfma_f32_16x16x32_bf16 v[156:159], a[96:99], a[148:151], v[156:159]\n\t"
"v_mfma_f32_16x16x32_bf16 v[156:159], a[100:103], a[164:167], v[156:159]\n\t"
"s_waitcnt lgkmcnt(0)\n\t"
"s_barrier\n\t"
"v_mfma_f32_16x16x32_bf16 v[152:155], a[120:123], a[152:155], v[152:155]\n\t"
"ds_read_b128 a[32:35], v231 offset:26624\n\t"
"v_mfma_f32_16x16x32_bf16 v[152:155], a[124:127], a[168:171], v[152:155]\n\t"
"ds_read_b128 a[24:27], v229 offset:26624\n\t"
"v_mfma_f32_16x16x32_bf16 v[168:171], a[128:131], a[152:155], v[168:171]\n\t"
"v_mfma_f32_16x16x32_bf16 v[168:171], a[132:135], a[168:171], v[168:171]\n\t"
"v_mfma_f32_16x16x32_bf16 v[184:187], a[136:139], a[152:155], v[184:187]\n\t"
"ds_read_b128 a[28:31], v230 offset:26624\n\t"
"s_nop 2\n\t"
"v_cvt_pk_bf16_f32 v152, v152, v153\n\t"
"v_mfma_f32_16x16x32_bf16 v[184:187], a[140:143], a[168:171], v[184:187]\n\t"
"v_cvt_pk_bf16_f32 v153, v154, v155\n\t"
"v_mfma_f32_16x16x32_bf16 v[188:191], a[136:139], a[156:159], v[188:191]\n\t"
"s_waitcnt lgkmcnt(2)\n\t"
"buffer_store_dwordx4 a[32:35], v234, s[16:19], s20 offen nt\n\t"
"v_mfma_f32_16x16x32_bf16 v[188:191], a[140:143], a[172:175], v[188:191]\n\t"
"v_cvt_pk_bf16_f32 v168, v168, v169\n\t"
"v_cvt_pk_bf16_f32 v169, v170, v171\n\t"
"v_mfma_f32_16x16x32_bf16 v[172:175], a[128:131], a[156:159], v[172:175]\n\t"
"v_cvt_pk_bf16_f32 v184, v184, v185\n\t"
"v_cvt_pk_bf16_f32 v185, v186, v187\n\t"
"v_mfma_f32_16x16x32_bf16 v[172:175], a[132:135], a[172:175], v[172:175]\n\t"
"s_waitcnt lgkmcnt(1)\n\t"
"buffer_store_dwordx4 a[24:27], v232, s[16:19], s20 offen nt\n\t"
"v_mfma_f32_16x16x32_bf16 v[156:159], a[120:123], a[156:159], v[156:159]\n\t"
"v_cvt_pk_bf16_f32 v188, v188, v189\n\t"
"v_cvt_pk_bf16_f32 v189, v190, v191\n\t"
"v_mfma_f32_16x16x32_bf16 v[156:159], a[124:127], a[172:175], v[156:159]\n\t"
"s_waitcnt lgkmcnt(0)\n\t"
"buffer_store_dwordx4 a[28:31], v233, s[16:19], s20 offen nt\n\t"
"s_nop 5\n\t"
"v_cvt_pk_bf16_f32 v156, v156, v157\n\t"
"v_cvt_pk_bf16_f32 v157, v158, v159\n\t"
"ds_write2st64_b64 v228, v[152:153], v[156:157] offset0:78 offset1:91\n\t"
"v_cvt_pk_bf16_f32 v172, v172, v173\n\t"
"v_cvt_pk_bf16_f32 v173, v174, v175\n\t"
"ds_write2st64_b64 v246, v[168:169], v[172:173] offset0:78 offset1:91\n\t"
"ds_write2st64_b64 v247, v[184:185], v[188:189] offset0:78 offset1:91\n\t"
"s_waitcnt lgkmcnt(0)\n\t"
"s_barrier\n\t"
"ds_read_b128 a[36:39], v229 offset:39936\n\t"
"ds_read_b128 a[40:43], v230 offset:39936\n\t"
"ds_read_b128 a[44:47], v231 offset:39936\n\t"
"s_waitcnt lgkmcnt(2)\n\t"
"buffer_store_dwordx4 a[36:39], v232, s[16:19], s25 offen nt\n\t"
"s_waitcnt lgkmcnt(1)\n\t"
"buffer_store_dwordx4 a[40:43], v233, s[16:19], s25 offen nt\n\t"
"s_waitcnt lgkmcnt(0)\n\t"
"buffer_store_dwordx4 a[44:47], v234, s[16:19], s25 offen nt\n\t"
"s_waitcnt vmcnt(0)\n\t"
"Lwino_end:\n\t"
  : "+{v205}"(tid)
  : "s"(__builtin_amdgcn_kernarg_segment_ptr()), "s"(blockIdx.x), "s"((unsigned)(size_t)(__attribute__((address_space(3))) char*)smem)
  : "v0", "v1", "v2", "v3", "v4", "v5", "v6", "v7", "v8", "v9", "v10", "v11", "v12", "v13", "v14", "v15", "v16", "v17", "v18", "v19", "v20", "v21", "v22", "v23", "v24", "v25", "v26", "v27", "v28", "v29", "v30", "v31", "v32", "v33", "v34", "v35", "v36", "v37", "v38", "v39", "v40", "v41", "v42", "v43", "v44", "v45", "v46", "v47", "v48", "v49", "v50", "v51", "v52", "v53", "v54", "v55", "v56", "v57", "v58", "v59", "v60", "v61", "v62", "v63", "v64", "v65", "v66", "v67", "v68", "v69", "v70", "v71", "v72", "v73", "v74", "v75", "v76", "v77", "v78", "v79", "v80", "v81", "v82", "v83", "v84", "v85", "v86", "v87", "v88", "v89", "v90", "v91", "v92", "v93", "v94", "v95", "v96", "v97", "v98", "v99", "v100", "v101", "v102", "v103", "v104", "v105", "v106", "v107", "v108", "v109", "v110", "v111", "v112", "v113", "v114", "v115", "v116", "v117", "v118", "v119", "v120", "v121", "v122", "v123", "v124", "v125", "v126", "v127", "v128", "v129", "v130", "v131", "v132", "v133", "v134", "v135", "v136", "v137", "v138", "v139", "v140", "v141", "v142", "v143", "v144", "v145", "v146", "v147", "v148", "v149", "v150", "v151", "v152", "v153", "v154", "v155", "v156", "v157", "v158", "v159", "v160", "v161", "v162", "v163", "v164", "v165", "v166", "v167", "v168", "v169", "v170", "v171", "v172", "v173", "v174", "v175", "v176", "v177", "v178", "v179", "v180", "v181", "v182", "v183", "v184", "v185", "v186", "v187", "v188", "v189", "v190", "v191", "v192", "v193", "v194", "v195", "v196", "v197", "v198", "v199", "v200", "v201", "v202", "v203", "v204", "v206", "v207", "v208", "v209", "v210", "v211", "v212", "v213", "v214", "v215", "v216", "v217", "v218", "v219", "v220", "v221", "v222", "v223", "v224", "v225", "v226", "v227", "v228", "v229", "v230", "v231", "v232", "v233", "v234", "v235", "v236", "v237", "v238", "v239", "v240", "v241", "v242", "v243", "v244", "v245", "v246", "v247", "v248", "v249", "v250", "v251", "v252", "v253", "v254", "v255", "a0", "a1", "a2", "a3", "a4", "a5", "a6", "a7", "a8", "a9", "a10", "a11", "a12", "a13", "a14", "a15", "a16", "a17", "a18", "a19", "a20", "a21", "a22", "a23", "a24", "a25", "a26", "a27", "a28", "a29", "a30", "a31", "a32", "a33", "a34", "a35", "a36", "a37", "a38", "a39", "a40", "a41", "a42", "a43", "a44", "a45", "a46", "a47", "a48", "a49", "a50", "a51", "a52", "a53", "a54", "a55", "a56", "a57", "a58", "a59", "a60", "a61", "a62", "a63", "a64", "a65", "a66", "a67", "a68", "a69", "a70", "a71", "a72", "a73", "a74", "a75", "a76", "a77", "a78", "a79", "a80", "a81", "a82", "a83", "a84", "a85", "a86", "a87", "a88", "a89", "a90", "a91", "a92", "a93", "a94", "a95", "a96", "a97", "a98", "a99", "a100", "a101", "a102", "a103", "a104", "a105", "a106", "a107", "a108", "a109", "a110", "a111", "a112", "a113", "a114", "a115", "a116", "a117", "a118", "a119", "a120", "a121", "a122", "a123", "a124", "a125", "a126", "a127", "a128", "a129", "a130", "a131", "a132", "a133", "a134", "a135", "a136", "a137", "a138", "a139", "a140", "a141", "a142", "a143", "a144", "a145", "a146", "a147", "a148", "a149", "a150", "a151", "a152", "a153", "a154", "a155", "a156", "a157", "a158", "a159", "a160", "a161", "a162", "a163", "a164", "a165", "a166", "a167", "a168", "a169", "a170", "a171", "a172", "a173", "a174", "a175", "a176", "a177", "a178", "a179", "a180", "a181", "a182", "a183", "a184", "a185", "a186", "a187", "a188", "a189", "a190", "a191", "a192", "a193", "a194", "a195", "a196", "a197", "a198", "a199", "a200", "a201", "a202", "a203", "a204", "a205", "a206", "a207", "a208", "a209", "a210", "a211", "a212", "a213", "a214", "a215", "a216", "a217", "a218", "a219", "a220", "a221", "a222", "a223", "a224", "a225", "a226", "a227", "a228", "a229", "a230", "a231", "a232", "a233", "a234", "a235", "a236", "a237", "a238", "a239", "a240", "a241", "a242", "a243", "a244", "a245", "a246", "a247", "a248", "a249", "a250", "a251", "a252", "a253", "a254", "a255", "s6", "s7", "s8", "s9", "s10", "s11", "s12", "s13", "s14", "s15", "s16", "s17", "s18", "s19", "s20", "s21", "s22", "s23", "s24", "s25", "s26", "s27", "s28", "s29", "s30", "s31", "s32", "s33", "s34", "s35", "s36", "s37", "s38", "s39", "s40", "s41", "s42", "s43", "s44", "s45", "s46", "s47", "s48", "s49", "s50", "s51", "s52", "s53", "s54", "s55", "s56", "s57", "s58", "s59", "s60", "s61", "s62", "s63", "s64", "s65", "s66", "s67", "s68", "s69", "s70", "s71", "s72", "s73", "s74", "s75", "s76", "s77", "s78", "s79", "s80", "s81", "s82", "s83", "s84", "s85", "s86", "s87", "s88", "s89", "s90", "s91", "s92", "s93", "s94", "s95", "s96", "s97", "s98", "s99", "scc", "vcc", "memory");
}

extern "C" void kernel_launch(void* const* d_in, const int* in_sizes, int n_in, void* d_out, int out_size, void* d_ws,
                              size_t ws_size, hipStream_t stream) {
  wino_main_kernel<<<256, 256, 0, stream>>>((const char*)d_in[0], (char*)d_ws, (char*)d_out, (const char*)d_in[1]);
}
```

```cpp
#include <hip/hip_runtime.h>
#include <hip/hip_bf16.h>
#include <stdint.h>

#define U_BYTES 1179648

__global__ __launch_bounds__(256, 1) __attribute__((section(".text.w"))) void wino_main_kernel(const char* __restrict__ x, char* __restrict__ Uq, char* __restrict__ out, const char* __restrict__ wt) {
  __shared__ __attribute__((aligned(1024))) char smem[163840];
  unsigned tid = threadIdx.x;
  asm volatile(
".pushsection .text\n\t"
".p2align 12\n\t"
".fill 576, 4, 0xbf800000\n\t"
".popsection\n\t"
".pushsection .text.x,\"ax\"\n\t"
".fill 256, 4, 0xbf800000\n\t"
".popsection\n\t"
"s_load_dwordx8 s[76:83], %1, 0x0\n\t"
"s_mov_b32 s66, %2\n\t"
"s_getpc_b64 s[6:7]\n\t"
"Lwino_pc:\n\t"
"s_bfe_u32 s42, %2, 0x50003\n\t"
"s_lshl_b32 s42, s42, 12\n\t"
"v_lshl_add_u32 v203, v205, 4, s42\n\t"
"v_min_u32 v203, (Lwino_end-Lwino_pc-16)&-16, v203\n\t"
"global_load_dwordx4 v[196:199], v203, s[6:7]\n\t"
"v_and_b32 v204, 63, v205\n\t"
"v_readfirstlane_b32 s73, v205\n\t"
"v_lshrrev_b32 v120, 3, v204\n\t"
"v_and_b32 v121, 7, v204\n\t"
"s_lshr_b32 s73, s73, 6\n\t"
"s_lshr_b32 s56, s66, 5\n\t"
"s_mul_i32 s56, s56, 12288\n\t"
"s_bfe_u32 s61, s66, 0x20001\n\t"
"s_mul_i32 s61, s61, 1536\n\t"
"s_add_u32 s56, s56, s61\n\t"
"s_bitcmp1_b32 s66, 0\n\t"
"s_cselect_b32 s61, 48, 0\n\t"
"s_add_u32 s56, s56, s61\n\t"
"s_bfe_u32 s62, s66, 0x20003\n\t"
"s_mul_i32 s61, s62, 96\n\t"
"s_add_u32 s56, s56, s61\n\t"
"s_cmp_lt_u32 s66, 192\n\t"
"s_cselect_b32 s56, s56, 2.0\n\t"
"v_bfe_u32 v42, v204, 3, 2\n\t"
"v_lshlrev_b32 v43, 2, v204\n\t"
"v_and_or_b32 v42, v43, 16, v42\n\t"
"v_mul_u32_u24 v42, 0x180, v42\n\t"
"v_lshrrev_b32 v44, 5, v204\n\t"
"v_mul_u32_u24 v43, 0x12000, v44\n\t"
"v_and_b32 v45, 3, v204\n\t"
"v_min_u32 v41, 2, v45\n\t"
"v_lshl_add_u32 v41, v41, 4, v42\n\t"
"v_add_u32 v41, v41, v43\n\t"
"v_lshlrev_b32 v43, 4, v204\n\t"
"v_bfe_u32 v45, v204, 1, 2\n\t"
"v_and_or_b32 v45, v43, 16, v45\n\t"
"v_mul_u32_u24 v45, 0x180, v45\n\t"
"v_lshl_add_u32 v44, v44, 3, v120\n\t"
"v_lshl_add_u32 v45, v44, 1, v45\n\t"
"v_mov_b32 v40, 0\n\t"
"s_mul_i32 s61, s73, 147456\n\t"
"s_add_u32 s61, s61, s56\n\t"
"s_lshl3_add_u32 s62, s73, s56\n\t"
"s_add_u32 s62, s62, 589824\n\t"
"s_lshl_b32 s64, s73, 10\n\t"
"s_and_b32 s60, s66, 7\n\t"
"s_lshr_b32 s65, s66, 3\n\t"
"s_mul_i32 s24, s60, 12582912\n\t"
"s_mul_i32 s63, s65, 6144\n\t"
"s_add_u32 s24, s24, s63\n\t"
"s_add_u32 s22, s24, 196224\n\t"
"s_mul_i32 s63, s73, 786432\n\t"
"s_mul_i32 s26, s73, 4608\n\t"
"s_mov_b32 s48, 189696\n\t"
"s_cmp_eq_u32 s65, 0\n\t"
"s_cselect_b64 s[40:41], 0xff, 0\n\t"
"s_cselect_b64 s[46:47], -1, 0\n\t"
"v_mul_u32_u24 v122, 0x300, v120\n\t"
"v_xor_b32 v123, 4, v121\n\t"
"v_add_u32 v122, s63, v122\n\t"
"v_and_b32 v124, 2, v120\n\t"
"v_xor_b32 v124, v124, v123\n\t"
"v_lshl_add_u32 v128, v124, 4, v122\n\t"
"v_cndmask_b32 v241, v128, 2.0, s[40:41]\n\t"
"v_add_u32 v122, 194816, v122\n\t"
"v_add_u32 v124, -1, v120\n\t"
"v_ashrrev_i32 v125, 31, v124\n\t"
"v_sub_u32 v124, v124, v125\n\t"
"v_and_b32 v124, 2, v124\n\t"
"v_xor_b32 v124, v124, v123\n\t"
"v_mad_i32_i24 v125, v125, s48, v122\n\t"
"v_lshl_add_u32 v129, v124, 4, v125\n\t"
"s_lshl_b64 s[40:41], s[40:41], 8\n\t"
"v_cndmask_b32 v242, v129, 2.0, s[40:41]\n\t"
"v_add_u32 v122, 194816, v122\n\t"
"v_add_u32 v124, -2, v120\n\t"
"v_ashrrev_i32 v125, 31, v124\n\t"
"v_sub_u32 v124, v124, v125\n\t"
"v_and_b32 v124, 2, v124\n\t"
"v_and_or_b32 v124, v125, 4, v124\n\t"
"v_xor_b32 v124, v124, v121\n\t"
"v_mad_i32_i24 v125, v125, s48, v122\n\t"
"v_lshl_add_u32 v130, v124, 4, v125\n\t"
"s_lshl_b64 s[40:41], s[40:41], 8\n\t"
"v_cndmask_b32 v243, v130, 2.0, s[40:41]\n\t"
"v_add_u32 v122, 194816, v122\n\t"
"v_add_u32 v124, -3, v120\n\t"
"v_ashrrev_i32 v125, 31, v124\n\t"
"v_sub_u32 v124, v124, v125\n\t"
"v_and_b32 v124, 2, v124\n\t"
"v_xor_b32 v124, v124, v121\n\t"
"v_mad_i32_i24 v125, v125, s48, v122\n\t"
"v_lshl_add_u32 v131, v124, 4, v125\n\t"
"s_lshl_b64 s[40:41], s[40:41], 8\n\t"
"v_cndmask_b32 v244, v131, 2.0, s[40:41]\n\t"
"v_add_u32 v124, 5, v120\n\t"
"v_and_b32 v124, 2, v124\n\t"
"v_xor_b32 v124, v124, v121\n\t"
"v_add_u32 v122, 2560, v122\n\t"
"v_lshl_add_u32 v245, v124, 4, v122\n\t"
"s_mov_b32 s37, -1\n\t"
"s_mov_b32 s36, 0\n\t"
"s_mul_i32 s67, s73, 48\n\t"
"s_add_u32 s67, s67, 118784\n\t"
"s_mov_b32 s10, 0x200000\n\t"
"s_mov_b32 s11, 0x20000\n\t"
"v_min_u32 v189, 47, v204\n\t"
"v_add_u32 v189, s67, v189\n\t"
"v_mul_u32_u24 v189, 16, v189\n\t"
"s_mov_b32 s54, 663552\n\t"
"s_mov_b32 s55, 0x20000\n\t"
"s_mov_b32 s14, 100663296\n\t"
"s_mov_b32 s15, 0x20000\n\t"
"s_add_u32 m0, s64, 0\n\t"
"s_waitcnt lgkmcnt(0)\n\t"
"s_mov_b32 s52, s82\n\t"
"s_and_b32 s53, s83, 0xffff\n\t"
"buffer_load_dwordx4 v41, s[52:55], s61 offen lds\n\t"
"buffer_load_short_d16_hi v40, v45, s[52:55], s62 offen\n\t"
"s_mov_b32 s8, s78\n\t"
"s_and_b32 s9, s79, 0xffff\n\t"
"buffer_load_dword v188, v189, s[8:11], 0 offen sc1\n\t"
"s_mov_b32 s12, s76\n\t"
"s_and_b32 s13, s77, 0xffff\n\t"
"s_add_u32 m0, s26, 103936\n\t"
"s_nop 0\n\t"
"buffer_load_dwordx4 v241, s[12:15], s22 offen offset:0 nt lds\n\t"
"buffer_load_dwordx4 v242, s[12:15], s22 offen offset:1024 nt lds\n\t"
"buffer_load_dwordx4 v243, s[12:15], s22 offen offset:2048 nt lds\n\t"
"buffer_load_dwordx4 v244, s[12:15], s22 offen offset:3072 nt lds\n\t"
"s_mov_b64 exec, s[36:37]\n\t"
"buffer_load_dwordx4 v245, s[12:15], s22 offen offset:3584 nt lds\n\t"
"s_mov_b64 exec, -1\n\t"
"s_mul_i32 s49, s73, 36\n\t"
"s_mul_i32 s43, s73, 576\n\t"
"s_sub_u32 s42, s22, 203520\n\t"
"s_mov_b32 s44, -1\n\t"
"s_mov_b32 s45, 15\n\t"
"v_min_u32 v124, 35, v204\n\t"
"v_add_u32 v124, s49, v124\n\t"
"v_lshrrev_b32 v119, 3, v124\n\t"
"v_and_b32 v124, 7, v124\n\t"
"v_mul_u32_u24 v127, 0x300, v119\n\t"
"v_cmp_gt_u32 vcc, 9, v119\n\t"
"v_add_u32 v119, -9, v119\n\t"
"v_ashrrev_i32 v125, 31, v119\n\t"
"v_add_u32 v127, s42, v127\n\t"
"v_mad_i32_i24 v127, v125, s48, v127\n\t"
"v_and_b32 v125, 9, v125\n\t"
"v_add_u32 v119, v119, v125\n\t"
"v_and_b32 v125, 2, v119\n\t"
"v_xor_b32 v124, v124, v125\n\t"
"v_lshl_add_u32 v133, v124, 4, v127\n\t"
"s_cmp_eq_u32 s60, 0\n\t"
"s_cselect_b64 s[50:51], vcc, 0\n\t"
"v_cmp_eq_u32 vcc, 0, v119\n\t"
"v_cndmask_b32 v246, v133, 2.0, s[50:51]\n\t"
"s_and_b64 vcc, vcc, s[46:47]\n\t"
"v_cndmask_b32 v246, v246, 2.0, vcc\n\t"
"s_add_u32 m0, s43, 101632\n\t"
"s_mov_b64 exec, s[44:45]\n\t"
"buffer_load_dwordx4 v246, s[12:15], 0 offen offset:0 nt lds\n\t"
"s_mov_b64 exec, -1\n\t"
"s_mul_i32 s70, s66, 16\n\t"
"s_add_u32 s70, s70, 1900544\n\t"
"s_lshl_b32 s61, s73, 8\n\t"
"s_or_b32 s61, s61, s66\n\t"
"s_cmp_lt_u32 s61, 192\n\t"
"s_cselect_b32 s70, s70, 2.0\n\t"
"s_lshr_b32 s61, s66, 3\n\t"
"s_lshl2_add_u32 s61, s61, s73\n\t"
"s_lshl_b32 s60, s61, 8\n\t"
"s_cmp_lt_u32 s61, 96\n\t"
"s_cselect_b32 s60, s60, 2.0\n\t"
"v_lshrrev_b32 v118, 1, v204\n\t"
"v_mul_u32_u24 v118, 220928, v118\n\t"
"v_lshl_add_u32 v118, v204, 7, v118\n\t"
"s_mov_b64 exec, 63\n\t"
"buffer_load_dword v118, v118, s[52:55], s60 offen\n\t"
"s_mov_b64 exec, -1\n\t"
"buffer_load_dword v187, off, s[8:11], s70 sc1\n\t"
"v_add_u32 v107, 0, v204\n\t"
"v_mul_u32_u24 v104, 0xab, v107\n\t"
"v_lshrrev_b32 v104, 10, v104\n\t"
"v_mul_u32_u24 v104, 0x120, v104\n\t"
"v_lshl_add_u32 v104, v107, 4, v104\n\t"
"v_add_u32 v107, 64, v204\n\t"
"v_mul_u32_u24 v105, 0xab, v107\n\t"
"v_lshrrev_b32 v105, 10, v105\n\t"
"v_mul_u32_u24 v105, 0x120, v105\n\t"
"v_lshl_add_u32 v105, v107, 4, v105\n\t"
"v_add_u32 v107, 128, v204\n\t"
"v_mul_u32_u24 v106, 0xab, v107\n\t"
"v_lshrrev_b32 v106, 10, v106\n\t"
"v_mul_u32_u24 v106, 0x120, v106\n\t"
"v_lshl_add_u32 v106, v107, 4, v106\n\t"
"s_mul_i32 s74, s73, 20736\n\t"
"s_mul_i32 s72, s73, 96\n\t"
"s_add_u32 s74, s74, 33280\n\t"
"s_add_u32 s75, s74, 12288\n\t"
"s_cmp_eq_u32 s73, 3\n\t"
"s_cselect_b32 s74, 16544, s74\n\t"
"s_cselect_b32 s75, 95488, s75\n\t"
"s_lshr_b32 s60, s66, 5\n\t"
"s_bfe_u32 s62, s66, 0x20003\n\t"
"s_and_b32 s61, s60, 1\n\t"
"s_lshl1_add_u32 s61, s73, s61\n\t"
"s_mul_i32 s62, s62, 288\n\t"
"s_add_u32 s61, s61, s62\n\t"
"s_lshr_b32 s60, s60, 1\n\t"
"s_mul_i32 s62, s60, 96\n\t"
"s_add_u32 s61, s61, s62\n\t"
"s_lshl_b32 s57, s61, 10\n\t"
"s_and_b32 s61, s66, 7\n\t"
"s_lshl_b32 s61, s61, 7\n\t"
"s_add_u32 s57, s57, s61\n\t"
"s_cmp_lt_u32 s73, 3\n\t"
"s_cselect_b32 s67, s66, 0xff\n\t"
"s_cmp_lt_u32 s67, 192\n\t"
"s_cselect_b32 s57, s57, 2.0\n\t"
"s_lshl_b32 s60, s73, 3\n\t"
"s_add_u32 s60, s60, 0\n\t"
"s_lshl_b32 s61, s73, 11\n\t"
"s_add_u32 s61, s61, 4096\n\t"
"v_lshl_add_u32 v46, v121, 6, s60\n\t"
"v_lshl_add_u32 v46, v44, 1, v46\n\t"
"v_lshl_add_u32 v42, v204, 1, s61\n\t"
"v_lshl_add_u32 v43, v204, 4, s61\n\t"
"v_mul_u32_u24 v44, 6144, v120\n\t"
"v_lshl_add_u32 v44, v121, 4, v44\n\t"
"v_mov_b32 v32, 0\n\t"
"v_mov_b32 v33, 0\n\t"
"v_mov_b32 v34, 0\n\t"
"v_mov_b32 v35, 0\n\t"
"v_mov_b32 v36, 0\n\t"
"v_mov_b32 v37, 0\n\t"
"v_mov_b32 v38, 0\n\t"
"v_mov_b32 v39, 0\n\t"
"s_waitcnt vmcnt(9)\n\t"
"s_barrier\n\t"
".p2align 3\n\t"
"ds_read_u16_d16_hi v32, v46 offset:0\n\t"
"ds_read_u16_d16_hi v33, v46 offset:512\n\t"
"ds_read_u16_d16_hi v34, v46 offset:1024\n\t"
"ds_read_u16_d16_hi v35, v46 offset:1536\n\t"
"ds_read_u16_d16_hi v36, v46 offset:2048\n\t"
"ds_read_u16_d16_hi v37, v46 offset:2560\n\t"
"ds_read_u16_d16_hi v38, v46 offset:3072\n\t"
"ds_read_u16_d16_hi v39, v46 offset:3584\n\t"
"s_waitcnt lgkmcnt(1)\n\t"
"v_add_f32 v48, v32, v38\n\t"
"s_waitcnt lgkmcnt(0)\n\t"
"v_add_f32 v49, v33, v39\n\t"
"v_add_f32 v50, v34, v40\n\t"
"v_add_f32 v51, v48, v35\n\t"
"v_sub_f32 v54, v48, v35\n\t"
"v_add_f32 v52, v49, v36\n\t"
"v_sub_f32 v55, v49, v36\n\t"
"v_add_f32 v53, v50, v37\n\t"
"v_sub_f32 v56, v50, v37\n\t"
"v_add_f32 v57, v32, v34\n\t"
"v_add_f32 v58, v51, v53\n\t"
"v_add_f32 v59, v54, v56\n\t"
"v_add_f32 v60, v38, v40\n\t"
"v_add_f32 v62, v57, v33\n\t"
"v_sub_f32 v63, v57, v33\n\t"
"v_add_f32 v64, v58, v52\n\t"
"v_sub_f32 v65, v58, v52\n\t"
"v_add_f32 v66, v59, v55\n\t"
"v_sub_f32 v67, v59, v55\n\t"
"v_add_f32 v68, v60, v39\n\t"
"v_sub_f32 v69, v60, v39\n\t"
"v_mul_f32 v71, 0.5, v62\n\t"
"v_mul_f32 v72, 0.5, v63\n\t"
"v_mul_f32 v74, 0.5, v51\n\t"
"v_mul_f32 v75, 0x3e800000, v64\n\t"
"v_mul_f32 v76, 0x3e800000, v65\n\t"
"v_mul_f32 v77, -0.5, v53\n\t"
"v_mul_f32 v79, 0.5, v66\n\t"
"v_mul_f32 v80, 0.5, v67\n\t"
"v_mul_f32 v83, -0.5, v68\n\t"
"v_mul_f32 v84, -0.5, v69\n\t"
".p2align 3\n\t"
"v_cvt_pk_bf16_f32 v86, v32, v71\n\t"
"v_cvt_pk_bf16_f32 v87, v72, -v34\n\t"
"v_cvt_pk_bf16_f32 v88, v74, v75\n\t"
"v_cvt_pk_bf16_f32 v89, v76, v77\n\t"
"v_cvt_pk_bf16_f32 v90, v54, v79\n\t"
"v_cvt_pk_bf16_f32 v91, v80, -v56\n\t"
"v_cvt_pk_bf16_f32 v92, -v38, v83\n\t"
"v_cvt_pk_bf16_f32 v93, v84, v40\n\t"
"ds_write_b16 v42, v86 offset:0\n\t"
"ds_write_b16_d16_hi v42, v86 offset:128\n\t"
"ds_write_b16 v42, v87 offset:256\n\t"
"ds_write_b16_d16_hi v42, v87 offset:384\n\t"
"ds_write_b16 v42, v88 offset:512\n\t"
"ds_write_b16_d16_hi v42, v88 offset:640\n\t"
"ds_write_b16 v42, v89 offset:768\n\t"
"ds_write_b16_d16_hi v42, v89 offset:896\n\t"
"ds_write_b16 v42, v90 offset:1024\n\t"
"ds_write_b16_d16_hi v42, v90 offset:1152\n\t"
"ds_write_b16 v42, v91 offset:1280\n\t"
"ds_write_b16_d16_hi v42, v91 offset:1408\n\t"
"ds_write_b16 v42, v92 offset:1536\n\t"
"ds_write_b16_d16_hi v42, v92 offset:1664\n\t"
"ds_write_b16 v42, v93 offset:1792\n\t"
"ds_write_b16_d16_hi v42, v93 offset:1920\n\t"
"ds_read_b128 v[96:99], v43 offset:0\n\t"
"ds_read_b128 v[100:103], v43 offset:1024\n\t"
"s_add_u32 s60, s57, 0\n\t"
"s_waitcnt lgkmcnt(1)\n\t"
"buffer_store_dwordx4 v[96:99], v44, s[8:11], s60 offen sc1\n\t"
"s_add_u32 s60, s57, 49152\n\t"
"s_waitcnt lgkmcnt(0)\n\t"
"buffer_store_dwordx4 v[100:103], v44, s[8:11], s60 offen sc1\n\t"
"v_lshrrev_b32 v123, 4, v204\n\t"
"v_bfe_u32 v124, v204, 2, 2\n\t"
"v_and_b32 v125, 3, v204\n\t"
"v_lshlrev_b32 v125, 3, v125\n\t"
"v_and_b32 v138, 49, v204\n\t"
"v_lshl_or_b32 v138, v124, 1, v138\n\t"
"v_and_b32 v139, 2, v204\n\t"
"v_lshl_or_b32 v138, v139, 2, v138\n\t"
"s_add_u32 s63, s26, 1152\n\t"
"v_and_b32 v126, 48, v204\n\t"
"v_and_b32 v127, 6, v204\n\t"
"v_lshl_add_u32 v126, v127, 5, v126\n\t"
"v_and_b32 v127, 8, v204\n\t"
"s_add_u32 m0, s74, 0\n\t"
"s_add_u32 s27, s72, 0\n\t"
"buffer_load_dwordx4 v104, s[52:55], s27 offen lds\n\t"
"v_mul_u32_u24 v134, 516, v127\n\t"
"v_and_b32 v127, 1, v204\n\t"
"v_mul_u32_u24 v127, 8320, v127\n\t"
"v_add3_u32 v224, v134, v126, v127\n\t"
"v_and_b32 v127, 16, v204\n\t"
"v_lshl_add_u32 v127, v127, 1, v125\n\t"
"v_lshl_add_u32 v127, v123, 8, v127\n\t"
"v_add_u32 v126, 4, v204\n\t"
"v_and_b32 v126, 8, v126\n\t"
"v_lshl_add_u32 v127, v126, 3, v127\n\t"
"v_mul_u32_u24 v126, 1152, v124\n\t"
"v_add3_u32 v226, v126, v127, s63\n\t"
"s_add_u32 m0, s74, 1024\n\t"
"s_add_u32 s27, s72, 0\n\t"
"buffer_load_dwordx4 v105, s[52:55], s27 offen lds\n\t"
"v_min_u32 v126, 2, v124\n\t"
"v_cmp_gt_u32 vcc, 48, v204\n\t"
"s_mov_b32 s48, 384\n\t"
"s_cmp_eq_u32 s65, 31\n\t"
"v_cndmask_b32 v126, v126, v123, vcc\n\t"
"v_cndmask_b32 v127, 4, v124, vcc\n\t"
"v_and_b32 v134, 1, v126\n\t"
"v_lshl_add_u32 v134, v134, 6, v125\n\t"
"v_lshl_add_u32 v134, v127, 7, v134\n\t"
"v_and_b32 v127, 2, v127\n\t"
"v_lshl_add_u32 v134, v127, 4, v134\n\t"
"v_mul_u32_u24 v126, 2304, v126\n\t"
"v_add3_u32 v227, v126, v134, s26\n\t"
"v_lshlrev_b32 v126, 14, v204\n\t"
"v_and_b32 v126, 0x8000, v126\n\t"
"v_or_b32 v134, 0x3f803f80, v126\n\t"
"v_and_b32 v126, 1, v204\n\t"
"v_cmp_eq_u32 vcc, 0, v126\n\t"
"s_cselect_b64 s[40:41], 0xff, 0\n\t"
"s_cselect_b64 s[46:47], -1, 0\n\t"
"s_add_u32 m0, s74, 2048\n\t"
"s_add_u32 s27, s72, 0\n\t"
"buffer_load_dwordx4 v106, s[52:55], s27 offen lds\n\t"
"v_cndmask_b32 v135, v134, 0, vcc\n\t"
"v_cndmask_b32 v134, 0, v134, vcc\n\t"
"v_xor_b32 v136, 0x80008000, v134\n\t"
"v_xor_b32 v137, 0x80008000, v135\n\t"
"s_add_u32 m0, s74, 3072\n\t"
"s_add_u32 s27, s72, 12288\n\t"
"buffer_load_dwordx4 v104, s[52:55], s27 offen lds\n\t"
"v_accvgpr_write_b32 a220, v134\n\t"
"v_accvgpr_write_b32 a221, v135\n\t"
"v_accvgpr_write_b32 a222, v136\n\t"
"v_accvgpr_write_b32 a223, v137\n\t"
"v_xad_u32 v236, v128, 64, s48\n\t"
"v_xad_u32 v237, v129, 64, s48\n\t"
"v_cndmask_b32 v237, v237, 2.0, s[40:41]\n\t"
"s_lshl_b64 s[40:41], s[40:41], 8\n\t"
"v_xad_u32 v238, v130, 64, s48\n\t"
"v_cndmask_b32 v238, v238, 2.0, s[40:41]\n\t"
"s_lshl_b64 s[40:41], s[40:41], 8\n\t"
"v_xad_u32 v239, v131, 64, s48\n\t"
"v_cndmask_b32 v239, v239, 2.0, s[40:41]\n\t"
"s_add_u32 m0, s74, 4096\n\t"
"s_add_u32 s27, s72, 12288\n\t"
"buffer_load_dwordx4 v105, s[52:55], s27 offen lds\n\t"
"s_lshl_b64 s[40:41], s[40:41], 40\n\t"
"v_xad_u32 v240, v245, 64, s48\n\t"
"v_cndmask_b32 v240, v240, 2.0, s[40:41]\n\t"
"v_cmp_eq_u32 vcc, 8, v119\n\t"
"v_xad_u32 v247, v133, 64, s48\n\t"
"s_and_b64 vcc, vcc, s[46:47]\n\t"
"v_cndmask_b32 v247, v247, 2.0, s[50:51]\n\t"
"v_cndmask_b32 v247, v247, 2.0, vcc\n\t"
"v_and_b32 v139, 7, v138\n\t"
"v_bfe_u32 v126, v138, 3, 1\n\t"
"v_lshrrev_b32 v127, 4, v138\n\t"
"v_sub_u32 v127, v127, v126\n\t"
"s_add_u32 m0, s74, 5120\n\t"
"s_add_u32 s27, s72, 12288\n\t"
"buffer_load_dwordx4 v106, s[52:55], s27 offen lds\n\t"
"v_mov_b32 v134, 0xbf803f80\n\t"
"v_mov_b32 v135, 0x3f80bf80\n\t"
"v_cmp_eq_u32 vcc, 0, v127\n\t"
"v_add_u32 v126, 4, v126\n\t"
"s_nop 0\n\t"
"v_cndmask_b32 v134, 0, v134, vcc\n\t"
"v_cmp_eq_u32 vcc, 1, v127\n\t"
"s_nop 1\n\t"
"v_cndmask_b32 v134, v134, v135, vcc\n\t"
"v_cmp_eq_u32 vcc, 7, v139\n\t"
"s_nop 1\n\t"
"v_cndmask_b32 v135, 0, v135, vcc\n\t"
"v_cmp_gt_u32 vcc, 48, v138\n\t"
"s_nop 1\n\t"
"v_cndmask_b32 v134, v135, v134, vcc\n\t"
"v_mov_b32 v135, 0\n\t"
"s_add_u32 m0, s74, 6144\n\t"
"s_add_u32 s27, s72, 442368\n\t"
"buffer_load_dwordx4 v104, s[52:55], s27 offen lds\n\t"
"v_cndmask_b32 v136, v126, v139, vcc\n\t"
"v_and_b32 v137, 1, v136\n\t"
"v_lshlrev_b32 v137, 4, v137\n\t"
"v_lshrrev_b32 v136, 1, v136\n\t"
"v_lshlrev_b64 v[134:135], v137, v[134:135]\n\t"
"v_cmp_eq_u32 vcc, 0, v136\n\t"
"s_nop 1\n\t"
"v_cndmask_b32 v140, 0, v134, vcc\n\t"
"v_accvgpr_write_b32 a224, v140\n\t"
"v_cndmask_b32 v141, 0, v135, vcc\n\t"
"v_cmp_eq_u32 vcc, 1, v136\n\t"
"v_and_b32 v140, 0xff7fff7f, v140\n\t"
"v_xor_b32 v148, 0x80008000, v140\n\t"
"s_add_u32 m0, s74, 7168\n\t"
"s_add_u32 s27, s72, 442368\n\t"
"buffer_load_dwordx4 v105, s[52:55], s27 offen lds\n\t"
"v_cndmask_b32 v141, v141, v134, vcc\n\t"
"v_cndmask_b32 v142, 0, v135, vcc\n\t"
"v_cmp_eq_u32 vcc, 2, v136\n\t"
"v_accvgpr_write_b32 a225, v141\n\t"
"v_accvgpr_write_b32 a216, v140\n\t"
"v_cndmask_b32 v142, v142, v134, vcc\n\t"
"v_cndmask_b32 v143, 0, v135, vcc\n\t"
"v_cmp_eq_u32 vcc, 3, v136\n\t"
"v_accvgpr_write_b32 a226, v142\n\t"
"v_and_b32 v141, 0xff7fff7f, v141\n\t"
"v_cndmask_b32 v143, v143, v134, vcc\n\t"
"v_accvgpr_write_b32 a227, v143\n\t"
"v_xor_b32 v149, 0x80008000, v141\n\t"
"s_add_u32 m0, s74, 8192\n\t"
"s_add_u32 s27, s72, 442368\n\t"
"buffer_load_dwordx4 v106, s[52:55], s27 offen lds\n\t"
"v_accvgpr_write_b32 a217, v141\n\t"
"v_and_b32 v142, 0xff7fff7f, v142\n\t"
"v_xor_b32 v150, 0x80008000, v142\n\t"
"v_accvgpr_write_b32 a218, v142\n\t"
"v_and_b32 v143, 0xff7fff7f, v143\n\t"
"v_xor_b32 v151, 0x80008000, v143\n\t"
"v_accvgpr_write_b32 a219, v143\n\t"
"s_mov_b32 s68, 3000\n\t"
"v_and_b32 v107, 51, v204\n\t"
"v_mul_u32_u24 v107, 24, v107\n\t"
"v_and_b32 v47, 8, v204\n\t"
"v_mad_u32_u24 v107, v47, 12, v107\n\t"
"s_add_u32 m0, s74, 9216\n\t"
"s_add_u32 s27, s72, 454656\n\t"
"buffer_load_dwordx4 v104, s[52:55], s27 offen lds\n\t"
"v_and_b32 v47, 4, v204\n\t"
"v_mul_u32_u24 v47, 0x180, v47\n\t"
"v_add_u32 v107, v47, v107\n\t"
"v_add_u32 v47, s75, v107\n\t"
"v_add_u32 v107, s74, v107\n\t"
"v_lshrrev_b32 v112, 4, v204\n\t"
"v_bfe_u32 v113, v204, 2, 2\n\t"
"v_cmp_eq_u32 vcc, v112, v113\n\t"
"v_lshlrev_b32 v112, 4, v204\n\t"
"v_mov_b32 v113, 0x3e80\n\t"
"v_lshlrev_b32 v113, v112, v113\n\t"
"v_cndmask_b32 v113, 0, v113, vcc\n\t"
"s_add_u32 m0, s74, 10240\n\t"
"s_add_u32 s27, s72, 454656\n\t"
"buffer_load_dwordx4 v105, s[52:55], s27 offen lds\n\t"
"v_and_b32 v112, 2, v204\n\t"
"v_cmp_eq_u32 vcc, 0, v112\n\t"
"v_mov_b32 v108, 0\n\t"
"v_mov_b32 v109, 0\n\t"
"v_cndmask_b32 v110, 0, v113, vcc\n\t"
"v_cndmask_b32 v111, v113, 0, vcc\n\t"
"v_xor_b32 v114, 0x80008000, v110\n\t"
"v_xor_b32 v115, 0x80008000, v111\n\t"
"v_mov_b32 v112, 0\n\t"
"v_mov_b32 v113, 0\n\t"
"v_mov_b32 v116, 0\n\t"
"v_mov_b32 v117, 0\n\t"
"s_add_u32 m0, s74, 11264\n\t"
"s_add_u32 s27, s72, 454656\n\t"
"buffer_load_dwordx4 v106, s[52:55], s27 offen lds\n\t"
"s_waitcnt vmcnt(16)\n\t"
"s_waitcnt vmcnt(12)\n\t"
"v_readfirstlane_b32 s71, v187\n\t"
"s_barrier\n\t"
"s_add_u32 s71, s71, 1\n\t"
"v_mov_b32 v215, s71\n\t"
"s_mov_b64 exec, 1\n\t"
"buffer_store_dword v215, off, s[8:11], s70 sc1\n\t"
"s_mov_b64 exec, -1\n\t"
"v_add_u32 v248, 101632, v226\n\t"
"v_xor_b32 v249, 32, v248\n\t"
"v_xor_b32 v250, 64, v248\n\t"
"v_xor_b32 v251, 96, v248\n\t"
"ds_read_b64_tr_b16 a[178:179], v248 offset:128\n\t"
"ds_read_b64_tr_b16 a[186:187], v249 offset:128\n\t"
"s_add_u32 m0, s75, 0\n\t"
"s_add_u32 s27, s72, 221184\n\t"
"buffer_load_dwordx4 v104, s[52:55], s27 offen lds\n\t"
"ds_read_b64_tr_b16 a[194:195], v250 offset:128\n\t"
"ds_read_b64_tr_b16 a[202:203], v251 offset:128\n\t"
"ds_read_b64_tr_b16 a[176:177], v248 offset:0\n\t"
"s_add_u32 m0, s75, 1024\n\t"
"s_add_u32 s27, s72, 221184\n\t"
"buffer_load_dwordx4 v105, s[52:55], s27 offen lds\n\t"
"ds_read_b64_tr_b16 a[184:185], v249 offset:0\n\t"
"ds_read_b64_tr_b16 a[180:181], v249 offset:256\n\t"
"ds_read_b64_tr_b16 a[188:189], v248 offset:256\n\t"
"s_add_u32 m0, s75, 2048\n\t"
"s_add_u32 s27, s72, 221184\n\t"
"buffer_load_dwordx4 v106, s[52:55], s27 offen lds\n\t"
"ds_read_b64_tr_b16 a[192:193], v250 offset:0\n\t"
"ds_read_b64_tr_b16 a[200:201], v251 offset:0\n\t"
"ds_read_b64_tr_b16 a[196:197], v251 offset:256\n\t"
"s_add_u32 m0, s75, 3072\n\t"
"s_add_u32 s27, s72, 233472\n\t"
"buffer_load_dwordx4 v104, s[52:55], s27 offen lds\n\t"
"ds_read_b64_tr_b16 a[204:205], v250 offset:256\n\t"
"s_waitcnt lgkmcnt(11)\n\t"
"v_mfma_f32_4x4x4_16b_bf16 v[8:11], a[220:221], a[178:179], 0\n\t"
"s_waitcnt lgkmcnt(10)\n\t"
"v_mfma_f32_4x4x4_16b_bf16 v[12:15], a[220:221], a[186:187], 0\n\t"
"s_waitcnt lgkmcnt(9)\n\t"
"v_mfma_f32_4x4x4_16b_bf16 v[24:27], a[220:221], a[194:195], 0\n\t"
"s_waitcnt lgkmcnt(8)\n\t"
"v_mfma_f32_4x4x4_16b_bf16 v[28:31], a[220:221], a[202:203], 0\n\t"
"s_waitcnt lgkmcnt(7)\n\t"
"v_mfma_f32_4x4x4_16b_bf16 v[0:3], a[222:223], a[176:177], v[8:11]\n\t"
"s_waitcnt lgkmcnt(6)\n\t"
"v_mfma_f32_4x4x4_16b_bf16 v[4:7], a[222:223], a[184:185], v[12:15]\n\t"
"s_add_u32 m0, s75, 4096\n\t"
"s_add_u32 s27, s72, 233472\n\t"
"buffer_load_dwordx4 v105, s[52:55], s27 offen lds\n\t"
"s_add_u32 m0, s75, 5120\n\t"
"s_add_u32 s27, s72, 233472\n\t"
"buffer_load_dwordx4 v106, s[52:55], s27 offen lds\n\t"
"s_waitcnt lgkmcnt(5)\n\t"
"v_mfma_f32_4x4x4_16b_bf16 v[8:11], a[222:223], a[180:181], v[8:11]\n\t"
"s_waitcnt lgkmcnt(4)\n\t"
"v_mfma_f32_4x4x4_16b_bf16 v[12:15], a[222:223], a[188:189], v[12:15]\n\t"
"s_waitcnt lgkmcnt(3)\n\t"
"v_mfma_f32_4x4x4_16b_bf16 v[16:19], a[222:223], a[192:193], v[24:27]\n\t"
"s_waitcnt lgkmcnt(2)\n\t"
"v_mfma_f32_4x4x4_16b_bf16 v[20:23], a[222:223], a[200:201], v[28:31]\n\t"
"s_waitcnt lgkmcnt(1)\n\t"
"v_mfma_f32_4x4x4_16b_bf16 v[24:27], a[222:223], a[196:197], v[24:27]\n\t"
"s_waitcnt lgkmcnt(0)\n\t"
"v_mfma_f32_4x4x4_16b_bf16 v[28:31], a[222:223], a[204:205], v[28:31]\n\t"
"s_add_u32 m0, s26, 124544\n\t"
"s_nop 0\n\t"
"buffer_load_dwordx4 v241, s[12:15], s22 offen offset:128 nt lds\n\t"
"buffer_load_dwordx4 v242, s[12:15], s22 offen offset:1152 nt lds\n\t"
"v_cvt_pk_bf16_f32 v0, -v0, -v4\n\t"
"v_cvt_pk_bf16_f32 v1, -v1, -v5\n\t"
"v_cvt_pk_bf16_f32 v2, -v2, -v6\n\t"
"v_cvt_pk_bf16_f32 v3, -v3, -v7\n\t"
"s_lshl_b32 m0, s73, 8\n\t"
"s_nop 0\n\t"
".p2align 3\n\t"
"ds_write_addtid_b32 v0 offset:0\n\t"
"ds_write_addtid_b32 v1 offset:4128\n\t"
"ds_write_addtid_b32 v2 offset:1024\n\t"
"ds_write_addtid_b32 v3 offset:5152\n\t"
"v_cvt_pk_bf16_f32 v8, v8, v12\n\t"
"v_cvt_pk_bf16_f32 v9, v9, v13\n\t"
"v_cvt_pk_bf16_f32 v10, v10, v14\n\t"
"v_cvt_pk_bf16_f32 v11, v11, v15\n\t"
"ds_write_addtid_b32 v8 offset:8320\n\t"
"ds_write_addtid_b32 v9 offset:12448\n\t"
"ds_write_addtid_b32 v10 offset:9344\n\t"
"ds_write_addtid_b32 v11 offset:13472\n\t"
"v_cvt_pk_bf16_f32 v16, -v16, -v20\n\t"
"v_cvt_pk_bf16_f32 v17, -v17, -v21\n\t"
"v_cvt_pk_bf16_f32 v18, -v18, -v22\n\t"
"v_cvt_pk_bf16_f32 v19, -v19, -v23\n\t"
"ds_write_addtid_b32 v16 offset:2048\n\t"
"ds_write_addtid_b32 v17 offset:6176\n\t"
"ds_write_addtid_b32 v18 offset:3072\n\t"
"ds_write_addtid_b32 v19 offset:7200\n\t"
"v_cvt_pk_bf16_f32 v24, v24, v28\n\t"
"v_cvt_pk_bf16_f32 v25, v25, v29\n\t"
"v_cvt_pk_bf16_f32 v26, v26, v30\n\t"
"v_cvt_pk_bf16_f32 v27, v27, v31\n\t"
"ds_write_addtid_b32 v24 offset:10368\n\t"
"ds_write_addtid_b32 v25 offset:14496\n\t"
"ds_write_addtid_b32 v26 offset:11392\n\t"
"ds_write_addtid_b32 v27 offset:15520\n\t"
"v_add_u32 v248, 101632, v227\n\t"
"v_xor_b32 v249, 32, v248\n\t"
"v_xor_b32 v250, 64, v248\n\t"
"v_xor_b32 v251, 96, v248\n\t"
"ds_read_b64_tr_b16 a[176:177], v248 offset:0\n\t"
"ds_read_b64_tr_b16 a[178:179], v248 offset:512\n\t"
"ds_read_b64_tr_b16 a[180:181], v249 offset:0\n\t"
"ds_read_b64_tr_b16 a[182:183], v249 offset:512\n\t"
"s_add_u32 m0, s26, 124544\n\t"
"s_nop 0\n\t"
".p2align 3\n\t"
"buffer_load_dwordx4 v243, s[12:15], s22 offen offset:2176 nt lds\n\t"
"ds_read_b64_tr_b16 a[184:185], v250 offset:0\n\t"
"ds_read_b64_tr_b16 a[186:187], v250 offset:512\n\t"
"ds_read_b64_tr_b16 a[188:189], v251 offset:0\n\t"
"ds_read_b64_tr_b16 a[190:191], v251 offset:512\n\t"
"buffer_load_dwordx4 v244, s[12:15], s22 offen offset:3200 nt lds\n\t"
"ds_read_b64_tr_b16 a[192:193], v248 offset:1152\n\t"
"ds_read_b64_tr_b16 a[194:195], v248 offset:1664\n\t"
"ds_read_b64_tr_b16 a[196:197], v249 offset:1152\n\t"
"ds_read_b64_tr_b16 a[198:199], v249 offset:1664\n\t"
"s_mov_b64 exec, s[36:37]\n\t"
"buffer_load_dwordx4 v245, s[12:15], s22 offen offset:3712 nt lds\n\t"
"s_mov_b64 exec, -1\n\t"
"ds_read_b64_tr_b16 a[200:201], v250 offset:1152\n\t"
"ds_read_b64_tr_b16 a[202:203], v250 offset:1664\n\t"
"ds_read_b64_tr_b16 a[204:205], v251 offset:1152\n\t"
"ds_read_b64_tr_b16 a[206:207], v251 offset:1664\n\t"
"s_add_u32 m0, s43, 122240\n\t"
"s_mov_b64 exec, s[44:45]\n\t"
"buffer_load_dwordx4 v246, s[12:15], 0 offen offset:128 nt lds\n\t"
"s_mov_b64 exec, -1\n\t"
"s_mov_b64 exec, 0\n\t"
"s_cmp_eq_u32 s73, 0\n\t"
"s_cbranch_scc1 Lpad3_%=\n\t"
"s_cmp_eq_u32 s73, 1\n\t"
"s_cbranch_scc1 Lpad2_%=\n\t"
"s_cmp_eq_u32 s73, 2\n\t"
"s_cbranch_scc1 Lpad1_%=\n\t"
"s_branch Lpad0_%=\n\t"
".fill 7, 4, 0xbf800000\n\t"
"Lret0_%=:\n\t"
"s_mov_b64 exec, -1\n\t"
"s_waitcnt vmcnt(19)\n\t"
".p2align 3\n\t"
"ds_read_b64_tr_b16 a[48:49], v107 offset:0\n\t"
"ds_read_b64_tr_b16 a[50:51], v107 offset:192\n\t"
"ds_read_b64_tr_b16 a[52:53], v107 offset:3072\n\t"
"ds_read_b64_tr_b16 a[54:55], v107 offset:3264\n\t"
"ds_read_b64_tr_b16 a[56:57], v107 offset:8\n\t"
"ds_read_b64_tr_b16 a[58:59], v107 offset:200\n\t"
"ds_read_b64_tr_b16 a[60:61], v107 offset:3080\n\t"
"ds_read_b64_tr_b16 a[62:63], v107 offset:3272\n\t"
"ds_read_b64_tr_b16 a[64:65], v107 offset:16\n\t"
"ds_read_b64_tr_b16 a[66:67], v107 offset:208\n\t"
"ds_read_b64_tr_b16 a[68:69], v107 offset:3088\n\t"
"ds_read_b64_tr_b16 a[70:71], v107 offset:3280\n\t"
"s_waitcnt vmcnt(13)\n\t"
".p2align 3\n\t"
"ds_read_b64_tr_b16 a[72:73], v107 offset:6144\n\t"
"ds_read_b64_tr_b16 a[74:75], v107 offset:6336\n\t"
"ds_read_b64_tr_b16 a[76:77], v107 offset:9216\n\t"
"ds_read_b64_tr_b16 a[78:79], v107 offset:9408\n\t"
"ds_read_b64_tr_b16 a[80:81], v107 offset:6152\n\t"
"ds_read_b64_tr_b16 a[82:83], v107 offset:6344\n\t"
"ds_read_b64_tr_b16 a[84:85], v107 offset:9224\n\t"
"ds_read_b64_tr_b16 a[86:87], v107 offset:9416\n\t"
"ds_read_b64_tr_b16 a[88:89], v107 offset:6160\n\t"
"ds_read_b64_tr_b16 a[90:91], v107 offset:6352\n\t"
"ds_read_b64_tr_b16 a[92:93], v107 offset:9232\n\t"
"ds_read_b64_tr_b16 a[94:95], v107 offset:9424\n\t"
"s_waitcnt lgkmcnt(15)\n\t"
"v_mfma_f32_16x16x32_bf16 v[0:3], v[110:113], a[48:51], 0\n\t"
"s_waitcnt lgkmcnt(10)\n\t"
"v_mfma_f32_16x16x32_bf16 v[0:3], v[110:113], a[72:75], v[0:3]\n\t"
"s_nop 0\n\t"
"v_mfma_f32_16x16x32_bf16 v[8:11], v[108:111], a[48:51], 0\n\t"
"s_nop 0\n\t"
"v_mfma_f32_16x16x32_bf16 v[8:11], v[108:111], a[72:75], v[8:11]\n\t"
"s_add_u32 m0, s26, 145152\n\t"
"s_nop 0\n\t"
"buffer_load_dwordx4 v241, s[12:15], s22 offen offset:256 nt lds\n\t"
"v_mfma_f32_16x16x32_bf16 v[16:19], v[110:113], a[52:55], 0\n\t"
"s_waitcnt lgkmcnt(8)\n\t"
"v_mfma_f32_16x16x32_bf16 v[16:19], v[110:113], a[76:79], v[16:19]\n\t"
"s_nop 0\n\t"
"v_mfma_f32_16x16x32_bf16 v[24:27], v[108:111], a[52:55], 0\n\t"
"s_nop 0\n\t"
"v_mfma_f32_16x16x32_bf16 v[24:27], v[108:111], a[76:79], v[24:27]\n\t"
"buffer_load_dwordx4 v242, s[12:15], s22 offen offset:1280 nt lds\n\t"
"s_waitcnt vmcnt(8)\n\t"
".p2align 3\n\t"
"ds_read_b64_tr_b16 a[0:1], v47 offset:0\n\t"
"ds_read_b64_tr_b16 a[2:3], v47 offset:192\n\t"
"ds_read_b64_tr_b16 a[4:5], v47 offset:3072\n\t"
"ds_read_b64_tr_b16 a[6:7], v47 offset:3264\n\t"
"ds_read_b64_tr_b16 a[8:9], v47 offset:8\n\t"
"ds_read_b64_tr_b16 a[10:11], v47 offset:200\n\t"
"ds_read_b64_tr_b16 a[12:13], v47 offset:3080\n\t"
"ds_read_b64_tr_b16 a[14:15], v47 offset:3272\n\t"
"ds_read_b64_tr_b16 a[16:17], v47 offset:16\n\t"
"ds_read_b64_tr_b16 a[18:19], v47 offset:208\n\t"
"ds_read_b64_tr_b16 a[20:21], v47 offset:3088\n\t"
"ds_read_b64_tr_b16 a[22:23], v47 offset:3280\n\t"
"v_mfma_f32_16x16x32_bf16 v[32:35], v[110:113], a[56:59], 0\n\t"
"s_waitcnt lgkmcnt(15)\n\t"
"v_mfma_f32_16x16x32_bf16 v[32:35], v[110:113], a[80:83], v[32:35]\n\t"
"s_nop 0\n\t"
"v_mfma_f32_16x16x32_bf16 v[96:99], v[108:111], a[56:59], 0\n\t"
"s_nop 0\n\t"
"v_mfma_f32_16x16x32_bf16 v[96:99], v[108:111], a[80:83], v[96:99]\n\t"
"buffer_load_dwordx4 v243, s[12:15], s22 offen offset:2304 nt lds\n\t"
"v_mfma_f32_16x16x32_bf16 v[120:123], v[110:113], a[60:63], 0\n\t"
"s_nop 0\n\t"
"v_mfma_f32_16x16x32_bf16 v[120:123], v[110:113], a[84:87], v[120:123]\n\t"
"s_nop 0\n\t"
"v_mfma_f32_16x16x32_bf16 v[128:131], v[108:111], a[60:63], 0\n\t"
"s_nop 0\n\t"
"v_mfma_f32_16x16x32_bf16 v[128:131], v[108:111], a[84:87], v[128:131]\n\t"
"buffer_load_dwordx4 v244, s[12:15], s22 offen offset:3328 nt lds\n\t"
"v_mfma_f32_16x16x32_bf16 v[136:139], v[110:113], a[64:67], 0\n\t"
"s_waitcnt lgkmcnt(14)\n\t"
"v_mfma_f32_16x16x32_bf16 v[136:139], v[110:113], a[88:91], v[136:139]\n\t"
"s_nop 0\n\t"
"v_mfma_f32_16x16x32_bf16 v[152:155], v[108:111], a[64:67], 0\n\t"
"s_nop 0\n\t"
"v_mfma_f32_16x16x32_bf16 v[152:155], v[108:111], a[88:91], v[152:155]\n\t"
"s_mov_b64 exec, s[36:37]\n\t"
"buffer_load_dwordx4 v245, s[12:15], s22 offen offset:3840 nt lds\n\t"
"s_mov_b64 exec, -1\n\t"
"v_mfma_f32_16x16x32_bf16 v[160:163], v[110:113], a[68:71], 0\n\t"
"s_waitcnt lgkmcnt(12)\n\t"
"v_mfma_f32_16x16x32_bf16 v[160:163], v[110:113], a[92:95], v[160:163]\n\t"
"s_nop 0\n\t"
"v_mfma_f32_16x16x32_bf16 v[168:171], v[108:111], a[68:71], 0\n\t"
"s_nop 0\n\t"
"v_mfma_f32_16x16x32_bf16 v[168:171], v[108:111], a[92:95], v[168:171]\n\t"
"s_add_u32 m0, s43, 142848\n\t"
"s_mov_b64 exec, s[44:45]\n\t"
"buffer_load_dwordx4 v246, s[12:15], 0 offen offset:256 nt lds\n\t"
"s_mov_b64 exec, -1\n\t"
"s_waitcnt lgkmcnt(10)\n\t"
"v_mfma_f32_16x16x32_bf16 v[4:7], v[110:113], a[0:3], v[0:3]\n\t"
"s_nop 0\n\t"
"v_mfma_f32_16x16x32_bf16 v[0:3], v[114:117], a[0:3], v[0:3]\n\t"
"s_nop 0\n\t"
"v_mfma_f32_16x16x32_bf16 v[12:15], v[108:111], a[0:3], v[8:11]\n\t"
"s_nop 0\n\t"
"v_mfma_f32_16x16x32_bf16 v[8:11], v[112:115], a[0:3], v[8:11]\n\t"
"s_waitcnt lgkmcnt(8)\n\t"
"v_mfma_f32_16x16x32_bf16 v[20:23], v[110:113], a[4:7], v[16:19]\n\t"
"s_nop 0\n\t"
"v_mfma_f32_16x16x32_bf16 v[16:19], v[114:117], a[4:7], v[16:19]\n\t"
"s_nop 0\n\t"
"v_mfma_f32_16x16x32_bf16 v[28:31], v[108:111], a[4:7], v[24:27]\n\t"
"s_nop 0\n\t"
"v_cvt_pk_bf16_f32 v48, v4, v5\n\t"
"v_cvt_pk_bf16_f32 v49, v6, v7\n\t"
"v_mfma_f32_16x16x32_bf16 v[24:27], v[112:115], a[4:7], v[24:27]\n\t"
"s_nop 0\n\t"
"v_cvt_pk_bf16_f32 v72, v0, v1\n\t"
"v_cvt_pk_bf16_f32 v73, v2, v3\n\t"
"s_waitcnt lgkmcnt(6)\n\t"
"v_mfma_f32_16x16x32_bf16 v[36:39], v[110:113], a[8:11], v[32:35]\n\t"
"s_nop 0\n\t"
"v_cvt_pk_bf16_f32 v50, v12, v13\n\t"
"v_cvt_pk_bf16_f32 v51, v14, v15\n\t"
"v_mfma_f32_16x16x32_bf16 v[32:35], v[114:117], a[8:11], v[32:35]\n\t"
"s_nop 0\n\t"
"v_cvt_pk_bf16_f32 v74, v8, v9\n\t"
"v_cvt_pk_bf16_f32 v75, v10, v11\n\t"
"v_mfma_f32_16x16x32_bf16 v[100:103], v[108:111], a[8:11], v[96:99]\n\t"
"s_nop 0\n\t"
"v_cvt_pk_bf16_f32 v52, v20, v21\n\t"
"v_cvt_pk_bf16_f32 v53, v22, v23\n\t"
"v_mfma_f32_16x16x32_bf16 v[96:99], v[112:115], a[8:11], v[96:99]\n\t"
"s_nop 0\n\t"
"v_cvt_pk_bf16_f32 v76, v16, v17\n\t"
"v_cvt_pk_bf16_f32 v77, v18, v19\n\t"
"s_waitcnt lgkmcnt(4)\n\t"
"v_mfma_f32_16x16x32_bf16 v[124:127], v[110:113], a[12:15], v[120:123]\n\t"
"s_nop 0\n\t"
"v_cvt_pk_bf16_f32 v54, v28, v29\n\t"
"v_cvt_pk_bf16_f32 v55, v30, v31\n\t"
"v_mfma_f32_16x16x32_bf16 v[120:123], v[114:117], a[12:15], v[120:123]\n\t"
"s_nop 0\n\t"
"v_cvt_pk_bf16_f32 v78, v24, v25\n\t"
"v_cvt_pk_bf16_f32 v79, v26, v27\n\t"
"v_mfma_f32_16x16x32_bf16 v[132:135], v[108:111], a[12:15], v[128:131]\n\t"
"s_nop 0\n\t"
"v_cvt_pk_bf16_f32 v56, v36, v37\n\t"
"v_cvt_pk_bf16_f32 v57, v38, v39\n\t"
"v_mfma_f32_16x16x32_bf16 v[128:131], v[112:115], a[12:15], v[128:131]\n\t"
"s_nop 0\n\t"
"v_cvt_pk_bf16_f32 v80, v32, v33\n\t"
"v_cvt_pk_bf16_f32 v81, v34, v35\n\t"
"s_waitcnt lgkmcnt(2)\n\t"
"v_mfma_f32_16x16x32_bf16 v[140:143], v[110:113], a[16:19], v[136:139]\n\t"
"s_nop 0\n\t"
"v_cvt_pk_bf16_f32 v58, v100, v101\n\t"
"v_cvt_pk_bf16_f32 v59, v102, v103\n\t"
"v_mfma_f32_16x16x32_bf16 v[136:139], v[114:117], a[16:19], v[136:139]\n\t"
"s_nop 0\n\t"
"v_cvt_pk_bf16_f32 v82, v96, v97\n\t"
"v_cvt_pk_bf16_f32 v83, v98, v99\n\t"
"v_mfma_f32_16x16x32_bf16 v[156:159], v[108:111], a[16:19], v[152:155]\n\t"
"s_nop 0\n\t"
"v_cvt_pk_bf16_f32 v60, v124, v125\n\t"
"v_cvt_pk_bf16_f32 v61, v126, v127\n\t"
"v_mfma_f32_16x16x32_bf16 v[152:155], v[112:115], a[16:19], v[152:155]\n\t"
"s_nop 0\n\t"
"v_cvt_pk_bf16_f32 v84, v120, v121\n\t"
"v_cvt_pk_bf16_f32 v85, v122, v123\n\t"
"s_waitcnt lgkmcnt(0)\n\t"
"v_mfma_f32_16x16x32_bf16 v[164:167], v[110:113], a[20:23], v[160:163]\n\t"
"s_nop 0\n\t"
"v_cvt_pk_bf16_f32 v62, v132, v133\n\t"
"v_cvt_pk_bf16_f32 v63, v134, v135\n\t"
"v_mfma_f32_16x16x32_bf16 v[160:163], v[114:117], a[20:23], v[160:163]\n\t"
"s_nop 0\n\t"
"v_cvt_pk_bf16_f32 v86, v128, v129\n\t"
"v_cvt_pk_bf16_f32 v87, v130, v131\n\t"
"v_mfma_f32_16x16x32_bf16 v[172:175], v[108:111], a[20:23], v[168:171]\n\t"
"s_nop 0\n\t"
"v_cvt_pk_bf16_f32 v64, v140, v141\n\t"
"v_cvt_pk_bf16_f32 v65, v142, v143\n\t"
"v_mfma_f32_16x16x32_bf16 v[168:171], v[112:115], a[20:23], v[168:171]\n\t"
".p2align 3\n\t"
"v_cvt_pk_bf16_f32 v88, v136, v137\n\t"
"v_cvt_pk_bf16_f32 v89, v138, v139\n\t"
"v_cvt_pk_bf16_f32 v66, v156, v157\n\t"
"v_cvt_pk_bf16_f32 v67, v158, v159\n\t"
"v_cvt_pk_bf16_f32 v90, v152, v153\n\t"
"v_cvt_pk_bf16_f32 v91, v154, v155\n\t"
"v_cvt_pk_bf16_f32 v68, v164, v165\n\t"
"v_cvt_pk_bf16_f32 v69, v166, v167\n\t"
"v_cvt_pk_bf16_f32 v92, v160, v161\n\t"
"v_cvt_pk_bf16_f32 v93, v162, v163\n\t"
"v_cvt_pk_bf16_f32 v70, v172, v173\n\t"
"v_cvt_pk_bf16_f32 v71, v174, v175\n\t"
"v_cvt_pk_bf16_f32 v94, v168, v169\n\t"
"v_cvt_pk_bf16_f32 v95, v170, v171\n\t"
"s_barrier\n\t"
"v_mfma_f32_16x16x32_bf16 v[192:195], a[216:219], a[176:179], 0\n\t"
"s_add_u32 m0, s43, 53888\n\t"
"ds_read_b128 a[144:147], v224 offset:0\n\t"
"ds_read_b128 a[160:163], v224 offset:2048\n\t"
"ds_read_b128 a[148:151], v224 offset:256\n\t"
"v_mfma_f32_16x16x32_bf16 v[196:199], a[216:219], a[180:183], 0\n\t"
"s_mov_b64 exec, s[44:45]\n\t"
"buffer_load_dwordx4 v247, s[12:15], 0 offen offset:128 nt lds\n\t"
"s_mov_b64 exec, -1\n\t"
"v_mfma_f32_16x16x32_bf16 v[200:203], a[216:219], a[184:187], 0\n\t"
"s_add_u32 m0, s26, 56192\n\t"
"s_mul_i32 s92, s73, 294912\n\t"
"s_add_u32 s92, s92, s8\n\t"
"s_addc_u32 s93, s9, 0x100000\n\t"
"v_mfma_f32_16x16x32_bf16 v[204:207], a[216:219], a[188:191], 0\n\t"
"s_mov_b32 s94, s10\n\t"
"ds_read_b128 a[164:167], v224 offset:2304\n\t"
"ds_read_b128 a[152:155], v224 offset:512\n\t"
"v_mfma_f32_16x16x32_bf16 v[208:211], v[148:151], a[192:195], 0\n\t"
"s_mov_b32 s95, 0x800000\n\t"
"ds_read_b128 a[168:171], v224 offset:2560\n\t"
"ds_read_b128 a[156:159], v224 offset:768\n\t"
"v_mfma_f32_16x16x32_bf16 v[212:215], v[148:151], a[196:199], 0\n\t"
"s_waitcnt lgkmcnt(5)\n\t"
"ds_read_b128 a[172:175], v224 offset:2816\n\t"
"s_add_u32 s96, s92, 4096\n\t"
"s_addc_u32 s97, s93, 0\n\t"
"v_mfma_f32_16x16x32_bf16 v[96:99], v[48:51], a[144:147], 0\n\t"
"buffer_load_dwordx4 v236, s[12:15], s22 offen offset:128 nt lds\n\t"
"v_mfma_f32_16x16x32_bf16 v[96:99], v[52:55], a[160:163], v[96:99]\n\t"
"s_lshl_b32 m0, s73, 8\n\t"
"v_cvt_pk_bf16_f32 v192, v192, v196\n\t"
"v_cvt_pk_bf16_f32 v193, v193, v197\n\t"
"v_cvt_pk_bf16_f32 v194, v194, v198\n\t"
"s_mov_b64 s[98:99], s[94:95]\n\t"
"v_mfma_f32_16x16x32_bf16 v[112:115], v[56:59], a[144:147], 0\n\t"
"s_nop 0\n\t"
"ds_write_addtid_b32 v194 offset:20672\n\t"
"v_cvt_pk_bf16_f32 v195, v195, v199\n\t"
"v_mfma_f32_16x16x32_bf16 v[112:115], v[60:63], a[160:163], v[112:115]\n\t"
"s_mov_b32 s16, s80\n\t"
"ds_write_addtid_b32 v192 offset:16544\n\t"
"ds_write_addtid_b32 v193 offset:24864\n\t"
"ds_write_addtid_b32 v195 offset:28992\n\t"
"v_mfma_f32_16x16x32_bf16 v[128:131], v[64:67], a[144:147], 0\n\t"
"s_nop 0\n\t"
"ds_read_b128 a[144:147], v224 offset:1024\n\t"
"v_cvt_pk_bf16_f32 v200, v200, v204\n\t"
"v_mfma_f32_16x16x32_bf16 v[128:131], v[68:71], a[160:163], v[128:131]\n\t"
"s_waitcnt lgkmcnt(9)\n\t"
"v_cvt_pk_bf16_f32 v208, v208, v212\n\t"
"v_cvt_pk_bf16_f32 v209, v209, v213\n\t"
"v_cvt_pk_bf16_f32 v210, v210, v214\n\t"
"v_cvt_pk_bf16_f32 v211, v211, v215\n\t"
"s_and_b32 s17, s81, 0xffff\n\t"
"v_mfma_f32_16x16x32_bf16 v[132:135], v[64:67], a[148:151], 0\n\t"
"s_mov_b32 s18, 100663296\n\t"
"ds_read_b128 a[160:163], v224 offset:3072\n\t"
"v_cvt_pk_bf16_f32 v201, v201, v205\n\t"
"v_mfma_f32_16x16x32_bf16 v[132:135], v[68:71], a[164:167], v[132:135]\n\t"
"s_mov_b32 s19, 0x20000\n\t"
"ds_write_addtid_b32 v200 offset:18592\n\t"
"ds_write_addtid_b32 v210 offset:21696\n\t"
"v_mfma_f32_16x16x32_bf16 v[116:119], v[56:59], a[148:151], 0\n\t"
"s_mov_b32 s21, 4\n\t"
"ds_write_addtid_b32 v208 offset:17568\n\t"
"ds_write_addtid_b32 v211 offset:30016\n\t"
"v_mfma_f32_16x16x32_bf16 v[116:119], v[60:63], a[164:167], v[116:119]\n\t"
"s_waitcnt vmcnt(8)\n\t"
"s_barrier\n\t"
"v_mfma_f32_16x16x32_bf16 v[216:219], v[148:151], a[200:203], 0\n\t"
"Lpad0_%=:\n\t"
"s_cbranch_execz Lpad4_%=\n\t"
"s_mov_b32 s38, -2.0\n\t"
"ds_read_b64_tr_b16 a[176:177], v248 offset:20736\n\t"
"ds_read_b64_tr_b16 a[178:179], v248 offset:21248\n\t"
"v_cvt_pk_bf16_f32 v202, v202, v206\n\t"
"v_mfma_f32_16x16x32_bf16 v[100:103], v[48:51], a[148:151], 0\n\t"
"s_mov_b32 s39, -2.0\n\t"
"ds_read_b128 a[148:151], v224 offset:1280\n\t"
"v_mbcnt_lo_u32_b32 v176, -1, 0\n\t"
"v_mfma_f32_16x16x32_bf16 v[100:103], v[52:55], a[164:167], v[100:103]\n\t"
"s_waitcnt lgkmcnt(15)\n\t"
"ds_read_b64_tr_b16 a[180:181], v249 offset:20736\n\t"
"v_mbcnt_hi_u32_b32 v176, -1, v176\n\t"
"v_bfe_u32 v156, v176, 2, 2\n\t"
"v_mfma_f32_16x16x32_bf16 v[104:107], v[48:51], a[152:155], 0\n\t"
"ds_read_b64_tr_b16 a[182:183], v249 offset:21248\n\t"
"v_and_b32 v157, 3, v176\n\t"
"v_mfma_f32_16x16x32_bf16 v[104:107], v[52:55], a[168:171], v[104:107]\n\t"
"ds_read_b128 a[164:167], v224 offset:3328\n\t"
"v_cvt_pk_bf16_f32 v203, v203, v207\n\t"
"v_mfma_f32_16x16x32_bf16 v[220:223], v[148:151], a[204:207], 0\n\t"
"ds_read_b64_tr_b16 a[184:185], v250 offset:20736\n\t"
"v_lshlrev_b32 v157, 3, v157\n\t"
"v_and_b32 v158, 48, v176\n\t"
"v_mfma_f32_16x16x32_bf16 v[120:123], v[56:59], a[152:155], 0\n\t"
"ds_read_b64_tr_b16 a[186:187], v250 offset:21248\n\t"
"v_lshl_add_u32 v158, v157, 3, v158\n\t"
"v_mfma_f32_16x16x32_bf16 v[120:123], v[60:63], a[168:171], v[120:123]\n\t"
"ds_read_b64_tr_b16 a[188:189], v251 offset:20736\n\t"
"v_and_b32 v159, 8, v176\n\t"
"v_mul_u32_u24 v166, 516, v159\n\t"
"v_and_b32 v159, 4, v176\n\t"
"v_mul_u32_u24 v159, 2080, v159\n\t"
"v_mfma_f32_16x16x32_bf16 v[136:139], v[64:67], a[152:155], 0\n\t"
"ds_read_b128 a[152:155], v224 offset:1536\n\t"
"v_add3_u32 v166, v166, v158, v159\n\t"
"v_mfma_f32_16x16x32_bf16 v[136:139], v[68:71], a[168:171], v[136:139]\n\t"
"s_waitcnt lgkmcnt(15)\n\t"
"ds_read_b128 a[168:171], v224 offset:3584\n\t"
"buffer_load_dword v190, v189, s[8:11], 0 offen sc1\n\t"
"v_accvgpr_write_b32 a212, v224\n\t"
"v_mfma_f32_16x16x32_bf16 v[140:143], v[64:67], a[156:159], 0\n\t"
"ds_read_b64_tr_b16 a[190:191], v251 offset:21248\n\t"
"v_accvgpr_write_b32 a213, v166\n\t"
"v_mfma_f32_16x16x32_bf16 v[140:143], v[68:71], a[172:175], v[140:143]\n\t"
"ds_read_b64_tr_b16 a[192:193], v248 offset:21888\n\t"
"v_lshlrev_b32 v158, 15, v176\n\t"
"v_and_b32 v158, 0x8000, v158\n\t"
"v_cvt_pk_bf16_f32 v216, v216, v220\n\t"
"v_cvt_pk_bf16_f32 v217, v217, v221\n\t"
"v_mfma_f32_16x16x32_bf16 v[124:127], v[56:59], a[156:159], 0\n\t"
"ds_read_b64_tr_b16 a[194:195], v248 offset:22400\n\t"
"v_or_b32 v167, 0x3f803f80, v158\n\t"
"v_mfma_f32_16x16x32_bf16 v[124:127], v[60:63], a[172:175], v[124:127]\n\t"
"ds_write_addtid_b32 v217 offset:27936\n\t"
"buffer_load_dword v191, v189, s[8:11], 0 offen sc1\n\t"
"v_and_b32 v158, 2, v176\n\t"
"v_mul_u32_u24 v158, 0x4000, v158\n\t"
"v_cvt_pk_bf16_f32 v219, v219, v223\n\t"
"v_mfma_f32_16x16x32_bf16 v[108:111], v[48:51], a[156:159], 0\n\t"
"ds_write_addtid_b32 v216 offset:19616\n\t"
"v_lshl_or_b32 v158, v158, 16, v158\n\t"
"v_mfma_f32_16x16x32_bf16 v[108:111], v[52:55], a[172:175], v[108:111]\n\t"
"s_waitcnt lgkmcnt(15)\n\t"
"ds_write_addtid_b32 v201 offset:26912\n\t"
"ds_write_addtid_b32 v202 offset:22720\n\t"
"ds_write_addtid_b32 v209 offset:25888\n\t"
"ds_write_addtid_b32 v219 offset:32064\n\t"
"v_xor_b32 v166, v167, v158\n\t"
"v_accvgpr_write_b32 a210, v166\n\t"
"v_accvgpr_write_b32 a211, v167\n\t"
"s_add_u32 s61, s26, 4294900096\n\t"
"v_lshrrev_b32 v155, 4, v176\n\t"
"v_and_b32 v158, 1, v156\n\t"
"v_lshrrev_b32 v159, 1, v156\n\t"
"v_cvt_pk_bf16_f32 v218, v218, v222\n\t"
"v_mfma_f32_16x16x32_bf16 v[0:3], v[72:75], a[144:147], v[96:99]\n\t"
"ds_write_addtid_b32 v203 offset:31040\n\t"
"v_mul_u32_u24 v166, 1152, v159\n\t"
"v_mfma_f32_16x16x32_bf16 v[0:3], v[76:79], a[160:163], v[0:3]\n\t"
"ds_write_addtid_b32 v218 offset:23744\n\t"
"v_mul_u32_u24 v167, 68352, v158\n\t"
"v_add3_u32 v166, v166, v157, s61\n\t"
"v_add_u32 v166, v166, v167\n\t"
"v_mfma_f32_16x16x32_bf16 v[16:19], v[80:83], a[144:147], v[112:115]\n\t"
"s_add_u32 m0, s26, 56192\n\t"
"ds_read_b128 a[172:175], v224 offset:3840\n\t"
"v_xor_b32 v159, v159, v158\n\t"
"v_mfma_f32_16x16x32_bf16 v[16:19], v[84:87], a[160:163], v[16:19]\n\t"
"ds_read_b128 a[156:159], v224 offset:1792\n\t"
"buffer_load_dwordx4 v237, s[12:15], s22 offen offset:1152 nt lds\n\t"
"v_lshl_add_u32 v166, v159, 6, v166\n\t"
"v_mfma_f32_16x16x32_bf16 v[32:35], v[88:91], a[144:147], v[128:131]\n\t"
"ds_read_b64_tr_b16 a[196:197], v249 offset:21888\n\t"
"v_xad_u32 v167, v158, 0, v155\n\t"
"v_mfma_f32_16x16x32_bf16 v[32:35], v[92:95], a[160:163], v[32:35]\n\t"
"s_waitcnt lgkmcnt(15)\n\t"
"ds_read_b64_tr_b16 a[198:199], v249 offset:22400\n\t"
"buffer_load_dwordx4 v238, s[12:15], s22 offen offset:2176 nt lds\n\t"
"v_lshl_add_u32 v168, v167, 7, v166\n\t"
"v_and_b32 v167, 2, v167\n\t"
"v_mfma_f32_16x16x32_bf16 v[36:39], v[88:91], a[148:151], v[132:135]\n\t"
"ds_read_b64_tr_b16 a[200:201], v250 offset:21888\n\t"
"v_lshl_add_u32 v168, v167, 4, v168\n\t"
"v_mfma_f32_16x16x32_bf16 v[36:39], v[92:95], a[164:167], v[36:39]\n\t"
"ds_read_b64_tr_b16 a[202:203], v250 offset:22400\n\t"
"v_xor_b32 v168, 64, v168\n\t"
"v_accvgpr_write_b32 a208, v168\n\t"
"v_xad_u32 v167, v158, 1, v155\n\t"
"v_mfma_f32_16x16x32_bf16 v[20:23], v[80:83], a[148:151], v[116:119]\n\t"
"s_nop 0\n\t"
"ds_read_b64_tr_b16 a[204:205], v251 offset:21888\n\t"
"ds_read_b64_tr_b16 a[206:207], v251 offset:22400\n\t"
"v_mfma_f32_16x16x32_bf16 v[20:23], v[84:87], a[164:167], v[20:23]\n\t"
"buffer_load_dwordx4 v239, s[12:15], s22 offen offset:3200 nt lds\n\t"
"v_lshl_add_u32 v168, v167, 7, v166\n\t"
"v_and_b32 v167, 2, v167\n\t"
"v_mfma_f32_16x16x32_bf16 v[4:7], v[72:75], a[148:151], v[100:103]\n\t"
"s_lshl_b32 m0, s73, 8\n\t"
"v_lshl_add_u32 v168, v167, 4, v168\n\t"
"v_accvgpr_write_b32 a209, v168\n\t"
"v_mfma_f32_16x16x32_bf16 v[4:7], v[76:79], a[164:167], v[4:7]\n\t"
"s_waitcnt lgkmcnt(6)\n\t"
"s_barrier\n\t"
"v_mfma_f32_16x16x32_bf16 v[8:11], v[72:75], a[152:155], v[104:107]\n\t"
"s_add_u32 m0, s26, 56192\n\t"
"ds_read_b128 a[144:147], v224 offset:16544\n\t"
"ds_read_b128 a[160:163], v224 offset:18592\n\t"
"v_mfma_f32_16x16x32_bf16 v[8:11], v[76:79], a[168:171], v[8:11]\n\t"
"s_mov_b64 exec, s[36:37]\n\t"
"buffer_load_dwordx4 v240, s[12:15], s22 offen offset:3712 nt lds\n\t"
"s_mov_b64 exec, -1\n\t"
"v_mfma_f32_16x16x32_bf16 v[24:27], v[80:83], a[152:155], v[120:123]\n\t"
"s_lshl_b32 m0, s73, 8\n\t"
"ds_read_b128 a[148:151], v224 offset:16800\n\t"
"v_mbcnt_lo_u32_b32 v176, -1, 0\n\t"
"v_mfma_f32_16x16x32_bf16 v[24:27], v[84:87], a[168:171], v[24:27]\n\t"
"ds_read_b128 a[164:167], v224 offset:18848\n\t"
"v_mbcnt_hi_u32_b32 v176, -1, v176\n\t"
"v_mfma_f32_16x16x32_bf16 v[40:43], v[88:91], a[152:155], v[136:139]\n\t"
"v_and_b32 v154, 15, v176\n\t"
"v_lshrrev_b32 v155, 4, v176\n\t"
"v_mfma_f32_16x16x32_bf16 v[40:43], v[92:95], a[168:171], v[40:43]\n\t"
"v_bfe_u32 v156, v176, 2, 2\n\t"
"v_and_b32 v157, 3, v176\n\t"
"v_mfma_f32_16x16x32_bf16 v[44:47], v[88:91], a[156:159], v[140:143]\n\t"
"ds_read_b128 a[152:155], v224 offset:17056\n\t"
"v_lshlrev_b32 v157, 3, v157\n\t"
"s_add_u32 s60, s43, 33280\n\t"
"v_mfma_f32_16x16x32_bf16 v[44:47], v[92:95], a[172:175], v[44:47]\n\t"
"s_add_u32 s61, s26, 4294899008\n\t"
"ds_read_b128 a[168:171], v224 offset:19104\n\t"
"s_mul_i32 s62, s73, 96\n\t"
"v_mfma_f32_16x16x32_bf16 v[28:31], v[80:83], a[156:159], v[124:127]\n\t"
"s_add_u32 s62, s62, 75008\n\t"
"v_min_u32 v158, 35, v176\n\t"
"v_lshl_add_u32 v254, v158, 4, s60\n\t"
"v_mfma_f32_16x16x32_bf16 v[28:31], v[84:87], a[172:175], v[28:31]\n\t"
"buffer_load_dwordx4 a[248:251], v247, s[12:15], 0 offen offset:256 nt\n\t"
"v_add_u32 v255, 68352, v254\n\t"
"v_mfma_f32_16x16x32_bf16 v[12:15], v[72:75], a[156:159], v[108:111]\n\t"
"v_and_b32 v158, 1, v156\n\t"
"v_lshrrev_b32 v156, 3, v176\n\t"
"v_mfma_f32_16x16x32_bf16 v[12:15], v[76:79], a[172:175], v[12:15]\n\t"
"ds_read_b128 a[172:175], v224 offset:19360\n\t"
"v_add_u32 v159, v156, v158\n\t"
"v_mfma_f32_16x16x32_bf16 v[192:195], a[216:219], a[176:179], 0\n\t"
"v_lshl_add_u32 v166, v159, 7, v157\n\t"
"v_and_b32 v159, 2, v159\n\t"
"v_mfma_f32_16x16x32_bf16 v[196:199], a[216:219], a[180:183], 0\n\t"
"v_lshl_add_u32 v166, v159, 4, v166\n\t"
"v_xad_u32 v167, v158, 1, v156\n\t"
"v_mfma_f32_16x16x32_bf16 v[200:203], a[216:219], a[184:187], 0\n\t"
"s_mov_b64 exec, 0\n\t"
"s_cmp_eq_u32 s73, 0\n\t"
"s_cbranch_scc1 Lpad11_%=\n\t"
"s_cmp_eq_u32 s73, 1\n\t"
"s_cbranch_scc1 Lpad10_%=\n\t"
"s_cmp_eq_u32 s73, 2\n\t"
"s_cbranch_scc1 Lpad9_%=\n\t"
"s_branch Lpad8_%=\n\t"
"Lret1_%=:\n\t"
"s_mov_b64 exec, -1\n\t"
"s_waitcnt vmcnt(6)\n\t"
"v_cmp_eq_u32 vcc, v190, v188\n\t"
"s_nop 4\n\t"
"s_cmp_eq_u64 vcc, 0\n\t"
"s_cbranch_scc1 Lgo_%=\n\t"
"s_sub_u32 s68, s68, 1\n\t"
"s_waitcnt vmcnt(5)\n\t"
"Lchk_%=:\n\t"
"v_cmp_eq_u32 vcc, v191, v188\n\t"
"s_nop 4\n\t"
"s_cmp_eq_u64 vcc, 0\n\t"
"s_cbranch_scc1 Lgo_%=\n\t"
"s_sub_u32 s68, s68, 1\n\t"
"s_cmp_eq_u32 s68, 0\n\t"
"s_cbranch_scc1 Lfb_%=\n\t"
"buffer_load_dword v191, v189, s[8:11], 0 offen sc1\n\t"
"s_waitcnt vmcnt(0)\n\t"
"s_branch Lchk_%=\n\t"
"Lgo_%=:\n\t"
"v_lshl_add_u32 v168, v167, 7, v157\n\t"
"v_and_b32 v167, 2, v167\n\t"
"v_mfma_f32_16x16x32_bf16 v[204:207], a[216:219], a[188:191], 0\n\t"
"ds_read_b128 a[156:159], v224 offset:17312\n\t"
"s_mov_b32 s54, 98304\n\t"
".p2align 3\n\t"
"buffer_load_dwordx4 a[96:99], off, s[92:95], s54 offset:0 sc1\n\t"
"v_pk_fma_f32 v[96:97], v[96:97], s[38:39], v[0:1] neg_lo:[1,0,1] neg_hi:[1,0,1]\n\t"
"v_pk_fma_f32 v[98:99], v[98:99], s[38:39], v[2:3] neg_lo:[1,0,1] neg_hi:[1,0,1]\n\t"
"v_pk_fma_f32 v[112:113], v[112:113], s[38:39], v[16:17] neg_lo:[1,0,1] neg_hi:[1,0,1]\n\t"
"v_pk_fma_f32 v[114:115], v[114:115], s[38:39], v[18:19] neg_lo:[1,0,1] neg_hi:[1,0,1]\n\t"
"v_pk_fma_f32 v[128:129], v[128:129], s[38:39], v[32:33] neg_lo:[1,0,1] neg_hi:[1,0,1]\n\t"
"v_pk_fma_f32 v[130:131], v[130:131], s[38:39], v[34:35] neg_lo:[1,0,1] neg_hi:[1,0,1]\n\t"
"v_pk_fma_f32 v[132:133], v[132:133], s[38:39], v[36:37] neg_lo:[1,0,1] neg_hi:[1,0,1]\n\t"
"v_pk_fma_f32 v[134:135], v[134:135], s[38:39], v[38:39] neg_lo:[1,0,1] neg_hi:[1,0,1]\n\t"
"v_pk_fma_f32 v[116:117], v[116:117], s[38:39], v[20:21] neg_lo:[1,0,1] neg_hi:[1,0,1]\n\t"
"v_pk_fma_f32 v[118:119], v[118:119], s[38:39], v[22:23] neg_lo:[1,0,1] neg_hi:[1,0,1]\n\t"
"v_pk_fma_f32 v[100:101], v[100:101], s[38:39], v[4:5] neg_lo:[1,0,1] neg_hi:[1,0,1]\n\t"
"v_pk_fma_f32 v[102:103], v[102:103], s[38:39], v[6:7] neg_lo:[1,0,1] neg_hi:[1,0,1]\n\t"
"buffer_load_dwordx4 a[100:103], off, s[92:95], s54 offset:1024 sc1\n\t"
"v_pk_fma_f32 v[104:105], v[104:105], s[38:39], v[8:9] neg_lo:[1,0,1] neg_hi:[1,0,1]\n\t"
"v_pk_fma_f32 v[106:107], v[106:107], s[38:39], v[10:11] neg_lo:[1,0,1] neg_hi:[1,0,1]\n\t"
"v_pk_fma_f32 v[120:121], v[120:121], s[38:39], v[24:25] neg_lo:[1,0,1] neg_hi:[1,0,1]\n\t"
"v_pk_fma_f32 v[122:123], v[122:123], s[38:39], v[26:27] neg_lo:[1,0,1] neg_hi:[1,0,1]\n\t"
"v_pk_fma_f32 v[136:137], v[136:137], s[38:39], v[40:41] neg_lo:[1,0,1] neg_hi:[1,0,1]\n\t"
"v_pk_fma_f32 v[138:139], v[138:139], s[38:39], v[42:43] neg_lo:[1,0,1] neg_hi:[1,0,1]\n\t"
"v_pk_fma_f32 v[140:141], v[140:141], s[38:39], v[44:45] neg_lo:[1,0,1] neg_hi:[1,0,1]\n\t"
"v_pk_fma_f32 v[142:143], v[142:143], s[38:39], v[46:47] neg_lo:[1,0,1] neg_hi:[1,0,1]\n\t"
"v_pk_fma_f32 v[124:125], v[124:125], s[38:39], v[28:29] neg_lo:[1,0,1] neg_hi:[1,0,1]\n\t"
"v_pk_fma_f32 v[126:127], v[126:127], s[38:39], v[30:31] neg_lo:[1,0,1] neg_hi:[1,0,1]\n\t"
"v_pk_fma_f32 v[108:109], v[108:109], s[38:39], v[12:13] neg_lo:[1,0,1] neg_hi:[1,0,1]\n\t"
"v_pk_fma_f32 v[110:111], v[110:111], s[38:39], v[14:15] neg_lo:[1,0,1] neg_hi:[1,0,1]\n\t"
"v_mfma_f32_16x16x32_bf16 v[208:211], a[216:219], a[192:195], 0\n\t"
"s_waitcnt lgkmcnt(12)\n\t"
"v_lshl_add_u32 v168, v167, 4, v168\n\t"
"v_sub_u32 v168, v168, v166\n\t"
"v_mfma_f32_16x16x32_bf16 v[212:215], a[216:219], a[196:199], 0\n\t"
"s_waitcnt lgkmcnt(10)\n\t"
"v_mul_u32_u24 v159, 68288, v158\n\t"
"v_add_u32 v166, v166, v159\n\t"
"v_mfma_f32_16x16x32_bf16 v[216:219], a[216:219], a[200:203], 0\n\t"
"s_waitcnt lgkmcnt(8)\n\t"
"buffer_load_dwordx4 a[104:107], off, s[92:95], s54 offset:2048 sc1\n\t"
"v_lshl_add_u32 v168, v158, 7, v168\n\t"
"v_subrev_u32 v253, 64, v168\n\t"
"v_mfma_f32_16x16x32_bf16 v[220:223], a[216:219], a[204:207], 0\n\t"
"s_waitcnt lgkmcnt(6)\n\t"
"v_cvt_pk_bf16_f32 v193, v193, v197\n\t"
"v_cvt_pk_bf16_f32 v195, v195, v199\n\t"
"v_mfma_f32_16x16x32_bf16 v[0:3], a[48:51], a[144:147], v[0:3]\n\t"
"ds_write_addtid_b32 v195 offset:12448\n\t"
"v_add_u32 v225, s61, v166\n\t"
"v_mfma_f32_16x16x32_bf16 v[0:3], a[52:55], a[160:163], v[0:3]\n\t"
"ds_write_addtid_b32 v193 offset:8320\n\t"
"buffer_load_dwordx4 a[108:111], off, s[92:95], s54 offset:3072 sc1\n\t"
"v_mul_u32_u24 v158, 400, v154\n\t"
"v_mul_u32_u24 v159, 24, v155\n\t"
"v_add3_u32 v228, v158, v159, s62\n\t"
"v_cvt_pk_bf16_f32 v201, v201, v205\n\t"
"v_mfma_f32_16x16x32_bf16 v[16:19], a[56:59], a[144:147], v[16:19]\n\t"
"ds_write_addtid_b32 v201 offset:10368\n\t"
"v_cvt_pk_bf16_f32 v200, v200, v204\n\t"
"v_mfma_f32_16x16x32_bf16 v[16:19], a[60:63], a[160:163], v[16:19]\n\t"
"s_nop 0\n\t"
"ds_write_addtid_b32 v200 offset:2048\n\t"
"v_cvt_pk_bf16_f32 v203, v203, v207\n\t"
"v_mfma_f32_16x16x32_bf16 v[32:35], a[64:67], a[144:147], v[32:35]\n\t"
"s_nop 0\n\t"
"ds_read_b128 a[144:147], v224 offset:17568\n\t"
"v_cvt_pk_bf16_f32 v194, v194, v198\n\t"
"v_mfma_f32_16x16x32_bf16 v[32:35], a[68:71], a[160:163], v[32:35]\n\t"
"s_waitcnt lgkmcnt(9)\n\t"
"ds_write_addtid_b32 v194 offset:4128\n\t"
"buffer_load_dwordx4 a[112:115], off, s[96:99], s54 offset:0 sc1\n\t"
"v_mfma_f32_16x16x32_bf16 v[36:39], a[64:67], a[148:151], v[36:39]\n\t"
"s_nop 0\n\t"
"ds_read_b128 a[160:163], v224 offset:19616\n\t"
"v_cvt_pk_bf16_f32 v219, v219, v223\n\t"
"v_mfma_f32_16x16x32_bf16 v[36:39], a[68:71], a[164:167], v[36:39]\n\t"
"s_nop 0\n\t"
"ds_write_addtid_b32 v219 offset:15520\n\t"
"buffer_load_dwordx4 a[116:119], off, s[96:99], s54 offset:1024 sc1\n\t"
"v_cvt_pk_bf16_f32 v192, v192, v196\n\t"
"s_barrier\n\t"
"v_mfma_f32_16x16x32_bf16 v[20:23], a[56:59], a[148:151], v[20:23]\n\t"
"s_mul_i32 s60, s73, 3200\n\t"
"ds_read_b64_tr_b16 a[176:177], v248 offset:41472\n\t"
"ds_read_b64_tr_b16 a[178:179], v248 offset:41984\n\t"
"v_mfma_f32_16x16x32_bf16 v[20:23], a[60:63], a[164:167], v[20:23]\n\t"
"s_nop 0\n\t"
"ds_read_b64_tr_b16 a[180:181], v249 offset:41472\n\t"
"v_cvt_pk_bf16_f32 v209, v209, v213\n\t"
"v_mfma_f32_16x16x32_bf16 v[4:7], a[48:51], a[148:151], v[4:7]\n\t"
"s_nop 0\n\t"
"ds_read_b128 a[148:151], v224 offset:17824\n\t"
"v_cvt_pk_bf16_f32 v208, v208, v212\n\t"
"v_mfma_f32_16x16x32_bf16 v[4:7], a[52:55], a[164:167], v[4:7]\n\t"
"s_waitcnt lgkmcnt(14)\n\t"
"ds_read_b64_tr_b16 a[182:183], v249 offset:41984\n\t"
"s_mov_b32 s66, 172032\n\t"
"buffer_load_dwordx4 a[120:123], off, s[92:95], s66 offset:0 sc1\n\t"
"v_mfma_f32_16x16x32_bf16 v[8:11], a[48:51], a[152:155], v[8:11]\n\t"
"s_add_u32 s60, s60, 75008\n\t"
"ds_read_b128 a[164:167], v224 offset:19872\n\t"
"v_cvt_pk_bf16_f32 v216, v216, v220\n\t"
"v_mfma_f32_16x16x32_bf16 v[8:11], a[52:55], a[168:171], v[8:11]\n\t"
"s_and_b32 s61, s73, 2\n\t"
"ds_read_b64_tr_b16 a[184:185], v250 offset:41472\n\t"
"v_cvt_pk_bf16_f32 v217, v217, v221\n\t"
"v_mfma_f32_16x16x32_bf16 v[24:27], a[56:59], a[152:155], v[24:27]\n\t"
"s_lshl_b32 s61, s61, 7\n\t"
"ds_read_b64_tr_b16 a[186:187], v250 offset:41984\n\t"
"ds_read_b64_tr_b16 a[188:189], v251 offset:41472\n\t"
"v_mfma_f32_16x16x32_bf16 v[24:27], a[60:63], a[168:171], v[24:27]\n\t"
"buffer_load_dwordx4 a[124:127], off, s[92:95], s66 offset:1024 sc1\n\t"
"v_cvt_pk_bf16_f32 v202, v202, v206\n\t"
"v_cvt_pk_bf16_f32 v218, v218, v222\n\t"
"v_mfma_f32_16x16x32_bf16 v[40:43], a[64:67], a[152:155], v[40:43]\n\t"
"s_add_u32 s60, s60, s61\n\t"
"ds_read_b128 a[152:155], v224 offset:18080\n\t"
"ds_write_addtid_b32 v218 offset:7200\n\t"
"v_mfma_f32_16x16x32_bf16 v[40:43], a[68:71], a[168:171], v[40:43]\n\t"
"s_waitcnt lgkmcnt(15)\n\t"
"ds_read_b128 a[168:171], v224 offset:20128\n\t"
"buffer_load_dwordx4 a[128:131], off, s[92:95], s66 offset:2048 sc1\n\t"
"v_mfma_f32_16x16x32_bf16 v[44:47], a[64:67], a[156:159], v[44:47]\n\t"
"s_nop 0\n\t"
"ds_read_b64_tr_b16 a[190:191], v251 offset:41984\n\t"
"v_cvt_pk_bf16_f32 v210, v210, v214\n\t"
"v_mfma_f32_16x16x32_bf16 v[44:47], a[68:71], a[172:175], v[44:47]\n\t"
"s_mul_i32 s61, s73, 393216\n\t"
"ds_write_addtid_b32 v203 offset:14496\n\t"
"ds_read_b64_tr_b16 a[192:193], v248 offset:42624\n\t"
"v_cvt_pk_bf16_f32 v211, v211, v215\n\t"
"v_mfma_f32_16x16x32_bf16 v[28:31], a[56:59], a[156:159], v[28:31]\n\t"
"s_nop 0\n\t"
"ds_write_addtid_b32 v202 offset:6176\n\t"
"v_add_u32 v158, 0, v176\n\t"
"v_mfma_f32_16x16x32_bf16 v[28:31], a[60:63], a[172:175], v[28:31]\n\t"
"s_nop 0\n\t"
"ds_read_b64_tr_b16 a[194:195], v248 offset:43136\n\t"
"buffer_load_dwordx4 a[132:135], off, s[92:95], s66 offset:3072 sc1\n\t"
"v_mul_u32_u24 v159, 0xab, v158\n\t"
"v_mfma_f32_16x16x32_bf16 v[12:15], a[48:51], a[156:159], v[12:15]\n\t"
"s_nop 0\n\t"
"ds_read_b128 a[156:159], v224 offset:18336\n\t"
"v_lshrrev_b32 v159, 12, v159\n\t"
"v_mfma_f32_16x16x32_bf16 v[12:15], a[52:55], a[172:175], v[12:15]\n\t"
"s_waitcnt lgkmcnt(15)\n\t"
"ds_write_addtid_b32 v192 offset:0\n\t"
"buffer_load_dwordx4 a[136:139], off, s[96:99], s66 offset:0 sc1\n\t"
"v_add_lshl_u32 v166, v158, v159, 4\n\t"
"v_add_u32 v229, s60, v166\n\t"
"v_mad_u32_u24 v158, v159, 24, v158\n\t"
"v_lshl_add_u32 v232, v158, 4, s61\n\t"
"v_mfma_f32_16x16x32_bf16 v[96:99], a[72:75], a[144:147], v[96:99]\n\t"
"s_nop 0\n\t"
"ds_write_addtid_b32 v209 offset:9344\n\t"
"v_add_u32 v158, 64, v176\n\t"
"v_mfma_f32_16x16x32_bf16 v[96:99], a[76:79], a[160:163], v[96:99]\n\t"
"s_nop 0\n\t"
"ds_write_addtid_b32 v208 offset:1024\n\t"
"ds_write_addtid_b32 v210 offset:5152\n\t"
"ds_write_addtid_b32 v216 offset:3072\n\t"
"ds_write_addtid_b32 v217 offset:11392\n\t"
"v_mul_u32_u24 v159, 0xab, v158\n\t"
"v_lshrrev_b32 v159, 12, v159\n\t"
"v_mfma_f32_16x16x32_bf16 v[112:115], a[80:83], a[144:147], v[112:115]\n\t"
"s_nop 0\n\t"
"ds_write_addtid_b32 v211 offset:13472\n\t"
"v_add_lshl_u32 v166, v158, v159, 4\n\t"
"v_mfma_f32_16x16x32_bf16 v[112:115], a[84:87], a[160:163], v[112:115]\n\t"
"s_nop 0\n\t"
"ds_read_b128 a[172:175], v224 offset:20384\n\t"
"buffer_load_dwordx4 a[140:143], off, s[96:99], s66 offset:1024 sc1\n\t"
"v_add_u32 v230, s60, v166\n\t"
"v_mfma_f32_16x16x32_bf16 v[128:131], a[88:91], a[144:147], v[128:131]\n\t"
"s_nop 0\n\t"
"ds_read_b64_tr_b16 a[196:197], v249 offset:42624\n\t"
"v_mad_u32_u24 v158, v159, 24, v158\n\t"
"v_mfma_f32_16x16x32_bf16 v[128:131], a[92:95], a[160:163], v[128:131]\n\t"
"s_waitcnt lgkmcnt(15)\n\t"
"ds_read_b64_tr_b16 a[198:199], v249 offset:43136\n\t"
"v_lshl_add_u32 v233, v158, 4, s61\n\t"
"v_mfma_f32_16x16x32_bf16 v[132:135], a[88:91], a[148:151], v[132:135]\n\t"
"s_nop 0\n\t"
"ds_read_b64_tr_b16 a[200:201], v250 offset:42624\n\t"
"v_add_u32 v158, 128, v176\n\t"
"v_mfma_f32_16x16x32_bf16 v[132:135], a[92:95], a[164:167], v[132:135]\n\t"
"s_nop 0\n\t"
"ds_read_b64_tr_b16 a[202:203], v250 offset:43136\n\t"
"s_mov_b32 s69, 196608\n\t"
"buffer_load_dwordx4 a[0:3], off, s[92:95], s69 offset:0 sc1\n\t"
"v_mul_u32_u24 v159, 0xab, v158\n\t"
"v_mfma_f32_16x16x32_bf16 v[116:119], a[80:83], a[148:151], v[116:119]\n\t"
"s_nop 0\n\t"
"ds_read_b64_tr_b16 a[204:205], v251 offset:42624\n\t"
"ds_read_b64_tr_b16 a[206:207], v251 offset:43136\n\t"
"v_mfma_f32_16x16x32_bf16 v[116:119], a[84:87], a[164:167], v[116:119]\n\t"
"s_nop 0\n\t"
"v_lshrrev_b32 v159, 12, v159\n\t"
"v_add_lshl_u32 v166, v158, v159, 4\n\t"
"v_add_u32 v231, s60, v166\n\t"
"v_mfma_f32_16x16x32_bf16 v[100:103], a[72:75], a[148:151], v[100:103]\n\t"
"s_nop 0\n\t"
"v_mad_u32_u24 v158, v159, 24, v158\n\t"
"v_lshl_add_u32 v234, v158, 4, s61\n\t"
"v_mfma_f32_16x16x32_bf16 v[100:103], a[76:79], a[164:167], v[100:103]\n\t"
"buffer_load_dwordx4 a[4:7], off, s[92:95], s69 offset:1024 sc1\n\t"
"s_waitcnt lgkmcnt(6)\n\t"
"s_add_u32 s71, s71, 1\n\t"
"v_mov_b32 v215, s71\n\t"
"s_mov_b64 exec, 1\n\t"
"buffer_store_dword v215, off, s[8:11], s70 sc1\n\t"
"s_mov_b64 exec, -1\n\t"
"s_barrier\n\t"
"v_mfma_f32_16x16x32_bf16 v[104:107], a[72:75], a[152:155], v[104:107]\n\t"
"s_nop 0\n\t"
"ds_read_b128 a[144:147], v224 offset:0\n\t"
"ds_read_b128 a[148:151], v224 offset:256\n\t"
"v_mfma_f32_16x16x32_bf16 v[104:107], a[76:79], a[168:171], v[104:107]\n\t"
"buffer_load_dwordx4 a[8:11], off, s[92:95], s69 offset:2048 sc1\n\t"
"v_mfma_f32_16x16x32_bf16 v[120:123], a[80:83], a[152:155], v[120:123]\n\t"
"s_nop 0\n\t"
"ds_read_b128 a[160:163], v224 offset:2048\n\t"
"v_add_u32 v248, 122368, v226\n\t"
"v_mfma_f32_16x16x32_bf16 v[120:123], a[84:87], a[168:171], v[120:123]\n\t"
"s_nop 0\n\t"
"ds_read_b128 a[164:167], v224 offset:2304\n\t"
"v_accvgpr_read_b32 v172, a220\n\t"
"v_mfma_f32_16x16x32_bf16 v[136:139], a[88:91], a[152:155], v[136:139]\n\t"
"s_nop 0\n\t"
"v_and_b32 v172, 0xff7fff7f, v172\n\t"
"v_accvgpr_write_b32 a252, v172\n\t"
"v_mfma_f32_16x16x32_bf16 v[136:139], a[92:95], a[168:171], v[136:139]\n\t"
"buffer_load_dwordx4 a[12:15], off, s[92:95], s69 offset:3072 sc1\n\t"
"v_mfma_f32_16x16x32_bf16 v[140:143], a[88:91], a[156:159], v[140:143]\n\t"
"s_nop 0\n\t"
"ds_read_b128 a[168:171], v224 offset:2560\n\t"
"v_xor_b32 v249, 32, v248\n\t"
"v_mfma_f32_16x16x32_bf16 v[140:143], a[92:95], a[172:175], v[140:143]\n\t"
"s_nop 0\n\t"
"ds_read_b128 a[152:155], v224 offset:512\n\t"
"buffer_load_dwordx4 a[16:19], off, s[96:99], s69 offset:0 sc1\n\t"
"v_mfma_f32_16x16x32_bf16 v[124:127], a[80:83], a[156:159], v[124:127]\n\t"
"s_nop 0\n\t"
"v_accvgpr_read_b32 v173, a221\n\t"
"v_xor_b32 v250, 64, v248\n\t"
"v_mfma_f32_16x16x32_bf16 v[124:127], a[84:87], a[172:175], v[124:127]\n\t"
"buffer_load_dwordx4 a[20:23], off, s[96:99], s69 offset:1024 sc1\n\t"
"v_mfma_f32_16x16x32_bf16 v[108:111], a[72:75], a[156:159], v[108:111]\n\t"
"s_mov_b32 s41, 16384\n\t"
"ds_read_b128 a[156:159], v224 offset:768\n\t"
"v_xor_b32 v251, 96, v248\n\t"
"v_mfma_f32_16x16x32_bf16 v[108:111], a[76:79], a[172:175], v[108:111]\n\t"
"s_nop 0\n\t"
"ds_read_b128 a[172:175], v224 offset:2816\n\t"
"v_and_b32 v173, 0xff7fff7f, v173\n\t"
"v_mfma_f32_16x16x32_bf16 v[192:195], a[216:219], a[176:179], 0\n\t"
"s_nop 0\n\t"
"v_accvgpr_write_b32 a253, v173\n\t"
"v_accvgpr_read_b32 v174, a222\n\t"
"v_mfma_f32_16x16x32_bf16 v[196:199], a[216:219], a[180:183], 0\n\t"
"s_mov_b32 s81, 270336\n\t"
"buffer_load_dwordx4 a[24:27], off, s[92:95], s81 offset:0 sc1\n\t"
"v_mfma_f32_16x16x32_bf16 v[200:203], a[216:219], a[184:187], 0\n\t"
"s_nop 0\n\t"
"ds_read_b64_tr_b16 a[178:179], v248 offset:128\n\t"
"v_and_b32 v174, 0xff7fff7f, v174\n\t"
"v_mfma_f32_16x16x32_bf16 v[204:207], a[216:219], a[188:191], 0\n\t"
"s_mov_b32 s40, 8192\n\t"
"ds_read_b64_tr_b16 a[176:177], v248 offset:0\n\t"
"v_accvgpr_write_b32 a254, v174\n\t"
"v_mfma_f32_16x16x32_bf16 v[208:211], a[216:219], a[192:195], 0\n\t"
"s_waitcnt lgkmcnt(14)\n\t"
"buffer_load_dwordx4 a[28:31], off, s[92:95], s81 offset:1024 sc1\n\t"
"v_mfma_f32_16x16x32_bf16 v[212:215], a[216:219], a[196:199], 0\n\t"
"s_waitcnt lgkmcnt(12)\n\t"
"ds_read_b64_tr_b16 a[180:181], v249 offset:256\n\t"
"v_accvgpr_read_b32 v175, a223\n\t"
"v_mfma_f32_16x16x32_bf16 v[216:219], a[216:219], a[200:203], 0\n\t"
"s_waitcnt lgkmcnt(11)\n\t"
"ds_read_b64_tr_b16 a[186:187], v249 offset:128\n\t"
"v_and_b32 v175, 0xff7fff7f, v175\n\t"
"v_mfma_f32_16x16x32_bf16 v[220:223], a[216:219], a[204:207], 0\n\t"
"s_waitcnt vmcnt(19) lgkmcnt(9)\n\t"
"ds_read_b64_tr_b16 a[184:185], v249 offset:0\n\t"
"buffer_load_dwordx4 a[32:35], off, s[92:95], s81 offset:2048 sc1\n\t"
"v_mfma_f32_16x16x32_bf16 v[0:3], a[96:99], a[144:147], v[0:3]\n\t"
"s_nop 0\n\t"
"v_accvgpr_write_b32 a255, v175\n\t"
"v_cvt_pk_bf16_f32 v195, v195, v199\n\t"
"v_mfma_f32_16x16x32_bf16 v[0:3], a[100:103], a[160:163], v[0:3]\n\t"
"s_waitcnt vmcnt(18)\n\t"
"ds_read_b64_tr_b16 a[188:189], v248 offset:256\n\t"
"v_cvt_pk_bf16_f32 v193, v193, v197\n\t"
"s_mov_b32 s90, 0.5\n\t"
"v_mfma_f32_16x16x32_bf16 v[16:19], a[104:107], a[144:147], v[16:19]\n\t"
"s_nop 0\n\t"
"ds_read_b64_tr_b16 a[194:195], v250 offset:128\n\t"
"v_cvt_pk_bf16_f32 v200, v200, v204\n\t"
"v_mfma_f32_16x16x32_bf16 v[16:19], a[108:111], a[160:163], v[16:19]\n\t"
"s_waitcnt vmcnt(16)\n\t"
"ds_read_b64_tr_b16 a[192:193], v250 offset:0\n\t"
"buffer_load_dwordx4 a[36:39], off, s[92:95], s81 offset:3072 sc1\n\t"
"v_mfma_f32_16x16x32_bf16 v[32:35], a[112:115], a[144:147], v[32:35]\n\t"
"s_nop 0\n\t"
"v_cvt_pk_bf16_f32 v203, v203, v207\n\t"
"v_cvt_pk_bf16_f32 v210, v210, v214\n\t"
"v_mfma_f32_16x16x32_bf16 v[32:35], a[116:119], a[160:163], v[32:35]\n\t"
"s_waitcnt lgkmcnt(12)\n\t"
"ds_read_b128 a[144:147], v224 offset:1024\n\t"
"v_cvt_pk_bf16_f32 v208, v208, v212\n\t"
"v_cvt_pk_bf16_f32 v217, v217, v221\n\t"
"s_mov_b32 s91, 0.5\n\t"
"v_mfma_f32_16x16x32_bf16 v[36:39], a[112:115], a[148:151], v[36:39]\n\t"
"s_add_u32 s84, s92, 18432\n\t"
"s_addc_u32 s85, s93, 0\n\t"
"ds_read_b128 a[160:163], v224 offset:3072\n\t"
"v_mfma_f32_16x16x32_bf16 v[36:39], a[116:119], a[164:167], v[36:39]\n\t"
"buffer_load_dwordx4 a[40:43], off, s[96:99], s81 offset:0 sc1\n\t"
"v_mfma_f32_16x16x32_bf16 v[20:23], a[104:107], a[148:151], v[20:23]\n\t"
"s_nop 0\n\t"
"ds_write_addtid_b32 v200 offset:18592\n\t"
"v_cvt_pk_bf16_f32 v216, v216, v220\n\t"
"v_mfma_f32_16x16x32_bf16 v[20:23], a[108:111], a[164:167], v[20:23]\n\t"
"s_mov_b32 s42, 24576\n\t"
"v_cvt_pk_bf16_f32 v194, v194, v198\n\t"
"v_cvt_pk_bf16_f32 v211, v211, v215\n\t"
"v_mfma_f32_16x16x32_bf16 v[4:7], a[96:99], a[148:151], v[4:7]\n\t"
"s_mov_b64 s[86:87], s[94:95]\n\t"
"ds_read_b128 a[148:151], v224 offset:1280\n\t"
"ds_write_addtid_b32 v216 offset:19616\n\t"
"v_mfma_f32_16x16x32_bf16 v[4:7], a[100:103], a[164:167], v[4:7]\n\t"
"s_waitcnt lgkmcnt(15)\n\t"
"buffer_load_dwordx4 a[44:47], off, s[96:99], s81 offset:1024 sc1\n\t"
"v_mfma_f32_16x16x32_bf16 v[8:11], a[96:99], a[152:155], v[8:11]\n\t"
"s_xor_b64 s[88:89], s[84:85], s[92:93]\n\t"
"ds_read_b128 a[164:167], v224 offset:3328\n\t"
"v_cvt_pk_bf16_f32 v219, v219, v223\n\t"
"v_mfma_f32_16x16x32_bf16 v[8:11], a[100:103], a[168:171], v[8:11]\n\t"
"s_nop 0\n\t"
"ds_write_addtid_b32 v193 offset:24864\n\t"
"v_cvt_pk_bf16_f32 v201, v201, v205\n\t"
"v_mfma_f32_16x16x32_bf16 v[24:27], a[104:107], a[152:155], v[24:27]\n\t"
"s_nop 0\n\t"
"ds_write_addtid_b32 v211 offset:30016\n\t"
"v_cvt_pk_bf16_f32 v202, v202, v206\n\t"
"v_mfma_f32_16x16x32_bf16 v[24:27], a[108:111], a[168:171], v[24:27]\n\t"
"s_mov_b32 s57, 122880\n\t"
"buffer_load_dwordx4 a[48:51], off, s[92:95], s57 offset:0 sc1\n\t"
"v_mfma_f32_16x16x32_bf16 v[40:43], a[112:115], a[152:155], v[40:43]\n\t"
"s_add_u32 s28, s84, 4096\n\t"
"s_addc_u32 s29, s85, 0\n\t"
"ds_read_b128 a[152:155], v224 offset:1536\n\t"
"v_mfma_f32_16x16x32_bf16 v[40:43], a[116:119], a[168:171], v[40:43]\n\t"
"s_waitcnt lgkmcnt(15)\n\t"
"ds_read_b128 a[168:171], v224 offset:3584\n\t"
"ds_write_addtid_b32 v208 offset:17568\n\t"
"s_mov_b32 s47, 40960\n\t"
"v_mfma_f32_16x16x32_bf16 v[44:47], a[112:115], a[156:159], v[44:47]\n\t"
"s_mov_b64 s[30:31], s[94:95]\n\t"
"v_cvt_pk_bf16_f32 v192, v192, v196\n\t"
"v_cvt_pk_bf16_f32 v209, v209, v213\n\t"
"v_mfma_f32_16x16x32_bf16 v[44:47], a[116:119], a[172:175], v[44:47]\n\t"
"buffer_load_dwordx4 a[52:55], off, s[92:95], s57 offset:1024 sc1\n\t"
"v_mfma_f32_16x16x32_bf16 v[28:31], a[104:107], a[156:159], v[28:31]\n\t"
"s_mov_b32 s48, 49152\n\t"
"ds_write_addtid_b32 v195 offset:28992\n\t"
"ds_write_addtid_b32 v202 offset:22720\n\t"
"v_mfma_f32_16x16x32_bf16 v[28:31], a[108:111], a[172:175], v[28:31]\n\t"
"s_add_u32 s25, s24, 0xffeb0000\n\t"
"ds_write_addtid_b32 v194 offset:20672\n\t"
"ds_write_addtid_b32 v203 offset:31040\n\t"
"v_mfma_f32_16x16x32_bf16 v[12:15], a[96:99], a[156:159], v[12:15]\n\t"
"s_mov_b32 s49, 57344\n\t"
"ds_write_addtid_b32 v201 offset:26912\n\t"
"ds_write_addtid_b32 v217 offset:27936\n\t"
"v_mfma_f32_16x16x32_bf16 v[12:15], a[100:103], a[172:175], v[12:15]\n\t"
"s_waitcnt vmcnt(19) lgkmcnt(15)\n\t"
"buffer_load_dwordx4 a[56:59], off, s[92:95], s57 offset:2048 sc1\n\t"
"v_mfma_f32_16x16x32_bf16 v[96:99], a[120:123], a[144:147], v[96:99]\n\t"
"s_mov_b32 s46, 32768\n\t"
"ds_write_addtid_b32 v209 offset:25888\n\t"
"ds_write_addtid_b32 v219 offset:32064\n\t"
"v_mfma_f32_16x16x32_bf16 v[96:99], a[124:127], a[160:163], v[96:99]\n\t"
"Lpad1_%=:\n\t"
"s_cbranch_execz Lpad5_%=\n\t"
"s_waitcnt vmcnt(18)\n\t"
"ds_write_addtid_b32 v192 offset:16544\n\t"
"v_cvt_pk_bf16_f32 v218, v218, v222\n\t"
"v_mfma_f32_16x16x32_bf16 v[112:115], a[128:131], a[144:147], v[112:115]\n\t"
"s_add_u32 s27, s24, 0xffe80000\n\t"
"ds_write_addtid_b32 v210 offset:21696\n\t"
"ds_write_addtid_b32 v218 offset:23744\n\t"
"v_mfma_f32_16x16x32_bf16 v[112:115], a[132:135], a[160:163], v[112:115]\n\t"
"s_waitcnt vmcnt(16)\n\t"
"buffer_load_dwordx4 a[60:63], off, s[92:95], s57 offset:3072 sc1\n\t"
"v_mfma_f32_16x16x32_bf16 v[128:131], a[136:139], a[144:147], v[128:131]\n\t"
"s_add_u32 s20, s24, 0xffd30000\n\t"
"ds_read_b128 a[156:159], v224 offset:1792\n\t"
"ds_read_b64_tr_b16 a[196:197], v251 offset:256\n\t"
"v_mfma_f32_16x16x32_bf16 v[128:131], a[140:143], a[160:163], v[128:131]\n\t"
"s_waitcnt lgkmcnt(15)\n\t"
"buffer_load_dwordx4 a[64:67], off, s[96:99], s57 offset:0 sc1\n\t"
"v_mfma_f32_16x16x32_bf16 v[132:135], a[136:139], a[148:151], v[132:135]\n\t"
"s_mov_b32 s50, 65536\n\t"
"ds_read_b128 a[172:175], v224 offset:3840\n\t"
"ds_read_b64_tr_b16 a[202:203], v251 offset:128\n\t"
"v_mfma_f32_16x16x32_bf16 v[132:135], a[140:143], a[164:167], v[132:135]\n\t"
"buffer_load_dwordx4 a[68:71], off, s[96:99], s57 offset:1024 sc1\n\t"
"v_mfma_f32_16x16x32_bf16 v[116:119], a[128:131], a[148:151], v[116:119]\n\t"
"s_add_u32 s24, s24, 0xffd00000\n\t"
"ds_read_b64_tr_b16 a[200:201], v251 offset:0\n\t"
"ds_read_b64_tr_b16 a[204:205], v250 offset:256\n\t"
"v_mfma_f32_16x16x32_bf16 v[116:119], a[132:135], a[164:167], v[116:119]\n\t"
"s_mov_b32 s63, 147456\n\t"
"buffer_load_dwordx4 a[72:75], off, s[92:95], s63 offset:0 sc1\n\t"
"v_mfma_f32_16x16x32_bf16 v[100:103], a[120:123], a[148:151], v[100:103]\n\t"
"v_mfma_f32_16x16x32_bf16 v[100:103], a[124:127], a[164:167], v[100:103]\n\t"
"buffer_load_dwordx4 a[76:79], off, s[92:95], s63 offset:1024 sc1\n\t"
"s_waitcnt lgkmcnt(3)\n\t"
"s_barrier\n\t"
"v_mfma_f32_16x16x32_bf16 v[104:107], a[120:123], a[152:155], v[104:107]\n\t"
"s_nop 0\n\t"
"ds_read_b128 a[144:147], v224 offset:16544\n\t"
"ds_read_b128 a[148:151], v224 offset:16800\n\t"
"v_mfma_f32_16x16x32_bf16 v[104:107], a[124:127], a[168:171], v[104:107]\n\t"
"ds_read_b128 a[164:167], v224 offset:18848\n\t"
"v_mfma_f32_16x16x32_bf16 v[120:123], a[128:131], a[152:155], v[120:123]\n\t"
"ds_read_b128 a[160:163], v224 offset:18592\n\t"
"v_mfma_f32_16x16x32_bf16 v[120:123], a[132:135], a[168:171], v[120:123]\n\t"
"buffer_load_dwordx4 a[80:83], off, s[92:95], s63 offset:2048 sc1\n\t"
"v_mfma_f32_16x16x32_bf16 v[136:139], a[136:139], a[152:155], v[136:139]\n\t"
"v_mfma_f32_16x16x32_bf16 v[136:139], a[140:143], a[168:171], v[136:139]\n\t"
"ds_read_b128 a[152:155], v224 offset:17056\n\t"
"v_mfma_f32_16x16x32_bf16 v[140:143], a[136:139], a[156:159], v[140:143]\n\t"
"ds_read_b128 a[168:171], v224 offset:19104\n\t"
"v_mfma_f32_16x16x32_bf16 v[140:143], a[140:143], a[172:175], v[140:143]\n\t"
"buffer_load_dwordx4 a[84:87], off, s[92:95], s63 offset:3072 sc1\n\t"
"v_mfma_f32_16x16x32_bf16 v[124:127], a[128:131], a[156:159], v[124:127]\n\t"
"v_mfma_f32_16x16x32_bf16 v[124:127], a[132:135], a[172:175], v[124:127]\n\t"
"v_mfma_f32_16x16x32_bf16 v[108:111], a[120:123], a[156:159], v[108:111]\n\t"
"ds_read_b128 a[156:159], v224 offset:17312\n\t"
"v_mfma_f32_16x16x32_bf16 v[108:111], a[124:127], a[172:175], v[108:111]\n\t"
"s_waitcnt lgkmcnt(7)\n\t"
"buffer_load_dwordx4 a[88:91], off, s[96:99], s63 offset:0 sc1\n\t"
"v_mfma_f32_4x4x4_16b_bf16 v[200:203], a[252:253], a[178:179], 0\n\t"
"ds_read_b128 a[172:175], v224 offset:19360\n\t"
"v_mfma_f32_4x4x4_16b_bf16 v[204:207], a[252:253], a[186:187], 0\n\t"
"ds_read_b64_tr_b16 a[178:179], v248 offset:20864\n\t"
"v_mfma_f32_4x4x4_16b_bf16 v[216:219], a[252:253], a[194:195], 0\n\t"
"ds_read_b64_tr_b16 a[186:187], v249 offset:20864\n\t"
"v_mfma_f32_4x4x4_16b_bf16 v[220:223], a[252:253], a[202:203], 0\n\t"
"buffer_load_dwordx4 a[92:95], off, s[96:99], s63 offset:1024 sc1\n\t"
"v_mfma_f32_4x4x4_16b_bf16 v[192:195], a[254:255], a[176:177], v[200:203]\n\t"
"ds_read_b64_tr_b16 a[194:195], v250 offset:20864\n\t"
"v_mfma_f32_4x4x4_16b_bf16 v[196:199], a[254:255], a[184:185], v[204:207]\n\t"
"ds_read_b64_tr_b16 a[202:203], v251 offset:20864\n\t"
"v_mfma_f32_4x4x4_16b_bf16 v[200:203], a[254:255], a[180:181], v[200:203]\n\t"
"ds_read_b64_tr_b16 a[176:177], v248 offset:20736\n\t"
"v_mfma_f32_4x4x4_16b_bf16 v[204:207], a[254:255], a[188:189], v[204:207]\n\t"
"s_mov_b32 s75, 221184\n\t"
"buffer_load_dwordx4 a[96:99], off, s[92:95], s75 offset:0 sc1\n\t"
"v_mfma_f32_4x4x4_16b_bf16 v[208:211], a[254:255], a[192:193], v[216:219]\n\t"
"ds_read_b64_tr_b16 a[184:185], v249 offset:20736\n\t"
"v_mfma_f32_4x4x4_16b_bf16 v[212:215], a[254:255], a[200:201], v[220:223]\n\t"
"ds_read_b64_tr_b16 a[180:181], v249 offset:20992\n\t"
"v_mfma_f32_4x4x4_16b_bf16 v[216:219], a[254:255], a[196:197], v[216:219]\n\t"
"ds_read_b64_tr_b16 a[188:189], v248 offset:20992\n\t"
"v_mfma_f32_4x4x4_16b_bf16 v[220:223], a[254:255], a[204:205], v[220:223]\n\t"
"s_waitcnt vmcnt(24) lgkmcnt(12)\n\t"
"buffer_load_dwordx4 a[100:103], off, s[92:95], s75 offset:1024 sc1\n\t"
"v_mfma_f32_16x16x32_bf16 v[0:3], a[0:3], a[144:147], v[0:3]\n\t"
"ds_read_b64_tr_b16 a[192:193], v250 offset:20736\n\t"
"v_cvt_pk_bf16_f32 v194, -v194, -v198\n\t"
"v_mfma_f32_16x16x32_bf16 v[0:3], a[4:7], a[160:163], v[0:3]\n\t"
"s_waitcnt vmcnt(22)\n\t"
"buffer_load_dwordx4 a[104:107], off, s[92:95], s75 offset:2048 sc1\n\t"
"v_mfma_f32_16x16x32_bf16 v[16:19], a[8:11], a[144:147], v[16:19]\n\t"
"ds_read_b64_tr_b16 a[200:201], v251 offset:20736\n\t"
"v_cvt_pk_bf16_f32 v201, v201, v205\n\t"
"v_mfma_f32_16x16x32_bf16 v[16:19], a[12:15], a[160:163], v[16:19]\n\t"
"s_waitcnt vmcnt(21)\n\t"
"buffer_load_dwordx4 a[108:111], off, s[92:95], s75 offset:3072 sc1\n\t"
"v_mfma_f32_16x16x32_bf16 v[32:35], a[16:19], a[144:147], v[32:35]\n\t"
"ds_read_b128 a[144:147], v224 offset:17568\n\t"
"v_cvt_pk_bf16_f32 v208, -v208, -v212\n\t"
"v_mfma_f32_16x16x32_bf16 v[32:35], a[20:23], a[160:163], v[32:35]\n\t"
"v_cvt_pk_bf16_f32 v200, v200, v204\n\t"
"v_cvt_pk_bf16_f32 v202, v202, v206\n\t"
"v_mfma_f32_16x16x32_bf16 v[36:39], a[16:19], a[148:151], v[36:39]\n\t"
"ds_read_b128 a[160:163], v224 offset:19616\n\t"
"v_cvt_pk_bf16_f32 v192, -v192, -v196\n\t"
"v_mfma_f32_16x16x32_bf16 v[36:39], a[20:23], a[164:167], v[36:39]\n\t"
"buffer_load_dwordx4 a[112:115], off, s[96:99], s75 offset:0 sc1\n\t"
"v_mfma_f32_16x16x32_bf16 v[20:23], a[8:11], a[148:151], v[20:23]\n\t"
"ds_write_addtid_b32 v200 offset:8320\n\t"
"v_cvt_pk_bf16_f32 v211, -v211, -v215\n\t"
"v_mfma_f32_16x16x32_bf16 v[20:23], a[12:15], a[164:167], v[20:23]\n\t"
"ds_write_addtid_b32 v202 offset:9344\n\t"
"v_cvt_pk_bf16_f32 v219, v219, v223\n\t"
"v_mfma_f32_16x16x32_bf16 v[4:7], a[0:3], a[148:151], v[4:7]\n\t"
"s_mov_b32 s55, 106496\n\t"
"ds_read_b128 a[148:151], v224 offset:17824\n\t"
"v_cvt_pk_bf16_f32 v203, v203, v207\n\t"
"v_mfma_f32_16x16x32_bf16 v[4:7], a[4:7], a[164:167], v[4:7]\n\t"
"s_waitcnt lgkmcnt(15)\n\t"
"buffer_load_dwordx4 a[116:119], off, s[96:99], s75 offset:1024 sc1\n\t"
"v_mfma_f32_16x16x32_bf16 v[8:11], a[0:3], a[152:155], v[8:11]\n\t"
"ds_read_b128 a[164:167], v224 offset:19872\n\t"
"v_cvt_pk_bf16_f32 v210, -v210, -v214\n\t"
"v_mfma_f32_16x16x32_bf16 v[8:11], a[4:7], a[168:171], v[8:11]\n\t"
"ds_write_addtid_b32 v210 offset:3072\n\t"
"v_cvt_pk_bf16_f32 v193, -v193, -v197\n\t"
"v_mfma_f32_16x16x32_bf16 v[24:27], a[8:11], a[152:155], v[24:27]\n\t"
"ds_write_addtid_b32 v194 offset:1024\n\t"
"v_cvt_pk_bf16_f32 v216, v216, v220\n\t"
"v_mfma_f32_16x16x32_bf16 v[24:27], a[12:15], a[168:171], v[24:27]\n\t"
"s_mov_b32 s78, 245760\n\t"
"buffer_load_dwordx4 a[120:123], off, s[92:95], s78 offset:0 sc1\n\t"
"v_mfma_f32_16x16x32_bf16 v[40:43], a[16:19], a[152:155], v[40:43]\n\t"
"ds_read_b128 a[152:155], v224 offset:18080\n\t"
"v_cvt_pk_bf16_f32 v217, v217, v221\n\t"
"v_mfma_f32_16x16x32_bf16 v[40:43], a[20:23], a[168:171], v[40:43]\n\t"
"ds_read_b128 a[168:171], v224 offset:20128\n\t"
"v_cvt_pk_bf16_f32 v209, -v209, -v213\n\t"
"v_mfma_f32_16x16x32_bf16 v[44:47], a[16:19], a[156:159], v[44:47]\n\t"
"ds_write_addtid_b32 v201 offset:12448\n\t"
"v_cvt_pk_bf16_f32 v195, -v195, -v199\n\t"
"v_mfma_f32_16x16x32_bf16 v[44:47], a[20:23], a[172:175], v[44:47]\n\t"
"buffer_load_dwordx4 a[124:127], off, s[92:95], s78 offset:1024 sc1\n\t"
"v_mfma_f32_16x16x32_bf16 v[28:31], a[8:11], a[156:159], v[28:31]\n\t"
"s_mov_b32 s51, 73728\n\t"
"ds_write_addtid_b32 v192 offset:0\n\t"
"ds_write_addtid_b32 v211 offset:7200\n\t"
"v_mfma_f32_16x16x32_bf16 v[28:31], a[12:15], a[172:175], v[28:31]\n\t"
"s_mov_b32 s56, 114688\n\t"
"ds_write_addtid_b32 v195 offset:5152\n\t"
"ds_write_addtid_b32 v208 offset:2048\n\t"
"v_mfma_f32_16x16x32_bf16 v[12:15], a[0:3], a[156:159], v[12:15]\n\t"
"ds_write_addtid_b32 v203 offset:13472\n\t"
"v_cvt_pk_bf16_f32 v218, v218, v222\n\t"
"v_mfma_f32_16x16x32_bf16 v[12:15], a[4:7], a[172:175], v[12:15]\n\t"
"s_waitcnt vmcnt(24) lgkmcnt(14)\n\t"
"buffer_load_dwordx4 a[128:131], off, s[92:95], s78 offset:2048 sc1\n\t"
"v_mfma_f32_16x16x32_bf16 v[96:99], a[24:27], a[144:147], v[96:99]\n\t"
"s_mov_b32 s52, 81920\n\t"
"ds_write_addtid_b32 v209 offset:6176\n\t"
"ds_write_addtid_b32 v219 offset:15520\n\t"
"v_mfma_f32_16x16x32_bf16 v[96:99], a[28:31], a[160:163], v[96:99]\n\t"
"s_waitcnt vmcnt(23)\n\t"
"ds_write_addtid_b32 v193 offset:4128\n\t"
"ds_write_addtid_b32 v218 offset:11392\n\t"
"s_mov_b32 s58, 131072\n\t"
"v_mfma_f32_16x16x32_bf16 v[112:115], a[32:35], a[144:147], v[112:115]\n\t"
"s_mov_b32 s53, 90112\n\t"
"ds_write_addtid_b32 v216 offset:10368\n\t"
"ds_write_addtid_b32 v217 offset:14496\n\t"
"v_mfma_f32_16x16x32_bf16 v[112:115], a[36:39], a[160:163], v[112:115]\n\t"
"s_waitcnt vmcnt(21)\n\t"
"buffer_load_dwordx4 a[132:135], off, s[92:95], s78 offset:3072 sc1\n\t"
"v_mfma_f32_16x16x32_bf16 v[128:131], a[40:43], a[144:147], v[128:131]\n\t"
"ds_read_b128 a[156:159], v224 offset:18336\n\t"
"v_mfma_f32_16x16x32_bf16 v[128:131], a[44:47], a[160:163], v[128:131]\n\t"
"s_waitcnt lgkmcnt(15)\n\t"
"buffer_load_dwordx4 a[136:139], off, s[96:99], s78 offset:0 sc1\n\t"
"v_mfma_f32_16x16x32_bf16 v[132:135], a[40:43], a[148:151], v[132:135]\n\t"
"ds_read_b128 a[172:175], v224 offset:20384\n\t"
"v_mfma_f32_16x16x32_bf16 v[132:135], a[44:47], a[164:167], v[132:135]\n\t"
"buffer_load_dwordx4 a[140:143], off, s[96:99], s78 offset:1024 sc1\n\t"
"v_mfma_f32_16x16x32_bf16 v[116:119], a[32:35], a[148:151], v[116:119]\n\t"
"s_mov_b32 s59, 139264\n\t"
"ds_read_b64_tr_b16 a[196:197], v251 offset:20992\n\t"
"ds_read_b64_tr_b16 a[204:205], v250 offset:20992\n\t"
"v_mfma_f32_16x16x32_bf16 v[116:119], a[36:39], a[164:167], v[116:119]\n\t"
"v_mfma_f32_16x16x32_bf16 v[100:103], a[24:27], a[148:151], v[100:103]\n\t"
"buffer_load_dwordx4 a[0:3], off, s[96:99], s58 offset:0 sc1\n\t"
"v_mfma_f32_16x16x32_bf16 v[100:103], a[28:31], a[164:167], v[100:103]\n\t"
"s_waitcnt lgkmcnt(2)\n\t"
"s_add_u32 s71, s71, 1\n\t"
"v_mov_b32 v215, s71\n\t"
"s_mov_b64 exec, 1\n\t"
"buffer_store_dword v215, off, s[8:11], s70 sc1\n\t"
"s_mov_b64 exec, -1\n\t"
"s_barrier\n\t"
"v_mfma_f32_16x16x32_bf16 v[104:107], a[24:27], a[152:155], v[104:107]\n\t"
"s_nop 0\n\t"
"ds_read_b128 a[144:147], v224 offset:0\n\t"
"ds_read_b128 a[160:163], v224 offset:2048\n\t"
"v_mfma_f32_16x16x32_bf16 v[104:107], a[28:31], a[168:171], v[104:107]\n\t"
"buffer_load_dwordx4 a[4:7], off, s[96:99], s58 offset:1024 sc1\n\t"
"v_mfma_f32_16x16x32_bf16 v[120:123], a[32:35], a[152:155], v[120:123]\n\t"
"ds_read_b128 a[148:151], v224 offset:256\n\t"
"v_mfma_f32_16x16x32_bf16 v[120:123], a[36:39], a[168:171], v[120:123]\n\t"
"ds_read_b128 a[164:167], v224 offset:2304\n\t"
"v_mfma_f32_16x16x32_bf16 v[136:139], a[40:43], a[152:155], v[136:139]\n\t"
"v_mfma_f32_16x16x32_bf16 v[136:139], a[44:47], a[168:171], v[136:139]\n\t"
"buffer_load_dwordx4 a[8:11], off, s[96:99], s58 offset:2048 sc1\n\t"
"v_mfma_f32_16x16x32_bf16 v[140:143], a[40:43], a[156:159], v[140:143]\n\t"
"v_accvgpr_read_b32 v248, a208\n\t"
"v_mfma_f32_16x16x32_bf16 v[140:143], a[44:47], a[172:175], v[140:143]\n\t"
"ds_read_b128 a[152:155], v224 offset:512\n\t"
"v_mfma_f32_16x16x32_bf16 v[124:127], a[32:35], a[156:159], v[124:127]\n\t"
"v_mfma_f32_16x16x32_bf16 v[124:127], a[36:39], a[172:175], v[124:127]\n\t"
"buffer_load_dwordx4 a[12:15], off, s[96:99], s58 offset:3072 sc1\n\t"
"v_mfma_f32_16x16x32_bf16 v[108:111], a[24:27], a[156:159], v[108:111]\n\t"
"v_mfma_f32_16x16x32_bf16 v[108:111], a[28:31], a[172:175], v[108:111]\n\t"
"s_waitcnt lgkmcnt(5)\n\t"
"buffer_load_dwordx4 a[16:19], off, s[92:95], s59 offset:0 sc1\n\t"
"v_mfma_f32_4x4x4_16b_bf16 v[200:203], a[252:253], a[178:179], 0\n\t"
"ds_read_b128 a[168:171], v224 offset:2560\n\t"
"v_mfma_f32_4x4x4_16b_bf16 v[204:207], a[252:253], a[186:187], 0\n\t"
"v_mfma_f32_4x4x4_16b_bf16 v[216:219], a[252:253], a[194:195], 0\n\t"
"v_mfma_f32_4x4x4_16b_bf16 v[220:223], a[252:253], a[202:203], 0\n\t"
".p2align 3\n\t"
"buffer_load_dwordx4 a[20:23], off, s[92:95], s59 offset:1024 sc1\n\t"
"v_pk_add_f32 v[96:97], v[0:1], v[96:97] neg_lo:[0,1] neg_hi:[0,1]\n\t"
"v_pk_add_f32 v[98:99], v[2:3], v[98:99] neg_lo:[0,1] neg_hi:[0,1]\n\t"
"v_pk_fma_f32 v[0:1], v[96:97], s[90:91], v[0:1] neg_lo:[1,0,0] neg_hi:[1,0,0]\n\t"
"v_pk_fma_f32 v[2:3], v[98:99], s[90:91], v[2:3] neg_lo:[1,0,0] neg_hi:[1,0,0]\n\t"
"v_pk_add_f32 v[112:113], v[16:17], v[112:113] neg_lo:[0,1] neg_hi:[0,1]\n\t"
"v_pk_add_f32 v[114:115], v[18:19], v[114:115] neg_lo:[0,1] neg_hi:[0,1]\n\t"
"v_pk_fma_f32 v[16:17], v[112:113], s[90:91], v[16:17] neg_lo:[1,0,0] neg_hi:[1,0,0]\n\t"
"v_pk_fma_f32 v[18:19], v[114:115], s[90:91], v[18:19] neg_lo:[1,0,0] neg_hi:[1,0,0]\n\t"
"v_pk_add_f32 v[128:129], v[32:33], v[128:129] neg_lo:[0,1] neg_hi:[0,1]\n\t"
"v_pk_add_f32 v[130:131], v[34:35], v[130:131] neg_lo:[0,1] neg_hi:[0,1]\n\t"
"v_pk_fma_f32 v[32:33], v[128:129], s[90:91], v[32:33] neg_lo:[1,0,0] neg_hi:[1,0,0]\n\t"
"v_pk_fma_f32 v[34:35], v[130:131], s[90:91], v[34:35] neg_lo:[1,0,0] neg_hi:[1,0,0]\n\t"
"v_pk_add_f32 v[132:133], v[36:37], v[132:133] neg_lo:[0,1] neg_hi:[0,1]\n\t"
"v_pk_add_f32 v[134:135], v[38:39], v[134:135] neg_lo:[0,1] neg_hi:[0,1]\n\t"
"v_pk_fma_f32 v[36:37], v[132:133], s[90:91], v[36:37] neg_lo:[1,0,0] neg_hi:[1,0,0]\n\t"
"v_pk_fma_f32 v[38:39], v[134:135], s[90:91], v[38:39] neg_lo:[1,0,0] neg_hi:[1,0,0]\n\t"
"v_pk_add_f32 v[116:117], v[20:21], v[116:117] neg_lo:[0,1] neg_hi:[0,1]\n\t"
"v_pk_add_f32 v[118:119], v[22:23], v[118:119] neg_lo:[0,1] neg_hi:[0,1]\n\t"
"v_pk_fma_f32 v[20:21], v[116:117], s[90:91], v[20:21] neg_lo:[1,0,0] neg_hi:[1,0,0]\n\t"
"v_pk_fma_f32 v[22:23], v[118:119], s[90:91], v[22:23] neg_lo:[1,0,0] neg_hi:[1,0,0]\n\t"
"v_pk_add_f32 v[100:101], v[4:5], v[100:101] neg_lo:[0,1] neg_hi:[0,1]\n\t"
"v_pk_add_f32 v[102:103], v[6:7], v[102:103] neg_lo:[0,1] neg_hi:[0,1]\n\t"
"v_pk_fma_f32 v[4:5], v[100:101], s[90:91], v[4:5] neg_lo:[1,0,0] neg_hi:[1,0,0]\n\t"
"v_pk_fma_f32 v[6:7], v[102:103], s[90:91], v[6:7] neg_lo:[1,0,0] neg_hi:[1,0,0]\n\t"
"v_pk_add_f32 v[104:105], v[8:9], v[104:105] neg_lo:[0,1] neg_hi:[0,1]\n\t"
"v_pk_add_f32 v[106:107], v[10:11], v[106:107] neg_lo:[0,1] neg_hi:[0,1]\n\t"
"v_pk_fma_f32 v[8:9], v[104:105], s[90:91], v[8:9] neg_lo:[1,0,0] neg_hi:[1,0,0]\n\t"
"v_pk_fma_f32 v[10:11], v[106:107], s[90:91], v[10:11] neg_lo:[1,0,0] neg_hi:[1,0,0]\n\t"
"v_pk_add_f32 v[120:121], v[24:25], v[120:121] neg_lo:[0,1] neg_hi:[0,1]\n\t"
"v_pk_add_f32 v[122:123], v[26:27], v[122:123] neg_lo:[0,1] neg_hi:[0,1]\n\t"
"v_pk_fma_f32 v[24:25], v[120:121], s[90:91], v[24:25] neg_lo:[1,0,0] neg_hi:[1,0,0]\n\t"
"v_pk_fma_f32 v[26:27], v[122:123], s[90:91], v[26:27] neg_lo:[1,0,0] neg_hi:[1,0,0]\n\t"
"v_pk_add_f32 v[136:137], v[40:41], v[136:137] neg_lo:[0,1] neg_hi:[0,1]\n\t"
"v_pk_add_f32 v[138:139], v[42:43], v[138:139] neg_lo:[0,1] neg_hi:[0,1]\n\t"
"v_pk_fma_f32 v[40:41], v[136:137], s[90:91], v[40:41] neg_lo:[1,0,0] neg_hi:[1,0,0]\n\t"
"v_pk_fma_f32 v[42:43], v[138:139], s[90:91], v[42:43] neg_lo:[1,0,0] neg_hi:[1,0,0]\n\t"
"v_pk_add_f32 v[140:141], v[44:45], v[140:141] neg_lo:[0,1] neg_hi:[0,1]\n\t"
"v_pk_add_f32 v[142:143], v[46:47], v[142:143] neg_lo:[0,1] neg_hi:[0,1]\n\t"
"v_pk_fma_f32 v[44:45], v[140:141], s[90:91], v[44:45] neg_lo:[1,0,0] neg_hi:[1,0,0]\n\t"
"v_pk_fma_f32 v[46:47], v[142:143], s[90:91], v[46:47] neg_lo:[1,0,0] neg_hi:[1,0,0]\n\t"
"v_pk_add_f32 v[124:125], v[28:29], v[124:125] neg_lo:[0,1] neg_hi:[0,1]\n\t"
"v_pk_add_f32 v[126:127], v[30:31], v[126:127] neg_lo:[0,1] neg_hi:[0,1]\n\t"
"v_pk_fma_f32 v[28:29], v[124:125], s[90:91], v[28:29] neg_lo:[1,0,0] neg_hi:[1,0,0]\n\t"
"v_pk_fma_f32 v[30:31], v[126:127], s[90:91], v[30:31] neg_lo:[1,0,0] neg_hi:[1,0,0]\n\t"
"v_pk_add_f32 v[108:109], v[12:13], v[108:109] neg_lo:[0,1] neg_hi:[0,1]\n\t"
"v_pk_add_f32 v[110:111], v[14:15], v[110:111] neg_lo:[0,1] neg_hi:[0,1]\n\t"
"v_pk_fma_f32 v[12:13], v[108:109], s[90:91], v[12:13] neg_lo:[1,0,0] neg_hi:[1,0,0]\n\t"
"v_pk_fma_f32 v[14:15], v[110:111], s[90:91], v[14:15] neg_lo:[1,0,0] neg_hi:[1,0,0]\n\t"
"v_mfma_f32_4x4x4_16b_bf16 v[192:195], a[254:255], a[176:177], v[200:203]\n\t"
"ds_read_b128 a[172:175], v224 offset:2816\n\t"
"v_mfma_f32_4x4x4_16b_bf16 v[196:199], a[254:255], a[184:185], v[204:207]\n\t"
"v_mfma_f32_4x4x4_16b_bf16 v[200:203], a[254:255], a[180:181], v[200:203]\n\t"
"buffer_load_dwordx4 a[24:27], off, s[96:99], s57 offset:2048 sc1\n\t"
"v_mfma_f32_4x4x4_16b_bf16 v[204:207], a[254:255], a[188:189], v[204:207]\n\t"
"v_mfma_f32_4x4x4_16b_bf16 v[208:211], a[254:255], a[192:193], v[216:219]\n\t"
"ds_read_b128 a[156:159], v224 offset:768\n\t"
"v_mfma_f32_4x4x4_16b_bf16 v[212:215], a[254:255], a[200:201], v[220:223]\n\t"
"v_mfma_f32_4x4x4_16b_bf16 v[216:219], a[254:255], a[196:197], v[216:219]\n\t"
"v_mfma_f32_4x4x4_16b_bf16 v[220:223], a[254:255], a[204:205], v[220:223]\n\t"
"s_waitcnt vmcnt(30) lgkmcnt(6)\n\t"
"buffer_load_dwordx4 a[28:31], off, s[96:99], s57 offset:3072 sc1\n\t"
"v_mfma_f32_16x16x32_bf16 v[0:3], a[48:51], a[144:147], v[0:3]\n\t"
"s_nop 0\n\t"
"v_cvt_pk_bf16_f32 v192, -v192, -v196\n\t"
"v_cvt_pk_bf16_f32 v194, -v194, -v198\n\t"
"v_mfma_f32_16x16x32_bf16 v[0:3], a[52:55], a[160:163], v[0:3]\n\t"
"s_waitcnt vmcnt(29)\n\t"
"ds_write_addtid_b32 v192 offset:16544\n\t"
"ds_write_addtid_b32 v194 offset:17568\n\t"
"s_mov_b32 s64, 155648\n\t"
"v_mfma_f32_16x16x32_bf16 v[16:19], a[56:59], a[144:147], v[16:19]\n\t"
"s_nop 0\n\t"
"v_cvt_pk_bf16_f32 v193, -v193, -v197\n\t"
"v_cvt_pk_bf16_f32 v195, -v195, -v199\n\t"
"v_mfma_f32_16x16x32_bf16 v[16:19], a[60:63], a[160:163], v[16:19]\n\t"
"s_waitcnt vmcnt(27)\n\t"
"buffer_load_dwordx4 a[32:35], off, s[92:95], s58 offset:0 sc1\n\t"
"v_mfma_f32_16x16x32_bf16 v[32:35], a[64:67], a[144:147], v[32:35]\n\t"
"s_nop 0\n\t"
"ds_read_b128 a[144:147], v224 offset:1024\n\t"
"v_cvt_pk_bf16_f32 v203, v203, v207\n\t"
"v_mfma_f32_16x16x32_bf16 v[32:35], a[68:71], a[160:163], v[32:35]\n\t"
"s_waitcnt lgkmcnt(7)\n\t"
"buffer_load_dwordx4 a[36:39], off, s[92:95], s58 offset:1024 sc1\n\t"
"v_mfma_f32_16x16x32_bf16 v[36:39], a[64:67], a[148:151], v[36:39]\n\t"
"s_nop 0\n\t"
"ds_read_b128 a[160:163], v224 offset:3072\n\t"
"v_cvt_pk_bf16_f32 v201, v201, v205\n\t"
"v_mfma_f32_16x16x32_bf16 v[36:39], a[68:71], a[164:167], v[36:39]\n\t"
"s_nop 0\n\t"
"v_cvt_pk_bf16_f32 v200, v200, v204\n\t"
"v_cvt_pk_bf16_f32 v217, v217, v221\n\t"
"v_mfma_f32_16x16x32_bf16 v[20:23], a[56:59], a[148:151], v[20:23]\n\t"
"s_nop 0\n\t"
"ds_write_addtid_b32 v201 offset:28992\n\t"
"v_cvt_pk_bf16_f32 v208, -v208, -v212\n\t"
"v_mfma_f32_16x16x32_bf16 v[20:23], a[60:63], a[164:167], v[20:23]\n\t"
"buffer_load_dwordx4 a[40:43], off, s[92:95], s58 offset:2048 sc1\n\t"
"v_mfma_f32_16x16x32_bf16 v[4:7], a[48:51], a[148:151], v[4:7]\n\t"
"s_nop 0\n\t"
"ds_read_b128 a[148:151], v224 offset:1280\n\t"
"v_cvt_pk_bf16_f32 v218, v218, v222\n\t"
"v_mfma_f32_16x16x32_bf16 v[4:7], a[52:55], a[164:167], v[4:7]\n\t"
"s_waitcnt lgkmcnt(8)\n\t"
"ds_read_b128 a[164:167], v224 offset:3328\n\t"
"v_cvt_pk_bf16_f32 v219, v219, v223\n\t"
"v_mfma_f32_16x16x32_bf16 v[8:11], a[48:51], a[152:155], v[8:11]\n\t"
"s_nop 0\n\t"
"ds_write_addtid_b32 v208 offset:18592\n\t"
"v_cvt_pk_bf16_f32 v209, -v209, -v213\n\t"
"v_mfma_f32_16x16x32_bf16 v[8:11], a[52:55], a[168:171], v[8:11]\n\t"
"buffer_load_dwordx4 a[44:47], off, s[92:95], s58 offset:3072 sc1\n\t"
"v_mfma_f32_16x16x32_bf16 v[24:27], a[56:59], a[152:155], v[24:27]\n\t"
"s_mov_b32 s67, 180224\n\t"
"ds_write_addtid_b32 v193 offset:20672\n\t"
"ds_write_addtid_b32 v209 offset:22720\n\t"
"v_mfma_f32_16x16x32_bf16 v[24:27], a[60:63], a[168:171], v[24:27]\n\t"
"s_nop 0\n\t"
"ds_write_addtid_b32 v217 offset:31040\n\t"
"v_mfma_f32_16x16x32_bf16 v[40:43], a[64:67], a[152:155], v[40:43]\n\t"
"s_nop 0\n\t"
"ds_write_addtid_b32 v218 offset:27936\n\t"
"v_cvt_pk_bf16_f32 v211, -v211, -v215\n\t"
"v_mfma_f32_16x16x32_bf16 v[40:43], a[68:71], a[168:171], v[40:43]\n\t"
"s_waitcnt lgkmcnt(12)\n\t"
"ds_write_addtid_b32 v219 offset:32064\n\t"
"v_cvt_pk_bf16_f32 v202, v202, v206\n\t"
"v_mfma_f32_16x16x32_bf16 v[44:47], a[64:67], a[156:159], v[44:47]\n\t"
"s_add_u32 m0, s43, 33280\n\t"
"ds_read_b128 a[168:171], v224 offset:3584\n\t"
"v_mfma_f32_16x16x32_bf16 v[44:47], a[68:71], a[172:175], v[44:47]\n\t"
"s_mov_b64 exec, s[44:45]\n\t"
"buffer_load_dwordx4 v247, s[12:15], 0 offen offset:0 nt lds\n\t"
"s_mov_b64 exec, -1\n\t"
"v_mfma_f32_16x16x32_bf16 v[28:31], a[56:59], a[156:159], v[28:31]\n\t"
"s_add_u32 m0, s26, 35584\n\t"
"ds_read_b128 a[152:155], v224 offset:1536\n\t"
"v_mfma_f32_16x16x32_bf16 v[28:31], a[60:63], a[172:175], v[28:31]\n\t"
"buffer_load_dwordx4 v236, s[12:15], s22 offen offset:0 nt lds\n\t"
"v_mfma_f32_16x16x32_bf16 v[12:15], a[48:51], a[156:159], v[12:15]\n\t"
"s_lshl_b32 m0, s73, 8\n\t"
"v_cvt_pk_bf16_f32 v210, -v210, -v214\n\t"
"v_cvt_pk_bf16_f32 v216, v216, v220\n\t"
"v_mfma_f32_16x16x32_bf16 v[12:15], a[52:55], a[172:175], v[12:15]\n\t"
"s_waitcnt vmcnt(31) lgkmcnt(11)\n\t"
"ds_write_addtid_b32 v210 offset:19616\n\t"
"ds_write_addtid_b32 v216 offset:26912\n\t"
"s_mov_b32 s74, 212992\n\t"
"v_mfma_f32_16x16x32_bf16 v[96:99], a[72:75], a[144:147], v[96:99]\n\t"
"s_nop 0\n\t"
"ds_write_addtid_b32 v195 offset:21696\n\t"
"v_mfma_f32_16x16x32_bf16 v[96:99], a[76:79], a[160:163], v[96:99]\n\t"
"s_waitcnt vmcnt(29)\n\t"
"ds_write_addtid_b32 v200 offset:24864\n\t"
"ds_write_addtid_b32 v203 offset:30016\n\t"
"s_mov_b32 s68, 188416\n\t"
"v_mfma_f32_16x16x32_bf16 v[112:115], a[80:83], a[144:147], v[112:115]\n\t"
"s_mov_b32 s72, 204800\n\t"
"ds_write_addtid_b32 v202 offset:25888\n\t"
"ds_write_addtid_b32 v211 offset:23744\n\t"
"v_mfma_f32_16x16x32_bf16 v[112:115], a[84:87], a[160:163], v[112:115]\n\t"
"s_waitcnt vmcnt(27)\n\t"
"v_mfma_f32_16x16x32_bf16 v[128:131], a[88:91], a[144:147], v[128:131]\n\t"
"s_add_u32 m0, s26, 35584\n\t"
"v_add_u32 v248, 101632, v248\n\t"
"v_xor_b32 v249, 32, v248\n\t"
"v_mfma_f32_16x16x32_bf16 v[128:131], a[92:95], a[160:163], v[128:131]\n\t"
"s_waitcnt lgkmcnt(15)\n\t"
"ds_read_b128 a[172:175], v224 offset:3840\n\t"
"s_mov_b32 s65, 163840\n\t"
"v_mfma_f32_16x16x32_bf16 v[132:135], a[88:91], a[148:151], v[132:135]\n\t"
"s_nop 0\n\t"
"ds_read_b128 a[156:159], v224 offset:1792\n\t"
"v_mfma_f32_16x16x32_bf16 v[132:135], a[92:95], a[164:167], v[132:135]\n\t"
"s_nop 0\n\t"
"v_xor_b32 v250, 64, v248\n\t"
"v_mfma_f32_16x16x32_bf16 v[116:119], a[80:83], a[148:151], v[116:119]\n\t"
"s_nop 0\n\t"
"v_mfma_f32_16x16x32_bf16 v[116:119], a[84:87], a[164:167], v[116:119]\n\t"
"s_nop 0\n\t"
"v_xor_b32 v251, 96, v248\n\t"
"v_mfma_f32_16x16x32_bf16 v[100:103], a[72:75], a[148:151], v[100:103]\n\t"
"buffer_load_dwordx4 v237, s[12:15], s22 offen offset:1024 nt lds\n\t"
"v_mfma_f32_16x16x32_bf16 v[100:103], a[76:79], a[164:167], v[100:103]\n\t"
"s_lshl_b32 m0, s73, 8\n\t"
"s_waitcnt lgkmcnt(0)\n\t"
"s_barrier\n\t"
"v_mfma_f32_16x16x32_bf16 v[104:107], a[72:75], a[152:155], v[104:107]\n\t"
"s_add_u32 m0, s26, 35584\n\t"
"ds_read_b128 a[144:147], v224 offset:16544\n\t"
"ds_read_b128 a[160:163], v224 offset:18592\n\t"
"v_mfma_f32_16x16x32_bf16 v[104:107], a[76:79], a[168:171], v[104:107]\n\t"
"s_nop 0\n\t"
"ds_read_b128 a[164:167], v224 offset:18848\n\t"
"v_mfma_f32_16x16x32_bf16 v[120:123], a[80:83], a[152:155], v[120:123]\n\t"
"s_nop 0\n\t"
"ds_read_b128 a[148:151], v224 offset:16800\n\t"
"v_mfma_f32_16x16x32_bf16 v[120:123], a[84:87], a[168:171], v[120:123]\n\t"
"buffer_load_dwordx4 v238, s[12:15], s22 offen offset:2048 nt lds\n\t"
"v_mfma_f32_16x16x32_bf16 v[136:139], a[88:91], a[152:155], v[136:139]\n\t"
"s_mov_b32 s76, 229376\n\t"
"ds_read_b64_tr_b16 a[176:177], v248 offset:20736\n\t"
"ds_read_b64_tr_b16 a[178:179], v249 offset:20736\n\t"
"v_mfma_f32_16x16x32_bf16 v[136:139], a[92:95], a[168:171], v[136:139]\n\t"
"s_nop 0\n\t"
"ds_read_b64_tr_b16 a[180:181], v248 offset:21248\n\t"
"ds_read_b64_tr_b16 a[182:183], v249 offset:21248\n\t"
"v_mfma_f32_16x16x32_bf16 v[140:143], a[88:91], a[156:159], v[140:143]\n\t"
"s_nop 0\n\t"
"ds_read_b64_tr_b16 a[184:185], v250 offset:23040\n\t"
"v_mfma_f32_16x16x32_bf16 v[140:143], a[92:95], a[172:175], v[140:143]\n\t"
"s_nop 0\n\t"
"ds_read_b64_tr_b16 a[186:187], v251 offset:23040\n\t"
"v_mfma_f32_16x16x32_bf16 v[124:127], a[80:83], a[156:159], v[124:127]\n\t"
"s_nop 0\n\t"
"ds_read_b64_tr_b16 a[188:189], v250 offset:23552\n\t"
"v_mfma_f32_16x16x32_bf16 v[124:127], a[84:87], a[172:175], v[124:127]\n\t"
"s_waitcnt lgkmcnt(3)\n\t"
"ds_read_b64_tr_b16 a[190:191], v251 offset:23552\n\t"
"v_mfma_f32_4x4x4_16b_bf16 v[192:195], a[210:211], a[176:177], 0\n\t"
"ds_read_b64_tr_b16 a[192:193], v250 offset:20736\n\t"
"v_mfma_f32_4x4x4_16b_bf16 v[196:199], a[210:211], a[178:179], 0\n\t"
"ds_read_b128 a[152:155], v224 offset:17056\n\t"
"v_mfma_f32_4x4x4_16b_bf16 v[200:203], a[210:211], a[180:181], 0\n\t"
"ds_read_b64_tr_b16 a[194:195], v251 offset:20736\n\t"
"v_mfma_f32_4x4x4_16b_bf16 v[204:207], a[210:211], a[182:183], 0\n\t"
"buffer_load_dwordx4 v239, s[12:15], s22 offen offset:3072 nt lds\n\t"
"v_mfma_f32_16x16x32_bf16 v[108:111], a[72:75], a[156:159], v[108:111]\n\t"
"s_nop 0\n\t"
"ds_read_b64_tr_b16 a[196:197], v250 offset:21248\n\t"
"ds_read_b64_tr_b16 a[198:199], v251 offset:21248\n\t"
"v_mfma_f32_16x16x32_bf16 v[108:111], a[76:79], a[172:175], v[108:111]\n\t"
"s_waitcnt vmcnt(28)\n\t"
"ds_read_b64_tr_b16 a[200:201], v248 offset:23040\n\t"
"ds_read_b64_tr_b16 a[202:203], v249 offset:23040\n\t"
"s_mov_b32 s77, 237568\n\t"
"v_mfma_f32_16x16x32_bf16 v[0:3], a[96:99], a[144:147], v[0:3]\n\t"
"s_nop 0\n\t"
"v_cvt_pk_bf16_f32 v196, v192, v196\n\t"
"v_cvt_pk_bf16_f32 v194, v194, v198\n\t"
"v_mfma_f32_16x16x32_bf16 v[0:3], a[100:103], a[160:163], v[0:3]\n\t"
"s_waitcnt vmcnt(26)\n\t"
"v_cvt_pk_bf16_f32 v197, v193, v197\n\t"
"v_cvt_pk_bf16_f32 v195, v195, v199\n\t"
"v_mfma_f32_16x16x32_bf16 v[16:19], a[104:107], a[144:147], v[16:19]\n\t"
"s_nop 0\n\t"
"ds_read_b128 a[156:159], v224 offset:17312\n\t"
"v_cvt_pk_bf16_f32 v199, v201, v205\n\t"
"v_mfma_f32_16x16x32_bf16 v[16:19], a[108:111], a[160:163], v[16:19]\n\t"
"s_waitcnt vmcnt(24)\n\t"
"ds_read_b128 a[168:171], v224 offset:19104\n\t"
"v_cvt_pk_bf16_f32 v193, v203, v207\n\t"
"v_mfma_f32_16x16x32_bf16 v[32:35], a[112:115], a[144:147], v[32:35]\n\t"
"s_nop 0\n\t"
"ds_read_b64_tr_b16 a[204:205], v248 offset:23552\n\t"
"v_cvt_pk_bf16_f32 v192, v202, v206\n\t"
"v_mfma_f32_16x16x32_bf16 v[32:35], a[116:119], a[160:163], v[32:35]\n\t"
"s_waitcnt lgkmcnt(10)\n\t"
"s_mov_b64 exec, s[36:37]\n\t"
"buffer_load_dwordx4 v240, s[12:15], s22 offen offset:3584 nt lds\n\t"
"s_mov_b64 exec, -1\n\t"
"v_mfma_f32_4x4x4_16b_bf16 v[208:211], a[210:211], a[184:185], 0\n\t"
"ds_read_b128 a[160:163], v224 offset:19616\n\t"
"v_mfma_f32_4x4x4_16b_bf16 v[212:215], a[210:211], a[186:187], 0\n\t"
"ds_read_b64_tr_b16 a[206:207], v249 offset:23552\n\t"
"v_mfma_f32_4x4x4_16b_bf16 v[216:219], a[210:211], a[188:189], 0\n\t"
"ds_read_b128 a[172:175], v224 offset:19360\n\t"
"v_mfma_f32_4x4x4_16b_bf16 v[220:223], a[210:211], a[190:191], 0\n\t"
"buffer_load_dwordx4 a[48:51], off, s[96:99], s64 offset:0 sc1\n\t"
"v_mfma_f32_16x16x32_bf16 v[36:39], a[112:115], a[148:151], v[36:39]\n\t"
"s_lshl_b32 m0, s73, 8\n\t"
"ds_read_b128 a[144:147], v224 offset:17568\n\t"
"v_cvt_pk_bf16_f32 v198, v200, v204\n\t"
"v_mfma_f32_16x16x32_bf16 v[36:39], a[116:119], a[164:167], v[36:39]\n\t"
"buffer_load_dwordx4 a[52:55], off, s[96:99], s64 offset:1024 sc1\n\t"
"v_mfma_f32_16x16x32_bf16 v[20:23], a[104:107], a[148:151], v[20:23]\n\t"
"s_nop 0\n\t"
"v_cvt_pk_bf16_f32 v212, v208, v212\n\t"
"v_cvt_pk_bf16_f32 v213, v209, v213\n\t"
"v_mfma_f32_16x16x32_bf16 v[20:23], a[108:111], a[164:167], v[20:23]\n\t"
"s_nop 0\n\t"
"ds_write_addtid_b32 v199 offset:9344\n\t"
"buffer_load_dwordx4 a[56:59], off, s[96:99], s64 offset:2048 sc1\n\t"
"v_mfma_f32_16x16x32_bf16 v[4:7], a[96:99], a[148:151], v[4:7]\n\t"
"s_nop 0\n\t"
"ds_write_addtid_b32 v196 offset:0\n\t"
"v_cvt_pk_bf16_f32 v211, v211, v215\n\t"
"v_mfma_f32_16x16x32_bf16 v[4:7], a[100:103], a[164:167], v[4:7]\n\t"
"s_waitcnt lgkmcnt(11)\n\t"
"ds_write_addtid_b32 v197 offset:1024\n\t"
"v_cvt_pk_bf16_f32 v208, v218, v222\n\t"
"v_mfma_f32_4x4x4_16b_bf16 v[200:203], a[210:211], a[192:193], 0\n\t"
"ds_write_addtid_b32 v198 offset:8320\n\t"
"buffer_load_dwordx4 a[60:63], off, s[96:99], s64 offset:3072 sc1\n\t"
"v_cvt_pk_bf16_f32 v215, v217, v221\n\t"
"v_mfma_f32_4x4x4_16b_bf16 v[204:207], a[210:211], a[194:195], 0\n\t"
"ds_read_b128 a[164:167], v224 offset:19872\n\t"
"v_cvt_pk_bf16_f32 v210, v210, v214\n\t"
"v_mfma_f32_4x4x4_16b_bf16 v[196:199], a[210:211], a[196:197], 0\n\t"
"v_cvt_pk_bf16_f32 v214, v216, v220\n\t"
"v_cvt_pk_bf16_f32 v209, v219, v223\n\t"
"v_mfma_f32_4x4x4_16b_bf16 v[216:219], a[210:211], a[198:199], 0\n\t"
"s_waitcnt lgkmcnt(10)\n\t"
"buffer_load_dwordx4 a[64:67], off, s[92:95], s65 offset:0 sc1\n\t"
"v_mfma_f32_16x16x32_bf16 v[8:11], a[96:99], a[152:155], v[8:11]\n\t"
"s_mov_b32 s80, 262144\n\t"
"ds_read_b128 a[148:151], v224 offset:17824\n\t"
"ds_write_addtid_b32 v212 offset:4128\n\t"
"v_mfma_f32_16x16x32_bf16 v[8:11], a[100:103], a[168:171], v[8:11]\n\t"
"buffer_load_dwordx4 a[68:71], off, s[92:95], s65 offset:1024 sc1\n\t"
"v_mfma_f32_16x16x32_bf16 v[24:27], a[104:107], a[152:155], v[24:27]\n\t"
"s_nop 0\n\t"
"v_cvt_pk_bf16_f32 v202, v202, v206\n\t"
"v_cvt_pk_bf16_f32 v203, v203, v207\n\t"
"v_mfma_f32_16x16x32_bf16 v[24:27], a[108:111], a[168:171], v[24:27]\n\t"
"buffer_load_dwordx4 a[72:75], off, s[96:99], s63 offset:2048 sc1\n\t"
"v_mfma_f32_16x16x32_bf16 v[40:43], a[112:115], a[152:155], v[40:43]\n\t"
"s_nop 0\n\t"
"ds_read_b128 a[152:155], v224 offset:18080\n\t"
"v_cvt_pk_bf16_f32 v206, v196, v216\n\t"
"v_mfma_f32_16x16x32_bf16 v[40:43], a[116:119], a[168:171], v[40:43]\n\t"
"s_waitcnt lgkmcnt(9)\n\t"
"buffer_load_dwordx4 a[76:79], off, s[96:99], s63 offset:3072 sc1\n\t"
"v_mfma_f32_16x16x32_bf16 v[44:47], a[112:115], a[156:159], v[44:47]\n\t"
"s_nop 0\n\t"
"ds_write_addtid_b32 v215 offset:13472\n\t"
"v_cvt_pk_bf16_f32 v204, v200, v204\n\t"
"v_mfma_f32_16x16x32_bf16 v[44:47], a[116:119], a[172:175], v[44:47]\n\t"
"Lpad2_%=:\n\t"
"s_cbranch_execz Lpad6_%=\n\t"
"ds_write_addtid_b32 v213 offset:5152\n\t"
"v_cvt_pk_bf16_f32 v205, v201, v205\n\t"
"v_mfma_f32_4x4x4_16b_bf16 v[220:223], a[210:211], a[200:201], 0\n\t"
"ds_write_addtid_b32 v214 offset:12448\n\t"
"buffer_load_dwordx4 a[80:83], off, s[92:95], s64 offset:0 sc1\n\t"
"v_mfma_f32_4x4x4_16b_bf16 v[212:215], a[210:211], a[202:203], 0\n\t"
"v_cvt_pk_bf16_f32 v200, v198, v218\n\t"
"v_cvt_pk_bf16_f32 v201, v199, v219\n\t"
"v_cvt_pk_bf16_f32 v207, v197, v217\n\t"
"v_mfma_f32_4x4x4_16b_bf16 v[196:199], a[210:211], a[204:205], 0\n\t"
"ds_write_addtid_b32 v206 offset:10368\n\t"
"v_mfma_f32_4x4x4_16b_bf16 v[216:219], a[210:211], a[206:207], 0\n\t"
"buffer_load_dwordx4 a[84:87], off, s[92:95], s64 offset:1024 sc1\n\t"
"v_mfma_f32_16x16x32_bf16 v[28:31], a[104:107], a[156:159], v[28:31]\n\t"
"s_mov_b32 s82, 278528\n\t"
"ds_write_addtid_b32 v204 offset:2048\n\t"
"ds_write_addtid_b32 v205 offset:3072\n\t"
"v_mfma_f32_16x16x32_bf16 v[28:31], a[108:111], a[172:175], v[28:31]\n\t"
"buffer_load_dwordx4 a[88:91], off, s[92:95], s64 offset:2048 sc1\n\t"
"v_mfma_f32_16x16x32_bf16 v[12:15], a[96:99], a[156:159], v[12:15]\n\t"
"ds_write_addtid_b32 v207 offset:11392\n\t"
"v_cvt_pk_bf16_f32 v212, v220, v212\n\t"
"v_mfma_f32_16x16x32_bf16 v[12:15], a[100:103], a[172:175], v[12:15]\n\t"
"s_waitcnt vmcnt(34) lgkmcnt(15)\n\t"
"v_cvt_pk_bf16_f32 v222, v222, v214\n\t"
"v_cvt_pk_bf16_f32 v213, v221, v213\n\t"
"v_mfma_f32_16x16x32_bf16 v[96:99], a[120:123], a[144:147], v[96:99]\n\t"
"ds_write_addtid_b32 v213 offset:7200\n\t"
"v_cvt_pk_bf16_f32 v223, v223, v215\n\t"
"v_mfma_f32_16x16x32_bf16 v[96:99], a[124:127], a[160:163], v[96:99]\n\t"
"s_waitcnt vmcnt(32)\n\t"
"ds_write_addtid_b32 v212 offset:6176\n\t"
"v_cvt_pk_bf16_f32 v214, v196, v216\n\t"
"v_cvt_pk_bf16_f32 v215, v197, v217\n\t"
"v_mfma_f32_16x16x32_bf16 v[112:115], a[128:131], a[144:147], v[112:115]\n\t"
"s_mov_b32 s79, 253952\n\t"
"ds_write_addtid_b32 v214 offset:14496\n\t"
"ds_write_addtid_b32 v215 offset:15520\n\t"
"v_mfma_f32_16x16x32_bf16 v[112:115], a[132:135], a[160:163], v[112:115]\n\t"
"s_waitcnt vmcnt(30)\n\t"
"buffer_load_dwordx4 a[92:95], off, s[92:95], s64 offset:3072 sc1\n\t"
"v_mfma_f32_16x16x32_bf16 v[128:131], a[136:139], a[144:147], v[128:131]\n\t"
"ds_read_b128 a[172:175], v224 offset:20384\n\t"
"v_cvt_pk_bf16_f32 v220, v198, v218\n\t"
"v_mfma_f32_16x16x32_bf16 v[128:131], a[140:143], a[160:163], v[128:131]\n\t"
"s_waitcnt lgkmcnt(14)\n\t"
"buffer_load_dwordx4 a[108:111], off, s[96:99], s46 offset:3072 sc1\n\t"
"v_mfma_f32_16x16x32_bf16 v[132:135], a[136:139], a[148:151], v[132:135]\n\t"
"s_mov_b32 s83, 286720\n\t"
"ds_read_b128 a[168:171], v224 offset:20128\n\t"
"ds_read_b128 a[156:159], v224 offset:18336\n\t"
"v_mfma_f32_16x16x32_bf16 v[132:135], a[140:143], a[164:167], v[132:135]\n\t"
"buffer_load_dwordx4 a[112:115], off, s[92:95], s47 offset:0 sc1\n\t"
"v_mfma_f32_16x16x32_bf16 v[116:119], a[128:131], a[148:151], v[116:119]\n\t"
"v_accvgpr_read_b32 v224, a213\n\t"
"v_cvt_pk_bf16_f32 v221, v199, v219\n\t"
"v_mfma_f32_16x16x32_bf16 v[116:119], a[132:135], a[164:167], v[116:119]\n\t"
"buffer_load_dwordx4 a[116:119], off, s[92:95], s47 offset:1024 sc1\n\t"
"v_mfma_f32_16x16x32_bf16 v[100:103], a[120:123], a[148:151], v[100:103]\n\t"
"v_mfma_f32_16x16x32_bf16 v[100:103], a[124:127], a[164:167], v[100:103]\n\t"
"v_add_u32 v246, 8, v228\n\t"
"v_add_u32 v247, 16, v228\n\t"
"s_waitcnt lgkmcnt(0)\n\t"
"s_barrier\n\t"
"Lunit_%=:\n\t"
"v_mfma_f32_16x16x32_bf16 v[104:107], a[120:123], a[152:155], v[104:107]\n\t"
"s_nop 0\n\t"
"ds_read_b128 a[144:147], v224 offset:1024\n\t"
"ds_read_b128 a[148:151], v224 offset:1280\n\t"
"v_mfma_f32_16x16x32_bf16 v[104:107], a[124:127], a[168:171], v[104:107]\n\t"
"buffer_load_dwordx4 a[104:107], off, s[96:99], s46 offset:2048 sc1\n\t"
"v_mfma_f32_16x16x32_bf16 v[120:123], a[128:131], a[152:155], v[120:123]\n\t"
"ds_read_b128 a[160:163], v224 offset:3072\n\t"
"v_mfma_f32_16x16x32_bf16 v[120:123], a[132:135], a[168:171], v[120:123]\n\t"
"ds_read_b128 a[164:167], v224 offset:3328\n\t"
"v_mfma_f32_16x16x32_bf16 v[136:139], a[136:139], a[152:155], v[136:139]\n\t"
"ds_write_addtid_b32 v208 offset:28992\n\t"
"v_mfma_f32_16x16x32_bf16 v[136:139], a[140:143], a[168:171], v[136:139]\n\t"
"buffer_load_dwordx4 a[96:99], off, s[96:99], s46 offset:0 sc1\n\t"
"v_mfma_f32_16x16x32_bf16 v[140:143], a[136:139], a[156:159], v[140:143]\n\t"
"ds_read_b128 a[168:171], v224 offset:3584\n\t"
"v_mfma_f32_16x16x32_bf16 v[140:143], a[140:143], a[172:175], v[140:143]\n\t"
"ds_read_b128 a[152:155], v224 offset:1536\n\t"
"v_mfma_f32_16x16x32_bf16 v[124:127], a[128:131], a[156:159], v[124:127]\n\t"
"ds_write_addtid_b32 v203 offset:19616\n\t"
"v_mfma_f32_16x16x32_bf16 v[124:127], a[132:135], a[172:175], v[124:127]\n\t"
"buffer_load_dwordx4 a[100:103], off, s[96:99], s46 offset:1024 sc1\n\t"
"v_mfma_f32_16x16x32_bf16 v[108:111], a[120:123], a[156:159], v[108:111]\n\t"
"ds_read_b128 a[156:159], v224 offset:1792\n\t"
"v_mfma_f32_16x16x32_bf16 v[108:111], a[124:127], a[172:175], v[108:111]\n\t"
"s_waitcnt vmcnt(34) lgkmcnt(6)\n\t"
"ds_write_addtid_b32 v194 offset:16544\n\t"
"v_mfma_f32_16x16x32_bf16 v[48:51], a[0:3], a[144:147], v[0:3]\n\t"
"ds_read_b128 a[172:175], v224 offset:3840\n\t"
"v_mfma_f32_16x16x32_bf16 v[48:51], a[4:7], a[160:163], v[48:51]\n\t"
"s_waitcnt vmcnt(32) lgkmcnt(7)\n\t"
"buffer_load_dwordx4 a[136:139], off, s[92:95], s46 offset:2048 sc1\n\t"
"v_mfma_f32_16x16x32_bf16 v[64:67], a[8:11], a[144:147], v[16:19]\n\t"
"ds_write_addtid_b32 v195 offset:17568\n\t"
"v_mfma_f32_16x16x32_bf16 v[64:67], a[12:15], a[160:163], v[64:67]\n\t"
"s_waitcnt vmcnt(31) lgkmcnt(7)\n\t"
"ds_write_addtid_b32 v202 offset:18592\n\t"
"v_mfma_f32_16x16x32_bf16 v[80:83], a[16:19], a[144:147], v[32:35]\n\t"
"ds_read_b128 a[144:147], v224 offset:0\n\t"
"v_mfma_f32_16x16x32_bf16 v[80:83], a[20:23], a[160:163], v[80:83]\n\t"
"buffer_load_dwordx4 a[140:143], off, s[92:95], s46 offset:3072 sc1\n\t"
"v_mfma_f32_16x16x32_bf16 v[84:87], a[16:19], a[148:151], v[36:39]\n\t"
"ds_read_b128 a[160:163], v224 offset:2048\n\t"
"v_mfma_f32_16x16x32_bf16 v[84:87], a[20:23], a[164:167], v[84:87]\n\t"
"ds_write_addtid_b32 v223 offset:23744\n\t"
"v_mfma_f32_16x16x32_bf16 v[68:71], a[8:11], a[148:151], v[20:23]\n\t"
"ds_write_addtid_b32 v201 offset:27936\n\t"
"v_mfma_f32_16x16x32_bf16 v[68:71], a[12:15], a[164:167], v[68:71]\n\t"
"buffer_load_dwordx4 a[132:135], off, s[92:95], s46 offset:1024 sc1\n\t"
"v_mfma_f32_16x16x32_bf16 v[52:55], a[0:3], a[148:151], v[4:7]\n\t"
"ds_read_b128 a[148:151], v224 offset:256\n\t"
"v_mfma_f32_16x16x32_bf16 v[52:55], a[4:7], a[164:167], v[52:55]\n\t"
"s_waitcnt lgkmcnt(7)\n\t"
"ds_write_addtid_b32 v221 offset:32064\n\t"
"v_mfma_f32_16x16x32_bf16 v[56:59], a[0:3], a[152:155], v[8:11]\n\t"
"ds_read_b128 a[164:167], v224 offset:2304\n\t"
"v_mfma_f32_16x16x32_bf16 v[56:59], a[4:7], a[168:171], v[56:59]\n\t"
"buffer_load_dwordx4 a[128:131], off, s[92:95], s46 offset:0 sc1\n\t"
"v_mfma_f32_16x16x32_bf16 v[72:75], a[8:11], a[152:155], v[24:27]\n\t"
"ds_write_addtid_b32 v211 offset:21696\n\t"
"v_mfma_f32_16x16x32_bf16 v[72:75], a[12:15], a[168:171], v[72:75]\n\t"
"ds_write_addtid_b32 v193 offset:25888\n\t"
"v_mfma_f32_16x16x32_bf16 v[88:91], a[16:19], a[152:155], v[40:43]\n\t"
"v_mfma_f32_16x16x32_bf16 v[88:91], a[20:23], a[168:171], v[88:91]\n\t"
"ds_write_b128 v254, a[248:251] offset:41472\n\t"
"v_mfma_f32_16x16x32_bf16 v[92:95], a[16:19], a[156:159], v[44:47]\n\t"
"v_mfma_f32_16x16x32_bf16 v[92:95], a[20:23], a[172:175], v[92:95]\n\t"
"buffer_load_dwordx4 a[120:123], off, s[96:99], s42 offset:2048 sc1\n\t"
"v_mfma_f32_16x16x32_bf16 v[76:79], a[8:11], a[156:159], v[28:31]\n\t"
"v_mfma_f32_16x16x32_bf16 v[76:79], a[12:15], a[172:175], v[76:79]\n\t"
"s_nop 0\n\t"
"ds_read_b128 a[168:171], v224 offset:2560\n\t"
"ds_write_addtid_b32 v209 offset:30016\n\t"
"v_mfma_f32_16x16x32_bf16 v[60:63], a[0:3], a[156:159], v[12:15]\n\t"
"ds_write_addtid_b32 v210 offset:20672\n\t"
"v_mfma_f32_16x16x32_bf16 v[60:63], a[4:7], a[172:175], v[60:63]\n\t"
"s_waitcnt vmcnt(33) lgkmcnt(6)\n\t"
"buffer_load_dwordx4 a[124:127], off, s[96:99], s42 offset:3072 sc1\n\t"
"v_mfma_f32_16x16x32_bf16 v[0:3], a[24:27], a[144:147], v[0:3]\n\t"
"ds_write_addtid_b32 v222 offset:22720\n\t"
"v_mfma_f32_16x16x32_bf16 v[0:3], a[28:31], a[160:163], v[0:3]\n\t"
"s_waitcnt vmcnt(32) lgkmcnt(5)\n\t"
"ds_write_addtid_b32 v192 offset:24864\n\t"
"ds_write_addtid_b32 v200 offset:26912\n\t"
"v_mfma_f32_16x16x32_bf16 v[16:19], a[32:35], a[144:147], v[16:19]\n\t"
"ds_write_addtid_b32 v220 offset:31040\n\t"
"v_mfma_f32_16x16x32_bf16 v[16:19], a[36:39], a[160:163], v[16:19]\n\t"
"s_waitcnt vmcnt(30) lgkmcnt(7)\n\t"
"buffer_load_dwordx4 a[16:19], off, s[92:95], s50 offset:0 sc1\n\t"
"v_mfma_f32_16x16x32_bf16 v[32:35], a[40:43], a[144:147], v[32:35]\n\t"
"ds_read_b128 a[156:159], v224 offset:768\n\t"
"v_mfma_f32_16x16x32_bf16 v[32:35], a[44:47], a[160:163], v[32:35]\n\t"
"ds_read_b128 a[172:175], v224 offset:2816\n\t"
"v_mfma_f32_16x16x32_bf16 v[36:39], a[40:43], a[148:151], v[36:39]\n\t"
"ds_read_b128 a[152:155], v224 offset:512\n\t"
"v_mfma_f32_16x16x32_bf16 v[36:39], a[44:47], a[164:167], v[36:39]\n\t"
"buffer_load_dwordx4 a[20:23], off, s[92:95], s50 offset:1024 sc1\n\t"
"v_mfma_f32_16x16x32_bf16 v[20:23], a[32:35], a[148:151], v[20:23]\n\t"
"v_mfma_f32_16x16x32_bf16 v[20:23], a[36:39], a[164:167], v[20:23]\n\t"
"v_mfma_f32_16x16x32_bf16 v[4:7], a[24:27], a[148:151], v[4:7]\n\t"
"buffer_load_dwordx4 a[12:15], off, s[96:99], s49 offset:3072 sc1\n\t"
"v_mfma_f32_16x16x32_bf16 v[4:7], a[28:31], a[164:167], v[4:7]\n\t"
"s_waitcnt vmcnt(27) lgkmcnt(4)\n\t"
"s_waitcnt lgkmcnt(0)\n\t"
"s_barrier\n\t"
"v_mfma_f32_16x16x32_bf16 v[8:11], a[24:27], a[152:155], v[8:11]\n\t"
"s_add_u32 m0, s26, 76800\n\t"
"ds_read_b128 a[144:147], v224 offset:17568\n\t"
"ds_read_b128 a[160:163], v224 offset:19616\n\t"
"v_mfma_f32_16x16x32_bf16 v[8:11], a[28:31], a[168:171], v[8:11]\n\t"
"ds_read_b128 a[164:167], v224 offset:19872\n\t"
"v_mfma_f32_16x16x32_bf16 v[24:27], a[32:35], a[152:155], v[24:27]\n\t"
"ds_read_b128 a[148:151], v224 offset:17824\n\t"
"v_mfma_f32_16x16x32_bf16 v[24:27], a[36:39], a[168:171], v[24:27]\n\t"
"buffer_load_dwordx4 v236, s[12:15], s22 offen offset:256 nt lds\n\t"
"v_mfma_f32_16x16x32_bf16 v[40:43], a[40:43], a[152:155], v[40:43]\n\t"
"s_add_u32 s24, s24, 3145728\n\t"
"ds_read_b64_tr_b16 a[176:177], v248 offset:0\n\t"
"ds_read_b64_tr_b16 a[178:179], v249 offset:0\n\t"
"v_mfma_f32_16x16x32_bf16 v[40:43], a[44:47], a[168:171], v[40:43]\n\t"
"s_add_u32 s20, s20, 3145728\n\t"
"ds_read_b64_tr_b16 a[180:181], v248 offset:512\n\t"
"ds_read_b64_tr_b16 a[182:183], v249 offset:512\n\t"
"v_mfma_f32_16x16x32_bf16 v[44:47], a[40:43], a[156:159], v[44:47]\n\t"
"ds_read_b64_tr_b16 a[184:185], v250 offset:2304\n\t"
"v_mfma_f32_16x16x32_bf16 v[44:47], a[44:47], a[172:175], v[44:47]\n\t"
"buffer_load_dwordx4 v237, s[12:15], s22 offen offset:1280 nt lds\n\t"
"v_mfma_f32_16x16x32_bf16 v[28:31], a[32:35], a[156:159], v[28:31]\n\t"
"ds_read_b128 a[152:155], v224 offset:18080\n\t"
"v_mfma_f32_16x16x32_bf16 v[28:31], a[36:39], a[172:175], v[28:31]\n\t"
"s_waitcnt lgkmcnt(2)\n\t"
"ds_read_b64_tr_b16 a[186:187], v251 offset:2304\n\t"
"v_mfma_f32_4x4x4_16b_bf16 v[192:195], a[210:211], a[176:177], 0\n\t"
"ds_read_b128 a[168:171], v224 offset:20128\n\t"
"v_mfma_f32_4x4x4_16b_bf16 v[196:199], a[210:211], a[178:179], 0\n\t"
"ds_read_b64_tr_b16 a[188:189], v250 offset:2816\n\t"
"v_mfma_f32_4x4x4_16b_bf16 v[200:203], a[210:211], a[180:181], 0\n\t"
"ds_read_b64_tr_b16 a[190:191], v251 offset:2816\n\t"
"v_mfma_f32_4x4x4_16b_bf16 v[204:207], a[210:211], a[182:183], 0\n\t"
"buffer_load_dwordx4 v238, s[12:15], s22 offen offset:2304 nt lds\n\t"
"v_mfma_f32_16x16x32_bf16 v[12:15], a[24:27], a[156:159], v[12:15]\n\t"
"s_add_u32 s27, s27, 3145728\n\t"
"ds_read_b64_tr_b16 a[192:193], v250 offset:0\n\t"
"ds_read_b64_tr_b16 a[194:195], v251 offset:0\n\t"
"v_mfma_f32_16x16x32_bf16 v[12:15], a[28:31], a[172:175], v[12:15]\n\t"
"s_waitcnt vmcnt(28)\n\t"
"buffer_load_dwordx4 a[8:11], off, s[96:99], s49 offset:2048 sc1\n\t"
"v_mfma_f32_16x16x32_bf16 v[144:147], a[48:51], a[144:147], v[96:99]\n\t"
"ds_read_b128 a[156:159], v224 offset:18336\n\t"
"v_cvt_pk_bf16_f32 v194, v194, v198\n\t"
"v_mfma_f32_16x16x32_bf16 v[144:147], a[52:55], a[160:163], v[144:147]\n\t"
"s_waitcnt vmcnt(27) lgkmcnt(8)\n\t"
"v_cvt_pk_bf16_f32 v197, v193, v197\n\t"
"v_cvt_pk_bf16_f32 v195, v195, v199\n\t"
"v_mfma_f32_16x16x32_bf16 v[160:163], a[56:59], a[144:147], v[112:115]\n\t"
"ds_read_b128 a[172:175], v224 offset:20384\n\t"
"v_cvt_pk_bf16_f32 v196, v192, v196\n\t"
"v_mfma_f32_16x16x32_bf16 v[160:163], a[60:63], a[160:163], v[160:163]\n\t"
"s_waitcnt vmcnt(25) lgkmcnt(7)\n\t"
"buffer_load_dwordx4 v239, s[12:15], s22 offen offset:3328 nt lds\n\t"
"v_mfma_f32_16x16x32_bf16 v[176:179], a[64:67], a[144:147], v[128:131]\n\t"
"ds_read_b128 a[144:147], v224 offset:16544\n\t"
"v_cvt_pk_bf16_f32 v199, v201, v205\n\t"
"v_mfma_f32_16x16x32_bf16 v[176:179], a[68:71], a[160:163], v[176:179]\n\t"
"s_waitcnt lgkmcnt(5)\n\t"
"v_cvt_pk_bf16_f32 v192, v202, v206\n\t"
"v_cvt_pk_bf16_f32 v198, v200, v204\n\t"
"v_mfma_f32_4x4x4_16b_bf16 v[208:211], a[210:211], a[184:185], 0\n\t"
"ds_read_b64_tr_b16 a[196:197], v250 offset:512\n\t"
"v_mfma_f32_4x4x4_16b_bf16 v[212:215], a[210:211], a[186:187], 0\n\t"
"ds_read_b64_tr_b16 a[198:199], v251 offset:512\n\t"
"v_mfma_f32_4x4x4_16b_bf16 v[216:219], a[210:211], a[188:189], 0\n\t"
"ds_read_b64_tr_b16 a[200:201], v248 offset:2304\n\t"
"v_mfma_f32_4x4x4_16b_bf16 v[220:223], a[210:211], a[190:191], 0\n\t"
"s_mov_b64 exec, s[36:37]\n\t"
"buffer_load_dwordx4 v240, s[12:15], s22 offen offset:3840 nt lds\n\t"
"s_mov_b64 exec, -1\n\t"
"v_mfma_f32_16x16x32_bf16 v[180:183], a[64:67], a[148:151], v[132:135]\n\t"
"s_lshl_b32 m0, s73, 8\n\t"
"ds_read_b128 a[160:163], v224 offset:18592\n\t"
"v_cvt_pk_bf16_f32 v193, v203, v207\n\t"
"v_mfma_f32_16x16x32_bf16 v[180:183], a[68:71], a[164:167], v[180:183]\n\t"
"s_nop 0\n\t"
"ds_write_addtid_b32 v198 offset:8320\n\t"
"ds_write_addtid_b32 v197 offset:1024\n\t"
"v_mfma_f32_16x16x32_bf16 v[164:167], a[56:59], a[148:151], v[116:119]\n\t"
"v_cvt_pk_bf16_f32 v213, v209, v213\n\t"
"v_cvt_pk_bf16_f32 v211, v211, v215\n\t"
"v_mfma_f32_16x16x32_bf16 v[164:167], a[60:63], a[164:167], v[164:167]\n\t"
"v_cvt_pk_bf16_f32 v212, v208, v212\n\t"
"v_cvt_pk_bf16_f32 v210, v210, v214\n\t"
"v_mfma_f32_16x16x32_bf16 v[148:151], a[48:51], a[148:151], v[100:103]\n\t"
"ds_write_addtid_b32 v199 offset:9344\n\t"
"v_cvt_pk_bf16_f32 v209, v219, v223\n\t"
"v_mfma_f32_16x16x32_bf16 v[148:151], a[52:55], a[164:167], v[148:151]\n\t"
"s_waitcnt lgkmcnt(5)\n\t"
"ds_read_b128 a[148:151], v224 offset:16800\n\t"
"v_cvt_pk_bf16_f32 v214, v216, v220\n\t"
"v_mfma_f32_4x4x4_16b_bf16 v[200:203], a[210:211], a[192:193], 0\n\t"
"v_cvt_pk_bf16_f32 v208, v218, v222\n\t"
"v_mfma_f32_4x4x4_16b_bf16 v[204:207], a[210:211], a[194:195], 0\n\t"
"ds_write_addtid_b32 v196 offset:0\n\t"
"v_mfma_f32_4x4x4_16b_bf16 v[196:199], a[210:211], a[196:197], 0\n\t"
"v_cvt_pk_bf16_f32 v215, v217, v221\n\t"
"v_mfma_f32_4x4x4_16b_bf16 v[216:219], a[210:211], a[198:199], 0\n\t"
"ds_read_b128 a[164:167], v224 offset:18848\n\t"
"v_mfma_f32_16x16x32_bf16 v[152:155], a[48:51], a[152:155], v[104:107]\n\t"
"ds_read_b64_tr_b16 a[202:203], v249 offset:2304\n\t"
"v_mfma_f32_16x16x32_bf16 v[152:155], a[52:55], a[168:171], v[152:155]\n\t"
"ds_read_b64_tr_b16 a[204:205], v248 offset:2816\n\t"
"v_mfma_f32_16x16x32_bf16 v[168:171], a[56:59], a[152:155], v[120:123]\n\t"
"v_cvt_pk_bf16_f32 v202, v202, v206\n\t"
"v_cvt_pk_bf16_f32 v203, v203, v207\n\t"
"v_mfma_f32_16x16x32_bf16 v[168:171], a[60:63], a[168:171], v[168:171]\n\t"
"ds_read_b64_tr_b16 a[206:207], v249 offset:2816\n\t"
"v_cvt_pk_bf16_f32 v205, v201, v205\n\t"
"v_mfma_f32_16x16x32_bf16 v[184:187], a[64:67], a[152:155], v[136:139]\n\t"
"ds_read_b128 a[152:155], v224 offset:17056\n\t"
"v_cvt_pk_bf16_f32 v204, v200, v204\n\t"
"v_mfma_f32_16x16x32_bf16 v[184:187], a[68:71], a[168:171], v[184:187]\n\t"
"ds_write_addtid_b32 v213 offset:5152\n\t"
"v_cvt_pk_bf16_f32 v207, v197, v217\n\t"
"v_mfma_f32_16x16x32_bf16 v[188:191], a[64:67], a[156:159], v[140:143]\n\t"
"ds_write_addtid_b32 v215 offset:13472\n\t"
"v_cvt_pk_bf16_f32 v201, v199, v219\n\t"
"v_mfma_f32_16x16x32_bf16 v[188:191], a[68:71], a[172:175], v[188:191]\n\t"
"s_waitcnt lgkmcnt(3)\n\t"
"ds_write_addtid_b32 v212 offset:4128\n\t"
"v_cvt_pk_bf16_f32 v200, v198, v218\n\t"
"v_mfma_f32_4x4x4_16b_bf16 v[220:223], a[210:211], a[200:201], 0\n\t"
"ds_write_addtid_b32 v214 offset:12448\n\t"
"v_mfma_f32_4x4x4_16b_bf16 v[212:215], a[210:211], a[202:203], 0\n\t"
"v_cvt_pk_bf16_f32 v206, v196, v216\n\t"
"v_mfma_f32_4x4x4_16b_bf16 v[196:199], a[210:211], a[204:205], 0\n\t"
"ds_read_b128 a[168:171], v224 offset:19104\n\t"
"v_mfma_f32_4x4x4_16b_bf16 v[216:219], a[210:211], a[206:207], 0\n\t"
"ds_write_addtid_b32 v206 offset:10368\n\t"
"v_mfma_f32_16x16x32_bf16 v[172:175], a[56:59], a[156:159], v[124:127]\n\t"
"s_nop 0\n\t"
"ds_write_addtid_b32 v205 offset:3072\n\t"
"ds_write_addtid_b32 v207 offset:11392\n\t"
"v_mfma_f32_16x16x32_bf16 v[172:175], a[60:63], a[172:175], v[172:175]\n\t"
"ds_write_addtid_b32 v204 offset:2048\n\t"
"v_mfma_f32_16x16x32_bf16 v[156:159], a[48:51], a[156:159], v[108:111]\n\t"
"v_cvt_pk_bf16_f32 v212, v220, v212\n\t"
"v_cvt_pk_bf16_f32 v222, v222, v214\n\t"
"v_mfma_f32_16x16x32_bf16 v[156:159], a[52:55], a[172:175], v[156:159]\n\t"
"s_waitcnt vmcnt(22) lgkmcnt(8)\n\t"
"v_cvt_pk_bf16_f32 v213, v221, v213\n\t"
"v_cvt_pk_bf16_f32 v223, v223, v215\n\t"
"v_mfma_f32_16x16x32_bf16 v[96:99], a[72:75], a[144:147], v[96:99]\n\t"
"ds_write_addtid_b32 v212 offset:6176\n\t"
"v_cvt_pk_bf16_f32 v214, v196, v216\n\t"
"v_mfma_f32_16x16x32_bf16 v[96:99], a[76:79], a[160:163], v[96:99]\n\t"
"ds_write_addtid_b32 v213 offset:7200\n\t"
"v_cvt_pk_bf16_f32 v215, v197, v217\n\t"
"v_mfma_f32_16x16x32_bf16 v[112:115], a[80:83], a[144:147], v[112:115]\n\t"
"s_add_u32 s25, s25, 3145728\n\t"
"ds_write_addtid_b32 v214 offset:14496\n\t"
"ds_write_addtid_b32 v215 offset:15520\n\t"
"v_mfma_f32_16x16x32_bf16 v[112:115], a[84:87], a[160:163], v[112:115]\n\t"
"s_waitcnt vmcnt(21) lgkmcnt(9)\n\t"
"v_mfma_f32_16x16x32_bf16 v[128:131], a[88:91], a[144:147], v[128:131]\n\t"
"v_mfma_f32_16x16x32_bf16 v[128:131], a[92:95], a[160:163], v[128:131]\n\t"
"ds_read_b128 a[156:159], v224 offset:17312\n\t"
"v_mfma_f32_16x16x32_bf16 v[132:135], a[88:91], a[148:151], v[132:135]\n\t"
"ds_read_b128 a[172:175], v224 offset:19360\n\t"
"v_cvt_pk_bf16_f32 v221, v199, v219\n\t"
"v_mfma_f32_16x16x32_bf16 v[132:135], a[92:95], a[164:167], v[132:135]\n\t"
"v_mfma_f32_16x16x32_bf16 v[116:119], a[80:83], a[148:151], v[116:119]\n\t"
"v_cvt_pk_bf16_f32 v220, v198, v218\n\t"
"v_mfma_f32_16x16x32_bf16 v[116:119], a[84:87], a[164:167], v[116:119]\n\t"
"buffer_load_dwordx4 a[0:3], off, s[96:99], s49 offset:0 sc1\n\t"
"v_mfma_f32_16x16x32_bf16 v[100:103], a[72:75], a[148:151], v[100:103]\n\t"
"s_waitcnt lgkmcnt(0)\n\t"
"v_mfma_f32_16x16x32_bf16 v[100:103], a[76:79], a[164:167], v[100:103]\n\t"
"s_barrier\n\t"
"v_mfma_f32_16x16x32_bf16 v[104:107], a[72:75], a[152:155], v[104:107]\n\t"
"s_nop 0\n\t"
"ds_read_b128 a[144:147], v224 offset:1024\n\t"
"ds_read_b128 a[160:163], v224 offset:3072\n\t"
"v_mfma_f32_16x16x32_bf16 v[104:107], a[76:79], a[168:171], v[104:107]\n\t"
"buffer_load_dwordx4 a[4:7], off, s[96:99], s49 offset:1024 sc1\n\t"
"v_mfma_f32_16x16x32_bf16 v[120:123], a[80:83], a[152:155], v[120:123]\n\t"
"ds_read_b128 a[148:151], v224 offset:1280\n\t"
"v_mfma_f32_16x16x32_bf16 v[120:123], a[84:87], a[168:171], v[120:123]\n\t"
"ds_read_b128 a[164:167], v224 offset:3328\n\t"
"v_mfma_f32_16x16x32_bf16 v[136:139], a[88:91], a[152:155], v[136:139]\n\t"
"ds_read_b128 a[248:251], v255 offset:59904\n\t"
"v_mfma_f32_16x16x32_bf16 v[136:139], a[92:95], a[168:171], v[136:139]\n\t"
"buffer_load_dwordx4 a[40:43], off, s[92:95], s49 offset:2048 sc1\n\t"
"v_mfma_f32_16x16x32_bf16 v[140:143], a[88:91], a[156:159], v[140:143]\n\t"
"v_mfma_f32_16x16x32_bf16 v[140:143], a[92:95], a[172:175], v[140:143]\n\t"
"ds_read_b128 a[152:155], v224 offset:1536\n\t"
"v_mfma_f32_16x16x32_bf16 v[124:127], a[80:83], a[156:159], v[124:127]\n\t"
"ds_read_b128 a[168:171], v224 offset:3584\n\t"
"v_mfma_f32_16x16x32_bf16 v[124:127], a[84:87], a[172:175], v[124:127]\n\t"
"buffer_load_dwordx4 a[44:47], off, s[92:95], s49 offset:3072 sc1\n\t"
"v_mfma_f32_16x16x32_bf16 v[108:111], a[72:75], a[156:159], v[108:111]\n\t"
"s_nop 0\n\t"
"ds_write_addtid_b32 v223 offset:23744\n\t"
"v_mfma_f32_16x16x32_bf16 v[108:111], a[76:79], a[172:175], v[108:111]\n\t"
"s_waitcnt vmcnt(19) lgkmcnt(6)\n\t"
"ds_read_b128 a[156:159], v224 offset:1792\n\t"
"v_mfma_f32_16x16x32_bf16 v[48:51], a[96:99], a[144:147], v[48:51]\n\t"
"s_nop 0\n\t"
"ds_read_b128 a[172:175], v224 offset:3840\n\t"
"v_mfma_f32_16x16x32_bf16 v[48:51], a[100:103], a[160:163], v[48:51]\n\t"
"buffer_load_dwordx4 a[36:39], off, s[92:95], s49 offset:1024 sc1\n\t"
"v_mfma_f32_16x16x32_bf16 v[64:67], a[104:107], a[144:147], v[64:67]\n\t"
"s_nop 0\n\t"
"ds_write_addtid_b32 v192 offset:24864\n\t"
"v_mfma_f32_16x16x32_bf16 v[64:67], a[108:111], a[160:163], v[64:67]\n\t"
"s_nop 0\n\t"
"ds_write_addtid_b32 v200 offset:26912\n\t"
"v_mfma_f32_16x16x32_bf16 v[80:83], a[112:115], a[144:147], v[80:83]\n\t"
"s_nop 0\n\t"
"ds_read_b128 a[144:147], v224 offset:0\n\t"
"v_mfma_f32_16x16x32_bf16 v[80:83], a[116:119], a[160:163], v[80:83]\n\t"
"s_waitcnt lgkmcnt(7)\n\t"
"buffer_load_dwordx4 a[32:35], off, s[92:95], s49 offset:0 sc1\n\t"
"v_mfma_f32_16x16x32_bf16 v[84:87], a[112:115], a[148:151], v[84:87]\n\t"
"s_nop 0\n\t"
"ds_read_b128 a[160:163], v224 offset:2048\n\t"
"v_mfma_f32_16x16x32_bf16 v[84:87], a[116:119], a[164:167], v[84:87]\n\t"
"s_nop 0\n\t"
"ds_write_addtid_b32 v221 offset:32064\n\t"
"v_mfma_f32_16x16x32_bf16 v[68:71], a[104:107], a[148:151], v[68:71]\n\t"
"s_nop 0\n\t"
"ds_write_addtid_b32 v209 offset:30016\n\t"
"v_xor_b32 v226, 64, v226\n\t"
"v_mfma_f32_16x16x32_bf16 v[68:71], a[108:111], a[164:167], v[68:71]\n\t"
"buffer_load_dwordx4 a[24:27], off, s[96:99], s48 offset:2048 sc1\n\t"
"v_mfma_f32_16x16x32_bf16 v[52:55], a[96:99], a[148:151], v[52:55]\n\t"
"s_nop 0\n\t"
"ds_read_b128 a[148:151], v224 offset:256\n\t"
"v_mfma_f32_16x16x32_bf16 v[52:55], a[100:103], a[164:167], v[52:55]\n\t"
"s_waitcnt lgkmcnt(7)\n\t"
"ds_write_addtid_b32 v193 offset:25888\n\t"
"v_mfma_f32_16x16x32_bf16 v[56:59], a[96:99], a[152:155], v[56:59]\n\t"
"s_nop 0\n\t"
"ds_read_b128 a[164:167], v224 offset:2304\n\t"
"v_mfma_f32_16x16x32_bf16 v[56:59], a[100:103], a[168:171], v[56:59]\n\t"
"buffer_load_dwordx4 a[28:31], off, s[96:99], s48 offset:3072 sc1\n\t"
"v_mfma_f32_16x16x32_bf16 v[72:75], a[104:107], a[152:155], v[72:75]\n\t"
"s_nop 0\n\t"
"ds_write_addtid_b32 v210 offset:20672\n\t"
"v_xor_b32 v227, 64, v227\n\t"
"v_mfma_f32_16x16x32_bf16 v[72:75], a[108:111], a[168:171], v[72:75]\n\t"
"s_nop 0\n\t"
"ds_write_addtid_b32 v195 offset:17568\n\t"
"v_mfma_f32_16x16x32_bf16 v[88:91], a[112:115], a[152:155], v[88:91]\n\t"
"s_nop 0\n\t"
"ds_read_b128 a[152:155], v224 offset:512\n\t"
"v_mfma_f32_16x16x32_bf16 v[88:91], a[116:119], a[168:171], v[88:91]\n\t"
"buffer_load_dwordx4 a[64:67], off, s[92:95], s77 offset:0 sc1\n\t"
"v_mfma_f32_16x16x32_bf16 v[92:95], a[112:115], a[156:159], v[92:95]\n\t"
"s_nop 0\n\t"
"ds_read_b128 a[168:171], v224 offset:2560\n\t"
"v_mfma_f32_16x16x32_bf16 v[92:95], a[116:119], a[172:175], v[92:95]\n\t"
"s_nop 0\n\t"
"ds_write_addtid_b32 v202 offset:18592\n\t"
"v_mfma_f32_16x16x32_bf16 v[76:79], a[104:107], a[156:159], v[76:79]\n\t"
"s_nop 0\n\t"
"ds_write_addtid_b32 v208 offset:28992\n\t"
"v_mfma_f32_16x16x32_bf16 v[76:79], a[108:111], a[172:175], v[76:79]\n\t"
"buffer_load_dwordx4 a[68:71], off, s[92:95], s77 offset:1024 sc1\n\t"
"v_mfma_f32_16x16x32_bf16 v[60:63], a[96:99], a[156:159], v[60:63]\n\t"
"s_nop 0\n\t"
"ds_write_addtid_b32 v211 offset:21696\n\t"
"v_mfma_f32_16x16x32_bf16 v[60:63], a[100:103], a[172:175], v[60:63]\n\t"
"s_waitcnt vmcnt(19) lgkmcnt(7)\n\t"
"ds_write_addtid_b32 v222 offset:22720\n\t"
"v_mfma_f32_16x16x32_bf16 v[0:3], a[120:123], a[144:147], v[0:3]\n\t"
"s_nop 0\n\t"
"ds_write_addtid_b32 v194 offset:16544\n\t"
"v_mfma_f32_16x16x32_bf16 v[0:3], a[124:127], a[160:163], v[0:3]\n\t"
"s_nop 0\n\t"
"ds_write_addtid_b32 v220 offset:31040\n\t"
"v_mfma_f32_16x16x32_bf16 v[16:19], a[128:131], a[144:147], v[16:19]\n\t"
"s_add_u32 s22, s22, 3145728\n\t"
"ds_write_addtid_b32 v203 offset:19616\n\t"
"ds_write_addtid_b32 v201 offset:27936\n\t"
"v_mfma_f32_16x16x32_bf16 v[16:19], a[132:135], a[160:163], v[16:19]\n\t"
"buffer_load_dwordx4 a[60:63], off, s[96:99], s76 offset:3072 sc1\n\t"
"v_mfma_f32_16x16x32_bf16 v[32:35], a[136:139], a[144:147], v[32:35]\n\t"
"s_nop 0\n\t"
"ds_read_b128 a[172:175], v224 offset:2816\n\t"
"v_mfma_f32_16x16x32_bf16 v[32:35], a[140:143], a[160:163], v[32:35]\n\t"
"buffer_load_dwordx4 a[56:59], off, s[96:99], s76 offset:2048 sc1\n\t"
"v_mfma_f32_16x16x32_bf16 v[36:39], a[136:139], a[148:151], v[36:39]\n\t"
"s_nop 0\n\t"
"ds_read_b128 a[156:159], v224 offset:768\n\t"
"v_mfma_f32_16x16x32_bf16 v[36:39], a[140:143], a[164:167], v[36:39]\n\t"
"s_nop 0\n\t"
"v_mfma_f32_16x16x32_bf16 v[20:23], a[128:131], a[148:151], v[20:23]\n\t"
"s_cmp_eq_u32 s21, 1\n\t"
"s_cselect_b32 s22, 2.0, s22\n\t"
"v_mfma_f32_16x16x32_bf16 v[20:23], a[132:135], a[164:167], v[20:23]\n\t"
"buffer_load_dwordx4 a[48:51], off, s[96:99], s76 offset:0 sc1\n\t"
"v_mfma_f32_16x16x32_bf16 v[4:7], a[120:123], a[148:151], v[4:7]\n\t"
"s_nop 0\n\t"
"v_mfma_f32_16x16x32_bf16 v[4:7], a[124:127], a[164:167], v[4:7]\n\t"
"s_waitcnt vmcnt(13) lgkmcnt(4)\n\t"
"s_waitcnt lgkmcnt(0)\n\t"
"s_barrier\n\t"
"v_mfma_f32_16x16x32_bf16 v[8:11], a[120:123], a[152:155], v[8:11]\n\t"
"s_nop 0\n\t"
"ds_read_b128 a[144:147], v224 offset:17568\n\t"
"ds_read_b128 a[160:163], v224 offset:19616\n\t"
"v_mfma_f32_16x16x32_bf16 v[8:11], a[124:127], a[168:171], v[8:11]\n\t"
"buffer_load_dwordx4 a[52:55], off, s[96:99], s76 offset:1024 sc1\n\t"
"v_mfma_f32_16x16x32_bf16 v[24:27], a[128:131], a[152:155], v[24:27]\n\t"
"s_nop 0\n\t"
"ds_read_b128 a[148:151], v224 offset:17824\n\t"
"ds_read_b128 a[164:167], v224 offset:19872\n\t"
"v_mfma_f32_16x16x32_bf16 v[24:27], a[132:135], a[168:171], v[24:27]\n\t"
"buffer_load_dwordx4 a[88:91], off, s[92:95], s76 offset:2048 sc1\n\t"
"v_mfma_f32_16x16x32_bf16 v[40:43], a[136:139], a[152:155], v[40:43]\n\t"
"s_nop 0\n\t"
"ds_read_b64_tr_b16 a[176:177], v248 offset:41472\n\t"
"ds_read_b64_tr_b16 a[178:179], v249 offset:41472\n\t"
"v_mfma_f32_16x16x32_bf16 v[40:43], a[140:143], a[168:171], v[40:43]\n\t"
"s_nop 0\n\t"
"ds_read_b64_tr_b16 a[180:181], v248 offset:41984\n\t"
"ds_read_b64_tr_b16 a[182:183], v249 offset:41984\n\t"
"v_mfma_f32_16x16x32_bf16 v[44:47], a[136:139], a[156:159], v[44:47]\n\t"
"s_nop 0\n\t"
"ds_read_b64_tr_b16 a[184:185], v250 offset:43776\n\t"
"ds_read_b64_tr_b16 a[186:187], v251 offset:43776\n\t"
"v_mfma_f32_16x16x32_bf16 v[44:47], a[140:143], a[172:175], v[44:47]\n\t"
"buffer_load_dwordx4 a[92:95], off, s[92:95], s76 offset:3072 sc1\n\t"
"v_mfma_f32_16x16x32_bf16 v[28:31], a[128:131], a[156:159], v[28:31]\n\t"
"s_nop 0\n\t"
"ds_read_b64_tr_b16 a[188:189], v250 offset:44288\n\t"
"ds_read_b64_tr_b16 a[190:191], v251 offset:44288\n\t"
"v_mfma_f32_16x16x32_bf16 v[28:31], a[132:135], a[172:175], v[28:31]\n\t"
"s_waitcnt lgkmcnt(4)\n\t"
"buffer_load_dwordx4 a[84:87], off, s[92:95], s76 offset:1024 sc1\n\t"
"v_mfma_f32_4x4x4_16b_bf16 v[192:195], a[210:211], a[176:177], 0\n\t"
"ds_read_b128 a[152:155], v224 offset:18080\n\t"
"v_mfma_f32_4x4x4_16b_bf16 v[196:199], a[210:211], a[178:179], 0\n\t"
"ds_read_b128 a[168:171], v224 offset:20128\n\t"
"v_mfma_f32_4x4x4_16b_bf16 v[200:203], a[210:211], a[180:181], 0\n\t"
"ds_read_b64_tr_b16 a[192:193], v250 offset:41472\n\t"
"v_mfma_f32_4x4x4_16b_bf16 v[204:207], a[210:211], a[182:183], 0\n\t"
"buffer_load_dwordx4 a[80:83], off, s[92:95], s76 offset:0 sc1\n\t"
"v_mfma_f32_16x16x32_bf16 v[12:15], a[120:123], a[156:159], v[12:15]\n\t"
"s_nop 0\n\t"
"ds_read_b64_tr_b16 a[194:195], v251 offset:41472\n\t"
"ds_read_b64_tr_b16 a[196:197], v250 offset:41984\n\t"
"v_mfma_f32_16x16x32_bf16 v[12:15], a[124:127], a[172:175], v[12:15]\n\t"
"s_waitcnt vmcnt(16)\n\t"
"ds_read_b64_tr_b16 a[198:199], v251 offset:41984\n\t"
"ds_read_b64_tr_b16 a[200:201], v248 offset:43776\n\t"
"v_mfma_f32_16x16x32_bf16 v[144:147], a[0:3], a[144:147], v[144:147]\n\t"
"s_nop 0\n\t"
"v_cvt_pk_bf16_f32 v196, v192, v196\n\t"
"v_cvt_pk_bf16_f32 v194, v194, v198\n\t"
"v_mfma_f32_16x16x32_bf16 v[144:147], a[4:7], a[160:163], v[144:147]\n\t"
"buffer_load_dwordx4 a[72:75], off, s[96:99], s75 offset:2048 sc1\n\t"
"v_mfma_f32_16x16x32_bf16 v[160:163], a[8:11], a[144:147], v[160:163]\n\t"
"s_nop 0\n\t"
"ds_read_b128 a[156:159], v224 offset:18336\n\t"
"v_cvt_pk_bf16_f32 v197, v193, v197\n\t"
"v_mfma_f32_16x16x32_bf16 v[160:163], a[12:15], a[160:163], v[160:163]\n\t"
"s_nop 0\n\t"
"ds_read_b128 a[172:175], v224 offset:20384\n\t"
"v_cvt_pk_bf16_f32 v195, v195, v199\n\t"
"v_mfma_f32_16x16x32_bf16 v[176:179], a[16:19], a[144:147], v[176:179]\n\t"
"s_nop 0\n\t"
"v_cvt_pk_bf16_f32 v198, v200, v204\n\t"
"v_cvt_pk_bf16_f32 v199, v201, v205\n\t"
"v_mfma_f32_16x16x32_bf16 v[176:179], a[20:23], a[160:163], v[176:179]\n\t"
"s_waitcnt lgkmcnt(7)\n\t"
"buffer_load_dwordx4 a[76:79], off, s[96:99], s75 offset:3072 sc1\n\t"
"v_mfma_f32_4x4x4_16b_bf16 v[208:211], a[210:211], a[184:185], 0\n\t"
"ds_read_b128 a[144:147], v224 offset:16544\n\t"
"v_mfma_f32_4x4x4_16b_bf16 v[212:215], a[210:211], a[186:187], 0\n\t"
"ds_write_addtid_b32 v198 offset:8320\n\t"
"v_mfma_f32_4x4x4_16b_bf16 v[216:219], a[210:211], a[188:189], 0\n\t"
"ds_write_addtid_b32 v197 offset:1024\n\t"
"v_mfma_f32_4x4x4_16b_bf16 v[220:223], a[210:211], a[190:191], 0\n\t"
"ds_write_addtid_b32 v196 offset:0\n\t"
"v_cvt_pk_bf16_f32 v193, v203, v207\n\t"
"v_mfma_f32_16x16x32_bf16 v[180:183], a[16:19], a[148:151], v[180:183]\n\t"
"s_nop 0\n\t"
"ds_read_b128 a[160:163], v224 offset:18592\n\t"
"v_cvt_pk_bf16_f32 v192, v202, v206\n\t"
"v_mfma_f32_16x16x32_bf16 v[180:183], a[20:23], a[164:167], v[180:183]\n\t"
"Lpad3_%=:\n\t"
"s_cbranch_execz Lpad7_%=\n\t"
"buffer_load_dwordx4 a[112:115], off, s[92:95], s80 offset:0 sc1\n\t"
"v_mfma_f32_16x16x32_bf16 v[164:167], a[8:11], a[148:151], v[164:167]\n\t"
"ds_write_addtid_b32 v199 offset:9344\n\t"
"v_cvt_pk_bf16_f32 v210, v210, v214\n\t"
"v_mfma_f32_16x16x32_bf16 v[164:167], a[12:15], a[164:167], v[164:167]\n\t"
"v_cvt_pk_bf16_f32 v212, v208, v212\n\t"
"v_cvt_pk_bf16_f32 v213, v209, v213\n\t"
"v_mfma_f32_16x16x32_bf16 v[148:151], a[0:3], a[148:151], v[148:151]\n\t"
"ds_read_b128 a[148:151], v224 offset:16800\n\t"
"v_cvt_pk_bf16_f32 v211, v211, v215\n\t"
"v_mfma_f32_16x16x32_bf16 v[148:151], a[4:7], a[164:167], v[148:151]\n\t"
"s_waitcnt lgkmcnt(7)\n\t"
"v_cvt_pk_bf16_f32 v214, v216, v220\n\t"
"v_cvt_pk_bf16_f32 v209, v219, v223\n\t"
"v_cvt_pk_bf16_f32 v215, v217, v221\n\t"
"v_mfma_f32_4x4x4_16b_bf16 v[200:203], a[210:211], a[192:193], 0\n\t"
"ds_read_b128 a[164:167], v224 offset:18848\n\t"
"v_mfma_f32_4x4x4_16b_bf16 v[204:207], a[210:211], a[194:195], 0\n\t"
"v_cvt_pk_bf16_f32 v208, v218, v222\n\t"
"v_mfma_f32_4x4x4_16b_bf16 v[196:199], a[210:211], a[196:197], 0\n\t"
"ds_read_b64_tr_b16 a[202:203], v249 offset:43776\n\t"
"v_mfma_f32_4x4x4_16b_bf16 v[216:219], a[210:211], a[198:199], 0\n\t"
"buffer_load_dwordx4 a[116:119], off, s[92:95], s80 offset:1024 sc1\n\t"
"v_mfma_f32_16x16x32_bf16 v[152:155], a[0:3], a[152:155], v[152:155]\n\t"
"s_nop 0\n\t"
"ds_write_addtid_b32 v214 offset:12448\n\t"
"ds_read_b64_tr_b16 a[204:205], v248 offset:44288\n\t"
"v_mfma_f32_16x16x32_bf16 v[152:155], a[4:7], a[168:171], v[152:155]\n\t"
"buffer_load_dwordx4 a[108:111], off, s[96:99], s79 offset:3072 sc1\n\t"
"v_mfma_f32_16x16x32_bf16 v[168:171], a[8:11], a[152:155], v[168:171]\n\t"
"ds_read_b64_tr_b16 a[206:207], v249 offset:44288\n\t"
"v_cvt_pk_bf16_f32 v205, v201, v205\n\t"
"v_mfma_f32_16x16x32_bf16 v[168:171], a[12:15], a[168:171], v[168:171]\n\t"
"v_cvt_pk_bf16_f32 v204, v200, v204\n\t"
"v_cvt_pk_bf16_f32 v203, v203, v207\n\t"
"v_mfma_f32_16x16x32_bf16 v[184:187], a[16:19], a[152:155], v[184:187]\n\t"
"ds_read_b128 a[152:155], v224 offset:17056\n\t"
"v_cvt_pk_bf16_f32 v201, v199, v219\n\t"
"v_mfma_f32_16x16x32_bf16 v[184:187], a[20:23], a[168:171], v[184:187]\n\t"
"buffer_load_dwordx4 a[104:107], off, s[96:99], s79 offset:2048 sc1\n\t"
"v_cvt_pk_bf16_f32 v207, v197, v217\n\t"
"v_mfma_f32_16x16x32_bf16 v[188:191], a[16:19], a[156:159], v[188:191]\n\t"
"ds_write_addtid_b32 v215 offset:13472\n\t"
"v_cvt_pk_bf16_f32 v202, v202, v206\n\t"
"v_mfma_f32_16x16x32_bf16 v[188:191], a[20:23], a[172:175], v[188:191]\n\t"
"s_waitcnt lgkmcnt(2)\n\t"
"ds_write_addtid_b32 v213 offset:5152\n\t"
"v_cvt_pk_bf16_f32 v206, v196, v216\n\t"
"v_mfma_f32_4x4x4_16b_bf16 v[220:223], a[210:211], a[200:201], 0\n\t"
"ds_write_addtid_b32 v212 offset:4128\n\t"
"v_mfma_f32_4x4x4_16b_bf16 v[212:215], a[210:211], a[202:203], 0\n\t"
"v_cvt_pk_bf16_f32 v200, v198, v218\n\t"
"v_mfma_f32_4x4x4_16b_bf16 v[196:199], a[210:211], a[204:205], 0\n\t"
"ds_read_b128 a[168:171], v224 offset:19104\n\t"
"v_mfma_f32_4x4x4_16b_bf16 v[216:219], a[210:211], a[206:207], 0\n\t"
"buffer_load_dwordx4 a[96:99], off, s[96:99], s79 offset:0 sc1\n\t"
"v_mfma_f32_16x16x32_bf16 v[172:175], a[8:11], a[156:159], v[172:175]\n\t"
"s_nop 0\n\t"
"ds_write_addtid_b32 v204 offset:2048\n\t"
"ds_write_addtid_b32 v207 offset:11392\n\t"
"v_mfma_f32_16x16x32_bf16 v[172:175], a[12:15], a[172:175], v[172:175]\n\t"
"s_nop 0\n\t"
"ds_write_addtid_b32 v206 offset:10368\n\t"
"ds_write_addtid_b32 v205 offset:3072\n\t"
"v_mfma_f32_16x16x32_bf16 v[156:159], a[0:3], a[156:159], v[156:159]\n\t"
"v_cvt_pk_bf16_f32 v222, v222, v214\n\t"
"v_cvt_pk_bf16_f32 v213, v221, v213\n\t"
"v_mfma_f32_16x16x32_bf16 v[156:159], a[4:7], a[172:175], v[156:159]\n\t"
"s_waitcnt vmcnt(17) lgkmcnt(8)\n\t"
"v_cvt_pk_bf16_f32 v212, v220, v212\n\t"
"v_cvt_pk_bf16_f32 v223, v223, v215\n\t"
"v_mfma_f32_16x16x32_bf16 v[96:99], a[24:27], a[144:147], v[96:99]\n\t"
"ds_write_addtid_b32 v212 offset:6176\n\t"
"v_cvt_pk_bf16_f32 v214, v196, v216\n\t"
"v_mfma_f32_16x16x32_bf16 v[96:99], a[28:31], a[160:163], v[96:99]\n\t"
"ds_write_addtid_b32 v214 offset:14496\n\t"
"v_cvt_pk_bf16_f32 v215, v197, v217\n\t"
"v_mfma_f32_16x16x32_bf16 v[112:115], a[32:35], a[144:147], v[112:115]\n\t"
"s_nop 0\n\t"
"ds_write_addtid_b32 v213 offset:7200\n\t"
"ds_write_addtid_b32 v215 offset:15520\n\t"
"v_mfma_f32_16x16x32_bf16 v[112:115], a[36:39], a[160:163], v[112:115]\n\t"
"buffer_load_dwordx4 a[100:103], off, s[96:99], s79 offset:1024 sc1\n\t"
"v_mfma_f32_16x16x32_bf16 v[128:131], a[40:43], a[144:147], v[128:131]\n\t"
"ds_read_b128 a[172:175], v224 offset:19360\n\t"
"v_cvt_pk_bf16_f32 v220, v198, v218\n\t"
"v_mfma_f32_16x16x32_bf16 v[128:131], a[44:47], a[160:163], v[128:131]\n\t"
"buffer_load_dwordx4 a[136:139], off, s[92:95], s79 offset:2048 sc1\n\t"
"v_mfma_f32_16x16x32_bf16 v[132:135], a[40:43], a[148:151], v[132:135]\n\t"
"s_nop 0\n\t"
"ds_read_b128 a[156:159], v224 offset:17312\n\t"
"ds_read_b128 a[244:247], v255 offset:39168\n\t"
"v_mfma_f32_16x16x32_bf16 v[132:135], a[44:47], a[164:167], v[132:135]\n\t"
"buffer_load_dwordx4 a[140:143], off, s[92:95], s79 offset:3072 sc1\n\t"
"v_mfma_f32_16x16x32_bf16 v[116:119], a[32:35], a[148:151], v[116:119]\n\t"
"v_mfma_f32_16x16x32_bf16 v[116:119], a[36:39], a[164:167], v[116:119]\n\t"
"buffer_load_dwordx4 a[132:135], off, s[92:95], s79 offset:1024 sc1\n\t"
"v_mfma_f32_16x16x32_bf16 v[100:103], a[24:27], a[148:151], v[100:103]\n\t"
"s_waitcnt lgkmcnt(0)\n\t"
"v_cvt_pk_bf16_f32 v221, v199, v219\n\t"
"v_mfma_f32_16x16x32_bf16 v[100:103], a[28:31], a[164:167], v[100:103]\n\t"
"s_barrier\n\t"
"v_mfma_f32_16x16x32_bf16 v[104:107], a[24:27], a[152:155], v[104:107]\n\t"
"s_nop 0\n\t"
"ds_read_b128 a[144:147], v224 offset:1024\n\t"
"ds_read_b128 a[148:151], v224 offset:1280\n\t"
"v_mfma_f32_16x16x32_bf16 v[104:107], a[28:31], a[168:171], v[104:107]\n\t"
"buffer_load_dwordx4 a[128:131], off, s[92:95], s79 offset:0 sc1\n\t"
"v_mfma_f32_16x16x32_bf16 v[120:123], a[32:35], a[152:155], v[120:123]\n\t"
"ds_read_b128 a[160:163], v224 offset:3072\n\t"
"v_add_u32 v248, 101632, v225\n\t"
"v_mfma_f32_16x16x32_bf16 v[120:123], a[36:39], a[168:171], v[120:123]\n\t"
"ds_read_b128 a[164:167], v224 offset:3328\n\t"
"v_mfma_f32_16x16x32_bf16 v[136:139], a[40:43], a[152:155], v[136:139]\n\t"
"ds_read_b128 a[152:155], v224 offset:1536\n\t"
"v_xor_b32 v249, 32, v248\n\t"
"v_mfma_f32_16x16x32_bf16 v[136:139], a[44:47], a[168:171], v[136:139]\n\t"
"buffer_load_dwordx4 a[120:123], off, s[96:99], s78 offset:2048 sc1\n\t"
"v_mfma_f32_16x16x32_bf16 v[140:143], a[40:43], a[156:159], v[140:143]\n\t"
"ds_read_b128 a[240:243], v255 offset:18432\n\t"
"v_xor_b32 v250, 64, v248\n\t"
"v_mfma_f32_16x16x32_bf16 v[140:143], a[44:47], a[172:175], v[140:143]\n\t"
"ds_read_b128 a[168:171], v224 offset:3584\n\t"
"v_xor_b32 v251, 96, v248\n\t"
"v_mfma_f32_16x16x32_bf16 v[124:127], a[32:35], a[156:159], v[124:127]\n\t"
"ds_read_b64_tr_b16 a[178:179], v250 offset:43776\n\t"
"v_mfma_f32_16x16x32_bf16 v[124:127], a[36:39], a[172:175], v[124:127]\n\t"
"buffer_load_dwordx4 a[124:127], off, s[96:99], s78 offset:3072 sc1\n\t"
"v_mfma_f32_16x16x32_bf16 v[108:111], a[24:27], a[156:159], v[108:111]\n\t"
"ds_read_b128 a[156:159], v224 offset:1792\n\t"
"v_mfma_f32_16x16x32_bf16 v[108:111], a[28:31], a[172:175], v[108:111]\n\t"
"s_waitcnt vmcnt(18) lgkmcnt(6)\n\t"
"ds_read_b128 a[172:175], v224 offset:3840\n\t"
"v_mfma_f32_16x16x32_bf16 v[48:51], a[48:51], a[144:147], v[48:51]\n\t"
"ds_read_b64_tr_b16 a[186:187], v251 offset:43776\n\t"
"v_mfma_f32_16x16x32_bf16 v[48:51], a[52:55], a[160:163], v[48:51]\n\t"
"buffer_load_dwordx4 a[16:19], off, s[92:95], s74 offset:0 sc1\n\t"
"v_mfma_f32_16x16x32_bf16 v[64:67], a[56:59], a[144:147], v[64:67]\n\t"
"ds_read_b64_tr_b16 a[194:195], v248 offset:43776\n\t"
"v_mfma_f32_16x16x32_bf16 v[64:67], a[60:63], a[160:163], v[64:67]\n\t"
"s_nop 0\n\t"
"ds_read_b64_tr_b16 a[202:203], v249 offset:43776\n\t"
"ds_read_b64_tr_b16 a[176:177], v248 offset:41472\n\t"
"v_mfma_f32_16x16x32_bf16 v[80:83], a[64:67], a[144:147], v[80:83]\n\t"
"ds_read_b64_tr_b16 a[184:185], v249 offset:41472\n\t"
"v_mfma_f32_16x16x32_bf16 v[80:83], a[68:71], a[160:163], v[80:83]\n\t"
"s_waitcnt lgkmcnt(8)\n\t"
"buffer_load_dwordx4 a[20:23], off, s[92:95], s74 offset:1024 sc1\n\t"
"v_mfma_f32_16x16x32_bf16 v[84:87], a[64:67], a[148:151], v[84:87]\n\t"
"ds_read_b128 a[144:147], v224 offset:0\n\t"
"v_mfma_f32_16x16x32_bf16 v[84:87], a[68:71], a[164:167], v[84:87]\n\t"
"ds_read_b128 a[160:163], v224 offset:2048\n\t"
"v_mfma_f32_16x16x32_bf16 v[68:71], a[56:59], a[148:151], v[68:71]\n\t"
"ds_read_b64_tr_b16 a[180:181], v248 offset:46080\n\t"
"v_mfma_f32_16x16x32_bf16 v[68:71], a[60:63], a[164:167], v[68:71]\n\t"
"buffer_load_dwordx4 a[12:15], off, s[96:99], s72 offset:3072 sc1\n\t"
"v_mfma_f32_16x16x32_bf16 v[52:55], a[48:51], a[148:151], v[52:55]\n\t"
"ds_read_b128 a[148:151], v224 offset:256\n\t"
"v_mfma_f32_16x16x32_bf16 v[52:55], a[52:55], a[164:167], v[52:55]\n\t"
"ds_read_b128 a[164:167], v224 offset:2304\n\t"
"v_mfma_f32_16x16x32_bf16 v[56:59], a[48:51], a[152:155], v[56:59]\n\t"
"s_nop 0\n\t"
"ds_write_addtid_b32 v209 offset:30016\n\t"
"ds_write_addtid_b32 v201 offset:27936\n\t"
"v_mfma_f32_16x16x32_bf16 v[56:59], a[52:55], a[168:171], v[56:59]\n\t"
"buffer_load_dwordx4 a[8:11], off, s[96:99], s72 offset:2048 sc1\n\t"
"v_mfma_f32_16x16x32_bf16 v[72:75], a[56:59], a[152:155], v[72:75]\n\t"
"s_nop 0\n\t"
"ds_write_addtid_b32 v194 offset:16544\n\t"
"ds_write_addtid_b32 v211 offset:21696\n\t"
"v_mfma_f32_16x16x32_bf16 v[72:75], a[60:63], a[168:171], v[72:75]\n\t"
"s_nop 0\n\t"
"ds_write_addtid_b32 v200 offset:26912\n\t"
"ds_write_addtid_b32 v222 offset:22720\n\t"
"v_mfma_f32_16x16x32_bf16 v[88:91], a[64:67], a[152:155], v[88:91]\n\t"
"ds_read_b128 a[152:155], v224 offset:512\n\t"
"v_mfma_f32_16x16x32_bf16 v[88:91], a[68:71], a[168:171], v[88:91]\n\t"
"s_waitcnt lgkmcnt(10)\n\t"
"buffer_load_dwordx4 a[0:3], off, s[96:99], s72 offset:0 sc1\n\t"
"v_mfma_f32_16x16x32_bf16 v[92:95], a[64:67], a[156:159], v[92:95]\n\t"
"ds_read_b128 a[168:171], v224 offset:2560\n\t"
"v_mfma_f32_16x16x32_bf16 v[92:95], a[68:71], a[172:175], v[92:95]\n\t"
"s_nop 0\n\t"
"ds_write_addtid_b32 v202 offset:18592\n\t"
"ds_write_addtid_b32 v203 offset:19616\n\t"
"v_mfma_f32_16x16x32_bf16 v[76:79], a[56:59], a[156:159], v[76:79]\n\t"
"s_nop 0\n\t"
"ds_write_addtid_b32 v192 offset:24864\n\t"
"ds_write_addtid_b32 v210 offset:20672\n\t"
"v_mfma_f32_16x16x32_bf16 v[76:79], a[60:63], a[172:175], v[76:79]\n\t"
"buffer_load_dwordx4 a[4:7], off, s[96:99], s72 offset:1024 sc1\n\t"
"v_mfma_f32_16x16x32_bf16 v[60:63], a[48:51], a[156:159], v[60:63]\n\t"
"ds_read_b128 a[156:159], v224 offset:768\n\t"
"v_mfma_f32_16x16x32_bf16 v[60:63], a[52:55], a[172:175], v[60:63]\n\t"
"s_waitcnt vmcnt(18) lgkmcnt(11)\n\t"
"ds_write_addtid_b32 v195 offset:17568\n\t"
"ds_write_addtid_b32 v193 offset:25888\n\t"
"v_mfma_f32_16x16x32_bf16 v[0:3], a[72:75], a[144:147], v[0:3]\n\t"
"s_nop 0\n\t"
"ds_write_addtid_b32 v208 offset:28992\n\t"
"ds_write_addtid_b32 v221 offset:32064\n\t"
"v_mfma_f32_16x16x32_bf16 v[0:3], a[76:79], a[160:163], v[0:3]\n\t"
"buffer_load_dwordx4 a[40:43], off, s[92:95], s72 offset:2048 sc1\n\t"
"v_mfma_f32_16x16x32_bf16 v[16:19], a[80:83], a[144:147], v[16:19]\n\t"
"s_nop 0\n\t"
"ds_write_addtid_b32 v223 offset:23744\n\t"
"ds_write_addtid_b32 v220 offset:31040\n\t"
"v_mfma_f32_16x16x32_bf16 v[16:19], a[84:87], a[160:163], v[16:19]\n\t"
"ds_read_b128 a[172:175], v224 offset:2816\n\t"
"v_mfma_f32_16x16x32_bf16 v[32:35], a[88:91], a[144:147], v[32:35]\n\t"
"ds_read_b64_tr_b16 a[188:189], v249 offset:46080\n\t"
"v_mfma_f32_16x16x32_bf16 v[32:35], a[92:95], a[160:163], v[32:35]\n\t"
"buffer_load_dwordx4 a[44:47], off, s[92:95], s72 offset:3072 sc1\n\t"
"v_mfma_f32_16x16x32_bf16 v[36:39], a[88:91], a[148:151], v[36:39]\n\t"
"ds_read_b64_tr_b16 a[192:193], v250 offset:41472\n\t"
"v_mfma_f32_16x16x32_bf16 v[36:39], a[92:95], a[164:167], v[36:39]\n\t"
"ds_read_b64_tr_b16 a[200:201], v251 offset:41472\n\t"
"v_mfma_f32_16x16x32_bf16 v[20:23], a[80:83], a[148:151], v[20:23]\n\t"
"s_nop 0\n\t"
"ds_read_b64_tr_b16 a[196:197], v250 offset:46080\n\t"
"ds_read_b64_tr_b16 a[204:205], v251 offset:46080\n\t"
"v_mfma_f32_16x16x32_bf16 v[20:23], a[84:87], a[164:167], v[20:23]\n\t"
"buffer_load_dwordx4 a[36:39], off, s[92:95], s72 offset:1024 sc1\n\t"
"v_mfma_f32_16x16x32_bf16 v[4:7], a[72:75], a[148:151], v[4:7]\n\t"
"s_waitcnt lgkmcnt(5)\n\t"
"v_mfma_f32_16x16x32_bf16 v[4:7], a[76:79], a[164:167], v[4:7]\n\t"
"s_barrier\n\t"
"v_mfma_f32_16x16x32_bf16 v[8:11], a[72:75], a[152:155], v[8:11]\n\t"
"s_nop 0\n\t"
"ds_read_b128 a[144:147], v224 offset:17568\n\t"
"ds_read_b128 a[160:163], v224 offset:19616\n\t"
"v_mfma_f32_16x16x32_bf16 v[8:11], a[76:79], a[168:171], v[8:11]\n\t"
"buffer_load_dwordx4 a[32:35], off, s[92:95], s72 offset:0 sc1\n\t"
"v_mfma_f32_16x16x32_bf16 v[24:27], a[80:83], a[152:155], v[24:27]\n\t"
"ds_read_b128 a[148:151], v224 offset:17824\n\t"
"v_mfma_f32_16x16x32_bf16 v[24:27], a[84:87], a[168:171], v[24:27]\n\t"
"ds_read_b128 a[164:167], v224 offset:19872\n\t"
"v_mfma_f32_16x16x32_bf16 v[40:43], a[88:91], a[152:155], v[40:43]\n\t"
"v_mfma_f32_16x16x32_bf16 v[40:43], a[92:95], a[168:171], v[40:43]\n\t"
"buffer_load_dwordx4 a[24:27], off, s[96:99], s69 offset:2048 sc1\n\t"
"v_mfma_f32_16x16x32_bf16 v[44:47], a[88:91], a[156:159], v[44:47]\n\t"
"ds_read_b128 a[168:171], v224 offset:20128\n\t"
"v_mfma_f32_16x16x32_bf16 v[44:47], a[92:95], a[172:175], v[44:47]\n\t"
"buffer_load_dwordx4 a[28:31], off, s[96:99], s69 offset:3072 sc1\n\t"
"v_mfma_f32_16x16x32_bf16 v[28:31], a[80:83], a[156:159], v[28:31]\n\t"
"ds_read_b128 a[152:155], v224 offset:18080\n\t"
"v_mfma_f32_16x16x32_bf16 v[28:31], a[84:87], a[172:175], v[28:31]\n\t"
"buffer_load_dwordx4 a[64:67], off, s[92:95], s83 offset:0 sc1\n\t"
"v_mfma_f32_16x16x32_bf16 v[12:15], a[72:75], a[156:159], v[12:15]\n\t"
"ds_read_b128 a[156:159], v224 offset:18336\n\t"
"v_mfma_f32_16x16x32_bf16 v[12:15], a[76:79], a[172:175], v[12:15]\n\t"
"s_waitcnt lgkmcnt(5)\n\t"
"buffer_load_dwordx4 a[68:71], off, s[92:95], s83 offset:1024 sc1\n\t"
"v_mfma_f32_4x4x4_16b_bf16 v[200:203], a[220:221], a[178:179], 0\n\t"
"ds_read_b128 a[172:175], v224 offset:20384\n\t"
"v_mfma_f32_4x4x4_16b_bf16 v[204:207], a[220:221], a[186:187], 0\n\t"
"ds_read_b64_tr_b16 a[178:179], v250 offset:44928\n\t"
"v_mfma_f32_4x4x4_16b_bf16 v[216:219], a[220:221], a[194:195], 0\n\t"
"ds_read_b64_tr_b16 a[186:187], v251 offset:44928\n\t"
"v_mfma_f32_4x4x4_16b_bf16 v[220:223], a[220:221], a[202:203], 0\n\t"
"v_mfma_f32_4x4x4_16b_bf16 v[192:195], a[222:223], a[176:177], v[200:203]\n\t"
"ds_read_b64_tr_b16 a[194:195], v248 offset:44928\n\t"
"v_mfma_f32_4x4x4_16b_bf16 v[196:199], a[222:223], a[184:185], v[204:207]\n\t"
"ds_read_b64_tr_b16 a[202:203], v249 offset:44928\n\t"
"v_mfma_f32_4x4x4_16b_bf16 v[200:203], a[222:223], a[180:181], v[200:203]\n\t"
"ds_read_b64_tr_b16 a[176:177], v248 offset:42624\n\t"
"v_mfma_f32_4x4x4_16b_bf16 v[204:207], a[222:223], a[188:189], v[204:207]\n\t"
"ds_read_b64_tr_b16 a[184:185], v249 offset:42624\n\t"
"v_mfma_f32_4x4x4_16b_bf16 v[208:211], a[222:223], a[192:193], v[216:219]\n\t"
"ds_read_b64_tr_b16 a[180:181], v248 offset:47232\n\t"
"v_mfma_f32_4x4x4_16b_bf16 v[212:215], a[222:223], a[200:201], v[220:223]\n\t"
"ds_read_b64_tr_b16 a[188:189], v249 offset:47232\n\t"
"v_mfma_f32_4x4x4_16b_bf16 v[216:219], a[222:223], a[196:197], v[216:219]\n\t"
"ds_read_b64_tr_b16 a[192:193], v250 offset:42624\n\t"
"v_mfma_f32_4x4x4_16b_bf16 v[220:223], a[222:223], a[204:205], v[220:223]\n\t"
"s_waitcnt vmcnt(20) lgkmcnt(8)\n\t"
"buffer_load_dwordx4 a[60:63], off, s[96:99], s82 offset:3072 sc1\n\t"
"v_mfma_f32_16x16x32_bf16 v[144:147], a[96:99], a[144:147], v[144:147]\n\t"
"ds_read_b64_tr_b16 a[200:201], v251 offset:42624\n\t"
"v_mfma_f32_16x16x32_bf16 v[144:147], a[100:103], a[160:163], v[144:147]\n\t"
"buffer_load_dwordx4 a[56:59], off, s[96:99], s82 offset:2048 sc1\n\t"
"v_mfma_f32_16x16x32_bf16 v[160:163], a[104:107], a[144:147], v[160:163]\n\t"
"ds_read_b64_tr_b16 a[196:197], v250 offset:47232\n\t"
"v_cvt_pk_bf16_f32 v192, -v192, -v196\n\t"
"v_mfma_f32_16x16x32_bf16 v[160:163], a[108:111], a[160:163], v[160:163]\n\t"
"ds_write_addtid_b32 v192 offset:0\n\t"
"v_cvt_pk_bf16_f32 v201, v201, v205\n\t"
"v_mfma_f32_16x16x32_bf16 v[176:179], a[112:115], a[144:147], v[176:179]\n\t"
"ds_read_b128 a[144:147], v224 offset:16544\n\t"
"v_cvt_pk_bf16_f32 v211, -v211, -v215\n\t"
"v_mfma_f32_16x16x32_bf16 v[176:179], a[116:119], a[160:163], v[176:179]\n\t"
"buffer_load_dwordx4 a[48:51], off, s[96:99], s82 offset:0 sc1\n\t"
"v_mfma_f32_16x16x32_bf16 v[180:183], a[112:115], a[148:151], v[180:183]\n\t"
"ds_read_b128 a[160:163], v224 offset:18592\n\t"
"v_cvt_pk_bf16_f32 v202, v202, v206\n\t"
"v_mfma_f32_16x16x32_bf16 v[180:183], a[116:119], a[164:167], v[180:183]\n\t"
"ds_write_addtid_b32 v202 offset:5152\n\t"
"v_cvt_pk_bf16_f32 v210, -v210, -v214\n\t"
"v_mfma_f32_16x16x32_bf16 v[164:167], a[104:107], a[148:151], v[164:167]\n\t"
"ds_write_addtid_b32 v210 offset:3072\n\t"
"v_cvt_pk_bf16_f32 v219, v219, v223\n\t"
"v_mfma_f32_16x16x32_bf16 v[164:167], a[108:111], a[164:167], v[164:167]\n\t"
"buffer_load_dwordx4 a[52:55], off, s[96:99], s82 offset:1024 sc1\n\t"
"v_mfma_f32_16x16x32_bf16 v[148:151], a[96:99], a[148:151], v[148:151]\n\t"
"ds_read_b128 a[148:151], v224 offset:16800\n\t"
"v_cvt_pk_bf16_f32 v208, -v208, -v212\n\t"
"v_mfma_f32_16x16x32_bf16 v[148:151], a[100:103], a[164:167], v[148:151]\n\t"
"ds_write_addtid_b32 v208 offset:2048\n\t"
"v_cvt_pk_bf16_f32 v216, v216, v220\n\t"
"v_mfma_f32_16x16x32_bf16 v[152:155], a[96:99], a[152:155], v[152:155]\n\t"
"ds_read_b128 a[164:167], v224 offset:18848\n\t"
"v_cvt_pk_bf16_f32 v217, v217, v221\n\t"
"v_mfma_f32_16x16x32_bf16 v[152:155], a[100:103], a[168:171], v[152:155]\n\t"
"buffer_load_dwordx4 a[88:91], off, s[92:95], s82 offset:2048 sc1\n\t"
"v_mfma_f32_16x16x32_bf16 v[168:171], a[104:107], a[152:155], v[168:171]\n\t"
"ds_write_addtid_b32 v219 offset:15520\n\t"
"v_cvt_pk_bf16_f32 v203, v203, v207\n\t"
"v_mfma_f32_16x16x32_bf16 v[168:171], a[108:111], a[168:171], v[168:171]\n\t"
"ds_write_addtid_b32 v216 offset:6176\n\t"
"v_cvt_pk_bf16_f32 v218, v218, v222\n\t"
"v_mfma_f32_16x16x32_bf16 v[184:187], a[112:115], a[152:155], v[184:187]\n\t"
"ds_read_b128 a[152:155], v224 offset:17056\n\t"
"v_cvt_pk_bf16_f32 v200, v200, v204\n\t"
"v_mfma_f32_16x16x32_bf16 v[184:187], a[116:119], a[168:171], v[184:187]\n\t"
"buffer_load_dwordx4 a[92:95], off, s[92:95], s82 offset:3072 sc1\n\t"
"v_mfma_f32_16x16x32_bf16 v[188:191], a[112:115], a[156:159], v[188:191]\n\t"
"ds_read_b128 a[168:171], v224 offset:19104\n\t"
"v_cvt_pk_bf16_f32 v194, -v194, -v198\n\t"
"v_mfma_f32_16x16x32_bf16 v[188:191], a[116:119], a[172:175], v[188:191]\n\t"
"ds_write_addtid_b32 v203 offset:13472\n\t"
"v_cvt_pk_bf16_f32 v193, -v193, -v197\n\t"
"v_mfma_f32_16x16x32_bf16 v[172:175], a[104:107], a[156:159], v[172:175]\n\t"
"ds_write_addtid_b32 v200 offset:4128\n\t"
"v_cvt_pk_bf16_f32 v195, -v195, -v199\n\t"
"v_mfma_f32_16x16x32_bf16 v[172:175], a[108:111], a[172:175], v[172:175]\n\t"
"buffer_load_dwordx4 a[84:87], off, s[92:95], s82 offset:1024 sc1\n\t"
"v_mfma_f32_16x16x32_bf16 v[156:159], a[96:99], a[156:159], v[156:159]\n\t"
"s_nop 0\n\t"
"ds_write_addtid_b32 v193 offset:8320\n\t"
"ds_write_addtid_b32 v201 offset:12448\n\t"
"v_mfma_f32_16x16x32_bf16 v[156:159], a[100:103], a[172:175], v[156:159]\n\t"
"s_waitcnt vmcnt(21) lgkmcnt(8)\n\t"
"ds_write_addtid_b32 v217 offset:14496\n\t"
"v_cvt_pk_bf16_f32 v209, -v209, -v213\n\t"
"v_mfma_f32_16x16x32_bf16 v[96:99], a[120:123], a[144:147], v[96:99]\n\t"
"ds_write_addtid_b32 v209 offset:10368\n\t"
"v_mfma_f32_16x16x32_bf16 v[96:99], a[124:127], a[160:163], v[96:99]\n\t"
"s_nop 0\n\t"
"ds_write_addtid_b32 v211 offset:11392\n\t"
"ds_write_addtid_b32 v218 offset:7200\n\t"
"v_mfma_f32_16x16x32_bf16 v[112:115], a[128:131], a[144:147], v[112:115]\n\t"
"s_nop 0\n\t"
"ds_write_addtid_b32 v194 offset:1024\n\t"
"ds_write_addtid_b32 v195 offset:9344\n\t"
"v_mfma_f32_16x16x32_bf16 v[112:115], a[132:135], a[160:163], v[112:115]\n\t"
".p2align 3\n\t"
"buffer_load_dwordx4 a[80:83], off, s[92:95], s82 offset:0 sc1\n\t"
"v_pk_fma_f32 v[48:49], v[144:145], s[90:91], v[48:49]\n\t"
"v_pk_fma_f32 v[50:51], v[146:147], s[90:91], v[50:51]\n\t"
"v_pk_add_f32 v[144:145], v[48:49], v[144:145] neg_lo:[0,1] neg_hi:[0,1]\n\t"
"v_pk_add_f32 v[146:147], v[50:51], v[146:147] neg_lo:[0,1] neg_hi:[0,1]\n\t"
"v_pk_fma_f32 v[64:65], v[160:161], s[90:91], v[64:65]\n\t"
"v_pk_fma_f32 v[66:67], v[162:163], s[90:91], v[66:67]\n\t"
"v_pk_add_f32 v[160:161], v[64:65], v[160:161] neg_lo:[0,1] neg_hi:[0,1]\n\t"
"v_pk_add_f32 v[162:163], v[66:67], v[162:163] neg_lo:[0,1] neg_hi:[0,1]\n\t"
"v_pk_fma_f32 v[80:81], v[176:177], s[90:91], v[80:81]\n\t"
"v_pk_fma_f32 v[82:83], v[178:179], s[90:91], v[82:83]\n\t"
"v_pk_add_f32 v[176:177], v[80:81], v[176:177] neg_lo:[0,1] neg_hi:[0,1]\n\t"
"v_pk_add_f32 v[178:179], v[82:83], v[178:179] neg_lo:[0,1] neg_hi:[0,1]\n\t"
"buffer_load_dwordx4 a[72:75], off, s[96:99], s81 offset:2048 sc1\n\t"
"v_pk_fma_f32 v[84:85], v[180:181], s[90:91], v[84:85]\n\t"
"v_pk_fma_f32 v[86:87], v[182:183], s[90:91], v[86:87]\n\t"
"v_pk_add_f32 v[180:181], v[84:85], v[180:181] neg_lo:[0,1] neg_hi:[0,1]\n\t"
"v_pk_add_f32 v[182:183], v[86:87], v[182:183] neg_lo:[0,1] neg_hi:[0,1]\n\t"
"v_pk_fma_f32 v[68:69], v[164:165], s[90:91], v[68:69]\n\t"
"v_pk_fma_f32 v[70:71], v[166:167], s[90:91], v[70:71]\n\t"
"v_pk_add_f32 v[164:165], v[68:69], v[164:165] neg_lo:[0,1] neg_hi:[0,1]\n\t"
"v_pk_add_f32 v[166:167], v[70:71], v[166:167] neg_lo:[0,1] neg_hi:[0,1]\n\t"
"v_pk_fma_f32 v[52:53], v[148:149], s[90:91], v[52:53]\n\t"
"v_pk_fma_f32 v[54:55], v[150:151], s[90:91], v[54:55]\n\t"
"v_pk_add_f32 v[148:149], v[52:53], v[148:149] neg_lo:[0,1] neg_hi:[0,1]\n\t"
"v_pk_add_f32 v[150:151], v[54:55], v[150:151] neg_lo:[0,1] neg_hi:[0,1]\n\t"
"buffer_load_dwordx4 a[76:79], off, s[96:99], s81 offset:3072 sc1\n\t"
"v_pk_fma_f32 v[56:57], v[152:153], s[90:91], v[56:57]\n\t"
"v_pk_fma_f32 v[58:59], v[154:155], s[90:91], v[58:59]\n\t"
"v_pk_add_f32 v[152:153], v[56:57], v[152:153] neg_lo:[0,1] neg_hi:[0,1]\n\t"
"v_pk_add_f32 v[154:155], v[58:59], v[154:155] neg_lo:[0,1] neg_hi:[0,1]\n\t"
"v_pk_fma_f32 v[72:73], v[168:169], s[90:91], v[72:73]\n\t"
"v_pk_fma_f32 v[74:75], v[170:171], s[90:91], v[74:75]\n\t"
"v_pk_add_f32 v[168:169], v[72:73], v[168:169] neg_lo:[0,1] neg_hi:[0,1]\n\t"
"v_pk_add_f32 v[170:171], v[74:75], v[170:171] neg_lo:[0,1] neg_hi:[0,1]\n\t"
"v_pk_fma_f32 v[88:89], v[184:185], s[90:91], v[88:89]\n\t"
"v_pk_fma_f32 v[90:91], v[186:187], s[90:91], v[90:91]\n\t"
"v_pk_add_f32 v[184:185], v[88:89], v[184:185] neg_lo:[0,1] neg_hi:[0,1]\n\t"
"v_pk_add_f32 v[186:187], v[90:91], v[186:187] neg_lo:[0,1] neg_hi:[0,1]\n\t"
"buffer_load_dwordx4 a[112:115], off, s[92:95], s56 offset:0 sc1\n\t"
"v_pk_fma_f32 v[92:93], v[188:189], s[90:91], v[92:93]\n\t"
"v_pk_fma_f32 v[94:95], v[190:191], s[90:91], v[94:95]\n\t"
"v_pk_add_f32 v[188:189], v[92:93], v[188:189] neg_lo:[0,1] neg_hi:[0,1]\n\t"
"v_pk_add_f32 v[190:191], v[94:95], v[190:191] neg_lo:[0,1] neg_hi:[0,1]\n\t"
"v_pk_fma_f32 v[76:77], v[172:173], s[90:91], v[76:77]\n\t"
"v_pk_fma_f32 v[78:79], v[174:175], s[90:91], v[78:79]\n\t"
"v_pk_add_f32 v[172:173], v[76:77], v[172:173] neg_lo:[0,1] neg_hi:[0,1]\n\t"
"v_pk_add_f32 v[174:175], v[78:79], v[174:175] neg_lo:[0,1] neg_hi:[0,1]\n\t"
"v_pk_fma_f32 v[60:61], v[156:157], s[90:91], v[60:61]\n\t"
"v_pk_fma_f32 v[62:63], v[158:159], s[90:91], v[62:63]\n\t"
"v_pk_add_f32 v[156:157], v[60:61], v[156:157] neg_lo:[0,1] neg_hi:[0,1]\n\t"
"v_pk_add_f32 v[158:159], v[62:63], v[158:159] neg_lo:[0,1] neg_hi:[0,1]\n\t"
"v_mfma_f32_16x16x32_bf16 v[128:131], a[136:139], a[144:147], v[128:131]\n\t"
"ds_read_b128 a[172:175], v224 offset:19360\n\t"
"v_mfma_f32_16x16x32_bf16 v[128:131], a[140:143], a[160:163], v[128:131]\n\t"
"buffer_load_dwordx4 a[116:119], off, s[92:95], s56 offset:1024 sc1\n\t"
"v_mfma_f32_16x16x32_bf16 v[132:135], a[136:139], a[148:151], v[132:135]\n\t"
"ds_read_b128 a[156:159], v224 offset:17312\n\t"
"v_mfma_f32_16x16x32_bf16 v[132:135], a[140:143], a[164:167], v[132:135]\n\t"
"buffer_load_dwordx4 a[108:111], off, s[96:99], s55 offset:3072 sc1\n\t"
"v_mfma_f32_16x16x32_bf16 v[116:119], a[128:131], a[148:151], v[116:119]\n\t"
"ds_read_b64_tr_b16 a[204:205], v251 offset:47232\n\t"
"v_mfma_f32_16x16x32_bf16 v[116:119], a[132:135], a[164:167], v[116:119]\n\t"
"buffer_load_dwordx4 a[104:107], off, s[96:99], s55 offset:2048 sc1\n\t"
"v_mfma_f32_16x16x32_bf16 v[100:103], a[120:123], a[148:151], v[100:103]\n\t"
"v_accvgpr_read_b32 v224, a212\n\t"
"v_mfma_f32_16x16x32_bf16 v[100:103], a[124:127], a[164:167], v[100:103]\n\t"
"s_waitcnt lgkmcnt(0)\n\t"
"s_barrier\n\t"
"v_mfma_f32_16x16x32_bf16 v[104:107], a[120:123], a[152:155], v[104:107]\n\t"
"s_add_u32 m0, s26, 145152\n\t"
"ds_read_b128 a[144:147], v224 offset:1024\n\t"
"v_mfma_f32_16x16x32_bf16 v[104:107], a[124:127], a[168:171], v[104:107]\n\t"
"ds_read_b128 a[160:163], v224 offset:3072\n\t"
"v_mfma_f32_16x16x32_bf16 v[120:123], a[128:131], a[152:155], v[120:123]\n\t"
"ds_read_b128 a[148:151], v224 offset:1280\n\t"
"v_mfma_f32_16x16x32_bf16 v[120:123], a[132:135], a[168:171], v[120:123]\n\t"
"ds_read_b128 a[164:167], v224 offset:3328\n\t"
"v_mfma_f32_16x16x32_bf16 v[136:139], a[136:139], a[152:155], v[136:139]\n\t"
"v_mfma_f32_16x16x32_bf16 v[136:139], a[140:143], a[168:171], v[136:139]\n\t"
"buffer_load_dwordx4 v236, s[12:15], s22 offen offset:256 nt lds\n\t"
"v_mfma_f32_16x16x32_bf16 v[140:143], a[136:139], a[156:159], v[140:143]\n\t"
"ds_read_b128 a[152:155], v224 offset:1536\n\t"
"v_mfma_f32_16x16x32_bf16 v[140:143], a[140:143], a[172:175], v[140:143]\n\t"
"ds_read_b128 a[228:231], v254 offset:59904\n\t"
"v_mfma_f32_16x16x32_bf16 v[124:127], a[128:131], a[156:159], v[124:127]\n\t"
"ds_read_b128 a[168:171], v224 offset:3584\n\t"
"v_mfma_f32_16x16x32_bf16 v[124:127], a[132:135], a[172:175], v[124:127]\n\t"
"buffer_load_dwordx4 v237, s[12:15], s22 offen offset:1280 nt lds\n\t"
"v_mfma_f32_16x16x32_bf16 v[108:111], a[120:123], a[156:159], v[108:111]\n\t"
"s_nop 0\n\t"
"ds_read_b128 a[156:159], v224 offset:1792\n\t"
"v_mfma_f32_16x16x32_bf16 v[108:111], a[124:127], a[172:175], v[108:111]\n\t"
"buffer_load_dwordx4 a[96:99], off, s[96:99], s55 offset:0 sc1\n\t"
"v_mfma_f32_4x4x4_16b_bf16 v[200:203], a[220:221], a[178:179], 0\n\t"
"ds_read_b128 a[172:175], v224 offset:3840\n\t"
"v_mfma_f32_4x4x4_16b_bf16 v[204:207], a[220:221], a[186:187], 0\n\t"
"ds_read_b64_tr_b16 a[178:179], v250 offset:23040\n\t"
"v_mfma_f32_4x4x4_16b_bf16 v[216:219], a[220:221], a[194:195], 0\n\t"
"ds_read_b64_tr_b16 a[186:187], v251 offset:23040\n\t"
"v_mfma_f32_4x4x4_16b_bf16 v[220:223], a[220:221], a[202:203], 0\n\t"
"buffer_load_dwordx4 v238, s[12:15], s22 offen offset:2304 nt lds\n\t"
"v_mfma_f32_4x4x4_16b_bf16 v[192:195], a[222:223], a[176:177], v[200:203]\n\t"
"ds_read_b64_tr_b16 a[194:195], v248 offset:23040\n\t"
"v_mfma_f32_4x4x4_16b_bf16 v[196:199], a[222:223], a[184:185], v[204:207]\n\t"
"ds_read_b64_tr_b16 a[202:203], v249 offset:23040\n\t"
"v_mfma_f32_4x4x4_16b_bf16 v[200:203], a[222:223], a[180:181], v[200:203]\n\t"
"ds_read_b64_tr_b16 a[176:177], v248 offset:20736\n\t"
"v_mfma_f32_4x4x4_16b_bf16 v[204:207], a[222:223], a[188:189], v[204:207]\n\t"
".p2align 3\n\t"
"ds_read_b64_tr_b16 a[184:185], v249 offset:20736\n\t"
"v_pk_fma_f32 v[0:1], v[96:97], s[90:91], v[0:1]\n\t"
"v_pk_fma_f32 v[2:3], v[98:99], s[90:91], v[2:3]\n\t"
"v_pk_add_f32 v[96:97], v[0:1], v[96:97] neg_lo:[0,1] neg_hi:[0,1]\n\t"
"v_pk_add_f32 v[98:99], v[2:3], v[98:99] neg_lo:[0,1] neg_hi:[0,1]\n\t"
"v_pk_fma_f32 v[16:17], v[112:113], s[90:91], v[16:17]\n\t"
"v_pk_fma_f32 v[18:19], v[114:115], s[90:91], v[18:19]\n\t"
"v_pk_add_f32 v[112:113], v[16:17], v[112:113] neg_lo:[0,1] neg_hi:[0,1]\n\t"
"v_pk_add_f32 v[114:115], v[18:19], v[114:115] neg_lo:[0,1] neg_hi:[0,1]\n\t"
"v_pk_fma_f32 v[32:33], v[128:129], s[90:91], v[32:33]\n\t"
"v_pk_fma_f32 v[34:35], v[130:131], s[90:91], v[34:35]\n\t"
"v_pk_add_f32 v[128:129], v[32:33], v[128:129] neg_lo:[0,1] neg_hi:[0,1]\n\t"
"v_pk_add_f32 v[130:131], v[34:35], v[130:131] neg_lo:[0,1] neg_hi:[0,1]\n\t"
"v_pk_fma_f32 v[36:37], v[132:133], s[90:91], v[36:37]\n\t"
"v_pk_fma_f32 v[38:39], v[134:135], s[90:91], v[38:39]\n\t"
"v_pk_add_f32 v[132:133], v[36:37], v[132:133] neg_lo:[0,1] neg_hi:[0,1]\n\t"
"v_pk_add_f32 v[134:135], v[38:39], v[134:135] neg_lo:[0,1] neg_hi:[0,1]\n\t"
"v_pk_fma_f32 v[20:21], v[116:117], s[90:91], v[20:21]\n\t"
"v_pk_fma_f32 v[22:23], v[118:119], s[90:91], v[22:23]\n\t"
"v_pk_add_f32 v[116:117], v[20:21], v[116:117] neg_lo:[0,1] neg_hi:[0,1]\n\t"
"v_pk_add_f32 v[118:119], v[22:23], v[118:119] neg_lo:[0,1] neg_hi:[0,1]\n\t"
"v_pk_fma_f32 v[4:5], v[100:101], s[90:91], v[4:5]\n\t"
"v_pk_fma_f32 v[6:7], v[102:103], s[90:91], v[6:7]\n\t"
"v_pk_add_f32 v[100:101], v[4:5], v[100:101] neg_lo:[0,1] neg_hi:[0,1]\n\t"
"v_pk_add_f32 v[102:103], v[6:7], v[102:103] neg_lo:[0,1] neg_hi:[0,1]\n\t"
"v_pk_fma_f32 v[8:9], v[104:105], s[90:91], v[8:9]\n\t"
"v_pk_fma_f32 v[10:11], v[106:107], s[90:91], v[10:11]\n\t"
"v_pk_add_f32 v[104:105], v[8:9], v[104:105] neg_lo:[0,1] neg_hi:[0,1]\n\t"
"v_pk_add_f32 v[106:107], v[10:11], v[106:107] neg_lo:[0,1] neg_hi:[0,1]\n\t"
"v_pk_fma_f32 v[24:25], v[120:121], s[90:91], v[24:25]\n\t"
"v_pk_fma_f32 v[26:27], v[122:123], s[90:91], v[26:27]\n\t"
"v_pk_add_f32 v[120:121], v[24:25], v[120:121] neg_lo:[0,1] neg_hi:[0,1]\n\t"
"v_pk_add_f32 v[122:123], v[26:27], v[122:123] neg_lo:[0,1] neg_hi:[0,1]\n\t"
"v_pk_fma_f32 v[40:41], v[136:137], s[90:91], v[40:41]\n\t"
"v_pk_fma_f32 v[42:43], v[138:139], s[90:91], v[42:43]\n\t"
"v_pk_add_f32 v[136:137], v[40:41], v[136:137] neg_lo:[0,1] neg_hi:[0,1]\n\t"
"v_pk_add_f32 v[138:139], v[42:43], v[138:139] neg_lo:[0,1] neg_hi:[0,1]\n\t"
"v_pk_fma_f32 v[44:45], v[140:141], s[90:91], v[44:45]\n\t"
"v_pk_fma_f32 v[46:47], v[142:143], s[90:91], v[46:47]\n\t"
"v_pk_add_f32 v[140:141], v[44:45], v[140:141] neg_lo:[0,1] neg_hi:[0,1]\n\t"
"v_pk_add_f32 v[142:143], v[46:47], v[142:143] neg_lo:[0,1] neg_hi:[0,1]\n\t"
"v_pk_fma_f32 v[28:29], v[124:125], s[90:91], v[28:29]\n\t"
"v_pk_fma_f32 v[30:31], v[126:127], s[90:91], v[30:31]\n\t"
"v_pk_add_f32 v[124:125], v[28:29], v[124:125] neg_lo:[0,1] neg_hi:[0,1]\n\t"
"v_pk_add_f32 v[126:127], v[30:31], v[126:127] neg_lo:[0,1] neg_hi:[0,1]\n\t"
"v_pk_fma_f32 v[12:13], v[108:109], s[90:91], v[12:13]\n\t"
"v_pk_fma_f32 v[14:15], v[110:111], s[90:91], v[14:15]\n\t"
"v_pk_add_f32 v[108:109], v[12:13], v[108:109] neg_lo:[0,1] neg_hi:[0,1]\n\t"
"v_pk_add_f32 v[110:111], v[14:15], v[110:111] neg_lo:[0,1] neg_hi:[0,1]\n\t"
"v_mfma_f32_4x4x4_16b_bf16 v[208:211], a[222:223], a[192:193], v[216:219]\n\t"
"ds_read_b64_tr_b16 a[180:181], v248 offset:25344\n\t"
"v_mfma_f32_4x4x4_16b_bf16 v[212:215], a[222:223], a[200:201], v[220:223]\n\t"
"ds_read_b64_tr_b16 a[188:189], v249 offset:25344\n\t"
"v_mfma_f32_4x4x4_16b_bf16 v[216:219], a[222:223], a[196:197], v[216:219]\n\t"
"ds_read_b64_tr_b16 a[192:193], v250 offset:20736\n\t"
"v_mfma_f32_4x4x4_16b_bf16 v[220:223], a[222:223], a[204:205], v[220:223]\n\t"
"s_waitcnt vmcnt(26) lgkmcnt(8)\n\t"
"buffer_load_dwordx4 v239, s[12:15], s22 offen offset:3328 nt lds\n\t"
"v_mfma_f32_16x16x32_bf16 v[48:51], a[0:3], a[144:147], v[48:51]\n\t"
"s_nop 0\n\t"
"v_cvt_pk_bf16_f32 v193, -v193, -v197\n\t"
"v_mfma_f32_16x16x32_bf16 v[48:51], a[4:7], a[160:163], v[48:51]\n\t"
"s_nop 0\n\t"
"ds_write_b128 v255, a[228:231] offset:41472\n\t"
"v_mfma_f32_16x16x32_bf16 v[64:67], a[8:11], a[144:147], v[64:67]\n\t"
"Lpad4_%=:\n\t"
"s_cbranch_execz Lret0_%=\n\t"
"v_mfma_f32_16x16x32_bf16 v[64:67], a[12:15], a[160:163], v[64:67]\n\t"
"s_mov_b64 exec, s[36:37]\n\t"
"buffer_load_dwordx4 v240, s[12:15], s22 offen offset:3840 nt lds\n\t"
"s_mov_b64 exec, -1\n\t"
"v_mfma_f32_16x16x32_bf16 v[80:83], a[16:19], a[144:147], v[80:83]\n\t"
"s_lshl_b32 m0, s73, 8\n\t"
"v_cvt_pk_bf16_f32 v211, -v211, -v215\n\t"
"v_mfma_f32_16x16x32_bf16 v[80:83], a[20:23], a[160:163], v[80:83]\n\t"
"ds_read_b128 a[160:163], v224 offset:2048\n\t"
"v_cvt_pk_bf16_f32 v203, v203, v207\n\t"
"v_mfma_f32_16x16x32_bf16 v[84:87], a[16:19], a[148:151], v[84:87]\n\t"
"ds_read_b128 a[144:147], v224 offset:0\n\t"
"v_cvt_pk_bf16_f32 v216, v216, v220\n\t"
"v_mfma_f32_16x16x32_bf16 v[84:87], a[20:23], a[164:167], v[84:87]\n\t"
"buffer_load_dwordx4 a[100:103], off, s[96:99], s55 offset:1024 sc1\n\t"
"v_mfma_f32_16x16x32_bf16 v[68:71], a[8:11], a[148:151], v[68:71]\n\t"
"ds_read_b64_tr_b16 a[200:201], v251 offset:20736\n\t"
"v_cvt_pk_bf16_f32 v192, -v192, -v196\n\t"
"v_mfma_f32_16x16x32_bf16 v[68:71], a[12:15], a[164:167], v[68:71]\n\t"
"v_cvt_pk_bf16_f32 v210, -v210, -v214\n\t"
"v_cvt_pk_bf16_f32 v217, v217, v221\n\t"
"v_mfma_f32_16x16x32_bf16 v[52:55], a[0:3], a[148:151], v[52:55]\n\t"
"ds_read_b128 a[148:151], v224 offset:256\n\t"
"v_mfma_f32_16x16x32_bf16 v[52:55], a[4:7], a[164:167], v[52:55]\n\t"
"ds_read_b128 a[164:167], v224 offset:2304\n\t"
"v_cvt_pk_bf16_f32 v208, -v208, -v212\n\t"
"v_mfma_f32_16x16x32_bf16 v[56:59], a[0:3], a[152:155], v[56:59]\n\t"
"ds_write_addtid_b32 v211 offset:27936\n\t"
"v_cvt_pk_bf16_f32 v201, v201, v205\n\t"
"v_mfma_f32_16x16x32_bf16 v[56:59], a[4:7], a[168:171], v[56:59]\n\t"
"ds_write_addtid_b32 v208 offset:18592\n\t"
"v_cvt_pk_bf16_f32 v195, -v195, -v199\n\t"
"v_mfma_f32_16x16x32_bf16 v[72:75], a[8:11], a[152:155], v[72:75]\n\t"
"ds_write_addtid_b32 v203 offset:30016\n\t"
"v_cvt_pk_bf16_f32 v200, v200, v204\n\t"
"v_mfma_f32_16x16x32_bf16 v[72:75], a[12:15], a[168:171], v[72:75]\n\t"
"ds_write_addtid_b32 v210 offset:19616\n\t"
"v_cvt_pk_bf16_f32 v194, -v194, -v198\n\t"
"v_mfma_f32_16x16x32_bf16 v[88:91], a[16:19], a[152:155], v[88:91]\n\t"
"ds_read_b128 a[152:155], v224 offset:512\n\t"
"v_cvt_pk_bf16_f32 v202, v202, v206\n\t"
"v_mfma_f32_16x16x32_bf16 v[88:91], a[20:23], a[168:171], v[88:91]\n\t"
"ds_write_addtid_b32 v201 offset:28992\n\t"
"v_cvt_pk_bf16_f32 v218, v218, v222\n\t"
"v_mfma_f32_16x16x32_bf16 v[92:95], a[16:19], a[156:159], v[92:95]\n\t"
"ds_write_addtid_b32 v192 offset:16544\n\t"
"v_mfma_f32_16x16x32_bf16 v[92:95], a[20:23], a[172:175], v[92:95]\n\t"
"ds_write_addtid_b32 v195 offset:25888\n\t"
"v_cvt_pk_bf16_f32 v219, v219, v223\n\t"
"v_mfma_f32_16x16x32_bf16 v[76:79], a[8:11], a[156:159], v[76:79]\n\t"
"s_nop 0\n\t"
"ds_write_addtid_b32 v202 offset:21696\n\t"
"ds_write_addtid_b32 v216 offset:22720\n\t"
"v_mfma_f32_16x16x32_bf16 v[76:79], a[12:15], a[172:175], v[76:79]\n\t"
"ds_write_addtid_b32 v217 offset:31040\n\t"
"v_mfma_f32_16x16x32_bf16 v[60:63], a[0:3], a[156:159], v[60:63]\n\t"
"ds_write_addtid_b32 v194 offset:17568\n\t"
"v_cvt_pk_bf16_f32 v209, -v209, -v213\n\t"
"v_mfma_f32_16x16x32_bf16 v[60:63], a[4:7], a[172:175], v[60:63]\n\t"
"s_waitcnt vmcnt(23) lgkmcnt(10)\n\t"
"ds_write_addtid_b32 v218 offset:23744\n\t"
"v_mfma_f32_16x16x32_bf16 v[0:3], a[24:27], a[144:147], v[0:3]\n\t"
"s_nop 0\n\t"
"ds_write_addtid_b32 v193 offset:24864\n\t"
"ds_write_addtid_b32 v219 offset:32064\n\t"
"v_mfma_f32_16x16x32_bf16 v[0:3], a[28:31], a[160:163], v[0:3]\n\t"
"ds_write_addtid_b32 v209 offset:26912\n\t"
"v_mfma_f32_16x16x32_bf16 v[16:19], a[32:35], a[144:147], v[16:19]\n\t"
"ds_write_addtid_b32 v200 offset:20672\n\t"
"v_mfma_f32_16x16x32_bf16 v[16:19], a[36:39], a[160:163], v[16:19]\n\t"
"ds_read_b128 a[172:175], v224 offset:2816\n\t"
"v_mfma_f32_16x16x32_bf16 v[32:35], a[40:43], a[144:147], v[32:35]\n\t"
"ds_read_b128 a[156:159], v224 offset:768\n\t"
"v_mfma_f32_16x16x32_bf16 v[32:35], a[44:47], a[160:163], v[32:35]\n\t"
"v_mfma_f32_16x16x32_bf16 v[36:39], a[40:43], a[148:151], v[36:39]\n\t"
"ds_read_b128 a[168:171], v224 offset:2560\n\t"
"v_mfma_f32_16x16x32_bf16 v[36:39], a[44:47], a[164:167], v[36:39]\n\t"
"ds_read_b64_tr_b16 a[196:197], v250 offset:25344\n\t"
"v_mfma_f32_16x16x32_bf16 v[20:23], a[32:35], a[148:151], v[20:23]\n\t"
"ds_read_b64_tr_b16 a[204:205], v251 offset:25344\n\t"
"v_mfma_f32_16x16x32_bf16 v[20:23], a[36:39], a[164:167], v[20:23]\n\t"
"v_mfma_f32_16x16x32_bf16 v[4:7], a[24:27], a[148:151], v[4:7]\n\t"
"s_waitcnt lgkmcnt(2)\n\t"
"v_mfma_f32_16x16x32_bf16 v[4:7], a[28:31], a[164:167], v[4:7]\n\t"
"s_barrier\n\t"
"v_mfma_f32_16x16x32_bf16 v[8:11], a[24:27], a[152:155], v[8:11]\n\t"
"s_nop 0\n\t"
"ds_read_b128 a[144:147], v224 offset:17568\n\t"
"ds_read_b128 a[148:151], v224 offset:17824\n\t"
"v_mfma_f32_16x16x32_bf16 v[8:11], a[28:31], a[168:171], v[8:11]\n\t"
"buffer_load_dwordx4 a[136:139], off, s[92:95], s55 offset:2048 sc1\n\t"
"v_mfma_f32_16x16x32_bf16 v[24:27], a[32:35], a[152:155], v[24:27]\n\t"
"ds_read_b128 a[160:163], v224 offset:19616\n\t"
"v_mfma_f32_16x16x32_bf16 v[24:27], a[36:39], a[168:171], v[24:27]\n\t"
"ds_read_b128 a[164:167], v224 offset:19872\n\t"
"v_mfma_f32_16x16x32_bf16 v[40:43], a[40:43], a[152:155], v[40:43]\n\t"
"v_mfma_f32_16x16x32_bf16 v[40:43], a[44:47], a[168:171], v[40:43]\n\t"
"buffer_load_dwordx4 a[140:143], off, s[92:95], s55 offset:3072 sc1\n\t"
"v_mfma_f32_16x16x32_bf16 v[44:47], a[40:43], a[156:159], v[44:47]\n\t"
"ds_read_b128 a[168:171], v224 offset:20128\n\t"
"v_mfma_f32_16x16x32_bf16 v[44:47], a[44:47], a[172:175], v[44:47]\n\t"
"buffer_load_dwordx4 a[132:135], off, s[92:95], s55 offset:1024 sc1\n\t"
"v_mfma_f32_16x16x32_bf16 v[28:31], a[32:35], a[156:159], v[28:31]\n\t"
"ds_read_b128 a[152:155], v224 offset:18080\n\t"
"v_mfma_f32_16x16x32_bf16 v[28:31], a[36:39], a[172:175], v[28:31]\n\t"
"v_mfma_f32_16x16x32_bf16 v[12:15], a[24:27], a[156:159], v[12:15]\n\t"
"ds_read_b128 a[156:159], v224 offset:18336\n\t"
"v_mfma_f32_16x16x32_bf16 v[12:15], a[28:31], a[172:175], v[12:15]\n\t"
"s_waitcnt lgkmcnt(5)\n\t"
"buffer_load_dwordx4 a[128:131], off, s[92:95], s55 offset:0 sc1\n\t"
"v_mfma_f32_4x4x4_16b_bf16 v[200:203], a[220:221], a[178:179], 0\n\t"
"ds_read_b128 a[172:175], v224 offset:20384\n\t"
"v_mfma_f32_4x4x4_16b_bf16 v[204:207], a[220:221], a[186:187], 0\n\t"
"ds_read_b64_tr_b16 a[178:179], v250 offset:2304\n\t"
"v_mfma_f32_4x4x4_16b_bf16 v[216:219], a[220:221], a[194:195], 0\n\t"
"ds_read_b64_tr_b16 a[186:187], v251 offset:2304\n\t"
"v_mfma_f32_4x4x4_16b_bf16 v[220:223], a[220:221], a[202:203], 0\n\t"
"v_mfma_f32_4x4x4_16b_bf16 v[192:195], a[222:223], a[176:177], v[200:203]\n\t"
"ds_read_b64_tr_b16 a[194:195], v248 offset:2304\n\t"
"v_mfma_f32_4x4x4_16b_bf16 v[196:199], a[222:223], a[184:185], v[204:207]\n\t"
"ds_read_b64_tr_b16 a[202:203], v249 offset:2304\n\t"
"v_mfma_f32_4x4x4_16b_bf16 v[200:203], a[222:223], a[180:181], v[200:203]\n\t"
"ds_read_b64_tr_b16 a[176:177], v248 offset:0\n\t"
"v_mfma_f32_4x4x4_16b_bf16 v[204:207], a[222:223], a[188:189], v[204:207]\n\t"
"v_mfma_f32_4x4x4_16b_bf16 v[208:211], a[222:223], a[192:193], v[216:219]\n\t"
"ds_read_b64_tr_b16 a[184:185], v249 offset:0\n\t"
"v_mfma_f32_4x4x4_16b_bf16 v[212:215], a[222:223], a[200:201], v[220:223]\n\t"
"ds_read_b64_tr_b16 a[180:181], v248 offset:4608\n\t"
"v_mfma_f32_4x4x4_16b_bf16 v[216:219], a[222:223], a[196:197], v[216:219]\n\t"
"ds_read_b64_tr_b16 a[188:189], v249 offset:4608\n\t"
"v_mfma_f32_4x4x4_16b_bf16 v[220:223], a[222:223], a[204:205], v[220:223]\n\t"
"s_waitcnt vmcnt(21) lgkmcnt(7)\n\t"
"buffer_load_dwordx4 a[120:123], off, s[96:99], s54 offset:2048 sc1\n\t"
"v_mfma_f32_16x16x32_bf16 v[144:147], a[48:51], a[144:147], v[144:147]\n\t"
"ds_read_b64_tr_b16 a[192:193], v250 offset:0\n\t"
"v_cvt_pk_bf16_f32 v192, -v192, -v196\n\t"
"v_mfma_f32_16x16x32_bf16 v[144:147], a[52:55], a[160:163], v[144:147]\n\t"
"buffer_load_dwordx4 a[124:127], off, s[96:99], s54 offset:3072 sc1\n\t"
"v_mfma_f32_16x16x32_bf16 v[160:163], a[56:59], a[144:147], v[160:163]\n\t"
"ds_read_b64_tr_b16 a[200:201], v251 offset:0\n\t"
"v_cvt_pk_bf16_f32 v193, -v193, -v197\n\t"
"v_mfma_f32_16x16x32_bf16 v[160:163], a[60:63], a[160:163], v[160:163]\n\t"
"ds_write_addtid_b32 v193 offset:8320\n\t"
"v_cvt_pk_bf16_f32 v194, -v194, -v198\n\t"
"v_mfma_f32_16x16x32_bf16 v[176:179], a[64:67], a[144:147], v[176:179]\n\t"
"ds_read_b128 a[144:147], v224 offset:16544\n\t"
"v_cvt_pk_bf16_f32 v211, -v211, -v215\n\t"
"v_mfma_f32_16x16x32_bf16 v[176:179], a[68:71], a[160:163], v[176:179]\n\t"
"buffer_load_dwordx4 a[16:19], off, s[92:95], s41 offset:0 sc1\n\t"
"v_mfma_f32_16x16x32_bf16 v[180:183], a[64:67], a[148:151], v[180:183]\n\t"
"ds_read_b128 a[160:163], v224 offset:18592\n\t"
"v_cvt_pk_bf16_f32 v219, v219, v223\n\t"
"v_mfma_f32_16x16x32_bf16 v[180:183], a[68:71], a[164:167], v[180:183]\n\t"
"ds_write_addtid_b32 v194 offset:1024\n\t"
"v_cvt_pk_bf16_f32 v217, v217, v221\n\t"
"v_mfma_f32_16x16x32_bf16 v[164:167], a[56:59], a[148:151], v[164:167]\n\t"
"ds_write_addtid_b32 v192 offset:0\n\t"
"v_cvt_pk_bf16_f32 v209, -v209, -v213\n\t"
"v_mfma_f32_16x16x32_bf16 v[164:167], a[60:63], a[164:167], v[164:167]\n\t"
"buffer_load_dwordx4 a[20:23], off, s[92:95], s41 offset:1024 sc1\n\t"
"v_mfma_f32_16x16x32_bf16 v[148:151], a[48:51], a[148:151], v[148:151]\n\t"
"ds_read_b128 a[148:151], v224 offset:16800\n\t"
"v_cvt_pk_bf16_f32 v195, -v195, -v199\n\t"
"v_mfma_f32_16x16x32_bf16 v[148:151], a[52:55], a[164:167], v[148:151]\n\t"
"ds_write_addtid_b32 v211 offset:11392\n\t"
"v_cvt_pk_bf16_f32 v201, v201, v205\n\t"
"v_mfma_f32_16x16x32_bf16 v[152:155], a[48:51], a[152:155], v[152:155]\n\t"
"ds_read_b128 a[164:167], v224 offset:18848\n\t"
"v_cvt_pk_bf16_f32 v216, v216, v220\n\t"
"v_mfma_f32_16x16x32_bf16 v[152:155], a[52:55], a[168:171], v[152:155]\n\t"
"buffer_load_dwordx4 a[12:15], off, s[96:99], s40 offset:3072 sc1\n\t"
"v_mfma_f32_16x16x32_bf16 v[168:171], a[56:59], a[152:155], v[168:171]\n\t"
"ds_write_addtid_b32 v195 offset:9344\n\t"
"v_cvt_pk_bf16_f32 v210, -v210, -v214\n\t"
"v_mfma_f32_16x16x32_bf16 v[168:171], a[60:63], a[168:171], v[168:171]\n\t"
"ds_write_addtid_b32 v209 offset:10368\n\t"
"v_cvt_pk_bf16_f32 v218, v218, v222\n\t"
"v_mfma_f32_16x16x32_bf16 v[184:187], a[64:67], a[152:155], v[184:187]\n\t"
"ds_read_b128 a[152:155], v224 offset:17056\n\t"
"v_cvt_pk_bf16_f32 v200, v200, v204\n\t"
"v_mfma_f32_16x16x32_bf16 v[184:187], a[68:71], a[168:171], v[184:187]\n\t"
"buffer_load_dwordx4 a[8:11], off, s[96:99], s40 offset:2048 sc1\n\t"
"v_mfma_f32_16x16x32_bf16 v[188:191], a[64:67], a[156:159], v[188:191]\n\t"
"ds_read_b128 a[168:171], v224 offset:19104\n\t"
"v_cvt_pk_bf16_f32 v203, v203, v207\n\t"
"v_mfma_f32_16x16x32_bf16 v[188:191], a[68:71], a[172:175], v[188:191]\n\t"
"ds_write_addtid_b32 v217 offset:14496\n\t"
"v_cvt_pk_bf16_f32 v202, v202, v206\n\t"
"v_mfma_f32_16x16x32_bf16 v[172:175], a[56:59], a[156:159], v[172:175]\n\t"
"s_nop 0\n\t"
"ds_write_addtid_b32 v202 offset:5152\n\t"
"ds_write_addtid_b32 v216 offset:6176\n\t"
"v_mfma_f32_16x16x32_bf16 v[172:175], a[60:63], a[172:175], v[172:175]\n\t"
"buffer_load_dwordx4 a[0:3], off, s[96:99], s40 offset:0 sc1\n\t"
"v_mfma_f32_16x16x32_bf16 v[156:159], a[48:51], a[156:159], v[156:159]\n\t"
"s_nop 0\n\t"
"ds_write_addtid_b32 v210 offset:3072\n\t"
"ds_write_addtid_b32 v219 offset:15520\n\t"
"v_mfma_f32_16x16x32_bf16 v[156:159], a[52:55], a[172:175], v[156:159]\n\t"
"s_waitcnt vmcnt(22) lgkmcnt(9)\n\t"
"ds_write_addtid_b32 v203 offset:13472\n\t"
"v_mfma_f32_16x16x32_bf16 v[96:99], a[72:75], a[144:147], v[96:99]\n\t"
"ds_write_addtid_b32 v200 offset:4128\n\t"
"v_cvt_pk_bf16_f32 v208, -v208, -v212\n\t"
"v_mfma_f32_16x16x32_bf16 v[96:99], a[76:79], a[160:163], v[96:99]\n\t"
"ds_write_addtid_b32 v218 offset:7200\n\t"
"v_mfma_f32_16x16x32_bf16 v[112:115], a[80:83], a[144:147], v[112:115]\n\t"
"s_nop 0\n\t"
"ds_write_addtid_b32 v201 offset:12448\n\t"
"ds_write_addtid_b32 v208 offset:2048\n\t"
"v_mfma_f32_16x16x32_bf16 v[112:115], a[84:87], a[160:163], v[112:115]\n\t"
"buffer_load_dwordx4 a[4:7], off, s[96:99], s40 offset:1024 sc1\n\t"
"v_mfma_f32_16x16x32_bf16 v[128:131], a[88:91], a[144:147], v[128:131]\n\t"
"ds_read_b128 a[172:175], v224 offset:19360\n\t"
"v_mfma_f32_16x16x32_bf16 v[128:131], a[92:95], a[160:163], v[128:131]\n\t"
"buffer_load_dwordx4 a[40:43], off, s[92:95], s40 offset:2048 sc1\n\t"
"v_mfma_f32_16x16x32_bf16 v[132:135], a[88:91], a[148:151], v[132:135]\n\t"
"ds_read_b128 a[156:159], v224 offset:17312\n\t"
"v_mfma_f32_16x16x32_bf16 v[132:135], a[92:95], a[164:167], v[132:135]\n\t"
"ds_read_b64_tr_b16 a[196:197], v250 offset:4608\n\t"
"v_mfma_f32_16x16x32_bf16 v[116:119], a[80:83], a[148:151], v[116:119]\n\t"
"ds_read_b64_tr_b16 a[204:205], v251 offset:4608\n\t"
"v_mfma_f32_16x16x32_bf16 v[116:119], a[84:87], a[164:167], v[116:119]\n\t"
"buffer_load_dwordx4 a[44:47], off, s[92:95], s40 offset:3072 sc1\n\t"
"v_mfma_f32_16x16x32_bf16 v[100:103], a[72:75], a[148:151], v[100:103]\n\t"
"s_waitcnt lgkmcnt(2)\n\t"
"v_mfma_f32_16x16x32_bf16 v[100:103], a[76:79], a[164:167], v[100:103]\n\t"
"buffer_load_dwordx4 a[36:39], off, s[92:95], s40 offset:1024 sc1\n\t"
"s_barrier\n\t"
"v_mfma_f32_16x16x32_bf16 v[104:107], a[72:75], a[152:155], v[104:107]\n\t"
"ds_read_b128 a[144:147], v224 offset:1024\n\t"
"v_mfma_f32_16x16x32_bf16 v[104:107], a[76:79], a[168:171], v[104:107]\n\t"
"ds_read_b128 a[160:163], v224 offset:3072\n\t"
"v_mfma_f32_16x16x32_bf16 v[120:123], a[80:83], a[152:155], v[120:123]\n\t"
"ds_read_b128 a[148:151], v224 offset:1280\n\t"
"v_mfma_f32_16x16x32_bf16 v[120:123], a[84:87], a[168:171], v[120:123]\n\t"
"ds_read_b128 a[164:167], v224 offset:3328\n\t"
"v_mfma_f32_16x16x32_bf16 v[136:139], a[88:91], a[152:155], v[136:139]\n\t"
"v_mfma_f32_16x16x32_bf16 v[136:139], a[92:95], a[168:171], v[136:139]\n\t"
"buffer_load_dwordx4 a[32:35], off, s[92:95], s40 offset:0 sc1\n\t"
"v_mfma_f32_16x16x32_bf16 v[140:143], a[88:91], a[156:159], v[140:143]\n\t"
"ds_read_b128 a[168:171], v224 offset:3584\n\t"
"v_mfma_f32_16x16x32_bf16 v[140:143], a[92:95], a[172:175], v[140:143]\n\t"
"ds_read_b128 a[152:155], v224 offset:1536\n\t"
"v_mfma_f32_16x16x32_bf16 v[124:127], a[80:83], a[156:159], v[124:127]\n\t"
"v_mfma_f32_16x16x32_bf16 v[124:127], a[84:87], a[172:175], v[124:127]\n\t"
"buffer_load_dwordx4 a[24:27], off, s[96:99], 0 offset:2048 sc1\n\t"
"v_mfma_f32_16x16x32_bf16 v[108:111], a[72:75], a[156:159], v[108:111]\n\t"
"ds_read_b128 a[156:159], v224 offset:1792\n\t"
"v_mfma_f32_16x16x32_bf16 v[108:111], a[76:79], a[172:175], v[108:111]\n\t"
"s_waitcnt lgkmcnt(5)\n\t"
"ds_read_b128 a[172:175], v224 offset:3840\n\t"
"v_mfma_f32_4x4x4_16b_bf16 v[200:203], a[220:221], a[178:179], 0\n\t"
"v_mfma_f32_4x4x4_16b_bf16 v[204:207], a[220:221], a[186:187], 0\n\t"
"ds_read_b64_tr_b16 a[178:179], v250 offset:24192\n\t"
"v_mfma_f32_4x4x4_16b_bf16 v[216:219], a[220:221], a[194:195], 0\n\t"
"ds_read_b64_tr_b16 a[186:187], v251 offset:24192\n\t"
"v_mfma_f32_4x4x4_16b_bf16 v[220:223], a[220:221], a[202:203], 0\n\t"
"ds_read_b64_tr_b16 a[194:195], v248 offset:24192\n\t"
"v_mfma_f32_4x4x4_16b_bf16 v[192:195], a[222:223], a[176:177], v[200:203]\n\t"
"ds_read_b64_tr_b16 a[202:203], v249 offset:24192\n\t"
"v_mfma_f32_4x4x4_16b_bf16 v[196:199], a[222:223], a[184:185], v[204:207]\n\t"
"v_mfma_f32_4x4x4_16b_bf16 v[200:203], a[222:223], a[180:181], v[200:203]\n\t"
"ds_read_b64_tr_b16 a[176:177], v248 offset:21888\n\t"
"v_mfma_f32_4x4x4_16b_bf16 v[204:207], a[222:223], a[188:189], v[204:207]\n\t"
"ds_read_b64_tr_b16 a[184:185], v249 offset:21888\n\t"
"v_mfma_f32_4x4x4_16b_bf16 v[208:211], a[222:223], a[192:193], v[216:219]\n\t"
"ds_read_b64_tr_b16 a[180:181], v248 offset:26496\n\t"
"v_mfma_f32_4x4x4_16b_bf16 v[212:215], a[222:223], a[200:201], v[220:223]\n\t"
"ds_read_b64_tr_b16 a[188:189], v249 offset:26496\n\t"
"v_mfma_f32_4x4x4_16b_bf16 v[216:219], a[222:223], a[196:197], v[216:219]\n\t"
"ds_read_b64_tr_b16 a[192:193], v250 offset:21888\n\t"
"v_mfma_f32_4x4x4_16b_bf16 v[220:223], a[222:223], a[204:205], v[220:223]\n\t"
"s_waitcnt vmcnt(17) lgkmcnt(8)\n\t"
"buffer_load_dwordx4 a[28:31], off, s[96:99], 0 offset:3072 sc1\n\t"
"v_mfma_f32_16x16x32_bf16 v[48:51], a[96:99], a[144:147], v[48:51]\n\t"
"ds_read_b64_tr_b16 a[200:201], v251 offset:21888\n\t"
"v_cvt_pk_bf16_f32 v192, -v192, -v196\n\t"
"v_mfma_f32_16x16x32_bf16 v[48:51], a[100:103], a[160:163], v[48:51]\n\t"
"buffer_load_dwordx4 a[64:67], off, s[92:95], s68 offset:0 sc1\n\t"
"v_mfma_f32_16x16x32_bf16 v[64:67], a[104:107], a[144:147], v[64:67]\n\t"
"ds_read_b64_tr_b16 a[196:197], v250 offset:26496\n\t"
"v_cvt_pk_bf16_f32 v195, -v195, -v199\n\t"
"v_mfma_f32_16x16x32_bf16 v[64:67], a[108:111], a[160:163], v[64:67]\n\t"
"ds_write_addtid_b32 v192 offset:16544\n\t"
"v_cvt_pk_bf16_f32 v202, v202, v206\n\t"
"v_mfma_f32_16x16x32_bf16 v[80:83], a[112:115], a[144:147], v[80:83]\n\t"
"ds_read_b128 a[144:147], v224 offset:0\n\t"
"v_cvt_pk_bf16_f32 v193, -v193, -v197\n\t"
"v_mfma_f32_16x16x32_bf16 v[80:83], a[116:119], a[160:163], v[80:83]\n\t"
"buffer_load_dwordx4 a[68:71], off, s[92:95], s68 offset:1024 sc1\n\t"
"v_mfma_f32_16x16x32_bf16 v[84:87], a[112:115], a[148:151], v[84:87]\n\t"
"ds_read_b128 a[160:163], v224 offset:2048\n\t"
"v_cvt_pk_bf16_f32 v203, v203, v207\n\t"
"v_mfma_f32_16x16x32_bf16 v[84:87], a[116:119], a[164:167], v[84:87]\n\t"
"ds_write_addtid_b32 v202 offset:21696\n\t"
"v_cvt_pk_bf16_f32 v200, v200, v204\n\t"
"v_mfma_f32_16x16x32_bf16 v[68:71], a[104:107], a[148:151], v[68:71]\n\t"
"ds_write_addtid_b32 v200 offset:20672\n\t"
"v_mfma_f32_16x16x32_bf16 v[68:71], a[108:111], a[164:167], v[68:71]\n\t"
"buffer_load_dwordx4 a[60:63], off, s[96:99], s67 offset:3072 sc1\n\t"
"v_mfma_f32_16x16x32_bf16 v[52:55], a[96:99], a[148:151], v[52:55]\n\t"
"ds_read_b128 a[148:151], v224 offset:256\n\t"
"v_cvt_pk_bf16_f32 v217, v217, v221\n\t"
"v_mfma_f32_16x16x32_bf16 v[52:55], a[100:103], a[164:167], v[52:55]\n\t"
"ds_write_addtid_b32 v217 offset:31040\n\t"
"v_cvt_pk_bf16_f32 v216, v216, v220\n\t"
"v_mfma_f32_16x16x32_bf16 v[56:59], a[96:99], a[152:155], v[56:59]\n\t"
"ds_read_b128 a[164:167], v224 offset:2304\n\t"
"v_cvt_pk_bf16_f32 v211, -v211, -v215\n\t"
"v_mfma_f32_16x16x32_bf16 v[56:59], a[100:103], a[168:171], v[56:59]\n\t"
"buffer_load_dwordx4 a[56:59], off, s[96:99], s67 offset:2048 sc1\n\t"
"v_mfma_f32_16x16x32_bf16 v[72:75], a[104:107], a[152:155], v[72:75]\n\t"
"ds_write_addtid_b32 v211 offset:27936\n\t"
"v_cvt_pk_bf16_f32 v208, -v208, -v212\n\t"
"v_mfma_f32_16x16x32_bf16 v[72:75], a[108:111], a[168:171], v[72:75]\n\t"
"ds_write_addtid_b32 v216 offset:22720\n\t"
"v_cvt_pk_bf16_f32 v218, v218, v222\n\t"
"v_mfma_f32_16x16x32_bf16 v[88:91], a[112:115], a[152:155], v[88:91]\n\t"
"ds_read_b128 a[152:155], v224 offset:512\n\t"
"v_cvt_pk_bf16_f32 v209, -v209, -v213\n\t"
"v_mfma_f32_16x16x32_bf16 v[88:91], a[116:119], a[168:171], v[88:91]\n\t"
"buffer_load_dwordx4 a[48:51], off, s[96:99], s67 offset:0 sc1\n\t"
"v_mfma_f32_16x16x32_bf16 v[92:95], a[112:115], a[156:159], v[92:95]\n\t"
"ds_read_b128 a[168:171], v224 offset:2560\n\t"
"v_cvt_pk_bf16_f32 v201, v201, v205\n\t"
"v_mfma_f32_16x16x32_bf16 v[92:95], a[116:119], a[172:175], v[92:95]\n\t"
"ds_write_addtid_b32 v209 offset:26912\n\t"
"v_cvt_pk_bf16_f32 v210, -v210, -v214\n\t"
"v_mfma_f32_16x16x32_bf16 v[76:79], a[104:107], a[156:159], v[76:79]\n\t"
"s_nop 0\n\t"
"ds_write_addtid_b32 v193 offset:24864\n\t"
"ds_write_addtid_b32 v195 offset:25888\n\t"
"v_mfma_f32_16x16x32_bf16 v[76:79], a[108:111], a[172:175], v[76:79]\n\t"
"buffer_load_dwordx4 a[52:55], off, s[96:99], s67 offset:1024 sc1\n\t"
"v_mfma_f32_16x16x32_bf16 v[60:63], a[96:99], a[156:159], v[60:63]\n\t"
"s_nop 0\n\t"
"ds_write_addtid_b32 v203 offset:30016\n\t"
"ds_write_addtid_b32 v210 offset:19616\n\t"
"v_mfma_f32_16x16x32_bf16 v[60:63], a[100:103], a[172:175], v[60:63]\n\t"
"s_waitcnt vmcnt(18) lgkmcnt(9)\n\t"
"ds_write_addtid_b32 v208 offset:18592\n\t"
"v_cvt_pk_bf16_f32 v219, v219, v223\n\t"
"v_mfma_f32_16x16x32_bf16 v[0:3], a[120:123], a[144:147], v[0:3]\n\t"
"ds_write_addtid_b32 v219 offset:32064\n\t"
"v_cvt_pk_bf16_f32 v194, -v194, -v198\n\t"
"v_mfma_f32_16x16x32_bf16 v[0:3], a[124:127], a[160:163], v[0:3]\n\t"
"ds_write_addtid_b32 v194 offset:17568\n\t"
"v_mfma_f32_16x16x32_bf16 v[16:19], a[128:131], a[144:147], v[16:19]\n\t"
"s_nop 0\n\t"
"ds_write_addtid_b32 v201 offset:28992\n\t"
"ds_write_addtid_b32 v218 offset:23744\n\t"
"v_mfma_f32_16x16x32_bf16 v[16:19], a[132:135], a[160:163], v[16:19]\n\t"
"buffer_load_dwordx4 a[88:91], off, s[92:95], s67 offset:2048 sc1\n\t"
"v_mfma_f32_16x16x32_bf16 v[32:35], a[136:139], a[144:147], v[32:35]\n\t"
"ds_read_b128 a[156:159], v224 offset:768\n\t"
"v_mfma_f32_16x16x32_bf16 v[32:35], a[140:143], a[160:163], v[32:35]\n\t"
"buffer_load_dwordx4 a[92:95], off, s[92:95], s67 offset:3072 sc1\n\t"
"v_mfma_f32_16x16x32_bf16 v[36:39], a[136:139], a[148:151], v[36:39]\n\t"
"ds_read_b128 a[172:175], v224 offset:2816\n\t"
"v_mfma_f32_16x16x32_bf16 v[36:39], a[140:143], a[164:167], v[36:39]\n\t"
"buffer_load_dwordx4 a[84:87], off, s[92:95], s67 offset:1024 sc1\n\t"
"v_mfma_f32_16x16x32_bf16 v[20:23], a[128:131], a[148:151], v[20:23]\n\t"
"ds_read_b64_tr_b16 a[204:205], v251 offset:26496\n\t"
"v_mfma_f32_16x16x32_bf16 v[20:23], a[132:135], a[164:167], v[20:23]\n\t"
"v_mfma_f32_16x16x32_bf16 v[4:7], a[120:123], a[148:151], v[4:7]\n\t"
"buffer_load_dwordx4 a[80:83], off, s[92:95], s67 offset:0 sc1\n\t"
"v_mfma_f32_16x16x32_bf16 v[4:7], a[124:127], a[164:167], v[4:7]\n\t"
"s_waitcnt lgkmcnt(0)\n\t"
"s_barrier\n\t"
"v_mfma_f32_16x16x32_bf16 v[8:11], a[120:123], a[152:155], v[8:11]\n\t"
"s_nop 0\n\t"
"ds_read_b128 a[144:147], v224 offset:17568\n\t"
"ds_read_b128 a[160:163], v224 offset:19616\n\t"
"v_mfma_f32_16x16x32_bf16 v[8:11], a[124:127], a[168:171], v[8:11]\n\t"
"buffer_load_dwordx4 a[72:75], off, s[96:99], s66 offset:2048 sc1\n\t"
"v_mfma_f32_16x16x32_bf16 v[24:27], a[128:131], a[152:155], v[24:27]\n\t"
"ds_read_b128 a[148:151], v224 offset:17824\n\t"
"v_mfma_f32_16x16x32_bf16 v[24:27], a[132:135], a[168:171], v[24:27]\n\t"
"ds_read_b128 a[164:167], v224 offset:19872\n\t"
"v_mfma_f32_16x16x32_bf16 v[40:43], a[136:139], a[152:155], v[40:43]\n\t"
"ds_read_b128 a[228:231], v254 offset:39168\n\t"
"v_mfma_f32_16x16x32_bf16 v[40:43], a[140:143], a[168:171], v[40:43]\n\t"
"buffer_load_dwordx4 a[76:79], off, s[96:99], s66 offset:3072 sc1\n\t"
"v_mfma_f32_16x16x32_bf16 v[44:47], a[136:139], a[156:159], v[44:47]\n\t"
"ds_read_b128 a[152:155], v224 offset:18080\n\t"
"v_mfma_f32_16x16x32_bf16 v[44:47], a[140:143], a[172:175], v[44:47]\n\t"
"buffer_load_dwordx4 a[112:115], off, s[92:95], s53 offset:0 sc1\n\t"
"v_mfma_f32_16x16x32_bf16 v[28:31], a[128:131], a[156:159], v[28:31]\n\t"
"ds_read_b128 a[168:171], v224 offset:20128\n\t"
"v_mfma_f32_16x16x32_bf16 v[28:31], a[132:135], a[172:175], v[28:31]\n\t"
"v_mfma_f32_16x16x32_bf16 v[12:15], a[120:123], a[156:159], v[12:15]\n\t"
"ds_read_b128 a[156:159], v224 offset:18336\n\t"
"v_mfma_f32_16x16x32_bf16 v[12:15], a[124:127], a[172:175], v[12:15]\n\t"
"buffer_load_dwordx4 a[116:119], off, s[92:95], s53 offset:1024 sc1\n\t"
"v_mfma_f32_4x4x4_16b_bf16 v[200:203], a[220:221], a[178:179], 0\n\t"
"ds_read_b64_tr_b16 a[178:179], v250 offset:3456\n\t"
"v_mfma_f32_4x4x4_16b_bf16 v[204:207], a[220:221], a[186:187], 0\n\t"
"ds_read_b64_tr_b16 a[186:187], v251 offset:3456\n\t"
"v_mfma_f32_4x4x4_16b_bf16 v[216:219], a[220:221], a[194:195], 0\n\t"
"ds_read_b64_tr_b16 a[194:195], v248 offset:3456\n\t"
"v_mfma_f32_4x4x4_16b_bf16 v[220:223], a[220:221], a[202:203], 0\n\t"
"buffer_load_dwordx4 a[108:111], off, s[96:99], s52 offset:3072 sc1\n\t"
"v_mfma_f32_4x4x4_16b_bf16 v[192:195], a[222:223], a[176:177], v[200:203]\n\t"
"s_waitcnt lgkmcnt(5)\n\t"
"ds_write_b128 v255, a[228:231] offset:20736\n\t"
"v_mfma_f32_4x4x4_16b_bf16 v[196:199], a[222:223], a[184:185], v[204:207]\n\t"
"v_mfma_f32_4x4x4_16b_bf16 v[200:203], a[222:223], a[180:181], v[200:203]\n\t"
"v_mfma_f32_4x4x4_16b_bf16 v[204:207], a[222:223], a[188:189], v[204:207]\n\t"
"v_mfma_f32_4x4x4_16b_bf16 v[208:211], a[222:223], a[192:193], v[216:219]\n\t"
"ds_read_b128 a[172:175], v224 offset:20384\n\t"
"v_mfma_f32_4x4x4_16b_bf16 v[212:215], a[222:223], a[200:201], v[220:223]\n\t"
"ds_read_b64_tr_b16 a[202:203], v249 offset:3456\n\t"
"v_mfma_f32_4x4x4_16b_bf16 v[216:219], a[222:223], a[196:197], v[216:219]\n\t"
"ds_read_b64_tr_b16 a[176:177], v248 offset:1152\n\t"
"v_mfma_f32_4x4x4_16b_bf16 v[220:223], a[222:223], a[204:205], v[220:223]\n\t"
"s_waitcnt vmcnt(21) lgkmcnt(5)\n\t"
"buffer_load_dwordx4 a[104:107], off, s[96:99], s52 offset:2048 sc1\n\t"
"v_mfma_f32_16x16x32_bf16 v[48:51], a[0:3], a[144:147], v[48:51]\n\t"
"s_nop 0\n\t"
"ds_read_b64_tr_b16 a[184:185], v249 offset:1152\n\t"
"v_cvt_pk_bf16_f32 v192, -v192, -v196\n\t"
"v_mfma_f32_16x16x32_bf16 v[48:51], a[4:7], a[160:163], v[48:51]\n\t"
"s_nop 0\n\t"
"ds_write_addtid_b32 v192 offset:0\n\t"
"v_cvt_pk_bf16_f32 v193, -v193, -v197\n\t"
"v_mfma_f32_16x16x32_bf16 v[64:67], a[8:11], a[144:147], v[64:67]\n\t"
"s_nop 0\n\t"
"ds_write_addtid_b32 v193 offset:8320\n\t"
"v_cvt_pk_bf16_f32 v195, -v195, -v199\n\t"
"v_mfma_f32_16x16x32_bf16 v[64:67], a[12:15], a[160:163], v[64:67]\n\t"
"buffer_load_dwordx4 a[96:99], off, s[96:99], s52 offset:0 sc1\n\t"
"v_mfma_f32_16x16x32_bf16 v[80:83], a[16:19], a[144:147], v[80:83]\n\t"
"s_nop 0\n\t"
"ds_read_b128 a[144:147], v224 offset:16544\n\t"
"v_cvt_pk_bf16_f32 v201, v201, v205\n\t"
"v_mfma_f32_16x16x32_bf16 v[80:83], a[20:23], a[160:163], v[80:83]\n\t"
"s_nop 0\n\t"
"ds_write_addtid_b32 v201 offset:12448\n\t"
"v_cvt_pk_bf16_f32 v194, -v194, -v198\n\t"
"v_mfma_f32_16x16x32_bf16 v[84:87], a[16:19], a[148:151], v[84:87]\n\t"
"s_nop 0\n\t"
"ds_read_b128 a[160:163], v224 offset:18592\n\t"
"v_cvt_pk_bf16_f32 v209, -v209, -v213\n\t"
"v_mfma_f32_16x16x32_bf16 v[84:87], a[20:23], a[164:167], v[84:87]\n\t"
"buffer_load_dwordx4 a[100:103], off, s[96:99], s52 offset:1024 sc1\n\t"
"v_mfma_f32_16x16x32_bf16 v[68:71], a[8:11], a[148:151], v[68:71]\n\t"
"s_nop 0\n\t"
"ds_write_addtid_b32 v195 offset:9344\n\t"
"v_cvt_pk_bf16_f32 v211, -v211, -v215\n\t"
"v_mfma_f32_16x16x32_bf16 v[68:71], a[12:15], a[164:167], v[68:71]\n\t"
"s_nop 0\n\t"
"ds_write_addtid_b32 v194 offset:1024\n\t"
"v_cvt_pk_bf16_f32 v210, -v210, -v214\n\t"
"v_mfma_f32_16x16x32_bf16 v[52:55], a[0:3], a[148:151], v[52:55]\n\t"
"s_nop 0\n\t"
"ds_read_b128 a[148:151], v224 offset:16800\n\t"
"v_cvt_pk_bf16_f32 v202, v202, v206\n\t"
"v_mfma_f32_16x16x32_bf16 v[52:55], a[4:7], a[164:167], v[52:55]\n\t"
"buffer_load_dwordx4 a[136:139], off, s[92:95], s52 offset:2048 sc1\n\t"
"v_mfma_f32_16x16x32_bf16 v[56:59], a[0:3], a[152:155], v[56:59]\n\t"
"s_nop 0\n\t"
"ds_read_b128 a[164:167], v224 offset:18848\n\t"
"v_cvt_pk_bf16_f32 v200, v200, v204\n\t"
"v_mfma_f32_16x16x32_bf16 v[56:59], a[4:7], a[168:171], v[56:59]\n\t"
"s_nop 0\n\t"
"ds_write_addtid_b32 v211 offset:11392\n\t"
"v_cvt_pk_bf16_f32 v219, v219, v223\n\t"
"v_mfma_f32_16x16x32_bf16 v[72:75], a[8:11], a[152:155], v[72:75]\n\t"
"s_nop 0\n\t"
"ds_write_addtid_b32 v210 offset:3072\n\t"
"v_cvt_pk_bf16_f32 v217, v217, v221\n\t"
"v_mfma_f32_16x16x32_bf16 v[72:75], a[12:15], a[168:171], v[72:75]\n\t"
"s_nop 0\n\t"
"ds_write_addtid_b32 v200 offset:4128\n\t"
"buffer_load_dwordx4 a[140:143], off, s[92:95], s52 offset:3072 sc1\n\t"
"v_mfma_f32_16x16x32_bf16 v[88:91], a[16:19], a[152:155], v[88:91]\n\t"
"s_nop 0\n\t"
"ds_write_addtid_b32 v202 offset:5152\n\t"
"v_cvt_pk_bf16_f32 v203, v203, v207\n\t"
"v_mfma_f32_16x16x32_bf16 v[88:91], a[20:23], a[168:171], v[88:91]\n\t"
"s_waitcnt lgkmcnt(8)\n\t"
"ds_write_addtid_b32 v209 offset:10368\n\t"
"ds_write_addtid_b32 v217 offset:14496\n\t"
"v_mfma_f32_16x16x32_bf16 v[92:95], a[16:19], a[156:159], v[92:95]\n\t"
"s_nop 0\n\t"
"ds_write_addtid_b32 v203 offset:13472\n\t"
"v_cvt_pk_bf16_f32 v216, v216, v220\n\t"
"v_mfma_f32_16x16x32_bf16 v[92:95], a[20:23], a[172:175], v[92:95]\n\t"
"s_nop 0\n\t"
"ds_write_addtid_b32 v219 offset:15520\n\t"
"v_cvt_pk_bf16_f32 v218, v218, v222\n\t"
"v_mfma_f32_16x16x32_bf16 v[76:79], a[8:11], a[156:159], v[76:79]\n\t"
"s_nop 0\n\t"
"ds_write_addtid_b32 v218 offset:7200\n\t"
"v_cvt_pk_bf16_f32 v208, -v208, -v212\n\t"
"v_mfma_f32_16x16x32_bf16 v[76:79], a[12:15], a[172:175], v[76:79]\n\t"
"s_nop 0\n\t"
"ds_write_addtid_b32 v208 offset:2048\n\t"
"ds_write_addtid_b32 v216 offset:6176\n\t"
"v_mfma_f32_16x16x32_bf16 v[60:63], a[0:3], a[156:159], v[60:63]\n\t"
"s_add_u32 m0, s26, 124544\n\t"
"ds_read_b128 a[152:155], v224 offset:17056\n\t"
"v_mfma_f32_16x16x32_bf16 v[60:63], a[4:7], a[172:175], v[60:63]\n\t"
"s_waitcnt vmcnt(20) lgkmcnt(11)\n\t"
"buffer_load_dwordx4 v236, s[12:15], s22 offen offset:128 nt lds\n\t"
"v_mfma_f32_16x16x32_bf16 v[0:3], a[24:27], a[144:147], v[0:3]\n\t"
"s_nop 0\n\t"
"ds_read_b128 a[156:159], v224 offset:17312\n\t"
"ds_read_b128 a[172:175], v224 offset:19360\n\t"
"v_mfma_f32_16x16x32_bf16 v[0:3], a[28:31], a[160:163], v[0:3]\n\t"
"buffer_load_dwordx4 v237, s[12:15], s22 offen offset:1152 nt lds\n\t"
"v_mfma_f32_16x16x32_bf16 v[16:19], a[32:35], a[144:147], v[16:19]\n\t"
"s_nop 0\n\t"
"ds_read_b128 a[168:171], v224 offset:19104\n\t"
"v_mfma_f32_16x16x32_bf16 v[16:19], a[36:39], a[160:163], v[16:19]\n\t"
"buffer_load_dwordx4 v238, s[12:15], s22 offen offset:2176 nt lds\n\t"
"v_mfma_f32_16x16x32_bf16 v[32:35], a[40:43], a[144:147], v[32:35]\n\t"
"s_nop 0\n\t"
"ds_read_b64_tr_b16 a[180:181], v248 offset:5760\n\t"
"ds_read_b64_tr_b16 a[188:189], v249 offset:5760\n\t"
"v_mfma_f32_16x16x32_bf16 v[32:35], a[44:47], a[160:163], v[32:35]\n\t"
"buffer_load_dwordx4 v239, s[12:15], s22 offen offset:3200 nt lds\n\t"
"v_mfma_f32_16x16x32_bf16 v[36:39], a[40:43], a[148:151], v[36:39]\n\t"
"s_nop 0\n\t"
"ds_read_b64_tr_b16 a[192:193], v250 offset:1152\n\t"
"ds_read_b64_tr_b16 a[200:201], v251 offset:1152\n\t"
"v_mfma_f32_16x16x32_bf16 v[36:39], a[44:47], a[164:167], v[36:39]\n\t"
"buffer_load_dwordx4 a[132:135], off, s[92:95], s52 offset:1024 sc1\n\t"
"v_mfma_f32_16x16x32_bf16 v[20:23], a[32:35], a[148:151], v[20:23]\n\t"
"s_nop 0\n\t"
"ds_read_b64_tr_b16 a[196:197], v250 offset:5760\n\t"
"ds_read_b64_tr_b16 a[204:205], v251 offset:5760\n\t"
"v_mfma_f32_16x16x32_bf16 v[20:23], a[36:39], a[164:167], v[20:23]\n\t"
"s_mov_b64 exec, s[36:37]\n\t"
"buffer_load_dwordx4 v240, s[12:15], s22 offen offset:3712 nt lds\n\t"
"s_mov_b64 exec, -1\n\t"
"v_mfma_f32_16x16x32_bf16 v[4:7], a[24:27], a[148:151], v[4:7]\n\t"
"Lpad5_%=:\n\t"
"s_cbranch_execz Lret0_%=\n\t"
"s_lshl_b32 m0, s73, 8\n\t"
"v_mfma_f32_16x16x32_bf16 v[4:7], a[28:31], a[164:167], v[4:7]\n\t"
"s_waitcnt lgkmcnt(0)\n\t"
"s_barrier\n\t"
"v_mfma_f32_16x16x32_bf16 v[8:11], a[24:27], a[152:155], v[8:11]\n\t"
"ds_read_b128 a[144:147], v224 offset:1024\n\t"
"v_add_u32 v248, 33280, v227\n\t"
"v_mfma_f32_16x16x32_bf16 v[8:11], a[28:31], a[168:171], v[8:11]\n\t"
"ds_read_b128 a[160:163], v224 offset:3072\n\t"
"v_xor_b32 v249, 32, v248\n\t"
"v_mfma_f32_16x16x32_bf16 v[24:27], a[32:35], a[152:155], v[24:27]\n\t"
"ds_read_b128 a[148:151], v224 offset:1280\n\t"
"v_xor_b32 v250, 64, v248\n\t"
"v_mfma_f32_16x16x32_bf16 v[24:27], a[36:39], a[168:171], v[24:27]\n\t"
"ds_read_b128 a[164:167], v224 offset:3328\n\t"
"v_xor_b32 v251, 96, v248\n\t"
"v_mfma_f32_16x16x32_bf16 v[40:43], a[40:43], a[152:155], v[40:43]\n\t"
"ds_read_b64_tr_b16 a[182:183], v249 offset:41984\n\t"
"v_mfma_f32_16x16x32_bf16 v[40:43], a[44:47], a[168:171], v[40:43]\n\t"
"ds_read_b128 a[152:155], v224 offset:1536\n\t"
"v_mfma_f32_16x16x32_bf16 v[44:47], a[40:43], a[156:159], v[44:47]\n\t"
"ds_read_b128 a[168:171], v224 offset:3584\n\t"
"v_mfma_f32_16x16x32_bf16 v[44:47], a[44:47], a[172:175], v[44:47]\n\t"
"ds_read_b64_tr_b16 a[190:191], v251 offset:41984\n\t"
"v_mfma_f32_16x16x32_bf16 v[28:31], a[32:35], a[156:159], v[28:31]\n\t"
"ds_read_b64_tr_b16 a[198:199], v249 offset:21248\n\t"
"v_mfma_f32_16x16x32_bf16 v[28:31], a[36:39], a[172:175], v[28:31]\n\t"
"ds_read_b64_tr_b16 a[206:207], v251 offset:21248\n\t"
"v_mfma_f32_16x16x32_bf16 v[12:15], a[24:27], a[156:159], v[12:15]\n\t"
"ds_read_b128 a[156:159], v224 offset:1792\n\t"
"v_mfma_f32_16x16x32_bf16 v[12:15], a[28:31], a[172:175], v[12:15]\n\t"
"ds_read_b128 a[172:175], v224 offset:3840\n\t"
"v_mfma_f32_4x4x4_16b_bf16 v[200:203], a[220:221], a[178:179], 0\n\t"
"ds_read_b64_tr_b16 a[178:179], v248 offset:41984\n\t"
"v_mfma_f32_4x4x4_16b_bf16 v[204:207], a[220:221], a[186:187], 0\n\t"
"ds_read_b64_tr_b16 a[186:187], v250 offset:41984\n\t"
"v_mfma_f32_4x4x4_16b_bf16 v[216:219], a[220:221], a[194:195], 0\n\t"
"ds_read_b64_tr_b16 a[194:195], v248 offset:21248\n\t"
"v_mfma_f32_4x4x4_16b_bf16 v[220:223], a[220:221], a[202:203], 0\n\t"
"ds_read_b64_tr_b16 a[202:203], v250 offset:21248\n\t"
"v_mfma_f32_4x4x4_16b_bf16 v[192:195], a[222:223], a[176:177], v[200:203]\n\t"
"ds_read_b64_tr_b16 a[176:177], v248 offset:41472\n\t"
"v_mfma_f32_4x4x4_16b_bf16 v[196:199], a[222:223], a[184:185], v[204:207]\n\t"
".p2align 3\n\t"
"ds_read_b64_tr_b16 a[184:185], v250 offset:41472\n\t"
"v_pk_add_f32 v[0:1], v[0:1], v[48:49]\n\t"
"v_pk_add_f32 v[2:3], v[2:3], v[50:51]\n\t"
"v_pk_fma_f32 v[48:49], v[48:49], s[38:39], v[0:1]\n\t"
"v_pk_fma_f32 v[50:51], v[50:51], s[38:39], v[2:3]\n\t"
"v_pk_add_f32 v[16:17], v[16:17], v[64:65]\n\t"
"v_pk_add_f32 v[18:19], v[18:19], v[66:67]\n\t"
"v_pk_fma_f32 v[64:65], v[64:65], s[38:39], v[16:17]\n\t"
"v_pk_fma_f32 v[66:67], v[66:67], s[38:39], v[18:19]\n\t"
"v_pk_add_f32 v[32:33], v[32:33], v[80:81]\n\t"
"v_pk_add_f32 v[34:35], v[34:35], v[82:83]\n\t"
"v_pk_fma_f32 v[80:81], v[80:81], s[38:39], v[32:33]\n\t"
"v_pk_fma_f32 v[82:83], v[82:83], s[38:39], v[34:35]\n\t"
"v_pk_add_f32 v[36:37], v[36:37], v[84:85]\n\t"
"v_pk_add_f32 v[38:39], v[38:39], v[86:87]\n\t"
"v_pk_fma_f32 v[84:85], v[84:85], s[38:39], v[36:37]\n\t"
"v_pk_fma_f32 v[86:87], v[86:87], s[38:39], v[38:39]\n\t"
"v_pk_add_f32 v[20:21], v[20:21], v[68:69]\n\t"
"v_pk_add_f32 v[22:23], v[22:23], v[70:71]\n\t"
"v_pk_fma_f32 v[68:69], v[68:69], s[38:39], v[20:21]\n\t"
"v_pk_fma_f32 v[70:71], v[70:71], s[38:39], v[22:23]\n\t"
"v_pk_add_f32 v[4:5], v[4:5], v[52:53]\n\t"
"v_pk_add_f32 v[6:7], v[6:7], v[54:55]\n\t"
"v_pk_fma_f32 v[52:53], v[52:53], s[38:39], v[4:5]\n\t"
"v_pk_fma_f32 v[54:55], v[54:55], s[38:39], v[6:7]\n\t"
"v_pk_add_f32 v[8:9], v[8:9], v[56:57]\n\t"
"v_pk_add_f32 v[10:11], v[10:11], v[58:59]\n\t"
"v_pk_fma_f32 v[56:57], v[56:57], s[38:39], v[8:9]\n\t"
"v_pk_fma_f32 v[58:59], v[58:59], s[38:39], v[10:11]\n\t"
"v_pk_add_f32 v[24:25], v[24:25], v[72:73]\n\t"
"v_pk_add_f32 v[26:27], v[26:27], v[74:75]\n\t"
"v_pk_fma_f32 v[72:73], v[72:73], s[38:39], v[24:25]\n\t"
"v_pk_fma_f32 v[74:75], v[74:75], s[38:39], v[26:27]\n\t"
"v_pk_add_f32 v[40:41], v[40:41], v[88:89]\n\t"
"v_pk_add_f32 v[42:43], v[42:43], v[90:91]\n\t"
"v_pk_fma_f32 v[88:89], v[88:89], s[38:39], v[40:41]\n\t"
"v_pk_fma_f32 v[90:91], v[90:91], s[38:39], v[42:43]\n\t"
"v_pk_add_f32 v[44:45], v[44:45], v[92:93]\n\t"
"v_pk_add_f32 v[46:47], v[46:47], v[94:95]\n\t"
"v_pk_fma_f32 v[92:93], v[92:93], s[38:39], v[44:45]\n\t"
"v_pk_fma_f32 v[94:95], v[94:95], s[38:39], v[46:47]\n\t"
"v_pk_add_f32 v[28:29], v[28:29], v[76:77]\n\t"
"v_pk_add_f32 v[30:31], v[30:31], v[78:79]\n\t"
"v_pk_fma_f32 v[76:77], v[76:77], s[38:39], v[28:29]\n\t"
"v_pk_fma_f32 v[78:79], v[78:79], s[38:39], v[30:31]\n\t"
"v_pk_add_f32 v[12:13], v[12:13], v[60:61]\n\t"
"v_pk_add_f32 v[14:15], v[14:15], v[62:63]\n\t"
"v_pk_fma_f32 v[60:61], v[60:61], s[38:39], v[12:13]\n\t"
"v_pk_fma_f32 v[62:63], v[62:63], s[38:39], v[14:15]\n\t"
"v_cvt_pk_bf16_f32 v4, v4, v5\n\t"
"v_mfma_f32_4x4x4_16b_bf16 v[200:203], a[222:223], a[180:181], v[200:203]\n\t"
"ds_read_b64_tr_b16 a[180:181], v249 offset:41472\n\t"
"v_mfma_f32_4x4x4_16b_bf16 v[204:207], a[222:223], a[188:189], v[204:207]\n\t"
"ds_read_b64_tr_b16 a[188:189], v251 offset:41472\n\t"
"v_mfma_f32_4x4x4_16b_bf16 v[208:211], a[222:223], a[192:193], v[216:219]\n\t"
"v_cvt_pk_bf16_f32 v0, v0, v1\n\t"
"v_cvt_pk_bf16_f32 v1, v2, v3\n\t"
"v_cvt_pk_bf16_f32 v5, v6, v7\n\t"
"v_cvt_pk_bf16_f32 v24, v24, v25\n\t"
"v_mfma_f32_4x4x4_16b_bf16 v[212:215], a[222:223], a[200:201], v[220:223]\n\t"
"ds_write2st64_b64 v228, v[0:1], v[4:5] offset0:40 offset1:53\n\t"
"v_mfma_f32_4x4x4_16b_bf16 v[216:219], a[222:223], a[196:197], v[216:219]\n\t"
"v_cvt_pk_bf16_f32 v16, v16, v17\n\t"
"v_mfma_f32_4x4x4_16b_bf16 v[220:223], a[222:223], a[204:205], v[220:223]\n\t"
"s_waitcnt vmcnt(20) lgkmcnt(7)\n\t"
"v_cvt_pk_bf16_f32 v17, v18, v19\n\t"
"v_cvt_pk_bf16_f32 v20, v20, v21\n\t"
"v_mfma_f32_16x16x32_bf16 v[144:147], a[48:51], a[144:147], v[144:147]\n\t"
"v_cvt_pk_bf16_f32 v21, v22, v23\n\t"
"v_cvt_pk_bf16_f32 v28, v28, v29\n\t"
"v_mfma_f32_16x16x32_bf16 v[144:147], a[52:55], a[160:163], v[144:147]\n\t"
"ds_write2st64_b64 v246, v[16:17], v[20:21] offset0:40 offset1:53\n\t"
"v_mfma_f32_16x16x32_bf16 v[160:163], a[56:59], a[144:147], v[160:163]\n\t"
"v_cvt_pk_bf16_f32 v32, v32, v33\n\t"
"v_cvt_pk_bf16_f32 v29, v30, v31\n\t"
"v_mfma_f32_16x16x32_bf16 v[160:163], a[60:63], a[160:163], v[160:163]\n\t"
"v_cvt_pk_bf16_f32 v33, v34, v35\n\t"
"v_cvt_pk_bf16_f32 v25, v26, v27\n\t"
"v_mfma_f32_16x16x32_bf16 v[176:179], a[64:67], a[144:147], v[176:179]\n\t"
"v_cvt_pk_bf16_f32 v195, -v195, -v199\n\t"
"v_cvt_pk_bf16_f32 v12, v12, v13\n\t"
"v_mfma_f32_16x16x32_bf16 v[176:179], a[68:71], a[160:163], v[176:179]\n\t"
"s_nop 0\n\t"
"ds_read_b128 a[144:147], v224 offset:0\n\t"
"ds_read_b128 a[160:163], v224 offset:2048\n\t"
"ds_write2st64_b64 v246, v[24:25], v[28:29] offset0:66 offset1:79\n\t"
"v_cvt_pk_bf16_f32 v40, v40, v41\n\t"
"v_mfma_f32_16x16x32_bf16 v[180:183], a[64:67], a[148:151], v[180:183]\n\t"
"v_cvt_pk_bf16_f32 v208, -v208, -v212\n\t"
"v_cvt_pk_bf16_f32 v36, v36, v37\n\t"
"v_mfma_f32_16x16x32_bf16 v[180:183], a[68:71], a[164:167], v[180:183]\n\t"
"v_cvt_pk_bf16_f32 v216, v216, v220\n\t"
"v_cvt_pk_bf16_f32 v37, v38, v39\n\t"
"v_mfma_f32_16x16x32_bf16 v[164:167], a[56:59], a[148:151], v[164:167]\n\t"
"v_cvt_pk_bf16_f32 v8, v8, v9\n\t"
"v_cvt_pk_bf16_f32 v44, v44, v45\n\t"
"v_mfma_f32_16x16x32_bf16 v[164:167], a[60:63], a[164:167], v[164:167]\n\t"
"ds_write_addtid_b32 v195 offset:25888\n\t"
"ds_write2st64_b64 v247, v[32:33], v[36:37] offset0:40 offset1:53\n\t"
"v_mfma_f32_16x16x32_bf16 v[148:151], a[48:51], a[148:151], v[148:151]\n\t"
"v_cvt_pk_bf16_f32 v9, v10, v11\n\t"
"v_cvt_pk_bf16_f32 v45, v46, v47\n\t"
"v_mfma_f32_16x16x32_bf16 v[148:151], a[52:55], a[164:167], v[148:151]\n\t"
"buffer_load_dwordx4 a[128:131], off, s[92:95], s52 offset:0 sc1\n\t"
"v_mfma_f32_16x16x32_bf16 v[152:155], a[48:51], a[152:155], v[152:155]\n\t"
"v_cvt_pk_bf16_f32 v202, v202, v206\n\t"
"v_cvt_pk_bf16_f32 v13, v14, v15\n\t"
"v_mfma_f32_16x16x32_bf16 v[152:155], a[52:55], a[168:171], v[152:155]\n\t"
"s_nop 0\n\t"
"ds_read_b128 a[148:151], v224 offset:256\n\t"
"ds_read_b128 a[164:167], v224 offset:2304\n\t"
"ds_write2st64_b64 v228, v[8:9], v[12:13] offset0:66 offset1:79\n\t"
"v_mfma_f32_16x16x32_bf16 v[168:171], a[56:59], a[152:155], v[168:171]\n\t"
"v_cvt_pk_bf16_f32 v217, v217, v221\n\t"
"v_cvt_pk_bf16_f32 v41, v42, v43\n\t"
"v_mfma_f32_16x16x32_bf16 v[168:171], a[60:63], a[168:171], v[168:171]\n\t"
"v_cvt_pk_bf16_f32 v193, -v193, -v197\n\t"
"v_cvt_pk_bf16_f32 v200, v200, v204\n\t"
"v_mfma_f32_16x16x32_bf16 v[184:187], a[64:67], a[152:155], v[184:187]\n\t"
"v_cvt_pk_bf16_f32 v210, -v210, -v214\n\t"
"v_cvt_pk_bf16_f32 v219, v219, v223\n\t"
"v_mfma_f32_16x16x32_bf16 v[184:187], a[68:71], a[168:171], v[184:187]\n\t"
"ds_write_addtid_b32 v202 offset:21696\n\t"
"ds_write2st64_b64 v247, v[40:41], v[44:45] offset0:66 offset1:79\n\t"
"v_mfma_f32_16x16x32_bf16 v[188:191], a[64:67], a[156:159], v[188:191]\n\t"
"v_cvt_pk_bf16_f32 v201, v201, v205\n\t"
"v_cvt_pk_bf16_f32 v218, v218, v222\n\t"
"v_mfma_f32_16x16x32_bf16 v[188:191], a[68:71], a[172:175], v[188:191]\n\t"
"v_cvt_pk_bf16_f32 v194, -v194, -v198\n\t"
"v_cvt_pk_bf16_f32 v203, v203, v207\n\t"
"v_mfma_f32_16x16x32_bf16 v[172:175], a[56:59], a[156:159], v[172:175]\n\t"
"v_cvt_pk_bf16_f32 v209, -v209, -v213\n\t"
"v_cvt_pk_bf16_f32 v211, -v211, -v215\n\t"
"v_mfma_f32_16x16x32_bf16 v[172:175], a[60:63], a[172:175], v[172:175]\n\t"
"s_nop 0\n\t"
"ds_write_addtid_b32 v193 offset:24864\n\t"
"ds_write_addtid_b32 v201 offset:28992\n\t"
"ds_write_addtid_b32 v203 offset:30016\n\t"
"ds_write_addtid_b32 v209 offset:26912\n\t"
"ds_write_addtid_b32 v210 offset:19616\n\t"
"v_mfma_f32_16x16x32_bf16 v[156:159], a[48:51], a[156:159], v[156:159]\n\t"
"s_nop 0\n\t"
"ds_write_addtid_b32 v208 offset:18592\n\t"
"ds_write_addtid_b32 v216 offset:22720\n\t"
"v_mfma_f32_16x16x32_bf16 v[156:159], a[52:55], a[172:175], v[156:159]\n\t"
"s_waitcnt vmcnt(15) lgkmcnt(9)\n\t"
"ds_write_addtid_b32 v218 offset:23744\n\t"
"v_cvt_pk_bf16_f32 v192, -v192, -v196\n\t"
"v_mfma_f32_16x16x32_bf16 v[96:99], a[72:75], a[144:147], v[96:99]\n\t"
"s_nop 0\n\t"
"ds_write_addtid_b32 v217 offset:31040\n\t"
"ds_write_addtid_b32 v219 offset:32064\n\t"
"v_mfma_f32_16x16x32_bf16 v[96:99], a[76:79], a[160:163], v[96:99]\n\t"
"s_nop 0\n\t"
"ds_write_addtid_b32 v192 offset:16544\n\t"
"ds_write_addtid_b32 v200 offset:20672\n\t"
"v_mfma_f32_16x16x32_bf16 v[112:115], a[80:83], a[144:147], v[112:115]\n\t"
"s_nop 0\n\t"
"ds_write_addtid_b32 v194 offset:17568\n\t"
"ds_write_addtid_b32 v211 offset:27936\n\t"
"v_mfma_f32_16x16x32_bf16 v[112:115], a[84:87], a[160:163], v[112:115]\n\t"
"buffer_load_dwordx4 a[120:123], off, s[96:99], s51 offset:2048 sc1\n\t"
"v_mfma_f32_16x16x32_bf16 v[128:131], a[88:91], a[144:147], v[128:131]\n\t"
"s_nop 0\n\t"
"ds_read_b128 a[168:171], v224 offset:2560\n\t"
"ds_read_b128 a[156:159], v224 offset:768\n\t"
"v_mfma_f32_16x16x32_bf16 v[128:131], a[92:95], a[160:163], v[128:131]\n\t"
"buffer_load_dwordx4 a[124:127], off, s[96:99], s51 offset:3072 sc1\n\t"
"v_mfma_f32_16x16x32_bf16 v[132:135], a[88:91], a[148:151], v[132:135]\n\t"
"s_nop 0\n\t"
"ds_read_b128 a[152:155], v224 offset:512\n\t"
"ds_read_b128 a[172:175], v224 offset:2816\n\t"
"v_mfma_f32_16x16x32_bf16 v[132:135], a[92:95], a[164:167], v[132:135]\n\t"
"s_nop 0\n\t"
"ds_read_b64_tr_b16 a[192:193], v248 offset:20736\n\t"
"ds_read_b64_tr_b16 a[200:201], v250 offset:20736\n\t"
"v_mfma_f32_16x16x32_bf16 v[116:119], a[80:83], a[148:151], v[116:119]\n\t"
"s_nop 0\n\t"
"ds_read_b64_tr_b16 a[196:197], v249 offset:20736\n\t"
"ds_read_b64_tr_b16 a[204:205], v251 offset:20736\n\t"
"v_mfma_f32_16x16x32_bf16 v[116:119], a[84:87], a[164:167], v[116:119]\n\t"
"buffer_load_dwordx4 a[16:19], off, s[28:31], s69 offset:0 sc1\n\t"
"v_mfma_f32_16x16x32_bf16 v[100:103], a[72:75], a[148:151], v[100:103]\n\t"
"s_waitcnt lgkmcnt(4)\n\t"
"v_mfma_f32_16x16x32_bf16 v[100:103], a[76:79], a[164:167], v[100:103]\n\t"
"s_barrier\n\t"
"s_cmp_eq_u32 s21, 1\n\t"
"s_cbranch_scc1 Ltail_%=\n\t"
"v_mfma_f32_16x16x32_bf16 v[104:107], a[72:75], a[152:155], v[104:107]\n\t"
"s_nop 0\n\t"
"ds_read_b128 a[144:147], v224 offset:17568\n\t"
"ds_read_b128 a[160:163], v224 offset:19616\n\t"
"v_mfma_f32_16x16x32_bf16 v[104:107], a[76:79], a[168:171], v[104:107]\n\t"
"buffer_load_dwordx4 a[20:23], off, s[28:31], s69 offset:1024 sc1\n\t"
"v_mfma_f32_16x16x32_bf16 v[120:123], a[80:83], a[152:155], v[120:123]\n\t"
"ds_read_b128 a[148:151], v224 offset:17824\n\t"
"v_mfma_f32_16x16x32_bf16 v[120:123], a[84:87], a[168:171], v[120:123]\n\t"
"ds_read_b128 a[164:167], v224 offset:19872\n\t"
"v_mfma_f32_16x16x32_bf16 v[136:139], a[88:91], a[152:155], v[136:139]\n\t"
"ds_read_b128 a[152:155], v224 offset:18080\n\t"
"v_mfma_f32_16x16x32_bf16 v[136:139], a[92:95], a[168:171], v[136:139]\n\t"
"buffer_load_dwordx4 a[12:15], off, s[84:87], s69 offset:3072 sc1\n\t"
"v_mfma_f32_16x16x32_bf16 v[140:143], a[88:91], a[156:159], v[140:143]\n\t"
"ds_read_b128 a[168:171], v224 offset:20128\n\t"
"v_mfma_f32_16x16x32_bf16 v[140:143], a[92:95], a[172:175], v[140:143]\n\t"
"buffer_load_dwordx4 a[8:11], off, s[84:87], s69 offset:2048 sc1\n\t"
"v_mfma_f32_16x16x32_bf16 v[124:127], a[80:83], a[156:159], v[124:127]\n\t"
"ds_read_b128 v[8:11], v229 offset:20480\n\t"
"v_mfma_f32_16x16x32_bf16 v[124:127], a[84:87], a[172:175], v[124:127]\n\t"
"buffer_load_dwordx4 a[0:3], off, s[84:87], s69 offset:0 sc1\n\t"
"v_mfma_f32_16x16x32_bf16 v[108:111], a[72:75], a[156:159], v[108:111]\n\t"
"ds_read_b128 a[156:159], v224 offset:18336\n\t"
"v_mfma_f32_16x16x32_bf16 v[108:111], a[76:79], a[172:175], v[108:111]\n\t"
"ds_read_b128 a[172:175], v224 offset:20384\n\t"
"v_mfma_f32_16x16x32_bf16 v[192:195], a[224:227], a[176:179], 0\n\t"
"s_nop 0\n\t"
"ds_read_b64_tr_b16 a[176:177], v248 offset:0\n\t"
"ds_read_b64_tr_b16 a[178:179], v248 offset:512\n\t"
"v_mfma_f32_16x16x32_bf16 v[196:199], a[224:227], a[180:183], 0\n\t"
"s_nop 0\n\t"
"ds_read_b64_tr_b16 a[180:181], v249 offset:0\n\t"
"ds_read_b64_tr_b16 a[182:183], v249 offset:512\n\t"
"v_mfma_f32_16x16x32_bf16 v[200:203], a[224:227], a[184:187], 0\n\t"
"buffer_load_dwordx4 a[4:7], off, s[84:87], s69 offset:1024 sc1\n\t"
"v_mfma_f32_16x16x32_bf16 v[204:207], a[224:227], a[188:191], 0\n\t"
"s_waitcnt lgkmcnt(8)\n\t"
"ds_read_b64_tr_b16 a[184:185], v250 offset:0\n\t"
"v_mfma_f32_16x16x32_bf16 v[208:211], a[224:227], a[192:195], 0\n\t"
"s_nop 0\n\t"
"ds_read_b64_tr_b16 a[186:187], v250 offset:512\n\t"
"ds_read_b64_tr_b16 a[188:189], v251 offset:0\n\t"
"v_mfma_f32_16x16x32_bf16 v[212:215], a[224:227], a[196:199], 0\n\t"
"s_nop 0\n\t"
"ds_read_b64_tr_b16 a[190:191], v251 offset:512\n\t"
"ds_read_b64_tr_b16 a[192:193], v248 offset:1152\n\t"
"v_mfma_f32_16x16x32_bf16 v[216:219], a[224:227], a[200:203], 0\n\t"
"s_waitcnt lgkmcnt(11)\n\t"
"buffer_store_dwordx4 v[8:11], v232, s[16:19], s24 offen nt\n\t"
"v_mfma_f32_16x16x32_bf16 v[220:223], a[224:227], a[204:207], 0\n\t"
"s_waitcnt vmcnt(18)\n\t"
"ds_read_b64_tr_b16 a[194:195], v248 offset:1664\n\t"
"v_cvt_pk_bf16_f32 v195, v195, v199\n\t"
"v_mfma_f32_16x16x32_bf16 v[144:147], a[96:99], a[144:147], v[144:147]\n\t"
"ds_read_b64_tr_b16 a[196:197], v249 offset:1152\n\t"
"v_cvt_pk_bf16_f32 v194, v194, v198\n\t"
"v_mfma_f32_16x16x32_bf16 v[144:147], a[100:103], a[160:163], v[144:147]\n\t"
"buffer_load_dwordx4 a[40:43], off, s[28:31], s54 offset:0 sc1\n\t"
"v_mfma_f32_16x16x32_bf16 v[160:163], a[104:107], a[144:147], v[160:163]\n\t"
"s_nop 0\n\t"
"ds_write_addtid_b32 v195 offset:12448\n\t"
"ds_read_b64_tr_b16 a[198:199], v249 offset:1664\n\t"
"v_mfma_f32_16x16x32_bf16 v[160:163], a[108:111], a[160:163], v[160:163]\n\t"
"ds_write_addtid_b32 v194 offset:4128\n\t"
"v_cvt_pk_bf16_f32 v201, v201, v205\n\t"
"v_mfma_f32_16x16x32_bf16 v[176:179], a[112:115], a[144:147], v[176:179]\n\t"
"ds_read_b128 a[144:147], v224 offset:16544\n\t"
"v_cvt_pk_bf16_f32 v211, v211, v215\n\t"
"v_mfma_f32_16x16x32_bf16 v[176:179], a[116:119], a[160:163], v[176:179]\n\t"
"buffer_load_dwordx4 a[44:47], off, s[28:31], s54 offset:1024 sc1\n\t"
"v_mfma_f32_16x16x32_bf16 v[180:183], a[112:115], a[148:151], v[180:183]\n\t"
"ds_read_b128 a[160:163], v224 offset:18592\n\t"
"v_cvt_pk_bf16_f32 v208, v208, v212\n\t"
"v_mfma_f32_16x16x32_bf16 v[180:183], a[116:119], a[164:167], v[180:183]\n\t"
"ds_read_b128 v[24:27], v230 offset:20480\n\t"
"v_cvt_pk_bf16_f32 v202, v202, v206\n\t"
"v_mfma_f32_16x16x32_bf16 v[164:167], a[104:107], a[148:151], v[164:167]\n\t"
"ds_read_b128 v[12:15], v229 offset:33792\n\t"
"v_cvt_pk_bf16_f32 v200, v200, v204\n\t"
"v_mfma_f32_16x16x32_bf16 v[164:167], a[108:111], a[164:167], v[164:167]\n\t"
"buffer_load_dwordx4 a[36:39], off, s[84:87], s54 offset:3072 sc1\n\t"
"v_mfma_f32_16x16x32_bf16 v[148:151], a[96:99], a[148:151], v[148:151]\n\t"
"ds_read_b128 v[28:31], v230 offset:33792\n\t"
"v_cvt_pk_bf16_f32 v216, v216, v220\n\t"
"v_mfma_f32_16x16x32_bf16 v[148:151], a[100:103], a[164:167], v[148:151]\n\t"
"ds_read_b128 a[164:167], v224 offset:18848\n\t"
"v_cvt_pk_bf16_f32 v209, v209, v213\n\t"
"v_mfma_f32_16x16x32_bf16 v[152:155], a[96:99], a[152:155], v[152:155]\n\t"
"ds_read_b128 a[148:151], v224 offset:16800\n\t"
"v_cvt_pk_bf16_f32 v218, v218, v222\n\t"
"v_mfma_f32_16x16x32_bf16 v[152:155], a[100:103], a[168:171], v[152:155]\n\t"
"buffer_load_dwordx4 a[32:35], off, s[84:87], s54 offset:2048 sc1\n\t"
"v_mfma_f32_16x16x32_bf16 v[168:171], a[104:107], a[152:155], v[168:171]\n\t"
"ds_write_addtid_b32 v202 offset:6176\n\t"
"v_cvt_pk_bf16_f32 v203, v203, v207\n\t"
"v_mfma_f32_16x16x32_bf16 v[168:171], a[108:111], a[168:171], v[168:171]\n\t"
"s_nop 0\n\t"
"ds_write_addtid_b32 v200 offset:2048\n\t"
"ds_write_addtid_b32 v203 offset:14496\n\t"
"ds_write_addtid_b32 v211 offset:13472\n\t"
"v_mfma_f32_16x16x32_bf16 v[184:187], a[112:115], a[152:155], v[184:187]\n\t"
"ds_read_b128 a[152:155], v224 offset:17056\n\t"
"v_cvt_pk_bf16_f32 v210, v210, v214\n\t"
"v_mfma_f32_16x16x32_bf16 v[184:187], a[116:119], a[168:171], v[184:187]\n\t"
"s_waitcnt lgkmcnt(9)\n\t"
"buffer_store_dwordx4 v[24:27], v233, s[16:19], s24 offen nt\n\t"
"v_mfma_f32_16x16x32_bf16 v[188:191], a[112:115], a[156:159], v[188:191]\n\t"
"ds_read_b128 a[168:171], v224 offset:19104\n\t"
"v_cvt_pk_bf16_f32 v217, v217, v221\n\t"
"v_mfma_f32_16x16x32_bf16 v[188:191], a[116:119], a[172:175], v[188:191]\n\t"
"ds_read_b128 v[44:47], v231 offset:33792\n\t"
"v_cvt_pk_bf16_f32 v192, v192, v196\n\t"
"v_mfma_f32_16x16x32_bf16 v[172:175], a[104:107], a[156:159], v[172:175]\n\t"
"s_nop 0\n\t"
"ds_write_addtid_b32 v209 offset:9344\n\t"
"ds_write_addtid_b32 v210 offset:5152\n\t"
"v_mfma_f32_16x16x32_bf16 v[172:175], a[108:111], a[172:175], v[172:175]\n\t"
"ds_write_addtid_b32 v216 offset:3072\n\t"
"buffer_load_dwordx4 a[24:27], off, s[84:87], s54 offset:0 sc1\n\t"
"v_mfma_f32_16x16x32_bf16 v[156:159], a[96:99], a[156:159], v[156:159]\n\t"
"ds_read_b128 v[40:43], v231 offset:20480\n\t"
"v_cvt_pk_bf16_f32 v219, v219, v223\n\t"
"v_mfma_f32_16x16x32_bf16 v[156:159], a[100:103], a[172:175], v[156:159]\n\t"
"s_waitcnt vmcnt(13) lgkmcnt(11)\n\t"
"ds_write_addtid_b32 v208 offset:1024\n\t"
"v_cvt_pk_bf16_f32 v193, v193, v197\n\t"
"v_mfma_f32_16x16x32_bf16 v[96:99], a[120:123], a[144:147], v[96:99]\n\t"
"s_nop 0\n\t"
"ds_write_addtid_b32 v217 offset:11392\n\t"
"ds_write_addtid_b32 v218 offset:7200\n\t"
"v_mfma_f32_16x16x32_bf16 v[96:99], a[124:127], a[160:163], v[96:99]\n\t"
"s_nop 0\n\t"
"ds_write_addtid_b32 v193 offset:8320\n\t"
"ds_write_addtid_b32 v201 offset:10368\n\t"
"v_mfma_f32_16x16x32_bf16 v[112:115], a[128:131], a[144:147], v[112:115]\n\t"
"s_nop 0\n\t"
"ds_write_addtid_b32 v192 offset:0\n\t"
"ds_write_addtid_b32 v219 offset:15520\n\t"
"v_mfma_f32_16x16x32_bf16 v[112:115], a[132:135], a[160:163], v[112:115]\n\t"
"buffer_load_dwordx4 a[28:31], off, s[84:87], s54 offset:1024 sc1\n\t"
"v_mfma_f32_16x16x32_bf16 v[128:131], a[136:139], a[144:147], v[128:131]\n\t"
"s_nop 0\n\t"
"ds_read_b128 a[156:159], v224 offset:17312\n\t"
"ds_read_b128 a[172:175], v224 offset:19360\n\t"
"v_mfma_f32_16x16x32_bf16 v[128:131], a[140:143], a[160:163], v[128:131]\n\t"
"buffer_load_dwordx4 a[64:67], off, s[28:31], 0 offset:0 sc1\n\t"
"v_mfma_f32_16x16x32_bf16 v[132:135], a[136:139], a[148:151], v[132:135]\n\t"
"s_nop 0\n\t"
"ds_read_b64_tr_b16 a[200:201], v250 offset:1152\n\t"
"ds_read_b64_tr_b16 a[202:203], v250 offset:1664\n\t"
"v_mfma_f32_16x16x32_bf16 v[132:135], a[140:143], a[164:167], v[132:135]\n\t"
"buffer_load_dwordx4 a[68:71], off, s[28:31], 0 offset:1024 sc1\n\t"
"v_mfma_f32_16x16x32_bf16 v[116:119], a[128:131], a[148:151], v[116:119]\n\t"
"s_nop 0\n\t"
"ds_read_b64_tr_b16 a[204:205], v251 offset:1152\n\t"
"ds_read_b64_tr_b16 a[206:207], v251 offset:1664\n\t"
"v_mfma_f32_16x16x32_bf16 v[116:119], a[132:135], a[164:167], v[116:119]\n\t"
"s_waitcnt lgkmcnt(12)\n\t"
"buffer_store_dwordx4 v[40:43], v234, s[16:19], s24 offen nt\n\t"
"v_mfma_f32_16x16x32_bf16 v[100:103], a[120:123], a[148:151], v[100:103]\n\t"
"v_mfma_f32_16x16x32_bf16 v[100:103], a[124:127], a[164:167], v[100:103]\n\t"
"s_waitcnt lgkmcnt(4)\n\t"
"s_barrier\n\t"
"v_mfma_f32_16x16x32_bf16 v[104:107], a[120:123], a[152:155], v[104:107]\n\t"
"ds_read_b128 a[144:147], v224 offset:0\n\t"
"v_mfma_f32_16x16x32_bf16 v[104:107], a[124:127], a[168:171], v[104:107]\n\t"
"ds_read_b128 a[148:151], v224 offset:256\n\t"
"v_mfma_f32_16x16x32_bf16 v[120:123], a[128:131], a[152:155], v[120:123]\n\t"
"ds_read_b128 a[160:163], v224 offset:2048\n\t"
"v_add_u32 v248, 122368, v227\n\t"
"v_mfma_f32_16x16x32_bf16 v[120:123], a[132:135], a[168:171], v[120:123]\n\t"
"ds_read_b128 a[164:167], v224 offset:2304\n\t"
"v_mfma_f32_16x16x32_bf16 v[136:139], a[136:139], a[152:155], v[136:139]\n\t"
"v_mfma_f32_16x16x32_bf16 v[136:139], a[140:143], a[168:171], v[136:139]\n\t"
"buffer_load_dwordx4 a[60:63], off, s[84:87], 0 offset:3072 sc1\n\t"
"v_mfma_f32_16x16x32_bf16 v[140:143], a[136:139], a[156:159], v[140:143]\n\t"
"ds_read_b128 a[152:155], v224 offset:512\n\t"
"v_xor_b32 v249, 32, v248\n\t"
"v_mfma_f32_16x16x32_bf16 v[140:143], a[140:143], a[172:175], v[140:143]\n\t"
"s_nop 0\n\t"
"ds_read_b128 a[168:171], v224 offset:2560\n\t"
"v_xor_b32 v250, 64, v248\n\t"
"v_mfma_f32_16x16x32_bf16 v[124:127], a[128:131], a[156:159], v[124:127]\n\t"
"s_nop 0\n\t"
"v_mfma_f32_16x16x32_bf16 v[124:127], a[132:135], a[172:175], v[124:127]\n\t"
"buffer_load_dwordx4 a[56:59], off, s[84:87], 0 offset:2048 sc1\n\t"
"v_mfma_f32_16x16x32_bf16 v[108:111], a[120:123], a[156:159], v[108:111]\n\t"
"s_nop 0\n\t"
"ds_read_b128 a[156:159], v224 offset:768\n\t"
"v_mfma_f32_16x16x32_bf16 v[108:111], a[124:127], a[172:175], v[108:111]\n\t"
"s_nop 0\n\t"
"ds_read_b128 a[172:175], v224 offset:2816\n\t"
"v_xor_b32 v251, 96, v248\n\t"
"v_mfma_f32_16x16x32_bf16 v[192:195], a[224:227], a[176:179], 0\n\t"
"s_nop 0\n\t"
"ds_read_b64_tr_b16 a[176:177], v248 offset:20736\n\t"
"ds_read_b64_tr_b16 a[178:179], v248 offset:21248\n\t"
"v_mfma_f32_16x16x32_bf16 v[196:199], a[224:227], a[180:183], 0\n\t"
"buffer_load_dwordx4 a[48:51], off, s[84:87], 0 offset:0 sc1\n\t"
"v_mfma_f32_16x16x32_bf16 v[200:203], a[224:227], a[184:187], 0\n\t"
"s_nop 0\n\t"
"ds_read_b64_tr_b16 a[180:181], v249 offset:20736\n\t"
"ds_read_b64_tr_b16 a[182:183], v249 offset:21248\n\t"
"v_mfma_f32_16x16x32_bf16 v[204:207], a[224:227], a[188:191], 0\n\t"
"buffer_load_dwordx4 a[52:55], off, s[84:87], 0 offset:1024 sc1\n\t"
"v_mfma_f32_16x16x32_bf16 v[208:211], a[224:227], a[192:195], 0\n\t"
"s_nop 0\n\t"
"ds_read_b64_tr_b16 a[184:185], v250 offset:20736\n\t"
"ds_read_b64_tr_b16 a[186:187], v250 offset:21248\n\t"
"v_mfma_f32_16x16x32_bf16 v[212:215], a[224:227], a[196:199], 0\n\t"
"s_waitcnt lgkmcnt(8)\n\t"
"ds_read_b64_tr_b16 a[188:189], v251 offset:20736\n\t"
"ds_read_b64_tr_b16 a[190:191], v251 offset:21248\n\t"
"v_mfma_f32_16x16x32_bf16 v[216:219], a[224:227], a[200:203], 0\n\t"
".p2align 3\n\t"
"ds_read_b64_tr_b16 a[192:193], v248 offset:0\n\t"
"buffer_load_dwordx4 a[88:91], off, s[28:31], s51 offset:0 sc1\n\t"
"v_pk_add_f32 v[96:97], v[96:97], v[144:145]\n\t"
"v_pk_add_f32 v[98:99], v[98:99], v[146:147]\n\t"
"v_pk_fma_f32 v[144:145], v[144:145], s[38:39], v[96:97]\n\t"
"v_pk_fma_f32 v[146:147], v[146:147], s[38:39], v[98:99]\n\t"
"v_pk_add_f32 v[112:113], v[112:113], v[160:161]\n\t"
"v_pk_add_f32 v[114:115], v[114:115], v[162:163]\n\t"
"v_pk_fma_f32 v[160:161], v[160:161], s[38:39], v[112:113]\n\t"
"v_pk_fma_f32 v[162:163], v[162:163], s[38:39], v[114:115]\n\t"
"v_pk_add_f32 v[128:129], v[128:129], v[176:177]\n\t"
"v_pk_add_f32 v[130:131], v[130:131], v[178:179]\n\t"
"v_pk_fma_f32 v[176:177], v[176:177], s[38:39], v[128:129]\n\t"
"v_pk_fma_f32 v[178:179], v[178:179], s[38:39], v[130:131]\n\t"
"buffer_load_dwordx4 a[92:95], off, s[28:31], s51 offset:1024 sc1\n\t"
"v_pk_add_f32 v[132:133], v[132:133], v[180:181]\n\t"
"v_pk_add_f32 v[134:135], v[134:135], v[182:183]\n\t"
"v_pk_fma_f32 v[180:181], v[180:181], s[38:39], v[132:133]\n\t"
"v_pk_fma_f32 v[182:183], v[182:183], s[38:39], v[134:135]\n\t"
"v_pk_add_f32 v[116:117], v[116:117], v[164:165]\n\t"
"v_pk_add_f32 v[118:119], v[118:119], v[166:167]\n\t"
"v_pk_fma_f32 v[164:165], v[164:165], s[38:39], v[116:117]\n\t"
"v_pk_fma_f32 v[166:167], v[166:167], s[38:39], v[118:119]\n\t"
"v_pk_add_f32 v[100:101], v[100:101], v[148:149]\n\t"
"v_pk_add_f32 v[102:103], v[102:103], v[150:151]\n\t"
"v_pk_fma_f32 v[148:149], v[148:149], s[38:39], v[100:101]\n\t"
"v_pk_fma_f32 v[150:151], v[150:151], s[38:39], v[102:103]\n\t"
"buffer_load_dwordx4 a[84:87], off, s[84:87], s51 offset:3072 sc1\n\t"
"v_pk_add_f32 v[104:105], v[104:105], v[152:153]\n\t"
"v_pk_add_f32 v[106:107], v[106:107], v[154:155]\n\t"
"v_pk_fma_f32 v[152:153], v[152:153], s[38:39], v[104:105]\n\t"
"v_pk_fma_f32 v[154:155], v[154:155], s[38:39], v[106:107]\n\t"
"v_pk_add_f32 v[120:121], v[120:121], v[168:169]\n\t"
"v_pk_add_f32 v[122:123], v[122:123], v[170:171]\n\t"
"v_pk_fma_f32 v[168:169], v[168:169], s[38:39], v[120:121]\n\t"
"v_pk_fma_f32 v[170:171], v[170:171], s[38:39], v[122:123]\n\t"
"v_pk_add_f32 v[136:137], v[136:137], v[184:185]\n\t"
"v_pk_add_f32 v[138:139], v[138:139], v[186:187]\n\t"
"v_pk_fma_f32 v[184:185], v[184:185], s[38:39], v[136:137]\n\t"
"v_pk_fma_f32 v[186:187], v[186:187], s[38:39], v[138:139]\n\t"
"buffer_load_dwordx4 a[80:83], off, s[84:87], s51 offset:2048 sc1\n\t"
"v_pk_add_f32 v[140:141], v[140:141], v[188:189]\n\t"
"v_pk_add_f32 v[142:143], v[142:143], v[190:191]\n\t"
"v_pk_fma_f32 v[188:189], v[188:189], s[38:39], v[140:141]\n\t"
"v_pk_fma_f32 v[190:191], v[190:191], s[38:39], v[142:143]\n\t"
"v_pk_add_f32 v[124:125], v[124:125], v[172:173]\n\t"
"v_pk_add_f32 v[126:127], v[126:127], v[174:175]\n\t"
"v_pk_fma_f32 v[172:173], v[172:173], s[38:39], v[124:125]\n\t"
"v_pk_fma_f32 v[174:175], v[174:175], s[38:39], v[126:127]\n\t"
"v_pk_add_f32 v[108:109], v[108:109], v[156:157]\n\t"
"v_pk_add_f32 v[110:111], v[110:111], v[158:159]\n\t"
"v_pk_fma_f32 v[156:157], v[156:157], s[38:39], v[108:109]\n\t"
"v_pk_fma_f32 v[158:159], v[158:159], s[38:39], v[110:111]\n\t"
"v_cvt_pk_bf16_f32 v100, v100, v101\n\t"
"v_cvt_pk_bf16_f32 v101, v102, v103\n\t"
"v_cvt_pk_bf16_f32 v108, v108, v109\n\t"
"v_mfma_f32_16x16x32_bf16 v[220:223], a[224:227], a[204:207], 0\n\t"
"s_waitcnt vmcnt(19)\n\t"
"ds_read_b64_tr_b16 a[194:195], v248 offset:512\n\t"
"buffer_store_dwordx4 v[12:15], v232, s[16:19], s27 offen nt\n\t"
"v_cvt_pk_bf16_f32 v193, v193, v197\n\t"
"v_cvt_pk_bf16_f32 v96, v96, v97\n\t"
"v_cvt_pk_bf16_f32 v116, v116, v117\n\t"
"v_cvt_pk_bf16_f32 v132, v132, v133\n\t"
"v_mfma_f32_16x16x32_bf16 v[48:51], a[0:3], a[144:147], v[48:51]\n\t"
"s_nop 0\n\t"
"v_cvt_pk_bf16_f32 v97, v98, v99\n\t"
"v_cvt_pk_bf16_f32 v128, v128, v129\n\t"
"v_mfma_f32_16x16x32_bf16 v[48:51], a[4:7], a[160:163], v[48:51]\n\t"
"s_nop 0\n\t"
"ds_write_addtid_b32 v193 offset:24864\n\t"
"ds_write2st64_b64 v228, v[96:97], v[100:101] offset0:40 offset1:53\n\t"
"v_cvt_pk_bf16_f32 v117, v118, v119\n\t"
"v_mfma_f32_16x16x32_bf16 v[64:67], a[8:11], a[144:147], v[64:67]\n\t"
"s_nop 0\n\t"
"v_cvt_pk_bf16_f32 v133, v134, v135\n\t"
"v_cvt_pk_bf16_f32 v104, v104, v105\n\t"
"v_mfma_f32_16x16x32_bf16 v[64:67], a[12:15], a[160:163], v[64:67]\n\t"
"buffer_load_dwordx4 a[72:75], off, s[84:87], s51 offset:0 sc1\n\t"
"v_cvt_pk_bf16_f32 v210, v210, v214\n\t"
"v_cvt_pk_bf16_f32 v105, v106, v107\n\t"
"v_mfma_f32_16x16x32_bf16 v[80:83], a[16:19], a[144:147], v[80:83]\n\t"
"s_nop 0\n\t"
"v_cvt_pk_bf16_f32 v112, v112, v113\n\t"
"v_cvt_pk_bf16_f32 v113, v114, v115\n\t"
"v_mfma_f32_16x16x32_bf16 v[80:83], a[20:23], a[160:163], v[80:83]\n\t"
"s_nop 0\n\t"
"ds_read_b128 a[144:147], v224 offset:1024\n\t"
"ds_read_b128 a[160:163], v224 offset:3072\n\t"
"ds_write2st64_b64 v246, v[112:113], v[116:117] offset0:40 offset1:53\n\t"
"v_mfma_f32_16x16x32_bf16 v[84:87], a[16:19], a[148:151], v[84:87]\n\t"
"s_nop 0\n\t"
"v_cvt_pk_bf16_f32 v195, v195, v199\n\t"
"v_cvt_pk_bf16_f32 v218, v218, v222\n\t"
"v_mfma_f32_16x16x32_bf16 v[84:87], a[20:23], a[164:167], v[84:87]\n\t"
"Lpad6_%=:\n\t"
"s_cbranch_execz Lret0_%=\n\t"
"buffer_load_dwordx4 a[76:79], off, s[84:87], s51 offset:1024 sc1\n\t"
"v_mfma_f32_16x16x32_bf16 v[68:71], a[8:11], a[148:151], v[68:71]\n\t"
"v_cvt_pk_bf16_f32 v129, v130, v131\n\t"
"v_cvt_pk_bf16_f32 v109, v110, v111\n\t"
"v_mfma_f32_16x16x32_bf16 v[68:71], a[12:15], a[164:167], v[68:71]\n\t"
"ds_write_addtid_b32 v210 offset:21696\n\t"
"ds_write2st64_b64 v247, v[128:129], v[132:133] offset0:40 offset1:53\n\t"
"v_cvt_pk_bf16_f32 v140, v140, v141\n\t"
"v_mfma_f32_16x16x32_bf16 v[52:55], a[0:3], a[148:151], v[52:55]\n\t"
"v_cvt_pk_bf16_f32 v202, v202, v206\n\t"
"v_cvt_pk_bf16_f32 v124, v124, v125\n\t"
"v_mfma_f32_16x16x32_bf16 v[52:55], a[4:7], a[164:167], v[52:55]\n\t"
"buffer_load_dwordx4 a[112:115], off, s[28:31], s81 offset:0 sc1\n\t"
"v_cvt_pk_bf16_f32 v120, v120, v121\n\t"
"v_mfma_f32_16x16x32_bf16 v[56:59], a[0:3], a[152:155], v[56:59]\n\t"
"v_cvt_pk_bf16_f32 v208, v208, v212\n\t"
"v_cvt_pk_bf16_f32 v219, v219, v223\n\t"
"v_mfma_f32_16x16x32_bf16 v[56:59], a[4:7], a[168:171], v[56:59]\n\t"
"s_nop 0\n\t"
"ds_read_b128 a[148:151], v224 offset:1280\n\t"
"ds_read_b128 a[164:167], v224 offset:3328\n\t"
"ds_write_addtid_b32 v208 offset:17568\n\t"
"ds_write_addtid_b32 v219 offset:32064\n\t"
"v_cvt_pk_bf16_f32 v203, v203, v207\n\t"
"v_mfma_f32_16x16x32_bf16 v[72:75], a[8:11], a[152:155], v[72:75]\n\t"
"v_cvt_pk_bf16_f32 v194, v194, v198\n\t"
"v_cvt_pk_bf16_f32 v201, v201, v205\n\t"
"v_mfma_f32_16x16x32_bf16 v[72:75], a[12:15], a[168:171], v[72:75]\n\t"
"ds_write_addtid_b32 v203 offset:31040\n\t"
"buffer_store_dwordx4 v[28:31], v233, s[16:19], s27 offen nt\n\t"
"ds_write2st64_b64 v228, v[104:105], v[108:109] offset0:66 offset1:79\n\t"
"v_cvt_pk_bf16_f32 v136, v136, v137\n\t"
"v_mfma_f32_16x16x32_bf16 v[88:91], a[16:19], a[152:155], v[88:91]\n\t"
"v_cvt_pk_bf16_f32 v121, v122, v123\n\t"
"v_cvt_pk_bf16_f32 v137, v138, v139\n\t"
"v_mfma_f32_16x16x32_bf16 v[88:91], a[20:23], a[168:171], v[88:91]\n\t"
"s_waitcnt lgkmcnt(8)\n\t"
"v_cvt_pk_bf16_f32 v217, v217, v221\n\t"
"v_cvt_pk_bf16_f32 v125, v126, v127\n\t"
"v_mfma_f32_16x16x32_bf16 v[92:95], a[16:19], a[156:159], v[92:95]\n\t"
"v_cvt_pk_bf16_f32 v200, v200, v204\n\t"
"v_cvt_pk_bf16_f32 v141, v142, v143\n\t"
"v_mfma_f32_16x16x32_bf16 v[92:95], a[20:23], a[172:175], v[92:95]\n\t"
"s_nop 0\n\t"
"ds_write_addtid_b32 v195 offset:28992\n\t"
"ds_write_addtid_b32 v200 offset:18592\n\t"
"ds_write_addtid_b32 v201 offset:26912\n\t"
"ds_write_addtid_b32 v202 offset:22720\n\t"
"ds_write_addtid_b32 v217 offset:27936\n\t"
"ds_write2st64_b64 v246, v[120:121], v[124:125] offset0:66 offset1:79\n\t"
"v_mfma_f32_16x16x32_bf16 v[76:79], a[8:11], a[156:159], v[76:79]\n\t"
"v_cvt_pk_bf16_f32 v209, v209, v213\n\t"
"v_cvt_pk_bf16_f32 v216, v216, v220\n\t"
"v_mfma_f32_16x16x32_bf16 v[76:79], a[12:15], a[172:175], v[76:79]\n\t"
"buffer_load_dwordx4 a[116:119], off, s[28:31], s81 offset:1024 sc1\n\t"
"v_mfma_f32_16x16x32_bf16 v[60:63], a[0:3], a[156:159], v[60:63]\n\t"
"v_cvt_pk_bf16_f32 v192, v192, v196\n\t"
"v_cvt_pk_bf16_f32 v211, v211, v215\n\t"
"v_mfma_f32_16x16x32_bf16 v[60:63], a[4:7], a[172:175], v[60:63]\n\t"
"s_waitcnt vmcnt(17) lgkmcnt(8)\n\t"
"ds_write_addtid_b32 v192 offset:16544\n\t"
"ds_write_addtid_b32 v194 offset:20672\n\t"
"ds_write_addtid_b32 v209 offset:25888\n\t"
"ds_write_addtid_b32 v211 offset:30016\n\t"
"ds_write2st64_b64 v247, v[136:137], v[140:141] offset0:66 offset1:79\n\t"
"v_mfma_f32_16x16x32_bf16 v[48:51], a[24:27], a[144:147], v[48:51]\n\t"
"buffer_load_dwordx4 a[108:111], off, s[84:87], s81 offset:3072 sc1\n\t"
"v_mfma_f32_16x16x32_bf16 v[48:51], a[28:31], a[160:163], v[48:51]\n\t"
"s_nop 0\n\t"
"ds_write_addtid_b32 v216 offset:19616\n\t"
"ds_write_addtid_b32 v218 offset:23744\n\t"
"v_mfma_f32_16x16x32_bf16 v[64:67], a[32:35], a[144:147], v[64:67]\n\t"
"buffer_load_dwordx4 a[104:107], off, s[84:87], s81 offset:2048 sc1\n\t"
"v_mfma_f32_16x16x32_bf16 v[64:67], a[36:39], a[160:163], v[64:67]\n\t"
"s_nop 0\n\t"
"ds_read_b128 a[152:155], v224 offset:1536\n\t"
"ds_read_b128 a[156:159], v224 offset:1792\n\t"
"v_mfma_f32_16x16x32_bf16 v[80:83], a[40:43], a[144:147], v[80:83]\n\t"
"s_nop 0\n\t"
"ds_read_b128 a[168:171], v224 offset:3584\n\t"
"ds_read_b128 a[172:175], v224 offset:3840\n\t"
"v_mfma_f32_16x16x32_bf16 v[80:83], a[44:47], a[160:163], v[80:83]\n\t"
"buffer_load_dwordx4 a[96:99], off, s[84:87], s81 offset:0 sc1\n\t"
"v_mfma_f32_16x16x32_bf16 v[84:87], a[40:43], a[148:151], v[84:87]\n\t"
"s_nop 0\n\t"
"ds_read_b64_tr_b16 a[196:197], v249 offset:0\n\t"
"ds_read_b64_tr_b16 a[198:199], v249 offset:512\n\t"
"v_mfma_f32_16x16x32_bf16 v[84:87], a[44:47], a[164:167], v[84:87]\n\t"
"s_nop 0\n\t"
"ds_read_b64_tr_b16 a[200:201], v250 offset:0\n\t"
"ds_read_b64_tr_b16 a[202:203], v250 offset:512\n\t"
"v_mfma_f32_16x16x32_bf16 v[68:71], a[32:35], a[148:151], v[68:71]\n\t"
"s_nop 0\n\t"
"ds_read_b64_tr_b16 a[204:205], v251 offset:0\n\t"
"ds_read_b64_tr_b16 a[206:207], v251 offset:512\n\t"
"v_mfma_f32_16x16x32_bf16 v[68:71], a[36:39], a[164:167], v[68:71]\n\t"
"buffer_store_dwordx4 v[44:47], v234, s[16:19], s27 offen nt\n\t"
"v_mfma_f32_16x16x32_bf16 v[52:55], a[24:27], a[148:151], v[52:55]\n\t"
"s_waitcnt lgkmcnt(6)\n\t"
"v_mfma_f32_16x16x32_bf16 v[52:55], a[28:31], a[164:167], v[52:55]\n\t"
"buffer_load_dwordx4 a[100:103], off, s[84:87], s81 offset:1024 sc1\n\t"
"s_barrier\n\t"
"v_mfma_f32_16x16x32_bf16 v[56:59], a[24:27], a[152:155], v[56:59]\n\t"
"s_nop 0\n\t"
"ds_read_b128 a[144:147], v224 offset:16544\n\t"
"ds_read_b128 a[164:167], v224 offset:18848\n\t"
"v_mfma_f32_16x16x32_bf16 v[56:59], a[28:31], a[168:171], v[56:59]\n\t"
"buffer_load_dwordx4 a[136:139], off, s[28:31], s66 offset:0 sc1\n\t"
"v_mfma_f32_16x16x32_bf16 v[72:75], a[32:35], a[152:155], v[72:75]\n\t"
"s_nop 0\n\t"
"ds_read_b128 a[160:163], v224 offset:18592\n\t"
"ds_read_b128 a[148:151], v224 offset:16800\n\t"
"v_mfma_f32_16x16x32_bf16 v[72:75], a[36:39], a[168:171], v[72:75]\n\t"
"buffer_load_dwordx4 a[140:143], off, s[28:31], s66 offset:1024 sc1\n\t"
"v_mfma_f32_16x16x32_bf16 v[88:91], a[40:43], a[152:155], v[88:91]\n\t"
"ds_read_b128 v[108:111], v229 offset:33792\n\t"
"v_add_u32 v248, 54016, v227\n\t"
"v_mfma_f32_16x16x32_bf16 v[88:91], a[44:47], a[168:171], v[88:91]\n\t"
"ds_read_b128 v[104:107], v229 offset:20480\n\t"
"v_xor_b32 v249, 32, v248\n\t"
"v_mfma_f32_16x16x32_bf16 v[92:95], a[40:43], a[156:159], v[92:95]\n\t"
"s_nop 0\n\t"
"ds_read_b128 v[136:139], v231 offset:20480\n\t"
"ds_read_b128 a[232:235], v230 offset:33792\n\t"
"v_mfma_f32_16x16x32_bf16 v[92:95], a[44:47], a[172:175], v[92:95]\n\t"
"buffer_load_dwordx4 a[132:135], off, s[84:87], s66 offset:3072 sc1\n\t"
"v_mfma_f32_16x16x32_bf16 v[76:79], a[32:35], a[156:159], v[76:79]\n\t"
"ds_read_b128 a[236:239], v231 offset:33792\n\t"
"v_xor_b32 v250, 64, v248\n\t"
"v_mfma_f32_16x16x32_bf16 v[76:79], a[36:39], a[172:175], v[76:79]\n\t"
"ds_read_b128 a[152:155], v224 offset:17056\n\t"
"v_xor_b32 v251, 96, v248\n\t"
"v_mfma_f32_16x16x32_bf16 v[60:63], a[24:27], a[156:159], v[60:63]\n\t"
"ds_read_b128 v[120:123], v230 offset:20480\n\t"
"v_mfma_f32_16x16x32_bf16 v[60:63], a[28:31], a[172:175], v[60:63]\n\t"
"buffer_load_dwordx4 a[128:131], off, s[84:87], s66 offset:2048 sc1\n\t"
"v_mfma_f32_16x16x32_bf16 v[192:195], a[216:219], a[176:179], 0\n\t"
"s_nop 0\n\t"
"ds_read_b128 a[168:171], v224 offset:19104\n\t"
"ds_read_b64_tr_b16 a[176:177], v248 offset:21888\n\t"
"v_mfma_f32_16x16x32_bf16 v[196:199], a[216:219], a[180:183], 0\n\t"
"ds_read_b64_tr_b16 a[178:179], v248 offset:22400\n\t"
"v_mfma_f32_16x16x32_bf16 v[200:203], a[216:219], a[184:187], 0\n\t"
"s_waitcnt lgkmcnt(8)\n\t"
"buffer_store_dwordx4 v[104:107], v232, s[16:19], s20 offen nt\n\t"
"v_mfma_f32_16x16x32_bf16 v[204:207], a[216:219], a[188:191], 0\n\t"
"s_nop 0\n\t"
"ds_read_b64_tr_b16 a[180:181], v249 offset:21888\n\t"
"ds_read_b64_tr_b16 a[182:183], v249 offset:22400\n\t"
"v_mfma_f32_16x16x32_bf16 v[208:211], a[216:219], a[192:195], 0\n\t"
"s_nop 0\n\t"
"ds_read_b64_tr_b16 a[184:185], v250 offset:21888\n\t"
"ds_read_b64_tr_b16 a[186:187], v250 offset:22400\n\t"
"v_mfma_f32_16x16x32_bf16 v[212:215], a[216:219], a[196:199], 0\n\t"
"s_waitcnt lgkmcnt(7)\n\t"
"s_barrier\n\t"
"v_mfma_f32_16x16x32_bf16 v[216:219], a[216:219], a[200:203], 0\n\t"
"buffer_load_dwordx4 a[120:123], off, s[84:87], s66 offset:0 sc1\n\t"
"v_mfma_f32_16x16x32_bf16 v[220:223], a[216:219], a[204:207], 0\n\t"
"s_waitcnt vmcnt(21)\n\t"
"ds_read_b128 a[156:159], v224 offset:17312\n\t"
"v_cvt_pk_bf16_f32 v195, v195, v199\n\t"
"v_mfma_f32_16x16x32_bf16 v[48:51], a[48:51], a[144:147], v[48:51]\n\t"
"ds_read_b128 a[172:175], v224 offset:19360\n\t"
"v_cvt_pk_bf16_f32 v194, v194, v198\n\t"
"v_mfma_f32_16x16x32_bf16 v[48:51], a[52:55], a[160:163], v[48:51]\n\t"
"ds_read_b64_tr_b16 a[188:189], v251 offset:21888\n\t"
"v_cvt_pk_bf16_f32 v200, v200, v204\n\t"
"v_mfma_f32_16x16x32_bf16 v[64:67], a[56:59], a[144:147], v[64:67]\n\t"
"ds_read_b64_tr_b16 a[190:191], v251 offset:22400\n\t"
"v_cvt_pk_bf16_f32 v203, v203, v207\n\t"
"v_mfma_f32_16x16x32_bf16 v[64:67], a[60:63], a[160:163], v[64:67]\n\t"
"buffer_load_dwordx4 a[124:127], off, s[84:87], s66 offset:1024 sc1\n\t"
"v_mfma_f32_16x16x32_bf16 v[80:83], a[64:67], a[144:147], v[80:83]\n\t"
"ds_read_b128 a[144:147], v224 offset:17568\n\t"
"v_cvt_pk_bf16_f32 v201, v201, v205\n\t"
"v_mfma_f32_16x16x32_bf16 v[80:83], a[68:71], a[160:163], v[80:83]\n\t"
"ds_read_b128 a[160:163], v224 offset:19616\n\t"
"v_cvt_pk_bf16_f32 v208, v208, v212\n\t"
"v_mfma_f32_16x16x32_bf16 v[84:87], a[64:67], a[148:151], v[84:87]\n\t"
"ds_write_addtid_b32 v194 offset:4128\n\t"
"v_cvt_pk_bf16_f32 v219, v219, v223\n\t"
"v_mfma_f32_16x16x32_bf16 v[84:87], a[68:71], a[164:167], v[84:87]\n\t"
"ds_write_addtid_b32 v203 offset:14496\n\t"
"buffer_store_dwordx4 v[120:123], v233, s[16:19], s20 offen nt\n\t"
"v_mfma_f32_16x16x32_bf16 v[68:71], a[56:59], a[148:151], v[68:71]\n\t"
"ds_write_addtid_b32 v195 offset:12448\n\t"
"v_cvt_pk_bf16_f32 v216, v216, v220\n\t"
"v_mfma_f32_16x16x32_bf16 v[68:71], a[60:63], a[164:167], v[68:71]\n\t"
"ds_write_addtid_b32 v208 offset:1024\n\t"
"v_cvt_pk_bf16_f32 v192, v192, v196\n\t"
"v_mfma_f32_16x16x32_bf16 v[52:55], a[48:51], a[148:151], v[52:55]\n\t"
"ds_read_b128 a[148:151], v224 offset:17824\n\t"
"v_cvt_pk_bf16_f32 v210, v210, v214\n\t"
"v_mfma_f32_16x16x32_bf16 v[52:55], a[52:55], a[164:167], v[52:55]\n\t"
"s_waitcnt lgkmcnt(8)\n\t"
"ds_write_addtid_b32 v216 offset:3072\n\t"
"v_cvt_pk_bf16_f32 v202, v202, v206\n\t"
"v_mfma_f32_16x16x32_bf16 v[56:59], a[48:51], a[152:155], v[56:59]\n\t"
"ds_read_b128 a[164:167], v224 offset:19872\n\t"
"v_cvt_pk_bf16_f32 v209, v209, v213\n\t"
"v_mfma_f32_16x16x32_bf16 v[56:59], a[52:55], a[168:171], v[56:59]\n\t"
"ds_write_addtid_b32 v210 offset:5152\n\t"
"v_cvt_pk_bf16_f32 v217, v217, v221\n\t"
"v_mfma_f32_16x16x32_bf16 v[72:75], a[56:59], a[152:155], v[72:75]\n\t"
"s_nop 0\n\t"
"ds_write_addtid_b32 v201 offset:10368\n\t"
"ds_write_addtid_b32 v217 offset:11392\n\t"
"v_mfma_f32_16x16x32_bf16 v[72:75], a[60:63], a[168:171], v[72:75]\n\t"
"buffer_store_dwordx4 v[136:139], v234, s[16:19], s20 offen nt\n\t"
"v_mfma_f32_16x16x32_bf16 v[88:91], a[64:67], a[152:155], v[88:91]\n\t"
"s_nop 0\n\t"
"ds_write_addtid_b32 v202 offset:6176\n\t"
"ds_write_addtid_b32 v209 offset:9344\n\t"
"v_mfma_f32_16x16x32_bf16 v[88:91], a[68:71], a[168:171], v[88:91]\n\t"
"s_nop 0\n\t"
"ds_write_addtid_b32 v200 offset:2048\n\t"
"ds_write_addtid_b32 v219 offset:15520\n\t"
"v_mfma_f32_16x16x32_bf16 v[92:95], a[64:67], a[156:159], v[92:95]\n\t"
"s_add_u32 m0, s26, 103936\n\t"
"ds_read_b128 a[152:155], v224 offset:18080\n\t"
"v_cvt_pk_bf16_f32 v193, v193, v197\n\t"
"v_mfma_f32_16x16x32_bf16 v[92:95], a[68:71], a[172:175], v[92:95]\n\t"
"buffer_load_dwordx4 v236, s[12:15], s22 offen offset:0 nt lds\n\t"
"v_mfma_f32_16x16x32_bf16 v[76:79], a[56:59], a[156:159], v[76:79]\n\t"
"ds_read_b128 a[168:171], v224 offset:20128\n\t"
"v_cvt_pk_bf16_f32 v218, v218, v222\n\t"
"v_mfma_f32_16x16x32_bf16 v[76:79], a[60:63], a[172:175], v[76:79]\n\t"
"buffer_load_dwordx4 v237, s[12:15], s22 offen offset:1024 nt lds\n\t"
"v_mfma_f32_16x16x32_bf16 v[60:63], a[48:51], a[156:159], v[60:63]\n\t"
"s_lshl_b32 m0, s73, 8\n\t"
"ds_read_b128 a[156:159], v224 offset:18336\n\t"
"v_cvt_pk_bf16_f32 v211, v211, v215\n\t"
"v_mfma_f32_16x16x32_bf16 v[60:63], a[52:55], a[172:175], v[60:63]\n\t"
"s_waitcnt vmcnt(19) lgkmcnt(9)\n\t"
"ds_write_addtid_b32 v192 offset:0\n\t"
"ds_write_addtid_b32 v193 offset:8320\n\t"
"v_mfma_f32_16x16x32_bf16 v[144:147], a[72:75], a[144:147], v[144:147]\n\t"
"s_nop 0\n\t"
"ds_write_addtid_b32 v211 offset:13472\n\t"
"ds_write_addtid_b32 v218 offset:7200\n\t"
"v_mfma_f32_16x16x32_bf16 v[144:147], a[76:79], a[160:163], v[144:147]\n\t"
"s_add_u32 m0, s26, 103936\n\t"
"ds_read_b128 a[172:175], v224 offset:20384\n\t"
"v_mfma_f32_16x16x32_bf16 v[160:163], a[80:83], a[144:147], v[160:163]\n\t"
"buffer_load_dwordx4 v238, s[12:15], s22 offen offset:2048 nt lds\n\t"
"v_mfma_f32_16x16x32_bf16 v[160:163], a[84:87], a[160:163], v[160:163]\n\t"
"s_nop 0\n\t"
"ds_read_b64_tr_b16 a[192:193], v248 offset:1152\n\t"
"ds_read_b64_tr_b16 a[194:195], v248 offset:1664\n\t"
"v_mfma_f32_16x16x32_bf16 v[176:179], a[88:91], a[144:147], v[176:179]\n\t"
"s_nop 0\n\t"
"ds_read_b64_tr_b16 a[196:197], v249 offset:1152\n\t"
"ds_read_b64_tr_b16 a[198:199], v249 offset:1664\n\t"
"v_mfma_f32_16x16x32_bf16 v[176:179], a[92:95], a[160:163], v[176:179]\n\t"
"buffer_load_dwordx4 v239, s[12:15], s22 offen offset:3072 nt lds\n\t"
"v_mfma_f32_16x16x32_bf16 v[180:183], a[88:91], a[148:151], v[180:183]\n\t"
"s_nop 0\n\t"
"ds_read_b64_tr_b16 a[200:201], v250 offset:1152\n\t"
"ds_read_b64_tr_b16 a[202:203], v250 offset:1664\n\t"
"v_mfma_f32_16x16x32_bf16 v[180:183], a[92:95], a[164:167], v[180:183]\n\t"
"s_mov_b64 exec, s[36:37]\n\t"
"buffer_load_dwordx4 v240, s[12:15], s22 offen offset:3584 nt lds\n\t"
"s_mov_b64 exec, -1\n\t"
"v_mfma_f32_16x16x32_bf16 v[164:167], a[80:83], a[148:151], v[164:167]\n\t"
"s_lshl_b32 m0, s73, 8\n\t"
"ds_read_b64_tr_b16 a[204:205], v251 offset:1152\n\t"
"ds_read_b64_tr_b16 a[206:207], v251 offset:1664\n\t"
"v_mfma_f32_16x16x32_bf16 v[164:167], a[84:87], a[164:167], v[164:167]\n\t"
"buffer_store_dwordx4 v[108:111], v232, s[16:19], s25 offen nt\n\t"
"v_mfma_f32_16x16x32_bf16 v[148:151], a[72:75], a[148:151], v[148:151]\n\t"
"s_waitcnt lgkmcnt(8)\n\t"
"v_mfma_f32_16x16x32_bf16 v[148:151], a[76:79], a[164:167], v[148:151]\n\t"
"s_barrier\n\t"
"v_mfma_f32_16x16x32_bf16 v[152:155], a[72:75], a[152:155], v[152:155]\n\t"
"ds_read_b128 a[144:147], v224 offset:0\n\t"
"v_mfma_f32_16x16x32_bf16 v[152:155], a[76:79], a[168:171], v[152:155]\n\t"
"ds_read_b128 a[160:163], v224 offset:2048\n\t"
"v_add_u32 v248, 54016, v226\n\t"
"v_mfma_f32_16x16x32_bf16 v[168:171], a[80:83], a[152:155], v[168:171]\n\t"
"ds_read_b128 a[148:151], v224 offset:256\n\t"
"v_mfma_f32_16x16x32_bf16 v[168:171], a[84:87], a[168:171], v[168:171]\n\t"
"ds_read_b128 a[164:167], v224 offset:2304\n\t"
"v_mfma_f32_16x16x32_bf16 v[184:187], a[88:91], a[152:155], v[184:187]\n\t"
"v_mfma_f32_16x16x32_bf16 v[184:187], a[92:95], a[168:171], v[184:187]\n\t"
"ds_read_b128 a[152:155], v224 offset:512\n\t"
"v_xor_b32 v249, 32, v248\n\t"
"v_mfma_f32_16x16x32_bf16 v[188:191], a[88:91], a[156:159], v[188:191]\n\t"
"ds_read_b128 a[228:231], v254 offset:18432\n\t"
"v_mfma_f32_16x16x32_bf16 v[188:191], a[92:95], a[172:175], v[188:191]\n\t"
"ds_read_b128 a[168:171], v224 offset:2560\n\t"
"v_mfma_f32_16x16x32_bf16 v[172:175], a[80:83], a[156:159], v[172:175]\n\t"
"v_mfma_f32_16x16x32_bf16 v[172:175], a[84:87], a[172:175], v[172:175]\n\t"
"v_xor_b32 v250, 64, v248\n\t"
"v_mfma_f32_16x16x32_bf16 v[156:159], a[72:75], a[156:159], v[156:159]\n\t"
"ds_read_b128 a[156:159], v224 offset:768\n\t"
"v_xor_b32 v251, 96, v248\n\t"
"v_mfma_f32_16x16x32_bf16 v[156:159], a[76:79], a[172:175], v[156:159]\n\t"
"ds_read_b128 a[172:175], v224 offset:2816\n\t"
"v_mfma_f32_16x16x32_bf16 v[192:195], a[224:227], a[176:179], 0\n\t"
"ds_read_b64_tr_b16 a[178:179], v248 offset:128\n\t"
"v_mfma_f32_16x16x32_bf16 v[196:199], a[224:227], a[180:183], 0\n\t"
"ds_read_b64_tr_b16 a[176:177], v248 offset:0\n\t"
"v_mfma_f32_16x16x32_bf16 v[200:203], a[224:227], a[184:187], 0\n\t"
"ds_read_b64_tr_b16 a[180:181], v249 offset:256\n\t"
"v_mfma_f32_16x16x32_bf16 v[204:207], a[224:227], a[188:191], 0\n\t"
"s_waitcnt lgkmcnt(7)\n\t"
"v_mfma_f32_16x16x32_bf16 v[208:211], a[224:227], a[192:195], 0\n\t"
"s_waitcnt lgkmcnt(6)\n\t"
"ds_write_b128 v255, a[228:231] offset:0\n\t"
"v_mfma_f32_16x16x32_bf16 v[212:215], a[224:227], a[196:199], 0\n\t"
"v_mfma_f32_16x16x32_bf16 v[216:219], a[224:227], a[200:203], 0\n\t"
"v_mfma_f32_16x16x32_bf16 v[220:223], a[224:227], a[204:207], 0\n\t"
"v_cvt_pk_bf16_f32 v195, v195, v199\n\t"
"v_mfma_f32_16x16x32_bf16 v[0:3], a[0:3], a[144:147], 0\n\t"
"ds_read_b64_tr_b16 a[186:187], v249 offset:128\n\t"
"v_cvt_pk_bf16_f32 v193, v193, v197\n\t"
"v_mfma_f32_16x16x32_bf16 v[0:3], a[4:7], a[160:163], v[0:3]\n\t"
"ds_read_b64_tr_b16 a[184:185], v249 offset:0\n\t"
"v_mfma_f32_16x16x32_bf16 v[16:19], a[8:11], a[144:147], 0\n\t"
"ds_read_b64_tr_b16 a[188:189], v248 offset:256\n\t"
"v_cvt_pk_bf16_f32 v201, v201, v205\n\t"
"v_mfma_f32_16x16x32_bf16 v[16:19], a[12:15], a[160:163], v[16:19]\n\t"
"ds_read_b64_tr_b16 a[194:195], v250 offset:128\n\t"
"v_cvt_pk_bf16_f32 v200, v200, v204\n\t"
"v_mfma_f32_16x16x32_bf16 v[32:35], a[16:19], a[144:147], 0\n\t"
"ds_read_b128 a[144:147], v224 offset:1024\n\t"
"v_mfma_f32_16x16x32_bf16 v[32:35], a[20:23], a[160:163], v[32:35]\n\t"
"ds_write_addtid_b32 v201 offset:26912\n\t"
"v_cvt_pk_bf16_f32 v203, v203, v207\n\t"
"v_mfma_f32_16x16x32_bf16 v[36:39], a[16:19], a[148:151], 0\n\t"
"ds_read_b64_tr_b16 a[192:193], v250 offset:0\n\t"
"v_cvt_pk_bf16_f32 v211, v211, v215\n\t"
"v_mfma_f32_16x16x32_bf16 v[36:39], a[20:23], a[164:167], v[36:39]\n\t"
"ds_write_addtid_b32 v193 offset:24864\n\t"
"v_cvt_pk_bf16_f32 v219, v219, v223\n\t"
"v_mfma_f32_16x16x32_bf16 v[20:23], a[8:11], a[148:151], 0\n\t"
"ds_read_b128 a[160:163], v224 offset:3072\n\t"
"v_cvt_pk_bf16_f32 v209, v209, v213\n\t"
"v_mfma_f32_16x16x32_bf16 v[20:23], a[12:15], a[164:167], v[20:23]\n\t"
"ds_write_addtid_b32 v195 offset:28992\n\t"
"v_mfma_f32_16x16x32_bf16 v[4:7], a[0:3], a[148:151], 0\n\t"
"ds_read_b128 a[148:151], v224 offset:1280\n\t"
"v_cvt_pk_bf16_f32 v216, v216, v220\n\t"
"v_mfma_f32_16x16x32_bf16 v[4:7], a[4:7], a[164:167], v[4:7]\n\t"
"s_waitcnt lgkmcnt(9)\n\t"
"ds_write_addtid_b32 v200 offset:18592\n\t"
"v_cvt_pk_bf16_f32 v202, v202, v206\n\t"
"v_mfma_f32_16x16x32_bf16 v[8:11], a[0:3], a[152:155], 0\n\t"
"ds_read_b128 a[164:167], v224 offset:3328\n\t"
"v_cvt_pk_bf16_f32 v218, v218, v222\n\t"
"v_mfma_f32_16x16x32_bf16 v[8:11], a[4:7], a[168:171], v[8:11]\n\t"
"ds_write_addtid_b32 v219 offset:32064\n\t"
"v_cvt_pk_bf16_f32 v217, v217, v221\n\t"
"v_mfma_f32_16x16x32_bf16 v[24:27], a[8:11], a[152:155], 0\n\t"
"ds_write_addtid_b32 v217 offset:27936\n\t"
"v_cvt_pk_bf16_f32 v194, v194, v198\n\t"
"v_mfma_f32_16x16x32_bf16 v[24:27], a[12:15], a[168:171], v[24:27]\n\t"
"ds_write_addtid_b32 v216 offset:19616\n\t"
"v_cvt_pk_bf16_f32 v210, v210, v214\n\t"
"v_mfma_f32_16x16x32_bf16 v[40:43], a[16:19], a[152:155], 0\n\t"
"ds_read_b128 a[152:155], v224 offset:1536\n\t"
"v_mfma_f32_16x16x32_bf16 v[40:43], a[20:23], a[168:171], v[40:43]\n\t"
"ds_read_b128 a[168:171], v224 offset:3584\n\t"
"v_mfma_f32_16x16x32_bf16 v[44:47], a[16:19], a[156:159], 0\n\t"
"ds_write_addtid_b32 v218 offset:23744\n\t"
"v_mfma_f32_16x16x32_bf16 v[44:47], a[20:23], a[172:175], v[44:47]\n\t"
"ds_write_addtid_b32 v203 offset:31040\n\t"
"v_cvt_pk_bf16_f32 v192, v192, v196\n\t"
"v_mfma_f32_16x16x32_bf16 v[28:31], a[8:11], a[156:159], 0\n\t"
"ds_write_addtid_b32 v210 offset:21696\n\t"
"v_cvt_pk_bf16_f32 v208, v208, v212\n\t"
"v_mfma_f32_16x16x32_bf16 v[28:31], a[12:15], a[172:175], v[28:31]\n\t"
"ds_write_addtid_b32 v208 offset:17568\n\t"
"v_mfma_f32_16x16x32_bf16 v[12:15], a[0:3], a[156:159], 0\n\t"
"ds_write_addtid_b32 v202 offset:22720\n\t"
"v_mfma_f32_16x16x32_bf16 v[12:15], a[4:7], a[172:175], v[12:15]\n\t"
"s_waitcnt lgkmcnt(9)\n\t"
"ds_write_addtid_b32 v192 offset:16544\n\t"
"v_mfma_f32_16x16x32_bf16 v[0:3], a[24:27], a[144:147], v[0:3]\n\t"
"ds_write_addtid_b32 v209 offset:25888\n\t"
"v_mfma_f32_16x16x32_bf16 v[0:3], a[28:31], a[160:163], v[0:3]\n\t"
"ds_write_addtid_b32 v211 offset:30016\n\t"
"v_mfma_f32_16x16x32_bf16 v[16:19], a[32:35], a[144:147], v[16:19]\n\t"
"ds_write_addtid_b32 v194 offset:20672\n\t"
"v_mfma_f32_16x16x32_bf16 v[16:19], a[36:39], a[160:163], v[16:19]\n\t"
"ds_read_b128 a[156:159], v224 offset:1792\n\t"
"v_mfma_f32_16x16x32_bf16 v[32:35], a[40:43], a[144:147], v[32:35]\n\t"
"ds_read_b128 a[172:175], v224 offset:3840\n\t"
"v_mfma_f32_16x16x32_bf16 v[32:35], a[44:47], a[160:163], v[32:35]\n\t"
"ds_read_b64_tr_b16 a[196:197], v251 offset:256\n\t"
"v_mfma_f32_16x16x32_bf16 v[36:39], a[40:43], a[148:151], v[36:39]\n\t"
"ds_read_b64_tr_b16 a[202:203], v251 offset:128\n\t"
"v_mfma_f32_16x16x32_bf16 v[36:39], a[44:47], a[164:167], v[36:39]\n\t"
"ds_read_b64_tr_b16 a[200:201], v251 offset:0\n\t"
"v_mfma_f32_16x16x32_bf16 v[20:23], a[32:35], a[148:151], v[20:23]\n\t"
"ds_read_b64_tr_b16 a[204:205], v250 offset:256\n\t"
"v_mfma_f32_16x16x32_bf16 v[20:23], a[36:39], a[164:167], v[20:23]\n\t"
"v_mfma_f32_16x16x32_bf16 v[4:7], a[24:27], a[148:151], v[4:7]\n\t"
"v_mfma_f32_16x16x32_bf16 v[4:7], a[28:31], a[164:167], v[4:7]\n\t"
"s_waitcnt lgkmcnt(0)\n\t"
"s_barrier\n\t"
"v_mfma_f32_16x16x32_bf16 v[8:11], a[24:27], a[152:155], v[8:11]\n\t"
"ds_read_b128 a[160:163], v224 offset:18592\n\t"
"v_mfma_f32_16x16x32_bf16 v[8:11], a[28:31], a[168:171], v[8:11]\n\t"
"ds_read_b128 a[144:147], v224 offset:16544\n\t"
"v_mfma_f32_16x16x32_bf16 v[24:27], a[32:35], a[152:155], v[24:27]\n\t"
"ds_read_b128 a[148:151], v224 offset:16800\n\t"
"v_swap_b32 v236, v241\n\t"
"v_mfma_f32_16x16x32_bf16 v[24:27], a[36:39], a[168:171], v[24:27]\n\t"
"ds_read_b128 a[164:167], v224 offset:18848\n\t"
"v_mfma_f32_16x16x32_bf16 v[40:43], a[40:43], a[152:155], v[40:43]\n\t"
"v_swap_b32 v237, v242\n\t"
"v_mfma_f32_16x16x32_bf16 v[40:43], a[44:47], a[168:171], v[40:43]\n\t"
"v_swap_b32 v238, v243\n\t"
"v_add_u32 v248, 101632, v227\n\t"
"v_mfma_f32_16x16x32_bf16 v[44:47], a[40:43], a[156:159], v[44:47]\n\t"
"v_swap_b32 v239, v244\n\t"
"v_mfma_f32_16x16x32_bf16 v[44:47], a[44:47], a[172:175], v[44:47]\n\t"
"ds_write_b128 v254, a[244:247] offset:20736\n\t"
"v_mfma_f32_16x16x32_bf16 v[28:31], a[32:35], a[156:159], v[28:31]\n\t"
"v_swap_b32 v240, v245\n\t"
"v_mfma_f32_16x16x32_bf16 v[28:31], a[36:39], a[172:175], v[28:31]\n\t"
"v_mfma_f32_16x16x32_bf16 v[12:15], a[24:27], a[156:159], v[12:15]\n\t"
"v_add_u32 v225, v225, v253\n\t"
"v_mfma_f32_16x16x32_bf16 v[12:15], a[28:31], a[172:175], v[12:15]\n\t"
"ds_read_b128 a[156:159], v224 offset:17312\n\t"
"v_sub_u32 v253, 0, v253\n\t"
"v_mfma_f32_4x4x4_16b_bf16 v[200:203], a[220:221], a[178:179], 0\n\t"
"v_accvgpr_mov_b32 a228, a208\n\t"
"v_mfma_f32_4x4x4_16b_bf16 v[204:207], a[220:221], a[186:187], 0\n\t"
"v_mfma_f32_4x4x4_16b_bf16 v[216:219], a[220:221], a[194:195], 0\n\t"
"v_mfma_f32_4x4x4_16b_bf16 v[220:223], a[220:221], a[202:203], 0\n\t"
"ds_read_b128 a[152:155], v224 offset:17056\n\t"
"v_mfma_f32_4x4x4_16b_bf16 v[192:195], a[222:223], a[176:177], v[200:203]\n\t"
"v_xor_b32 v249, 32, v248\n\t"
"v_mfma_f32_4x4x4_16b_bf16 v[196:199], a[222:223], a[184:185], v[204:207]\n\t"
"v_mfma_f32_4x4x4_16b_bf16 v[200:203], a[222:223], a[180:181], v[200:203]\n\t"
"ds_read_b128 a[168:171], v224 offset:19104\n\t"
"v_mfma_f32_4x4x4_16b_bf16 v[204:207], a[222:223], a[188:189], v[204:207]\n\t"
"v_accvgpr_mov_b32 a208, a209\n\t"
"v_mfma_f32_4x4x4_16b_bf16 v[208:211], a[222:223], a[192:193], v[216:219]\n\t"
"v_xor_b32 v250, 64, v248\n\t"
"v_mfma_f32_4x4x4_16b_bf16 v[212:215], a[222:223], a[200:201], v[220:223]\n\t"
"v_xor_b32 v251, 96, v248\n\t"
"v_mfma_f32_4x4x4_16b_bf16 v[216:219], a[222:223], a[196:197], v[216:219]\n\t"
"ds_read_b128 a[172:175], v224 offset:19360\n\t"
"v_mfma_f32_4x4x4_16b_bf16 v[220:223], a[222:223], a[204:205], v[220:223]\n\t"
"s_waitcnt vmcnt(15) lgkmcnt(3)\n\t"
"buffer_load_dwordx4 a[16:19], off, s[28:31], s57 offset:0 sc1\n\t"
"v_mfma_f32_16x16x32_bf16 v[144:147], a[96:99], a[144:147], v[144:147]\n\t"
"v_cvt_pk_bf16_f32 v193, -v193, -v197\n\t"
"v_cvt_pk_bf16_f32 v194, -v194, -v198\n\t"
"v_mfma_f32_16x16x32_bf16 v[144:147], a[100:103], a[160:163], v[144:147]\n\t"
"ds_write_addtid_b32 v193 offset:4128\n\t"
"v_cvt_pk_bf16_f32 v192, -v192, -v196\n\t"
"v_mfma_f32_16x16x32_bf16 v[160:163], a[104:107], a[144:147], v[160:163]\n\t"
"ds_write_addtid_b32 v194 offset:1024\n\t"
"v_cvt_pk_bf16_f32 v200, v200, v204\n\t"
"v_mfma_f32_16x16x32_bf16 v[160:163], a[108:111], a[160:163], v[160:163]\n\t"
"buffer_load_dwordx4 a[20:23], off, s[28:31], s57 offset:1024 sc1\n\t"
"v_mfma_f32_16x16x32_bf16 v[176:179], a[112:115], a[144:147], v[176:179]\n\t"
"ds_read_b128 a[144:147], v224 offset:17568\n\t"
"v_cvt_pk_bf16_f32 v211, -v211, -v215\n\t"
"v_mfma_f32_16x16x32_bf16 v[176:179], a[116:119], a[160:163], v[176:179]\n\t"
"buffer_load_dwordx4 a[12:15], off, s[84:87], s57 offset:3072 sc1\n\t"
"v_mfma_f32_16x16x32_bf16 v[180:183], a[112:115], a[148:151], v[180:183]\n\t"
"ds_read_b128 a[160:163], v224 offset:19616\n\t"
"v_cvt_pk_bf16_f32 v208, -v208, -v212\n\t"
"v_mfma_f32_16x16x32_bf16 v[180:183], a[116:119], a[164:167], v[180:183]\n\t"
"buffer_load_dwordx4 a[8:11], off, s[84:87], s57 offset:2048 sc1\n\t"
"v_mfma_f32_16x16x32_bf16 v[164:167], a[104:107], a[148:151], v[164:167]\n\t"
"ds_write_addtid_b32 v211 offset:7200\n\t"
"v_cvt_pk_bf16_f32 v201, v201, v205\n\t"
"v_mfma_f32_16x16x32_bf16 v[164:167], a[108:111], a[164:167], v[164:167]\n\t"
"s_nop 0\n\t"
"ds_write_addtid_b32 v208 offset:2048\n\t"
"v_cvt_pk_bf16_f32 v209, -v209, -v213\n\t"
"v_mfma_f32_16x16x32_bf16 v[148:151], a[96:99], a[148:151], v[148:151]\n\t"
"s_nop 0\n\t"
"ds_read_b128 a[148:151], v224 offset:17824\n\t"
"v_cvt_pk_bf16_f32 v218, v218, v222\n\t"
"v_mfma_f32_16x16x32_bf16 v[148:151], a[100:103], a[164:167], v[148:151]\n\t"
"s_waitcnt lgkmcnt(6)\n\t"
"buffer_load_dwordx4 a[0:3], off, s[84:87], s57 offset:0 sc1\n\t"
"v_mfma_f32_16x16x32_bf16 v[152:155], a[96:99], a[152:155], v[152:155]\n\t"
"s_nop 0\n\t"
"ds_read_b128 a[164:167], v224 offset:19872\n\t"
"v_cvt_pk_bf16_f32 v203, v203, v207\n\t"
"v_mfma_f32_16x16x32_bf16 v[152:155], a[100:103], a[168:171], v[152:155]\n\t"
"s_nop 0\n\t"
"ds_write_addtid_b32 v218 offset:11392\n\t"
"v_cvt_pk_bf16_f32 v219, v219, v223\n\t"
"v_mfma_f32_16x16x32_bf16 v[168:171], a[104:107], a[152:155], v[168:171]\n\t"
"s_nop 0\n\t"
"ds_write_addtid_b32 v200 offset:8320\n\t"
"v_cvt_pk_bf16_f32 v216, v216, v220\n\t"
"v_mfma_f32_16x16x32_bf16 v[168:171], a[108:111], a[168:171], v[168:171]\n\t"
"buffer_load_dwordx4 a[4:7], off, s[84:87], s57 offset:1024 sc1\n\t"
"v_mfma_f32_16x16x32_bf16 v[184:187], a[112:115], a[152:155], v[184:187]\n\t"
"s_nop 0\n\t"
"ds_read_b128 a[152:155], v224 offset:18080\n\t"
"v_cvt_pk_bf16_f32 v195, -v195, -v199\n\t"
"v_mfma_f32_16x16x32_bf16 v[184:187], a[116:119], a[168:171], v[184:187]\n\t"
"s_nop 0\n\t"
"ds_read_b128 a[168:171], v224 offset:20128\n\t"
"v_cvt_pk_bf16_f32 v217, v217, v221\n\t"
"v_mfma_f32_16x16x32_bf16 v[188:191], a[112:115], a[156:159], v[188:191]\n\t"
"s_nop 0\n\t"
"ds_write_addtid_b32 v219 offset:15520\n\t"
"v_cvt_pk_bf16_f32 v202, v202, v206\n\t"
"v_mfma_f32_16x16x32_bf16 v[188:191], a[116:119], a[172:175], v[188:191]\n\t"
"buffer_load_dwordx4 a[40:43], off, s[28:31], s63 offset:0 sc1\n\t"
"v_mfma_f32_16x16x32_bf16 v[172:175], a[104:107], a[156:159], v[172:175]\n\t"
"s_nop 0\n\t"
"ds_write_addtid_b32 v209 offset:6176\n\t"
"v_cvt_pk_bf16_f32 v210, -v210, -v214\n\t"
"v_mfma_f32_16x16x32_bf16 v[172:175], a[108:111], a[172:175], v[172:175]\n\t"
"s_nop 0\n\t"
"ds_write_addtid_b32 v202 offset:9344\n\t"
"ds_write_addtid_b32 v216 offset:10368\n\t"
"v_mfma_f32_16x16x32_bf16 v[156:159], a[96:99], a[156:159], v[156:159]\n\t"
"s_nop 0\n\t"
"ds_write_addtid_b32 v203 offset:13472\n\t"
"v_mfma_f32_16x16x32_bf16 v[156:159], a[100:103], a[172:175], v[156:159]\n\t"
"s_waitcnt vmcnt(15) lgkmcnt(8)\n\t"
"buffer_load_dwordx4 a[44:47], off, s[28:31], s63 offset:1024 sc1\n\t"
"v_mfma_f32_16x16x32_bf16 v[144:147], a[120:123], a[144:147], v[144:147]\n\t"
"s_nop 0\n\t"
"ds_write_addtid_b32 v201 offset:12448\n\t"
"ds_write_addtid_b32 v210 offset:3072\n\t"
"v_mfma_f32_16x16x32_bf16 v[144:147], a[124:127], a[160:163], v[144:147]\n\t"
"s_nop 0\n\t"
"ds_write_addtid_b32 v217 offset:14496\n\t"
"v_mfma_f32_16x16x32_bf16 v[160:163], a[128:131], a[144:147], v[160:163]\n\t"
"s_nop 0\n\t"
"ds_write_addtid_b32 v192 offset:0\n\t"
"ds_write_addtid_b32 v195 offset:5152\n\t"
"v_mfma_f32_16x16x32_bf16 v[160:163], a[132:135], a[160:163], v[160:163]\n\t"
"buffer_load_dwordx4 a[36:39], off, s[84:87], s63 offset:3072 sc1\n\t"
"v_mfma_f32_16x16x32_bf16 v[176:179], a[136:139], a[144:147], v[176:179]\n\t"
"s_nop 0\n\t"
"ds_read_b128 a[156:159], v224 offset:18336\n\t"
"v_mfma_f32_16x16x32_bf16 v[176:179], a[140:143], a[160:163], v[176:179]\n\t"
"buffer_load_dwordx4 a[32:35], off, s[84:87], s63 offset:2048 sc1\n\t"
"v_mfma_f32_16x16x32_bf16 v[180:183], a[136:139], a[148:151], v[180:183]\n\t"
"s_nop 0\n\t"
"ds_read_b128 a[172:175], v224 offset:20384\n\t"
"v_mfma_f32_16x16x32_bf16 v[180:183], a[140:143], a[164:167], v[180:183]\n\t"
"buffer_load_dwordx4 a[24:27], off, s[84:87], s63 offset:0 sc1\n\t"
"v_mfma_f32_16x16x32_bf16 v[164:167], a[128:131], a[148:151], v[164:167]\n\t"
"Lpad7_%=:\n\t"
"s_cbranch_execz Lret0_%=\n\t"
"v_mfma_f32_16x16x32_bf16 v[164:167], a[132:135], a[164:167], v[164:167]\n\t"
"buffer_load_dwordx4 a[28:31], off, s[84:87], s63 offset:1024 sc1\n\t"
"v_mfma_f32_16x16x32_bf16 v[148:151], a[120:123], a[148:151], v[148:151]\n\t"
"v_accvgpr_mov_b32 a209, a228\n\t"
"v_mfma_f32_16x16x32_bf16 v[148:151], a[124:127], a[164:167], v[148:151]\n\t"
"s_waitcnt vmcnt(13) lgkmcnt(4)\n\t"
"s_waitcnt lgkmcnt(0)\n\t"
"s_barrier\n\t"
"v_mfma_f32_16x16x32_bf16 v[152:155], a[120:123], a[152:155], v[152:155]\n\t"
"s_add_u32 m0, s26, 56192\n\t"
"ds_read_b128 a[144:147], v224 offset:0\n\t"
"ds_read_b128 a[160:163], v224 offset:2048\n\t"
"v_mfma_f32_16x16x32_bf16 v[152:155], a[124:127], a[168:171], v[152:155]\n\t"
"buffer_load_dwordx4 v236, s[12:15], s22 offen offset:128 nt lds\n\t"
"v_mfma_f32_16x16x32_bf16 v[168:171], a[128:131], a[152:155], v[168:171]\n\t"
"ds_read_b128 a[148:151], v224 offset:256\n\t"
"v_mfma_f32_16x16x32_bf16 v[168:171], a[132:135], a[168:171], v[168:171]\n\t"
"ds_read_b128 a[164:167], v224 offset:2304\n\t"
"v_mfma_f32_16x16x32_bf16 v[184:187], a[136:139], a[152:155], v[184:187]\n\t"
"s_nop 0\n\t"
"ds_read_b64_tr_b16 a[176:177], v248 offset:0\n\t"
"ds_read_b64_tr_b16 a[178:179], v248 offset:512\n\t"
"v_mfma_f32_16x16x32_bf16 v[184:187], a[140:143], a[168:171], v[184:187]\n\t"
"buffer_store_dwordx4 a[232:235], v233, s[16:19], s25 offen nt\n\t"
"v_mfma_f32_16x16x32_bf16 v[188:191], a[136:139], a[156:159], v[188:191]\n\t"
"s_nop 0\n\t"
"ds_read_b128 a[152:155], v224 offset:512\n\t"
"ds_read_b64_tr_b16 a[180:181], v249 offset:0\n\t"
"v_mfma_f32_16x16x32_bf16 v[188:191], a[140:143], a[172:175], v[188:191]\n\t"
"ds_read_b64_tr_b16 a[182:183], v249 offset:512\n\t"
"v_mfma_f32_16x16x32_bf16 v[172:175], a[128:131], a[156:159], v[172:175]\n\t"
"buffer_load_dwordx4 v237, s[12:15], s22 offen offset:1152 nt lds\n\t"
"v_mfma_f32_16x16x32_bf16 v[172:175], a[132:135], a[172:175], v[172:175]\n\t"
"s_nop 0\n\t"
"ds_read_b64_tr_b16 a[184:185], v250 offset:0\n\t"
"ds_read_b64_tr_b16 a[186:187], v250 offset:512\n\t"
"v_mfma_f32_16x16x32_bf16 v[156:159], a[120:123], a[156:159], v[156:159]\n\t"
"s_nop 0\n\t"
"ds_read_b128 a[168:171], v224 offset:2560\n\t"
"ds_read_b128 a[156:159], v224 offset:768\n\t"
"v_mfma_f32_16x16x32_bf16 v[156:159], a[124:127], a[172:175], v[156:159]\n\t"
"s_waitcnt vmcnt(9) lgkmcnt(11)\n\t"
".p2align 3\n\t"
"buffer_store_dwordx4 a[236:239], v234, s[16:19], s25 offen nt\n\t"
"v_pk_add_f32 v[144:145], v[48:49], v[144:145] neg_lo:[0,1] neg_hi:[0,1]\n\t"
"v_pk_add_f32 v[146:147], v[50:51], v[146:147] neg_lo:[0,1] neg_hi:[0,1]\n\t"
"v_pk_fma_f32 v[48:49], v[144:145], s[90:91], v[48:49] neg_lo:[1,0,0] neg_hi:[1,0,0]\n\t"
"v_pk_fma_f32 v[50:51], v[146:147], s[90:91], v[50:51] neg_lo:[1,0,0] neg_hi:[1,0,0]\n\t"
"v_pk_add_f32 v[160:161], v[64:65], v[160:161] neg_lo:[0,1] neg_hi:[0,1]\n\t"
"v_pk_add_f32 v[162:163], v[66:67], v[162:163] neg_lo:[0,1] neg_hi:[0,1]\n\t"
"v_pk_fma_f32 v[64:65], v[160:161], s[90:91], v[64:65] neg_lo:[1,0,0] neg_hi:[1,0,0]\n\t"
"v_pk_fma_f32 v[66:67], v[162:163], s[90:91], v[66:67] neg_lo:[1,0,0] neg_hi:[1,0,0]\n\t"
"v_pk_add_f32 v[176:177], v[80:81], v[176:177] neg_lo:[0,1] neg_hi:[0,1]\n\t"
"v_pk_add_f32 v[178:179], v[82:83], v[178:179] neg_lo:[0,1] neg_hi:[0,1]\n\t"
"v_pk_fma_f32 v[80:81], v[176:177], s[90:91], v[80:81] neg_lo:[1,0,0] neg_hi:[1,0,0]\n\t"
"v_pk_fma_f32 v[82:83], v[178:179], s[90:91], v[82:83] neg_lo:[1,0,0] neg_hi:[1,0,0]\n\t"
"v_pk_add_f32 v[180:181], v[84:85], v[180:181] neg_lo:[0,1] neg_hi:[0,1]\n\t"
"v_pk_add_f32 v[182:183], v[86:87], v[182:183] neg_lo:[0,1] neg_hi:[0,1]\n\t"
"v_pk_fma_f32 v[84:85], v[180:181], s[90:91], v[84:85] neg_lo:[1,0,0] neg_hi:[1,0,0]\n\t"
"v_pk_fma_f32 v[86:87], v[182:183], s[90:91], v[86:87] neg_lo:[1,0,0] neg_hi:[1,0,0]\n\t"
"v_pk_add_f32 v[164:165], v[68:69], v[164:165] neg_lo:[0,1] neg_hi:[0,1]\n\t"
"v_pk_add_f32 v[166:167], v[70:71], v[166:167] neg_lo:[0,1] neg_hi:[0,1]\n\t"
"v_pk_fma_f32 v[68:69], v[164:165], s[90:91], v[68:69] neg_lo:[1,0,0] neg_hi:[1,0,0]\n\t"
"v_pk_fma_f32 v[70:71], v[166:167], s[90:91], v[70:71] neg_lo:[1,0,0] neg_hi:[1,0,0]\n\t"
"v_pk_add_f32 v[148:149], v[52:53], v[148:149] neg_lo:[0,1] neg_hi:[0,1]\n\t"
"v_pk_add_f32 v[150:151], v[54:55], v[150:151] neg_lo:[0,1] neg_hi:[0,1]\n\t"
"v_pk_fma_f32 v[52:53], v[148:149], s[90:91], v[52:53] neg_lo:[1,0,0] neg_hi:[1,0,0]\n\t"
"v_pk_fma_f32 v[54:55], v[150:151], s[90:91], v[54:55] neg_lo:[1,0,0] neg_hi:[1,0,0]\n\t"
"v_pk_add_f32 v[152:153], v[56:57], v[152:153] neg_lo:[0,1] neg_hi:[0,1]\n\t"
"v_pk_add_f32 v[154:155], v[58:59], v[154:155] neg_lo:[0,1] neg_hi:[0,1]\n\t"
"v_pk_fma_f32 v[56:57], v[152:153], s[90:91], v[56:57] neg_lo:[1,0,0] neg_hi:[1,0,0]\n\t"
"v_pk_fma_f32 v[58:59], v[154:155], s[90:91], v[58:59] neg_lo:[1,0,0] neg_hi:[1,0,0]\n\t"
"v_pk_add_f32 v[168:169], v[72:73], v[168:169] neg_lo:[0,1] neg_hi:[0,1]\n\t"
"v_pk_add_f32 v[170:171], v[74:75], v[170:171] neg_lo:[0,1] neg_hi:[0,1]\n\t"
"v_pk_fma_f32 v[72:73], v[168:169], s[90:91], v[72:73] neg_lo:[1,0,0] neg_hi:[1,0,0]\n\t"
"v_pk_fma_f32 v[74:75], v[170:171], s[90:91], v[74:75] neg_lo:[1,0,0] neg_hi:[1,0,0]\n\t"
"v_pk_add_f32 v[184:185], v[88:89], v[184:185] neg_lo:[0,1] neg_hi:[0,1]\n\t"
"v_pk_add_f32 v[186:187], v[90:91], v[186:187] neg_lo:[0,1] neg_hi:[0,1]\n\t"
"v_pk_fma_f32 v[88:89], v[184:185], s[90:91], v[88:89] neg_lo:[1,0,0] neg_hi:[1,0,0]\n\t"
"v_pk_fma_f32 v[90:91], v[186:187], s[90:91], v[90:91] neg_lo:[1,0,0] neg_hi:[1,0,0]\n\t"
"v_pk_add_f32 v[188:189], v[92:93], v[188:189] neg_lo:[0,1] neg_hi:[0,1]\n\t"
"v_pk_add_f32 v[190:191], v[94:95], v[190:191] neg_lo:[0,1] neg_hi:[0,1]\n\t"
"v_pk_fma_f32 v[92:93], v[188:189], s[90:91], v[92:93] neg_lo:[1,0,0] neg_hi:[1,0,0]\n\t"
"v_pk_fma_f32 v[94:95], v[190:191], s[90:91], v[94:95] neg_lo:[1,0,0] neg_hi:[1,0,0]\n\t"
"v_pk_add_f32 v[172:173], v[76:77], v[172:173] neg_lo:[0,1] neg_hi:[0,1]\n\t"
"v_pk_add_f32 v[174:175], v[78:79], v[174:175] neg_lo:[0,1] neg_hi:[0,1]\n\t"
"v_pk_fma_f32 v[76:77], v[172:173], s[90:91], v[76:77] neg_lo:[1,0,0] neg_hi:[1,0,0]\n\t"
"v_pk_fma_f32 v[78:79], v[174:175], s[90:91], v[78:79] neg_lo:[1,0,0] neg_hi:[1,0,0]\n\t"
"v_pk_add_f32 v[156:157], v[60:61], v[156:157] neg_lo:[0,1] neg_hi:[0,1]\n\t"
"v_pk_add_f32 v[158:159], v[62:63], v[158:159] neg_lo:[0,1] neg_hi:[0,1]\n\t"
"v_pk_fma_f32 v[60:61], v[156:157], s[90:91], v[60:61] neg_lo:[1,0,0] neg_hi:[1,0,0]\n\t"
"v_pk_fma_f32 v[62:63], v[158:159], s[90:91], v[62:63] neg_lo:[1,0,0] neg_hi:[1,0,0]\n\t"
"v_mfma_f32_16x16x32_bf16 v[48:51], a[0:3], a[144:147], v[48:51]\n\t"
"s_nop 0\n\t"
"ds_read_b128 a[172:175], v224 offset:2816\n\t"
"ds_read_b64_tr_b16 a[188:189], v251 offset:0\n\t"
"v_mfma_f32_16x16x32_bf16 v[48:51], a[4:7], a[160:163], v[48:51]\n\t"
"s_nop 0\n\t"
"ds_read_b64_tr_b16 a[190:191], v251 offset:512\n\t"
"ds_read_b64_tr_b16 a[192:193], v248 offset:1152\n\t"
"v_mfma_f32_16x16x32_bf16 v[64:67], a[8:11], a[144:147], v[64:67]\n\t"
"ds_read_b64_tr_b16 a[194:195], v248 offset:1664\n\t"
"v_mfma_f32_16x16x32_bf16 v[64:67], a[12:15], a[160:163], v[64:67]\n\t"
"buffer_load_dwordx4 v238, s[12:15], s22 offen offset:2176 nt lds\n\t"
"v_mfma_f32_16x16x32_bf16 v[80:83], a[16:19], a[144:147], v[80:83]\n\t"
"s_nop 0\n\t"
"ds_read_b64_tr_b16 a[196:197], v249 offset:1152\n\t"
"ds_read_b64_tr_b16 a[198:199], v249 offset:1664\n\t"
"v_mfma_f32_16x16x32_bf16 v[80:83], a[20:23], a[160:163], v[80:83]\n\t"
"s_waitcnt lgkmcnt(11)\n\t"
"ds_read_b128 a[160:163], v224 offset:3072\n\t"
"v_mfma_f32_16x16x32_bf16 v[192:195], a[216:219], a[176:179], 0\n\t"
"ds_read_b64_tr_b16 a[200:201], v250 offset:1152\n\t"
"v_mfma_f32_16x16x32_bf16 v[196:199], a[216:219], a[180:183], 0\n\t"
"s_waitcnt lgkmcnt(11)\n\t"
"ds_read_b64_tr_b16 a[202:203], v250 offset:1664\n\t"
"v_mfma_f32_16x16x32_bf16 v[200:203], a[216:219], a[184:187], 0\n\t"
"s_waitcnt lgkmcnt(7)\n\t"
"ds_read_b64_tr_b16 a[204:205], v251 offset:1152\n\t"
"ds_read_b64_tr_b16 a[206:207], v251 offset:1664\n\t"
"v_mfma_f32_16x16x32_bf16 v[204:207], a[216:219], a[188:191], 0\n\t"
"s_waitcnt lgkmcnt(7)\n\t"
"buffer_load_dwordx4 v239, s[12:15], s22 offen offset:3200 nt lds\n\t"
"v_mfma_f32_16x16x32_bf16 v[208:211], a[216:219], a[192:195], 0\n\t"
"s_waitcnt lgkmcnt(5)\n\t"
"ds_read_b64_tr_b16 a[176:177], v248 offset:42624\n\t"
"ds_read_b64_tr_b16 a[178:179], v248 offset:43136\n\t"
"v_mfma_f32_16x16x32_bf16 v[212:215], a[216:219], a[196:199], 0\n\t"
"s_waitcnt lgkmcnt(4)\n\t"
"ds_read_b64_tr_b16 a[180:181], v249 offset:42624\n\t"
"v_mfma_f32_16x16x32_bf16 v[216:219], a[216:219], a[200:203], 0\n\t"
"s_waitcnt lgkmcnt(3)\n\t"
"ds_read_b64_tr_b16 a[182:183], v249 offset:43136\n\t"
"ds_read_b64_tr_b16 a[184:185], v250 offset:42624\n\t"
"v_mfma_f32_16x16x32_bf16 v[220:223], a[216:219], a[204:207], 0\n\t"
"s_mov_b64 exec, s[36:37]\n\t"
"buffer_load_dwordx4 v240, s[12:15], s22 offen offset:3712 nt lds\n\t"
"s_mov_b64 exec, -1\n\t"
"v_mfma_f32_16x16x32_bf16 v[84:87], a[16:19], a[148:151], v[84:87]\n\t"
"s_lshl_b32 m0, s73, 8\n\t"
"ds_read_b64_tr_b16 a[186:187], v250 offset:43136\n\t"
"v_cvt_pk_bf16_f32 v193, v193, v197\n\t"
"v_mfma_f32_16x16x32_bf16 v[84:87], a[20:23], a[164:167], v[84:87]\n\t"
"ds_read_b128 a[144:147], v224 offset:1024\n\t"
"v_cvt_pk_bf16_f32 v201, v201, v205\n\t"
"v_mfma_f32_16x16x32_bf16 v[68:71], a[8:11], a[148:151], v[68:71]\n\t"
"ds_read_b64_tr_b16 a[188:189], v251 offset:42624\n\t"
"v_cvt_pk_bf16_f32 v200, v200, v204\n\t"
"v_mfma_f32_16x16x32_bf16 v[68:71], a[12:15], a[164:167], v[68:71]\n\t"
"ds_read_b64_tr_b16 a[190:191], v251 offset:43136\n\t"
"v_cvt_pk_bf16_f32 v194, v194, v198\n\t"
"v_mfma_f32_16x16x32_bf16 v[52:55], a[0:3], a[148:151], v[52:55]\n\t"
"ds_read_b64_tr_b16 a[192:193], v248 offset:21888\n\t"
"v_cvt_pk_bf16_f32 v210, -v210, -v214\n\t"
"v_mfma_f32_16x16x32_bf16 v[52:55], a[4:7], a[164:167], v[52:55]\n\t"
"ds_read_b128 a[148:151], v224 offset:1280\n\t"
"v_cvt_pk_bf16_f32 v211, -v211, -v215\n\t"
"v_mfma_f32_16x16x32_bf16 v[56:59], a[0:3], a[152:155], v[56:59]\n\t"
"ds_read_b128 a[164:167], v224 offset:3328\n\t"
"v_cvt_pk_bf16_f32 v195, v195, v199\n\t"
"v_mfma_f32_16x16x32_bf16 v[56:59], a[4:7], a[168:171], v[56:59]\n\t"
"ds_write_addtid_b32 v195 offset:28992\n\t"
"v_cvt_pk_bf16_f32 v209, -v209, -v213\n\t"
"v_mfma_f32_16x16x32_bf16 v[72:75], a[8:11], a[152:155], v[72:75]\n\t"
"ds_write_addtid_b32 v211 offset:30016\n\t"
"v_cvt_pk_bf16_f32 v217, -v217, -v221\n\t"
"v_mfma_f32_16x16x32_bf16 v[72:75], a[12:15], a[168:171], v[72:75]\n\t"
"s_nop 0\n\t"
"ds_write_addtid_b32 v193 offset:24864\n\t"
"ds_write_addtid_b32 v210 offset:21696\n\t"
"v_mfma_f32_16x16x32_bf16 v[88:91], a[16:19], a[152:155], v[88:91]\n\t"
"ds_read_b128 a[152:155], v224 offset:1536\n\t"
"v_cvt_pk_bf16_f32 v208, -v208, -v212\n\t"
"v_mfma_f32_16x16x32_bf16 v[88:91], a[20:23], a[168:171], v[88:91]\n\t"
"ds_read_b128 a[168:171], v224 offset:3584\n\t"
"v_cvt_pk_bf16_f32 v202, v202, v206\n\t"
"v_mfma_f32_16x16x32_bf16 v[92:95], a[16:19], a[156:159], v[92:95]\n\t"
"ds_write_addtid_b32 v217 offset:27936\n\t"
"v_cvt_pk_bf16_f32 v218, -v218, -v222\n\t"
"v_mfma_f32_16x16x32_bf16 v[92:95], a[20:23], a[172:175], v[92:95]\n\t"
"ds_write_addtid_b32 v202 offset:22720\n\t"
"v_cvt_pk_bf16_f32 v219, -v219, -v223\n\t"
"v_mfma_f32_16x16x32_bf16 v[76:79], a[8:11], a[156:159], v[76:79]\n\t"
"s_nop 0\n\t"
"ds_write_addtid_b32 v200 offset:18592\n\t"
"ds_write_addtid_b32 v218 offset:23744\n\t"
"v_mfma_f32_16x16x32_bf16 v[76:79], a[12:15], a[172:175], v[76:79]\n\t"
"s_nop 0\n\t"
"ds_write_addtid_b32 v208 offset:17568\n\t"
"ds_write_addtid_b32 v209 offset:25888\n\t"
"v_mfma_f32_16x16x32_bf16 v[60:63], a[0:3], a[156:159], v[60:63]\n\t"
"ds_read_b128 a[156:159], v224 offset:1792\n\t"
"v_cvt_pk_bf16_f32 v216, -v216, -v220\n\t"
"v_mfma_f32_16x16x32_bf16 v[60:63], a[4:7], a[172:175], v[60:63]\n\t"
"s_waitcnt vmcnt(7) lgkmcnt(12)\n\t"
"ds_write_addtid_b32 v219 offset:32064\n\t"
"v_cvt_pk_bf16_f32 v203, v203, v207\n\t"
"v_mfma_f32_16x16x32_bf16 v[144:147], a[24:27], a[144:147], v[144:147]\n\t"
"s_nop 0\n\t"
"ds_write_addtid_b32 v201 offset:26912\n\t"
"ds_write_addtid_b32 v216 offset:19616\n\t"
"v_mfma_f32_16x16x32_bf16 v[144:147], a[28:31], a[160:163], v[144:147]\n\t"
"ds_write_addtid_b32 v203 offset:31040\n\t"
"v_cvt_pk_bf16_f32 v192, v192, v196\n\t"
"v_mfma_f32_16x16x32_bf16 v[160:163], a[32:35], a[144:147], v[160:163]\n\t"
"s_nop 0\n\t"
"ds_write_addtid_b32 v192 offset:16544\n\t"
"ds_write_addtid_b32 v194 offset:20672\n\t"
"v_mfma_f32_16x16x32_bf16 v[160:163], a[36:39], a[160:163], v[160:163]\n\t"
"ds_read_b128 a[172:175], v224 offset:3840\n\t"
"v_mfma_f32_16x16x32_bf16 v[176:179], a[40:43], a[144:147], v[176:179]\n\t"
"ds_read_b64_tr_b16 a[194:195], v248 offset:22400\n\t"
"v_mfma_f32_16x16x32_bf16 v[176:179], a[44:47], a[160:163], v[176:179]\n\t"
"ds_read_b64_tr_b16 a[196:197], v249 offset:21888\n\t"
"v_mfma_f32_16x16x32_bf16 v[180:183], a[40:43], a[148:151], v[180:183]\n\t"
"ds_read_b64_tr_b16 a[198:199], v249 offset:22400\n\t"
"v_mfma_f32_16x16x32_bf16 v[180:183], a[44:47], a[164:167], v[180:183]\n\t"
"s_nop 0\n\t"
"ds_read_b64_tr_b16 a[200:201], v250 offset:21888\n\t"
"ds_read_b64_tr_b16 a[202:203], v250 offset:22400\n\t"
"v_mfma_f32_16x16x32_bf16 v[164:167], a[32:35], a[148:151], v[164:167]\n\t"
"s_nop 0\n\t"
"ds_read_b64_tr_b16 a[204:205], v251 offset:21888\n\t"
"ds_read_b64_tr_b16 a[206:207], v251 offset:22400\n\t"
"v_mfma_f32_16x16x32_bf16 v[164:167], a[36:39], a[164:167], v[164:167]\n\t"
"v_mfma_f32_16x16x32_bf16 v[148:151], a[24:27], a[148:151], v[148:151]\n\t"
"s_waitcnt lgkmcnt(7)\n\t"
"v_mfma_f32_16x16x32_bf16 v[148:151], a[28:31], a[164:167], v[148:151]\n\t"
"s_barrier\n\t"
"v_mfma_f32_16x16x32_bf16 v[152:155], a[24:27], a[152:155], v[152:155]\n\t"
"ds_read_b128 a[148:151], v224 offset:16800\n\t"
"v_add_u32 v248, 33280, v226\n\t"
"v_mfma_f32_16x16x32_bf16 v[152:155], a[28:31], a[168:171], v[152:155]\n\t"
"ds_read_b128 a[144:147], v224 offset:16544\n\t"
"v_xor_b32 v249, 32, v248\n\t"
"v_mfma_f32_16x16x32_bf16 v[168:171], a[32:35], a[152:155], v[168:171]\n\t"
"ds_read_b128 a[160:163], v224 offset:18592\n\t"
"v_mfma_f32_16x16x32_bf16 v[168:171], a[36:39], a[168:171], v[168:171]\n\t"
"ds_read_b128 a[164:167], v224 offset:18848\n\t"
"v_mfma_f32_16x16x32_bf16 v[184:187], a[40:43], a[152:155], v[184:187]\n\t"
"v_mfma_f32_16x16x32_bf16 v[184:187], a[44:47], a[168:171], v[184:187]\n\t"
"v_mfma_f32_16x16x32_bf16 v[188:191], a[40:43], a[156:159], v[188:191]\n\t"
"ds_read_b128 a[152:155], v224 offset:17056\n\t"
"v_mfma_f32_16x16x32_bf16 v[188:191], a[44:47], a[172:175], v[188:191]\n\t"
"ds_read_b128 a[168:171], v224 offset:19104\n\t"
"v_xor_b32 v250, 64, v248\n\t"
"v_mfma_f32_16x16x32_bf16 v[172:175], a[32:35], a[156:159], v[172:175]\n\t"
"v_mfma_f32_16x16x32_bf16 v[172:175], a[36:39], a[172:175], v[172:175]\n\t"
"v_mfma_f32_16x16x32_bf16 v[156:159], a[24:27], a[156:159], v[156:159]\n\t"
"ds_read_b128 a[156:159], v224 offset:17312\n\t"
"v_xor_b32 v251, 96, v248\n\t"
"v_mfma_f32_16x16x32_bf16 v[156:159], a[28:31], a[172:175], v[156:159]\n\t"
"ds_read_b128 a[172:175], v224 offset:19360\n\t"
"v_mfma_f32_16x16x32_bf16 v[192:195], a[216:219], a[176:179], 0\n\t"
"ds_read_b64_tr_b16 a[178:179], v248 offset:128\n\t"
"v_mfma_f32_16x16x32_bf16 v[196:199], a[216:219], a[180:183], 0\n\t"
"v_mfma_f32_16x16x32_bf16 v[200:203], a[216:219], a[184:187], 0\n\t"
"ds_read_b64_tr_b16 a[176:177], v248 offset:0\n\t"
"v_mfma_f32_16x16x32_bf16 v[204:207], a[216:219], a[188:191], 0\n\t"
"s_waitcnt lgkmcnt(6)\n\t"
"ds_read_b64_tr_b16 a[180:181], v249 offset:256\n\t"
"v_mfma_f32_16x16x32_bf16 v[208:211], a[216:219], a[192:195], 0\n\t"
"ds_read_b64_tr_b16 a[186:187], v249 offset:128\n\t"
"v_mfma_f32_16x16x32_bf16 v[212:215], a[216:219], a[196:199], 0\n\t"
"v_mfma_f32_16x16x32_bf16 v[216:219], a[216:219], a[200:203], 0\n\t"
"ds_read_b64_tr_b16 a[184:185], v249 offset:0\n\t"
"v_mfma_f32_16x16x32_bf16 v[220:223], a[216:219], a[204:207], 0\n\t"
"ds_read_b64_tr_b16 a[188:189], v248 offset:256\n\t"
"v_mfma_f32_16x16x32_bf16 v[0:3], a[48:51], a[144:147], v[0:3]\n\t"
"ds_read_b64_tr_b16 a[194:195], v250 offset:128\n\t"
"v_mfma_f32_16x16x32_bf16 v[0:3], a[52:55], a[160:163], v[0:3]\n\t"
"v_cvt_pk_bf16_f32 v195, -v195, -v199\n\t"
"v_cvt_pk_bf16_f32 v201, -v201, -v205\n\t"
"v_mfma_f32_16x16x32_bf16 v[16:19], a[56:59], a[144:147], v[16:19]\n\t"
"ds_read_b64_tr_b16 a[192:193], v250 offset:0\n\t"
"v_mfma_f32_16x16x32_bf16 v[16:19], a[60:63], a[160:163], v[16:19]\n\t"
"ds_read_b64_tr_b16 a[196:197], v251 offset:256\n\t"
"v_cvt_pk_bf16_f32 v200, -v200, -v204\n\t"
"v_mfma_f32_16x16x32_bf16 v[32:35], a[64:67], a[144:147], v[32:35]\n\t"
"ds_read_b128 a[144:147], v224 offset:17568\n\t"
"v_cvt_pk_bf16_f32 v209, -v209, -v213\n\t"
"v_mfma_f32_16x16x32_bf16 v[32:35], a[68:71], a[160:163], v[32:35]\n\t"
"v_cvt_pk_bf16_f32 v203, -v203, -v207\n\t"
"v_cvt_pk_bf16_f32 v217, -v217, -v221\n\t"
"v_mfma_f32_16x16x32_bf16 v[36:39], a[64:67], a[148:151], v[36:39]\n\t"
"ds_read_b128 a[160:163], v224 offset:19616\n\t"
"v_cvt_pk_bf16_f32 v219, -v219, -v223\n\t"
"v_mfma_f32_16x16x32_bf16 v[36:39], a[68:71], a[164:167], v[36:39]\n\t"
"ds_read_b64_tr_b16 a[202:203], v251 offset:128\n\t"
"v_cvt_pk_bf16_f32 v208, -v208, -v212\n\t"
"v_mfma_f32_16x16x32_bf16 v[20:23], a[56:59], a[148:151], v[20:23]\n\t"
"ds_write_addtid_b32 v203 offset:14496\n\t"
"v_cvt_pk_bf16_f32 v194, -v194, -v198\n\t"
"v_mfma_f32_16x16x32_bf16 v[20:23], a[60:63], a[164:167], v[20:23]\n\t"
"ds_write_addtid_b32 v200 offset:2048\n\t"
"v_cvt_pk_bf16_f32 v211, -v211, -v215\n\t"
"v_mfma_f32_16x16x32_bf16 v[4:7], a[48:51], a[148:151], v[4:7]\n\t"
"ds_read_b128 a[148:151], v224 offset:17824\n\t"
"v_mfma_f32_16x16x32_bf16 v[4:7], a[52:55], a[164:167], v[4:7]\n\t"
"s_waitcnt lgkmcnt(8)\n\t"
"ds_write_addtid_b32 v201 offset:10368\n\t"
"v_cvt_pk_bf16_f32 v218, -v218, -v222\n\t"
"v_mfma_f32_16x16x32_bf16 v[8:11], a[48:51], a[152:155], v[8:11]\n\t"
"ds_read_b128 a[164:167], v224 offset:19872\n\t"
"v_cvt_pk_bf16_f32 v210, -v210, -v214\n\t"
"v_mfma_f32_16x16x32_bf16 v[8:11], a[52:55], a[168:171], v[8:11]\n\t"
"ds_write_addtid_b32 v194 offset:4128\n\t"
"v_cvt_pk_bf16_f32 v202, -v202, -v206\n\t"
"v_mfma_f32_16x16x32_bf16 v[24:27], a[56:59], a[152:155], v[24:27]\n\t"
"ds_write_addtid_b32 v209 offset:9344\n\t"
"v_mfma_f32_16x16x32_bf16 v[24:27], a[60:63], a[168:171], v[24:27]\n\t"
"ds_write_addtid_b32 v202 offset:6176\n\t"
"v_mfma_f32_16x16x32_bf16 v[40:43], a[64:67], a[152:155], v[40:43]\n\t"
"ds_read_b128 a[152:155], v224 offset:18080\n\t"
"v_cvt_pk_bf16_f32 v193, -v193, -v197\n\t"
"v_mfma_f32_16x16x32_bf16 v[40:43], a[68:71], a[168:171], v[40:43]\n\t"
"ds_read_b128 a[168:171], v224 offset:20128\n\t"
"v_mfma_f32_16x16x32_bf16 v[44:47], a[64:67], a[156:159], v[44:47]\n\t"
"ds_write_addtid_b32 v210 offset:5152\n\t"
"v_cvt_pk_bf16_f32 v192, -v192, -v196\n\t"
"v_mfma_f32_16x16x32_bf16 v[44:47], a[68:71], a[172:175], v[44:47]\n\t"
"buffer_load_dwordx4 a[64:67], off, s[28:31], s42 offset:0 sc1\n\t"
"v_mfma_f32_16x16x32_bf16 v[28:31], a[56:59], a[156:159], v[28:31]\n\t"
"ds_write_addtid_b32 v217 offset:11392\n\t"
"v_mfma_f32_16x16x32_bf16 v[28:31], a[60:63], a[172:175], v[28:31]\n\t"
"s_xor_b32 s24, s24, 384\n\t"
"ds_write_addtid_b32 v195 offset:12448\n\t"
"ds_write_addtid_b32 v208 offset:1024\n\t"
"v_mfma_f32_16x16x32_bf16 v[12:15], a[48:51], a[156:159], v[12:15]\n\t"
"ds_write_addtid_b32 v219 offset:15520\n\t"
"v_mfma_f32_16x16x32_bf16 v[12:15], a[52:55], a[172:175], v[12:15]\n\t"
"s_waitcnt lgkmcnt(9)\n\t"
"buffer_load_dwordx4 a[68:71], off, s[28:31], s42 offset:1024 sc1\n\t"
"v_mfma_f32_16x16x32_bf16 v[96:99], a[72:75], a[144:147], v[0:3]\n\t"
"s_xor_b32 s20, s20, 384\n\t"
"ds_write_addtid_b32 v193 offset:8320\n\t"
"ds_write_addtid_b32 v211 offset:13472\n\t"
"v_mfma_f32_16x16x32_bf16 v[96:99], a[76:79], a[160:163], v[96:99]\n\t"
"ds_write_addtid_b32 v218 offset:7200\n\t"
"v_cvt_pk_bf16_f32 v216, -v216, -v220\n\t"
"v_mfma_f32_16x16x32_bf16 v[112:115], a[80:83], a[144:147], v[16:19]\n\t"
"s_xor_b32 s27, s27, 384\n\t"
"ds_write_addtid_b32 v192 offset:0\n\t"
"ds_write_addtid_b32 v216 offset:3072\n\t"
"v_mfma_f32_16x16x32_bf16 v[112:115], a[84:87], a[160:163], v[112:115]\n\t"
"buffer_load_dwordx4 a[60:63], off, s[84:87], s42 offset:3072 sc1\n\t"
"v_mfma_f32_16x16x32_bf16 v[128:131], a[88:91], a[144:147], v[32:35]\n\t"
"ds_read_b128 a[156:159], v224 offset:18336\n\t"
"v_mfma_f32_16x16x32_bf16 v[128:131], a[92:95], a[160:163], v[128:131]\n\t"
"buffer_load_dwordx4 a[56:59], off, s[84:87], s42 offset:2048 sc1\n\t"
"v_mfma_f32_16x16x32_bf16 v[132:135], a[88:91], a[148:151], v[36:39]\n\t"
"ds_read_b128 a[172:175], v224 offset:20384\n\t"
"v_mfma_f32_16x16x32_bf16 v[132:135], a[92:95], a[164:167], v[132:135]\n\t"
"buffer_load_dwordx4 a[48:51], off, s[84:87], s42 offset:0 sc1\n\t"
"v_mfma_f32_16x16x32_bf16 v[116:119], a[80:83], a[148:151], v[20:23]\n\t"
"s_xor_b32 s25, s25, 384\n\t"
"ds_read_b64_tr_b16 a[200:201], v251 offset:0\n\t"
"ds_read_b64_tr_b16 a[204:205], v250 offset:256\n\t"
"v_mfma_f32_16x16x32_bf16 v[116:119], a[84:87], a[164:167], v[116:119]\n\t"
"buffer_load_dwordx4 a[52:55], off, s[84:87], s42 offset:1024 sc1\n\t"
"v_mfma_f32_16x16x32_bf16 v[100:103], a[72:75], a[148:151], v[4:7]\n\t"
"v_mfma_f32_16x16x32_bf16 v[100:103], a[76:79], a[164:167], v[100:103]\n\t"
"s_waitcnt lgkmcnt(0)\n\t"
"s_barrier\n\t"
"v_mfma_f32_16x16x32_bf16 v[104:107], a[72:75], a[152:155], v[8:11]\n\t"
"ds_read_b128 a[160:163], v224 offset:2048\n\t"
"v_mfma_f32_16x16x32_bf16 v[104:107], a[76:79], a[168:171], v[104:107]\n\t"
"ds_read_b128 a[144:147], v224 offset:0\n\t"
"v_mfma_f32_16x16x32_bf16 v[120:123], a[80:83], a[152:155], v[24:27]\n\t"
"ds_read_b128 a[148:151], v224 offset:256\n\t"
"v_mfma_f32_16x16x32_bf16 v[120:123], a[84:87], a[168:171], v[120:123]\n\t"
"ds_read_b128 a[164:167], v224 offset:2304\n\t"
"v_mfma_f32_16x16x32_bf16 v[136:139], a[88:91], a[152:155], v[40:43]\n\t"
"v_mfma_f32_16x16x32_bf16 v[136:139], a[92:95], a[168:171], v[136:139]\n\t"
"ds_write_b128 v254, a[240:243] offset:0\n\t"
"v_mfma_f32_16x16x32_bf16 v[140:143], a[88:91], a[156:159], v[44:47]\n\t"
"v_mfma_f32_16x16x32_bf16 v[140:143], a[92:95], a[172:175], v[140:143]\n\t"
"buffer_load_dwordx4 a[88:91], off, s[28:31], s48 offset:0 sc1\n\t"
"v_mfma_f32_16x16x32_bf16 v[124:127], a[80:83], a[156:159], v[28:31]\n\t"
"v_mfma_f32_16x16x32_bf16 v[124:127], a[84:87], a[172:175], v[124:127]\n\t"
"buffer_load_dwordx4 a[92:95], off, s[28:31], s48 offset:1024 sc1\n\t"
"v_mfma_f32_16x16x32_bf16 v[108:111], a[72:75], a[156:159], v[12:15]\n\t"
"ds_read_b128 a[152:155], v224 offset:512\n\t"
"v_mfma_f32_16x16x32_bf16 v[108:111], a[76:79], a[172:175], v[108:111]\n\t"
"buffer_load_dwordx4 a[84:87], off, s[84:87], s48 offset:3072 sc1\n\t"
"v_mfma_f32_4x4x4_16b_bf16 v[200:203], a[220:221], a[178:179], 0\n\t"
"ds_read_b128 a[168:171], v224 offset:2560\n\t"
"v_mfma_f32_4x4x4_16b_bf16 v[204:207], a[220:221], a[186:187], 0\n\t"
"ds_read_b64_tr_b16 a[178:179], v248 offset:41600\n\t"
"v_mfma_f32_4x4x4_16b_bf16 v[216:219], a[220:221], a[194:195], 0\n\t"
"ds_read_b128 a[156:159], v224 offset:768\n\t"
"v_mfma_f32_4x4x4_16b_bf16 v[220:223], a[220:221], a[202:203], 0\n\t"
"v_mfma_f32_4x4x4_16b_bf16 v[192:195], a[222:223], a[176:177], v[200:203]\n\t"
"ds_read_b128 a[172:175], v224 offset:2816\n\t"
"v_mfma_f32_4x4x4_16b_bf16 v[196:199], a[222:223], a[184:185], v[204:207]\n\t"
"ds_read_b64_tr_b16 a[186:187], v249 offset:41600\n\t"
"v_mfma_f32_4x4x4_16b_bf16 v[200:203], a[222:223], a[180:181], v[200:203]\n\t"
"ds_read_b64_tr_b16 a[194:195], v250 offset:41600\n\t"
"v_mfma_f32_4x4x4_16b_bf16 v[204:207], a[222:223], a[188:189], v[204:207]\n\t"
"ds_read_b64_tr_b16 a[202:203], v251 offset:41600\n\t"
"v_mfma_f32_4x4x4_16b_bf16 v[208:211], a[222:223], a[192:193], v[216:219]\n\t"
"ds_read_b64_tr_b16 a[176:177], v248 offset:41472\n\t"
"v_mfma_f32_4x4x4_16b_bf16 v[212:215], a[222:223], a[200:201], v[220:223]\n\t"
"ds_read_b64_tr_b16 a[184:185], v249 offset:41472\n\t"
"v_mfma_f32_4x4x4_16b_bf16 v[216:219], a[222:223], a[196:197], v[216:219]\n\t"
"ds_read_b64_tr_b16 a[180:181], v249 offset:41728\n\t"
"v_mfma_f32_4x4x4_16b_bf16 v[220:223], a[222:223], a[204:205], v[220:223]\n\t"
"s_waitcnt lgkmcnt(8)\n\t"
"buffer_load_dwordx4 a[80:83], off, s[84:87], s48 offset:2048 sc1\n\t"
"v_mfma_f32_16x16x32_bf16 v[96:99], a[96:99], a[144:147], v[96:99]\n\t"
"ds_read_b64_tr_b16 a[188:189], v248 offset:41728\n\t"
"v_cvt_pk_bf16_f32 v195, -v195, -v199\n\t"
"v_mfma_f32_16x16x32_bf16 v[96:99], a[100:103], a[160:163], v[96:99]\n\t"
"ds_read_b64_tr_b16 a[192:193], v250 offset:41472\n\t"
"v_cvt_pk_bf16_f32 v193, -v193, -v197\n\t"
"v_mfma_f32_16x16x32_bf16 v[112:115], a[104:107], a[144:147], v[112:115]\n\t"
"ds_read_b64_tr_b16 a[200:201], v251 offset:41472\n\t"
"v_cvt_pk_bf16_f32 v201, v201, v205\n\t"
"v_mfma_f32_16x16x32_bf16 v[112:115], a[108:111], a[160:163], v[112:115]\n\t"
"buffer_load_dwordx4 a[72:75], off, s[84:87], s48 offset:0 sc1\n\t"
"v_mfma_f32_16x16x32_bf16 v[128:131], a[112:115], a[144:147], v[128:131]\n\t"
"ds_read_b128 a[144:147], v224 offset:1024\n\t"
"v_cvt_pk_bf16_f32 v200, v200, v204\n\t"
"v_mfma_f32_16x16x32_bf16 v[128:131], a[116:119], a[160:163], v[128:131]\n\t"
"ds_read_b64_tr_b16 a[196:197], v251 offset:41728\n\t"
"v_cvt_pk_bf16_f32 v194, -v194, -v198\n\t"
"v_mfma_f32_16x16x32_bf16 v[132:135], a[112:115], a[148:151], v[132:135]\n\t"
"s_nop 0\n\t"
"ds_read_b128 a[160:163], v224 offset:3072\n\t"
"v_cvt_pk_bf16_f32 v217, v217, v221\n\t"
"v_mfma_f32_16x16x32_bf16 v[132:135], a[116:119], a[164:167], v[132:135]\n\t"
"buffer_load_dwordx4 a[76:79], off, s[84:87], s48 offset:1024 sc1\n\t"
"v_mfma_f32_16x16x32_bf16 v[116:119], a[104:107], a[148:151], v[116:119]\n\t"
"s_nop 0\n\t"
"ds_write_addtid_b32 v193 offset:20672\n\t"
"v_cvt_pk_bf16_f32 v219, v219, v223\n\t"
"v_mfma_f32_16x16x32_bf16 v[116:119], a[108:111], a[164:167], v[116:119]\n\t"
"s_nop 0\n\t"
"ds_write_addtid_b32 v194 offset:17568\n\t"
"v_cvt_pk_bf16_f32 v210, -v210, -v214\n\t"
"v_mfma_f32_16x16x32_bf16 v[100:103], a[96:99], a[148:151], v[100:103]\n\t"
"s_nop 0\n\t"
"ds_read_b128 a[148:151], v224 offset:1280\n\t"
"v_cvt_pk_bf16_f32 v211, -v211, -v215\n\t"
"v_mfma_f32_16x16x32_bf16 v[100:103], a[100:103], a[164:167], v[100:103]\n\t"
"s_nop 0\n\t"
"ds_write_addtid_b32 v201 offset:28992\n\t"
"v_cvt_pk_bf16_f32 v218, v218, v222\n\t"
"v_mfma_f32_16x16x32_bf16 v[104:107], a[96:99], a[152:155], v[104:107]\n\t"
"s_nop 0\n\t"
"ds_read_b128 a[164:167], v224 offset:3328\n\t"
"v_cvt_pk_bf16_f32 v203, v203, v207\n\t"
"v_mfma_f32_16x16x32_bf16 v[104:107], a[100:103], a[168:171], v[104:107]\n\t"
"s_nop 0\n\t"
"ds_write_addtid_b32 v218 offset:27936\n\t"
"v_mfma_f32_16x16x32_bf16 v[120:123], a[104:107], a[152:155], v[120:123]\n\t"
"s_nop 0\n\t"
"ds_write_addtid_b32 v217 offset:31040\n\t"
"v_cvt_pk_bf16_f32 v208, -v208, -v212\n\t"
"v_mfma_f32_16x16x32_bf16 v[120:123], a[108:111], a[168:171], v[120:123]\n\t"
"s_nop 0\n\t"
"ds_write_addtid_b32 v208 offset:18592\n\t"
"v_mfma_f32_16x16x32_bf16 v[136:139], a[112:115], a[152:155], v[136:139]\n\t"
"s_nop 0\n\t"
"ds_read_b128 a[152:155], v224 offset:1536\n\t"
"v_mfma_f32_16x16x32_bf16 v[136:139], a[116:119], a[168:171], v[136:139]\n\t"
"s_waitcnt lgkmcnt(9)\n\t"
"ds_read_b128 a[168:171], v224 offset:3584\n\t"
"v_cvt_pk_bf16_f32 v216, v216, v220\n\t"
"v_mfma_f32_16x16x32_bf16 v[140:143], a[112:115], a[156:159], v[140:143]\n\t"
"s_nop 0\n\t"
"ds_write_addtid_b32 v210 offset:19616\n\t"
"v_cvt_pk_bf16_f32 v209, -v209, -v213\n\t"
"v_mfma_f32_16x16x32_bf16 v[140:143], a[116:119], a[172:175], v[140:143]\n\t"
"buffer_load_dwordx4 a[112:115], off, s[28:31], s75 offset:0 sc1\n\t"
"v_mfma_f32_16x16x32_bf16 v[124:127], a[104:107], a[156:159], v[124:127]\n\t"
"s_nop 0\n\t"
"ds_write_addtid_b32 v203 offset:30016\n\t"
"v_cvt_pk_bf16_f32 v202, v202, v206\n\t"
"v_mfma_f32_16x16x32_bf16 v[124:127], a[108:111], a[172:175], v[124:127]\n\t"
"s_nop 0\n\t"
"ds_write_addtid_b32 v202 offset:25888\n\t"
"ds_write_addtid_b32 v216 offset:26912\n\t"
"v_mfma_f32_16x16x32_bf16 v[108:111], a[96:99], a[156:159], v[108:111]\n\t"
"s_nop 0\n\t"
"ds_write_addtid_b32 v219 offset:32064\n\t"
"v_cvt_pk_bf16_f32 v192, -v192, -v196\n\t"
"v_mfma_f32_16x16x32_bf16 v[108:111], a[100:103], a[172:175], v[108:111]\n\t"
"buffer_load_dwordx4 a[116:119], off, s[28:31], s75 offset:1024 sc1\n\t"
"v_mfma_f32_16x16x32_bf16 v[96:99], a[120:123], a[144:147], v[96:99]\n\t"
"s_nop 0\n\t"
"ds_write_addtid_b32 v209 offset:22720\n\t"
"ds_write_addtid_b32 v211 offset:23744\n\t"
"v_mfma_f32_16x16x32_bf16 v[96:99], a[124:127], a[160:163], v[96:99]\n\t"
"s_nop 0\n\t"
"ds_write_addtid_b32 v195 offset:21696\n\t"
"v_mfma_f32_16x16x32_bf16 v[112:115], a[128:131], a[144:147], v[112:115]\n\t"
"s_nop 0\n\t"
"ds_write_addtid_b32 v192 offset:16544\n\t"
"ds_write_addtid_b32 v200 offset:24864\n\t"
"v_mfma_f32_16x16x32_bf16 v[112:115], a[132:135], a[160:163], v[112:115]\n\t"
"buffer_load_dwordx4 a[108:111], off, s[84:87], s75 offset:3072 sc1\n\t"
"v_mfma_f32_16x16x32_bf16 v[128:131], a[136:139], a[144:147], v[128:131]\n\t"
"s_nop 0\n\t"
"ds_read_b128 a[172:175], v224 offset:3840\n\t"
"v_mfma_f32_16x16x32_bf16 v[128:131], a[140:143], a[160:163], v[128:131]\n\t"
"s_waitcnt lgkmcnt(10)\n\t"
"buffer_load_dwordx4 a[104:107], off, s[84:87], s75 offset:2048 sc1\n\t"
"v_mfma_f32_16x16x32_bf16 v[132:135], a[136:139], a[148:151], v[132:135]\n\t"
"s_nop 0\n\t"
"ds_read_b128 a[156:159], v224 offset:1792\n\t"
"v_mfma_f32_16x16x32_bf16 v[132:135], a[140:143], a[164:167], v[132:135]\n\t"
"buffer_load_dwordx4 a[96:99], off, s[84:87], s75 offset:0 sc1\n\t"
"v_mfma_f32_16x16x32_bf16 v[116:119], a[128:131], a[148:151], v[116:119]\n\t"
"s_nop 0\n\t"
"ds_read_b64_tr_b16 a[204:205], v250 offset:41728\n\t"
"v_mfma_f32_16x16x32_bf16 v[116:119], a[132:135], a[164:167], v[116:119]\n\t"
"buffer_load_dwordx4 a[100:103], off, s[84:87], s75 offset:1024 sc1\n\t"
"v_mfma_f32_16x16x32_bf16 v[100:103], a[120:123], a[148:151], v[100:103]\n\t"
"s_nop 0\n\t"
"v_mfma_f32_16x16x32_bf16 v[100:103], a[124:127], a[164:167], v[100:103]\n\t"
"s_waitcnt lgkmcnt(0)\n\t"
"s_barrier\n\t"
"v_mfma_f32_16x16x32_bf16 v[104:107], a[120:123], a[152:155], v[104:107]\n\t"
"s_nop 0\n\t"
"ds_read_b128 a[144:147], v224 offset:16544\n\t"
"v_add_u32 v248, 101632, v226\n\t"
"v_mfma_f32_16x16x32_bf16 v[104:107], a[124:127], a[168:171], v[104:107]\n\t"
"s_nop 0\n\t"
"ds_read_b128 a[148:151], v224 offset:16800\n\t"
"v_mfma_f32_16x16x32_bf16 v[120:123], a[128:131], a[152:155], v[120:123]\n\t"
"s_nop 0\n\t"
"ds_read_b128 a[160:163], v224 offset:18592\n\t"
"v_xor_b32 v249, 32, v248\n\t"
"v_mfma_f32_16x16x32_bf16 v[120:123], a[132:135], a[168:171], v[120:123]\n\t"
"s_nop 0\n\t"
"ds_read_b128 a[164:167], v224 offset:18848\n\t"
"v_mfma_f32_16x16x32_bf16 v[136:139], a[136:139], a[152:155], v[136:139]\n\t"
"s_nop 0\n\t"
"v_xor_b32 v250, 64, v248\n\t"
"v_mfma_f32_16x16x32_bf16 v[136:139], a[140:143], a[168:171], v[136:139]\n\t"
"s_nop 0\n\t"
"v_mfma_f32_16x16x32_bf16 v[140:143], a[136:139], a[156:159], v[140:143]\n\t"
"s_nop 0\n\t"
"ds_read_b128 a[152:155], v224 offset:17056\n\t"
"v_mfma_f32_16x16x32_bf16 v[140:143], a[140:143], a[172:175], v[140:143]\n\t"
"buffer_load_dwordx4 a[136:139], off, s[28:31], s78 offset:0 sc1\n\t"
"v_mfma_f32_16x16x32_bf16 v[124:127], a[128:131], a[156:159], v[124:127]\n\t"
"s_nop 0\n\t"
"ds_read_b128 a[168:171], v224 offset:19104\n\t"
"v_xor_b32 v251, 96, v248\n\t"
"v_mfma_f32_16x16x32_bf16 v[124:127], a[132:135], a[172:175], v[124:127]\n\t"
"Lpad8_%=:\n\t"
"s_cbranch_execz Lpad12_%=\n\t"
"v_mfma_f32_16x16x32_bf16 v[108:111], a[120:123], a[156:159], v[108:111]\n\t"
"ds_read_b128 a[156:159], v224 offset:17312\n\t"
"v_mfma_f32_16x16x32_bf16 v[108:111], a[124:127], a[172:175], v[108:111]\n\t"
"buffer_load_dwordx4 a[140:143], off, s[28:31], s78 offset:1024 sc1\n\t"
"v_mfma_f32_4x4x4_16b_bf16 v[200:203], a[220:221], a[178:179], 0\n\t"
"ds_read_b64_tr_b16 a[178:179], v248 offset:20864\n\t"
"v_mfma_f32_4x4x4_16b_bf16 v[204:207], a[220:221], a[186:187], 0\n\t"
"v_mfma_f32_4x4x4_16b_bf16 v[216:219], a[220:221], a[194:195], 0\n\t"
"ds_read_b64_tr_b16 a[186:187], v249 offset:20864\n\t"
"v_mfma_f32_4x4x4_16b_bf16 v[220:223], a[220:221], a[202:203], 0\n\t"
".p2align 3\n\t"
"ds_read_b128 a[172:175], v224 offset:19360\n\t"
"v_pk_fma_f32 v[0:1], v[96:97], s[90:91], v[0:1] neg_lo:[1,0,0] neg_hi:[1,0,0]\n\t"
"v_pk_fma_f32 v[2:3], v[98:99], s[90:91], v[2:3] neg_lo:[1,0,0] neg_hi:[1,0,0]\n\t"
"v_pk_fma_f32 v[16:17], v[112:113], s[90:91], v[16:17] neg_lo:[1,0,0] neg_hi:[1,0,0]\n\t"
"v_pk_fma_f32 v[18:19], v[114:115], s[90:91], v[18:19] neg_lo:[1,0,0] neg_hi:[1,0,0]\n\t"
"v_pk_fma_f32 v[32:33], v[128:129], s[90:91], v[32:33] neg_lo:[1,0,0] neg_hi:[1,0,0]\n\t"
"v_pk_fma_f32 v[34:35], v[130:131], s[90:91], v[34:35] neg_lo:[1,0,0] neg_hi:[1,0,0]\n\t"
"v_pk_fma_f32 v[36:37], v[132:133], s[90:91], v[36:37] neg_lo:[1,0,0] neg_hi:[1,0,0]\n\t"
"v_pk_fma_f32 v[38:39], v[134:135], s[90:91], v[38:39] neg_lo:[1,0,0] neg_hi:[1,0,0]\n\t"
"v_pk_fma_f32 v[20:21], v[116:117], s[90:91], v[20:21] neg_lo:[1,0,0] neg_hi:[1,0,0]\n\t"
"v_pk_fma_f32 v[22:23], v[118:119], s[90:91], v[22:23] neg_lo:[1,0,0] neg_hi:[1,0,0]\n\t"
"v_pk_fma_f32 v[4:5], v[100:101], s[90:91], v[4:5] neg_lo:[1,0,0] neg_hi:[1,0,0]\n\t"
"v_pk_fma_f32 v[6:7], v[102:103], s[90:91], v[6:7] neg_lo:[1,0,0] neg_hi:[1,0,0]\n\t"
"v_pk_fma_f32 v[8:9], v[104:105], s[90:91], v[8:9] neg_lo:[1,0,0] neg_hi:[1,0,0]\n\t"
"v_pk_fma_f32 v[10:11], v[106:107], s[90:91], v[10:11] neg_lo:[1,0,0] neg_hi:[1,0,0]\n\t"
"v_pk_fma_f32 v[24:25], v[120:121], s[90:91], v[24:25] neg_lo:[1,0,0] neg_hi:[1,0,0]\n\t"
"v_pk_fma_f32 v[26:27], v[122:123], s[90:91], v[26:27] neg_lo:[1,0,0] neg_hi:[1,0,0]\n\t"
"v_pk_fma_f32 v[40:41], v[136:137], s[90:91], v[40:41] neg_lo:[1,0,0] neg_hi:[1,0,0]\n\t"
"v_pk_fma_f32 v[42:43], v[138:139], s[90:91], v[42:43] neg_lo:[1,0,0] neg_hi:[1,0,0]\n\t"
"v_pk_fma_f32 v[44:45], v[140:141], s[90:91], v[44:45] neg_lo:[1,0,0] neg_hi:[1,0,0]\n\t"
"v_pk_fma_f32 v[46:47], v[142:143], s[90:91], v[46:47] neg_lo:[1,0,0] neg_hi:[1,0,0]\n\t"
"v_pk_fma_f32 v[28:29], v[124:125], s[90:91], v[28:29] neg_lo:[1,0,0] neg_hi:[1,0,0]\n\t"
"v_pk_fma_f32 v[30:31], v[126:127], s[90:91], v[30:31] neg_lo:[1,0,0] neg_hi:[1,0,0]\n\t"
"v_pk_fma_f32 v[12:13], v[108:109], s[90:91], v[12:13] neg_lo:[1,0,0] neg_hi:[1,0,0]\n\t"
"v_pk_fma_f32 v[14:15], v[110:111], s[90:91], v[14:15] neg_lo:[1,0,0] neg_hi:[1,0,0]\n\t"
"v_mfma_f32_4x4x4_16b_bf16 v[192:195], a[222:223], a[176:177], v[200:203]\n\t"
"ds_read_b64_tr_b16 a[194:195], v250 offset:20864\n\t"
"v_mfma_f32_4x4x4_16b_bf16 v[196:199], a[222:223], a[184:185], v[204:207]\n\t"
"ds_read_b64_tr_b16 a[202:203], v251 offset:20864\n\t"
"v_mfma_f32_4x4x4_16b_bf16 v[200:203], a[222:223], a[180:181], v[200:203]\n\t"
"ds_read_b64_tr_b16 a[176:177], v248 offset:20736\n\t"
"v_mfma_f32_4x4x4_16b_bf16 v[204:207], a[222:223], a[188:189], v[204:207]\n\t"
"v_mfma_f32_4x4x4_16b_bf16 v[208:211], a[222:223], a[192:193], v[216:219]\n\t"
"ds_read_b64_tr_b16 a[184:185], v249 offset:20736\n\t"
"v_mfma_f32_4x4x4_16b_bf16 v[212:215], a[222:223], a[200:201], v[220:223]\n\t"
"ds_read_b64_tr_b16 a[180:181], v249 offset:20992\n\t"
"v_mfma_f32_4x4x4_16b_bf16 v[216:219], a[222:223], a[196:197], v[216:219]\n\t"
"ds_read_b64_tr_b16 a[188:189], v248 offset:20992\n\t"
"v_mfma_f32_4x4x4_16b_bf16 v[220:223], a[222:223], a[204:205], v[220:223]\n\t"
"s_waitcnt vmcnt(14) lgkmcnt(8)\n\t"
"buffer_load_dwordx4 a[132:135], off, s[84:87], s78 offset:3072 sc1\n\t"
"v_mfma_f32_16x16x32_bf16 v[48:51], a[48:51], a[144:147], v[48:51]\n\t"
"ds_read_b64_tr_b16 a[192:193], v250 offset:20736\n\t"
"v_cvt_pk_bf16_f32 v195, -v195, -v199\n\t"
"v_mfma_f32_16x16x32_bf16 v[48:51], a[52:55], a[160:163], v[48:51]\n\t"
"ds_read_b64_tr_b16 a[200:201], v251 offset:20736\n\t"
"v_cvt_pk_bf16_f32 v193, -v193, -v197\n\t"
"v_mfma_f32_16x16x32_bf16 v[64:67], a[56:59], a[144:147], v[64:67]\n\t"
"ds_write_addtid_b32 v193 offset:4128\n\t"
"v_cvt_pk_bf16_f32 v202, v202, v206\n\t"
"v_mfma_f32_16x16x32_bf16 v[64:67], a[60:63], a[160:163], v[64:67]\n\t"
"buffer_load_dwordx4 a[128:131], off, s[84:87], s78 offset:2048 sc1\n\t"
"v_mfma_f32_16x16x32_bf16 v[80:83], a[64:67], a[144:147], v[80:83]\n\t"
"ds_read_b128 a[144:147], v224 offset:17568\n\t"
"v_mfma_f32_16x16x32_bf16 v[80:83], a[68:71], a[160:163], v[80:83]\n\t"
"ds_write_addtid_b32 v202 offset:9344\n\t"
"v_cvt_pk_bf16_f32 v211, -v211, -v215\n\t"
"v_mfma_f32_16x16x32_bf16 v[84:87], a[64:67], a[148:151], v[84:87]\n\t"
"ds_read_b128 a[160:163], v224 offset:19616\n\t"
"v_cvt_pk_bf16_f32 v203, v203, v207\n\t"
"v_mfma_f32_16x16x32_bf16 v[84:87], a[68:71], a[164:167], v[84:87]\n\t"
"buffer_load_dwordx4 a[120:123], off, s[84:87], s78 offset:0 sc1\n\t"
"v_mfma_f32_16x16x32_bf16 v[68:71], a[56:59], a[148:151], v[68:71]\n\t"
"ds_write_addtid_b32 v195 offset:5152\n\t"
"v_mfma_f32_16x16x32_bf16 v[68:71], a[60:63], a[164:167], v[68:71]\n\t"
"ds_write_addtid_b32 v203 offset:13472\n\t"
"v_cvt_pk_bf16_f32 v192, -v192, -v196\n\t"
"v_mfma_f32_16x16x32_bf16 v[52:55], a[48:51], a[148:151], v[52:55]\n\t"
"ds_read_b128 a[148:151], v224 offset:17824\n\t"
"v_cvt_pk_bf16_f32 v201, v201, v205\n\t"
"v_mfma_f32_16x16x32_bf16 v[52:55], a[52:55], a[164:167], v[52:55]\n\t"
"buffer_load_dwordx4 a[124:127], off, s[84:87], s78 offset:1024 sc1\n\t"
"v_mfma_f32_16x16x32_bf16 v[56:59], a[48:51], a[152:155], v[56:59]\n\t"
"ds_read_b128 a[164:167], v224 offset:19872\n\t"
"v_cvt_pk_bf16_f32 v194, -v194, -v198\n\t"
"v_mfma_f32_16x16x32_bf16 v[56:59], a[52:55], a[168:171], v[56:59]\n\t"
"ds_write_addtid_b32 v194 offset:1024\n\t"
"v_mfma_f32_16x16x32_bf16 v[72:75], a[56:59], a[152:155], v[72:75]\n\t"
"ds_write_addtid_b32 v201 offset:12448\n\t"
"v_cvt_pk_bf16_f32 v218, v218, v222\n\t"
"v_mfma_f32_16x16x32_bf16 v[72:75], a[60:63], a[168:171], v[72:75]\n\t"
"ds_write_addtid_b32 v211 offset:7200\n\t"
"v_mfma_f32_16x16x32_bf16 v[88:91], a[64:67], a[152:155], v[88:91]\n\t"
"ds_read_b128 a[152:155], v224 offset:18080\n\t"
"v_cvt_pk_bf16_f32 v200, v200, v204\n\t"
"v_mfma_f32_16x16x32_bf16 v[88:91], a[68:71], a[168:171], v[88:91]\n\t"
"s_waitcnt lgkmcnt(8)\n\t"
"ds_read_b128 a[168:171], v224 offset:20128\n\t"
"v_cvt_pk_bf16_f32 v208, -v208, -v212\n\t"
"v_mfma_f32_16x16x32_bf16 v[92:95], a[64:67], a[156:159], v[92:95]\n\t"
"ds_write_addtid_b32 v200 offset:8320\n\t"
"v_cvt_pk_bf16_f32 v209, -v209, -v213\n\t"
"v_mfma_f32_16x16x32_bf16 v[92:95], a[68:71], a[172:175], v[92:95]\n\t"
"ds_write_addtid_b32 v209 offset:6176\n\t"
"v_cvt_pk_bf16_f32 v217, v217, v221\n\t"
"v_mfma_f32_16x16x32_bf16 v[76:79], a[56:59], a[156:159], v[76:79]\n\t"
"ds_write_addtid_b32 v192 offset:0\n\t"
"v_cvt_pk_bf16_f32 v210, -v210, -v214\n\t"
"v_mfma_f32_16x16x32_bf16 v[76:79], a[60:63], a[172:175], v[76:79]\n\t"
"ds_write_addtid_b32 v208 offset:2048\n\t"
"v_cvt_pk_bf16_f32 v216, v216, v220\n\t"
"v_mfma_f32_16x16x32_bf16 v[60:63], a[48:51], a[156:159], v[60:63]\n\t"
"ds_write_addtid_b32 v210 offset:3072\n\t"
"v_cvt_pk_bf16_f32 v219, v219, v223\n\t"
"v_mfma_f32_16x16x32_bf16 v[60:63], a[52:55], a[172:175], v[60:63]\n\t"
"s_waitcnt vmcnt(12) lgkmcnt(9)\n\t"
"ds_write_addtid_b32 v219 offset:15520\n\t"
"v_mfma_f32_16x16x32_bf16 v[144:147], a[72:75], a[144:147], v[144:147]\n\t"
"ds_write_addtid_b32 v218 offset:11392\n\t"
"v_mfma_f32_16x16x32_bf16 v[144:147], a[76:79], a[160:163], v[144:147]\n\t"
"ds_write_addtid_b32 v217 offset:14496\n\t"
"v_mfma_f32_16x16x32_bf16 v[160:163], a[80:83], a[144:147], v[160:163]\n\t"
"ds_write_addtid_b32 v216 offset:10368\n\t"
"v_mfma_f32_16x16x32_bf16 v[160:163], a[84:87], a[160:163], v[160:163]\n\t"
"ds_read_b128 a[156:159], v224 offset:18336\n\t"
"v_mfma_f32_16x16x32_bf16 v[176:179], a[88:91], a[144:147], v[176:179]\n\t"
"v_mfma_f32_16x16x32_bf16 v[176:179], a[92:95], a[160:163], v[176:179]\n\t"
"ds_read_b128 a[172:175], v224 offset:20384\n\t"
"v_mfma_f32_16x16x32_bf16 v[180:183], a[88:91], a[148:151], v[180:183]\n\t"
"v_mfma_f32_16x16x32_bf16 v[180:183], a[92:95], a[164:167], v[180:183]\n\t"
"ds_read_b64_tr_b16 a[196:197], v251 offset:20992\n\t"
"v_mfma_f32_16x16x32_bf16 v[164:167], a[80:83], a[148:151], v[164:167]\n\t"
"ds_read_b64_tr_b16 a[204:205], v250 offset:20992\n\t"
"v_mfma_f32_16x16x32_bf16 v[164:167], a[84:87], a[164:167], v[164:167]\n\t"
"v_mfma_f32_16x16x32_bf16 v[148:151], a[72:75], a[148:151], v[148:151]\n\t"
"s_waitcnt lgkmcnt(2)\n\t"
"v_mfma_f32_16x16x32_bf16 v[148:151], a[76:79], a[164:167], v[148:151]\n\t"
"s_barrier\n\t"
"v_mfma_f32_16x16x32_bf16 v[152:155], a[72:75], a[152:155], v[152:155]\n\t"
"s_add_u32 m0, s26, 35584\n\t"
"ds_read_b128 a[144:147], v224 offset:0\n\t"
"ds_read_b128 a[160:163], v224 offset:2048\n\t"
"v_mfma_f32_16x16x32_bf16 v[152:155], a[76:79], a[168:171], v[152:155]\n\t"
"buffer_load_dwordx4 v236, s[12:15], s22 offen offset:0 nt lds\n\t"
"v_mfma_f32_16x16x32_bf16 v[168:171], a[80:83], a[152:155], v[168:171]\n\t"
"ds_read_b128 a[148:151], v224 offset:256\n\t"
"v_mfma_f32_16x16x32_bf16 v[168:171], a[84:87], a[168:171], v[168:171]\n\t"
"ds_read_b128 a[164:167], v224 offset:2304\n\t"
"v_mfma_f32_16x16x32_bf16 v[184:187], a[88:91], a[152:155], v[184:187]\n\t"
"v_mfma_f32_16x16x32_bf16 v[184:187], a[92:95], a[168:171], v[184:187]\n\t"
"buffer_load_dwordx4 v237, s[12:15], s22 offen offset:1024 nt lds\n\t"
"v_mfma_f32_16x16x32_bf16 v[188:191], a[88:91], a[156:159], v[188:191]\n\t"
"ds_read_b128 a[168:171], v224 offset:2560\n\t"
"v_mfma_f32_16x16x32_bf16 v[188:191], a[92:95], a[172:175], v[188:191]\n\t"
"ds_read_b128 a[152:155], v224 offset:512\n\t"
"v_mfma_f32_16x16x32_bf16 v[172:175], a[80:83], a[156:159], v[172:175]\n\t"
"v_mfma_f32_16x16x32_bf16 v[172:175], a[84:87], a[172:175], v[172:175]\n\t"
"buffer_load_dwordx4 v238, s[12:15], s22 offen offset:2048 nt lds\n\t"
"v_mfma_f32_16x16x32_bf16 v[156:159], a[72:75], a[156:159], v[156:159]\n\t"
"ds_read_b128 a[156:159], v224 offset:768\n\t"
"v_mfma_f32_16x16x32_bf16 v[156:159], a[76:79], a[172:175], v[156:159]\n\t"
"s_waitcnt lgkmcnt(5)\n\t"
"ds_read_b128 a[172:175], v224 offset:2816\n\t"
"v_mfma_f32_4x4x4_16b_bf16 v[200:203], a[252:253], a[178:179], 0\n\t"
"v_mfma_f32_4x4x4_16b_bf16 v[204:207], a[252:253], a[186:187], 0\n\t"
"buffer_load_dwordx4 v239, s[12:15], s22 offen offset:3072 nt lds\n\t"
"v_mfma_f32_4x4x4_16b_bf16 v[216:219], a[252:253], a[194:195], 0\n\t"
"ds_read_b64_tr_b16 a[178:179], v248 offset:128\n\t"
"v_mfma_f32_4x4x4_16b_bf16 v[220:223], a[252:253], a[202:203], 0\n\t"
"ds_read_b64_tr_b16 a[186:187], v249 offset:128\n\t"
"v_mfma_f32_4x4x4_16b_bf16 v[192:195], a[254:255], a[176:177], v[200:203]\n\t"
"ds_read_b64_tr_b16 a[194:195], v250 offset:128\n\t"
"v_mfma_f32_4x4x4_16b_bf16 v[196:199], a[254:255], a[184:185], v[204:207]\n\t"
"s_mov_b64 exec, s[36:37]\n\t"
"buffer_load_dwordx4 v240, s[12:15], s22 offen offset:3584 nt lds\n\t"
"s_mov_b64 exec, -1\n\t"
"v_mfma_f32_4x4x4_16b_bf16 v[200:203], a[254:255], a[180:181], v[200:203]\n\t"
"ds_read_b64_tr_b16 a[202:203], v251 offset:128\n\t"
"v_mfma_f32_4x4x4_16b_bf16 v[204:207], a[254:255], a[188:189], v[204:207]\n\t"
"ds_read_b64_tr_b16 a[176:177], v248 offset:0\n\t"
"v_mfma_f32_4x4x4_16b_bf16 v[208:211], a[254:255], a[192:193], v[216:219]\n\t"
"ds_read_b64_tr_b16 a[184:185], v249 offset:0\n\t"
"v_mfma_f32_4x4x4_16b_bf16 v[212:215], a[254:255], a[200:201], v[220:223]\n\t"
"s_lshl_b32 m0, s73, 8\n\t"
"v_mfma_f32_4x4x4_16b_bf16 v[216:219], a[254:255], a[196:197], v[216:219]\n\t"
"ds_read_b64_tr_b16 a[180:181], v249 offset:256\n\t"
"v_mfma_f32_4x4x4_16b_bf16 v[220:223], a[254:255], a[204:205], v[220:223]\n\t"
"s_waitcnt vmcnt(11) lgkmcnt(7)\n\t"
"ds_read_b64_tr_b16 a[188:189], v248 offset:256\n\t"
"v_mfma_f32_16x16x32_bf16 v[48:51], a[96:99], a[144:147], v[48:51]\n\t"
"ds_read_b64_tr_b16 a[192:193], v250 offset:0\n\t"
"v_mfma_f32_16x16x32_bf16 v[48:51], a[100:103], a[160:163], v[48:51]\n\t"
"v_cvt_pk_bf16_f32 v193, -v193, -v197\n\t"
"v_cvt_pk_bf16_f32 v195, -v195, -v199\n\t"
"v_mfma_f32_16x16x32_bf16 v[64:67], a[104:107], a[144:147], v[64:67]\n\t"
"ds_read_b64_tr_b16 a[200:201], v251 offset:0\n\t"
"v_mfma_f32_16x16x32_bf16 v[64:67], a[108:111], a[160:163], v[64:67]\n\t"
"ds_write_addtid_b32 v193 offset:20672\n\t"
"v_cvt_pk_bf16_f32 v194, -v194, -v198\n\t"
"v_mfma_f32_16x16x32_bf16 v[80:83], a[112:115], a[144:147], v[80:83]\n\t"
"ds_read_b128 a[144:147], v224 offset:1024\n\t"
"v_cvt_pk_bf16_f32 v201, v201, v205\n\t"
"v_mfma_f32_16x16x32_bf16 v[80:83], a[116:119], a[160:163], v[80:83]\n\t"
"v_cvt_pk_bf16_f32 v209, -v209, -v213\n\t"
"v_mfma_f32_16x16x32_bf16 v[84:87], a[112:115], a[148:151], v[84:87]\n\t"
"ds_read_b128 a[160:163], v224 offset:3072\n\t"
"v_mfma_f32_16x16x32_bf16 v[84:87], a[116:119], a[164:167], v[84:87]\n\t"
"ds_write_addtid_b32 v201 offset:28992\n\t"
"v_cvt_pk_bf16_f32 v217, v217, v221\n\t"
"v_mfma_f32_16x16x32_bf16 v[68:71], a[104:107], a[148:151], v[68:71]\n\t"
"ds_write_addtid_b32 v195 offset:21696\n\t"
"v_cvt_pk_bf16_f32 v203, v203, v207\n\t"
"v_mfma_f32_16x16x32_bf16 v[68:71], a[108:111], a[164:167], v[68:71]\n\t"
"v_cvt_pk_bf16_f32 v200, v200, v204\n\t"
"v_cvt_pk_bf16_f32 v216, v216, v220\n\t"
"v_mfma_f32_16x16x32_bf16 v[52:55], a[96:99], a[148:151], v[52:55]\n\t"
"ds_read_b128 a[148:151], v224 offset:1280\n\t"
"v_mfma_f32_16x16x32_bf16 v[52:55], a[100:103], a[164:167], v[52:55]\n\t"
"ds_read_b128 a[164:167], v224 offset:3328\n\t"
"v_mfma_f32_16x16x32_bf16 v[56:59], a[96:99], a[152:155], v[56:59]\n\t"
"ds_write_addtid_b32 v194 offset:17568\n\t"
"v_cvt_pk_bf16_f32 v211, -v211, -v215\n\t"
"v_mfma_f32_16x16x32_bf16 v[56:59], a[100:103], a[168:171], v[56:59]\n\t"
"ds_write_addtid_b32 v211 offset:23744\n\t"
"v_cvt_pk_bf16_f32 v219, v219, v223\n\t"
"v_mfma_f32_16x16x32_bf16 v[72:75], a[104:107], a[152:155], v[72:75]\n\t"
"ds_write_addtid_b32 v209 offset:22720\n\t"
"v_cvt_pk_bf16_f32 v208, -v208, -v212\n\t"
"v_mfma_f32_16x16x32_bf16 v[72:75], a[108:111], a[168:171], v[72:75]\n\t"
"ds_write_addtid_b32 v203 offset:30016\n\t"
"v_cvt_pk_bf16_f32 v218, v218, v222\n\t"
"v_mfma_f32_16x16x32_bf16 v[88:91], a[112:115], a[152:155], v[88:91]\n\t"
"ds_read_b128 a[152:155], v224 offset:1536\n\t"
"v_cvt_pk_bf16_f32 v210, -v210, -v214\n\t"
"v_mfma_f32_16x16x32_bf16 v[88:91], a[116:119], a[168:171], v[88:91]\n\t"
"ds_read_b128 a[168:171], v224 offset:3584\n\t"
"v_cvt_pk_bf16_f32 v192, -v192, -v196\n\t"
"v_mfma_f32_16x16x32_bf16 v[92:95], a[112:115], a[156:159], v[92:95]\n\t"
"ds_write_addtid_b32 v217 offset:31040\n\t"
"v_mfma_f32_16x16x32_bf16 v[92:95], a[116:119], a[172:175], v[92:95]\n\t"
"ds_write_addtid_b32 v216 offset:26912\n\t"
"v_mfma_f32_16x16x32_bf16 v[76:79], a[104:107], a[156:159], v[76:79]\n\t"
"ds_write_addtid_b32 v200 offset:24864\n\t"
"v_cvt_pk_bf16_f32 v202, v202, v206\n\t"
"v_mfma_f32_16x16x32_bf16 v[76:79], a[108:111], a[172:175], v[76:79]\n\t"
"ds_write_addtid_b32 v210 offset:19616\n\t"
"v_mfma_f32_16x16x32_bf16 v[60:63], a[96:99], a[156:159], v[60:63]\n\t"
"ds_write_addtid_b32 v208 offset:18592\n\t"
"v_mfma_f32_16x16x32_bf16 v[60:63], a[100:103], a[172:175], v[60:63]\n\t"
"s_waitcnt vmcnt(5) lgkmcnt(9)\n\t"
"ds_write_addtid_b32 v218 offset:27936\n\t"
"v_mfma_f32_16x16x32_bf16 v[144:147], a[120:123], a[144:147], v[144:147]\n\t"
"ds_write_addtid_b32 v219 offset:32064\n\t"
"v_mfma_f32_16x16x32_bf16 v[144:147], a[124:127], a[160:163], v[144:147]\n\t"
"ds_write_addtid_b32 v202 offset:25888\n\t"
"v_mfma_f32_16x16x32_bf16 v[160:163], a[128:131], a[144:147], v[160:163]\n\t"
"ds_write_addtid_b32 v192 offset:16544\n\t"
"v_mfma_f32_16x16x32_bf16 v[160:163], a[132:135], a[160:163], v[160:163]\n\t"
"ds_read_b128 a[156:159], v224 offset:1792\n\t"
"v_mfma_f32_16x16x32_bf16 v[176:179], a[136:139], a[144:147], v[176:179]\n\t"
"v_mfma_f32_16x16x32_bf16 v[176:179], a[140:143], a[160:163], v[176:179]\n\t"
"ds_read_b128 a[172:175], v224 offset:3840\n\t"
"v_mfma_f32_16x16x32_bf16 v[180:183], a[136:139], a[148:151], v[180:183]\n\t"
"v_mfma_f32_16x16x32_bf16 v[180:183], a[140:143], a[164:167], v[180:183]\n\t"
"ds_read_b64_tr_b16 a[196:197], v251 offset:256\n\t"
"v_mfma_f32_16x16x32_bf16 v[164:167], a[128:131], a[148:151], v[164:167]\n\t"
"ds_read_b64_tr_b16 a[204:205], v250 offset:256\n\t"
"v_mfma_f32_16x16x32_bf16 v[164:167], a[132:135], a[164:167], v[164:167]\n\t"
"v_mfma_f32_16x16x32_bf16 v[148:151], a[120:123], a[148:151], v[148:151]\n\t"
"s_waitcnt lgkmcnt(2)\n\t"
"v_mfma_f32_16x16x32_bf16 v[148:151], a[124:127], a[164:167], v[148:151]\n\t"
"s_barrier\n\t"
"v_mfma_f32_16x16x32_bf16 v[152:155], a[120:123], a[152:155], v[152:155]\n\t"
"ds_read_b128 a[160:163], v224 offset:18592\n\t"
"v_mfma_f32_16x16x32_bf16 v[152:155], a[124:127], a[168:171], v[152:155]\n\t"
"ds_read_b128 a[144:147], v224 offset:16544\n\t"
"v_mfma_f32_16x16x32_bf16 v[168:171], a[128:131], a[152:155], v[168:171]\n\t"
"ds_read_b128 a[148:151], v224 offset:16800\n\t"
"v_mfma_f32_16x16x32_bf16 v[168:171], a[132:135], a[168:171], v[168:171]\n\t"
"ds_read_b128 a[164:167], v224 offset:18848\n\t"
"v_mfma_f32_16x16x32_bf16 v[184:187], a[136:139], a[152:155], v[184:187]\n\t"
"v_mfma_f32_16x16x32_bf16 v[184:187], a[140:143], a[168:171], v[184:187]\n\t"
"v_mfma_f32_16x16x32_bf16 v[188:191], a[136:139], a[156:159], v[188:191]\n\t"
"ds_read_b128 a[152:155], v224 offset:17056\n\t"
"v_mfma_f32_16x16x32_bf16 v[188:191], a[140:143], a[172:175], v[188:191]\n\t"
"ds_read_b128 a[168:171], v224 offset:19104\n\t"
"v_mfma_f32_16x16x32_bf16 v[172:175], a[128:131], a[156:159], v[172:175]\n\t"
"v_mfma_f32_16x16x32_bf16 v[172:175], a[132:135], a[172:175], v[172:175]\n\t"
"v_mfma_f32_16x16x32_bf16 v[156:159], a[120:123], a[156:159], v[156:159]\n\t"
"ds_read_b128 a[156:159], v224 offset:17312\n\t"
"v_mfma_f32_16x16x32_bf16 v[156:159], a[124:127], a[172:175], v[156:159]\n\t"
"s_waitcnt lgkmcnt(5)\n\t"
"ds_read_b128 a[172:175], v224 offset:19360\n\t"
"v_mfma_f32_4x4x4_16b_bf16 v[200:203], a[252:253], a[178:179], 0\n\t"
"ds_read_b64_tr_b16 a[178:179], v248 offset:41600\n\t"
"v_mfma_f32_4x4x4_16b_bf16 v[204:207], a[252:253], a[186:187], 0\n\t"
"ds_read_b64_tr_b16 a[186:187], v249 offset:41600\n\t"
"v_mfma_f32_4x4x4_16b_bf16 v[216:219], a[252:253], a[194:195], 0\n\t"
"ds_read_b64_tr_b16 a[194:195], v250 offset:41600\n\t"
"v_mfma_f32_4x4x4_16b_bf16 v[220:223], a[252:253], a[202:203], 0\n\t"
".p2align 3\n\t"
"ds_read_b64_tr_b16 a[202:203], v251 offset:41600\n\t"
"v_pk_fma_f32 v[48:49], v[144:145], s[90:91], v[48:49]\n\t"
"v_pk_fma_f32 v[50:51], v[146:147], s[90:91], v[50:51]\n\t"
"v_pk_add_f32 v[144:145], v[48:49], v[144:145] neg_lo:[0,1] neg_hi:[0,1]\n\t"
"v_pk_add_f32 v[146:147], v[50:51], v[146:147] neg_lo:[0,1] neg_hi:[0,1]\n\t"
"v_pk_fma_f32 v[64:65], v[160:161], s[90:91], v[64:65]\n\t"
"v_pk_fma_f32 v[66:67], v[162:163], s[90:91], v[66:67]\n\t"
"v_pk_add_f32 v[160:161], v[64:65], v[160:161] neg_lo:[0,1] neg_hi:[0,1]\n\t"
"v_pk_add_f32 v[162:163], v[66:67], v[162:163] neg_lo:[0,1] neg_hi:[0,1]\n\t"
"v_pk_fma_f32 v[80:81], v[176:177], s[90:91], v[80:81]\n\t"
"v_pk_fma_f32 v[82:83], v[178:179], s[90:91], v[82:83]\n\t"
"v_pk_add_f32 v[176:177], v[80:81], v[176:177] neg_lo:[0,1] neg_hi:[0,1]\n\t"
"v_pk_add_f32 v[178:179], v[82:83], v[178:179] neg_lo:[0,1] neg_hi:[0,1]\n\t"
"v_pk_fma_f32 v[84:85], v[180:181], s[90:91], v[84:85]\n\t"
"v_pk_fma_f32 v[86:87], v[182:183], s[90:91], v[86:87]\n\t"
"v_pk_add_f32 v[180:181], v[84:85], v[180:181] neg_lo:[0,1] neg_hi:[0,1]\n\t"
"v_pk_add_f32 v[182:183], v[86:87], v[182:183] neg_lo:[0,1] neg_hi:[0,1]\n\t"
"v_pk_fma_f32 v[68:69], v[164:165], s[90:91], v[68:69]\n\t"
"v_pk_fma_f32 v[70:71], v[166:167], s[90:91], v[70:71]\n\t"
"v_pk_add_f32 v[164:165], v[68:69], v[164:165] neg_lo:[0,1] neg_hi:[0,1]\n\t"
"v_pk_add_f32 v[166:167], v[70:71], v[166:167] neg_lo:[0,1] neg_hi:[0,1]\n\t"
"v_pk_fma_f32 v[52:53], v[148:149], s[90:91], v[52:53]\n\t"
"v_pk_fma_f32 v[54:55], v[150:151], s[90:91], v[54:55]\n\t"
"v_pk_add_f32 v[148:149], v[52:53], v[148:149] neg_lo:[0,1] neg_hi:[0,1]\n\t"
"v_pk_add_f32 v[150:151], v[54:55], v[150:151] neg_lo:[0,1] neg_hi:[0,1]\n\t"
"v_pk_fma_f32 v[56:57], v[152:153], s[90:91], v[56:57]\n\t"
"v_pk_fma_f32 v[58:59], v[154:155], s[90:91], v[58:59]\n\t"
"v_pk_add_f32 v[152:153], v[56:57], v[152:153] neg_lo:[0,1] neg_hi:[0,1]\n\t"
"v_pk_add_f32 v[154:155], v[58:59], v[154:155] neg_lo:[0,1] neg_hi:[0,1]\n\t"
"v_pk_fma_f32 v[72:73], v[168:169], s[90:91], v[72:73]\n\t"
"v_pk_fma_f32 v[74:75], v[170:171], s[90:91], v[74:75]\n\t"
"v_pk_add_f32 v[168:169], v[72:73], v[168:169] neg_lo:[0,1] neg_hi:[0,1]\n\t"
"v_pk_add_f32 v[170:171], v[74:75], v[170:171] neg_lo:[0,1] neg_hi:[0,1]\n\t"
"v_pk_fma_f32 v[88:89], v[184:185], s[90:91], v[88:89]\n\t"
"v_pk_fma_f32 v[90:91], v[186:187], s[90:91], v[90:91]\n\t"
"v_pk_add_f32 v[184:185], v[88:89], v[184:185] neg_lo:[0,1] neg_hi:[0,1]\n\t"
"v_pk_add_f32 v[186:187], v[90:91], v[186:187] neg_lo:[0,1] neg_hi:[0,1]\n\t"
"v_pk_fma_f32 v[92:93], v[188:189], s[90:91], v[92:93]\n\t"
"v_pk_fma_f32 v[94:95], v[190:191], s[90:91], v[94:95]\n\t"
"v_pk_add_f32 v[188:189], v[92:93], v[188:189] neg_lo:[0,1] neg_hi:[0,1]\n\t"
"v_pk_add_f32 v[190:191], v[94:95], v[190:191] neg_lo:[0,1] neg_hi:[0,1]\n\t"
"v_pk_fma_f32 v[76:77], v[172:173], s[90:91], v[76:77]\n\t"
"v_pk_fma_f32 v[78:79], v[174:175], s[90:91], v[78:79]\n\t"
"v_pk_add_f32 v[172:173], v[76:77], v[172:173] neg_lo:[0,1] neg_hi:[0,1]\n\t"
"v_pk_add_f32 v[174:175], v[78:79], v[174:175] neg_lo:[0,1] neg_hi:[0,1]\n\t"
"v_pk_fma_f32 v[60:61], v[156:157], s[90:91], v[60:61]\n\t"
"v_pk_fma_f32 v[62:63], v[158:159], s[90:91], v[62:63]\n\t"
"v_pk_add_f32 v[156:157], v[60:61], v[156:157] neg_lo:[0,1] neg_hi:[0,1]\n\t"
"v_pk_add_f32 v[158:159], v[62:63], v[158:159] neg_lo:[0,1] neg_hi:[0,1]\n\t"
"v_cvt_pk_bf16_f32 v64, v64, v65\n\t"
"v_mfma_f32_4x4x4_16b_bf16 v[192:195], a[254:255], a[176:177], v[200:203]\n\t"
"ds_read_b64_tr_b16 a[176:177], v248 offset:41472\n\t"
"v_cvt_pk_bf16_f32 v48, v48, v49\n\t"
"v_cvt_pk_bf16_f32 v68, v68, v69\n\t"
"v_mfma_f32_4x4x4_16b_bf16 v[196:199], a[254:255], a[184:185], v[204:207]\n\t"
"ds_read_b64_tr_b16 a[184:185], v249 offset:41472\n\t"
"v_cvt_pk_bf16_f32 v52, v52, v53\n\t"
"v_cvt_pk_bf16_f32 v65, v66, v67\n\t"
"v_mfma_f32_4x4x4_16b_bf16 v[200:203], a[254:255], a[180:181], v[200:203]\n\t"
"ds_read_b64_tr_b16 a[180:181], v249 offset:41728\n\t"
"v_cvt_pk_bf16_f32 v53, v54, v55\n\t"
"v_mfma_f32_4x4x4_16b_bf16 v[204:207], a[254:255], a[188:189], v[204:207]\n\t"
"v_cvt_pk_bf16_f32 v49, v50, v51\n\t"
"v_mfma_f32_4x4x4_16b_bf16 v[208:211], a[254:255], a[192:193], v[216:219]\n\t"
"ds_write2st64_b64 v228, v[48:49], v[52:53] offset0:0 offset1:13\n\t"
"v_mfma_f32_4x4x4_16b_bf16 v[212:215], a[254:255], a[200:201], v[220:223]\n\t"
"v_cvt_pk_bf16_f32 v69, v70, v71\n\t"
"v_mfma_f32_4x4x4_16b_bf16 v[216:219], a[254:255], a[196:197], v[216:219]\n\t"
"v_cvt_pk_bf16_f32 v56, v56, v57\n\t"
"v_mfma_f32_4x4x4_16b_bf16 v[220:223], a[254:255], a[204:205], v[220:223]\n\t"
"ds_write2st64_b64 v246, v[64:65], v[68:69] offset0:0 offset1:13\n\t"
"v_mfma_f32_16x16x32_bf16 v[0:3], a[0:3], a[144:147], v[0:3]\n\t"
"v_cvt_pk_bf16_f32 v84, v84, v85\n\t"
"v_cvt_pk_bf16_f32 v60, v60, v61\n\t"
"v_mfma_f32_16x16x32_bf16 v[0:3], a[4:7], a[160:163], v[0:3]\n\t"
"v_cvt_pk_bf16_f32 v80, v80, v81\n\t"
"v_cvt_pk_bf16_f32 v61, v62, v63\n\t"
"v_mfma_f32_16x16x32_bf16 v[16:19], a[8:11], a[144:147], v[16:19]\n\t"
"v_cvt_pk_bf16_f32 v195, -v195, -v199\n\t"
"v_cvt_pk_bf16_f32 v57, v58, v59\n\t"
"v_mfma_f32_16x16x32_bf16 v[16:19], a[12:15], a[160:163], v[16:19]\n\t"
"ds_read_b64_tr_b16 a[188:189], v248 offset:41728\n\t"
"ds_write2st64_b64 v228, v[56:57], v[60:61] offset0:26 offset1:39\n\t"
"v_mfma_f32_16x16x32_bf16 v[32:35], a[16:19], a[144:147], v[32:35]\n\t"
"v_cvt_pk_bf16_f32 v208, -v208, -v212\n\t"
"v_cvt_pk_bf16_f32 v81, v82, v83\n\t"
"v_mfma_f32_16x16x32_bf16 v[32:35], a[20:23], a[160:163], v[32:35]\n\t"
"s_waitcnt lgkmcnt(4)\n\t"
"v_cvt_pk_bf16_f32 v201, v201, v205\n\t"
"v_cvt_pk_bf16_f32 v76, v76, v77\n\t"
"v_mfma_f32_16x16x32_bf16 v[36:39], a[16:19], a[148:151], v[36:39]\n\t"
"v_cvt_pk_bf16_f32 v202, v202, v206\n\t"
"v_cvt_pk_bf16_f32 v85, v86, v87\n\t"
"v_mfma_f32_16x16x32_bf16 v[36:39], a[20:23], a[164:167], v[36:39]\n\t"
"s_nop 0\n\t"
"ds_read_b128 a[144:147], v224 offset:17568\n\t"
"ds_read_b128 a[160:163], v224 offset:19616\n\t"
"ds_write2st64_b64 v247, v[80:81], v[84:85] offset0:0 offset1:13\n\t"
"v_mfma_f32_16x16x32_bf16 v[20:23], a[8:11], a[148:151], v[20:23]\n\t"
"v_cvt_pk_bf16_f32 v72, v72, v73\n\t"
"v_cvt_pk_bf16_f32 v73, v74, v75\n\t"
"v_mfma_f32_16x16x32_bf16 v[20:23], a[12:15], a[164:167], v[20:23]\n\t"
"v_cvt_pk_bf16_f32 v211, -v211, -v215\n\t"
"v_cvt_pk_bf16_f32 v219, v219, v223\n\t"
"v_mfma_f32_16x16x32_bf16 v[4:7], a[0:3], a[148:151], v[4:7]\n\t"
"v_cvt_pk_bf16_f32 v200, v200, v204\n\t"
"v_cvt_pk_bf16_f32 v77, v78, v79\n\t"
"v_mfma_f32_16x16x32_bf16 v[4:7], a[4:7], a[164:167], v[4:7]\n\t"
"s_nop 0\n\t"
"ds_read_b128 a[148:151], v224 offset:17824\n\t"
"ds_read_b128 a[164:167], v224 offset:19872\n\t"
"ds_write_addtid_b32 v195 offset:5152\n\t"
"ds_write_addtid_b32 v211 offset:7200\n\t"
"v_mfma_f32_16x16x32_bf16 v[8:11], a[0:3], a[152:155], v[8:11]\n\t"
"ds_write_addtid_b32 v201 offset:12448\n\t"
"v_cvt_pk_bf16_f32 v209, -v209, -v213\n\t"
"v_mfma_f32_16x16x32_bf16 v[8:11], a[4:7], a[168:171], v[8:11]\n\t"
"ds_write_addtid_b32 v209 offset:6176\n\t"
"v_cvt_pk_bf16_f32 v194, -v194, -v198\n\t"
"v_mfma_f32_16x16x32_bf16 v[24:27], a[8:11], a[152:155], v[24:27]\n\t"
"ds_write_addtid_b32 v194 offset:1024\n\t"
"v_cvt_pk_bf16_f32 v92, v92, v93\n\t"
"v_mfma_f32_16x16x32_bf16 v[24:27], a[12:15], a[168:171], v[24:27]\n\t"
"s_nop 0\n\t"
"ds_write_addtid_b32 v202 offset:9344\n\t"
"ds_write_addtid_b32 v208 offset:2048\n\t"
"ds_write_addtid_b32 v219 offset:15520\n\t"
"v_mfma_f32_16x16x32_bf16 v[40:43], a[16:19], a[152:155], v[40:43]\n\t"
"v_cvt_pk_bf16_f32 v192, -v192, -v196\n\t"
"v_cvt_pk_bf16_f32 v93, v94, v95\n\t"
"v_mfma_f32_16x16x32_bf16 v[40:43], a[20:23], a[168:171], v[40:43]\n\t"
"ds_read_b128 a[152:155], v224 offset:18080\n\t"
"ds_write2st64_b64 v246, v[72:73], v[76:77] offset0:26 offset1:39\n\t"
"v_mfma_f32_16x16x32_bf16 v[44:47], a[16:19], a[156:159], v[44:47]\n\t"
"v_cvt_pk_bf16_f32 v218, v218, v222\n\t"
"v_cvt_pk_bf16_f32 v88, v88, v89\n\t"
"v_mfma_f32_16x16x32_bf16 v[44:47], a[20:23], a[172:175], v[44:47]\n\t"
"buffer_load_dwordx4 a[16:19], off, s[92:95], s59 offset:0 sc1\n\t"
"v_mfma_f32_16x16x32_bf16 v[28:31], a[8:11], a[156:159], v[28:31]\n\t"
"v_cvt_pk_bf16_f32 v216, v216, v220\n\t"
"v_cvt_pk_bf16_f32 v89, v90, v91\n\t"
"v_mfma_f32_16x16x32_bf16 v[28:31], a[12:15], a[172:175], v[28:31]\n\t"
"ds_read_b128 a[168:171], v224 offset:20128\n\t"
"ds_write2st64_b64 v247, v[88:89], v[92:93] offset0:26 offset1:39\n\t"
"v_mfma_f32_16x16x32_bf16 v[12:15], a[0:3], a[156:159], v[12:15]\n\t"
"v_cvt_pk_bf16_f32 v210, -v210, -v214\n\t"
"v_cvt_pk_bf16_f32 v217, v217, v221\n\t"
"v_mfma_f32_16x16x32_bf16 v[12:15], a[4:7], a[172:175], v[12:15]\n\t"
"s_waitcnt lgkmcnt(8)\n\t"
"buffer_load_dwordx4 a[20:23], off, s[92:95], s59 offset:1024 sc1\n\t"
"v_mfma_f32_16x16x32_bf16 v[96:99], a[24:27], a[144:147], v[96:99]\n\t"
"v_cvt_pk_bf16_f32 v193, -v193, -v197\n\t"
"v_cvt_pk_bf16_f32 v203, v203, v207\n\t"
"v_mfma_f32_16x16x32_bf16 v[96:99], a[28:31], a[160:163], v[96:99]\n\t"
"s_nop 0\n\t"
"ds_write_addtid_b32 v192 offset:0\n\t"
"ds_write_addtid_b32 v193 offset:4128\n\t"
"ds_write_addtid_b32 v200 offset:8320\n\t"
"ds_write_addtid_b32 v210 offset:3072\n\t"
"ds_write_addtid_b32 v216 offset:10368\n\t"
"ds_write_addtid_b32 v218 offset:11392\n\t"
"v_mfma_f32_16x16x32_bf16 v[112:115], a[32:35], a[144:147], v[112:115]\n\t"
"s_nop 0\n\t"
"ds_write_addtid_b32 v203 offset:13472\n\t"
"ds_write_addtid_b32 v217 offset:14496\n\t"
"v_mfma_f32_16x16x32_bf16 v[112:115], a[36:39], a[160:163], v[112:115]\n\t"
"buffer_load_dwordx4 a[12:15], off, s[96:99], s58 offset:3072 sc1\n\t"
"v_mfma_f32_16x16x32_bf16 v[128:131], a[40:43], a[144:147], v[128:131]\n\t"
"s_nop 0\n\t"
"ds_read_b128 a[156:159], v224 offset:18336\n\t"
"ds_read_b128 a[172:175], v224 offset:20384\n\t"
"v_mfma_f32_16x16x32_bf16 v[128:131], a[44:47], a[160:163], v[128:131]\n\t"
"buffer_load_dwordx4 a[8:11], off, s[96:99], s58 offset:2048 sc1\n\t"
"v_mfma_f32_16x16x32_bf16 v[132:135], a[40:43], a[148:151], v[132:135]\n\t"
"s_nop 0\n\t"
"ds_read_b64_tr_b16 a[192:193], v250 offset:41472\n\t"
"ds_read_b64_tr_b16 a[200:201], v251 offset:41472\n\t"
"v_mfma_f32_16x16x32_bf16 v[132:135], a[44:47], a[164:167], v[132:135]\n\t"
"buffer_load_dwordx4 a[0:3], off, s[96:99], s58 offset:0 sc1\n\t"
"v_mfma_f32_16x16x32_bf16 v[116:119], a[32:35], a[148:151], v[116:119]\n\t"
"s_nop 0\n\t"
"ds_read_b64_tr_b16 a[196:197], v251 offset:41728\n\t"
"ds_read_b64_tr_b16 a[204:205], v250 offset:41728\n\t"
"v_mfma_f32_16x16x32_bf16 v[116:119], a[36:39], a[164:167], v[116:119]\n\t"
"buffer_load_dwordx4 a[4:7], off, s[96:99], s58 offset:1024 sc1\n\t"
"v_mfma_f32_16x16x32_bf16 v[100:103], a[24:27], a[148:151], v[100:103]\n\t"
"s_waitcnt lgkmcnt(4)\n\t"
"v_mfma_f32_16x16x32_bf16 v[100:103], a[28:31], a[164:167], v[100:103]\n\t"
"s_barrier\n\t"
"v_mfma_f32_16x16x32_bf16 v[104:107], a[24:27], a[152:155], v[104:107]\n\t"
"ds_read_b128 a[144:147], v224 offset:0\n\t"
"v_cvt_pk_bf16_f32 v144, v144, v145\n\t"
"v_mfma_f32_16x16x32_bf16 v[104:107], a[28:31], a[168:171], v[104:107]\n\t"
"ds_read_b128 a[148:151], v224 offset:256\n\t"
"v_cvt_pk_bf16_f32 v180, v180, v181\n\t"
"v_mfma_f32_16x16x32_bf16 v[120:123], a[32:35], a[152:155], v[120:123]\n\t"
"ds_read_b128 a[160:163], v224 offset:2048\n\t"
"v_cvt_pk_bf16_f32 v160, v160, v161\n\t"
"v_mfma_f32_16x16x32_bf16 v[120:123], a[36:39], a[168:171], v[120:123]\n\t"
"s_nop 0\n\t"
"ds_read_b128 a[164:167], v224 offset:2304\n\t"
"v_cvt_pk_bf16_f32 v161, v162, v163\n\t"
"v_mfma_f32_16x16x32_bf16 v[136:139], a[40:43], a[152:155], v[136:139]\n\t"
"s_nop 0\n\t"
"v_cvt_pk_bf16_f32 v145, v146, v147\n\t"
"v_cvt_pk_bf16_f32 v172, v172, v173\n\t"
"v_mfma_f32_16x16x32_bf16 v[136:139], a[44:47], a[168:171], v[136:139]\n\t"
"s_nop 0\n\t"
"v_cvt_pk_bf16_f32 v176, v176, v177\n\t"
"v_cvt_pk_bf16_f32 v168, v168, v169\n\t"
"v_mfma_f32_16x16x32_bf16 v[140:143], a[40:43], a[156:159], v[140:143]\n\t"
"s_nop 0\n\t"
"ds_read_b128 v[56:59], v229 offset:0\n\t"
"v_cvt_pk_bf16_f32 v148, v148, v149\n\t"
"v_mfma_f32_16x16x32_bf16 v[140:143], a[44:47], a[172:175], v[140:143]\n\t"
"s_nop 0\n\t"
"ds_read_b128 v[72:75], v230 offset:0\n\t"
"buffer_load_dwordx4 a[40:43], off, s[92:95], s58 offset:2048 sc1\n\t"
"v_mfma_f32_16x16x32_bf16 v[124:127], a[32:35], a[156:159], v[124:127]\n\t"
"s_nop 0\n\t"
"v_cvt_pk_bf16_f32 v177, v178, v179\n\t"
"v_cvt_pk_bf16_f32 v156, v156, v157\n\t"
"v_mfma_f32_16x16x32_bf16 v[124:127], a[36:39], a[172:175], v[124:127]\n\t"
"Lpad9_%=:\n\t"
"s_cbranch_execz Lret1_%=\n\t"
"buffer_load_dwordx4 a[44:47], off, s[92:95], s58 offset:3072 sc1\n\t"
"v_mfma_f32_16x16x32_bf16 v[108:111], a[24:27], a[156:159], v[108:111]\n\t"
"ds_read_b128 v[92:95], v231 offset:13312\n\t"
"v_cvt_pk_bf16_f32 v184, v184, v185\n\t"
"v_mfma_f32_16x16x32_bf16 v[108:111], a[28:31], a[172:175], v[108:111]\n\t"
"s_waitcnt lgkmcnt(5)\n\t"
"ds_read_b128 v[76:79], v230 offset:13312\n\t"
"v_cvt_pk_bf16_f32 v164, v164, v165\n\t"
"v_mfma_f32_4x4x4_16b_bf16 v[200:203], a[252:253], a[178:179], 0\n\t"
"buffer_load_dwordx4 a[36:39], off, s[92:95], s58 offset:1024 sc1\n\t"
"v_mfma_f32_4x4x4_16b_bf16 v[204:207], a[252:253], a[186:187], 0\n\t"
"v_cvt_pk_bf16_f32 v157, v158, v159\n\t"
"v_mfma_f32_4x4x4_16b_bf16 v[216:219], a[252:253], a[194:195], 0\n\t"
"ds_read_b128 v[88:91], v231 offset:0\n\t"
"v_mfma_f32_4x4x4_16b_bf16 v[220:223], a[252:253], a[202:203], 0\n\t"
"ds_read_b128 v[60:63], v229 offset:13312\n\t"
"buffer_load_dwordx4 a[32:35], off, s[92:95], s58 offset:0 sc1\n\t"
"v_mfma_f32_4x4x4_16b_bf16 v[192:195], a[254:255], a[176:177], v[200:203]\n\t"
"v_cvt_pk_bf16_f32 v149, v150, v151\n\t"
"v_cvt_pk_bf16_f32 v181, v182, v183\n\t"
"v_mfma_f32_4x4x4_16b_bf16 v[196:199], a[254:255], a[184:185], v[204:207]\n\t"
"ds_read_b128 a[156:159], v224 offset:768\n\t"
"v_mfma_f32_4x4x4_16b_bf16 v[200:203], a[254:255], a[180:181], v[200:203]\n\t"
"ds_read_b128 a[168:171], v224 offset:2560\n\t"
"buffer_load_dwordx4 a[24:27], off, s[96:99], s57 offset:2048 sc1\n\t"
"v_mfma_f32_4x4x4_16b_bf16 v[204:207], a[254:255], a[188:189], v[204:207]\n\t"
"v_cvt_pk_bf16_f32 v152, v152, v153\n\t"
"v_mfma_f32_4x4x4_16b_bf16 v[208:211], a[254:255], a[192:193], v[216:219]\n\t"
"ds_read_b128 a[152:155], v224 offset:512\n\t"
"v_mfma_f32_4x4x4_16b_bf16 v[212:215], a[254:255], a[200:201], v[220:223]\n\t"
"ds_read_b128 a[172:175], v224 offset:2816\n\t"
"s_waitcnt lgkmcnt(6)\n\t"
"buffer_store_dwordx4 v[56:59], v232, s[16:19], s24 offen nt\n\t"
"v_mfma_f32_4x4x4_16b_bf16 v[216:219], a[254:255], a[196:197], v[216:219]\n\t"
"v_cvt_pk_bf16_f32 v165, v166, v167\n\t"
"v_mfma_f32_4x4x4_16b_bf16 v[220:223], a[254:255], a[204:205], v[220:223]\n\t"
"s_waitcnt lgkmcnt(4)\n\t"
"s_barrier\n\t"
"v_mfma_f32_16x16x32_bf16 v[0:3], a[48:51], a[144:147], v[0:3]\n\t"
"ds_write2st64_b64 v228, v[144:145], v[148:149] offset0:0 offset1:13\n\t"
"v_mfma_f32_16x16x32_bf16 v[0:3], a[52:55], a[160:163], v[0:3]\n\t"
"buffer_load_dwordx4 a[28:31], off, s[96:99], s57 offset:3072 sc1\n\t"
"v_mfma_f32_16x16x32_bf16 v[16:19], a[56:59], a[144:147], v[16:19]\n\t"
"v_cvt_pk_bf16_f32 v203, v203, v207\n\t"
"v_cvt_pk_bf16_f32 v153, v154, v155\n\t"
"v_mfma_f32_16x16x32_bf16 v[16:19], a[60:63], a[160:163], v[16:19]\n\t"
"ds_write2st64_b64 v246, v[160:161], v[164:165] offset0:0 offset1:13\n\t"
"v_mfma_f32_16x16x32_bf16 v[32:35], a[64:67], a[144:147], v[32:35]\n\t"
"v_cvt_pk_bf16_f32 v208, -v208, -v212\n\t"
"v_cvt_pk_bf16_f32 v188, v188, v189\n\t"
"v_mfma_f32_16x16x32_bf16 v[32:35], a[68:71], a[160:163], v[32:35]\n\t"
"buffer_store_dwordx4 v[72:75], v233, s[16:19], s24 offen nt\n\t"
"v_mfma_f32_16x16x32_bf16 v[36:39], a[64:67], a[148:151], v[36:39]\n\t"
"v_cvt_pk_bf16_f32 v209, -v209, -v213\n\t"
"v_cvt_pk_bf16_f32 v218, v218, v222\n\t"
"v_mfma_f32_16x16x32_bf16 v[36:39], a[68:71], a[164:167], v[36:39]\n\t"
"s_nop 0\n\t"
"ds_read_b128 a[144:147], v224 offset:1024\n\t"
"ds_read_b128 a[160:163], v224 offset:3072\n\t"
"ds_write2st64_b64 v247, v[176:177], v[180:181] offset0:0 offset1:13\n\t"
"v_mfma_f32_16x16x32_bf16 v[20:23], a[56:59], a[148:151], v[20:23]\n\t"
"v_cvt_pk_bf16_f32 v194, -v194, -v198\n\t"
"v_cvt_pk_bf16_f32 v169, v170, v171\n\t"
"v_mfma_f32_16x16x32_bf16 v[20:23], a[60:63], a[164:167], v[20:23]\n\t"
"buffer_store_dwordx4 v[88:91], v234, s[16:19], s24 offen nt\n\t"
"v_mfma_f32_16x16x32_bf16 v[4:7], a[48:51], a[148:151], v[4:7]\n\t"
"v_cvt_pk_bf16_f32 v219, v219, v223\n\t"
"v_cvt_pk_bf16_f32 v173, v174, v175\n\t"
"v_mfma_f32_16x16x32_bf16 v[4:7], a[52:55], a[164:167], v[4:7]\n\t"
"s_waitcnt lgkmcnt(4)\n\t"
"ds_read_b128 a[148:151], v224 offset:1280\n\t"
"ds_read_b128 a[164:167], v224 offset:3328\n\t"
"ds_write2st64_b64 v246, v[168:169], v[172:173] offset0:26 offset1:39\n\t"
"v_mfma_f32_16x16x32_bf16 v[8:11], a[48:51], a[152:155], v[8:11]\n\t"
"v_cvt_pk_bf16_f32 v201, v201, v205\n\t"
"v_cvt_pk_bf16_f32 v216, v216, v220\n\t"
"v_mfma_f32_16x16x32_bf16 v[8:11], a[52:55], a[168:171], v[8:11]\n\t"
"buffer_store_dwordx4 v[60:63], v232, s[16:19], s27 offen nt\n\t"
"v_mfma_f32_16x16x32_bf16 v[24:27], a[56:59], a[152:155], v[24:27]\n\t"
"v_cvt_pk_bf16_f32 v200, v200, v204\n\t"
"v_cvt_pk_bf16_f32 v211, -v211, -v215\n\t"
"v_mfma_f32_16x16x32_bf16 v[24:27], a[60:63], a[168:171], v[24:27]\n\t"
"s_nop 0\n\t"
"ds_write_addtid_b32 v208 offset:18592\n\t"
"ds_write_addtid_b32 v209 offset:22720\n\t"
"ds_write_addtid_b32 v216 offset:26912\n\t"
"ds_write_addtid_b32 v219 offset:32064\n\t"
"ds_write2st64_b64 v228, v[152:153], v[156:157] offset0:26 offset1:39\n\t"
"v_mfma_f32_16x16x32_bf16 v[40:43], a[64:67], a[152:155], v[40:43]\n\t"
"v_cvt_pk_bf16_f32 v185, v186, v187\n\t"
"v_cvt_pk_bf16_f32 v189, v190, v191\n\t"
"v_mfma_f32_16x16x32_bf16 v[40:43], a[68:71], a[168:171], v[40:43]\n\t"
"v_cvt_pk_bf16_f32 v192, -v192, -v196\n\t"
"v_cvt_pk_bf16_f32 v210, -v210, -v214\n\t"
"v_mfma_f32_16x16x32_bf16 v[44:47], a[64:67], a[156:159], v[44:47]\n\t"
"buffer_load_dwordx4 a[64:67], off, s[92:95], s65 offset:0 sc1\n\t"
"v_mfma_f32_16x16x32_bf16 v[44:47], a[68:71], a[172:175], v[44:47]\n\t"
"ds_write_addtid_b32 v200 offset:24864\n\t"
"ds_write2st64_b64 v247, v[184:185], v[188:189] offset0:26 offset1:39\n\t"
"v_mfma_f32_16x16x32_bf16 v[28:31], a[56:59], a[156:159], v[28:31]\n\t"
"buffer_load_dwordx4 a[68:71], off, s[92:95], s65 offset:1024 sc1\n\t"
"v_mfma_f32_16x16x32_bf16 v[28:31], a[60:63], a[172:175], v[28:31]\n\t"
"v_cvt_pk_bf16_f32 v202, v202, v206\n\t"
"v_cvt_pk_bf16_f32 v217, v217, v221\n\t"
"v_mfma_f32_16x16x32_bf16 v[12:15], a[48:51], a[156:159], v[12:15]\n\t"
"v_cvt_pk_bf16_f32 v193, -v193, -v197\n\t"
"v_cvt_pk_bf16_f32 v195, -v195, -v199\n\t"
"v_mfma_f32_16x16x32_bf16 v[12:15], a[52:55], a[172:175], v[12:15]\n\t"
"s_waitcnt lgkmcnt(7)\n\t"
"ds_write_addtid_b32 v192 offset:16544\n\t"
"ds_write_addtid_b32 v201 offset:28992\n\t"
"ds_write_addtid_b32 v203 offset:30016\n\t"
"ds_write_addtid_b32 v211 offset:23744\n\t"
"ds_write_addtid_b32 v218 offset:27936\n\t"
"buffer_store_dwordx4 v[76:79], v233, s[16:19], s27 offen nt\n\t"
"v_mfma_f32_16x16x32_bf16 v[96:99], a[72:75], a[144:147], v[96:99]\n\t"
"s_nop 0\n\t"
"ds_write_addtid_b32 v210 offset:19616\n\t"
"ds_write_addtid_b32 v217 offset:31040\n\t"
"v_mfma_f32_16x16x32_bf16 v[96:99], a[76:79], a[160:163], v[96:99]\n\t"
"s_nop 0\n\t"
"ds_write_addtid_b32 v193 offset:20672\n\t"
"ds_write_addtid_b32 v194 offset:17568\n\t"
"v_mfma_f32_16x16x32_bf16 v[112:115], a[80:83], a[144:147], v[112:115]\n\t"
"s_nop 0\n\t"
"ds_write_addtid_b32 v195 offset:21696\n\t"
"ds_write_addtid_b32 v202 offset:25888\n\t"
"v_mfma_f32_16x16x32_bf16 v[112:115], a[84:87], a[160:163], v[112:115]\n\t"
"buffer_load_dwordx4 a[60:63], off, s[96:99], s64 offset:3072 sc1\n\t"
"buffer_load_dwordx4 a[56:59], off, s[96:99], s64 offset:2048 sc1\n\t"
"v_mfma_f32_16x16x32_bf16 v[128:131], a[88:91], a[144:147], v[128:131]\n\t"
"ds_read_b128 a[152:155], v224 offset:1536\n\t"
"v_accvgpr_read_b32 v248, a208\n\t"
"v_mfma_f32_16x16x32_bf16 v[128:131], a[92:95], a[160:163], v[128:131]\n\t"
"ds_read_b128 a[168:171], v224 offset:3584\n\t"
"buffer_load_dwordx4 a[48:51], off, s[96:99], s64 offset:0 sc1\n\t"
"v_mfma_f32_16x16x32_bf16 v[132:135], a[88:91], a[148:151], v[132:135]\n\t"
"s_nop 0\n\t"
"ds_read_b128 a[156:159], v224 offset:1792\n\t"
"ds_read_b128 a[172:175], v224 offset:3840\n\t"
"v_mfma_f32_16x16x32_bf16 v[132:135], a[92:95], a[164:167], v[132:135]\n\t"
"buffer_store_dwordx4 v[92:95], v234, s[16:19], s27 offen nt\n\t"
"v_mfma_f32_16x16x32_bf16 v[116:119], a[80:83], a[148:151], v[116:119]\n\t"
"v_add_u32 v248, 101632, v248\n\t"
"v_xor_b32 v249, 32, v248\n\t"
"v_mfma_f32_16x16x32_bf16 v[116:119], a[84:87], a[164:167], v[116:119]\n\t"
"v_xor_b32 v250, 64, v248\n\t"
"v_xor_b32 v251, 96, v248\n\t"
"v_mfma_f32_16x16x32_bf16 v[100:103], a[72:75], a[148:151], v[100:103]\n\t"
"buffer_load_dwordx4 a[52:55], off, s[96:99], s64 offset:1024 sc1\n\t"
"v_mfma_f32_16x16x32_bf16 v[100:103], a[76:79], a[164:167], v[100:103]\n\t"
"s_waitcnt lgkmcnt(0)\n\t"
"s_barrier\n\t"
"v_mfma_f32_16x16x32_bf16 v[104:107], a[72:75], a[152:155], v[104:107]\n\t"
"s_nop 0\n\t"
"ds_read_b128 a[144:147], v224 offset:16544\n\t"
"ds_read_b128 a[160:163], v224 offset:18592\n\t"
"v_mfma_f32_16x16x32_bf16 v[104:107], a[76:79], a[168:171], v[104:107]\n\t"
"s_nop 0\n\t"
"ds_read_b128 a[148:151], v224 offset:16800\n\t"
"ds_read_b128 a[164:167], v224 offset:18848\n\t"
"v_mfma_f32_16x16x32_bf16 v[120:123], a[80:83], a[152:155], v[120:123]\n\t"
"ds_read_b64_tr_b16 a[176:177], v248 offset:20736\n\t"
"v_mfma_f32_16x16x32_bf16 v[120:123], a[84:87], a[168:171], v[120:123]\n\t"
"ds_read_b64_tr_b16 a[178:179], v249 offset:20736\n\t"
"v_mfma_f32_16x16x32_bf16 v[136:139], a[88:91], a[152:155], v[136:139]\n\t"
"s_nop 0\n\t"
"ds_read_b64_tr_b16 a[180:181], v248 offset:21248\n\t"
"ds_read_b64_tr_b16 a[182:183], v249 offset:21248\n\t"
"v_mfma_f32_16x16x32_bf16 v[136:139], a[92:95], a[168:171], v[136:139]\n\t"
"s_nop 0\n\t"
"ds_read_b64_tr_b16 a[184:185], v250 offset:23040\n\t"
"ds_read_b64_tr_b16 a[186:187], v251 offset:23040\n\t"
"v_mfma_f32_16x16x32_bf16 v[140:143], a[88:91], a[156:159], v[140:143]\n\t"
"ds_read_b64_tr_b16 a[188:189], v250 offset:23552\n\t"
"v_mfma_f32_16x16x32_bf16 v[140:143], a[92:95], a[172:175], v[140:143]\n\t"
"s_nop 0\n\t"
"ds_read_b64_tr_b16 a[190:191], v251 offset:23552\n\t"
"ds_read_b64_tr_b16 a[192:193], v250 offset:20736\n\t"
"v_mfma_f32_16x16x32_bf16 v[124:127], a[80:83], a[156:159], v[124:127]\n\t"
"s_nop 0\n\t"
"ds_read_b64_tr_b16 a[194:195], v251 offset:20736\n\t"
"ds_read_b64_tr_b16 a[196:197], v250 offset:21248\n\t"
"v_mfma_f32_16x16x32_bf16 v[124:127], a[84:87], a[172:175], v[124:127]\n\t"
"s_waitcnt lgkmcnt(7)\n\t"
"ds_read_b64_tr_b16 a[198:199], v251 offset:21248\n\t"
"ds_read_b64_tr_b16 a[200:201], v248 offset:23040\n\t"
"v_mfma_f32_4x4x4_16b_bf16 v[192:195], a[210:211], a[176:177], 0\n\t"
"ds_read_b64_tr_b16 a[202:203], v249 offset:23040\n\t"
"v_mfma_f32_4x4x4_16b_bf16 v[196:199], a[210:211], a[178:179], 0\n\t"
"ds_read_b128 v[152:155], v229 offset:0\n\t"
"v_mfma_f32_4x4x4_16b_bf16 v[200:203], a[210:211], a[180:181], 0\n\t"
"ds_read_b128 a[168:171], v224 offset:19104\n\t"
"v_mfma_f32_4x4x4_16b_bf16 v[204:207], a[210:211], a[182:183], 0\n\t"
"buffer_load_dwordx4 a[88:91], off, s[92:95], s64 offset:2048 sc1\n\t"
"v_mfma_f32_16x16x32_bf16 v[108:111], a[72:75], a[156:159], v[108:111]\n\t"
"s_nop 0\n\t"
"ds_read_b128 a[156:159], v224 offset:17312\n\t"
"ds_read_b64_tr_b16 a[204:205], v248 offset:23552\n\t"
"v_mfma_f32_16x16x32_bf16 v[108:111], a[76:79], a[172:175], v[108:111]\n\t"
"buffer_load_dwordx4 a[92:95], off, s[92:95], s64 offset:3072 sc1\n\t"
"v_mfma_f32_16x16x32_bf16 v[0:3], a[96:99], a[144:147], v[0:3]\n\t"
"ds_read_b128 v[168:171], v230 offset:0\n\t"
"v_cvt_pk_bf16_f32 v194, v194, v198\n\t"
"v_mfma_f32_16x16x32_bf16 v[0:3], a[100:103], a[160:163], v[0:3]\n\t"
"v_cvt_pk_bf16_f32 v196, v192, v196\n\t"
"v_cvt_pk_bf16_f32 v195, v195, v199\n\t"
"v_mfma_f32_16x16x32_bf16 v[16:19], a[104:107], a[144:147], v[16:19]\n\t"
"ds_read_b128 a[172:175], v224 offset:19360\n\t"
"v_cvt_pk_bf16_f32 v199, v201, v205\n\t"
"v_mfma_f32_16x16x32_bf16 v[16:19], a[108:111], a[160:163], v[16:19]\n\t"
"s_waitcnt lgkmcnt(5)\n\t"
"buffer_store_dwordx4 v[152:155], v232, s[16:19], s20 offen nt\n\t"
"v_mfma_f32_16x16x32_bf16 v[32:35], a[112:115], a[144:147], v[32:35]\n\t"
"ds_read_b128 a[152:155], v224 offset:17056\n\t"
"v_cvt_pk_bf16_f32 v198, v200, v204\n\t"
"v_mfma_f32_16x16x32_bf16 v[32:35], a[116:119], a[160:163], v[32:35]\n\t"
"ds_read_b128 v[184:187], v231 offset:0\n\t"
"v_cvt_pk_bf16_f32 v197, v193, v197\n\t"
"v_mfma_f32_4x4x4_16b_bf16 v[208:211], a[210:211], a[184:185], 0\n\t"
"ds_write_addtid_b32 v198 offset:8320\n\t"
"v_mfma_f32_4x4x4_16b_bf16 v[212:215], a[210:211], a[186:187], 0\n\t"
"s_waitcnt lgkmcnt(4)\n\t"
"buffer_store_dwordx4 v[168:171], v233, s[16:19], s20 offen nt\n\t"
"v_mfma_f32_4x4x4_16b_bf16 v[216:219], a[210:211], a[188:189], 0\n\t"
"ds_read_b128 v[156:159], v229 offset:13312\n\t"
"v_mfma_f32_4x4x4_16b_bf16 v[220:223], a[210:211], a[190:191], 0\n\t"
"ds_read_b128 a[144:147], v224 offset:17568\n\t"
"v_cvt_pk_bf16_f32 v193, v203, v207\n\t"
"v_mfma_f32_16x16x32_bf16 v[36:39], a[112:115], a[148:151], v[36:39]\n\t"
"ds_write_addtid_b32 v196 offset:0\n\t"
"v_cvt_pk_bf16_f32 v192, v202, v206\n\t"
"v_mfma_f32_16x16x32_bf16 v[36:39], a[116:119], a[164:167], v[36:39]\n\t"
"buffer_load_dwordx4 a[84:87], off, s[92:95], s64 offset:1024 sc1\n\t"
"v_mfma_f32_16x16x32_bf16 v[20:23], a[104:107], a[148:151], v[20:23]\n\t"
"ds_write_addtid_b32 v199 offset:9344\n\t"
"v_cvt_pk_bf16_f32 v212, v208, v212\n\t"
"v_mfma_f32_16x16x32_bf16 v[20:23], a[108:111], a[164:167], v[20:23]\n\t"
"ds_read_b128 v[172:175], v230 offset:13312\n\t"
"v_cvt_pk_bf16_f32 v210, v210, v214\n\t"
"v_mfma_f32_16x16x32_bf16 v[4:7], a[96:99], a[148:151], v[4:7]\n\t"
"v_cvt_pk_bf16_f32 v208, v218, v222\n\t"
"v_cvt_pk_bf16_f32 v213, v209, v213\n\t"
"v_mfma_f32_16x16x32_bf16 v[4:7], a[100:103], a[164:167], v[4:7]\n\t"
"s_waitcnt lgkmcnt(6)\n\t"
"buffer_store_dwordx4 v[184:187], v234, s[16:19], s20 offen nt\n\t"
"v_cvt_pk_bf16_f32 v211, v211, v215\n\t"
"v_mfma_f32_4x4x4_16b_bf16 v[200:203], a[210:211], a[192:193], 0\n\t"
"ds_read_b128 a[148:151], v224 offset:17824\n\t"
"v_cvt_pk_bf16_f32 v215, v217, v221\n\t"
"v_mfma_f32_4x4x4_16b_bf16 v[204:207], a[210:211], a[194:195], 0\n\t"
"ds_write_addtid_b32 v197 offset:1024\n\t"
"v_cvt_pk_bf16_f32 v214, v216, v220\n\t"
"v_mfma_f32_4x4x4_16b_bf16 v[196:199], a[210:211], a[196:197], 0\n\t"
"v_cvt_pk_bf16_f32 v209, v219, v223\n\t"
"v_mfma_f32_4x4x4_16b_bf16 v[216:219], a[210:211], a[198:199], 0\n\t"
"buffer_load_dwordx4 a[80:83], off, s[92:95], s64 offset:0 sc1\n\t"
"v_mfma_f32_16x16x32_bf16 v[8:11], a[96:99], a[152:155], v[8:11]\n\t"
"s_nop 0\n\t"
"ds_read_b128 a[160:163], v224 offset:19616\n\t"
"ds_write_addtid_b32 v215 offset:13472\n\t"
"v_mfma_f32_16x16x32_bf16 v[8:11], a[100:103], a[168:171], v[8:11]\n\t"
"s_nop 0\n\t"
"ds_write_addtid_b32 v212 offset:4128\n\t"
"ds_read_b64_tr_b16 a[206:207], v249 offset:23552\n\t"
"v_mfma_f32_16x16x32_bf16 v[24:27], a[104:107], a[152:155], v[24:27]\n\t"
"v_cvt_pk_bf16_f32 v204, v200, v204\n\t"
"v_cvt_pk_bf16_f32 v202, v202, v206\n\t"
"v_mfma_f32_16x16x32_bf16 v[24:27], a[108:111], a[168:171], v[24:27]\n\t"
"s_waitcnt lgkmcnt(6)\n\t"
"buffer_store_dwordx4 v[156:159], v232, s[16:19], s25 offen nt\n\t"
"v_cvt_pk_bf16_f32 v203, v203, v207\n\t"
"v_mfma_f32_16x16x32_bf16 v[40:43], a[112:115], a[152:155], v[40:43]\n\t"
"ds_read_b128 v[188:191], v231 offset:13312\n\t"
"v_cvt_pk_bf16_f32 v206, v196, v216\n\t"
"v_mfma_f32_16x16x32_bf16 v[40:43], a[116:119], a[168:171], v[40:43]\n\t"
"ds_read_b128 a[164:167], v224 offset:19872\n\t"
"v_cvt_pk_bf16_f32 v207, v197, v217\n\t"
"v_mfma_f32_16x16x32_bf16 v[44:47], a[112:115], a[156:159], v[44:47]\n\t"
"v_cvt_pk_bf16_f32 v200, v198, v218\n\t"
"v_cvt_pk_bf16_f32 v205, v201, v205\n\t"
"v_mfma_f32_16x16x32_bf16 v[44:47], a[116:119], a[172:175], v[44:47]\n\t"
"s_waitcnt lgkmcnt(2)\n\t"
"ds_write_addtid_b32 v213 offset:5152\n\t"
"buffer_load_dwordx4 a[72:75], off, s[96:99], s63 offset:2048 sc1\n\t"
"v_mfma_f32_4x4x4_16b_bf16 v[220:223], a[210:211], a[200:201], 0\n\t"
"ds_write_addtid_b32 v214 offset:12448\n\t"
"v_mfma_f32_4x4x4_16b_bf16 v[212:215], a[210:211], a[202:203], 0\n\t"
"v_cvt_pk_bf16_f32 v201, v199, v219\n\t"
"v_mfma_f32_4x4x4_16b_bf16 v[196:199], a[210:211], a[204:205], 0\n\t"
"buffer_store_dwordx4 v[172:175], v233, s[16:19], s25 offen nt\n\t"
"v_mfma_f32_4x4x4_16b_bf16 v[216:219], a[210:211], a[206:207], 0\n\t"
"s_nop 0\n\t"
"ds_write_addtid_b32 v205 offset:3072\n\t"
"ds_write_addtid_b32 v207 offset:11392\n\t"
"v_mfma_f32_16x16x32_bf16 v[28:31], a[104:107], a[156:159], v[28:31]\n\t"
"s_nop 0\n\t"
"ds_write_addtid_b32 v204 offset:2048\n\t"
"ds_write_addtid_b32 v206 offset:10368\n\t"
"v_mfma_f32_16x16x32_bf16 v[28:31], a[108:111], a[172:175], v[28:31]\n\t"
"s_waitcnt lgkmcnt(7)\n\t"
"buffer_store_dwordx4 v[188:191], v234, s[16:19], s25 offen nt\n\t"
"v_mfma_f32_16x16x32_bf16 v[12:15], a[96:99], a[156:159], v[12:15]\n\t"
"v_cvt_pk_bf16_f32 v222, v222, v214\n\t"
"v_cvt_pk_bf16_f32 v213, v221, v213\n\t"
"v_mfma_f32_16x16x32_bf16 v[12:15], a[100:103], a[172:175], v[12:15]\n\t"
"v_cvt_pk_bf16_f32 v212, v220, v212\n\t"
"v_cvt_pk_bf16_f32 v223, v223, v215\n\t"
"v_mfma_f32_16x16x32_bf16 v[96:99], a[120:123], a[144:147], v[96:99]\n\t"
"ds_write_addtid_b32 v213 offset:7200\n\t"
"v_cvt_pk_bf16_f32 v214, v196, v216\n\t"
"v_mfma_f32_16x16x32_bf16 v[96:99], a[124:127], a[160:163], v[96:99]\n\t"
"ds_write_addtid_b32 v214 offset:14496\n\t"
"v_cvt_pk_bf16_f32 v215, v197, v217\n\t"
"v_mfma_f32_16x16x32_bf16 v[112:115], a[128:131], a[144:147], v[112:115]\n\t"
"s_nop 0\n\t"
"ds_write_addtid_b32 v212 offset:6176\n\t"
"ds_write_addtid_b32 v215 offset:15520\n\t"
"v_mfma_f32_16x16x32_bf16 v[112:115], a[132:135], a[160:163], v[112:115]\n\t"
"buffer_load_dwordx4 a[76:79], off, s[96:99], s63 offset:3072 sc1\n\t"
"v_mfma_f32_16x16x32_bf16 v[128:131], a[136:139], a[144:147], v[128:131]\n\t"
"ds_read_b128 a[152:155], v224 offset:18080\n\t"
"v_cvt_pk_bf16_f32 v221, v199, v219\n\t"
"v_mfma_f32_16x16x32_bf16 v[128:131], a[140:143], a[160:163], v[128:131]\n\t"
"s_waitcnt lgkmcnt(7)\n\t"
"ds_read_b128 a[172:175], v224 offset:20384\n\t"
"v_mfma_f32_16x16x32_bf16 v[132:135], a[136:139], a[148:151], v[132:135]\n\t"
"s_nop 0\n\t"
"ds_read_b128 a[168:171], v224 offset:20128\n\t"
"ds_read_b128 a[156:159], v224 offset:18336\n\t"
"v_mfma_f32_16x16x32_bf16 v[132:135], a[140:143], a[164:167], v[132:135]\n\t"
"buffer_load_dwordx4 a[112:115], off, s[92:95], s47 offset:0 sc1\n\t"
"v_mfma_f32_16x16x32_bf16 v[116:119], a[128:131], a[148:151], v[116:119]\n\t"
"v_cvt_pk_bf16_f32 v220, v198, v218\n\t"
"v_mfma_f32_16x16x32_bf16 v[116:119], a[132:135], a[164:167], v[116:119]\n\t"
"buffer_load_dwordx4 a[116:119], off, s[92:95], s47 offset:1024 sc1\n\t"
"v_mfma_f32_16x16x32_bf16 v[100:103], a[120:123], a[148:151], v[100:103]\n\t"
"s_waitcnt lgkmcnt(0)\n\t"
"v_accvgpr_read_b32 v224, a213\n\t"
"v_mfma_f32_16x16x32_bf16 v[100:103], a[124:127], a[164:167], v[100:103]\n\t"
"buffer_load_dwordx4 a[108:111], off, s[96:99], s46 offset:3072 sc1\n\t"
"s_barrier\n\t"
"s_add_u32 s71, s71, 1\n\t"
"v_mov_b32 v215, s71\n\t"
"s_mov_b64 exec, 1\n\t"
"buffer_store_dword v215, off, s[8:11], s70 sc1\n\t"
"s_mov_b64 exec, -1\n\t"
"s_sub_u32 s21, s21, 1\n\t"
"s_xor_b64 s[84:85], s[84:85], s[88:89]\n\t"
"s_add_u32 s28, s84, 4096\n\t"
"s_addc_u32 s29, s85, 0\n\t"
"s_branch Lunit_%=\n\t"
"Lfb_%=:\n\t"
"v_mbcnt_lo_u32_b32 v162, -1, 0\n\t"
"v_mbcnt_hi_u32_b32 v162, -1, v162\n\t"
"v_bfe_u32 v161, v162, 1, 2\n\t"
"v_lshlrev_b32 v163, 4, v162\n\t"
"v_and_or_b32 v161, v163, 16, v161\n\t"
"v_mul_u32_u24 v161, 0xc0, v161\n\t"
"v_lshrrev_b32 v163, 5, v162\n\t"
"v_mul_u32_u24 v163, 12, v163\n\t"
"v_bfe_u32 v164, v162, 3, 2\n\t"
"v_add3_u32 v161, v161, v163, v164\n\t"
"v_lshlrev_b32 v161, 1, v161\n\t"
"v_lshlrev_b32 v162, 1, v162\n\t"
"v_mov_b32 v152, 0\n\t"
"v_mov_b32 v153, 0\n\t"
"v_mov_b32 v234, 0\n\t"
"v_mov_b32 v233, 0\n\t"
"v_mov_b32 v156, 0\n\t"
"v_mov_b32 v232, 0\n\t"
"v_mov_b32 v231, 0\n\t"
"v_mov_b32 v159, 0\n\t"
"v_mov_b32 v160, 0\n\t"
"s_mov_b32 s67, 0\n\t"
"Lfb0_%=:\n\t"
"s_mov_b32 s69, 0\n\t"
"Lfb1_%=:\n\t"
"s_lshr_b32 s60, s69, 5\n\t"
"s_mul_i32 s56, s60, 12288\n\t"
"s_bfe_u32 s91, s69, 0x20001\n\t"
"s_mul_i32 s91, s91, 1536\n\t"
"s_add_u32 s56, s56, s91\n\t"
"s_bitcmp1_b32 s69, 0\n\t"
"s_cselect_b32 s91, 48, 0\n\t"
"s_add_u32 s56, s56, s91\n\t"
"s_bfe_u32 s90, s69, 0x20003\n\t"
"s_mul_i32 s91, s90, 96\n\t"
"s_add_u32 s56, s56, s91\n\t"
"s_lshl3_add_u32 s56, s67, s56\n\t"
"s_lshr_b32 s60, s69, 5\n\t"
"s_bfe_u32 s90, s69, 0x20003\n\t"
"s_and_b32 s91, s60, 1\n\t"
"s_lshl1_add_u32 s91, s67, s91\n\t"
"s_mul_i32 s90, s90, 288\n\t"
"s_add_u32 s91, s91, s90\n\t"
"s_lshr_b32 s60, s60, 1\n\t"
"s_mul_i32 s90, s60, 96\n\t"
"s_add_u32 s91, s91, s90\n\t"
"s_lshl_b32 s57, s91, 10\n\t"
"s_and_b32 s91, s69, 7\n\t"
"s_lshl_b32 s91, s91, 7\n\t"
"s_add_u32 s57, s57, s91\n\t"
"s_add_u32 s60, s56, 0\n\t"
"buffer_load_short_d16_hi v152, v161, s[52:55], s60 offen\n\t"
"s_add_u32 s60, s56, 73728\n\t"
"buffer_load_short_d16_hi v153, v161, s[52:55], s60 offen\n\t"
"s_add_u32 s60, s56, 147456\n\t"
"buffer_load_short_d16_hi v234, v161, s[52:55], s60 offen\n\t"
"s_add_u32 s60, s56, 221184\n\t"
"buffer_load_short_d16_hi v233, v161, s[52:55], s60 offen\n\t"
"s_add_u32 s60, s56, 294912\n\t"
"buffer_load_short_d16_hi v156, v161, s[52:55], s60 offen\n\t"
"s_add_u32 s60, s56, 368640\n\t"
"buffer_load_short_d16_hi v232, v161, s[52:55], s60 offen\n\t"
"s_add_u32 s60, s56, 442368\n\t"
"buffer_load_short_d16_hi v231, v161, s[52:55], s60 offen\n\t"
"s_add_u32 s60, s56, 516096\n\t"
"buffer_load_short_d16_hi v159, v161, s[52:55], s60 offen\n\t"
"s_add_u32 s60, s56, 589824\n\t"
"buffer_load_short_d16_hi v160, v161, s[52:55], s60 offen\n\t"
"s_waitcnt vmcnt(2)\n\t"
"v_add_f32 v168, v152, v231\n\t"
"s_waitcnt vmcnt(1)\n\t"
"v_add_f32 v169, v153, v159\n\t"
"s_waitcnt vmcnt(0)\n\t"
"v_add_f32 v170, v234, v160\n\t"
"v_add_f32 v171, v168, v233\n\t"
"v_sub_f32 v174, v168, v233\n\t"
"v_add_f32 v172, v169, v156\n\t"
"v_sub_f32 v175, v169, v156\n\t"
"v_add_f32 v173, v170, v232\n\t"
"v_sub_f32 v228, v170, v232\n\t"
"v_add_f32 v177, v152, v234\n\t"
"v_add_f32 v178, v171, v173\n\t"
"v_add_f32 v179, v174, v228\n\t"
"v_add_f32 v180, v231, v160\n\t"
"v_add_f32 v182, v177, v153\n\t"
"v_sub_f32 v183, v177, v153\n\t"
"v_add_f32 v184, v178, v172\n\t"
"v_sub_f32 v185, v178, v172\n\t"
"v_add_f32 v186, v179, v175\n\t"
"v_sub_f32 v187, v179, v175\n\t"
"v_add_f32 v188, v180, v159\n\t"
"v_sub_f32 v189, v180, v159\n\t"
"v_mul_f32 v191, 0.5, v182\n\t"
"v_mul_f32 v225, 0.5, v183\n\t"
"v_mul_f32 v222, 0.5, v171\n\t"
"v_mul_f32 v221, 0x3e800000, v184\n\t"
"v_mul_f32 v220, 0x3e800000, v185\n\t"
"v_mul_f32 v219, -0.5, v173\n\t"
"v_mul_f32 v217, 0.5, v186\n\t"
"v_mul_f32 v216, 0.5, v187\n\t"
"v_mul_f32 v151, -0.5, v188\n\t"
"v_mul_f32 v204, -0.5, v189\n\t"
"v_cvt_pk_bf16_f32 v206, v152, v191\n\t"
"v_cvt_pk_bf16_f32 v207, v225, -v234\n\t"
"v_cvt_pk_bf16_f32 v208, v222, v221\n\t"
"v_cvt_pk_bf16_f32 v209, v220, v219\n\t"
"v_cvt_pk_bf16_f32 v210, v174, v217\n\t"
"v_cvt_pk_bf16_f32 v211, v216, -v228\n\t"
"v_cvt_pk_bf16_f32 v212, -v231, v151\n\t"
"v_cvt_pk_bf16_f32 v213, v204, v160\n\t"
"s_add_u32 s60, s57, 0\n\t"
"buffer_store_short v206, v162, s[8:11], s60 offen sc1\n\t"
"s_add_u32 s60, s57, 6144\n\t"
"buffer_store_short_d16_hi v206, v162, s[8:11], s60 offen sc1\n\t"
"s_add_u32 s60, s57, 12288\n\t"
"buffer_store_short v207, v162, s[8:11], s60 offen sc1\n\t"
"s_add_u32 s60, s57, 18432\n\t"
"buffer_store_short_d16_hi v207, v162, s[8:11], s60 offen sc1\n\t"
"s_add_u32 s60, s57, 24576\n\t"
"buffer_store_short v208, v162, s[8:11], s60 offen sc1\n\t"
"s_add_u32 s60, s57, 30720\n\t"
"buffer_store_short_d16_hi v208, v162, s[8:11], s60 offen sc1\n\t"
"s_add_u32 s60, s57, 36864\n\t"
"buffer_store_short v209, v162, s[8:11], s60 offen sc1\n\t"
"s_add_u32 s60, s57, 43008\n\t"
"buffer_store_short_d16_hi v209, v162, s[8:11], s60 offen sc1\n\t"
"s_add_u32 s60, s57, 49152\n\t"
"buffer_store_short v210, v162, s[8:11], s60 offen sc1\n\t"
"s_add_u32 s60, s57, 55296\n\t"
"buffer_store_short_d16_hi v210, v162, s[8:11], s60 offen sc1\n\t"
"s_add_u32 s60, s57, 61440\n\t"
"buffer_store_short v211, v162, s[8:11], s60 offen sc1\n\t"
"s_add_u32 s60, s57, 67584\n\t"
"buffer_store_short_d16_hi v211, v162, s[8:11], s60 offen sc1\n\t"
"s_add_u32 s60, s57, 73728\n\t"
"buffer_store_short v212, v162, s[8:11], s60 offen sc1\n\t"
"s_add_u32 s60, s57, 79872\n\t"
"buffer_store_short_d16_hi v212, v162, s[8:11], s60 offen sc1\n\t"
"s_add_u32 s60, s57, 86016\n\t"
"buffer_store_short v213, v162, s[8:11], s60 offen sc1\n\t"
"s_add_u32 s60, s57, 92160\n\t"
"buffer_store_short_d16_hi v213, v162, s[8:11], s60 offen sc1\n\t"
"s_waitcnt vmcnt(0)\n\t"
"s_add_u32 s69, s69, 1\n\t"
"s_cmp_lt_u32 s69, 192\n\t"
"s_cbranch_scc1 Lfb1_%=\n\t"
"s_add_u32 s67, s67, 1\n\t"
"s_cmp_lt_u32 s67, 3\n\t"
"s_cbranch_scc1 Lfb0_%=\n\t"
"s_branch Lgo_%=\n\t"
".p2align 6\n\t.fill 448, 4, 0xbf800000\n\t"
"Lpad10_%=:\n\t"
"s_cbranch_execz Lret1_%=\n\t"
"s_nop 0\n\t"
"Ltail_%=:\n\t"
"v_mfma_f32_16x16x32_bf16 v[104:107], a[72:75], a[152:155], v[104:107]\n\t"
"s_nop 0\n\t"
"ds_read_b128 a[144:147], v224 offset:17568\n\t"
"ds_read_b128 a[160:163], v224 offset:19616\n\t"
"v_mfma_f32_16x16x32_bf16 v[104:107], a[76:79], a[168:171], v[104:107]\n\t"
"buffer_load_dwordx4 a[20:23], off, s[28:31], s69 offset:1024 sc1\n\t"
"v_mfma_f32_16x16x32_bf16 v[120:123], a[80:83], a[152:155], v[120:123]\n\t"
"ds_read_b128 a[148:151], v224 offset:17824\n\t"
"v_add_u32 v248, 33280, v226\n\t"
"v_mfma_f32_16x16x32_bf16 v[120:123], a[84:87], a[168:171], v[120:123]\n\t"
"ds_read_b128 a[164:167], v224 offset:19872\n\t"
"v_xor_b32 v249, 32, v248\n\t"
"v_mfma_f32_16x16x32_bf16 v[136:139], a[88:91], a[152:155], v[136:139]\n\t"
"ds_read_b128 a[152:155], v224 offset:18080\n\t"
"v_mfma_f32_16x16x32_bf16 v[136:139], a[92:95], a[168:171], v[136:139]\n\t"
"buffer_load_dwordx4 a[12:15], off, s[84:87], s69 offset:3072 sc1\n\t"
"v_mfma_f32_16x16x32_bf16 v[140:143], a[88:91], a[156:159], v[140:143]\n\t"
"ds_read_b128 v[8:11], v229 offset:20480\n\t"
"v_mfma_f32_16x16x32_bf16 v[140:143], a[92:95], a[172:175], v[140:143]\n\t"
"buffer_load_dwordx4 a[8:11], off, s[84:87], s69 offset:2048 sc1\n\t"
"v_mfma_f32_16x16x32_bf16 v[124:127], a[80:83], a[156:159], v[124:127]\n\t"
"ds_read_b128 a[168:171], v224 offset:20128\n\t"
"v_xor_b32 v250, 64, v248\n\t"
"v_mfma_f32_16x16x32_bf16 v[124:127], a[84:87], a[172:175], v[124:127]\n\t"
"buffer_load_dwordx4 a[0:3], off, s[84:87], s69 offset:0 sc1\n\t"
"v_mfma_f32_16x16x32_bf16 v[108:111], a[72:75], a[156:159], v[108:111]\n\t"
"ds_read_b128 a[156:159], v224 offset:18336\n\t"
"v_xor_b32 v251, 96, v248\n\t"
"v_mfma_f32_16x16x32_bf16 v[108:111], a[76:79], a[172:175], v[108:111]\n\t"
"buffer_load_dwordx4 a[4:7], off, s[84:87], s69 offset:1024 sc1\n\t"
"v_mfma_f32_16x16x32_bf16 v[192:195], a[224:227], a[176:179], 0\n\t"
"s_nop 0\n\t"
"ds_read_b64_tr_b16 a[178:179], v248 offset:20864\n\t"
"ds_read_b64_tr_b16 a[176:177], v248 offset:20736\n\t"
"v_mfma_f32_16x16x32_bf16 v[196:199], a[224:227], a[180:183], 0\n\t"
"ds_read_b64_tr_b16 a[180:181], v249 offset:20992\n\t"
"v_mfma_f32_16x16x32_bf16 v[200:203], a[224:227], a[184:187], 0\n\t"
"s_nop 0\n\t"
"ds_read_b64_tr_b16 a[186:187], v249 offset:20864\n\t"
"ds_read_b64_tr_b16 a[184:185], v249 offset:20736\n\t"
"v_mfma_f32_16x16x32_bf16 v[204:207], a[224:227], a[188:191], 0\n\t"
"s_waitcnt lgkmcnt(8)\n\t"
"s_waitcnt lgkmcnt(7)\n\t"
"buffer_store_dwordx4 v[8:11], v232, s[16:19], s24 offen nt\n\t"
"v_mfma_f32_16x16x32_bf16 v[208:211], a[224:227], a[192:195], 0\n\t"
"ds_read_b128 v[24:27], v230 offset:20480\n\t"
"v_mfma_f32_16x16x32_bf16 v[212:215], a[224:227], a[196:199], 0\n\t"
"ds_read_b64_tr_b16 a[188:189], v248 offset:20992\n\t"
"v_mfma_f32_16x16x32_bf16 v[216:219], a[224:227], a[200:203], 0\n\t"
"s_nop 0\n\t"
"ds_read_b64_tr_b16 a[194:195], v250 offset:20864\n\t"
"ds_read_b64_tr_b16 a[192:193], v250 offset:20736\n\t"
"v_mfma_f32_16x16x32_bf16 v[220:223], a[224:227], a[204:207], 0\n\t"
"s_waitcnt vmcnt(18) lgkmcnt(10)\n\t"
"buffer_load_dwordx4 a[40:43], off, s[28:31], s54 offset:0 sc1\n\t"
"v_mfma_f32_16x16x32_bf16 v[144:147], a[96:99], a[144:147], v[144:147]\n\t"
"ds_read_b64_tr_b16 a[196:197], v251 offset:20992\n\t"
"v_cvt_pk_bf16_f32 v195, v195, v199\n\t"
"v_mfma_f32_16x16x32_bf16 v[144:147], a[100:103], a[160:163], v[144:147]\n\t"
"ds_read_b128 a[172:175], v224 offset:20384\n\t"
"v_cvt_pk_bf16_f32 v194, v194, v198\n\t"
"v_mfma_f32_16x16x32_bf16 v[160:163], a[104:107], a[144:147], v[160:163]\n\t"
"ds_read_b64_tr_b16 a[202:203], v251 offset:20864\n\t"
"v_cvt_pk_bf16_f32 v200, v200, v204\n\t"
"v_mfma_f32_16x16x32_bf16 v[160:163], a[108:111], a[160:163], v[160:163]\n\t"
"buffer_load_dwordx4 a[44:47], off, s[28:31], s54 offset:1024 sc1\n\t"
"v_mfma_f32_16x16x32_bf16 v[176:179], a[112:115], a[144:147], v[176:179]\n\t"
"ds_read_b128 v[12:15], v229 offset:33792\n\t"
"v_cvt_pk_bf16_f32 v209, v209, v213\n\t"
"v_mfma_f32_16x16x32_bf16 v[176:179], a[116:119], a[160:163], v[176:179]\n\t"
"ds_read_b128 a[144:147], v224 offset:16544\n\t"
"v_cvt_pk_bf16_f32 v193, v193, v197\n\t"
"v_mfma_f32_16x16x32_bf16 v[180:183], a[112:115], a[148:151], v[180:183]\n\t"
"ds_read_b128 a[160:163], v224 offset:18592\n\t"
"v_cvt_pk_bf16_f32 v216, v216, v220\n\t"
"v_mfma_f32_16x16x32_bf16 v[180:183], a[116:119], a[164:167], v[180:183]\n\t"
"s_waitcnt lgkmcnt(8)\n\t"
"buffer_store_dwordx4 v[24:27], v233, s[16:19], s24 offen nt\n\t"
"v_mfma_f32_16x16x32_bf16 v[164:167], a[104:107], a[148:151], v[164:167]\n\t"
"ds_write_addtid_b32 v200 offset:2048\n\t"
"v_cvt_pk_bf16_f32 v217, v217, v221\n\t"
"v_mfma_f32_16x16x32_bf16 v[164:167], a[108:111], a[164:167], v[164:167]\n\t"
"s_nop 0\n\t"
"ds_write_addtid_b32 v193 offset:8320\n\t"
"ds_write_addtid_b32 v209 offset:9344\n\t"
"v_mfma_f32_16x16x32_bf16 v[148:151], a[96:99], a[148:151], v[148:151]\n\t"
"ds_read_b128 v[40:43], v231 offset:20480\n\t"
"v_cvt_pk_bf16_f32 v201, v201, v205\n\t"
"v_mfma_f32_16x16x32_bf16 v[148:151], a[100:103], a[164:167], v[148:151]\n\t"
"buffer_load_dwordx4 a[36:39], off, s[84:87], s54 offset:3072 sc1\n\t"
"v_mfma_f32_16x16x32_bf16 v[152:155], a[96:99], a[152:155], v[152:155]\n\t"
"ds_read_b128 v[28:31], v230 offset:33792\n\t"
"v_cvt_pk_bf16_f32 v192, v192, v196\n\t"
"v_mfma_f32_16x16x32_bf16 v[152:155], a[100:103], a[168:171], v[152:155]\n\t"
"ds_read_b128 a[148:151], v224 offset:16800\n\t"
"v_cvt_pk_bf16_f32 v210, v210, v214\n\t"
"v_mfma_f32_16x16x32_bf16 v[168:171], a[104:107], a[152:155], v[168:171]\n\t"
"ds_read_b128 a[164:167], v224 offset:18848\n\t"
"v_cvt_pk_bf16_f32 v219, v219, v223\n\t"
"v_mfma_f32_16x16x32_bf16 v[168:171], a[108:111], a[168:171], v[168:171]\n\t"
"buffer_load_dwordx4 a[32:35], off, s[84:87], s54 offset:2048 sc1\n\t"
"v_mfma_f32_16x16x32_bf16 v[184:187], a[112:115], a[152:155], v[184:187]\n\t"
"ds_read_b128 a[152:155], v224 offset:17056\n\t"
"v_cvt_pk_bf16_f32 v202, v202, v206\n\t"
"v_mfma_f32_16x16x32_bf16 v[184:187], a[116:119], a[168:171], v[184:187]\n\t"
"s_waitcnt lgkmcnt(8)\n\t"
"ds_read_b128 v[44:47], v231 offset:33792\n\t"
"v_cvt_pk_bf16_f32 v208, v208, v212\n\t"
"v_mfma_f32_16x16x32_bf16 v[188:191], a[112:115], a[156:159], v[188:191]\n\t"
"ds_write_addtid_b32 v195 offset:12448\n\t"
"v_cvt_pk_bf16_f32 v203, v203, v207\n\t"
"v_mfma_f32_16x16x32_bf16 v[188:191], a[116:119], a[172:175], v[188:191]\n\t"
"ds_write_addtid_b32 v194 offset:4128\n\t"
"v_cvt_pk_bf16_f32 v211, v211, v215\n\t"
"v_mfma_f32_16x16x32_bf16 v[172:175], a[104:107], a[156:159], v[172:175]\n\t"
"ds_write_addtid_b32 v201 offset:10368\n\t"
"v_cvt_pk_bf16_f32 v218, v218, v222\n\t"
"v_mfma_f32_16x16x32_bf16 v[172:175], a[108:111], a[172:175], v[172:175]\n\t"
"s_nop 0\n\t"
"ds_write_addtid_b32 v218 offset:7200\n\t"
"ds_write_addtid_b32 v219 offset:15520\n\t"
"v_mfma_f32_16x16x32_bf16 v[156:159], a[96:99], a[156:159], v[156:159]\n\t"
"s_nop 0\n\t"
"ds_write_addtid_b32 v202 offset:6176\n\t"
"ds_write_addtid_b32 v210 offset:5152\n\t"
"v_mfma_f32_16x16x32_bf16 v[156:159], a[100:103], a[172:175], v[156:159]\n\t"
"s_waitcnt vmcnt(12) lgkmcnt(10)\n\t"
"buffer_store_dwordx4 v[40:43], v234, s[16:19], s24 offen nt\n\t"
"v_mfma_f32_16x16x32_bf16 v[96:99], a[120:123], a[144:147], v[96:99]\n\t"
"s_nop 0\n\t"
"ds_write_addtid_b32 v192 offset:0\n\t"
"ds_write_addtid_b32 v211 offset:13472\n\t"
"v_mfma_f32_16x16x32_bf16 v[96:99], a[124:127], a[160:163], v[96:99]\n\t"
"s_nop 0\n\t"
"ds_write_addtid_b32 v203 offset:14496\n\t"
"ds_write_addtid_b32 v216 offset:3072\n\t"
"v_mfma_f32_16x16x32_bf16 v[112:115], a[128:131], a[144:147], v[112:115]\n\t"
"s_nop 0\n\t"
"ds_write_addtid_b32 v208 offset:1024\n\t"
"ds_write_addtid_b32 v217 offset:11392\n\t"
"v_mfma_f32_16x16x32_bf16 v[112:115], a[132:135], a[160:163], v[112:115]\n\t"
"buffer_load_dwordx4 a[24:27], off, s[84:87], s54 offset:0 sc1\n\t"
"v_mfma_f32_16x16x32_bf16 v[128:131], a[136:139], a[144:147], v[128:131]\n\t"
"ds_read_b128 a[172:175], v224 offset:19360\n\t"
"v_mfma_f32_16x16x32_bf16 v[128:131], a[140:143], a[160:163], v[128:131]\n\t"
"s_waitcnt lgkmcnt(12)\n\t"
"ds_read_b128 a[168:171], v224 offset:19104\n\t"
"v_mfma_f32_16x16x32_bf16 v[132:135], a[136:139], a[148:151], v[132:135]\n\t"
"ds_read_b128 a[156:159], v224 offset:17312\n\t"
"v_mfma_f32_16x16x32_bf16 v[132:135], a[140:143], a[164:167], v[132:135]\n\t"
"buffer_load_dwordx4 a[28:31], off, s[84:87], s54 offset:1024 sc1\n\t"
"v_mfma_f32_16x16x32_bf16 v[116:119], a[128:131], a[148:151], v[116:119]\n\t"
"s_nop 0\n\t"
"ds_read_b64_tr_b16 a[200:201], v251 offset:20736\n\t"
"ds_read_b64_tr_b16 a[204:205], v250 offset:20992\n\t"
"v_mfma_f32_16x16x32_bf16 v[116:119], a[132:135], a[164:167], v[116:119]\n\t"
"v_mfma_f32_16x16x32_bf16 v[100:103], a[120:123], a[148:151], v[100:103]\n\t"
"buffer_load_dwordx4 a[64:67], off, s[28:31], s57 offset:0 sc1\n\t"
"v_mfma_f32_16x16x32_bf16 v[100:103], a[124:127], a[164:167], v[100:103]\n\t"
"s_waitcnt lgkmcnt(0)\n\t"
"s_barrier\n\t"
"v_mfma_f32_16x16x32_bf16 v[104:107], a[120:123], a[152:155], v[104:107]\n\t"
"s_nop 0\n\t"
"ds_read_b128 a[144:147], v224 offset:0\n\t"
"ds_read_b128 a[148:151], v224 offset:256\n\t"
"v_mfma_f32_16x16x32_bf16 v[104:107], a[124:127], a[168:171], v[104:107]\n\t"
"buffer_store_dwordx4 v[12:15], v232, s[16:19], s27 offen nt\n\t"
"v_mfma_f32_16x16x32_bf16 v[120:123], a[128:131], a[152:155], v[120:123]\n\t"
"ds_read_b128 a[160:163], v224 offset:2048\n\t"
"v_mfma_f32_16x16x32_bf16 v[120:123], a[132:135], a[168:171], v[120:123]\n\t"
"ds_read_b128 a[164:167], v224 offset:2304\n\t"
"v_mfma_f32_16x16x32_bf16 v[136:139], a[136:139], a[152:155], v[136:139]\n\t"
"v_mfma_f32_16x16x32_bf16 v[136:139], a[140:143], a[168:171], v[136:139]\n\t"
"buffer_load_dwordx4 a[68:71], off, s[28:31], s57 offset:1024 sc1\n\t"
"v_mfma_f32_16x16x32_bf16 v[140:143], a[136:139], a[156:159], v[140:143]\n\t"
"ds_read_b128 a[168:171], v224 offset:2560\n\t"
"v_mfma_f32_16x16x32_bf16 v[140:143], a[140:143], a[172:175], v[140:143]\n\t"
"ds_read_b128 a[152:155], v224 offset:512\n\t"
"v_mfma_f32_16x16x32_bf16 v[124:127], a[128:131], a[156:159], v[124:127]\n\t"
"v_mfma_f32_16x16x32_bf16 v[124:127], a[132:135], a[172:175], v[124:127]\n\t"
"v_mfma_f32_16x16x32_bf16 v[108:111], a[120:123], a[156:159], v[108:111]\n\t"
"ds_read_b128 a[156:159], v224 offset:768\n\t"
"v_mfma_f32_16x16x32_bf16 v[108:111], a[124:127], a[172:175], v[108:111]\n\t"
"ds_read_b128 a[172:175], v224 offset:2816\n\t"
"buffer_load_dwordx4 a[60:63], off, s[84:87], s57 offset:3072 sc1\n\t"
"v_mfma_f32_4x4x4_16b_bf16 v[200:203], a[220:221], a[178:179], 0\n\t"
"ds_read_b64_tr_b16 a[178:179], v248 offset:128\n\t"
"v_mfma_f32_4x4x4_16b_bf16 v[204:207], a[220:221], a[186:187], 0\n\t"
"ds_read_b64_tr_b16 a[186:187], v249 offset:128\n\t"
"v_mfma_f32_4x4x4_16b_bf16 v[216:219], a[220:221], a[194:195], 0\n\t"
"ds_read_b64_tr_b16 a[194:195], v250 offset:128\n\t"
"v_mfma_f32_4x4x4_16b_bf16 v[220:223], a[220:221], a[202:203], 0\n\t"
"ds_read_b64_tr_b16 a[202:203], v251 offset:128\n\t"
"buffer_load_dwordx4 a[56:59], off, s[84:87], s57 offset:2048 sc1\n\t"
"v_mfma_f32_4x4x4_16b_bf16 v[192:195], a[222:223], a[176:177], v[200:203]\n\t"
"ds_read_b64_tr_b16 a[176:177], v248 offset:0\n\t"
"v_mfma_f32_4x4x4_16b_bf16 v[196:199], a[222:223], a[184:185], v[204:207]\n\t"
"ds_read_b64_tr_b16 a[184:185], v249 offset:0\n\t"
"v_mfma_f32_4x4x4_16b_bf16 v[200:203], a[222:223], a[180:181], v[200:203]\n\t"
".p2align 3\n\t"
"ds_read_b64_tr_b16 a[180:181], v249 offset:256\n\t"
"buffer_store_dwordx4 v[28:31], v233, s[16:19], s27 offen nt\n\t"
"v_pk_add_f32 v[96:97], v[96:97], v[144:145]\n\t"
"v_pk_add_f32 v[98:99], v[98:99], v[146:147]\n\t"
"v_pk_fma_f32 v[144:145], v[144:145], s[38:39], v[96:97]\n\t"
"v_pk_fma_f32 v[146:147], v[146:147], s[38:39], v[98:99]\n\t"
"v_pk_add_f32 v[112:113], v[112:113], v[160:161]\n\t"
"v_pk_add_f32 v[114:115], v[114:115], v[162:163]\n\t"
"v_pk_fma_f32 v[160:161], v[160:161], s[38:39], v[112:113]\n\t"
"v_pk_fma_f32 v[162:163], v[162:163], s[38:39], v[114:115]\n\t"
"v_pk_add_f32 v[128:129], v[128:129], v[176:177]\n\t"
"v_pk_add_f32 v[130:131], v[130:131], v[178:179]\n\t"
"v_pk_fma_f32 v[176:177], v[176:177], s[38:39], v[128:129]\n\t"
"v_pk_fma_f32 v[178:179], v[178:179], s[38:39], v[130:131]\n\t"
"v_pk_add_f32 v[132:133], v[132:133], v[180:181]\n\t"
"v_pk_add_f32 v[134:135], v[134:135], v[182:183]\n\t"
"v_pk_fma_f32 v[180:181], v[180:181], s[38:39], v[132:133]\n\t"
"v_pk_fma_f32 v[182:183], v[182:183], s[38:39], v[134:135]\n\t"
"v_pk_add_f32 v[116:117], v[116:117], v[164:165]\n\t"
"v_pk_add_f32 v[118:119], v[118:119], v[166:167]\n\t"
"v_pk_fma_f32 v[164:165], v[164:165], s[38:39], v[116:117]\n\t"
"v_pk_fma_f32 v[166:167], v[166:167], s[38:39], v[118:119]\n\t"
"v_pk_add_f32 v[100:101], v[100:101], v[148:149]\n\t"
"v_pk_add_f32 v[102:103], v[102:103], v[150:151]\n\t"
"v_pk_fma_f32 v[148:149], v[148:149], s[38:39], v[100:101]\n\t"
"v_pk_fma_f32 v[150:151], v[150:151], s[38:39], v[102:103]\n\t"
"v_pk_add_f32 v[104:105], v[104:105], v[152:153]\n\t"
"v_pk_add_f32 v[106:107], v[106:107], v[154:155]\n\t"
"v_pk_fma_f32 v[152:153], v[152:153], s[38:39], v[104:105]\n\t"
"v_pk_fma_f32 v[154:155], v[154:155], s[38:39], v[106:107]\n\t"
"v_pk_add_f32 v[120:121], v[120:121], v[168:169]\n\t"
"v_pk_add_f32 v[122:123], v[122:123], v[170:171]\n\t"
"v_pk_fma_f32 v[168:169], v[168:169], s[38:39], v[120:121]\n\t"
"v_pk_fma_f32 v[170:171], v[170:171], s[38:39], v[122:123]\n\t"
"v_pk_add_f32 v[136:137], v[136:137], v[184:185]\n\t"
"v_pk_add_f32 v[138:139], v[138:139], v[186:187]\n\t"
"v_pk_fma_f32 v[184:185], v[184:185], s[38:39], v[136:137]\n\t"
"v_pk_fma_f32 v[186:187], v[186:187], s[38:39], v[138:139]\n\t"
"v_pk_add_f32 v[140:141], v[140:141], v[188:189]\n\t"
"v_pk_add_f32 v[142:143], v[142:143], v[190:191]\n\t"
"v_pk_fma_f32 v[188:189], v[188:189], s[38:39], v[140:141]\n\t"
"v_pk_fma_f32 v[190:191], v[190:191], s[38:39], v[142:143]\n\t"
"v_pk_add_f32 v[124:125], v[124:125], v[172:173]\n\t"
"v_pk_add_f32 v[126:127], v[126:127], v[174:175]\n\t"
"v_pk_fma_f32 v[172:173], v[172:173], s[38:39], v[124:125]\n\t"
"v_pk_fma_f32 v[174:175], v[174:175], s[38:39], v[126:127]\n\t"
"v_pk_add_f32 v[108:109], v[108:109], v[156:157]\n\t"
"v_pk_add_f32 v[110:111], v[110:111], v[158:159]\n\t"
"v_pk_fma_f32 v[156:157], v[156:157], s[38:39], v[108:109]\n\t"
"v_pk_fma_f32 v[158:159], v[158:159], s[38:39], v[110:111]\n\t"
"v_cvt_pk_bf16_f32 v116, v116, v117\n\t"
"v_mfma_f32_4x4x4_16b_bf16 v[204:207], a[222:223], a[188:189], v[204:207]\n\t"
"ds_read_b64_tr_b16 a[188:189], v248 offset:256\n\t"
"v_cvt_pk_bf16_f32 v96, v96, v97\n\t"
"v_mfma_f32_4x4x4_16b_bf16 v[208:211], a[222:223], a[192:193], v[216:219]\n\t"
"v_cvt_pk_bf16_f32 v97, v98, v99\n\t"
"v_cvt_pk_bf16_f32 v100, v100, v101\n\t"
"v_cvt_pk_bf16_f32 v101, v102, v103\n\t"
"v_cvt_pk_bf16_f32 v112, v112, v113\n\t"
"v_mfma_f32_4x4x4_16b_bf16 v[212:215], a[222:223], a[200:201], v[220:223]\n\t"
"buffer_load_dwordx4 a[48:51], off, s[84:87], s57 offset:0 sc1\n\t"
"ds_write2st64_b64 v228, v[96:97], v[100:101] offset0:40 offset1:53\n\t"
"v_cvt_pk_bf16_f32 v117, v118, v119\n\t"
"v_mfma_f32_4x4x4_16b_bf16 v[216:219], a[222:223], a[196:197], v[216:219]\n\t"
"v_cvt_pk_bf16_f32 v124, v124, v125\n\t"
"v_cvt_pk_bf16_f32 v125, v126, v127\n\t"
"v_mfma_f32_4x4x4_16b_bf16 v[220:223], a[222:223], a[204:205], v[220:223]\n\t"
"s_waitcnt vmcnt(16) lgkmcnt(7)\n\t"
"v_cvt_pk_bf16_f32 v113, v114, v115\n\t"
"v_cvt_pk_bf16_f32 v140, v140, v141\n\t"
"v_mfma_f32_16x16x32_bf16 v[48:51], a[0:3], a[144:147], v[48:51]\n\t"
"v_cvt_pk_bf16_f32 v192, -v192, -v196\n\t"
"v_cvt_pk_bf16_f32 v120, v120, v121\n\t"
"v_mfma_f32_16x16x32_bf16 v[48:51], a[4:7], a[160:163], v[48:51]\n\t"
"ds_write_addtid_b32 v192 offset:16544\n\t"
"buffer_load_dwordx4 a[52:55], off, s[84:87], s57 offset:1024 sc1\n\t"
"ds_write2st64_b64 v246, v[112:113], v[116:117] offset0:40 offset1:53\n\t"
"v_cvt_pk_bf16_f32 v136, v136, v137\n\t"
"v_mfma_f32_16x16x32_bf16 v[64:67], a[8:11], a[144:147], v[64:67]\n\t"
"v_cvt_pk_bf16_f32 v104, v104, v105\n\t"
"v_cvt_pk_bf16_f32 v108, v108, v109\n\t"
"v_mfma_f32_16x16x32_bf16 v[64:67], a[12:15], a[160:163], v[64:67]\n\t"
"v_cvt_pk_bf16_f32 v128, v128, v129\n\t"
"v_cvt_pk_bf16_f32 v132, v132, v133\n\t"
"v_cvt_pk_bf16_f32 v133, v134, v135\n\t"
"v_mfma_f32_16x16x32_bf16 v[80:83], a[16:19], a[144:147], v[80:83]\n\t"
"v_cvt_pk_bf16_f32 v203, v203, v207\n\t"
"v_cvt_pk_bf16_f32 v129, v130, v131\n\t"
"v_mfma_f32_16x16x32_bf16 v[80:83], a[20:23], a[160:163], v[80:83]\n\t"
"s_nop 0\n\t"
"ds_read_b128 a[144:147], v224 offset:1024\n\t"
"ds_read_b128 a[160:163], v224 offset:3072\n\t"
"ds_write2st64_b64 v247, v[128:129], v[132:133] offset0:40 offset1:53\n\t"
"v_mfma_f32_16x16x32_bf16 v[84:87], a[16:19], a[148:151], v[84:87]\n\t"
"v_cvt_pk_bf16_f32 v105, v106, v107\n\t"
"v_cvt_pk_bf16_f32 v109, v110, v111\n\t"
"v_mfma_f32_16x16x32_bf16 v[84:87], a[20:23], a[164:167], v[84:87]\n\t"
"buffer_load_dwordx4 a[88:91], off, s[28:31], s63 offset:0 sc1\n\t"
"v_mfma_f32_16x16x32_bf16 v[68:71], a[8:11], a[148:151], v[68:71]\n\t"
"v_cvt_pk_bf16_f32 v195, -v195, -v199\n\t"
"v_cvt_pk_bf16_f32 v217, v217, v221\n\t"
"v_mfma_f32_16x16x32_bf16 v[68:71], a[12:15], a[164:167], v[68:71]\n\t"
"ds_write_addtid_b32 v203 offset:30016\n\t"
"ds_write2st64_b64 v228, v[104:105], v[108:109] offset0:66 offset1:79\n\t"
"v_mfma_f32_16x16x32_bf16 v[52:55], a[0:3], a[148:151], v[52:55]\n\t"
"v_cvt_pk_bf16_f32 v137, v138, v139\n\t"
"v_cvt_pk_bf16_f32 v141, v142, v143\n\t"
"v_mfma_f32_16x16x32_bf16 v[52:55], a[4:7], a[164:167], v[52:55]\n\t"
"buffer_store_dwordx4 v[44:47], v234, s[16:19], s27 offen nt\n\t"
"v_mfma_f32_16x16x32_bf16 v[56:59], a[0:3], a[152:155], v[56:59]\n\t"
"v_cvt_pk_bf16_f32 v211, -v211, -v215\n\t"
"v_cvt_pk_bf16_f32 v121, v122, v123\n\t"
"v_mfma_f32_16x16x32_bf16 v[56:59], a[4:7], a[168:171], v[56:59]\n\t"
"s_nop 0\n\t"
"ds_read_b128 a[148:151], v224 offset:1280\n\t"
"ds_read_b128 a[164:167], v224 offset:3328\n\t"
"ds_write2st64_b64 v247, v[136:137], v[140:141] offset0:66 offset1:79\n\t"
"v_mfma_f32_16x16x32_bf16 v[72:75], a[8:11], a[152:155], v[72:75]\n\t"
"v_cvt_pk_bf16_f32 v208, -v208, -v212\n\t"
"v_cvt_pk_bf16_f32 v210, -v210, -v214\n\t"
"v_mfma_f32_16x16x32_bf16 v[72:75], a[12:15], a[168:171], v[72:75]\n\t"
"buffer_load_dwordx4 a[92:95], off, s[28:31], s63 offset:1024 sc1\n\t"
"v_mfma_f32_16x16x32_bf16 v[88:91], a[16:19], a[152:155], v[88:91]\n\t"
"v_cvt_pk_bf16_f32 v200, v200, v204\n\t"
"v_cvt_pk_bf16_f32 v202, v202, v206\n\t"
"v_mfma_f32_16x16x32_bf16 v[88:91], a[20:23], a[168:171], v[88:91]\n\t"
"ds_write_addtid_b32 v208 offset:18592\n\t"
"ds_write2st64_b64 v246, v[120:121], v[124:125] offset0:66 offset1:79\n\t"
"v_mfma_f32_16x16x32_bf16 v[92:95], a[16:19], a[156:159], v[92:95]\n\t"
"v_cvt_pk_bf16_f32 v193, -v193, -v197\n\t"
"v_cvt_pk_bf16_f32 v201, v201, v205\n\t"
"v_mfma_f32_16x16x32_bf16 v[92:95], a[20:23], a[172:175], v[92:95]\n\t"
"buffer_load_dwordx4 a[84:87], off, s[84:87], s63 offset:3072 sc1\n\t"
"v_mfma_f32_16x16x32_bf16 v[76:79], a[8:11], a[156:159], v[76:79]\n\t"
"v_cvt_pk_bf16_f32 v216, v216, v220\n\t"
"v_cvt_pk_bf16_f32 v219, v219, v223\n\t"
"v_mfma_f32_16x16x32_bf16 v[76:79], a[12:15], a[172:175], v[76:79]\n\t"
"s_nop 0\n\t"
"ds_write_addtid_b32 v193 offset:20672\n\t"
"ds_write_addtid_b32 v210 offset:19616\n\t"
"ds_write_addtid_b32 v211 offset:23744\n\t"
"ds_write_addtid_b32 v216 offset:26912\n\t"
"ds_write_addtid_b32 v219 offset:32064\n\t"
"v_mfma_f32_16x16x32_bf16 v[60:63], a[0:3], a[156:159], v[60:63]\n\t"
"s_nop 0\n\t"
"ds_write_addtid_b32 v217 offset:31040\n\t"
"v_cvt_pk_bf16_f32 v194, -v194, -v198\n\t"
"v_mfma_f32_16x16x32_bf16 v[60:63], a[4:7], a[172:175], v[60:63]\n\t"
"s_waitcnt vmcnt(12) lgkmcnt(8)\n\t"
"ds_write_addtid_b32 v201 offset:28992\n\t"
"v_cvt_pk_bf16_f32 v218, v218, v222\n\t"
"v_mfma_f32_16x16x32_bf16 v[48:51], a[24:27], a[144:147], v[48:51]\n\t"
"s_nop 0\n\t"
"ds_write_addtid_b32 v202 offset:25888\n\t"
"v_cvt_pk_bf16_f32 v209, -v209, -v213\n\t"
"v_mfma_f32_16x16x32_bf16 v[48:51], a[28:31], a[160:163], v[48:51]\n\t"
"s_nop 0\n\t"
"ds_write_addtid_b32 v194 offset:17568\n\t"
"ds_write_addtid_b32 v209 offset:22720\n\t"
"ds_write_addtid_b32 v218 offset:27936\n\t"
"v_mfma_f32_16x16x32_bf16 v[64:67], a[32:35], a[144:147], v[64:67]\n\t"
"s_nop 0\n\t"
"ds_write_addtid_b32 v195 offset:21696\n\t"
"ds_write_addtid_b32 v200 offset:24864\n\t"
"v_mfma_f32_16x16x32_bf16 v[64:67], a[36:39], a[160:163], v[64:67]\n\t"
"buffer_load_dwordx4 a[80:83], off, s[84:87], s63 offset:2048 sc1\n\t"
"v_mfma_f32_16x16x32_bf16 v[80:83], a[40:43], a[144:147], v[80:83]\n\t"
"s_nop 0\n\t"
"ds_read_b128 a[156:159], v224 offset:1792\n\t"
"ds_read_b128 a[172:175], v224 offset:3840\n\t"
"v_mfma_f32_16x16x32_bf16 v[80:83], a[44:47], a[160:163], v[80:83]\n\t"
"buffer_load_dwordx4 a[72:75], off, s[84:87], s63 offset:0 sc1\n\t"
"v_mfma_f32_16x16x32_bf16 v[84:87], a[40:43], a[148:151], v[84:87]\n\t"
"s_nop 0\n\t"
"ds_read_b128 a[152:155], v224 offset:1536\n\t"
"ds_read_b128 a[168:171], v224 offset:3584\n\t"
"v_mfma_f32_16x16x32_bf16 v[84:87], a[44:47], a[164:167], v[84:87]\n\t"
"s_nop 0\n\t"
"ds_read_b64_tr_b16 a[192:193], v250 offset:0\n\t"
"ds_read_b64_tr_b16 a[200:201], v251 offset:0\n\t"
"v_mfma_f32_16x16x32_bf16 v[68:71], a[32:35], a[148:151], v[68:71]\n\t"
"s_nop 0\n\t"
"ds_read_b64_tr_b16 a[196:197], v251 offset:256\n\t"
"ds_read_b64_tr_b16 a[204:205], v250 offset:256\n\t"
"v_mfma_f32_16x16x32_bf16 v[68:71], a[36:39], a[164:167], v[68:71]\n\t"
"Lpad11_%=:\n\t"
"s_cbranch_execz Lret1_%=\n\t"
"v_mfma_f32_16x16x32_bf16 v[52:55], a[24:27], a[148:151], v[52:55]\n\t"
"buffer_load_dwordx4 a[76:79], off, s[84:87], s63 offset:1024 sc1\n\t"
"v_mfma_f32_16x16x32_bf16 v[52:55], a[28:31], a[164:167], v[52:55]\n\t"
"s_waitcnt lgkmcnt(0)\n\t"
"s_barrier\n\t"
"v_mfma_f32_16x16x32_bf16 v[56:59], a[24:27], a[152:155], v[56:59]\n\t"
"ds_read_b128 a[144:147], v224 offset:16544\n\t"
"v_mfma_f32_16x16x32_bf16 v[56:59], a[28:31], a[168:171], v[56:59]\n\t"
"ds_read_b128 a[160:163], v224 offset:18592\n\t"
"v_mfma_f32_16x16x32_bf16 v[72:75], a[32:35], a[152:155], v[72:75]\n\t"
"ds_read_b128 a[148:151], v224 offset:16800\n\t"
"v_mfma_f32_16x16x32_bf16 v[72:75], a[36:39], a[168:171], v[72:75]\n\t"
"ds_read_b128 a[164:167], v224 offset:18848\n\t"
"v_mfma_f32_16x16x32_bf16 v[88:91], a[40:43], a[152:155], v[88:91]\n\t"
"v_mfma_f32_16x16x32_bf16 v[88:91], a[44:47], a[168:171], v[88:91]\n\t"
"buffer_load_dwordx4 a[112:115], off, s[28:31], s42 offset:0 sc1\n\t"
"v_mfma_f32_16x16x32_bf16 v[92:95], a[40:43], a[156:159], v[92:95]\n\t"
"ds_read_b128 a[168:171], v224 offset:19104\n\t"
"v_mfma_f32_16x16x32_bf16 v[92:95], a[44:47], a[172:175], v[92:95]\n\t"
"ds_read_b128 v[104:107], v229 offset:20480\n\t"
"v_mfma_f32_16x16x32_bf16 v[76:79], a[32:35], a[156:159], v[76:79]\n\t"
"v_mfma_f32_16x16x32_bf16 v[76:79], a[36:39], a[172:175], v[76:79]\n\t"
"ds_read_b128 a[152:155], v224 offset:17056\n\t"
"v_mfma_f32_16x16x32_bf16 v[60:63], a[24:27], a[156:159], v[60:63]\n\t"
"ds_read_b128 a[156:159], v224 offset:17312\n\t"
"v_mfma_f32_16x16x32_bf16 v[60:63], a[28:31], a[172:175], v[60:63]\n\t"
"buffer_load_dwordx4 a[116:119], off, s[28:31], s42 offset:1024 sc1\n\t"
"v_mfma_f32_4x4x4_16b_bf16 v[200:203], a[220:221], a[178:179], 0\n\t"
"ds_read_b64_tr_b16 a[178:179], v248 offset:41600\n\t"
"v_mfma_f32_4x4x4_16b_bf16 v[204:207], a[220:221], a[186:187], 0\n\t"
"ds_read_b64_tr_b16 a[186:187], v249 offset:41600\n\t"
"v_mfma_f32_4x4x4_16b_bf16 v[216:219], a[220:221], a[194:195], 0\n\t"
"ds_read_b128 a[172:175], v224 offset:19360\n\t"
"v_mfma_f32_4x4x4_16b_bf16 v[220:223], a[220:221], a[202:203], 0\n\t"
"s_waitcnt lgkmcnt(5)\n\t"
".p2align 3\n\t"
"buffer_store_dwordx4 v[104:107], v232, s[16:19], s20 offen nt\n\t"
"v_pk_add_f32 v[144:145], v[48:49], v[144:145] neg_lo:[0,1] neg_hi:[0,1]\n\t"
"v_pk_add_f32 v[146:147], v[50:51], v[146:147] neg_lo:[0,1] neg_hi:[0,1]\n\t"
"v_pk_fma_f32 v[48:49], v[144:145], s[90:91], v[48:49] neg_lo:[1,0,0] neg_hi:[1,0,0]\n\t"
"v_pk_fma_f32 v[50:51], v[146:147], s[90:91], v[50:51] neg_lo:[1,0,0] neg_hi:[1,0,0]\n\t"
"v_pk_add_f32 v[160:161], v[64:65], v[160:161] neg_lo:[0,1] neg_hi:[0,1]\n\t"
"v_pk_add_f32 v[162:163], v[66:67], v[162:163] neg_lo:[0,1] neg_hi:[0,1]\n\t"
"v_pk_fma_f32 v[64:65], v[160:161], s[90:91], v[64:65] neg_lo:[1,0,0] neg_hi:[1,0,0]\n\t"
"v_pk_fma_f32 v[66:67], v[162:163], s[90:91], v[66:67] neg_lo:[1,0,0] neg_hi:[1,0,0]\n\t"
"v_pk_add_f32 v[176:177], v[80:81], v[176:177] neg_lo:[0,1] neg_hi:[0,1]\n\t"
"v_pk_add_f32 v[178:179], v[82:83], v[178:179] neg_lo:[0,1] neg_hi:[0,1]\n\t"
"v_pk_fma_f32 v[80:81], v[176:177], s[90:91], v[80:81] neg_lo:[1,0,0] neg_hi:[1,0,0]\n\t"
"v_pk_fma_f32 v[82:83], v[178:179], s[90:91], v[82:83] neg_lo:[1,0,0] neg_hi:[1,0,0]\n\t"
"v_pk_add_f32 v[180:181], v[84:85], v[180:181] neg_lo:[0,1] neg_hi:[0,1]\n\t"
"v_pk_add_f32 v[182:183], v[86:87], v[182:183] neg_lo:[0,1] neg_hi:[0,1]\n\t"
"v_pk_fma_f32 v[84:85], v[180:181], s[90:91], v[84:85] neg_lo:[1,0,0] neg_hi:[1,0,0]\n\t"
"v_pk_fma_f32 v[86:87], v[182:183], s[90:91], v[86:87] neg_lo:[1,0,0] neg_hi:[1,0,0]\n\t"
"v_pk_add_f32 v[164:165], v[68:69], v[164:165] neg_lo:[0,1] neg_hi:[0,1]\n\t"
"v_pk_add_f32 v[166:167], v[70:71], v[166:167] neg_lo:[0,1] neg_hi:[0,1]\n\t"
"v_pk_fma_f32 v[68:69], v[164:165], s[90:91], v[68:69] neg_lo:[1,0,0] neg_hi:[1,0,0]\n\t"
"v_pk_fma_f32 v[70:71], v[166:167], s[90:91], v[70:71] neg_lo:[1,0,0] neg_hi:[1,0,0]\n\t"
"v_pk_add_f32 v[148:149], v[52:53], v[148:149] neg_lo:[0,1] neg_hi:[0,1]\n\t"
"v_pk_add_f32 v[150:151], v[54:55], v[150:151] neg_lo:[0,1] neg_hi:[0,1]\n\t"
"v_pk_fma_f32 v[52:53], v[148:149], s[90:91], v[52:53] neg_lo:[1,0,0] neg_hi:[1,0,0]\n\t"
"v_pk_fma_f32 v[54:55], v[150:151], s[90:91], v[54:55] neg_lo:[1,0,0] neg_hi:[1,0,0]\n\t"
"v_pk_add_f32 v[152:153], v[56:57], v[152:153] neg_lo:[0,1] neg_hi:[0,1]\n\t"
"v_pk_add_f32 v[154:155], v[58:59], v[154:155] neg_lo:[0,1] neg_hi:[0,1]\n\t"
"v_pk_fma_f32 v[56:57], v[152:153], s[90:91], v[56:57] neg_lo:[1,0,0] neg_hi:[1,0,0]\n\t"
"v_pk_fma_f32 v[58:59], v[154:155], s[90:91], v[58:59] neg_lo:[1,0,0] neg_hi:[1,0,0]\n\t"
"v_pk_add_f32 v[168:169], v[72:73], v[168:169] neg_lo:[0,1] neg_hi:[0,1]\n\t"
"v_pk_add_f32 v[170:171], v[74:75], v[170:171] neg_lo:[0,1] neg_hi:[0,1]\n\t"
"v_pk_fma_f32 v[72:73], v[168:169], s[90:91], v[72:73] neg_lo:[1,0,0] neg_hi:[1,0,0]\n\t"
"v_pk_fma_f32 v[74:75], v[170:171], s[90:91], v[74:75] neg_lo:[1,0,0] neg_hi:[1,0,0]\n\t"
"v_pk_add_f32 v[184:185], v[88:89], v[184:185] neg_lo:[0,1] neg_hi:[0,1]\n\t"
"v_pk_add_f32 v[186:187], v[90:91], v[186:187] neg_lo:[0,1] neg_hi:[0,1]\n\t"
"v_pk_fma_f32 v[88:89], v[184:185], s[90:91], v[88:89] neg_lo:[1,0,0] neg_hi:[1,0,0]\n\t"
"v_pk_fma_f32 v[90:91], v[186:187], s[90:91], v[90:91] neg_lo:[1,0,0] neg_hi:[1,0,0]\n\t"
"v_pk_add_f32 v[188:189], v[92:93], v[188:189] neg_lo:[0,1] neg_hi:[0,1]\n\t"
"v_pk_add_f32 v[190:191], v[94:95], v[190:191] neg_lo:[0,1] neg_hi:[0,1]\n\t"
"v_pk_fma_f32 v[92:93], v[188:189], s[90:91], v[92:93] neg_lo:[1,0,0] neg_hi:[1,0,0]\n\t"
"v_pk_fma_f32 v[94:95], v[190:191], s[90:91], v[94:95] neg_lo:[1,0,0] neg_hi:[1,0,0]\n\t"
"v_pk_add_f32 v[172:173], v[76:77], v[172:173] neg_lo:[0,1] neg_hi:[0,1]\n\t"
"v_pk_add_f32 v[174:175], v[78:79], v[174:175] neg_lo:[0,1] neg_hi:[0,1]\n\t"
"v_pk_fma_f32 v[76:77], v[172:173], s[90:91], v[76:77] neg_lo:[1,0,0] neg_hi:[1,0,0]\n\t"
"v_pk_fma_f32 v[78:79], v[174:175], s[90:91], v[78:79] neg_lo:[1,0,0] neg_hi:[1,0,0]\n\t"
"v_pk_add_f32 v[156:157], v[60:61], v[156:157] neg_lo:[0,1] neg_hi:[0,1]\n\t"
"v_pk_add_f32 v[158:159], v[62:63], v[158:159] neg_lo:[0,1] neg_hi:[0,1]\n\t"
"v_pk_fma_f32 v[60:61], v[156:157], s[90:91], v[60:61] neg_lo:[1,0,0] neg_hi:[1,0,0]\n\t"
"v_pk_fma_f32 v[62:63], v[158:159], s[90:91], v[62:63] neg_lo:[1,0,0] neg_hi:[1,0,0]\n\t"
"v_mfma_f32_4x4x4_16b_bf16 v[192:195], a[222:223], a[176:177], v[200:203]\n\t"
"ds_read_b128 v[120:123], v230 offset:20480\n\t"
"v_mfma_f32_4x4x4_16b_bf16 v[196:199], a[222:223], a[184:185], v[204:207]\n\t"
"ds_read_b64_tr_b16 a[194:195], v250 offset:41600\n\t"
"v_mfma_f32_4x4x4_16b_bf16 v[200:203], a[222:223], a[180:181], v[200:203]\n\t"
"buffer_load_dwordx4 a[108:111], off, s[84:87], s42 offset:3072 sc1\n\t"
"v_mfma_f32_4x4x4_16b_bf16 v[204:207], a[222:223], a[188:189], v[204:207]\n\t"
"ds_read_b64_tr_b16 a[202:203], v251 offset:41600\n\t"
"v_mfma_f32_4x4x4_16b_bf16 v[208:211], a[222:223], a[192:193], v[216:219]\n\t"
"ds_read_b64_tr_b16 a[176:177], v248 offset:41472\n\t"
"v_mfma_f32_4x4x4_16b_bf16 v[212:215], a[222:223], a[200:201], v[220:223]\n\t"
"ds_read_b64_tr_b16 a[184:185], v249 offset:41472\n\t"
"v_mfma_f32_4x4x4_16b_bf16 v[216:219], a[222:223], a[196:197], v[216:219]\n\t"
"ds_read_b64_tr_b16 a[180:181], v249 offset:41728\n\t"
"v_mfma_f32_4x4x4_16b_bf16 v[220:223], a[222:223], a[204:205], v[220:223]\n\t"
"s_waitcnt vmcnt(11) lgkmcnt(7)\n\t"
"buffer_load_dwordx4 a[104:107], off, s[84:87], s42 offset:2048 sc1\n\t"
"v_mfma_f32_16x16x32_bf16 v[48:51], a[48:51], a[144:147], v[48:51]\n\t"
"ds_read_b64_tr_b16 a[188:189], v248 offset:41728\n\t"
"v_cvt_pk_bf16_f32 v195, -v195, -v199\n\t"
"v_mfma_f32_16x16x32_bf16 v[48:51], a[52:55], a[160:163], v[48:51]\n\t"
"buffer_load_dwordx4 a[96:99], off, s[84:87], s42 offset:0 sc1\n\t"
"v_mfma_f32_16x16x32_bf16 v[64:67], a[56:59], a[144:147], v[64:67]\n\t"
"ds_write_addtid_b32 v195 offset:5152\n\t"
"v_cvt_pk_bf16_f32 v194, -v194, -v198\n\t"
"v_mfma_f32_16x16x32_bf16 v[64:67], a[60:63], a[160:163], v[64:67]\n\t"
"ds_write_addtid_b32 v194 offset:1024\n\t"
"v_cvt_pk_bf16_f32 v203, v203, v207\n\t"
"v_mfma_f32_16x16x32_bf16 v[80:83], a[64:67], a[144:147], v[80:83]\n\t"
"ds_read_b128 a[232:235], v230 offset:33792\n\t"
"v_cvt_pk_bf16_f32 v211, -v211, -v215\n\t"
"v_mfma_f32_16x16x32_bf16 v[80:83], a[68:71], a[160:163], v[80:83]\n\t"
"s_waitcnt lgkmcnt(7)\n\t"
"buffer_store_dwordx4 v[120:123], v233, s[16:19], s20 offen nt\n\t"
"v_mfma_f32_16x16x32_bf16 v[84:87], a[64:67], a[148:151], v[84:87]\n\t"
"ds_read_b128 a[144:147], v224 offset:17568\n\t"
"v_cvt_pk_bf16_f32 v201, v201, v205\n\t"
"v_mfma_f32_16x16x32_bf16 v[84:87], a[68:71], a[164:167], v[84:87]\n\t"
"ds_read_b128 v[136:139], v231 offset:20480\n\t"
"v_cvt_pk_bf16_f32 v202, v202, v206\n\t"
"v_mfma_f32_16x16x32_bf16 v[68:71], a[56:59], a[148:151], v[68:71]\n\t"
"ds_read_b128 a[160:163], v224 offset:19616\n\t"
"v_cvt_pk_bf16_f32 v193, -v193, -v197\n\t"
"v_mfma_f32_16x16x32_bf16 v[68:71], a[60:63], a[164:167], v[68:71]\n\t"
"buffer_load_dwordx4 a[100:103], off, s[84:87], s42 offset:1024 sc1\n\t"
"v_mfma_f32_16x16x32_bf16 v[52:55], a[48:51], a[148:151], v[52:55]\n\t"
"ds_read_b128 a[148:151], v224 offset:17824\n\t"
"v_cvt_pk_bf16_f32 v209, -v209, -v213\n\t"
"v_mfma_f32_16x16x32_bf16 v[52:55], a[52:55], a[164:167], v[52:55]\n\t"
"ds_write_addtid_b32 v211 offset:7200\n\t"
"v_cvt_pk_bf16_f32 v200, v200, v204\n\t"
"v_mfma_f32_16x16x32_bf16 v[56:59], a[48:51], a[152:155], v[56:59]\n\t"
"ds_read_b128 a[236:239], v231 offset:33792\n\t"
"v_cvt_pk_bf16_f32 v216, v216, v220\n\t"
"v_mfma_f32_16x16x32_bf16 v[56:59], a[52:55], a[168:171], v[56:59]\n\t"
"buffer_load_dwordx4 a[136:139], off, s[28:31], s48 offset:0 sc1\n\t"
"v_mfma_f32_16x16x32_bf16 v[72:75], a[56:59], a[152:155], v[72:75]\n\t"
"ds_read_b128 a[164:167], v224 offset:19872\n\t"
"v_cvt_pk_bf16_f32 v210, -v210, -v214\n\t"
"v_mfma_f32_16x16x32_bf16 v[72:75], a[60:63], a[168:171], v[72:75]\n\t"
"ds_write_addtid_b32 v193 offset:4128\n\t"
"v_cvt_pk_bf16_f32 v218, v218, v222\n\t"
"v_mfma_f32_16x16x32_bf16 v[88:91], a[64:67], a[152:155], v[88:91]\n\t"
"s_nop 0\n\t"
"ds_write_addtid_b32 v201 offset:12448\n\t"
"ds_write_addtid_b32 v202 offset:9344\n\t"
"v_mfma_f32_16x16x32_bf16 v[88:91], a[68:71], a[168:171], v[88:91]\n\t"
"s_waitcnt lgkmcnt(8)\n\t"
"buffer_store_dwordx4 v[136:139], v234, s[16:19], s20 offen nt\n\t"
"v_mfma_f32_16x16x32_bf16 v[92:95], a[64:67], a[156:159], v[92:95]\n\t"
"ds_read_b128 v[108:111], v229 offset:33792\n\t"
"v_cvt_pk_bf16_f32 v192, -v192, -v196\n\t"
"v_mfma_f32_16x16x32_bf16 v[92:95], a[68:71], a[172:175], v[92:95]\n\t"
"ds_write_addtid_b32 v210 offset:3072\n\t"
"v_cvt_pk_bf16_f32 v219, v219, v223\n\t"
"v_mfma_f32_16x16x32_bf16 v[76:79], a[56:59], a[156:159], v[76:79]\n\t"
"ds_write_addtid_b32 v219 offset:15520\n\t"
"v_cvt_pk_bf16_f32 v208, -v208, -v212\n\t"
"v_mfma_f32_16x16x32_bf16 v[76:79], a[60:63], a[172:175], v[76:79]\n\t"
"ds_write_addtid_b32 v200 offset:8320\n\t"
"buffer_load_dwordx4 a[140:143], off, s[28:31], s48 offset:1024 sc1\n\t"
"v_mfma_f32_16x16x32_bf16 v[60:63], a[48:51], a[156:159], v[60:63]\n\t"
"s_nop 0\n\t"
"ds_write_addtid_b32 v209 offset:6176\n\t"
"ds_write_addtid_b32 v218 offset:11392\n\t"
"v_mfma_f32_16x16x32_bf16 v[60:63], a[52:55], a[172:175], v[60:63]\n\t"
"s_waitcnt vmcnt(11) lgkmcnt(10)\n\t"
"ds_write_addtid_b32 v192 offset:0\n\t"
"ds_write_addtid_b32 v216 offset:10368\n\t"
"v_mfma_f32_16x16x32_bf16 v[144:147], a[72:75], a[144:147], v[144:147]\n\t"
"ds_write_addtid_b32 v203 offset:13472\n\t"
"v_cvt_pk_bf16_f32 v217, v217, v221\n\t"
"v_mfma_f32_16x16x32_bf16 v[144:147], a[76:79], a[160:163], v[144:147]\n\t"
"buffer_load_dwordx4 a[132:135], off, s[84:87], s48 offset:3072 sc1\n\t"
"v_mfma_f32_16x16x32_bf16 v[160:163], a[80:83], a[144:147], v[160:163]\n\t"
"s_nop 0\n\t"
"ds_write_addtid_b32 v208 offset:2048\n\t"
"ds_write_addtid_b32 v217 offset:14496\n\t"
"v_mfma_f32_16x16x32_bf16 v[160:163], a[84:87], a[160:163], v[160:163]\n\t"
"s_nop 0\n\t"
"ds_read_b128 a[152:155], v224 offset:18080\n\t"
"ds_read_b128 a[156:159], v224 offset:18336\n\t"
"v_mfma_f32_16x16x32_bf16 v[176:179], a[88:91], a[144:147], v[176:179]\n\t"
"s_nop 0\n\t"
"ds_read_b128 a[172:175], v224 offset:20384\n\t"
"ds_read_b64_tr_b16 a[192:193], v250 offset:41472\n\t"
"v_mfma_f32_16x16x32_bf16 v[176:179], a[92:95], a[160:163], v[176:179]\n\t"
"s_waitcnt lgkmcnt(13)\n\t"
"buffer_store_dwordx4 v[108:111], v232, s[16:19], s25 offen nt\n\t"
"v_mfma_f32_16x16x32_bf16 v[180:183], a[88:91], a[148:151], v[180:183]\n\t"
"s_nop 0\n\t"
"ds_read_b128 a[168:171], v224 offset:20128\n\t"
"ds_read_b64_tr_b16 a[200:201], v251 offset:41472\n\t"
"v_mfma_f32_16x16x32_bf16 v[180:183], a[92:95], a[164:167], v[180:183]\n\t"
"s_nop 0\n\t"
"ds_read_b64_tr_b16 a[196:197], v251 offset:41728\n\t"
"ds_read_b64_tr_b16 a[204:205], v250 offset:41728\n\t"
"v_mfma_f32_16x16x32_bf16 v[164:167], a[80:83], a[148:151], v[164:167]\n\t"
"buffer_load_dwordx4 a[128:131], off, s[84:87], s48 offset:2048 sc1\n\t"
"v_mfma_f32_16x16x32_bf16 v[164:167], a[84:87], a[164:167], v[164:167]\n\t"
"v_mfma_f32_16x16x32_bf16 v[148:151], a[72:75], a[148:151], v[148:151]\n\t"
"buffer_load_dwordx4 a[120:123], off, s[84:87], s48 offset:0 sc1\n\t"
"v_mfma_f32_16x16x32_bf16 v[148:151], a[76:79], a[164:167], v[148:151]\n\t"
"s_waitcnt lgkmcnt(0)\n\t"
"s_barrier\n\t"
"v_mfma_f32_16x16x32_bf16 v[152:155], a[72:75], a[152:155], v[152:155]\n\t"
"ds_read_b128 a[144:147], v224 offset:0\n\t"
"v_mfma_f32_16x16x32_bf16 v[152:155], a[76:79], a[168:171], v[152:155]\n\t"
"ds_read_b128 a[160:163], v224 offset:2048\n\t"
"v_mfma_f32_16x16x32_bf16 v[168:171], a[80:83], a[152:155], v[168:171]\n\t"
"ds_read_b128 a[148:151], v224 offset:256\n\t"
"v_add_u32 v248, 33280, v227\n\t"
"v_mfma_f32_16x16x32_bf16 v[168:171], a[84:87], a[168:171], v[168:171]\n\t"
"ds_read_b128 a[164:167], v224 offset:2304\n\t"
"v_xor_b32 v249, 32, v248\n\t"
"v_mfma_f32_16x16x32_bf16 v[184:187], a[88:91], a[152:155], v[184:187]\n\t"
"v_xor_b32 v250, 64, v248\n\t"
"v_xor_b32 v251, 96, v248\n\t"
"v_mfma_f32_16x16x32_bf16 v[184:187], a[92:95], a[168:171], v[184:187]\n\t"
"buffer_load_dwordx4 a[124:127], off, s[84:87], s48 offset:1024 sc1\n\t"
"v_mfma_f32_16x16x32_bf16 v[188:191], a[88:91], a[156:159], v[188:191]\n\t"
"ds_read_b128 a[168:171], v224 offset:2560\n\t"
"v_mfma_f32_16x16x32_bf16 v[188:191], a[92:95], a[172:175], v[188:191]\n\t"
"ds_read_b128 a[152:155], v224 offset:512\n\t"
"v_mfma_f32_16x16x32_bf16 v[172:175], a[80:83], a[156:159], v[172:175]\n\t"
"v_mfma_f32_16x16x32_bf16 v[172:175], a[84:87], a[172:175], v[172:175]\n\t"
"ds_read_b64_tr_b16 a[182:183], v249 offset:512\n\t"
"v_mfma_f32_16x16x32_bf16 v[156:159], a[72:75], a[156:159], v[156:159]\n\t"
"ds_read_b128 a[156:159], v224 offset:768\n\t"
"v_mfma_f32_16x16x32_bf16 v[156:159], a[76:79], a[172:175], v[156:159]\n\t"
"buffer_load_dwordx4 a[16:19], off, s[28:31], s75 offset:0 sc1\n\t"
"v_mfma_f32_4x4x4_16b_bf16 v[200:203], a[220:221], a[178:179], 0\n\t"
"ds_read_b64_tr_b16 a[190:191], v251 offset:512\n\t"
"v_mfma_f32_4x4x4_16b_bf16 v[204:207], a[220:221], a[186:187], 0\n\t"
"ds_read_b64_tr_b16 a[198:199], v249 offset:1664\n\t"
"v_mfma_f32_4x4x4_16b_bf16 v[216:219], a[220:221], a[194:195], 0\n\t"
"ds_read_b128 a[172:175], v224 offset:2816\n\t"
"v_mfma_f32_4x4x4_16b_bf16 v[220:223], a[220:221], a[202:203], 0\n\t"
"buffer_store_dwordx4 a[232:235], v233, s[16:19], s25 offen nt\n\t"
"v_mfma_f32_4x4x4_16b_bf16 v[192:195], a[222:223], a[176:177], v[200:203]\n\t"
"ds_read_b64_tr_b16 a[206:207], v251 offset:1664\n\t"
"v_mfma_f32_4x4x4_16b_bf16 v[196:199], a[222:223], a[184:185], v[204:207]\n\t"
"ds_read_b64_tr_b16 a[178:179], v248 offset:512\n\t"
"v_mfma_f32_4x4x4_16b_bf16 v[200:203], a[222:223], a[180:181], v[200:203]\n\t"
"ds_read_b64_tr_b16 a[186:187], v250 offset:512\n\t"
"v_mfma_f32_4x4x4_16b_bf16 v[204:207], a[222:223], a[188:189], v[204:207]\n\t"
"ds_read_b64_tr_b16 a[194:195], v248 offset:1664\n\t"
"v_mfma_f32_4x4x4_16b_bf16 v[208:211], a[222:223], a[192:193], v[216:219]\n\t"
"ds_read_b64_tr_b16 a[202:203], v250 offset:1664\n\t"
"v_mfma_f32_4x4x4_16b_bf16 v[212:215], a[222:223], a[200:201], v[220:223]\n\t"
"ds_read_b64_tr_b16 a[176:177], v248 offset:0\n\t"
"v_mfma_f32_4x4x4_16b_bf16 v[216:219], a[222:223], a[196:197], v[216:219]\n\t"
"ds_read_b64_tr_b16 a[184:185], v250 offset:0\n\t"
"v_mfma_f32_4x4x4_16b_bf16 v[220:223], a[222:223], a[204:205], v[220:223]\n\t"
"s_waitcnt vmcnt(10) lgkmcnt(8)\n\t"
"buffer_load_dwordx4 a[20:23], off, s[28:31], s75 offset:1024 sc1\n\t"
"v_mfma_f32_16x16x32_bf16 v[48:51], a[96:99], a[144:147], v[48:51]\n\t"
"ds_read_b64_tr_b16 a[180:181], v249 offset:0\n\t"
"v_cvt_pk_bf16_f32 v193, -v193, -v197\n\t"
"v_mfma_f32_16x16x32_bf16 v[48:51], a[100:103], a[160:163], v[48:51]\n\t"
"ds_read_b64_tr_b16 a[188:189], v251 offset:0\n\t"
"v_cvt_pk_bf16_f32 v194, -v194, -v198\n\t"
"v_mfma_f32_16x16x32_bf16 v[64:67], a[104:107], a[144:147], v[64:67]\n\t"
"ds_read_b64_tr_b16 a[192:193], v248 offset:1152\n\t"
"v_cvt_pk_bf16_f32 v201, v201, v205\n\t"
"v_mfma_f32_16x16x32_bf16 v[64:67], a[108:111], a[160:163], v[64:67]\n\t"
"buffer_store_dwordx4 a[236:239], v234, s[16:19], s25 offen nt\n\t"
"v_mfma_f32_16x16x32_bf16 v[80:83], a[112:115], a[144:147], v[80:83]\n\t"
"ds_read_b128 a[144:147], v224 offset:1024\n\t"
"v_cvt_pk_bf16_f32 v211, -v211, -v215\n\t"
"v_mfma_f32_16x16x32_bf16 v[80:83], a[116:119], a[160:163], v[80:83]\n\t"
"ds_read_b64_tr_b16 a[200:201], v250 offset:1152\n\t"
"v_cvt_pk_bf16_f32 v202, v202, v206\n\t"
"v_mfma_f32_16x16x32_bf16 v[84:87], a[112:115], a[148:151], v[84:87]\n\t"
"ds_read_b128 a[160:163], v224 offset:3072\n\t"
"v_cvt_pk_bf16_f32 v208, -v208, -v212\n\t"
"v_mfma_f32_16x16x32_bf16 v[84:87], a[116:119], a[164:167], v[84:87]\n\t"
"buffer_load_dwordx4 a[12:15], off, s[84:87], s75 offset:3072 sc1\n\t"
"v_mfma_f32_16x16x32_bf16 v[68:71], a[104:107], a[148:151], v[68:71]\n\t"
"ds_write_addtid_b32 v201 offset:28992\n\t"
"v_cvt_pk_bf16_f32 v192, -v192, -v196\n\t"
"v_mfma_f32_16x16x32_bf16 v[68:71], a[108:111], a[164:167], v[68:71]\n\t"
"ds_write_addtid_b32 v211 offset:23744\n\t"
"v_cvt_pk_bf16_f32 v203, v203, v207\n\t"
"v_mfma_f32_16x16x32_bf16 v[52:55], a[96:99], a[148:151], v[52:55]\n\t"
"ds_read_b128 a[148:151], v224 offset:1280\n\t"
"v_mfma_f32_16x16x32_bf16 v[52:55], a[100:103], a[164:167], v[52:55]\n\t"
"ds_write_addtid_b32 v193 offset:20672\n\t"
"v_cvt_pk_bf16_f32 v218, v218, v222\n\t"
"v_mfma_f32_16x16x32_bf16 v[56:59], a[96:99], a[152:155], v[56:59]\n\t"
"ds_read_b128 a[164:167], v224 offset:3328\n\t"
"v_cvt_pk_bf16_f32 v217, v217, v221\n\t"
"v_mfma_f32_16x16x32_bf16 v[56:59], a[100:103], a[168:171], v[56:59]\n\t"
"buffer_load_dwordx4 a[8:11], off, s[84:87], s75 offset:2048 sc1\n\t"
"v_mfma_f32_16x16x32_bf16 v[72:75], a[104:107], a[152:155], v[72:75]\n\t"
"ds_write_addtid_b32 v218 offset:27936\n\t"
"v_cvt_pk_bf16_f32 v195, -v195, -v199\n\t"
"v_mfma_f32_16x16x32_bf16 v[72:75], a[108:111], a[168:171], v[72:75]\n\t"
"ds_write_addtid_b32 v217 offset:31040\n\t"
"v_mfma_f32_16x16x32_bf16 v[88:91], a[112:115], a[152:155], v[88:91]\n\t"
"ds_read_b128 a[152:155], v224 offset:1536\n\t"
"v_cvt_pk_bf16_f32 v219, v219, v223\n\t"
"v_mfma_f32_16x16x32_bf16 v[88:91], a[116:119], a[168:171], v[88:91]\n\t"
"s_waitcnt lgkmcnt(8)\n\t"
"buffer_load_dwordx4 a[0:3], off, s[84:87], s75 offset:0 sc1\n\t"
"v_mfma_f32_16x16x32_bf16 v[92:95], a[112:115], a[156:159], v[92:95]\n\t"
"ds_write_addtid_b32 v194 offset:17568\n\t"
"v_cvt_pk_bf16_f32 v210, -v210, -v214\n\t"
"v_mfma_f32_16x16x32_bf16 v[92:95], a[116:119], a[172:175], v[92:95]\n\t"
"s_nop 0\n\t"
"ds_write_addtid_b32 v192 offset:16544\n\t"
"ds_write_addtid_b32 v202 offset:25888\n\t"
"v_mfma_f32_16x16x32_bf16 v[76:79], a[104:107], a[156:159], v[76:79]\n\t"
"ds_write_addtid_b32 v210 offset:19616\n\t"
"v_mfma_f32_16x16x32_bf16 v[76:79], a[108:111], a[172:175], v[76:79]\n\t"
"ds_write_addtid_b32 v208 offset:18592\n\t"
"v_cvt_pk_bf16_f32 v209, -v209, -v213\n\t"
"v_mfma_f32_16x16x32_bf16 v[60:63], a[96:99], a[156:159], v[60:63]\n\t"
"ds_write_addtid_b32 v219 offset:32064\n\t"
"v_cvt_pk_bf16_f32 v216, v216, v220\n\t"
"v_mfma_f32_16x16x32_bf16 v[60:63], a[100:103], a[172:175], v[60:63]\n\t"
"s_waitcnt vmcnt(7) lgkmcnt(9)\n\t"
"ds_write_addtid_b32 v203 offset:30016\n\t"
"ds_write_addtid_b32 v216 offset:26912\n\t"
"v_mfma_f32_16x16x32_bf16 v[144:147], a[120:123], a[144:147], v[144:147]\n\t"
"ds_write_addtid_b32 v195 offset:21696\n\t"
"v_cvt_pk_bf16_f32 v200, v200, v204\n\t"
"v_mfma_f32_16x16x32_bf16 v[144:147], a[124:127], a[160:163], v[144:147]\n\t"
"ds_write_addtid_b32 v200 offset:24864\n\t"
"v_mfma_f32_16x16x32_bf16 v[160:163], a[128:131], a[144:147], v[160:163]\n\t"
"ds_write_addtid_b32 v209 offset:22720\n\t"
"v_mfma_f32_16x16x32_bf16 v[160:163], a[132:135], a[160:163], v[160:163]\n\t"
"ds_read_b128 a[172:175], v224 offset:3840\n\t"
"v_mfma_f32_16x16x32_bf16 v[176:179], a[136:139], a[144:147], v[176:179]\n\t"
"ds_read_b128 a[168:171], v224 offset:3584\n\t"
"v_mfma_f32_16x16x32_bf16 v[176:179], a[140:143], a[160:163], v[176:179]\n\t"
"buffer_load_dwordx4 a[4:7], off, s[84:87], s75 offset:1024 sc1\n\t"
"v_mfma_f32_16x16x32_bf16 v[180:183], a[136:139], a[148:151], v[180:183]\n\t"
"ds_read_b128 a[156:159], v224 offset:1792\n\t"
"v_mfma_f32_16x16x32_bf16 v[180:183], a[140:143], a[164:167], v[180:183]\n\t"
"ds_read_b64_tr_b16 a[196:197], v249 offset:1152\n\t"
"v_mfma_f32_16x16x32_bf16 v[164:167], a[128:131], a[148:151], v[164:167]\n\t"
"ds_read_b64_tr_b16 a[204:205], v251 offset:1152\n\t"
"v_mfma_f32_16x16x32_bf16 v[164:167], a[132:135], a[164:167], v[164:167]\n\t"
"buffer_load_dwordx4 a[40:43], off, s[28:31], s78 offset:0 sc1\n\t"
"v_mfma_f32_16x16x32_bf16 v[148:151], a[120:123], a[148:151], v[148:151]\n\t"
"v_mfma_f32_16x16x32_bf16 v[148:151], a[124:127], a[164:167], v[148:151]\n\t"
"s_waitcnt lgkmcnt(0)\n\t"
"s_barrier\n\t"
"v_mfma_f32_16x16x32_bf16 v[152:155], a[120:123], a[152:155], v[152:155]\n\t"
"ds_read_b128 a[144:147], v224 offset:16544\n\t"
"v_mfma_f32_16x16x32_bf16 v[152:155], a[124:127], a[168:171], v[152:155]\n\t"
"ds_read_b128 a[148:151], v224 offset:16800\n\t"
"v_mfma_f32_16x16x32_bf16 v[168:171], a[128:131], a[152:155], v[168:171]\n\t"
"ds_read_b128 a[160:163], v224 offset:18592\n\t"
"v_mfma_f32_16x16x32_bf16 v[168:171], a[132:135], a[168:171], v[168:171]\n\t"
"ds_read_b128 a[164:167], v224 offset:18848\n\t"
"v_mfma_f32_16x16x32_bf16 v[184:187], a[136:139], a[152:155], v[184:187]\n\t"
"v_mfma_f32_16x16x32_bf16 v[184:187], a[140:143], a[168:171], v[184:187]\n\t"
"buffer_load_dwordx4 a[44:47], off, s[28:31], s78 offset:1024 sc1\n\t"
"v_mfma_f32_16x16x32_bf16 v[188:191], a[136:139], a[156:159], v[188:191]\n\t"
"ds_read_b128 a[168:171], v224 offset:19104\n\t"
"v_mfma_f32_16x16x32_bf16 v[188:191], a[140:143], a[172:175], v[188:191]\n\t"
"buffer_load_dwordx4 a[36:39], off, s[84:87], s78 offset:3072 sc1\n\t"
"v_mfma_f32_16x16x32_bf16 v[172:175], a[128:131], a[156:159], v[172:175]\n\t"
"ds_read_b128 a[152:155], v224 offset:17056\n\t"
"v_mfma_f32_16x16x32_bf16 v[172:175], a[132:135], a[172:175], v[172:175]\n\t"
"buffer_load_dwordx4 a[32:35], off, s[84:87], s78 offset:2048 sc1\n\t"
"v_mfma_f32_16x16x32_bf16 v[156:159], a[120:123], a[156:159], v[156:159]\n\t"
"ds_read_b128 a[156:159], v224 offset:17312\n\t"
"v_mfma_f32_16x16x32_bf16 v[156:159], a[124:127], a[172:175], v[156:159]\n\t"
"ds_read_b128 a[172:175], v224 offset:19360\n\t"
"v_mfma_f32_16x16x32_bf16 v[192:195], a[224:227], a[176:179], 0\n\t"
"ds_read_b64_tr_b16 a[176:177], v248 offset:42624\n\t"
"v_mfma_f32_16x16x32_bf16 v[196:199], a[224:227], a[180:183], 0\n\t"
"buffer_load_dwordx4 a[24:27], off, s[84:87], s78 offset:0 sc1\n\t"
"v_mfma_f32_16x16x32_bf16 v[200:203], a[224:227], a[184:187], 0\n\t"
"ds_read_b64_tr_b16 a[178:179], v248 offset:43136\n\t"
"v_mfma_f32_16x16x32_bf16 v[204:207], a[224:227], a[188:191], 0\n\t"
"ds_read_b64_tr_b16 a[180:181], v249 offset:42624\n\t"
"v_mfma_f32_16x16x32_bf16 v[208:211], a[224:227], a[192:195], 0\n\t"
"ds_read_b64_tr_b16 a[182:183], v249 offset:43136\n\t"
"v_mfma_f32_16x16x32_bf16 v[212:215], a[224:227], a[196:199], 0\n\t"
"buffer_load_dwordx4 a[28:31], off, s[84:87], s78 offset:1024 sc1\n\t"
"v_mfma_f32_16x16x32_bf16 v[216:219], a[224:227], a[200:203], 0\n\t"
"ds_read_b64_tr_b16 a[184:185], v250 offset:42624\n\t"
"v_mfma_f32_16x16x32_bf16 v[220:223], a[224:227], a[204:207], 0\n\t"
"s_waitcnt vmcnt(6) lgkmcnt(7)\n\t"
"ds_read_b64_tr_b16 a[186:187], v250 offset:43136\n\t"
"v_cvt_pk_bf16_f32 v192, v192, v196\n\t"
"v_mfma_f32_16x16x32_bf16 v[48:51], a[0:3], a[144:147], v[48:51]\n\t"
"ds_read_b64_tr_b16 a[188:189], v251 offset:42624\n\t"
"v_cvt_pk_bf16_f32 v194, v194, v198\n\t"
"v_mfma_f32_16x16x32_bf16 v[48:51], a[4:7], a[160:163], v[48:51]\n\t"
"ds_read_b64_tr_b16 a[190:191], v251 offset:43136\n\t"
"v_cvt_pk_bf16_f32 v193, v193, v197\n\t"
"v_mfma_f32_16x16x32_bf16 v[64:67], a[8:11], a[144:147], v[64:67]\n\t"
"ds_read_b64_tr_b16 a[192:193], v248 offset:21888\n\t"
"v_mfma_f32_16x16x32_bf16 v[64:67], a[12:15], a[160:163], v[64:67]\n\t"
"ds_read_b64_tr_b16 a[194:195], v248 offset:22400\n\t"
"v_cvt_pk_bf16_f32 v211, v211, v215\n\t"
"v_mfma_f32_16x16x32_bf16 v[80:83], a[16:19], a[144:147], v[80:83]\n\t"
"ds_read_b128 a[144:147], v224 offset:17568\n\t"
"v_cvt_pk_bf16_f32 v209, v209, v213\n\t"
"v_mfma_f32_16x16x32_bf16 v[80:83], a[20:23], a[160:163], v[80:83]\n\t"
"ds_read_b64_tr_b16 a[196:197], v249 offset:21888\n\t"
"v_cvt_pk_bf16_f32 v219, v219, v223\n\t"
"v_mfma_f32_16x16x32_bf16 v[84:87], a[16:19], a[148:151], v[84:87]\n\t"
"ds_write_addtid_b32 v209 offset:9344\n\t"
"v_mfma_f32_16x16x32_bf16 v[84:87], a[20:23], a[164:167], v[84:87]\n\t"
"ds_read_b64_tr_b16 a[198:199], v249 offset:22400\n\t"
"v_cvt_pk_bf16_f32 v200, v200, v204\n\t"
"v_mfma_f32_16x16x32_bf16 v[68:71], a[8:11], a[148:151], v[68:71]\n\t"
"ds_read_b128 a[160:163], v224 offset:19616\n\t"
"v_cvt_pk_bf16_f32 v208, v208, v212\n\t"
"v_mfma_f32_16x16x32_bf16 v[68:71], a[12:15], a[164:167], v[68:71]\n\t"
"ds_write_addtid_b32 v219 offset:15520\n\t"
"v_cvt_pk_bf16_f32 v217, v217, v221\n\t"
"v_mfma_f32_16x16x32_bf16 v[52:55], a[0:3], a[148:151], v[52:55]\n\t"
"ds_write_addtid_b32 v200 offset:2048\n\t"
"v_cvt_pk_bf16_f32 v218, v218, v222\n\t"
"v_mfma_f32_16x16x32_bf16 v[52:55], a[4:7], a[164:167], v[52:55]\n\t"
"ds_write_addtid_b32 v211 offset:13472\n\t"
"v_mfma_f32_16x16x32_bf16 v[56:59], a[0:3], a[152:155], v[56:59]\n\t"
"ds_read_b128 a[148:151], v224 offset:17824\n\t"
"v_cvt_pk_bf16_f32 v216, v216, v220\n\t"
"v_mfma_f32_16x16x32_bf16 v[56:59], a[4:7], a[168:171], v[56:59]\n\t"
"ds_read_b128 a[164:167], v224 offset:19872\n\t"
"v_cvt_pk_bf16_f32 v195, v195, v199\n\t"
"v_mfma_f32_16x16x32_bf16 v[72:75], a[8:11], a[152:155], v[72:75]\n\t"
"ds_write_addtid_b32 v195 offset:12448\n\t"
"v_cvt_pk_bf16_f32 v210, v210, v214\n\t"
"v_mfma_f32_16x16x32_bf16 v[72:75], a[12:15], a[168:171], v[72:75]\n\t"
"buffer_load_dwordx4 a[64:67], off, s[28:31], 0 offset:0 sc1\n\t"
"v_mfma_f32_16x16x32_bf16 v[88:91], a[16:19], a[152:155], v[88:91]\n\t"
"ds_read_b128 a[152:155], v224 offset:18080\n\t"
"v_cvt_pk_bf16_f32 v201, v201, v205\n\t"
"v_mfma_f32_16x16x32_bf16 v[88:91], a[20:23], a[168:171], v[88:91]\n\t"
"s_waitcnt lgkmcnt(8)\n\t"
"ds_read_b128 a[168:171], v224 offset:20128\n\t"
"v_cvt_pk_bf16_f32 v202, v202, v206\n\t"
"v_mfma_f32_16x16x32_bf16 v[92:95], a[16:19], a[156:159], v[92:95]\n\t"
"ds_write_addtid_b32 v216 offset:3072\n\t"
"v_cvt_pk_bf16_f32 v203, v203, v207\n\t"
"v_mfma_f32_16x16x32_bf16 v[92:95], a[20:23], a[172:175], v[92:95]\n\t"
"ds_write_addtid_b32 v194 offset:4128\n\t"
"v_mfma_f32_16x16x32_bf16 v[76:79], a[8:11], a[156:159], v[76:79]\n\t"
"s_xor_b32 s24, s24, 384\n\t"
"ds_write_addtid_b32 v208 offset:1024\n\t"
"ds_write_addtid_b32 v217 offset:11392\n\t"
"v_mfma_f32_16x16x32_bf16 v[76:79], a[12:15], a[172:175], v[76:79]\n\t"
"buffer_load_dwordx4 a[68:71], off, s[28:31], 0 offset:1024 sc1\n\t"
"v_mfma_f32_16x16x32_bf16 v[60:63], a[0:3], a[156:159], v[60:63]\n\t"
"s_xor_b32 s20, s20, 384\n\t"
"ds_read_b128 a[156:159], v224 offset:18336\n\t"
"ds_write_addtid_b32 v218 offset:7200\n\t"
"v_mfma_f32_16x16x32_bf16 v[60:63], a[4:7], a[172:175], v[60:63]\n\t"
"s_waitcnt vmcnt(2) lgkmcnt(9)\n\t"
"buffer_load_dwordx4 a[60:63], off, s[84:87], 0 offset:3072 sc1\n\t"
"v_mfma_f32_16x16x32_bf16 v[144:147], a[24:27], a[144:147], v[144:147]\n\t"
"s_nop 0\n\t"
"ds_write_addtid_b32 v192 offset:0\n\t"
"ds_write_addtid_b32 v202 offset:6176\n\t"
"v_mfma_f32_16x16x32_bf16 v[144:147], a[28:31], a[160:163], v[144:147]\n\t"
"s_nop 0\n\t"
"ds_write_addtid_b32 v193 offset:8320\n\t"
"ds_write_addtid_b32 v210 offset:5152\n\t"
"v_mfma_f32_16x16x32_bf16 v[160:163], a[32:35], a[144:147], v[160:163]\n\t"
"s_xor_b32 s27, s27, 384\n\t"
"ds_write_addtid_b32 v201 offset:10368\n\t"
"ds_write_addtid_b32 v203 offset:14496\n\t"
"v_mfma_f32_16x16x32_bf16 v[160:163], a[36:39], a[160:163], v[160:163]\n\t"
"buffer_load_dwordx4 a[56:59], off, s[84:87], 0 offset:2048 sc1\n\t"
"v_mfma_f32_16x16x32_bf16 v[176:179], a[40:43], a[144:147], v[176:179]\n\t"
"ds_read_b128 a[172:175], v224 offset:20384\n\t"
"v_mfma_f32_16x16x32_bf16 v[176:179], a[44:47], a[160:163], v[176:179]\n\t"
"buffer_load_dwordx4 a[48:51], off, s[84:87], 0 offset:0 sc1\n\t"
"v_mfma_f32_16x16x32_bf16 v[180:183], a[40:43], a[148:151], v[180:183]\n\t"
"ds_read_b64_tr_b16 a[200:201], v250 offset:21888\n\t"
"v_mfma_f32_16x16x32_bf16 v[180:183], a[44:47], a[164:167], v[180:183]\n\t"
"ds_read_b64_tr_b16 a[202:203], v250 offset:22400\n\t"
"v_mfma_f32_16x16x32_bf16 v[164:167], a[32:35], a[148:151], v[164:167]\n\t"
"s_xor_b32 s25, s25, 384\n\t"
"ds_read_b64_tr_b16 a[204:205], v251 offset:21888\n\t"
"ds_read_b64_tr_b16 a[206:207], v251 offset:22400\n\t"
"v_mfma_f32_16x16x32_bf16 v[164:167], a[36:39], a[164:167], v[164:167]\n\t"
"buffer_load_dwordx4 a[52:55], off, s[84:87], 0 offset:1024 sc1\n\t"
"v_mfma_f32_16x16x32_bf16 v[148:151], a[24:27], a[148:151], v[148:151]\n\t"
"v_mfma_f32_16x16x32_bf16 v[148:151], a[28:31], a[164:167], v[148:151]\n\t"
"s_waitcnt lgkmcnt(0)\n\t"
"s_barrier\n\t"
"v_mfma_f32_16x16x32_bf16 v[152:155], a[24:27], a[152:155], v[152:155]\n\t"
"ds_read_b128 a[160:163], v224 offset:2048\n\t"
"v_mfma_f32_16x16x32_bf16 v[152:155], a[28:31], a[168:171], v[152:155]\n\t"
"ds_read_b128 a[144:147], v224 offset:0\n\t"
"v_mfma_f32_16x16x32_bf16 v[168:171], a[32:35], a[152:155], v[168:171]\n\t"
"ds_read_b128 a[148:151], v224 offset:256\n\t"
"v_mfma_f32_16x16x32_bf16 v[168:171], a[36:39], a[168:171], v[168:171]\n\t"
"s_nop 0\n\t"
"ds_read_b128 a[164:167], v224 offset:2304\n\t"
"v_mfma_f32_16x16x32_bf16 v[184:187], a[40:43], a[152:155], v[184:187]\n\t"
"s_nop 0\n\t"
"v_mfma_f32_16x16x32_bf16 v[184:187], a[44:47], a[168:171], v[184:187]\n\t"
"s_nop 0\n\t"
"v_mfma_f32_16x16x32_bf16 v[188:191], a[40:43], a[156:159], v[188:191]\n\t"
"s_nop 0\n\t"
"ds_read_b128 a[152:155], v224 offset:512\n\t"
"v_mfma_f32_16x16x32_bf16 v[188:191], a[44:47], a[172:175], v[188:191]\n\t"
"s_nop 0\n\t"
"v_mfma_f32_16x16x32_bf16 v[172:175], a[32:35], a[156:159], v[172:175]\n\t"
"s_nop 0\n\t"
"v_mfma_f32_16x16x32_bf16 v[172:175], a[36:39], a[172:175], v[172:175]\n\t"
"buffer_load_dwordx4 a[88:91], off, s[28:31], s51 offset:0 sc1\n\t"
"v_mfma_f32_16x16x32_bf16 v[156:159], a[24:27], a[156:159], v[156:159]\n\t"
"s_nop 0\n\t"
"ds_read_b128 a[168:171], v224 offset:2560\n\t"
"v_mfma_f32_16x16x32_bf16 v[156:159], a[28:31], a[172:175], v[156:159]\n\t"
"buffer_load_dwordx4 a[92:95], off, s[28:31], s51 offset:1024 sc1\n\t"
"v_mfma_f32_16x16x32_bf16 v[192:195], a[224:227], a[176:179], 0\n\t"
"Lpad12_%=:\n\t"
"s_cbranch_execz Lret1_%=\n\t"
"v_mfma_f32_16x16x32_bf16 v[196:199], a[224:227], a[180:183], 0\n\t"
"buffer_load_dwordx4 a[84:87], off, s[84:87], s51 offset:3072 sc1\n\t"
"v_mfma_f32_16x16x32_bf16 v[200:203], a[224:227], a[184:187], 0\n\t"
"ds_read_b128 a[156:159], v224 offset:768\n\t"
"v_mfma_f32_16x16x32_bf16 v[204:207], a[224:227], a[188:191], 0\n\t"
".p2align 3\n\t"
"buffer_load_dwordx4 a[80:83], off, s[84:87], s51 offset:2048 sc1\n\t"
"v_pk_fma_f32 v[48:49], v[144:145], s[90:91], v[48:49]\n\t"
"v_pk_fma_f32 v[50:51], v[146:147], s[90:91], v[50:51]\n\t"
"v_pk_add_f32 v[144:145], v[48:49], v[144:145] neg_lo:[0,1] neg_hi:[0,1]\n\t"
"v_pk_add_f32 v[146:147], v[50:51], v[146:147] neg_lo:[0,1] neg_hi:[0,1]\n\t"
"v_pk_fma_f32 v[64:65], v[160:161], s[90:91], v[64:65]\n\t"
"v_pk_fma_f32 v[66:67], v[162:163], s[90:91], v[66:67]\n\t"
"v_pk_add_f32 v[160:161], v[64:65], v[160:161] neg_lo:[0,1] neg_hi:[0,1]\n\t"
"v_pk_add_f32 v[162:163], v[66:67], v[162:163] neg_lo:[0,1] neg_hi:[0,1]\n\t"
"v_pk_fma_f32 v[80:81], v[176:177], s[90:91], v[80:81]\n\t"
"v_pk_fma_f32 v[82:83], v[178:179], s[90:91], v[82:83]\n\t"
"v_pk_add_f32 v[176:177], v[80:81], v[176:177] neg_lo:[0,1] neg_hi:[0,1]\n\t"
"v_pk_add_f32 v[178:179], v[82:83], v[178:179] neg_lo:[0,1] neg_hi:[0,1]\n\t"
"v_pk_fma_f32 v[84:85], v[180:181], s[90:91], v[84:85]\n\t"
"v_pk_fma_f32 v[86:87], v[182:183], s[90:91], v[86:87]\n\t"
"v_pk_add_f32 v[180:181], v[84:85], v[180:181] neg_lo:[0,1] neg_hi:[0,1]\n\t"
"v_pk_add_f32 v[182:183], v[86:87], v[182:183] neg_lo:[0,1] neg_hi:[0,1]\n\t"
"v_pk_fma_f32 v[68:69], v[164:165], s[90:91], v[68:69]\n\t"
"v_pk_fma_f32 v[70:71], v[166:167], s[90:91], v[70:71]\n\t"
"v_pk_add_f32 v[164:165], v[68:69], v[164:165] neg_lo:[0,1] neg_hi:[0,1]\n\t"
"v_pk_add_f32 v[166:167], v[70:71], v[166:167] neg_lo:[0,1] neg_hi:[0,1]\n\t"
"v_pk_fma_f32 v[52:53], v[148:149], s[90:91], v[52:53]\n\t"
"v_pk_fma_f32 v[54:55], v[150:151], s[90:91], v[54:55]\n\t"
"v_pk_add_f32 v[148:149], v[52:53], v[148:149] neg_lo:[0,1] neg_hi:[0,1]\n\t"
"v_pk_add_f32 v[150:151], v[54:55], v[150:151] neg_lo:[0,1] neg_hi:[0,1]\n\t"
"v_pk_fma_f32 v[56:57], v[152:153], s[90:91], v[56:57]\n\t"
"v_pk_fma_f32 v[58:59], v[154:155], s[90:91], v[58:59]\n\t"
"v_pk_add_f32 v[152:153], v[56:57], v[152:153] neg_lo:[0,1] neg_hi:[0,1]\n\t"
"v_pk_add_f32 v[154:155], v[58:59], v[154:155] neg_lo:[0,1] neg_hi:[0,1]\n\t"
"v_pk_fma_f32 v[72:73], v[168:169], s[90:91], v[72:73]\n\t"
"v_pk_fma_f32 v[74:75], v[170:171], s[90:91], v[74:75]\n\t"
"v_pk_add_f32 v[168:169], v[72:73], v[168:169] neg_lo:[0,1] neg_hi:[0,1]\n\t"
"v_pk_add_f32 v[170:171], v[74:75], v[170:171] neg_lo:[0,1] neg_hi:[0,1]\n\t"
"v_pk_fma_f32 v[88:89], v[184:185], s[90:91], v[88:89]\n\t"
"v_pk_fma_f32 v[90:91], v[186:187], s[90:91], v[90:91]\n\t"
"v_pk_add_f32 v[184:185], v[88:89], v[184:185] neg_lo:[0,1] neg_hi:[0,1]\n\t"
"v_pk_add_f32 v[186:187], v[90:91], v[186:187] neg_lo:[0,1] neg_hi:[0,1]\n\t"
"v_pk_fma_f32 v[92:93], v[188:189], s[90:91], v[92:93]\n\t"
"v_pk_fma_f32 v[94:95], v[190:191], s[90:91], v[94:95]\n\t"
"v_pk_add_f32 v[188:189], v[92:93], v[188:189] neg_lo:[0,1] neg_hi:[0,1]\n\t"
"v_pk_add_f32 v[190:191], v[94:95], v[190:191] neg_lo:[0,1] neg_hi:[0,1]\n\t"
"v_pk_fma_f32 v[76:77], v[172:173], s[90:91], v[76:77]\n\t"
"v_pk_fma_f32 v[78:79], v[174:175], s[90:91], v[78:79]\n\t"
"v_pk_add_f32 v[172:173], v[76:77], v[172:173] neg_lo:[0,1] neg_hi:[0,1]\n\t"
"v_pk_add_f32 v[174:175], v[78:79], v[174:175] neg_lo:[0,1] neg_hi:[0,1]\n\t"
"v_pk_fma_f32 v[60:61], v[156:157], s[90:91], v[60:61]\n\t"
"v_pk_fma_f32 v[62:63], v[158:159], s[90:91], v[62:63]\n\t"
"v_pk_add_f32 v[156:157], v[60:61], v[156:157] neg_lo:[0,1] neg_hi:[0,1]\n\t"
"v_pk_add_f32 v[158:159], v[62:63], v[158:159] neg_lo:[0,1] neg_hi:[0,1]\n\t"
"v_mfma_f32_16x16x32_bf16 v[208:211], a[224:227], a[192:195], 0\n\t"
"buffer_load_dwordx4 a[72:75], off, s[84:87], s51 offset:0 sc1\n\t"
"v_mfma_f32_16x16x32_bf16 v[212:215], a[224:227], a[196:199], 0\n\t"
"ds_read_b128 a[172:175], v224 offset:2816\n\t"
"v_mfma_f32_16x16x32_bf16 v[216:219], a[224:227], a[200:203], 0\n\t"
"buffer_load_dwordx4 a[76:79], off, s[84:87], s51 offset:1024 sc1\n\t"
"v_mfma_f32_16x16x32_bf16 v[220:223], a[224:227], a[204:207], 0\n\t"
"s_waitcnt vmcnt(6) lgkmcnt(3)\n\t"
"v_cvt_pk_bf16_f32 v193, v193, v197\n\t"
"v_cvt_pk_bf16_f32 v194, v194, v198\n\t"
"v_mfma_f32_16x16x32_bf16 v[48:51], a[48:51], a[144:147], v[48:51]\n\t"
"ds_write_addtid_b32 v194 offset:20672\n\t"
"v_cvt_pk_bf16_f32 v195, v195, v199\n\t"
"v_mfma_f32_16x16x32_bf16 v[48:51], a[52:55], a[160:163], v[48:51]\n\t"
"ds_write_addtid_b32 v193 offset:24864\n\t"
"v_cvt_pk_bf16_f32 v201, v201, v205\n\t"
"v_mfma_f32_16x16x32_bf16 v[64:67], a[56:59], a[144:147], v[64:67]\n\t"
"ds_write_addtid_b32 v195 offset:28992\n\t"
"v_cvt_pk_bf16_f32 v192, v192, v196\n\t"
"v_mfma_f32_16x16x32_bf16 v[64:67], a[60:63], a[160:163], v[64:67]\n\t"
"ds_write_addtid_b32 v192 offset:16544\n\t"
"v_cvt_pk_bf16_f32 v209, v209, v213\n\t"
"v_mfma_f32_16x16x32_bf16 v[80:83], a[64:67], a[144:147], v[80:83]\n\t"
"ds_read_b128 a[144:147], v224 offset:1024\n\t"
"v_cvt_pk_bf16_f32 v200, v200, v204\n\t"
"v_mfma_f32_16x16x32_bf16 v[80:83], a[68:71], a[160:163], v[80:83]\n\t"
"ds_write_addtid_b32 v201 offset:26912\n\t"
"v_cvt_pk_bf16_f32 v48, v48, v49\n\t"
"v_mfma_f32_16x16x32_bf16 v[84:87], a[64:67], a[148:151], v[84:87]\n\t"
"ds_read_b128 a[160:163], v224 offset:3072\n\t"
"v_cvt_pk_bf16_f32 v218, v218, v222\n\t"
"v_mfma_f32_16x16x32_bf16 v[84:87], a[68:71], a[164:167], v[84:87]\n\t"
"ds_write_addtid_b32 v209 offset:25888\n\t"
"v_cvt_pk_bf16_f32 v216, v216, v220\n\t"
"v_cvt_pk_bf16_f32 v49, v50, v51\n\t"
"v_mfma_f32_16x16x32_bf16 v[68:71], a[56:59], a[148:151], v[68:71]\n\t"
"ds_write_addtid_b32 v218 offset:23744\n\t"
"v_cvt_pk_bf16_f32 v202, v202, v206\n\t"
"v_mfma_f32_16x16x32_bf16 v[68:71], a[60:63], a[164:167], v[68:71]\n\t"
"ds_write_addtid_b32 v200 offset:18592\n\t"
"v_cvt_pk_bf16_f32 v219, v219, v223\n\t"
"v_mfma_f32_16x16x32_bf16 v[52:55], a[48:51], a[148:151], v[52:55]\n\t"
"ds_read_b128 a[148:151], v224 offset:1280\n\t"
"v_cvt_pk_bf16_f32 v80, v80, v81\n\t"
"v_mfma_f32_16x16x32_bf16 v[52:55], a[52:55], a[164:167], v[52:55]\n\t"
"s_waitcnt lgkmcnt(9)\n\t"
"ds_read_b128 a[164:167], v224 offset:3328\n\t"
"v_cvt_pk_bf16_f32 v208, v208, v212\n\t"
"v_cvt_pk_bf16_f32 v210, v210, v214\n\t"
"v_cvt_pk_bf16_f32 v211, v211, v215\n\t"
"v_cvt_pk_bf16_f32 v217, v217, v221\n\t"
"v_mfma_f32_16x16x32_bf16 v[56:59], a[48:51], a[152:155], v[56:59]\n\t"
"ds_write_addtid_b32 v211 offset:30016\n\t"
"v_cvt_pk_bf16_f32 v203, v203, v207\n\t"
"v_mfma_f32_16x16x32_bf16 v[56:59], a[52:55], a[168:171], v[56:59]\n\t"
"s_nop 0\n\t"
"ds_write_addtid_b32 v202 offset:22720\n\t"
"ds_write_addtid_b32 v203 offset:31040\n\t"
"ds_write_addtid_b32 v208 offset:17568\n\t"
"ds_write_addtid_b32 v216 offset:19616\n\t"
"buffer_load_dwordx4 a[112:115], off, s[28:31], s81 offset:0 sc1\n\t"
"buffer_load_dwordx4 a[116:119], off, s[28:31], s81 offset:1024 sc1\n\t"
"v_cvt_pk_bf16_f32 v84, v84, v85\n\t"
"v_mfma_f32_16x16x32_bf16 v[72:75], a[56:59], a[152:155], v[72:75]\n\t"
"ds_write_addtid_b32 v219 offset:32064\n\t"
"v_cvt_pk_bf16_f32 v81, v82, v83\n\t"
"v_mfma_f32_16x16x32_bf16 v[72:75], a[60:63], a[168:171], v[72:75]\n\t"
"ds_write_addtid_b32 v217 offset:27936\n\t"
"v_cvt_pk_bf16_f32 v52, v52, v53\n\t"
"v_cvt_pk_bf16_f32 v68, v68, v69\n\t"
"v_mfma_f32_16x16x32_bf16 v[88:91], a[64:67], a[152:155], v[88:91]\n\t"
"ds_read_b128 a[152:155], v224 offset:1536\n\t"
"v_cvt_pk_bf16_f32 v53, v54, v55\n\t"
"v_mfma_f32_16x16x32_bf16 v[88:91], a[68:71], a[168:171], v[88:91]\n\t"
"ds_read_b128 a[168:171], v224 offset:3584\n\t"
"buffer_load_dwordx4 a[108:111], off, s[84:87], s81 offset:3072 sc1\n\t"
"buffer_load_dwordx4 a[104:107], off, s[84:87], s81 offset:2048 sc1\n\t"
"v_mfma_f32_16x16x32_bf16 v[92:95], a[64:67], a[156:159], v[92:95]\n\t"
"v_cvt_pk_bf16_f32 v69, v70, v71\n\t"
"v_cvt_pk_bf16_f32 v85, v86, v87\n\t"
"v_mfma_f32_16x16x32_bf16 v[92:95], a[68:71], a[172:175], v[92:95]\n\t"
"ds_write_addtid_b32 v210 offset:21696\n\t"
"ds_write2st64_b64 v247, v[80:81], v[84:85] offset0:0 offset1:13\n\t"
"v_mfma_f32_16x16x32_bf16 v[76:79], a[56:59], a[156:159], v[76:79]\n\t"
"v_cvt_pk_bf16_f32 v64, v64, v65\n\t"
"v_cvt_pk_bf16_f32 v88, v88, v89\n\t"
"v_mfma_f32_16x16x32_bf16 v[76:79], a[60:63], a[172:175], v[76:79]\n\t"
"buffer_load_dwordx4 a[96:99], off, s[84:87], s81 offset:0 sc1\n\t"
"buffer_load_dwordx4 a[100:103], off, s[84:87], s81 offset:1024 sc1\n\t"
"v_cvt_pk_bf16_f32 v89, v90, v91\n\t"
"v_mfma_f32_16x16x32_bf16 v[60:63], a[48:51], a[156:159], v[60:63]\n\t"
"v_cvt_pk_bf16_f32 v65, v66, v67\n\t"
"v_cvt_pk_bf16_f32 v72, v72, v73\n\t"
"v_mfma_f32_16x16x32_bf16 v[60:63], a[52:55], a[172:175], v[60:63]\n\t"
"s_waitcnt vmcnt(6) lgkmcnt(6)\n\t"
"ds_read_b128 a[156:159], v224 offset:1792\n\t"
"ds_read_b128 a[172:175], v224 offset:3840\n\t"
"ds_write2st64_b64 v228, v[48:49], v[52:53] offset0:0 offset1:13\n\t"
"v_cvt_pk_bf16_f32 v73, v74, v75\n\t"
"v_mfma_f32_16x16x32_bf16 v[144:147], a[72:75], a[144:147], v[144:147]\n\t"
"v_cvt_pk_bf16_f32 v56, v56, v57\n\t"
"v_cvt_pk_bf16_f32 v92, v92, v93\n\t"
"v_mfma_f32_16x16x32_bf16 v[144:147], a[76:79], a[160:163], v[144:147]\n\t"
"buffer_load_dwordx4 a[136:139], off, s[28:31], s66 offset:0 sc1\n\t"
"buffer_load_dwordx4 a[140:143], off, s[28:31], s66 offset:1024 sc1\n\t"
"ds_write2st64_b64 v246, v[64:65], v[68:69] offset0:0 offset1:13\n\t"
"v_cvt_pk_bf16_f32 v57, v58, v59\n\t"
"v_cvt_pk_bf16_f32 v93, v94, v95\n\t"
"v_mfma_f32_16x16x32_bf16 v[160:163], a[80:83], a[144:147], v[160:163]\n\t"
"v_cvt_pk_bf16_f32 v60, v60, v61\n\t"
"v_cvt_pk_bf16_f32 v76, v76, v77\n\t"
"v_mfma_f32_16x16x32_bf16 v[160:163], a[84:87], a[160:163], v[160:163]\n\t"
"buffer_load_dwordx4 a[132:135], off, s[84:87], s66 offset:3072 sc1\n\t"
"ds_write2st64_b64 v247, v[88:89], v[92:93] offset0:26 offset1:39\n\t"
"v_mfma_f32_16x16x32_bf16 v[176:179], a[88:91], a[144:147], v[176:179]\n\t"
"v_cvt_pk_bf16_f32 v61, v62, v63\n\t"
"v_cvt_pk_bf16_f32 v77, v78, v79\n\t"
"v_mfma_f32_16x16x32_bf16 v[176:179], a[92:95], a[160:163], v[176:179]\n\t"
"ds_write2st64_b64 v228, v[56:57], v[60:61] offset0:26 offset1:39\n\t"
"v_mfma_f32_16x16x32_bf16 v[180:183], a[88:91], a[148:151], v[180:183]\n\t"
"buffer_load_dwordx4 a[128:131], off, s[84:87], s66 offset:2048 sc1\n\t"
"v_mfma_f32_16x16x32_bf16 v[180:183], a[92:95], a[164:167], v[180:183]\n\t"
"ds_write2st64_b64 v246, v[72:73], v[76:77] offset0:26 offset1:39\n\t"
"v_mfma_f32_16x16x32_bf16 v[164:167], a[80:83], a[148:151], v[164:167]\n\t"
"buffer_load_dwordx4 a[120:123], off, s[84:87], s66 offset:0 sc1\n\t"
"v_mfma_f32_16x16x32_bf16 v[164:167], a[84:87], a[164:167], v[164:167]\n\t"
"v_mfma_f32_16x16x32_bf16 v[148:151], a[72:75], a[148:151], v[148:151]\n\t"
"buffer_load_dwordx4 a[124:127], off, s[84:87], s66 offset:1024 sc1\n\t"
"v_mfma_f32_16x16x32_bf16 v[148:151], a[76:79], a[164:167], v[148:151]\n\t"
"s_waitcnt lgkmcnt(0)\n\t"
"s_barrier\n\t"
"v_mfma_f32_16x16x32_bf16 v[152:155], a[72:75], a[152:155], v[152:155]\n\t"
"s_nop 0\n\t"
"ds_read_b128 a[148:151], v224 offset:16800\n\t"
"ds_read_b128 a[12:15], v229 offset:13312\n\t"
"v_mfma_f32_16x16x32_bf16 v[152:155], a[76:79], a[168:171], v[152:155]\n\t"
"s_nop 0\n\t"
"ds_read_b128 a[144:147], v224 offset:16544\n\t"
"ds_read_b128 a[160:163], v224 offset:18592\n\t"
"v_mfma_f32_16x16x32_bf16 v[168:171], a[80:83], a[152:155], v[168:171]\n\t"
"ds_read_b128 a[0:3], v229 offset:0\n\t"
"v_mfma_f32_16x16x32_bf16 v[168:171], a[84:87], a[168:171], v[168:171]\n\t"
"ds_read_b128 a[164:167], v224 offset:18848\n\t"
"v_mfma_f32_16x16x32_bf16 v[184:187], a[88:91], a[152:155], v[184:187]\n\t"
"s_nop 0\n\t"
"ds_read_b128 a[4:7], v230 offset:0\n\t"
"ds_read_b128 a[16:19], v230 offset:13312\n\t"
"v_mfma_f32_16x16x32_bf16 v[184:187], a[92:95], a[168:171], v[184:187]\n\t"
"s_nop 0\n\t"
"ds_read_b128 a[8:11], v231 offset:0\n\t"
"ds_read_b128 a[20:23], v231 offset:13312\n\t"
"v_mfma_f32_16x16x32_bf16 v[188:191], a[88:91], a[156:159], v[188:191]\n\t"
"s_waitcnt lgkmcnt(8)\n\t"
"buffer_store_dwordx4 a[12:15], v232, s[16:19], s27 offen nt\n\t"
"v_mfma_f32_16x16x32_bf16 v[188:191], a[92:95], a[172:175], v[188:191]\n\t"
"ds_read_b128 a[152:155], v224 offset:17568\n\t"
"v_mfma_f32_16x16x32_bf16 v[172:175], a[80:83], a[156:159], v[172:175]\n\t"
"ds_read_b128 a[168:171], v224 offset:19616\n\t"
"v_mfma_f32_16x16x32_bf16 v[172:175], a[84:87], a[172:175], v[172:175]\n\t"
"s_waitcnt lgkmcnt(7)\n\t"
"buffer_store_dwordx4 a[0:3], v232, s[16:19], s24 offen nt\n\t"
"v_mfma_f32_16x16x32_bf16 v[156:159], a[72:75], a[156:159], v[156:159]\n\t"
"v_mfma_f32_16x16x32_bf16 v[156:159], a[76:79], a[172:175], v[156:159]\n\t"
"s_waitcnt vmcnt(8)\n\t"
"s_waitcnt lgkmcnt(5)\n\t"
"buffer_store_dwordx4 a[4:7], v233, s[16:19], s24 offen nt\n\t"
"v_mfma_f32_16x16x32_bf16 v[144:147], a[96:99], a[144:147], v[144:147]\n\t"
"ds_read_b128 a[156:159], v224 offset:17824\n\t"
"v_mfma_f32_16x16x32_bf16 v[144:147], a[100:103], a[160:163], v[144:147]\n\t"
"s_waitcnt lgkmcnt(5)\n\t"
"buffer_store_dwordx4 a[16:19], v233, s[16:19], s27 offen nt\n\t"
"v_mfma_f32_16x16x32_bf16 v[160:163], a[104:107], a[144:147], v[160:163]\n\t"
"ds_read_b128 a[172:175], v224 offset:19872\n\t"
"v_mfma_f32_16x16x32_bf16 v[160:163], a[108:111], a[160:163], v[160:163]\n\t"
"s_waitcnt lgkmcnt(4)\n\t"
"buffer_store_dwordx4 a[8:11], v234, s[16:19], s24 offen nt\n\t"
"v_mfma_f32_16x16x32_bf16 v[176:179], a[112:115], a[144:147], v[176:179]\n\t"
"ds_read_b128 a[144:147], v224 offset:17056\n\t"
"v_mfma_f32_16x16x32_bf16 v[176:179], a[116:119], a[160:163], v[176:179]\n\t"
"v_mfma_f32_16x16x32_bf16 v[180:183], a[112:115], a[148:151], v[180:183]\n\t"
"buffer_store_dwordx4 a[20:23], v234, s[16:19], s27 offen nt\n\t"
"v_mfma_f32_16x16x32_bf16 v[180:183], a[116:119], a[164:167], v[180:183]\n\t"
"ds_read_b128 a[160:163], v224 offset:19104\n\t"
"v_mfma_f32_16x16x32_bf16 v[164:167], a[104:107], a[148:151], v[164:167]\n\t"
"v_mfma_f32_16x16x32_bf16 v[164:167], a[108:111], a[164:167], v[164:167]\n\t"
"v_mfma_f32_16x16x32_bf16 v[148:151], a[96:99], a[148:151], v[148:151]\n\t"
"ds_read_b128 a[148:151], v224 offset:17312\n\t"
"v_mfma_f32_16x16x32_bf16 v[148:151], a[100:103], a[164:167], v[148:151]\n\t"
"s_waitcnt vmcnt(6) lgkmcnt(4)\n\t"
"v_mfma_f32_16x16x32_bf16 v[144:147], a[120:123], a[152:155], v[144:147]\n\t"
"ds_read_b128 a[164:167], v224 offset:19360\n\t"
"v_mfma_f32_16x16x32_bf16 v[144:147], a[124:127], a[168:171], v[144:147]\n\t"
"v_mfma_f32_16x16x32_bf16 v[160:163], a[128:131], a[152:155], v[160:163]\n\t"
"v_mfma_f32_16x16x32_bf16 v[160:163], a[132:135], a[168:171], v[160:163]\n\t"
"v_mfma_f32_16x16x32_bf16 v[176:179], a[136:139], a[152:155], v[176:179]\n\t"
"ds_read_b128 a[152:155], v224 offset:18080\n\t"
"v_mfma_f32_16x16x32_bf16 v[176:179], a[140:143], a[168:171], v[176:179]\n\t"
"s_waitcnt lgkmcnt(3)\n\t"
"s_nop 1\n\t"
"v_cvt_pk_bf16_f32 v144, v144, v145\n\t"
"v_cvt_pk_bf16_f32 v145, v146, v147\n\t"
"v_mfma_f32_16x16x32_bf16 v[180:183], a[136:139], a[156:159], v[180:183]\n\t"
"ds_read_b128 a[168:171], v224 offset:20128\n\t"
"v_mfma_f32_16x16x32_bf16 v[180:183], a[140:143], a[172:175], v[180:183]\n\t"
"v_mfma_f32_16x16x32_bf16 v[164:167], a[128:131], a[156:159], v[164:167]\n\t"
"v_cvt_pk_bf16_f32 v160, v160, v161\n\t"
"v_cvt_pk_bf16_f32 v176, v176, v177\n\t"
"v_mfma_f32_16x16x32_bf16 v[164:167], a[132:135], a[172:175], v[164:167]\n\t"
"v_cvt_pk_bf16_f32 v161, v162, v163\n\t"
"v_mfma_f32_16x16x32_bf16 v[148:151], a[120:123], a[156:159], v[148:151]\n\t"
"ds_read_b128 a[156:159], v224 offset:18336\n\t"
"s_nop 0\n\t"
"v_cvt_pk_bf16_f32 v180, v180, v181\n\t"
"v_mfma_f32_16x16x32_bf16 v[148:151], a[124:127], a[172:175], v[148:151]\n\t"
"ds_read_b128 a[172:175], v224 offset:20384\n\t"
"v_cvt_pk_bf16_f32 v181, v182, v183\n\t"
"v_mfma_f32_16x16x32_bf16 v[152:155], a[96:99], a[144:147], v[152:155]\n\t"
"v_cvt_pk_bf16_f32 v177, v178, v179\n\t"
"v_mfma_f32_16x16x32_bf16 v[152:155], a[100:103], a[160:163], v[152:155]\n\t"
"ds_write2st64_b64 v247, v[176:177], v[180:181] offset0:52 offset1:65\n\t"
"v_mfma_f32_16x16x32_bf16 v[168:171], a[104:107], a[144:147], v[168:171]\n\t"
"v_cvt_pk_bf16_f32 v164, v164, v165\n\t"
"v_cvt_pk_bf16_f32 v165, v166, v167\n\t"
"v_mfma_f32_16x16x32_bf16 v[168:171], a[108:111], a[160:163], v[168:171]\n\t"
"v_cvt_pk_bf16_f32 v148, v148, v149\n\t"
"v_cvt_pk_bf16_f32 v149, v150, v151\n\t"
"v_mfma_f32_16x16x32_bf16 v[184:187], a[112:115], a[144:147], v[184:187]\n\t"
"ds_write2st64_b64 v228, v[144:145], v[148:149] offset0:52 offset1:65\n\t"
"v_mfma_f32_16x16x32_bf16 v[184:187], a[116:119], a[160:163], v[184:187]\n\t"
"s_waitcnt lgkmcnt(4)\n\t"
"v_mfma_f32_16x16x32_bf16 v[188:191], a[112:115], a[148:151], v[188:191]\n\t"
"v_mfma_f32_16x16x32_bf16 v[188:191], a[116:119], a[164:167], v[188:191]\n\t"
"ds_write2st64_b64 v246, v[160:161], v[164:165] offset0:52 offset1:65\n\t"
"v_mfma_f32_16x16x32_bf16 v[172:175], a[104:107], a[148:151], v[172:175]\n\t"
"v_mfma_f32_16x16x32_bf16 v[172:175], a[108:111], a[164:167], v[172:175]\n\t"
"v_mfma_f32_16x16x32_bf16 v[156:159], a[96:99], a[148:151], v[156:159]\n\t"
"v_mfma_f32_16x16x32_bf16 v[156:159], a[100:103], a[164:167], v[156:159]\n\t"
"s_waitcnt lgkmcnt(0)\n\t"
"s_barrier\n\t"
"v_mfma_f32_16x16x32_bf16 v[152:155], a[120:123], a[152:155], v[152:155]\n\t"
"ds_read_b128 a[32:35], v231 offset:26624\n\t"
"v_mfma_f32_16x16x32_bf16 v[152:155], a[124:127], a[168:171], v[152:155]\n\t"
"ds_read_b128 a[24:27], v229 offset:26624\n\t"
"v_mfma_f32_16x16x32_bf16 v[168:171], a[128:131], a[152:155], v[168:171]\n\t"
"v_mfma_f32_16x16x32_bf16 v[168:171], a[132:135], a[168:171], v[168:171]\n\t"
"v_mfma_f32_16x16x32_bf16 v[184:187], a[136:139], a[152:155], v[184:187]\n\t"
"ds_read_b128 a[28:31], v230 offset:26624\n\t"
"s_nop 2\n\t"
"v_cvt_pk_bf16_f32 v152, v152, v153\n\t"
"v_mfma_f32_16x16x32_bf16 v[184:187], a[140:143], a[168:171], v[184:187]\n\t"
"v_cvt_pk_bf16_f32 v153, v154, v155\n\t"
"v_mfma_f32_16x16x32_bf16 v[188:191], a[136:139], a[156:159], v[188:191]\n\t"
"s_waitcnt lgkmcnt(2)\n\t"
"buffer_store_dwordx4 a[32:35], v234, s[16:19], s20 offen nt\n\t"
"v_mfma_f32_16x16x32_bf16 v[188:191], a[140:143], a[172:175], v[188:191]\n\t"
"v_cvt_pk_bf16_f32 v168, v168, v169\n\t"
"v_cvt_pk_bf16_f32 v169, v170, v171\n\t"
"v_mfma_f32_16x16x32_bf16 v[172:175], a[128:131], a[156:159], v[172:175]\n\t"
"v_cvt_pk_bf16_f32 v184, v184, v185\n\t"
"v_cvt_pk_bf16_f32 v185, v186, v187\n\t"
"v_mfma_f32_16x16x32_bf16 v[172:175], a[132:135], a[172:175], v[172:175]\n\t"
"s_waitcnt lgkmcnt(1)\n\t"
"buffer_store_dwordx4 a[24:27], v232, s[16:19], s20 offen nt\n\t"
"v_mfma_f32_16x16x32_bf16 v[156:159], a[120:123], a[156:159], v[156:159]\n\t"
"v_cvt_pk_bf16_f32 v188, v188, v189\n\t"
"v_cvt_pk_bf16_f32 v189, v190, v191\n\t"
"v_mfma_f32_16x16x32_bf16 v[156:159], a[124:127], a[172:175], v[156:159]\n\t"
"s_waitcnt lgkmcnt(0)\n\t"
"buffer_store_dwordx4 a[28:31], v233, s[16:19], s20 offen nt\n\t"
"s_nop 5\n\t"
"v_cvt_pk_bf16_f32 v156, v156, v157\n\t"
"v_cvt_pk_bf16_f32 v157, v158, v159\n\t"
"ds_write2st64_b64 v228, v[152:153], v[156:157] offset0:78 offset1:91\n\t"
"v_cvt_pk_bf16_f32 v172, v172, v173\n\t"
"v_cvt_pk_bf16_f32 v173, v174, v175\n\t"
"ds_write2st64_b64 v246, v[168:169], v[172:173] offset0:78 offset1:91\n\t"
"ds_write2st64_b64 v247, v[184:185], v[188:189] offset0:78 offset1:91\n\t"
"s_waitcnt lgkmcnt(0)\n\t"
"s_barrier\n\t"
"ds_read_b128 a[36:39], v229 offset:39936\n\t"
"ds_read_b128 a[40:43], v230 offset:39936\n\t"
"ds_read_b128 a[44:47], v231 offset:39936\n\t"
"s_waitcnt lgkmcnt(2)\n\t"
"buffer_store_dwordx4 a[36:39], v232, s[16:19], s25 offen nt\n\t"
"s_waitcnt lgkmcnt(1)\n\t"
"buffer_store_dwordx4 a[40:43], v233, s[16:19], s25 offen nt\n\t"
"s_waitcnt lgkmcnt(0)\n\t"
"buffer_store_dwordx4 a[44:47], v234, s[16:19], s25 offen nt\n\t"
"s_waitcnt vmcnt(0)\n\t"
"Lwino_end:\n\t"
  : "+{v205}"(tid)
  : "s"(__builtin_amdgcn_kernarg_segment_ptr()), "s"(blockIdx.x), "s"((unsigned)(size_t)(__attribute__((address_space(3))) char*)smem)
  : "v0", "v1", "v2", "v3", "v4", "v5", "v6", "v7", "v8", "v9", "v10", "v11", "v12", "v13", "v14", "v15", "v16", "v17", "v18", "v19", "v20", "v21", "v22", "v23", "v24", "v25", "v26", "v27", "v28", "v29", "v30", "v31", "v32", "v33", "v34", "v35", "v36", "v37", "v38", "v39", "v40", "v41", "v42", "v43", "v44", "v45", "v46", "v47", "v48", "v49", "v50", "v51", "v52", "v53", "v54", "v55", "v56", "v57", "v58", "v59", "v60", "v61", "v62", "v63", "v64", "v65", "v66", "v67", "v68", "v69", "v70", "v71", "v72", "v73", "v74", "v75", "v76", "v77", "v78", "v79", "v80", "v81", "v82", "v83", "v84", "v85", "v86", "v87", "v88", "v89", "v90", "v91", "v92", "v93", "v94", "v95", "v96", "v97", "v98", "v99", "v100", "v101", "v102", "v103", "v104", "v105", "v106", "v107", "v108", "v109", "v110", "v111", "v112", "v113", "v114", "v115", "v116", "v117", "v118", "v119", "v120", "v121", "v122", "v123", "v124", "v125", "v126", "v127", "v128", "v129", "v130", "v131", "v132", "v133", "v134", "v135", "v136", "v137", "v138", "v139", "v140", "v141", "v142", "v143", "v144", "v145", "v146", "v147", "v148", "v149", "v150", "v151", "v152", "v153", "v154", "v155", "v156", "v157", "v158", "v159", "v160", "v161", "v162", "v163", "v164", "v165", "v166", "v167", "v168", "v169", "v170", "v171", "v172", "v173", "v174", "v175", "v176", "v177", "v178", "v179", "v180", "v181", "v182", "v183", "v184", "v185", "v186", "v187", "v188", "v189", "v190", "v191", "v192", "v193", "v194", "v195", "v196", "v197", "v198", "v199", "v200", "v201", "v202", "v203", "v204", "v206", "v207", "v208", "v209", "v210", "v211", "v212", "v213", "v214", "v215", "v216", "v217", "v218", "v219", "v220", "v221", "v222", "v223", "v224", "v225", "v226", "v227", "v228", "v229", "v230", "v231", "v232", "v233", "v234", "v235", "v236", "v237", "v238", "v239", "v240", "v241", "v242", "v243", "v244", "v245", "v246", "v247", "v248", "v249", "v250", "v251", "v252", "v253", "v254", "v255", "a0", "a1", "a2", "a3", "a4", "a5", "a6", "a7", "a8", "a9", "a10", "a11", "a12", "a13", "a14", "a15", "a16", "a17", "a18", "a19", "a20", "a21", "a22", "a23", "a24", "a25", "a26", "a27", "a28", "a29", "a30", "a31", "a32", "a33", "a34", "a35", "a36", "a37", "a38", "a39", "a40", "a41", "a42", "a43", "a44", "a45", "a46", "a47", "a48", "a49", "a50", "a51", "a52", "a53", "a54", "a55", "a56", "a57", "a58", "a59", "a60", "a61", "a62", "a63", "a64", "a65", "a66", "a67", "a68", "a69", "a70", "a71", "a72", "a73", "a74", "a75", "a76", "a77", "a78", "a79", "a80", "a81", "a82", "a83", "a84", "a85", "a86", "a87", "a88", "a89", "a90", "a91", "a92", "a93", "a94", "a95", "a96", "a97", "a98", "a99", "a100", "a101", "a102", "a103", "a104", "a105", "a106", "a107", "a108", "a109", "a110", "a111", "a112", "a113", "a114", "a115", "a116", "a117", "a118", "a119", "a120", "a121", "a122", "a123", "a124", "a125", "a126", "a127", "a128", "a129", "a130", "a131", "a132", "a133", "a134", "a135", "a136", "a137", "a138", "a139", "a140", "a141", "a142", "a143", "a144", "a145", "a146", "a147", "a148", "a149", "a150", "a151", "a152", "a153", "a154", "a155", "a156", "a157", "a158", "a159", "a160", "a161", "a162", "a163", "a164", "a165", "a166", "a167", "a168", "a169", "a170", "a171", "a172", "a173", "a174", "a175", "a176", "a177", "a178", "a179", "a180", "a181", "a182", "a183", "a184", "a185", "a186", "a187", "a188", "a189", "a190", "a191", "a192", "a193", "a194", "a195", "a196", "a197", "a198", "a199", "a200", "a201", "a202", "a203", "a204", "a205", "a206", "a207", "a208", "a209", "a210", "a211", "a212", "a213", "a214", "a215", "a216", "a217", "a218", "a219", "a220", "a221", "a222", "a223", "a224", "a225", "a226", "a227", "a228", "a229", "a230", "a231", "a232", "a233", "a234", "a235", "a236", "a237", "a238", "a239", "a240", "a241", "a242", "a243", "a244", "a245", "a246", "a247", "a248", "a249", "a250", "a251", "a252", "a253", "a254", "a255", "s6", "s7", "s8", "s9", "s10", "s11", "s12", "s13", "s14", "s15", "s16", "s17", "s18", "s19", "s20", "s21", "s22", "s23", "s24", "s25", "s26", "s27", "s28", "s29", "s30", "s31", "s32", "s33", "s34", "s35", "s36", "s37", "s38", "s39", "s40", "s41", "s42", "s43", "s44", "s45", "s46", "s47", "s48", "s49", "s50", "s51", "s52", "s53", "s54", "s55", "s56", "s57", "s58", "s59", "s60", "s61", "s62", "s63", "s64", "s65", "s66", "s67", "s68", "s69", "s70", "s71", "s72", "s73", "s74", "s75", "s76", "s77", "s78", "s79", "s80", "s81", "s82", "s83", "s84", "s85", "s86", "s87", "s88", "s89", "s90", "s91", "s92", "s93", "s94", "s95", "s96", "s97", "s98", "s99", "scc", "vcc", "memory");
}

extern "C" void kernel_launch(void* const* d_in, const int* in_sizes, int n_in, void* d_out, int out_size, void* d_ws,
                              size_t ws_size, hipStream_t stream) {
  wino_main_kernel<<<256, 256, 0, stream>>>((const char*)d_in[0], (char*)d_ws, (char*)d_out, (const char*)d_in[1]);
}
```
